```python
import torch

def _softmax(x, axis=-1):
    return torch.softmax(x, dim=axis)

def _gelu(x):
    return torch.nn.functional.gelu(x, approximate='tanh')

def _silu(x):
    return torch.nn.functional.silu(x)

def _sigmoid(x):
    return torch.sigmoid(x)

def _layernorm(x, axis=-1, eps=1e-05):
    m = x.mean(dim=axis, keepdim=True)
    v = x.var(dim=axis, keepdim=True, unbiased=False)
    return (x - m) / torch.sqrt(v + eps)

def _scatter_add(tgt, idx, src, axis=0):
    return tgt.clone().index_add_(axis, idx, src)

def _segment_sum(src, seg, n):
    out = torch.zeros((n,) + src.shape[1:], dtype=src.dtype)
    return out.index_add_(0, seg, src)

def _pad(x, pw):
    flat = tuple((v for pair in reversed(pw) for v in pair))
    return torch.nn.functional.pad(x, flat)
kernel_name = 'interleave_layers__ssm_conv_s4d__memory_aug_recurrent_mem_tr__prompt'

def setup_inputs():

    _w = lambda a: (lambda t: t.to(torch.bfloat16) if t.is_floating_point() else t)(torch.as_tensor(a))

    def _A_setup():
        Bsz, L, D, N = (1, 2048, 512, 128)
        return {'u': _w((lambda *s: torch.randn(*s, dtype=torch.bfloat16) * 0.05)(Bsz, L, D)), 'log_A_real': _w((lambda *s: torch.randn(*s, dtype=torch.bfloat16) * 3.2)(D, N) * 0.5), 'A_imag': _w((lambda *s: torch.randn(*s, dtype=torch.bfloat16) * 0.05)(D, N) * 3.14), 'C_real': _w((lambda *s: torch.randn(*s, dtype=torch.bfloat16) * 0.05)(D, N) / N ** 0.5), 'C_imag': _w((lambda *s: torch.randn(*s, dtype=torch.bfloat16) * 0.05)(D, N) / N ** 0.5), 'log_dt': _w((lambda *s: torch.randn(*s, dtype=torch.bfloat16) * 0.05)(D) * 0.5 - 4.0), 'D_skip': _w((lambda *s: torch.randn(*s, dtype=torch.bfloat16) * 0.05)(D))}
    _A_in = _A_setup()

    def _B_setup():
        B, S, Dm = (1, 2048, 512)
        H, Dh = (32, 128)
        M, seg = (8, 32)
        seg = min(seg, S)
        S = S // seg * seg
        return {'x': _w((lambda *s: torch.randn(*s, dtype=torch.bfloat16) * 0.05)(B, S, Dm)), 'mem0': _w((lambda *s: torch.randn(*s, dtype=torch.bfloat16) * 0.8)(B, M, Dm)), 'Wq': _w((lambda *s: torch.randn(*s, dtype=torch.bfloat16) * 1.6)(Dm, H, Dh) * Dm ** (-0.5)), 'Wk': _w((lambda *s: torch.randn(*s, dtype=torch.bfloat16) * 1.6)(Dm, H, Dh) * Dm ** (-0.5)), 'Wv': _w((lambda *s: torch.randn(*s, dtype=torch.bfloat16) * 0.8)(Dm, H, Dh) * Dm ** (-0.5)), 'Wo': _w((lambda *s: torch.randn(*s, dtype=torch.bfloat16) * 0.8)(H, Dh, Dm) * (H * Dh) ** (-0.5)), 'seg_len': seg}
    _B_in = _B_setup()
    return {'A_u': _A_in['u'], 'A_log_A_real': _A_in['log_A_real'], 'A_A_imag': _A_in['A_imag'], 'A_C_real': _A_in['C_real'], 'A_C_imag': _A_in['C_imag'], 'A_log_dt': _A_in['log_dt'], 'A_D_skip': _A_in['D_skip'], 'B_x': _B_in['x'], 'B_mem0': _B_in['mem0'], 'B_Wq': _B_in['Wq'], 'B_Wk': _B_in['Wk'], 'B_Wv': _B_in['Wv'], 'B_Wo': _B_in['Wo'], 'B_seg_len': _B_in['seg_len']}

def reference(A_u, A_log_A_real, A_A_imag, A_C_real, A_C_imag, A_log_dt, A_D_skip, B_x, B_mem0, B_Wq, B_Wk, B_Wv, B_Wo, B_seg_len):

    def _A_ref(u, log_A_real, A_imag, C_real, C_imag, log_dt, D_skip):
        L = u.shape[1]
        dt = torch.exp(log_dt)
        A = -torch.exp(log_A_real) + 1j * A_imag
        C = (C_real + 1j * C_imag) * dt[:, None]
        pos = (lambda x, dt: x.to(dt))(torch.arange(L), log_dt.dtype)
        P = torch.exp((A * dt[:, None])[:, :, None] * pos[None, None, :])
        K = 2.0 * torch.einsum('dn,dnl->dl', C, P).real
        L2 = 2 * L
        u_t = (lambda x, *d: x.permute(*d))(u, 0, 2, 1)
        Kf = (lambda x, *a, **k: torch.fft.rfft(x.float(), *a, **k))(K, L2)
        Uf = (lambda x, *a, **k: torch.fft.rfft(x.float(), *a, **k))(u_t, L2)
        y = (lambda x, *a, **k: torch.fft.irfft(x, *a, **k).to(torch.bfloat16))(Uf * Kf[None, :, :], L2)[:, :, :L]
        y = (lambda x, *d: x.permute(*d))(y, 0, 2, 1)
        return y + u * D_skip[None, None, :]

    def _B_ref(x, mem0, Wq, Wk, Wv, Wo, seg_len):
        B, S, Dm = x.shape
        H, Dh = (Wq.shape[1], Wq.shape[2])
        M = mem0.shape[1]
        scale = Dh ** (-0.5)
        n_seg = S // seg_len
        L = M + seg_len
        idx = torch.arange(L)
        mask = (idx[:, None] < M) | (idx[:, None] >= idx[None, :])
        mem = mem0
        outs = []
        for s in range(n_seg):
            xs = x[:, s * seg_len:(s + 1) * seg_len, :].clone()
            h = torch.concatenate([mem, xs], axis=1)
            q = torch.einsum('btd,dhe->bhte', h, Wq)
            k = torch.einsum('btd,dhe->bhte', h, Wk)
            v = torch.einsum('btd,dhe->bhte', h, Wv)
            scores = torch.einsum('bhqe,bhke->bhqk', q, k) * scale
            scores = torch.where(mask[None, None], scores, -1000000000.0)
            p = _softmax(scores, axis=-1)
            o = torch.einsum('bhqk,bhke->bhqe', p, v)
            o = torch.einsum('bhte,hed->btd', o, Wo)
            mem = o[:, :M, :].clone()
            outs.append(o[:, M:, :])
        return torch.concatenate(outs, axis=1)
    x = A_u
    x = x + _A_ref(_layernorm(x, axis=-1), A_log_A_real, A_A_imag, A_C_real, A_C_imag, A_log_dt, A_D_skip)
    x = x + _B_ref(_layernorm(x, axis=-1), B_mem0, B_Wq, B_Wk, B_Wv, B_Wo, B_seg_len)
    x = x + _A_ref(_layernorm(x, axis=-1), A_log_A_real, A_A_imag, A_C_real, A_C_imag, A_log_dt, A_D_skip)
    x = x + _B_ref(_layernorm(x, axis=-1), B_mem0, B_Wq, B_Wk, B_Wv, B_Wo, B_seg_len)
    return x
```

```cpp
#include <hip/hip_runtime.h>
#include <hip/hip_bf16.h>
#include <stdint.h>

constexpr int L_SEQ = 2048;
constexpr int DM    = 512;
constexpr int NST   = 128;
constexpr int NH    = 32;
constexpr int DH    = 128;
constexpr int HD    = NH * DH;
constexpr int MM    = 8;
constexpr int SEG   = 32;
constexpr int NSEG  = L_SEQ / SEG;
constexpr int LK    = MM + SEG;

typedef unsigned short u16;
using bf16x8 = __attribute__((ext_vector_type(8))) short;
using f32x4  = __attribute__((ext_vector_type(4))) float;
using f32x16 = __attribute__((ext_vector_type(16))) float;

static __device__ __forceinline__ u16 f2bf(float f) {
  unsigned u = __float_as_uint(f);
  u += 0x7fffu + ((u >> 16) & 1u);
  return (u16)(u >> 16);
}
static __device__ __forceinline__ float bf2f(u16 h) { return __uint_as_float(((unsigned)h) << 16); }
static __device__ __forceinline__ float bfr(float f) { return bf2f(f2bf(f)); }

constexpr size_t SZ_X      = (size_t)L_SEQ * DM * 2;
constexpr size_t OFF_KF    = 0;
constexpr size_t OFF_UT    = OFF_KF + (size_t)DM * L_SEQ * 4;
constexpr size_t OFF_YT    = OFF_UT + SZ_X;
constexpr size_t OFF_XA    = OFF_YT + SZ_X;
constexpr size_t OFF_H     = OFF_XA + SZ_X;
constexpr size_t OFF_X2    = OFF_H + SZ_X;
constexpr size_t OFF_U2    = OFF_X2 + SZ_X;
constexpr size_t OFF_X3    = OFF_U2 + SZ_X;
constexpr size_t SZ_QK     = (size_t)L_SEQ * 2 * HD * 2;
constexpr size_t OFF_QK    = OFF_X3 + SZ_X;
constexpr size_t SZ_O      = (size_t)L_SEQ * HD * 2;
constexpr size_t OFF_VF    = OFF_QK + 2 * SZ_QK;
constexpr size_t SZ_PW     = (size_t)2 * NSEG * 16 * SEG * DM * 2;
constexpr size_t OFF_PW    = OFF_VF + 2 * SZ_O;
constexpr size_t SZ_MEM    = (size_t)NSEG * MM * HD * 2;
constexpr size_t OFF_MEMK  = OFF_PW + SZ_PW;
constexpr size_t OFF_MEMV  = OFF_MEMK + 2 * SZ_MEM;
constexpr size_t OFF_GT    = OFF_MEMV + 2 * SZ_MEM;
constexpr size_t SZ_ACC    = (size_t)2 * 32768;
constexpr size_t OFF_Z     = OFF_GT + (size_t)64 * DM * 4;
constexpr size_t OFF_ACC   = OFF_Z;
constexpr size_t OFF_FL    = OFF_ACC + 2 * SZ_ACC;
constexpr int FL_FK  = 0;
constexpr int FL_FW  = FL_FK + 2 * 64 * 32 * 32;
constexpr int FL_FR  = FL_FW + 2 * 64 * 32 * 32;
constexpr int FL_FC  = FL_FR + 64 * 32 * 32;
constexpr int FL_FQ  = FL_FC + 2 * 64 * 64 * 32;
constexpr int FL_FS  = FL_FQ + 2 * 64 * 32 * 32;
constexpr int FL_F0  = FL_FS + 8 * 32 * 32;
constexpr int FL_FZ  = FL_F0 + 8 * 8 * 32;
constexpr int FL_FN  = FL_FZ + 16 * 32;
constexpr int FL_KG  = FL_FN + 2 * 64 * 16 * 32;
constexpr int FL_G   = FL_KG + 64 * 32;
constexpr int FL_TK  = FL_G + 2 * 8 * 32;
constexpr int FL_TMO = FL_TK + 2 * 256;
constexpr int FL_END = FL_TMO + 64;
constexpr size_t Z_BYTES   = OFF_FL + (size_t)FL_END * 4 - OFF_Z;
constexpr size_t WS_TOTAL  = OFF_Z + Z_BYTES;
static_assert(Z_BYTES % 16 == 0 && OFF_Z % 256 == 0, "zero region");
static_assert(WS_TOTAL + (4 << 20) <= (size_t)256 * 1024 * 1024, "workspace too large");

constexpr int CKN = 2208;
static_assert((size_t)DM * 2 * CKN * 2 <= (size_t)DM * L_SEQ * 4 + SZ_X, "K buffer");
__device__ __forceinline__ float pos_of_index(int p) {
  if (p < 256) return (float)p;
  if (p < 384) return (float)(256 + 2 * (p - 256));
  if (p < 512) return (float)(512 + 4 * (p - 384));
  if (p < 640) return (float)(1024 + 8 * (p - 512));
  return 2048.0f;
}
__device__ __forceinline__ int index_of_l(int l) {
  float pf = bfr((float)l);
  int p = (int)pf;
  if (p < 256) return p;
  if (p < 512) return 256 + ((p - 256) >> 1);
  if (p < 1024) return 384 + ((p - 512) >> 2);
  if (p < 2048) return 512 + ((p - 1024) >> 3);
  return 640;
}

template <int CTRL> __device__ __forceinline__ float dppr(float x) {
  return __builtin_bit_cast(float, __builtin_amdgcn_update_dpp(0, __builtin_bit_cast(int, x), CTRL, 0xf, 0xf, false));
}
__device__ __forceinline__ float wave_sum(float v) {
  v += dppr<0x121>(v);
  v += dppr<0x122>(v);
  v += dppr<0x124>(v);
  v += dppr<0x128>(v);
  {
    auto r = __builtin_amdgcn_permlane16_swap(__float_as_uint(v), __float_as_uint(v), false, false);
    v = __uint_as_float(r[0]) + __uint_as_float(r[1]);
  }
  {
    auto r = __builtin_amdgcn_permlane32_swap(__float_as_uint(v), __float_as_uint(v), false, false);
    v = __uint_as_float(r[0]) + __uint_as_float(r[1]);
  }
  return v;
}
__device__ __forceinline__ void ln_row8(float (&v)[8]) {
  float s = 0.f;
#pragma unroll
  for (int j = 0; j < 8; ++j) s += v[j];
  s = wave_sum(s);
  const float mean = s * (1.0f / DM);
  float q = 0.f;
#pragma unroll
  for (int j = 0; j < 8; ++j) { float t = v[j] - mean; q += t * t; }
  q = wave_sum(q);
  const float var = q * (1.0f / DM);
  const float mb = bfr(mean);
  const float sd = bfr(sqrtf(bfr(bfr(var) + 1e-5f)));
#pragma unroll
  for (int j = 0; j < 8; ++j) v[j] = bfr(bfr(v[j] - mb) / sd);
}

constexpr int ZBLK = 512;
__global__ __launch_bounds__(256) void zero_kernel(uint4* __restrict__ zero) {
  for (size_t i = (size_t)blockIdx.x * 256 + threadIdx.x; i < Z_BYTES / 16; i += (size_t)ZBLK * 256) zero[i] = uint4{0u, 0u, 0u, 0u};
}

#define RLX_AGENT __ATOMIC_RELAXED, __HIP_MEMORY_SCOPE_AGENT
typedef __attribute__((address_space(1))) unsigned gu32;
typedef unsigned v4u __attribute__((ext_vector_type(4)));
constexpr int AUX_SC1 = 16;
constexpr unsigned SPIN_CAP = 1u << 18;

__device__ __forceinline__ bool wait_flags(const unsigned* flags, int lane, int nfl, unsigned want, unsigned* tmo) {
  for (unsigned spins = 0;; ++spins) {
    unsigned v = want;
    if (lane < nfl) v = __hip_atomic_load((gu32*)flags + lane * 32, RLX_AGENT);
    if (__all(v >= want)) break;
    if (spins > SPIN_CAP) {
      if (lane == 0) __hip_atomic_store((gu32*)tmo, 1u, RLX_AGENT);
      return false;
    }
  }
  __builtin_amdgcn_fence(__ATOMIC_ACQUIRE, "wavefront");
  return true;
}

#define STAMP(i)
#define STAMP_DUMP()
#define WSTAMP(i)
template <int CTRL> __device__ __forceinline__ float dppf(float old, float x) {
  return __builtin_bit_cast(float, __builtin_amdgcn_update_dpp(__builtin_bit_cast(int, old), __builtin_bit_cast(int, x), CTRL, 0xf, 0xf, false));
}
__device__ __forceinline__ float wave_max_dpp(float x) {
  const float ninf = -3e38f;
  x = fmaxf(x, dppf<0x111>(ninf, x));
  x = fmaxf(x, dppf<0x112>(ninf, x));
  x = fmaxf(x, dppf<0x114>(ninf, x));
  x = fmaxf(x, dppf<0x118>(ninf, x));
  x = fmaxf(x, dppf<0x142>(ninf, x));
  x = fmaxf(x, dppf<0x143>(ninf, x));
  return __builtin_bit_cast(float, __builtin_amdgcn_readlane(__builtin_bit_cast(int, x), 63));
}
__device__ __forceinline__ float wave_sum_dpp(float x) {
  x += dppf<0x111>(0.f, x);
  x += dppf<0x112>(0.f, x);
  x += dppf<0x114>(0.f, x);
  x += dppf<0x118>(0.f, x);
  x += dppf<0x142>(0.f, x);
  x += dppf<0x143>(0.f, x);
  return __builtin_bit_cast(float, __builtin_amdgcn_readlane(__builtin_bit_cast(int, x), 63));
}
constexpr int SMS = 528;
constexpr int QS = 144;
constexpr int VS = 80;
constexpr int SOS = 144;
#define VAR_NREP 4
constexpr int NREP = VAR_NREP;
__device__ __forceinline__ float xsum16(float x) {
  auto r = __builtin_amdgcn_permlane16_swap(__float_as_uint(x), __float_as_uint(x), false, false);
  return __uint_as_float(r[0]) + __uint_as_float(r[1]);
}
__device__ __forceinline__ float xsum32(float x) {
  auto r = __builtin_amdgcn_permlane32_swap(__float_as_uint(x), __float_as_uint(x), false, false);
  return __uint_as_float(r[0]) + __uint_as_float(r[1]);
}
__device__ __forceinline__ float xmax16(float x) {
  auto r = __builtin_amdgcn_permlane16_swap(__float_as_uint(x), __float_as_uint(x), false, false);
  return fmaxf(__uint_as_float(r[0]), __uint_as_float(r[1]));
}
__device__ __forceinline__ float xmax32(float x) {
  auto r = __builtin_amdgcn_permlane32_swap(__float_as_uint(x), __float_as_uint(x), false, false);
  return fmaxf(__uint_as_float(r[0]), __uint_as_float(r[1]));
}
__device__ __forceinline__ float red_mod4(float x) {
  x += dppf<0x124>(0.f, x);
  x += dppf<0x128>(0.f, x);
  return xsum32(xsum16(x));
}
typedef __bf16 bf16x2_t __attribute__((ext_vector_type(2)));
typedef float f32x2_t __attribute__((ext_vector_type(2)));
__device__ __forceinline__ unsigned pk2(float a, float b) {
  f32x2_t v = {a, b};
  bf16x2_t r = __builtin_convertvector(v, bf16x2_t);
  return __builtin_bit_cast(unsigned, r);
}
__device__ __forceinline__ float dot2sq(const unsigned x, const float acc) {
  return __builtin_amdgcn_fdot2_f32_bf16(__builtin_bit_cast(bf16x2_t, x), __builtin_bit_cast(bf16x2_t, x), acc, false);
}
__device__ __forceinline__ float lo16(unsigned u) { return __uint_as_float(u << 16); }
__device__ __forceinline__ float hi16(unsigned u) { return __uint_as_float(u & 0xffff0000u); }
__device__ __forceinline__ void scale_pair(float a, float b, float scale, float& oa, float& ob) {
  unsigned u = pk2(a, b);
  unsigned v = pk2(lo16(u) * scale, hi16(u) * scale);
  oa = lo16(v);
  ob = hi16(v);
}

#define XSPLIT 1
__device__ __forceinline__ int role_no() {
  const int b = blockIdx.x, x = b & 7, j = b >> 3;
  if (XSPLIT == 1) {
    const int L = x >> 2, xx = x & 3;
    if (j < 8) return 32 * L + 8 * xx + j;
    if (j < 12) return 64 + 16 * L + 4 * xx + j - 8;
    if (j < 16) { const int i = 4 * xx + j - 12; return L ? 240 + i : i < 8 ? 96 + i : 224 + i; }
    if (j < 24) return 104 + 8 * x + j - 16;
    return 168 + 32 * L + 8 * xx + j - 24;
  }
  if (XSPLIT == 2 && j < 8) return 32 * (x >> 2) + 8 * (x & 3) + j;
  if (b < 64) return (b & 32) + 4 * x + (j & 3);
  if (b < 96) return (b & 112) + 2 * x + (j & 1);
  if (b >= 104 && b < 168) return 104 + 8 * x + j - 13;
  if (b >= 168 && b < 232) return 168 + ((b - 168) & 32) + 4 * x + ((j - 21) & 3);
  return b;
}
#define MSTAMP()
#define WSTAMP2()
#define SST(k)
#define TS(k, ss, t)
struct MegaArgs {
  const u16* mem0;
  const u16* Dskip;
  char* ws;
  u16* out;
  const u16* x0;
  const u16 *logAre, *Aim, *Cre, *Cim, *logdt;
  const u16 *Wq, *Wk, *Wv, *Wo;
  int rn;
};

__device__ __forceinline__ int lane_id() {
  int l;
  asm volatile("v_mbcnt_lo_u32_b32 %0, -1, 0\n\tv_mbcnt_hi_u32_b32 %0, -1, %0" : "=v"(l));
  return l;
}
__device__ __forceinline__ bool spin_timeout(unsigned spins, unsigned* tmo) {
  if (spins > SPIN_CAP) {
    if (lane_id() == 0) __hip_atomic_store((gu32*)tmo, 1u, RLX_AGENT);
    return true;
  }
  return false;
}
#define PSWH (h & 2)
#define CD 158
#define GP 230
#define GR 50
#define GM 0
#define ROT (4 * (h & 1))
#define CPRIO 1
#define P1PRIO 3
#define QLEAD 2
#define POLL_SLEEP 12
#define SLP_C POLL_SLEEP
#define SLP_A POLL_SLEEP
#define SLP_W POLL_SLEEP
#define SLP_R POLL_SLEEP
#define SLP_V POLL_SLEEP
#define SLP_Q POLL_SLEEP
#define SLP_M 4
#define AWPRIO 2
#define SLP_S 8
#define SPOS 4
#define CVPRE 6
#define QHV 0
#define QCHK 0
#define LEADN 99
#define GATE_V 200
#define GATE_R 440
#define GATE_N 440
#define AWAHEAD 1
#define GATE_M 190
#define PRI_A 3
#define PRI_WA 2
#define PRI_WB 1
#define AWSKIP 3
#define PRI_0 3
#define AWDEF 0
#define SLP_F 2
#define EXP_OFF 0
#define AWSPEC 1
#define CVTHR 48
#define NR_C 8
#define NR_R 8
#define NR_W 8
#define NR_N 2
#define QAHEAD 0
#define NS0 4
#define SLP_N POLL_SLEEP
template <int SLP, int WPL>
__device__ __forceinline__ bool wait_n(const unsigned* f, int n, unsigned* tmo) {
  const int l = lane_id();
  for (unsigned spins = 0;; ++spins) {
    unsigned v = 1u;
    if (l < n) v = __hip_atomic_load((gu32*)f + (l / WPL) * 32 + l % WPL, RLX_AGENT);
    if (__all(v != 0u)) break;
    if (spin_timeout(spins, tmo)) return false;
    __builtin_amdgcn_s_sleep(SLP);
  }
  asm volatile("" ::: "memory");
  return true;
}
__device__ __forceinline__ unsigned now10() { return (unsigned)__builtin_amdgcn_s_memrealtime(); }
__device__ __forceinline__ void gate(const unsigned t) {
  for (int i = 0; i < 64; ++i) {
    const int d = (int)(t - now10());
    if (d <= 0) break;
    if (d > 100) __builtin_amdgcn_s_sleep(32);
    else if (d > 25) __builtin_amdgcn_s_sleep(8);
    else __builtin_amdgcn_s_sleep(2);
  }
}
template <int SLP, int WPL, int GT>
__device__ __forceinline__ void grp_wait(const bool leader, volatile unsigned* sw, const unsigned val, const unsigned* f, int n,
                                         unsigned* tmo, unsigned& tl) {
  if (leader) {
    const int l = lane_id();
    for (unsigned spins = 0;; ++spins) {
      unsigned v = 1u;
      if (l < n) v = __hip_atomic_load((gu32*)f + (l / WPL) * 32 + l % WPL, RLX_AGENT);
      if (__all(v != 0u) || spin_timeout(spins, tmo)) break;
      if (GT && spins == 0) gate(tl);
      else __builtin_amdgcn_s_sleep(SLP);
    }
    if (GT) tl = now10() + GT;
    if ((threadIdx.x & 63) == 0) *sw = val;
  } else {
    for (unsigned spins = 0; *sw < val; ++spins) {
      if (spins > 64u * SPIN_CAP) break;
      if (GT && spins == 0) gate(tl);
      __builtin_amdgcn_s_sleep(SLP_F);
    }
    if (GT) tl = now10() + GT;
  }
  asm volatile("" ::: "memory");
}
template <int SLP, int WPL>
__device__ __forceinline__ bool wg_wait(const unsigned* f, int n, unsigned* tmo) {
  if (threadIdx.x < 64) (void)wait_n<SLP, WPL>(f, n, tmo);
  __syncthreads();
  asm volatile("" ::: "memory");
  return true;
}
__device__ __forceinline__ void set_flag(unsigned* f) { __hip_atomic_store((gu32*)f, 1u, RLX_AGENT); }
#define DRAIN() asm volatile("s_waitcnt vmcnt(0)" ::: "memory")
#define MKRS(ptr, bytes) __builtin_amdgcn_make_buffer_rsrc((void*)(ptr), 0, (int)(bytes), 0x00020000)
typedef unsigned v2u __attribute__((ext_vector_type(2)));
#define LD128(rs, voff, soff) __builtin_amdgcn_raw_buffer_load_b128(rs, voff, soff, AUX_SC1)
#define LD64(rs, voff, soff) __builtin_amdgcn_raw_buffer_load_b64(rs, voff, soff, AUX_SC1)
#define ST128(v, rs, voff, soff)                                              \
  do {                                                                        \
    const v4u st_d_ = (v);                                                    \
    __builtin_amdgcn_raw_buffer_store_b128(st_d_, rs, voff, soff, AUX_SC1);   \
    asm volatile("s_nop 1" ::"v"(st_d_) : "memory");                          \
  } while (0)
#define ST64(v, rs, voff, soff) __builtin_amdgcn_raw_buffer_store_b64(v, rs, voff, soff, AUX_SC1)
#define BF8(x) __builtin_bit_cast(bf16x8, x)
#define MFMA16(a, b, c) __builtin_amdgcn_mfma_f32_16x16x32_bf16(a, b, c, 0, 0, 0)
typedef short bf16x4 __attribute__((ext_vector_type(4)));
#define MFMA16K(a, b, c) \
  __builtin_amdgcn_mfma_f32_16x16x16bf16_1k(__builtin_bit_cast(bf16x4, a), __builtin_bit_cast(bf16x4, b), c, 0, 0, 0)
__device__ __forceinline__ float fmax_nn(float a, float b) { return __builtin_elementwise_maximum(a, b); }
typedef __bf16 bfv8 __attribute__((ext_vector_type(8)));
typedef __bf16 bfv16 __attribute__((ext_vector_type(16)));
typedef unsigned v8u __attribute__((ext_vector_type(8)));
#define SMF(a, b, c, idx) \
  __builtin_amdgcn_smfmac_f32_16x16x64_bf16(__builtin_bit_cast(bfv8, a), __builtin_bit_cast(bfv16, b), c, idx, 0, 0)
__device__ __forceinline__ float swap_add32(float a, float b) {
  auto r = __builtin_amdgcn_permlane32_swap(__float_as_uint(a), __float_as_uint(b), false, false);
  return __uint_as_float(r[0]) + __uint_as_float(r[1]);
}
__device__ __forceinline__ float swap_add16(float a, float b) {
  auto r = __builtin_amdgcn_permlane16_swap(__float_as_uint(a), __float_as_uint(b), false, false);
  return __uint_as_float(r[0]) + __uint_as_float(r[1]);
}
__device__ __forceinline__ v4u smf_fix(const v4u d) {
  auto x = __builtin_amdgcn_permlane16_swap(d.x, d.z, false, false);
  auto y = __builtin_amdgcn_permlane16_swap(d.y, d.w, false, false);
  return v4u{x[0], x[1], y[0], y[1]};
}
__device__ __forceinline__ v8u smf_bfrag(const u16* p) {
  const v2u a = *reinterpret_cast<const v2u*>(p), b = *reinterpret_cast<const v2u*>(p + 8);
  const v2u c = *reinterpret_cast<const v2u*>(p + 32), d = *reinterpret_cast<const v2u*>(p + 40);
  return v8u{a.x, b.x, a.y, b.y, c.x, d.x, c.y, d.y};
}

#define VAR_PPA 0
#define VAR_PPB 0
#define VAR_THR 0
#define VAR_THR2 0
#define CV_LEAD 100
#define VAR_EARLY 1
#define VAR_DRAIN 1
#define VAR_D1 0
#define VAR_D2 0
#define VAR_DR2 1
#define VAR_S1 0
#define VAR_S2 0
typedef __attribute__((address_space(1))) unsigned long long gu64;
typedef __attribute__((address_space(1))) char gch;
__device__ __forceinline__ int cvt_rpi(float x) {
  int r;
  asm("v_cvt_rpi_i32_f32 %0, %1" : "=v"(r) : "v"(x));
  return r;
}
typedef short v4s __attribute__((ext_vector_type(4)));
typedef __attribute__((address_space(3))) v4s* lp4s;
__device__ __forceinline__ void glds16(const void* sbase, const unsigned voff, const unsigned lds) {
  unsigned keep;
  asm volatile("s_nop 4\n\ts_mov_b32 %0, m0\n\ts_mov_b32 m0, %3\n\ts_nop 0\n\tglobal_load_lds_dwordx4 %1, %2\n\ts_mov_b32 m0, %0"
               : "=&s"(keep)
               : "v"(voff), "s"(sbase), "s"(lds)
               : "memory");
}
__device__ __forceinline__ void glds16c(const void* sbase, const unsigned voff, const unsigned lds) {
  unsigned keep;
  asm volatile("s_nop 4\n\ts_mov_b32 %0, m0\n\ts_mov_b32 m0, %3\n\ts_nop 0\n\tglobal_load_lds_dwordx4 %1, %2 sc1\n\ts_mov_b32 m0, %0"
               : "=&s"(keep)
               : "v"(voff), "s"(sbase), "s"(lds)
               : "memory");
}
__device__ __forceinline__ v2u trd(const unsigned a) {
  return __builtin_bit_cast(v2u, __builtin_amdgcn_ds_read_tr16_b64_v4i16((lp4s)(unsigned long)a));
}
__device__ __forceinline__ unsigned lds_base(char* smem) { return (unsigned)(unsigned long)(__attribute__((address_space(3))) char*)smem; }
#define RAW_BAR()                                           \
  do {                                                      \
    asm volatile("s_waitcnt lgkmcnt(0)" ::: "memory");      \
    __builtin_amdgcn_s_barrier();                           \
    asm volatile("" ::: "memory");                          \
  } while (0)
#define WAIT_VM(n) asm volatile("s_waitcnt vmcnt(" #n ")" ::: "memory")
template <bool SP>
__device__ __forceinline__ void wqkv_issue(const u16* Wm, const int hf, const int h, const int w, const int l, const unsigned sb) {
  const char* base = (const char*)Wm + (size_t)(256 * hf + 32 * w) * (HD * 2) + h * (DH * 2);
#pragma unroll
  for (int i = 0; i < 8; ++i) {
    const int r = l >> 4;
    const int f = SP ? ((r & 1) | ((i & 1) << 1) | (((i >> 1) & 1) << 2)) : (r | (((i >> 1) & 1) << 2));
    glds16(base, (unsigned)((4 * i + r) * (HD * 2) + (((l & 15) ^ (f << 1)) << 4)), sb + w * 16384 + hf * 8192 + i * 1024);
  }
}

__device__ __forceinline__ unsigned row_stats(const v4u raw) {
  float v[8];
#pragma unroll
  for (int e = 0; e < 4; ++e) { v[2 * e] = lo16(raw[e]); v[2 * e + 1] = hi16(raw[e]); }
  float s = 0.f;
#pragma unroll
  for (int e = 0; e < 8; ++e) s += v[e];
  const float mean = wave_sum(s) * (1.0f / DM);
  float q = 0.f;
#pragma unroll
  for (int e = 0; e < 8; ++e) { const float t = v[e] - mean; q += t * t; }
  const float var = wave_sum(q) * (1.0f / DM);
  const float sd = bfr(sqrtf(bfr(bfr(var) + 1e-5f)));
  return (unsigned)f2bf(mean) | ((unsigned)f2bf(sd) << 16);
}
__device__ __forceinline__ void start_kgen(const MegaArgs& A, char* smem, const int hi, const int xi) {
  const int tid = threadIdx.x, j = tid >> 7, u = tid & 127, d = 4 * hi + j;
  const int w = __builtin_amdgcn_readfirstlane(tid >> 6), l = tid & 63;
  float* prm = reinterpret_cast<float*>(smem) + j * 10 * NST;
  float* part = reinterpret_cast<float*>(smem) + 40 * NST + j * 8 * 400;
  float* kd = reinterpret_cast<float*>(smem) + 40 * NST + 32 * 400 + j * 392;
  v4u raw[8], ra, rb;
  const int r0 = 16 * xi + 2 * w;
  if (xi >= 0) {
    ra = *reinterpret_cast<const v4u*>(A.x0 + (size_t)r0 * DM + l * 8);
    rb = *reinterpret_cast<const v4u*>(A.x0 + (size_t)(r0 + 1) * DM + l * 8);
  }
  const u16 p0 = A.logdt[d], p1 = A.logAre[d * NST + u], p2 = A.Aim[d * NST + u], p3 = A.Cre[d * NST + u], p4 = A.Cim[d * NST + u];
  if (w >= 6) {
#pragma unroll
    for (int i = 0; i < 8; ++i) raw[i] = *reinterpret_cast<const v4u*>(A.x0 + (size_t)(16 * hi + 8 * (w - 6) + i) * DM + l * 8);
  }
  if (xi >= 0) {
    const unsigned sa = row_stats(ra), sb = row_stats(rb);
    if (l < 2) __hip_atomic_store((gu32*)(A.ws + OFF_GT) + r0 + l, l ? sb : sa, RLX_AGENT);
  }
  {
    const float dt = bfr(expf(bf2f(p0)));
    const float ar = -bfr(expf(bf2f(p1))) * dt, ai = bf2f(p2) * dt;
    prm[u] = ar;
    prm[NST + u] = ai;
    prm[2 * NST + u] = bf2f(p3) * dt;
    prm[3 * NST + u] = bf2f(p4) * dt;
#pragma unroll
    for (int cls = 1; cls < 4; ++cls) {
      const float st = (float)(1 << cls);
      const float e = expf(ar * st);
      float sn, cs;
      sincosf(ai * st, &sn, &cs);
      prm[(2 + 2 * cls) * NST + u] = e * cs;
      prm[(3 + 2 * cls) * NST + u] = e * sn;
    }
  }
  if (xi >= 0) {
    DRAIN();
    if (l < 8) set_flag((unsigned*)(A.ws + OFF_FL) + FL_F0 + (l * 8 + xi) * 32 + w);
    SST(11);
  }
  __syncthreads();
  SST(1);
  if (w >= 6) {
    unsigned stw = 0u;
#pragma unroll
    for (int i = 0; i < 8; ++i) {
      const unsigned t = row_stats(raw[i]);
      if (l == i) stw = t;
    }
    const int sx = 2 * hi + w - 6;
    if (l < 8) __hip_atomic_store((gu32*)(A.ws + OFF_GT) + 8 * sx + l, stw, RLX_AGENT);
    DRAIN();
    if (l < 8) set_flag((unsigned*)(A.ws + OFF_FL) + FL_FS + (l * 32 + (sx >> 3)) * 32 + (sx & 7));
    if (w == 6 && hi < 64) {
      unsigned* FL = (unsigned*)(A.ws + OFF_FL);
      const int s = hi >> 5, r = hi & 31;
      (void)wait_n<4, 8>(FL + FL_FC + (s * 8 + (blockIdx.x & (NR_C - 1))) * 8 * 32, 64, FL + FL_TMO);
      const unsigned st = row_stats(LD128(MKRS(A.ws + OFF_XA, SZ_X), l * 512, s * 32768 + r * 16));
      const unsigned long long v = ((unsigned long long)__float_as_uint(1.0f / hi16(st)) << 32) | ((st << 16) | 1u);
      if (l < NR_N) __hip_atomic_store((gu64*)(FL + FL_FN + (s * 8 + l) * 64) + r, v, RLX_AGENT);
    }
  } else {
    const int jm = tid / 96, um = tid % 96;
    const int r = um % 12, ng = um / 12;
    const float* prm = reinterpret_cast<float*>(smem) + jm * 10 * NST;
    float* part = reinterpret_cast<float*>(smem) + 40 * NST + jm * 8 * 400;
    const float* pw = prm + (4 + 2 * (r >> 2)) * NST;
    const float p0 = pos_of_index(256 + 32 * r);
    f32x2_t acc[33];
#pragma unroll
    for (int i = 0; i < 33; ++i) acc[i] = f32x2_t{0.f, 0.f};
#pragma unroll 1
    for (int bt = 0; bt < 4; ++bt) {
      const int n = 16 * ng + 4 * bt;
      const f32x4 ar = *reinterpret_cast<const f32x4*>(prm + n), ai = *reinterpret_cast<const f32x4*>(prm + NST + n);
      const f32x4 cr = *reinterpret_cast<const f32x4*>(prm + 2 * NST + n), ci = *reinterpret_cast<const f32x4*>(prm + 3 * NST + n);
      const f32x4 wr = *reinterpret_cast<const f32x4*>(pw + n), wi = *reinterpret_cast<const f32x4*>(pw + NST + n);
      float yr0[4], yi0[4];
#pragma unroll
      for (int k = 0; k < 4; ++k) {
        const float e = __expf(ar[k] * p0);
        float sn, cs;
        __sincosf(ai[k] * p0, &sn, &cs);
        const float zr = e * cs, zi = e * sn;
        yr0[k] = cr[k] * zr - ci[k] * zi;
        yi0[k] = cr[k] * zi + ci[k] * zr;
      }
      f32x2_t yra = {yr0[0], yr0[1]}, yia = {yi0[0], yi0[1]}, wra = {wr[0], wr[1]}, wia = {wi[0], wi[1]};
      f32x2_t yrb = {yr0[2], yr0[3]}, yib = {yi0[2], yi0[3]}, wrb = {wr[2], wr[3]}, wib = {wi[2], wi[3]};
#pragma unroll
      for (int i = 0; i < 33; ++i) {
        acc[i] += yra + yrb;
        const f32x2_t ta = yra * wra - yia * wia, tb = yrb * wrb - yib * wib;
        yia = yra * wia + yia * wra;
        yib = yrb * wib + yib * wrb;
        yra = ta;
        yrb = tb;
      }
    }
#pragma unroll
    for (int i = 0; i < 33; ++i) part[ng * 400 + 33 * r + i] = acc[i].x + acc[i].y;
  }
  __syncthreads();
  SST(2);
  for (int i = u; i < 385; i += 128) {
    const int o = i < 384 ? 33 * (i >> 5) + (i & 31) : 33 * 11 + 32;
    float v = 0.f;
#pragma unroll
    for (int g = 0; g < 8; ++g) v += part[g * 400 + o];
    kd[i] = 2.0f * v;
  }
  __syncthreads();
  const __amdgpu_buffer_rsrc_t rs = MKRS(A.ws + OFF_KF, DM * 2 * CKN * 2);
  for (int un = u; un < 229; un += 128) {
    v4u oh, ol;
#pragma unroll
    for (int e = 0; e < 4; ++e) {
      float kv[2];
#pragma unroll
      for (int k = 0; k < 2; ++k) {
        const int lag = 2080 - (8 * un + 2 * e + k);
        kv[k] = (lag >= 256 && lag < L_SEQ) ? kd[index_of_l(lag) - 256] : 0.f;
      }
      oh[e] = pk2(kv[0], kv[1]);
      ol[e] = pk2(kv[0] - lo16(oh[e]), kv[1] - hi16(oh[e]));
    }
    ST128(oh, rs, (d * 2 * CKN + 8 * un) * 2, 0);
    ST128(ol, rs, (d * 2 * CKN + CKN + 8 * un) * 2, 0);
  }
  DRAIN();
  __syncthreads();
  if (tid == 0) set_flag((unsigned*)(A.ws + OFF_FL) + FL_KG + (hi >> 1) * 32 + (hi & 1));
  SST(3);
}

constexpr int C_SM = 131072, C_SO = C_SM + 8192, C_KT = C_SO + 2048, C_X = C_KT + 16384, C_SQ = C_X + 1024;
constexpr int C_SK = C_SQ + (7 * QS + 128) * 2;
static_assert(C_SK + 8 * QS * 2 <= 163840, "lds");
#define PSW 1
#define CHAIN_ASM_ATT \
  "s_mov_b32 s92, 1152\n" \
  "s_mov_b32 s93, 576\n" \
  "s_mov_b32 s94, 2272\n" \
  "s_mov_b32 s95, 272\n" \
  "s_mov_b32 s96, 288\n" \
  "s_mov_b32 s91, 0x40000001\n" \
  "s_mov_b32 s84, 0x4b400000\n" \
  "s_mov_b32 s85, 0x4b400000\n" \
  "s_mov_b32 s78, 0x3e0293ee\n" \
  "s_mov_b32 s79, 0x3fb8aa3b\n" \
  "s_mov_b32 s76, 0xff00ff00\n" \
  "s_mov_b32 s77, 0xff00ff00\n" \
  "s_add_u32 s58, s56, s60\n" \
  "s_addc_u32 s59, s57, 0\n" \
  "s_xor_b32 s38, s35, 0\n" \
  "s_add_u32 s26, s58, s38\n" \
  "s_addc_u32 s27, s59, 0\n" \
  "s_xor_b32 s38, s35, 1024\n" \
  "s_add_u32 s28, s58, s38\n" \
  "s_addc_u32 s29, s59, 0\n" \
  "s_xor_b32 s38, s35, 2048\n" \
  "s_add_u32 s30, s58, s38\n" \
  "s_addc_u32 s31, s59, 0\n" \
  "s_xor_b32 s38, s35, 3072\n" \
  "s_add_u32 s32, s58, s38\n" \
  "s_addc_u32 s33, s59, 0\n" \
  "s_lshl_b32 s98, s73, 10\n" \
  "s_add_u32 s98, s98, s74\n" \
  "s_add_u32 s98, s98, 131072\n" \
  "s_lshl_b32 s97, s73, 5\n" \
  "s_add_u32 s97, s97, s74\n" \
  "s_add_u32 s97, s97, 158720\n" \
  "s_lshl_b32 s99, s73, 10\n" \
  "v_mbcnt_lo_u32_b32 v48, -1, 0\n" \
  "v_mbcnt_hi_u32_b32 v48, -1, v48\n" \
  "v_and_b32 v49, 15, v48\n" \
  "v_lshl_add_u32 v8, v49, 1, s97\n" \
  "v_bfe_u32 v49, v48, 4, 1\n" \
  "v_mad_u32_u24 v8, v49, s92, v8\n" \
  "v_lshrrev_b32 v49, 5, v48\n" \
  "v_mad_u32_u24 v9, v49, s93, v8\n" \
  "v_mad_u32_u24 v8, v49, s94, v8\n" \
  "v_add_u32 v10, s99, v1\n" \
  "s_lshr_b32 s88, s99, 2\n" \
  "s_add_u32 s88, s88, s74\n" \
  "s_add_u32 s88, s88, 139264\n" \
  "v_lshl_add_u32 v7, v49, 10, s88\n" \
  "v_and_b32 v49, 31, v48\n" \
  "v_lshl_add_u32 v7, v49, 3, v7\n" \
  "v_mov_b64 v[12:13], 0\n" \
  "v_mov_b64 v[14:15], 0\n" \
  "v_mov_b64 v[16:17], 0\n" \
  "v_mov_b64 v[18:19], 0\n" \
  "v_mov_b64 v[20:21], 0\n" \
  "v_mov_b64 v[22:23], 0\n" \
  "s_add_u32 s88, s74, 157760\n" \
  "v_mov_b32 v59, s88\n" \
  "v_mov_b32 v24, 0x44870000\n" \
  "v_mul_f32 v24, s72, v24\n" \
  "s_nop 0\n" \
  "v_readfirstlane_b32 s86, v24\n" \
  "s_lshr_b32 s86, s86, 23\n" \
  "s_addk_i32 s86, 0xff83\n" \
  "s_ashr_i32 s86, s86, 1\n" \
  "s_max_i32 s86, s86, -20\n" \
  "s_min_i32 s86, s86, 100\n" \
  "s_mov_b32 s82, s86\n" \
  "s_lshr_b32 s83, s72, 23\n" \
  "s_sub_i32 s83, s83, 242\n" \
  "s_lshl_b32 s88, s73, 10\n" \
  "v_add_u32 v24, s88, v1\n" \
  "ds_read_b128 v[32:35], v24 offset:0\n" \
  "s_waitcnt lgkmcnt(0)\n" \
  "v_mov_b32 v11, 0\n" \
  "v_dot2c_f32_bf16 v11, v32, v32\n" \
  "v_dot2c_f32_bf16 v11, v33, v33\n" \
  "v_dot2c_f32_bf16 v11, v34, v34\n" \
  "v_dot2c_f32_bf16 v11, v35, v35\n" \
  "s_nop 1\n" \
  "s_nop 0\n" \
  "v_cmpx_lt_f32 vcc, 2.0, v11\n" \
  "ds_max_u32 v59, v11\n" \
  "s_mov_b64 exec, -1\n" \
  ".LCA_TOP_%=:\n" \
  "s_waitcnt lgkmcnt(0)\n" \
  "s_barrier\n" \
  "ds_read_b128 v[40:43], v1 offset:0\n" \
  "ds_read_b128 v[44:47], v1 offset:1024\n" \
  "ds_read_b128 v[48:51], v1 offset:2048\n" \
  "ds_read_b128 v[52:55], v1 offset:3072\n" \
  "ds_read_b128 v[56:59], v1 offset:4096\n" \
  "ds_read_b128 v[60:63], v1 offset:5120\n" \
  "s_cmp_lg_u32 s73, 0\n" \
  "s_cbranch_scc1 .LCA_NOZ_%=\n" \
  "s_xor_b32 s88, s60, 0x8000\n" \
  "s_add_u32 s88, s88, s61\n" \
  "buffer_store_dwordx2 v[12:13], v5, s[40:43], s88 offen sc1\n" \
  ".LCA_NOZ_%=:\n" \
  "s_waitcnt lgkmcnt(5)\n" \
  "v_smfmac_f32_16x16x64_bf16 v[20:23], v[40:43], v[192:199], v0\n" \
  "v_smfmac_f32_16x16x64_bf16 v[12:15], v[40:43], v[64:71], v0\n" \
  "v_smfmac_f32_16x16x64_bf16 v[16:19], v[40:43], v[128:135], v0\n" \
  "ds_read_b128 v[40:43], v1 offset:6144\n" \
  "s_waitcnt lgkmcnt(5)\n" \
  "v_smfmac_f32_16x16x64_bf16 v[20:23], v[44:47], v[200:207], v0\n" \
  "v_smfmac_f32_16x16x64_bf16 v[12:15], v[44:47], v[72:79], v0\n" \
  "v_smfmac_f32_16x16x64_bf16 v[16:19], v[44:47], v[136:143], v0\n" \
  "ds_read_b128 v[44:47], v1 offset:7168\n" \
  "s_waitcnt lgkmcnt(5)\n" \
  "v_smfmac_f32_16x16x64_bf16 v[20:23], v[48:51], v[208:215], v0\n" \
  "v_smfmac_f32_16x16x64_bf16 v[12:15], v[48:51], v[80:87], v0\n" \
  "v_smfmac_f32_16x16x64_bf16 v[16:19], v[48:51], v[144:151], v0\n" \
  "ds_read_b128 v[24:27], v1 offset:10240\n" \
  "ds_read_b128 v[28:31], v1 offset:14336\n" \
  "s_waitcnt lgkmcnt(6)\n" \
  "v_smfmac_f32_16x16x64_bf16 v[20:23], v[52:55], v[216:223], v0\n" \
  "v_smfmac_f32_16x16x64_bf16 v[12:15], v[52:55], v[88:95], v0\n" \
  "v_smfmac_f32_16x16x64_bf16 v[16:19], v[52:55], v[152:159], v0\n" \
  "ds_read_b128 v[32:35], v1 offset:11264\n" \
  "ds_read_b128 v[36:39], v1 offset:15360\n" \
  "s_waitcnt lgkmcnt(7)\n" \
  "v_smfmac_f32_16x16x64_bf16 v[20:23], v[56:59], v[224:231], v0\n" \
  "v_smfmac_f32_16x16x64_bf16 v[12:15], v[56:59], v[96:103], v0\n" \
  "v_smfmac_f32_16x16x64_bf16 v[16:19], v[56:59], v[160:167], v0\n" \
  "s_waitcnt lgkmcnt(6)\n" \
  "v_smfmac_f32_16x16x64_bf16 v[20:23], v[60:63], v[232:239], v0\n" \
  "v_smfmac_f32_16x16x64_bf16 v[12:15], v[60:63], v[104:111], v0\n" \
  "v_smfmac_f32_16x16x64_bf16 v[16:19], v[60:63], v[168:175], v0\n" \
  "s_waitcnt lgkmcnt(5)\n" \
  "v_smfmac_f32_16x16x64_bf16 v[20:23], v[40:43], v[240:247], v0\n" \
  "v_smfmac_f32_16x16x64_bf16 v[12:15], v[40:43], v[112:119], v0\n" \
  "v_smfmac_f32_16x16x64_bf16 v[16:19], v[40:43], v[176:183], v0\n" \
  "s_waitcnt lgkmcnt(4)\n" \
  "v_smfmac_f32_16x16x64_bf16 v[20:23], v[44:47], v[248:255], v0\n" \
  "v_smfmac_f32_16x16x64_bf16 v[12:15], v[44:47], v[120:127], v0\n" \
  "v_smfmac_f32_16x16x64_bf16 v[16:19], v[44:47], v[184:191], v0\n" \
  "s_cmp_eq_u32 s71, 0\n" \
  "s_nop 4\n" \
  "v_permlane32_swap_b32 v20, v22\n" \
  "v_permlane32_swap_b32 v21, v23\n" \
  "v_pk_add_f32 v[20:21], v[20:21], v[22:23]\n" \
  "v_permlane32_swap_b32 v12, v16\n" \
  "v_permlane32_swap_b32 v13, v17\n" \
  "v_cvt_pk_bf16_f32 v20, v20, v21\n" \
  "v_pk_add_f32 v[12:13], v[12:13], v[16:17]\n" \
  "v_permlane32_swap_b32 v14, v18\n" \
  "v_permlane32_swap_b32 v15, v19\n" \
  "ds_write_b16 v9, v20 offset:128\n" \
  "v_cvt_pk_bf16_f32 v12, v12, v13\n" \
  "v_pk_add_f32 v[14:15], v[14:15], v[18:19]\n" \
  "ds_write_b16_d16_hi v9, v20 offset:416\n" \
  "ds_write_b16 v8, v12\n" \
  "v_cvt_pk_bf16_f32 v14, v14, v15\n" \
  "ds_write_b16_d16_hi v8, v12 offset:288\n" \
  "ds_write_b16 v8, v14 offset:576\n" \
  "ds_write_b16_d16_hi v8, v14 offset:864\n" \
  "s_setprio 3\n" \
  "s_waitcnt lgkmcnt(0)\n" \
  "s_barrier\n" \
  "s_cbranch_scc1 .LCA_LAST_%=\n" \
  "ds_read_b32 v11, v1 offset:26688\n" \
  "ds_read_b128 v[40:43], v2 offset:0\n" \
  "ds_read_b128 v[44:47], v3 offset:0\n" \
  "ds_read_b128 v[48:51], v2 offset:64\n" \
  "ds_read_b128 v[52:55], v3 offset:64\n" \
  "ds_read_b128 v[56:59], v2 offset:128\n" \
  "ds_read_b128 v[60:63], v3 offset:128\n" \
  "s_waitcnt lgkmcnt(5)\n" \
  "v_mfma_f32_16x16x32_bf16 v[12:15], v[24:27], v[40:43], 0\n" \
  "v_mfma_f32_16x16x32_bf16 v[16:19], v[28:31], v[40:43], 0\n" \
  "v_readfirstlane_b32 s86, v11\n" \
  "ds_read_b128 v[24:27], v1 offset:12288\n" \
  "ds_read_b128 v[28:31], v1 offset:16384\n" \
  "s_waitcnt lgkmcnt(6)\n" \
  "v_mfma_f32_16x16x32_bf16 v[20:23], v[44:47], v[40:43], 0\n" \
  "s_lshr_b32 s86, s86, 23\n" \
  "s_add_i32 s86, s86, s83\n" \
  "ds_read_b128 v[40:43], v2 offset:192\n" \
  "ds_read_b128 v[44:47], v3 offset:192\n" \
  "s_waitcnt lgkmcnt(7)\n" \
  "v_mfma_f32_16x16x32_bf16 v[12:15], v[32:35], v[48:51], v[12:15]\n" \
  "v_mfma_f32_16x16x32_bf16 v[16:19], v[36:39], v[48:51], v[16:19]\n" \
  "s_ashr_i32 s86, s86, 1\n" \
  "s_max_i32 s86, s86, s82\n" \
  "ds_read_b128 v[32:35], v1 offset:13312\n" \
  "ds_read_b128 v[36:39], v1 offset:17408\n" \
  "s_waitcnt lgkmcnt(8)\n" \
  "v_mfma_f32_16x16x32_bf16 v[20:23], v[52:55], v[48:51], v[20:23]\n" \
  "s_min_i32 s86, s86, 100\n" \
  "s_lshl_b32 s86, s86, 23\n" \
  "s_sub_i32 s84, 0x4a800000, s86\n" \
  "ds_read_b128 v[48:51], v10 offset:18432\n" \
  "ds_read_b128 v[52:55], v10 offset:22528\n" \
  "s_waitcnt lgkmcnt(8)\n" \
  "v_mfma_f32_16x16x32_bf16 v[20:23], v[60:63], v[56:59], v[20:23]\n" \
  "s_waitcnt lgkmcnt(6)\n" \
  "v_mfma_f32_16x16x32_bf16 v[12:15], v[24:27], v[56:59], v[12:15]\n" \
  "v_mfma_f32_16x16x32_bf16 v[16:19], v[28:31], v[56:59], v[16:19]\n" \
  "ds_read_b128 v[56:59], v6 offset:0\n" \
  "ds_read_b128 v[60:63], v6 offset:1024\n" \
  "s_waitcnt lgkmcnt(6)\n" \
  "v_mfma_f32_16x16x32_bf16 v[20:23], v[44:47], v[40:43], v[20:23]\n" \
  "ds_read_b128 v[24:27], v6 offset:2048\n" \
  "ds_read_b128 v[28:31], v6 offset:3072\n" \
  "s_waitcnt lgkmcnt(6)\n" \
  "v_mfma_f32_16x16x32_bf16 v[12:15], v[32:35], v[40:43], v[12:15]\n" \
  "v_mfma_f32_16x16x32_bf16 v[16:19], v[36:39], v[40:43], v[16:19]\n" \
  "ds_read_b128 v[32:35], v6 offset:8192\n" \
  "ds_read_b128 v[36:39], v6 offset:9216\n" \
  "s_waitcnt lgkmcnt(6)\n" \
  "v_cndmask_b32 v20, v20, v21, s[76:77]\n" \
  "v_mov_b32 v41, 0\n" \
  "v_mov_b32 v42, 0\n" \
  "s_nop 1\n" \
  "v_cndmask_b32 v12, v12, v16, s[76:77]\n" \
  "v_cndmask_b32 v13, v13, v17, s[76:77]\n" \
  "v_cndmask_b32 v14, v14, v18, s[76:77]\n" \
  "v_cndmask_b32 v15, v15, v19, s[76:77]\n" \
  "v_maximum3_f32 v16, v20, v12, v13\n" \
  "v_maximum3_f32 v16, v16, v14, v15\n" \
  "s_nop 1\n" \
  "v_max_f32_dpp v16, v16, v16 row_ror:8 row_mask:0xf bank_mask:0xf\n" \
  "v_mov_b32 v17, v16\n" \
  "s_nop 1\n" \
  "v_permlane16_swap_b32 v16, v17\n" \
  "v_max_f32 v16, v16, v17\n" \
  "v_mov_b32 v17, v16\n" \
  "s_nop 1\n" \
  "v_permlane32_swap_b32 v16, v17\n" \
  "v_max_f32 v16, v16, v17\n" \
  "v_mul_f32 v16, s78, v16\n" \
  "v_pk_fma_f32 v[12:13], v[12:13], s[78:79], v[16:17] op_sel_hi:[1,0,0] neg_lo:[0,0,1] neg_hi:[0,0,1]\n" \
  "v_pk_fma_f32 v[14:15], v[14:15], s[78:79], v[16:17] op_sel_hi:[1,0,0] neg_lo:[0,0,1] neg_hi:[0,0,1]\n" \
  "v_fma_f32 v20, v20, s78, -v16\n" \
  "v_exp_f32 v12, v12\n" \
  "v_exp_f32 v13, v13\n" \
  "v_exp_f32 v14, v14\n" \
  "v_exp_f32 v15, v15\n" \
  "v_exp_f32 v20, v20\n" \
  "s_barrier\n" \
  "ds_read_b64 v[22:23], v7 offset:0\n" \
  "buffer_load_dword v7, v5, s[16:19], 0 offen sc1\n" \
  "v_cvt_pk_bf16_f32 v44, v12, v13\n" \
  "v_cvt_pk_bf16_f32 v45, v14, v15\n" \
  "v_pk_add_f32 v[8:9], v[12:13], v[14:15]\n" \
  "v_mov_b32_dpp v46, v44 row_ror:8 row_mask:0xf bank_mask:0xf\n" \
  "v_mov_b32_dpp v17, v20 row_ror:8 row_mask:0xf bank_mask:0xf\n" \
  "v_mov_b32_dpp v47, v45 row_ror:8 row_mask:0xf bank_mask:0xf\n" \
  "v_add_f32 v10, v8, v20\n" \
  "v_cvt_pk_bf16_f32 v40, v20, v17\n" \
  "v_add_f32 v10, v10, v9\n" \
  "v_mfma_f32_16x16x32_bf16 v[12:15], v[48:51], v[44:47], 0\n" \
  "v_cvt_pk_bf16_f32 v43, v20, v17\n" \
  "v_add_f32_dpp v10, v10, v10 row_ror:8 row_mask:0xf bank_mask:0xf\n" \
  "v_mfma_f32_16x16x32_bf16 v[16:19], v[52:55], v[44:47], 0\n" \
  "v_mov_b32 v11, v10\n" \
  "s_waitcnt lgkmcnt(0)\n" \
  "v_mfma_f32_16x16x16_bf16 v[12:15], v[22:23], v[40:41], v[12:15]\n" \
  "v_mfma_f32_16x16x16_bf16 v[16:19], v[22:23], v[42:43], v[16:19]\n" \
  "v_permlane16_swap_b32 v10, v11\n" \
  "v_add_f32 v10, v10, v11\n" \
  "ds_read_b128 v[48:51], v6 offset:10240\n" \
  "v_mov_b32 v11, v10\n" \
  "ds_read_b128 v[52:55], v6 offset:11264\n" \
  "v_mov_b64 v[20:21], 0\n" \
  "v_mov_b64 v[22:23], 0\n" \
  "v_permlane32_swap_b32 v10, v11\n" \
  "v_add_f32 v10, v10, v11\n" \
  "v_rcp_f32 v10, v10\n" \
  "s_nop 0\n" \
  "v_pk_mul_f32 v[12:13], v[12:13], v[10:11] op_sel_hi:[1,0]\n" \
  "v_pk_mul_f32 v[14:15], v[14:15], v[10:11] op_sel_hi:[1,0]\n" \
  "v_pk_mul_f32 v[16:17], v[16:17], v[10:11] op_sel_hi:[1,0]\n" \
  "v_pk_mul_f32 v[18:19], v[18:19], v[10:11] op_sel_hi:[1,0]\n" \
  "v_cvt_pk_bf16_f32 v40, v12, v13\n" \
  "v_cvt_pk_bf16_f32 v41, v14, v15\n" \
  "v_cvt_pk_bf16_f32 v42, v16, v17\n" \
  "ds_write_b64 v4, v[40:41]\n" \
  "v_cvt_pk_bf16_f32 v43, v18, v19\n" \
  "ds_write_b64 v4, v[42:43] offset:1024\n" \
  "v_mov_b64 v[12:13], 0\n" \
  "v_mov_b64 v[14:15], 0\n" \
  "v_mov_b64 v[16:17], 0\n" \
  "v_mov_b64 v[18:19], 0\n" \
  "v_mov_b64 v[8:9], 0\n" \
  "v_mov_b64 v[10:11], 0\n" \
  "s_setprio 0\n" \
  "s_waitcnt lgkmcnt(0)\n" \
  "s_barrier\n" \
  "ds_read_b128 v[40:43], v1 offset:8192\n" \
  "ds_read_b128 v[44:47], v1 offset:9216\n" \
  "s_waitcnt lgkmcnt(1)\n" \
  "v_smfmac_f32_16x16x64_bf16 v[12:15], v[40:43], v[56:63], v0\n" \
  "ds_read_b128 v[56:59], v6 offset:4096\n" \
  "ds_read_b128 v[60:63], v6 offset:5120\n" \
  "v_smfmac_f32_16x16x64_bf16 v[16:19], v[40:43], v[24:31], v0\n" \
  "ds_read_b128 v[24:27], v6 offset:6144\n" \
  "ds_read_b128 v[28:31], v6 offset:7168\n" \
  "s_waitcnt lgkmcnt(4)\n" \
  "v_smfmac_f32_16x16x64_bf16 v[12:15], v[44:47], v[32:39], v0\n" \
  "ds_read_b128 v[32:35], v6 offset:12288\n" \
  "ds_read_b128 v[36:39], v6 offset:13312\n" \
  "v_smfmac_f32_16x16x64_bf16 v[16:19], v[44:47], v[48:55], v0\n" \
  "ds_read_b128 v[48:51], v6 offset:14336\n" \
  "ds_read_b128 v[52:55], v6 offset:15360\n" \
  "s_nop 0\n" \
  "s_waitcnt lgkmcnt(6)\n" \
  "v_smfmac_f32_16x16x64_bf16 v[20:23], v[40:43], v[56:63], v0\n" \
  "s_nop 0\n" \
  "s_waitcnt lgkmcnt(4)\n" \
  "v_smfmac_f32_16x16x64_bf16 v[8:11], v[40:43], v[24:31], v0\n" \
  "v_permlane32_swap_b32 v12, v17\n" \
  "v_permlane32_swap_b32 v13, v16\n" \
  "v_permlane32_swap_b32 v14, v19\n" \
  "v_permlane32_swap_b32 v15, v18\n" \
  "s_waitcnt lgkmcnt(2)\n" \
  "v_smfmac_f32_16x16x64_bf16 v[20:23], v[44:47], v[32:39], v0\n" \
  "v_pk_add_f32 v[12:13], v[12:13], v[16:17] op_sel:[0,1] op_sel_hi:[1,0]\n" \
  "v_pk_add_f32 v[14:15], v[14:15], v[18:19] op_sel:[0,1] op_sel_hi:[1,0]\n" \
  "v_pk_fma_f32 v[12:13], v[12:13], s[84:85], s[84:85] op_sel:[0,0,1] op_sel_hi:[1,0,1]\n" \
  "v_pk_fma_f32 v[14:15], v[14:15], s[84:85], s[84:85] op_sel:[0,0,1] op_sel_hi:[1,0,1]\n" \
  "s_waitcnt lgkmcnt(0)\n" \
  "v_smfmac_f32_16x16x64_bf16 v[8:11], v[44:47], v[48:55], v0\n" \
  "v_lshl_add_u32 v12, v12, 6, s91\n" \
  "v_lshl_add_u32 v13, v13, 2, s75\n" \
  "global_atomic_add_x2 v5, v[12:13], s[26:27]\n" \
  "v_lshl_add_u32 v14, v14, 6, s91\n" \
  "v_lshl_add_u32 v15, v15, 2, s75\n" \
  "global_atomic_add_x2 v5, v[14:15], s[30:31]\n" \
  "s_nop 1\n" \
  "v_permlane32_swap_b32 v20, v9\n" \
  "v_permlane32_swap_b32 v21, v8\n" \
  "v_permlane32_swap_b32 v22, v11\n" \
  "v_permlane32_swap_b32 v23, v10\n" \
  "v_pk_add_f32 v[20:21], v[20:21], v[8:9] op_sel:[0,1] op_sel_hi:[1,0]\n" \
  "v_pk_add_f32 v[22:23], v[22:23], v[10:11] op_sel:[0,1] op_sel_hi:[1,0]\n" \
  "v_pk_fma_f32 v[20:21], v[20:21], s[84:85], s[84:85] op_sel:[0,0,1] op_sel_hi:[1,0,1]\n" \
  "v_pk_fma_f32 v[22:23], v[22:23], s[84:85], s[84:85] op_sel:[0,0,1] op_sel_hi:[1,0,1]\n" \
  "v_lshl_add_u32 v20, v20, 6, s91\n" \
  "v_lshl_add_u32 v21, v21, 2, s75\n" \
  "global_atomic_add_x2 v5, v[20:21], s[32:33]\n" \
  "v_lshl_add_u32 v22, v22, 6, s91\n" \
  "v_lshl_add_u32 v23, v23, 2, s75\n" \
  "global_atomic_add_x2 v5, v[22:23], s[28:29]\n" \
  "v_mbcnt_lo_u32_b32 v48, -1, 0\n" \
  "v_mbcnt_hi_u32_b32 v48, -1, v48\n" \
  "v_lshrrev_b32 v49, 3, v48\n" \
  "v_and_b32 v50, 7, v48\n" \
  "v_lshlrev_b32 v50, 4, v50\n" \
  "v_lshl_or_b32 v44, v49, 8, v50\n" \
  "s_add_u32 s20, s20, 32\n" \
  "s_mov_b32 s21, 0x8000\n" \
  "v_mov_b32 v52, s22\n" \
  "v_mov_b32 v54, s20\n" \
  "s_waitcnt vmcnt(4)\n" \
  ".LCA_TKE_%=:\n" \
  "v_readlane_b32 s34, v7, 0\n" \
  "v_readlane_b32 s38, v7, 16\n" \
  "v_readlane_b32 s39, v7, 32\n" \
  "v_readlane_b32 s23, v7, 48\n" \
  "s_max_u32 s34, s34, s38\n" \
  "s_max_u32 s39, s39, s23\n" \
  "v_readlane_b32 s38, v7, 8\n" \
  "s_max_u32 s34, s34, s39\n" \
  "v_readlane_b32 s39, v7, 24\n" \
  "s_add_u32 s34, s34, %[cd]\n" \
  "v_readlane_b32 s23, v7, 40\n" \
  "s_add_u32 s38, s38, s39\n" \
  "v_readlane_b32 s39, v7, 56\n" \
  "s_add_u32 s38, s38, s23\n" \
  "v_mov_b32 v55, s34\n" \
  "s_add_u32 s38, s38, s39\n" \
  "s_cmp_ge_u32 s38, s20\n" \
  "s_cbranch_scc1 .LCA_TKO_%=\n" \
  "s_sub_u32 s21, s21, 1\n" \
  "s_cbranch_scc1 .LCA_TKO_%=\n" \
  "s_sleep 2\n" \
  "buffer_load_dword v7, v5, s[16:19], 0 offen sc1\n" \
  "s_waitcnt vmcnt(0)\n" \
  "s_branch .LCA_TKE_%=\n" \
  ".LCA_TKO_%=:\n" \
  "ds_write_b64 v52, v[54:55]\n" \
  "s_branch .LCA_WAIU_%=\n" \
  ".LCA_WAIS_%=:\n" \
  "s_cmp_lt_i32 s39, -1000\n" \
  "s_cbranch_scc1 .LCA_WAIE_%=\n" \
  "s_sleep 1\n" \
  ".LCA_WAIU_%=:\n" \
  "s_memrealtime s[38:39]\n" \
  "s_waitcnt lgkmcnt(0)\n" \
  "s_sub_i32 s39, s38, s34\n" \
  "s_cmp_lt_i32 s39, -7\n" \
  "s_cbranch_scc1 .LCA_WAIS_%=\n" \
  "s_cmp_lt_i32 s39, 0\n" \
  "s_cbranch_scc1 .LCA_WAIU_%=\n" \
  ".LCA_WAIE_%=:\n" \
  "buffer_load_dwordx4 v[24:27], v44, s[40:43], s60 offen sc1\n" \
  "buffer_load_dwordx4 v[28:31], v44, s[40:43], s60 offen offset:2048 sc1\n" \
  "v_mbcnt_lo_u32_b32 v48, -1, 0\n" \
  "v_mbcnt_hi_u32_b32 v48, -1, v48\n" \
  "v_and_b32 v49, 15, v48\n" \
  "v_lshl_add_u32 v8, v49, 1, s97\n" \
  "v_bfe_u32 v49, v48, 4, 1\n" \
  "v_mad_u32_u24 v8, v49, s92, v8\n" \
  "v_lshrrev_b32 v49, 5, v48\n" \
  "v_mad_u32_u24 v9, v49, s93, v8\n" \
  "v_mad_u32_u24 v8, v49, s94, v8\n" \
  "v_add_u32 v10, s99, v1\n" \
  "s_lshr_b32 s88, s99, 2\n" \
  "s_add_u32 s88, s88, s74\n" \
  "s_add_u32 s88, s88, 139264\n" \
  "v_lshl_add_u32 v7, v49, 10, s88\n" \
  "v_and_b32 v49, 31, v48\n" \
  "v_lshl_add_u32 v7, v49, 3, v7\n" \
  "v_mov_b64 v[12:13], 0\n" \
  "v_mov_b64 v[14:15], 0\n" \
  "v_mov_b64 v[16:17], 0\n" \
  "v_mov_b64 v[18:19], 0\n" \
  "v_mov_b64 v[20:21], 0\n" \
  "v_mov_b64 v[22:23], 0\n" \
  "s_add_u32 s88, s74, 157760\n" \
  "v_mov_b32 v59, s88\n" \
  "v_lshrrev_b32 v49, 3, v48\n" \
  "v_and_b32 v50, 7, v48\n" \
  "v_lshlrev_b32 v50, 4, v50\n" \
  "v_lshl_or_b32 v44, v49, 8, v50\n" \
  "v_and_b32 v50, 3, v48\n" \
  "v_lshl_add_u32 v40, v50, 2, s98\n" \
  "v_lshrrev_b32 v50, 4, v48\n" \
  "v_mad_u32_u24 v40, v50, s95, v40\n" \
  "v_and_b32 v49, 1, v49\n" \
  "v_lshl_add_u32 v40, v49, 6, v40\n" \
  "v_bfe_u32 v49, v48, 2, 1\n" \
  "v_lshl_add_u32 v40, v49, 7, v40\n" \
  "v_xor_b32 v41, 16, v40\n" \
  "v_xor_b32 v42, 32, v40\n" \
  "v_xor_b32 v43, 48, v40\n" \
  "s_add_i32 s80, s86, 0x32800000\n" \
  "s_add_i32 s81, s86, 0x33800000\n" \
  "s_add_i32 s88, s86, 0xb4000000\n" \
  "v_mov_b32 v46, s88\n" \
  "v_mov_b32 v47, 0\n" \
  "v_mov_b32 v11, 0\n" \
  "s_mov_b32 s89, s60\n" \
  "s_xor_b32 s60, s60, 0x80\n" \
  "s_bitcmp0_b32 s60, 7\n" \
  "s_cselect_b32 s88, 0x8000, 0\n" \
  "s_add_u32 s60, s60, s88\n" \
  "s_and_b32 s60, s60, 0xffff\n" \
  "s_add_u32 s58, s56, s60\n" \
  "s_addc_u32 s59, s57, 0\n" \
  "s_xor_b32 s38, s35, 0\n" \
  "s_add_u32 s26, s58, s38\n" \
  "s_addc_u32 s27, s59, 0\n" \
  "s_xor_b32 s38, s35, 1024\n" \
  "s_add_u32 s28, s58, s38\n" \
  "s_addc_u32 s29, s59, 0\n" \
  "s_xor_b32 s38, s35, 2048\n" \
  "s_add_u32 s30, s58, s38\n" \
  "s_addc_u32 s31, s59, 0\n" \
  "s_xor_b32 s38, s35, 3072\n" \
  "s_add_u32 s32, s58, s38\n" \
  "s_addc_u32 s33, s59, 0\n" \
  "s_add_u32 s66, s66, 0x1000\n" \
  "s_addc_u32 s67, s67, 0\n" \
  "s_sub_u32 s71, s71, 1\n" \
  "s_mov_b32 s87, 0x40000\n" \
  "s_branch .LCA_CHK_%=\n" \
  ".LCA_POLL_%=:\n" \
  "buffer_load_dwordx4 v[24:27], v44, s[40:43], s89 offen sc1\n" \
  "buffer_load_dwordx4 v[28:31], v44, s[40:43], s89 offen offset:2048 sc1\n" \
  ".LCA_CHK_%=:\n" \
  "s_sub_u32 s87, s87, 1\n" \
  "s_cbranch_scc1 .LCA_END_%=\n" \
  "s_waitcnt vmcnt(1)\n" \
  "v_bitop3_b32 v45, v24, v26, 32 bitop3:0x80\n" \
  "v_alignbit_b32 v32, v25, v24, 2\n" \
  "v_alignbit_b32 v34, v27, v26, 2\n" \
  "s_waitcnt vmcnt(0)\n" \
  "v_bitop3_b32 v45, v45, v28, v30 bitop3:0x80\n" \
  "v_cmp_eq_u32 vcc, 0, v45\n" \
  "v_cvt_f32_i32 v32, v32\n" \
  "v_cvt_f32_i32 v33, v25\n" \
  "s_cmp_eq_u64 vcc, 0\n" \
  "s_cbranch_scc0 .LCA_POLL_%=\n" \
  "v_cvt_f32_i32 v34, v34\n" \
  "v_cvt_f32_i32 v35, v27\n" \
  "v_pk_fma_f32 v[32:33], v[32:33], s[80:81], v[46:47]\n" \
  "v_alignbit_b32 v36, v29, v28, 2\n" \
  "v_pk_fma_f32 v[34:35], v[34:35], s[80:81], v[46:47]\n" \
  "v_alignbit_b32 v38, v31, v30, 2\n" \
  "v_cvt_pk_bf16_f32 v32, v32, v34\n" \
  "v_cvt_pk_bf16_f32 v33, v33, v35\n" \
  "v_cvt_f32_i32 v36, v36\n" \
  "ds_write_b32 v40, v32\n" \
  "v_cvt_f32_i32 v37, v29\n" \
  "ds_write_b32 v41, v33\n" \
  "v_cvt_f32_i32 v38, v38\n" \
  "v_cvt_f32_i32 v39, v31\n" \
  "v_pk_fma_f32 v[36:37], v[36:37], s[80:81], v[46:47]\n" \
  "v_pk_fma_f32 v[38:39], v[38:39], s[80:81], v[46:47]\n" \
  "v_dot2c_f32_bf16 v11, v32, v32\n" \
  "v_cvt_pk_bf16_f32 v36, v36, v38\n" \
  "v_cvt_pk_bf16_f32 v37, v37, v39\n" \
  "ds_write_b32 v42, v36\n" \
  "ds_write_b32 v43, v37\n" \
  "v_dot2c_f32_bf16 v11, v33, v33\n" \
  "v_dot2c_f32_bf16 v11, v36, v36\n" \
  "v_dot2c_f32_bf16 v11, v37, v37\n" \
  "s_nop 1\n" \
  "s_nop 0\n" \
  "v_cmpx_lt_f32 vcc, 2.0, v11\n" \
  "ds_max_u32 v59, v11\n" \
  "s_mov_b64 exec, -1\n" \
  "s_branch .LCA_TOP_%=\n" \
  ".LCA_LAST_%=:\n" \
  ".LCA_END_%=:\n" \
  "s_endpgm\n"
#define CHAIN_ASM_VO \
  "s_mov_b32 s92, 1152\n" \
  "s_mov_b32 s93, 576\n" \
  "s_mov_b32 s94, 2272\n" \
  "s_mov_b32 s95, 272\n" \
  "s_mov_b32 s96, 288\n" \
  "s_mov_b32 s91, 0x40000001\n" \
  "s_mov_b32 s84, 0x4b400000\n" \
  "s_mov_b32 s85, 0x4b400000\n" \
  "s_mov_b32 s78, 0x3e0293ee\n" \
  "s_mov_b32 s79, 0x3fb8aa3b\n" \
  "s_mov_b32 s76, 0xff00ff00\n" \
  "s_mov_b32 s77, 0xff00ff00\n" \
  "s_add_u32 s58, s56, s60\n" \
  "s_addc_u32 s59, s57, 0\n" \
  "s_xor_b32 s38, s35, 0\n" \
  "s_add_u32 s26, s58, s38\n" \
  "s_addc_u32 s27, s59, 0\n" \
  "s_xor_b32 s38, s35, 1024\n" \
  "s_add_u32 s28, s58, s38\n" \
  "s_addc_u32 s29, s59, 0\n" \
  "s_xor_b32 s38, s35, 2048\n" \
  "s_add_u32 s30, s58, s38\n" \
  "s_addc_u32 s31, s59, 0\n" \
  "s_xor_b32 s38, s35, 3072\n" \
  "s_add_u32 s32, s58, s38\n" \
  "s_addc_u32 s33, s59, 0\n" \
  "s_lshl_b32 s98, s73, 10\n" \
  "s_add_u32 s98, s98, s74\n" \
  "s_add_u32 s98, s98, 131072\n" \
  "s_lshl_b32 s97, s73, 5\n" \
  "s_add_u32 s97, s97, s74\n" \
  "s_add_u32 s97, s97, 160992\n" \
  "s_lshl_b32 s99, s73, 11\n" \
  "s_add_u32 s99, s99, s74\n" \
  "s_add_u32 s99, s99, 133120\n" \
  "v_mbcnt_lo_u32_b32 v48, -1, 0\n" \
  "v_mbcnt_hi_u32_b32 v48, -1, v48\n" \
  "s_sub_u32 s88, s73, 4\n" \
  "v_and_b32 v51, 15, v48\n" \
  "v_lshl_add_u32 v4, v51, 1, s97\n" \
  "v_bfe_u32 v49, v48, 4, 1\n" \
  "v_mad_u32_u24 v4, v49, s92, v4\n" \
  "v_lshrrev_b32 v50, 5, v48\n" \
  "v_mad_u32_u24 v4, v50, s93, v4\n" \
  "v_lshlrev_b32 v8, 3, v49\n" \
  "v_lshl_add_u32 v8, v51, 4, v8\n" \
  "v_lshl_add_u32 v8, v50, 10, v8\n" \
  "s_lshl_b32 s89, s88, 8\n" \
  "s_add_u32 s89, s89, s74\n" \
  "s_add_u32 s89, s89, 139264\n" \
  "v_and_b32 v49, 31, v48\n" \
  "v_lshl_add_u32 v5, v49, 3, s89\n" \
  "v_lshl_add_u32 v5, v50, 10, v5\n" \
  "v_lshrrev_b32 v49, 2, v51\n" \
  "v_and_b32 v50, 5, v51\n" \
  "v_and_or_b32 v49, v49, 2, v50\n" \
  "s_lshl_b32 s89, s88, 6\n" \
  "s_add_u32 s89, s89, s74\n" \
  "s_add_u32 s89, s89, 160992\n" \
  "v_and_b32 v50, 48, v48\n" \
  "v_add_u32 v6, s89, v50\n" \
  "v_mad_u32_u24 v6, v49, s96, v6\n" \
  "v_lshlrev_b32 v7, 3, v50\n" \
  "v_lshl_add_u32 v7, v49, 4, v7\n" \
  "v_and_b32 v49, 2, v51\n" \
  "v_lshl_add_u32 v7, v49, 29, v7\n" \
  "v_mov_b32 v9, 0\n" \
  "v_lshlrev_b32 v10, 4, v48\n" \
  "v_mov_b64 v[12:13], 0\n" \
  "v_mov_b64 v[14:15], 0\n" \
  "v_mov_b64 v[16:17], 0\n" \
  "v_mov_b64 v[18:19], 0\n" \
  "v_mov_b64 v[20:21], 0\n" \
  "v_mov_b64 v[22:23], 0\n" \
  "s_add_u32 s88, s74, 157760\n" \
  "v_mov_b32 v59, s88\n" \
  "v_mov_b32 v24, 0x44870000\n" \
  "v_mul_f32 v24, s72, v24\n" \
  "s_nop 0\n" \
  "v_readfirstlane_b32 s86, v24\n" \
  "s_lshr_b32 s86, s86, 23\n" \
  "s_addk_i32 s86, 0xff83\n" \
  "s_ashr_i32 s86, s86, 1\n" \
  "s_max_i32 s86, s86, -20\n" \
  "s_min_i32 s86, s86, 100\n" \
  "s_mov_b32 s82, s86\n" \
  "s_lshr_b32 s83, s72, 23\n" \
  "s_sub_i32 s83, s83, 242\n" \
  "s_lshl_b32 s88, s73, 10\n" \
  "v_add_u32 v24, s88, v1\n" \
  "ds_read_b128 v[32:35], v24 offset:0\n" \
  "s_waitcnt lgkmcnt(0)\n" \
  "v_mov_b32 v11, 0\n" \
  "v_dot2c_f32_bf16 v11, v32, v32\n" \
  "v_dot2c_f32_bf16 v11, v33, v33\n" \
  "v_dot2c_f32_bf16 v11, v34, v34\n" \
  "v_dot2c_f32_bf16 v11, v35, v35\n" \
  "s_nop 1\n" \
  "s_nop 0\n" \
  "v_cmpx_lt_f32 vcc, 2.0, v11\n" \
  "ds_max_u32 v59, v11\n" \
  "s_mov_b64 exec, -1\n" \
  ".LCV_TOP_%=:\n" \
  "s_waitcnt lgkmcnt(0)\n" \
  "s_barrier\n" \
  "s_setprio 3\n" \
  "buffer_load_dword v58, v9, s[44:47], s68 offen sc1\n" \
  "s_cmp_lg_u32 s73, 7\n" \
  "s_cbranch_scc1 .LCV_NOTK_%=\n" \
  "s_memrealtime s[38:39]\n" \
  "v_mov_b32 v57, 1\n" \
  "s_mov_b64 exec, 1\n" \
  "s_waitcnt lgkmcnt(0)\n" \
  "v_mov_b32 v56, s38\n" \
  "buffer_atomic_umax v56, off, s[16:19], s24\n" \
  "buffer_atomic_add v57, off, s[16:19], s24 offset:64\n" \
  "s_mov_b64 exec, -1\n" \
  ".LCV_NOTK_%=:\n" \
  "ds_read_b128 v[24:27], v1 offset:0\n" \
  "ds_read_b128 v[28:31], v1 offset:1024\n" \
  "ds_read_b128 v[32:35], v1 offset:2048\n" \
  "ds_read_b128 v[36:39], v1 offset:3072\n" \
  "ds_read_b128 v[40:43], v1 offset:4096\n" \
  "ds_read_b128 v[44:47], v1 offset:5120\n" \
  "ds_read_b128 v[48:51], v1 offset:6144\n" \
  "ds_read_b128 v[52:55], v1 offset:7168\n" \
  "s_waitcnt lgkmcnt(7)\n" \
  "v_smfmac_f32_16x16x64_bf16 v[12:15], v[24:27], v[64:71], v0\n" \
  "s_waitcnt lgkmcnt(6)\n" \
  "v_smfmac_f32_16x16x64_bf16 v[12:15], v[28:31], v[72:79], v0\n" \
  "s_waitcnt lgkmcnt(5)\n" \
  "v_smfmac_f32_16x16x64_bf16 v[12:15], v[32:35], v[80:87], v0\n" \
  "s_waitcnt lgkmcnt(4)\n" \
  "v_smfmac_f32_16x16x64_bf16 v[12:15], v[36:39], v[88:95], v0\n" \
  "s_waitcnt lgkmcnt(3)\n" \
  "v_smfmac_f32_16x16x64_bf16 v[12:15], v[40:43], v[96:103], v0\n" \
  "s_waitcnt lgkmcnt(2)\n" \
  "v_smfmac_f32_16x16x64_bf16 v[12:15], v[44:47], v[104:111], v0\n" \
  "s_waitcnt lgkmcnt(1)\n" \
  "v_smfmac_f32_16x16x64_bf16 v[12:15], v[48:51], v[112:119], v0\n" \
  "s_waitcnt lgkmcnt(0)\n" \
  "v_smfmac_f32_16x16x64_bf16 v[12:15], v[52:55], v[120:127], v0\n" \
  "ds_read_b32 v11, v1 offset:26688\n" \
  "s_nop 6\n" \
  "v_permlane32_swap_b32 v12, v14\n" \
  "v_permlane32_swap_b32 v13, v15\n" \
  "v_pk_add_f32 v[12:13], v[12:13], v[14:15]\n" \
  "s_nop 0\n" \
  "v_cvt_pk_bf16_f32 v12, v12, v13\n" \
  "ds_write_b16 v4, v12\n" \
  "ds_write_b16_d16_hi v4, v12 offset:288\n" \
  "s_setprio 0\n" \
  "v_mov_b64 v[12:13], 0\n" \
  "v_mov_b64 v[14:15], 0\n" \
  "s_waitcnt lgkmcnt(2)\n" \
  "v_readfirstlane_b32 s86, v11\n" \
  "s_lshr_b32 s86, s86, 23\n" \
  "s_add_i32 s86, s86, s83\n" \
  "s_ashr_i32 s86, s86, 1\n" \
  "s_max_i32 s86, s86, s82\n" \
  "s_min_i32 s86, s86, 100\n" \
  "s_lshl_b32 s86, s86, 23\n" \
  "s_sub_i32 s84, 0x4a800000, s86\n" \
  "s_cmp_eq_u32 s71, 0\n" \
  "s_waitcnt lgkmcnt(0)\n" \
  "s_barrier\n" \
  "ds_read_b128 v[60:63], v6 offset:0\n" \
  "v_smfmac_f32_16x16x64_bf16 v[16:19], v[24:27], v[128:135], v0\n" \
  "v_smfmac_f32_16x16x64_bf16 v[20:23], v[24:27], v[192:199], v0\n" \
  "v_smfmac_f32_16x16x64_bf16 v[16:19], v[28:31], v[136:143], v0\n" \
  "v_smfmac_f32_16x16x64_bf16 v[20:23], v[28:31], v[200:207], v0\n" \
  "v_smfmac_f32_16x16x64_bf16 v[16:19], v[32:35], v[144:151], v0\n" \
  "v_smfmac_f32_16x16x64_bf16 v[20:23], v[32:35], v[208:215], v0\n" \
  "v_smfmac_f32_16x16x64_bf16 v[16:19], v[36:39], v[152:159], v0\n" \
  "v_smfmac_f32_16x16x64_bf16 v[20:23], v[36:39], v[216:223], v0\n" \
  "v_smfmac_f32_16x16x64_bf16 v[16:19], v[40:43], v[160:167], v0\n" \
  "v_smfmac_f32_16x16x64_bf16 v[20:23], v[40:43], v[224:231], v0\n" \
  "v_smfmac_f32_16x16x64_bf16 v[16:19], v[44:47], v[168:175], v0\n" \
  "v_smfmac_f32_16x16x64_bf16 v[20:23], v[44:47], v[232:239], v0\n" \
  "v_smfmac_f32_16x16x64_bf16 v[16:19], v[48:51], v[176:183], v0\n" \
  "v_smfmac_f32_16x16x64_bf16 v[20:23], v[48:51], v[240:247], v0\n" \
  "v_smfmac_f32_16x16x64_bf16 v[16:19], v[52:55], v[184:191], v0\n" \
  "v_smfmac_f32_16x16x64_bf16 v[20:23], v[52:55], v[248:255], v0\n" \
  "ds_read_b128 v[24:27], v3 offset:0\n" \
  "ds_read_b128 v[28:31], v3 offset:1024\n" \
  "ds_read_b128 v[32:35], v3 offset:2048\n" \
  "ds_read_b128 v[36:39], v3 offset:3072\n" \
  "ds_read_b128 v[40:43], v3 offset:8192\n" \
  "ds_read_b128 v[44:47], v3 offset:9216\n" \
  "s_nop 0\n" \
  "v_permlane32_swap_b32 v16, v18\n" \
  "v_permlane32_swap_b32 v17, v19\n" \
  "v_permlane32_swap_b32 v20, v22\n" \
  "v_permlane32_swap_b32 v21, v23\n" \
  "v_pk_add_f32 v[16:17], v[16:17], v[18:19]\n" \
  "v_pk_add_f32 v[20:21], v[20:21], v[22:23]\n" \
  "v_cvt_pk_bf16_f32 v16, v16, v17\n" \
  "v_cvt_pk_bf16_f32 v17, v20, v21\n" \
  "ds_write_b64 v5, v[16:17]\n" \
  "s_cbranch_scc1 .LCV_LAST_%=\n" \
  "s_cmp_eq_u32 s73, 7\n" \
  "s_waitcnt vmcnt(2)\n" \
  "s_cbranch_scc1 .LCV_TKW_%=\n" \
  "s_waitcnt vmcnt(0)\n" \
  ".LCV_TKW_%=:\n" \
  "v_cmp_eq_u32 vcc, 0, v58\n" \
  "s_cmp_eq_u64 vcc, 0\n" \
  "s_cselect_b32 s90, 1, 0\n" \
  "s_waitcnt lgkmcnt(0)\n" \
  "s_barrier\n" \
  "s_cbranch_scc0 .LCV_NOTOK_%=\n" \
  "s_mov_b32 m0, s99\n" \
  "s_add_u32 s88, s99, 8192\n" \
  "global_load_lds_dwordx4 v10, s[62:63] sc1\n" \
  "global_load_lds_dwordx4 v10, s[62:63] offset:1024 sc1\n" \
  "s_mov_b32 m0, s88\n" \
  "s_nop 0\n" \
  "global_load_lds_dwordx4 v10, s[64:65] sc1\n" \
  "global_load_lds_dwordx4 v10, s[64:65] offset:1024 sc1\n" \
  ".LCV_NOTOK_%=:\n" \
  "ds_write_b32 v59, v12\n" \
  "ds_read_b128 v[48:51], v3 offset:10240\n" \
  "ds_read_b128 v[52:55], v3 offset:11264\n" \
  "v_permlane32_swap_b32 v16, v17\n" \
  "v_max_u32 v60, 1, v60\n" \
  "v_max_u32 v61, 1, v61\n" \
  "v_max_u32 v16, 1, v16\n" \
  "v_max_u32 v17, 1, v17\n" \
  "buffer_store_dwordx2 v[16:17], v8, s[52:55], s70 offen sc1\n" \
  "v_max_u32 v62, 1, v62\n" \
  "v_max_u32 v63, 1, v63\n" \
  "buffer_store_dwordx4 v[60:63], v7, s[48:51], s69 offen sc1\n" \
  "v_mov_b64 v[18:19], 0\n" \
  "v_mov_b64 v[20:21], 0\n" \
  "v_mov_b64 v[22:23], 0\n" \
  "v_mov_b64 v[16:17], 0\n" \
  "ds_read_b128 v[56:59], v3 offset:4096\n" \
  "ds_read_b128 v[60:63], v3 offset:5120\n" \
  "s_waitcnt lgkmcnt(0)\n" \
  "s_barrier\n" \
  "ds_read_b128 v[4:7], v1 offset:8192\n" \
  "ds_read_b128 v[8:11], v1 offset:9216\n" \
  "s_waitcnt lgkmcnt(1)\n" \
  "v_smfmac_f32_16x16x64_bf16 v[12:15], v[4:7], v[24:31], v0\n" \
  "ds_read_b128 v[24:27], v3 offset:6144\n" \
  "ds_read_b128 v[28:31], v3 offset:7168\n" \
  "v_smfmac_f32_16x16x64_bf16 v[16:19], v[4:7], v[32:39], v0\n" \
  "ds_read_b128 v[32:35], v3 offset:12288\n" \
  "ds_read_b128 v[36:39], v3 offset:13312\n" \
  "s_waitcnt lgkmcnt(4)\n" \
  "v_smfmac_f32_16x16x64_bf16 v[12:15], v[8:11], v[40:47], v0\n" \
  "v_smfmac_f32_16x16x64_bf16 v[16:19], v[8:11], v[48:55], v0\n" \
  "v_smfmac_f32_16x16x64_bf16 v[20:23], v[4:7], v[56:63], v0\n" \
  "ds_read_b128 v[56:59], v3 offset:14336\n" \
  "ds_read_b128 v[60:63], v3 offset:15360\n" \
  "v_mov_b64 v[40:41], 0\n" \
  "v_mov_b64 v[42:43], 0\n" \
  "s_waitcnt lgkmcnt(2)\n" \
  "v_smfmac_f32_16x16x64_bf16 v[20:23], v[8:11], v[32:39], v0\n" \
  "s_nop 0\n" \
  "v_permlane32_swap_b32 v12, v17\n" \
  "v_permlane32_swap_b32 v13, v16\n" \
  "v_permlane32_swap_b32 v14, v19\n" \
  "v_permlane32_swap_b32 v15, v18\n" \
  "v_smfmac_f32_16x16x64_bf16 v[40:43], v[4:7], v[24:31], v0\n" \
  "v_pk_add_f32 v[12:13], v[12:13], v[16:17] op_sel:[0,1] op_sel_hi:[1,0]\n" \
  "v_pk_add_f32 v[14:15], v[14:15], v[18:19] op_sel:[0,1] op_sel_hi:[1,0]\n" \
  "v_pk_fma_f32 v[12:13], v[12:13], s[84:85], s[84:85] op_sel:[0,0,1] op_sel_hi:[1,0,1]\n" \
  "v_pk_fma_f32 v[14:15], v[14:15], s[84:85], s[84:85] op_sel:[0,0,1] op_sel_hi:[1,0,1]\n" \
  "s_waitcnt lgkmcnt(0)\n" \
  "v_smfmac_f32_16x16x64_bf16 v[40:43], v[8:11], v[56:63], v0\n" \
  "v_lshl_add_u32 v12, v12, 6, s91\n" \
  "v_lshl_add_u32 v13, v13, 2, s75\n" \
  "global_atomic_add_x2 v2, v[12:13], s[26:27]\n" \
  "v_lshl_add_u32 v14, v14, 6, s91\n" \
  "v_lshl_add_u32 v15, v15, 2, s75\n" \
  "global_atomic_add_x2 v2, v[14:15], s[30:31]\n" \
  "s_nop 1\n" \
  "v_permlane32_swap_b32 v20, v41\n" \
  "v_permlane32_swap_b32 v21, v40\n" \
  "v_permlane32_swap_b32 v22, v43\n" \
  "v_permlane32_swap_b32 v23, v42\n" \
  "v_pk_add_f32 v[20:21], v[20:21], v[40:41] op_sel:[0,1] op_sel_hi:[1,0]\n" \
  "v_pk_add_f32 v[22:23], v[22:23], v[42:43] op_sel:[0,1] op_sel_hi:[1,0]\n" \
  "v_pk_fma_f32 v[20:21], v[20:21], s[84:85], s[84:85] op_sel:[0,0,1] op_sel_hi:[1,0,1]\n" \
  "v_pk_fma_f32 v[22:23], v[22:23], s[84:85], s[84:85] op_sel:[0,0,1] op_sel_hi:[1,0,1]\n" \
  "v_lshl_add_u32 v20, v20, 6, s91\n" \
  "v_lshl_add_u32 v21, v21, 2, s75\n" \
  "global_atomic_add_x2 v2, v[20:21], s[32:33]\n" \
  "v_lshl_add_u32 v22, v22, 6, s91\n" \
  "v_lshl_add_u32 v23, v23, 2, s75\n" \
  "global_atomic_add_x2 v2, v[22:23], s[28:29]\n" \
  "s_add_u32 s20, s20, 32\n" \
  "s_cmp_eq_u32 s90, 1\n" \
  "s_cbranch_scc0 .LCV_NOF_%=\n" \
  "v_mbcnt_lo_u32_b32 v48, -1, 0\n" \
  "v_mbcnt_hi_u32_b32 v48, -1, v48\n" \
  "v_lshrrev_b32 v49, 3, v48\n" \
  "v_and_b32 v50, 7, v48\n" \
  "v_lshlrev_b32 v50, 4, v50\n" \
  "v_lshl_or_b32 v44, v49, 8, v50\n" \
  "s_mov_b32 s21, 0x8000\n" \
  "v_mov_b32 v52, s22\n" \
  "v_mov_b32 v54, s20\n" \
  "s_branch .LCV_TKF_%=\n" \
  ".LCV_TKE_%=:\n" \
  "s_sleep 1\n" \
  ".LCV_TKF_%=:\n" \
  "ds_read_b64 v[54:55], v52\n" \
  "s_sub_u32 s21, s21, 1\n" \
  "s_cbranch_scc1 .LCV_TKO_%=\n" \
  "s_waitcnt lgkmcnt(0)\n" \
  "v_readfirstlane_b32 s38, v54\n" \
  "v_readfirstlane_b32 s34, v55\n" \
  "s_cmp_eq_u32 s38, s20\n" \
  "s_cbranch_scc0 .LCV_TKE_%=\n" \
  ".LCV_TKO_%=:\n" \
  "s_branch .LCV_WAIU_%=\n" \
  ".LCV_WAIS_%=:\n" \
  "s_cmp_lt_i32 s39, -1000\n" \
  "s_cbranch_scc1 .LCV_WAIE_%=\n" \
  "s_sleep 1\n" \
  ".LCV_WAIU_%=:\n" \
  "s_memrealtime s[38:39]\n" \
  "s_waitcnt lgkmcnt(0)\n" \
  "s_sub_i32 s39, s38, s34\n" \
  "s_cmp_lt_i32 s39, -7\n" \
  "s_cbranch_scc1 .LCV_WAIS_%=\n" \
  "s_cmp_lt_i32 s39, 0\n" \
  "s_cbranch_scc1 .LCV_WAIU_%=\n" \
  ".LCV_WAIE_%=:\n" \
  "buffer_load_dwordx4 v[24:27], v44, s[40:43], s60 offen sc1\n" \
  "buffer_load_dwordx4 v[28:31], v44, s[40:43], s60 offen offset:2048 sc1\n" \
  ".LCV_NOF_%=:\n" \
  "v_mbcnt_lo_u32_b32 v48, -1, 0\n" \
  "v_mbcnt_hi_u32_b32 v48, -1, v48\n" \
  "s_sub_u32 s88, s73, 4\n" \
  "v_and_b32 v51, 15, v48\n" \
  "v_lshl_add_u32 v4, v51, 1, s97\n" \
  "v_bfe_u32 v49, v48, 4, 1\n" \
  "v_mad_u32_u24 v4, v49, s92, v4\n" \
  "v_lshrrev_b32 v50, 5, v48\n" \
  "v_mad_u32_u24 v4, v50, s93, v4\n" \
  "v_lshlrev_b32 v8, 3, v49\n" \
  "v_lshl_add_u32 v8, v51, 4, v8\n" \
  "v_lshl_add_u32 v8, v50, 10, v8\n" \
  "s_lshl_b32 s89, s88, 8\n" \
  "s_add_u32 s89, s89, s74\n" \
  "s_add_u32 s89, s89, 139264\n" \
  "v_and_b32 v49, 31, v48\n" \
  "v_lshl_add_u32 v5, v49, 3, s89\n" \
  "v_lshl_add_u32 v5, v50, 10, v5\n" \
  "v_lshrrev_b32 v49, 2, v51\n" \
  "v_and_b32 v50, 5, v51\n" \
  "v_and_or_b32 v49, v49, 2, v50\n" \
  "s_lshl_b32 s89, s88, 6\n" \
  "s_add_u32 s89, s89, s74\n" \
  "s_add_u32 s89, s89, 160992\n" \
  "v_and_b32 v50, 48, v48\n" \
  "v_add_u32 v6, s89, v50\n" \
  "v_mad_u32_u24 v6, v49, s96, v6\n" \
  "v_lshlrev_b32 v7, 3, v50\n" \
  "v_lshl_add_u32 v7, v49, 4, v7\n" \
  "v_and_b32 v49, 2, v51\n" \
  "v_lshl_add_u32 v7, v49, 29, v7\n" \
  "v_mov_b32 v9, 0\n" \
  "v_lshlrev_b32 v10, 4, v48\n" \
  "v_mov_b64 v[12:13], 0\n" \
  "v_mov_b64 v[14:15], 0\n" \
  "v_mov_b64 v[16:17], 0\n" \
  "v_mov_b64 v[18:19], 0\n" \
  "v_mov_b64 v[20:21], 0\n" \
  "v_mov_b64 v[22:23], 0\n" \
  "s_add_u32 s88, s74, 157760\n" \
  "v_mov_b32 v59, s88\n" \
  "s_cmp_eq_u32 s90, 1\n" \
  "s_cbranch_scc1 .LCV_HAVE_%=\n" \
  "s_mov_b32 s87, 0x40000\n" \
  ".LCV_LATE_%=:\n" \
  "buffer_load_dword v58, v9, s[44:47], s68 offen sc1\n" \
  "s_sub_u32 s87, s87, 1\n" \
  "s_cbranch_scc1 .LCV_END_%=\n" \
  "s_waitcnt vmcnt(0)\n" \
  "v_cmp_eq_u32 vcc, 0, v58\n" \
  "s_cmp_eq_u64 vcc, 0\n" \
  "s_cbranch_scc1 .LCV_GOT_%=\n" \
  "s_sleep 12\n" \
  "s_branch .LCV_LATE_%=\n" \
  ".LCV_GOT_%=:\n" \
  "s_mov_b32 m0, s99\n" \
  "s_add_u32 s88, s99, 8192\n" \
  "global_load_lds_dwordx4 v10, s[62:63] sc1\n" \
  "global_load_lds_dwordx4 v10, s[62:63] offset:1024 sc1\n" \
  "s_mov_b32 m0, s88\n" \
  "s_nop 0\n" \
  "global_load_lds_dwordx4 v10, s[64:65] sc1\n" \
  "global_load_lds_dwordx4 v10, s[64:65] offset:1024 sc1\n" \
  ".LCV_HAVE_%=:\n" \
  "v_lshrrev_b32 v49, 3, v48\n" \
  "v_and_b32 v50, 7, v48\n" \
  "v_lshlrev_b32 v50, 4, v50\n" \
  "v_lshl_or_b32 v44, v49, 8, v50\n" \
  "v_and_b32 v50, 3, v48\n" \
  "v_lshl_add_u32 v40, v50, 2, s98\n" \
  "v_lshrrev_b32 v50, 4, v48\n" \
  "v_mad_u32_u24 v40, v50, s95, v40\n" \
  "v_and_b32 v49, 1, v49\n" \
  "v_lshl_add_u32 v40, v49, 6, v40\n" \
  "v_bfe_u32 v49, v48, 2, 1\n" \
  "v_lshl_add_u32 v40, v49, 7, v40\n" \
  "v_xor_b32 v41, 16, v40\n" \
  "v_xor_b32 v42, 32, v40\n" \
  "v_xor_b32 v43, 48, v40\n" \
  "s_add_u32 s62, s62, 0x80000\n" \
  "s_addc_u32 s63, s63, 0\n" \
  "s_add_u32 s64, s64, 0x40000\n" \
  "s_addc_u32 s65, s65, 0\n" \
  "s_add_u32 s68, s68, 0x1000\n" \
  "s_add_u32 s69, s69, 0x10000\n" \
  "s_add_u32 s70, s70, 0x10000\n" \
  "s_add_i32 s80, s86, 0x32800000\n" \
  "s_add_i32 s81, s86, 0x33800000\n" \
  "s_add_i32 s88, s86, 0xb4000000\n" \
  "v_mov_b32 v46, s88\n" \
  "v_mov_b32 v47, 0\n" \
  "v_mov_b32 v11, 0\n" \
  "s_mov_b32 s89, s60\n" \
  "s_xor_b32 s60, s60, 0x80\n" \
  "s_bitcmp0_b32 s60, 7\n" \
  "s_cselect_b32 s88, 0x8000, 0\n" \
  "s_add_u32 s60, s60, s88\n" \
  "s_and_b32 s60, s60, 0xffff\n" \
  "s_add_u32 s58, s56, s60\n" \
  "s_addc_u32 s59, s57, 0\n" \
  "s_xor_b32 s38, s35, 0\n" \
  "s_add_u32 s26, s58, s38\n" \
  "s_addc_u32 s27, s59, 0\n" \
  "s_xor_b32 s38, s35, 1024\n" \
  "s_add_u32 s28, s58, s38\n" \
  "s_addc_u32 s29, s59, 0\n" \
  "s_xor_b32 s38, s35, 2048\n" \
  "s_add_u32 s30, s58, s38\n" \
  "s_addc_u32 s31, s59, 0\n" \
  "s_xor_b32 s38, s35, 3072\n" \
  "s_add_u32 s32, s58, s38\n" \
  "s_addc_u32 s33, s59, 0\n" \
  "s_add_u32 s66, s66, 0x1000\n" \
  "s_addc_u32 s67, s67, 0\n" \
  "s_sub_u32 s71, s71, 1\n" \
  "s_mov_b32 s87, 0x40000\n" \
  "s_cmp_eq_u32 s90, 1\n" \
  "s_cbranch_scc1 .LCV_CHK_%=\n" \
  ".LCV_POLL_%=:\n" \
  "buffer_load_dwordx4 v[24:27], v44, s[40:43], s89 offen sc1\n" \
  "buffer_load_dwordx4 v[28:31], v44, s[40:43], s89 offen offset:2048 sc1\n" \
  ".LCV_CHK_%=:\n" \
  "s_sub_u32 s87, s87, 1\n" \
  "s_cbranch_scc1 .LCV_END_%=\n" \
  "s_waitcnt vmcnt(1)\n" \
  "v_bitop3_b32 v45, v24, v26, 32 bitop3:0x80\n" \
  "v_alignbit_b32 v32, v25, v24, 2\n" \
  "v_alignbit_b32 v34, v27, v26, 2\n" \
  "s_waitcnt vmcnt(0)\n" \
  "v_bitop3_b32 v45, v45, v28, v30 bitop3:0x80\n" \
  "v_cmp_eq_u32 vcc, 0, v45\n" \
  "v_cvt_f32_i32 v32, v32\n" \
  "v_cvt_f32_i32 v33, v25\n" \
  "s_cmp_eq_u64 vcc, 0\n" \
  "s_cbranch_scc0 .LCV_POLL_%=\n" \
  "v_cvt_f32_i32 v34, v34\n" \
  "v_cvt_f32_i32 v35, v27\n" \
  "v_pk_fma_f32 v[32:33], v[32:33], s[80:81], v[46:47]\n" \
  "v_alignbit_b32 v36, v29, v28, 2\n" \
  "v_pk_fma_f32 v[34:35], v[34:35], s[80:81], v[46:47]\n" \
  "v_alignbit_b32 v38, v31, v30, 2\n" \
  "v_cvt_pk_bf16_f32 v32, v32, v34\n" \
  "v_cvt_pk_bf16_f32 v33, v33, v35\n" \
  "v_cvt_f32_i32 v36, v36\n" \
  "ds_write_b32 v40, v32\n" \
  "v_cvt_f32_i32 v37, v29\n" \
  "ds_write_b32 v41, v33\n" \
  "v_cvt_f32_i32 v38, v38\n" \
  "v_cvt_f32_i32 v39, v31\n" \
  "v_pk_fma_f32 v[36:37], v[36:37], s[80:81], v[46:47]\n" \
  "v_pk_fma_f32 v[38:39], v[38:39], s[80:81], v[46:47]\n" \
  "v_dot2c_f32_bf16 v11, v32, v32\n" \
  "v_cvt_pk_bf16_f32 v36, v36, v38\n" \
  "v_cvt_pk_bf16_f32 v37, v37, v39\n" \
  "ds_write_b32 v42, v36\n" \
  "ds_write_b32 v43, v37\n" \
  "v_dot2c_f32_bf16 v11, v33, v33\n" \
  "v_dot2c_f32_bf16 v11, v36, v36\n" \
  "v_dot2c_f32_bf16 v11, v37, v37\n" \
  "s_nop 1\n" \
  "s_nop 0\n" \
  "v_cmpx_lt_f32 vcc, 2.0, v11\n" \
  "ds_max_u32 v59, v11\n" \
  "s_mov_b64 exec, -1\n" \
  "s_branch .LCV_TOP_%=\n" \
  ".LCV_LAST_%=:\n" \
  "s_waitcnt lgkmcnt(0)\n" \
  "s_nop 0\n" \
  "v_permlane32_swap_b32 v16, v17\n" \
  "v_max_u32 v60, 1, v60\n" \
  "v_max_u32 v61, 1, v61\n" \
  "v_max_u32 v16, 1, v16\n" \
  "v_max_u32 v17, 1, v17\n" \
  "buffer_store_dwordx2 v[16:17], v8, s[52:55], s70 offen sc1\n" \
  "v_max_u32 v62, 1, v62\n" \
  "v_max_u32 v63, 1, v63\n" \
  "buffer_store_dwordx4 v[60:63], v7, s[48:51], s69 offen sc1\n" \
  "s_waitcnt vmcnt(0)\n" \
  ".LCV_END_%=:\n" \
  "s_endpgm\n"
__device__ __forceinline__ v4u mk_srd(const void* p, const unsigned bytes) {
  const unsigned long long a = (unsigned long long)p;
  return v4u{(unsigned)a, (unsigned)(a >> 32) & 0xffffu, bytes, 0x00020000u};
}
template <bool ATT>
__device__ __forceinline__ void role_chain_t(const MegaArgs& A, char* smem, const int L, const int h, const int rep) {
  u16* swo = reinterpret_cast<u16*>(smem);
  const int w = __builtin_amdgcn_readfirstlane(threadIdx.x >> 6);
  const int wc = (w + ROT) & 7, psw = PSW ? (PSWH) : 0;
  char* ws = A.ws;
  unsigned* FL = (unsigned*)(ws + OFF_FL);
  unsigned* TMO = FL + FL_TMO;
#define LANE_IDS()                         \
  const int l = lane_id();                 \
  const int c = l & 15, g = l >> 4;        \
  (void)c; (void)g;
  float* sgm = reinterpret_cast<float*>(smem + C_X);
  float* sgf = sgm + 8;
  unsigned* FQ = FL + FL_FQ + L * 64 * 32 * 32;
  v8u w0[8], w1[8], w2[8];
  {
    LANE_IDS();
    const unsigned sb = lds_base(smem);
    const int q = (l >> 2) & 3, p = l & 3;
    const int f = (q & 1) | ((g & 1) << 1) | ((q >> 1) << 2);
    const unsigned ra = sb + (16 * (g >> 1) + 4 * (g & 1) + (q & 1) + 8 * (q >> 1)) * 256 + (w ^ f) * 32 + p * 8;
#define W_READ(W, hf, X)                                                       \
  {                                                                           \
    _Pragma("unroll") for (int t = 0; t < 4; ++t) {                           \
      const unsigned a_ = (ra ^ (X)) + (hf) * 8192 + t * 32768;               \
      const v2u r0 = trd(a_), r1 = trd(a_ + 512), r2 = trd(a_ + 16384), r3 = trd(a_ + 16384 + 512); \
      W[4 * (hf) + t] = v8u{r0.x, r0.y, r1.x, r1.y, r2.x, r2.y, r3.x, r3.y};  \
    }                                                                         \
    _Pragma("unroll") for (int t = 0; t < 4; ++t) asm volatile("" : "+v"(W[4 * (hf) + t])); \
  }
#define W_STAGE(M, hf, VM, NEXT)                                               \
  {                                                                           \
    WAIT_VM(VM);                                                              \
    RAW_BAR();                                                                \
    if ((M) == 0 && ATT) { W_READ(w0, hf, 0); W_READ(w2, hf, 128); }          \
    if ((M) == 1 && ATT) { W_READ(w1, hf, 0); }                               \
    if ((M) == 1 && !ATT) { W_READ(w0, hf, 0); }                              \
    if ((M) == 2 && !ATT) { W_READ(w1, hf, 128); W_READ(w2, hf, 0); }         \
    RAW_BAR();                                                                \
    NEXT;                                                                     \
  }
#define WO_ISSUE(hf)                                                          \
  {                                                                           \
    const char* base_ = (const char*)A.Wo + (size_t)(h * DH + 64 * (hf) + 8 * w) * (DM * 2); \
    _Pragma("unroll") for (int i = 0; i < 8; ++i) {                           \
      const int fd = (i & 1) | (((i >> 2) & 1) << 1) | ((w & 1) << 2);        \
      glds16(base_, (unsigned)(i * 1024 + ((l ^ (fd << 1)) << 4)), sb + w * 16384 + (hf) * 8192 + i * 1024); \
    }                                                                         \
  }
    wqkv_issue<true>(A.Wv, 0, h, w, l, sb);
    wqkv_issue<true>(A.Wv, 1, h, w, l, sb);
    v4u wr[16];
#define WR_LD(i, R) \
  asm volatile("global_load_dwordx4 %0, %1, %2" : "={" R "}"(wr[i]) : "v"(l * 16), "s"((const char*)A.Wo + (size_t)(h * DH + 16 * w + i) * (DM * 2)) : "memory")
    WR_LD(0, "v[64:67]");    WR_LD(1, "v[68:71]");    WR_LD(2, "v[72:75]");    WR_LD(3, "v[76:79]");
    WR_LD(4, "v[80:83]");    WR_LD(5, "v[84:87]");    WR_LD(6, "v[88:91]");    WR_LD(7, "v[92:95]");
    WR_LD(8, "v[96:99]");    WR_LD(9, "v[100:103]");    WR_LD(10, "v[104:107]");    WR_LD(11, "v[108:111]");
    WR_LD(12, "v[112:115]");    WR_LD(13, "v[116:119]");    WR_LD(14, "v[120:123]");    WR_LD(15, "v[124:127]");
#undef WR_LD
    W_STAGE(2, 0, 24, wqkv_issue<true>(A.Wk, 0, h, w, l, sb));
    SST(4);
    W_STAGE(2, 1, 24, wqkv_issue<true>(A.Wk, 1, h, w, l, sb));
    SST(5);
#define DOT2(x, acc) dot2sq(x, acc)
    float* cs = reinterpret_cast<float*>(smem + C_SM);
    {
      float f2 = 0.f;
      if (!ATT) {
#pragma unroll
        for (int t = 0; t < 8; ++t)
#pragma unroll
          for (int e = 0; e < 8; ++e) f2 = DOT2(w2[t][e], DOT2(w1[t][e], f2));
      }
      f2 = wave_sum_dpp(f2);
      if (l == 0) { sgf[w] = f2; sgm[16] = 0.f; sgm[32 + w] = 0.f; }
      WAIT_VM(16);
      f32x4 ca = {}, cb = {};
#pragma unroll
      for (int i = 0; i < 16; ++i) {
        asm volatile("" : "+v"(wr[i]), "+v"(ca), "+v"(cb));
        const f32x4 x = {lo16(wr[i].x), hi16(wr[i].x), lo16(wr[i].y), hi16(wr[i].y)};
        const f32x4 y = {lo16(wr[i].z), hi16(wr[i].z), lo16(wr[i].w), hi16(wr[i].w)};
        ca += x * x;
        cb += y * y;
      }
      *reinterpret_cast<f32x4*>(cs + w * 512 + l * 8) = ca;
      *reinterpret_cast<f32x4*>(cs + w * 512 + l * 8 + 4) = cb;
    }
    W_STAGE(1, 0, 8, wqkv_issue<true>(A.Wq, 0, h, w, l, sb));
    {
      float m2 = 0.f;
#pragma unroll
      for (int i = 0; i < 8; ++i) m2 += cs[i * 512 + w * 64 + l];
      m2 = wave_max_dpp(m2);
      if (l == 0) sgm[w] = m2;
    }
    W_STAGE(1, 1, 8, wqkv_issue<true>(A.Wq, 1, h, w, l, sb));
    if (w == 0) {
      float m2 = 0.f, f2 = 0.f;
#pragma unroll
      for (int i = 0; i < 8; ++i) { m2 = fmaxf(m2, sgm[i]); f2 += sgf[i]; }
      __builtin_amdgcn_raw_buffer_store_b32(__float_as_uint(sqrtf(m2 * f2) * 1.001f) | 1u, MKRS(FL + FL_G + L * 8 * 32, 8 * 128), l * 128 + h * 4, 0, AUX_SC1);
    }
    SST(6);
    W_STAGE(0, 0, 8, WO_ISSUE(0));
    W_STAGE(0, 1, 8, WO_ISSUE(1));
    SST(7);
#undef W_STAGE
#undef W_READ
#undef WO_ISSUE
#undef DOT2
    const unsigned rb = sb + (2 * (g >> 1) + (q >> 1)) * 16384 + (4 * (g & 1) + (q & 1)) * 1024 + p * 8;
#pragma unroll
    for (int hf = 0; hf < 2; ++hf) {
      if (hf == 0) WAIT_VM(8); else WAIT_VM(0);
      RAW_BAR();
      v4u fr[8];
#pragma unroll
      for (int i = 0; i < 8; ++i) {
        const unsigned a_ = rb + hf * 8192 + (i & 1) * 65536 + ((4 * wc + ((i >> 1) ^ psw)) ^ f) * 32;
        const v2u r0 = trd(a_), r1 = trd(a_ + 2048);
        fr[i] = v4u{r0.x, r0.y, r1.x, r1.y};
      }
#pragma unroll
      for (int i = 0; i < 8; ++i) asm volatile("" : "+v"(fr[i]));
      RAW_BAR();
#pragma unroll
      for (int i = 0; i < 8; ++i) *reinterpret_cast<v4u*>(swo + w * 8192 + hf * 4096 + i * 512 + l * 8) = fr[i];
    }
  }
  WSTAMP2();
  v4u m0r;
  {
    LANE_IDS();
    m0r = *reinterpret_cast<const v4u*>(A.mem0 + (c & 7) * DM + 64 * w + 16 * g + 8 * (c >> 3));
  }
  const unsigned sb = lds_base(smem);
  const char* gK = ws + OFF_QK + L * SZ_QK + ((size_t)h * 16 + 2 * w) * 1024;
  const char* gV = ws + OFF_VF + L * SZ_O + ((size_t)h * 8 + 2 * w - 8) * 1024;
  float gw2;
  {
    const unsigned* gp = FL + FL_G + (L * 8 + (blockIdx.x & 7)) * 32;
    unsigned gv;
    for (unsigned spins = 0;; ++spins) {
      gv = __hip_atomic_load((gu32*)gp + (lane_id() & 31), RLX_AGENT);
      if (__all(gv != 0u)) break;
      if (spin_timeout(spins, TMO)) return;
      __builtin_amdgcn_s_sleep(4);
    }
    const float gm = wave_max_dpp(__uint_as_float(gv));
    asm volatile("s_nop 0\n\tv_readfirstlane_b32 %0, %1" : "=s"(gw2) : "v"(gm * gm * 1.06f));
  }
  v8u pv;
  {
    LANE_IDS();
    *reinterpret_cast<v4u*>(smem + C_SM + w * 1024 + l * 16) = m0r;
    const unsigned voff = g * 256 + c * 8, wp = sb + w * 16384 + l * 16;
    pv[0] = (c & 8) ? 0xEEEE : 0x4444;
    pv[1] = sb + C_SM + l * 16;
    pv[2] = ATT ? sb + C_SQ + ((c & 7) * QS + 8 * g) * 2 : voff;
    pv[3] = ATT ? sb + C_SK + ((4 * ((c >> 2) & 1) + 2 * (c >> 3) + (c & 1)) * QS + 8 * g) * 2 : wp;
    pv[4] = sb + (c < 8 ? C_SO : C_SQ) + w * 256 + ((c & 7) + 8 * (g >> 1)) * 16 + 8 * (g & 1);
    pv[5] = voff;
    pv[6] = wp;
    pv[7] = sb + C_SO + (l >> 5) * 1024 + w * 256 + (l & 31) * 8;
  }
  if (!ATT) {
    if (!wait_n<4, 1>(FQ + h * 32, 1, TMO)) return;
    const unsigned lo = lane_id() * 16, d = sb + C_KT + (2 * w - 8) * 1024;
    glds16c(gK, lo, d);
    glds16c(gK + 1024, lo, d + 1024);
    glds16c(gV, lo, d + 8192);
    glds16c(gV + 1024, lo, d + 9216);
  }
  DRAIN();
  SST(8);
  const v4u rsA = mk_srd(ws + OFF_ACC + L * SZ_ACC, SZ_ACC);
  const v4u rsF = mk_srd(FQ, 64 * 32 * 32 * 4);
  const v4u rsMK = mk_srd(ws + OFF_MEMK + L * SZ_MEM, SZ_MEM);
  const v4u rsMV = mk_srd(ws + OFF_MEMV + L * SZ_MEM, SZ_MEM);
  const char* pAB = ws + OFF_ACC + L * SZ_ACC + (wc - w) * 4096;
  const char* pGK = gK + NH * 16 * 1024;
  const char* pGV = gV + NH * 8 * 1024;
  const unsigned* pFK = FL + FL_FK + (L * 64 * 32 + h) * 32 + w;
  unsigned sc[8];
  sc[0] = (32 + h) * 128;
  sc[1] = (h * 4 + w - 4) * 512;
  sc[2] = (h * DH + 16 * (w - 4)) * MM * 2;
  sc[3] = NSEG - 1;
  sc[4] = __float_as_uint(gw2);
  sc[5] = w;
  sc[6] = sb;
  sc[7] = h == 0 ? 0xd4000000u - 2u - (1u << 29) : 0xd4000000u;
  const char* dbg = ws;
  const unsigned tg = (now10() / GR) * GR;
  const v4u rsT = mk_srd(FL + FL_TK + L * 256, 1024);
#define CHAIN_OPS                                                                                                          \
  ::"{v[64:71]}"(w0[0]), "{v[72:79]}"(w0[1]), "{v[80:87]}"(w0[2]), "{v[88:95]}"(w0[3]), "{v[96:103]}"(w0[4]),              \
      "{v[104:111]}"(w0[5]), "{v[112:119]}"(w0[6]), "{v[120:127]}"(w0[7]), "{v[128:135]}"(w1[0]), "{v[136:143]}"(w1[1]),    \
      "{v[144:151]}"(w1[2]), "{v[152:159]}"(w1[3]), "{v[160:167]}"(w1[4]), "{v[168:175]}"(w1[5]), "{v[176:183]}"(w1[6]),    \
      "{v[184:191]}"(w1[7]), "{v[192:199]}"(w2[0]), "{v[200:207]}"(w2[1]), "{v[208:215]}"(w2[2]), "{v[216:223]}"(w2[3]),    \
      "{v[224:231]}"(w2[4]), "{v[232:239]}"(w2[5]), "{v[240:247]}"(w2[6]), "{v[248:255]}"(w2[7]), "{v[0:7]}"(pv),          \
      "{s[40:43]}"(rsA), "{s[44:47]}"(rsF), "{s[48:51]}"(rsMK), "{s[52:55]}"(rsMV), "{s[56:57]}"(pAB), "{s60}"(w * 4096), "{s61}"(h * 1024), \
      "{s[62:63]}"(pGK), "{s[64:65]}"(pGV), "{s[66:67]}"(pFK), "{s68}"(sc[0]), "{s69}"(sc[1]), "{s70}"(sc[2]),    \
      "{s71}"(sc[3]), "{s72}"(sc[4]), "{s73}"(sc[5]), "{s74}"(sc[6]), "{s75}"(sc[7]), "{s[36:37]}"(dbg),                   \
      "{s34}"(tg), "{s35}"(psw * 1536), [gp] "n"(GP), [gr] "n"(GR), [gm] "n"(GM), \
      "{s[16:19]}"(rsT), "{s20}"(0), "{s22}"(sb + C_X + 128 + 8 * (w & 3)), "{s24}"((h & 3) * 256), [cd] "n"(CD) \
      : "memory", "vcc", "scc", "s99", "v63"
  if (ATT) asm volatile("s_mov_b64 exec, -1\ns_nop 4\n" CHAIN_ASM_ATT CHAIN_OPS);
  else asm volatile("s_mov_b64 exec, -1\ns_nop 4\n" CHAIN_ASM_VO CHAIN_OPS);
#undef CHAIN_OPS
#undef LANE_IDS
}

__device__ __forceinline__ void role_chain(const MegaArgs& A, char* smem, const int L, const int h, const int rep) {
  if (__builtin_amdgcn_readfirstlane(threadIdx.x >> 6) < 4) role_chain_t<true>(A, smem, L, h, rep);
  else role_chain_t<false>(A, smem, L, h, rep);
}

constexpr int AW_SO = 0, AW_M = 32768, AW_TK = AW_M + 8192, AW_CNT = AW_TK + 98304;
typedef __attribute__((address_space(3))) unsigned lu32;
__device__ __forceinline__ unsigned umin3(unsigned a, unsigned b, unsigned c) {
  return __builtin_elementwise_min(__builtin_elementwise_min(a, b), c);
}
__device__ __forceinline__ bool all_set(const unsigned r) {
  unsigned long long m;
  asm volatile("v_cmp_eq_u32 %0, 0, %1" : "=s"(m) : "v"(r) : "memory");
  return m == 0ull;
}
template <bool AT>
__device__ __forceinline__ void role_aw_t(const MegaArgs& A, char* smem, const int L, const int a) {
  const int w = __builtin_amdgcn_readfirstlane(threadIdx.x >> 6);
  const int l = threadIdx.x & 63, c = l & 15, g = l >> 4;
  const int qt = w & 1, hh = (w >> 1) & 1, dh = AT ? 0 : 1, h = 2 * a + hh;
  char* ws = A.ws;
  unsigned* FL = (unsigned*)(ws + OFF_FL);
  unsigned* TMO = FL + FL_TMO;
  const unsigned sb = lds_base(smem);
  constexpr int NP = AT ? 1 : 3;
  const int cp0 = AT ? w : 3 * w - 8;
  v4u wo[2 * NP][8];
  {
    const int q = (l >> 2) & 3, p = l & 3, f = q | ((g & 1) << 2);
    const unsigned ra = sb + (4 * g + q) * 256 + (p & 1) * 16;
#pragma unroll
    for (int r = 0; r < 2; ++r) {
#pragma unroll
      for (int k = 0; k < 2; ++k) {
        const char* base_ = (const char*)A.Wo + ((size_t)((2 * a + r) * DH + 64 * (w & 1) + 32 * k) * DM + 128 * (w >> 1)) * 2;
#pragma unroll
        for (int i = 0; i < 8; ++i) {
          const int fd = g | ((i & 1) << 2);
          glds16(base_, (unsigned)((4 * i + g) * (DM * 2) + ((c ^ (fd << 1)) << 4)), sb + w * 16384 + k * 8192 + i * 1024);
        }
      }
      WAIT_VM(0);
      RAW_BAR();
#pragma unroll
      for (int ks = 0; ks < 4; ++ks) {
#pragma unroll
        for (int ct = 0; ct < 2 * NP; ++ct) {
          const int cp = cp0 + (ct >> 1);
          const unsigned a_ = ra + (cp >> 2) * 32768 + ks * 8192 + ((2 * (cp & 3) + (p >> 1)) ^ f) * 32 + (ct & 1) * 8;
          const v2u lo = trd(a_), hi = trd(a_ + 4096);
          wo[ct][4 * r + ks] = v4u{lo.x, lo.y, hi.x, hi.y};
        }
#pragma unroll
        for (int ct = 0; ct < 2 * NP; ++ct) asm volatile("" : "+v"(wo[ct][4 * r + ks]));
      }
      RAW_BAR();
    }
  }
  lu32* cnt = (lu32*)(unsigned long)(sb + AW_CNT);
  if (threadIdx.x < 10) cnt[threadIdx.x] = 0u;
  WSTAMP2();
  const __amdgpu_buffer_rsrc_t rsW = MKRS(ws + OFF_PW, SZ_PW);
  const unsigned* FQ = FL + FL_FQ + (L * 64 * 32 + h) * 32;
  const v4u rsF = mk_srd(FQ, (64 * 32 - h) * 128);
  const char* gQ = ws + OFF_QK + L * SZ_QK + ((size_t)h * 16 + 4 * (2 * qt + dh)) * 1024;
  const char* gV = ws + OFF_VF + L * SZ_O + ((size_t)h * 8 + 2 * (2 * qt + dh)) * 1024;
  const char* gM = ws + (qt ? OFF_MEMV : OFF_MEMK) + L * SZ_MEM + h * 2048;
  const int mo = AW_M + hh * 4096;
  const unsigned tq0 = __builtin_amdgcn_readfirstlane(sb + AW_TK + (hh * 24 + 4 * (2 * qt + dh)) * 1024);
  const unsigned tv0 = __builtin_amdgcn_readfirstlane(sb + AW_TK + (hh * 24 + 16 + 2 * (2 * qt + dh)) * 1024);
  const float scale = 0.08838834764831845f;
#define T_DMA(ss)                                                                                                       \
  {                                                                                                                     \
    const unsigned s_ = __builtin_amdgcn_readfirstlane(ss);                                                             \
    const unsigned d_ = tq0 + ((s_ & 1) ? 49152u : 0u), e_ = tv0 + ((s_ & 1) ? 49152u : 0u);                             \
    unsigned keep_;                                                                                                     \
    asm volatile(                                                                                                       \
        "s_nop 4\n\ts_mov_b32 %0, m0\n\ts_mov_b32 m0, %4\n\ts_nop 0\n\t"                                               \
        "global_load_lds_dwordx4 %1, %2 sc1\n\tglobal_load_lds_dwordx4 %1, %2 offset:1024 sc1\n\t"                       \
        "global_load_lds_dwordx4 %1, %2 offset:2048 sc1\n\tglobal_load_lds_dwordx4 %1, %2 offset:3072 sc1\n\t"           \
        "s_mov_b32 m0, %5\n\ts_nop 0\n\t"                                                                               \
        "global_load_lds_dwordx4 %1, %3 sc1\n\tglobal_load_lds_dwordx4 %1, %3 offset:1024 sc1\n\ts_mov_b32 m0, %0"       \
        : "=&s"(keep_)                                                                                                  \
        : "v"(l * 16), "s"(gQ + (size_t)s_ * (NH * 16 * 1024)), "s"(gV + (size_t)s_ * (NH * 8 * 1024)), "s"(d_), "s"(e_) \
        : "memory");                                                                                                    \
  }
#define M_DMA(ss)                                                                                                       \
  {                                                                                                                     \
    unsigned keep_;                                                                                                     \
    asm volatile("s_nop 4\n\ts_mov_b32 %0, m0\n\ts_mov_b32 m0, %3\n\ts_nop 0\n\tglobal_load_lds_dwordx4 %1, %2 sc1\n\t"     \
                 "global_load_lds_dwordx4 %1, %2 offset:1024 sc1\n\ts_mov_b32 m0, %0"                                    \
                 : "=&s"(keep_)                                                                                         \
                 : "v"(l * 16), "s"(gM + (size_t)__builtin_amdgcn_readfirstlane(ss) * (NH * 2048)), "s"(sb + mo + qt * 2048)                           \
                 : "memory");                                                                                           \
  }
#define M_TEST()                                                                                                        \
  {                                                                                                                     \
    const v4u t_ = *reinterpret_cast<const v4u*>(smem + mo + qt * 2048 + l * 16);                                        \
    const v4u u_ = *reinterpret_cast<const v4u*>(smem + mo + qt * 2048 + 1024 + l * 16);                                 \
    ok = __all(umin3(umin3(t_.x, t_.y, t_.z), umin3(u_.x, u_.y, u_.z), umin3(t_.w, u_.w, 1u)) != 0u);                   \
    if (ok) asm volatile("s_mov_b64 exec, 1\n\tds_add_u32 %0, %1\n\ts_mov_b64 exec, -1" ::"v"(sb + AW_CNT + 12 + 4 * hh), "v"(1u) : "memory"); \
  }
#define F_LOAD(r, ss) \
  asm volatile("s_nop 4\n\tbuffer_load_dword %0, %1, %2, %3 offen sc1" : "=v"(r) : "v"(0), "s"(rsF), "s"(__builtin_amdgcn_readfirstlane(ss) * 4096) : "memory")
#define F_TEST(r) all_set(r)
#define W_FLAG(ss, THEN, ELSE)                                                                                          \
  {                                                                                                                     \
    unsigned old_;                                                                                                      \
    asm volatile("s_mov_b64 exec, 1\n\tds_add_rtn_u32 %0, %1, %2\n\ts_mov_b64 exec, -1\n\ts_waitcnt lgkmcnt(0)"           \
                 : "=&v"(old_) : "v"(sb + AW_CNT + ((ss) & 1) * 36), "v"(1u) : "memory");                                                 \
    if ((__builtin_amdgcn_readfirstlane(old_) & 7u) == 7u) {                                                            \
      TS(4, ss, threadIdx.x & ~63u);                                                                                    \
      if (l < NR_W) set_flag(FL + FL_FW + (((L * 64 + (ss)) * 8 + l) * 4 + (a >> 2)) * 32 + (a & 3));                       \
      THEN;                                                                                                             \
    } else {                                                                                                            \
      ELSE;                                                                                                             \
    }                                                                                                                   \
    pend = false;                                                                                                       \
  }
  __builtin_amdgcn_s_waitcnt(0x0F70);
  bool have = false, late = true, pend = false, ahead = false;
  unsigned nf = 0u, tl = now10();
  int asked = 0, lead = 0, skip = 0;
  for (int s = 0; s < NSEG; ++s) {
    const bool spec = have;
    if (late) {
      if (pend) {
        WAIT_VM(0);
        W_FLAG(s - 1, , );
      }
      if (!have) {
        (void)wait_n<SLP_A, 1>(FQ + s * 1024, 1, TMO);
        T_DMA(s);
        WAIT_VM(0);
      }
      RAW_BAR();
    }
    TS(0, s, 0);
    const char* tk = smem + AW_TK + ((s & 1) * 2 + hh) * 24 * 1024 + l * 16;
#define TKF(i) (*reinterpret_cast<const bf16x8*>(tk + (i) * 1024))
    bf16x8 q[4], vt[8];
    float x[12], mx;
    bool ok = false;
    if (AT) {
      f32x4 s0 = {}, s1 = {};
      bf16x8 kd[4], k0[4];
#pragma unroll
      for (int ks = 0; ks < 4; ++ks) {
        q[ks] = TKF(4 * qt + ks);
        kd[ks] = TKF(8 + 4 * qt + ks);
        k0[ks] = TKF(8 + ks);
      }
#pragma unroll
      for (int i = 0; i < 8; ++i) vt[i] = TKF(16 + i);
      __builtin_amdgcn_sched_barrier(0);
#pragma unroll
      for (int ks = 0; ks < 4; ++ks) {
        s0 = MFMA16(kd[ks], q[ks], s0);
        s1 = MFMA16(k0[ks], q[ks], s1);
      }
      scale_pair(s0[0], s0[1], scale, x[0], x[1]);
      scale_pair(s0[2], s0[3], scale, x[2], x[3]);
      scale_pair(s1[0], s1[1], scale, x[4], x[5]);
      scale_pair(s1[2], s1[3], scale, x[6], x[7]);
#pragma unroll
      for (int i = 0; i < 4; ++i) {
        if (4 * g + i > c) x[i] = -3e38f;
        if (!qt) x[4 + i] = -3e38f;
      }
      mx = fmaxf(fmaxf(fmaxf(x[0], x[1]), fmaxf(x[2], x[3])), fmaxf(fmaxf(x[4], x[5]), fmaxf(x[6], x[7])));
      asm volatile("" : "+v"(mx), "+v"(vt[0]), "+v"(vt[1]), "+v"(vt[2]), "+v"(vt[3]), "+v"(vt[4]), "+v"(vt[5]), "+v"(vt[6]), "+v"(vt[7]));
    }
#undef TKF
    have = ahead;
    if (spec) {
      if (AT && asked == 2) WAIT_VM(1); else if (AT) WAIT_VM(2); else WAIT_VM(6);
      if (!ahead) have = F_TEST(nf);
      if (AT && asked) M_TEST();
      skip = ok ? 0 : asked == 2 ? AWSKIP : skip - 1;
      lead = ok ? lead + 1 : 0;
      if (have && !ahead) T_DMA(s + 1);
    }
    unsigned n3;
    if (AT) F_LOAD(n3, s + 2);
    if (!AT) ok = have;
    if (!ok) {
      if (pend) {
        if (!AT) WAIT_VM(0); else if (have && !ahead) WAIT_VM(7); else WAIT_VM(1);
        W_FLAG(s - 1, , );
      }
      if (AT && GATE_M) gate(tl);
    }
    for (unsigned spins = 0; !ok; ++spins) {
      if (spin_timeout(spins, TMO)) break;
      if (spins) __builtin_amdgcn_s_sleep(SLP_M);
      unsigned n2;
      if (AT) M_DMA(s);
      F_LOAD(n2, have ? NSEG : s + 1);
      WAIT_VM(0);
      if (AT) M_TEST() else ok = true;
      if (F_TEST(n2)) {
        have = true;
        T_DMA(s + 1);
      }
    }
    char* so = smem + AW_SO + (s & 1) * 16384 + l * 16;
    if (AT) {
      __builtin_amdgcn_s_setprio(PRI_A);
      tl = now10() + GATE_M;
      asked = 0;
      for (unsigned spins = 0; (unsigned)__builtin_amdgcn_readfirstlane(*(volatile lu32*)(cnt + 3 + hh)) < 2u * (s + 1) && spins < 64u * SPIN_CAP; ++spins)
        __builtin_amdgcn_s_sleep(1);
      v4u km[4];
      v2u vm[8];
#pragma unroll
      for (int ks = 0; ks < 4; ++ks) km[ks] = *reinterpret_cast<const v4u*>(smem + mo + (ks * 32 + g * 8 + (c & 7)) * 16);
#pragma unroll
      for (int i = 0; i < 8; ++i) vm[i] = *reinterpret_cast<const v2u*>(smem + mo + 2048 + ((16 * i + c) * MM + 4 * (g & 1)) * 2);
      TS(1, s, 0);
      f32x4 m0 = {};
#pragma unroll
      for (int ks = 0; ks < 4; ++ks) m0 = MFMA16(BF8(km[ks]), q[ks], m0);
      scale_pair(m0[0], m0[1], scale, x[8], x[9]);
      scale_pair(m0[2], m0[3], scale, x[10], x[11]);
#pragma unroll
      for (int i = 0; i < 4; ++i) {
        if (g >= 2) x[8 + i] = -3e38f;
        mx = fmaxf(mx, x[8 + i]);
      }
      mx = xmax32(xmax16(mx));
      float sum = 0.f;
#pragma unroll
      for (int i = 0; i < 12; ++i) {
        x[i] = __expf(x[i] - mx);
        sum += x[i];
      }
      sum = xsum32(xsum16(sum));
      const float inv = 1.0f / sum;
      const v2u pa = {pk2(x[0] * inv, x[1] * inv), pk2(x[2] * inv, x[3] * inv)};
      const v2u pb = {pk2(x[4] * inv, x[5] * inv), pk2(x[6] * inv, x[7] * inv)};
      const v4u p0 = qt ? v4u{pb.x, pb.y, pa.x, pa.y} : v4u{pa.x, pa.y, pb.x, pb.y};
      const v2u p1 = {pk2(x[8] * inv, x[9] * inv), pk2(x[10] * inv, x[11] * inv)};
#pragma unroll
      for (int k = 0; k < 4; ++k) {
        v4u of;
#pragma unroll
        for (int hf = 0; hf < 2; ++hf) {
          f32x4 ao = {};
          ao = MFMA16(vt[2 * k + hf], BF8(p0), ao);
          ao = MFMA16K(vm[2 * k + hf], p1, ao);
          of[2 * hf] = pk2(ao[0], ao[1]);
          of[2 * hf + 1] = pk2(ao[2], ao[3]);
        }
        *reinterpret_cast<v4u*>(so + ((hh * 2 + qt) * 4 + k) * 1024) = of;
      }
    }
    if (!have && l == 0) *(volatile lu32*)(cnt + 1 + (s & 1)) = s + 1;
    if (AT) {
      WAIT_VM(0);
      ahead = F_TEST(n3);
      if (ahead && l == 0) *(volatile lu32*)(cnt + 5 + 2 * hh + (s & 1)) = s + 1;
      ahead = ahead && have;
      if (pend) W_FLAG(s - 1, , );
    } else if (!AWDEF || !ahead || (unsigned)__builtin_amdgcn_readfirstlane(*(volatile lu32*)(cnt + 3 + hh)) < 2u * (s + 1)) {
      WAIT_VM(0);
      if (pend) W_FLAG(s - 1, , );
    }
    RAW_BAR();
    __builtin_amdgcn_s_setprio(AT ? PRI_WA : PRI_WB);
    TS(2, s, 256);
    const int voff = (c >> 1) * 2048 + (c & 1) * 64 + g * 16, soff = ((L * 64 + s) * 16 + a) * 32768 + cp0 * 128;
    const unsigned lt = *(volatile lu32*)(cnt + 1 + (s & 1));
    if (!AT) ahead = have && (unsigned)__builtin_amdgcn_readfirstlane(*(volatile lu32*)(cnt + 5 + 2 * hh + (s & 1))) == (unsigned)(s + 1);
    unsigned pr;
#define OFRAG(i) (*reinterpret_cast<const bf16x8*>(so + (((((i) & 7) >> 2) * 2 + ((i) >> 3)) * 4 + ((i) & 3)) * 1024))
    constexpr int NO = AT ? 16 : 3;
    bf16x8 o[NO];
#pragma unroll
    for (int i = 0; i < (AT ? 16 : 2); ++i) o[i] = OFRAG(i);
    if (AT)
      asm volatile("s_nop 4\n\tglobal_load_dword %0, %1, %2 sc1"
                   : "=v"(pr)
                   : "v"(2044), "s"(gM + (size_t)(s < NSEG - 1 ? s + 1 : s) * (NH * 2048))
                   : "memory");
    if (have) {
      F_LOAD(nf, ahead ? NSEG : s + 2);
      if (ahead) T_DMA(s + 2);
    }
    __builtin_amdgcn_sched_barrier(0);
    late = __builtin_amdgcn_readfirstlane(lt) == (unsigned)(s + 1);
#pragma unroll
    for (int rt = 0; rt < 2; ++rt) {
      f32x4 acc[2 * NP] = {};
#pragma unroll
      for (int kk = 0; kk < 8; ++kk) {
        const int i = 8 * rt + kk;
        if (!AT && i + 2 < 16) o[(i + 2) % NO] = OFRAG(i + 2);
#pragma unroll
        for (int ct = 0; ct < 2 * NP; ++ct) acc[ct] = MFMA16(BF8(wo[ct][kk]), o[i % NO], acc[ct]);
        if (!AT) __builtin_amdgcn_sched_barrier(0);
      }
      if (AT && rt == 1) {
        if (ahead) WAIT_VM(8); else WAIT_VM(1);
        if (all_set(pr) && have) {
          M_DMA(s + 1);
          asked = 2;
        }
      }
      if (AWDEF && !AT && rt == 0 && pend) {
        if (ahead) WAIT_VM(7); else WAIT_VM(1);
        W_FLAG(s - 1, , );
      }
#pragma unroll
      for (int u = 0; u < NP; ++u)
        ST128((v4u{pk2(acc[2 * u][0], acc[2 * u][1]), pk2(acc[2 * u][2], acc[2 * u][3]), pk2(acc[2 * u + 1][0], acc[2 * u + 1][1]),
                   pk2(acc[2 * u + 1][2], acc[2 * u + 1][3])}),
              rsW, voff + u * 128, soff + rt * 16384);
    }
    __builtin_amdgcn_s_setprio(AT ? PRI_0 : 0);
    pend = true;
    TS(3, s, 256);
    TS(5, s, 0);
    MSTAMP();
  }
  WAIT_VM(0);
  W_FLAG(NSEG - 1, , );
#undef OFRAG
#undef T_DMA
#undef M_DMA
#undef M_TEST
#undef F_LOAD
#undef F_TEST
#undef W_FLAG
}
__device__ __forceinline__ void role_aw(const MegaArgs& A, char* smem, const int L, const int a) {
  if (__builtin_amdgcn_readfirstlane(threadIdx.x >> 6) < 4) role_aw_t<true>(A, smem, L, a);
  else role_aw_t<false>(A, smem, L, a);
}

constexpr int RG = 2, RW = 2;
__device__ __forceinline__ void role_red(const MegaArgs& A, char* smem, const int L, const int idx) {
  const int w = __builtin_amdgcn_readfirstlane(threadIdx.x >> 6);
  const int l = threadIdx.x & 63;
  const int gr = (w >> 1) & 1, r = idx * 2 + (w & 1);
  char* ws = A.ws;
  unsigned* FL = (unsigned*)(ws + OFF_FL);
  unsigned* TMO = FL + FL_TMO;
  const __amdgpu_buffer_rsrc_t rsW = MKRS(ws + OFF_PW, SZ_PW);
  const __amdgpu_buffer_rsrc_t rsX = MKRS(ws + (L ? OFF_X3 : OFF_XA), SZ_X);
  const __amdgpu_buffer_rsrc_t rsY = L ? MKRS(A.out, SZ_X) : MKRS(ws + OFF_X2, SZ_X);
  volatile unsigned* sw = reinterpret_cast<volatile unsigned*>(smem) + (w >> 1) * 32;
  if (threadIdx.x < 128) reinterpret_cast<volatile unsigned*>(smem)[threadIdx.x] = 0u;
  __syncthreads();
  if ((EXP_OFF & 2) && L == 0 && w < 4) return;
  if ((EXP_OFF & 64) && L == 1 && w < 4) return;
  if ((EXP_OFF & 8) && L == 1 && w >= 4) return;
  unsigned tl = now10();
  if (w >= 4) {
    for (int s = gr + (L ? 0 : 2); s < NSEG; s += RG) {
      grp_wait<SLP_N, 8, GATE_N>((w & 1) == 0, sw, s + 1, FL + FL_FC + ((L * 64 + s) * 8 + (blockIdx.x & (NR_C - 1))) * 8 * 32, 64, TMO, tl);
      if (L == 0) tl = now10();
      const unsigned st = row_stats(LD128(rsX, l * 512, s * 32768 + r * 16));
      const unsigned long long v = ((unsigned long long)__float_as_uint(1.0f / hi16(st)) << 32) | ((st << 16) | 1u);
      if (l < NR_N) __hip_atomic_store((gu64*)(FL + FL_FN + ((L * 64 + s) * 8 + l) * 64) + r, v, RLX_AGENT);
      TS(7, s, 256 + gr * 128);
    }
    return;
  }
  if (L == 0) {
    for (int s = gr; s < NSEG; s += RG) ST128((v4u{0u, 0u, 0u, 0u}), rsY, l * 16, (s * SEG + r) * DM * 2);
    DRAIN();
    if (l < 8) set_flag(FL + FL_FZ + (2 * l + (idx >> 3)) * 32 + (idx & 7) * 4 + w);
  }
  for (int s = gr; s < NSEG; s += RG) {
    grp_wait<SLP_R, 4, GATE_R>((w & 1) == 0, sw, s + 1, FL + FL_FW + ((L * 64 + s) * 8 + (blockIdx.x & (NR_W - 1))) * 4 * 32, 16, TMO, tl);
    if (s >= NSEG - 6) tl = now10();
    TS(0, s, gr * RW * 64);
    TS(4, s, gr * RW * 64 + (RW - 1) * 64);
    v4u p[16];
    const v4u xr = LD128(rsX, l * 512, s * 32768 + r * 16);
#pragma unroll
    for (int k = 0; k < 16; ++k)
      p[k] = LD128(rsW, (l >> 2) * 128 + (l & 3) * 16, ((L * 64 + s) * 16 + k) * 32768 + (r >> 1) * 2048 + (r & 1) * 64);
    v4u xo;
#pragma unroll
    for (int e = 0; e < 4; ++e) {
      f32x2_t t[4];
#pragma unroll
      for (int k = 0; k < 4; ++k)
        t[k] = (f32x2_t{lo16(p[4 * k][e]), hi16(p[4 * k][e])} + f32x2_t{lo16(p[4 * k + 1][e]), hi16(p[4 * k + 1][e])}) +
               (f32x2_t{lo16(p[4 * k + 2][e]), hi16(p[4 * k + 2][e])} + f32x2_t{lo16(p[4 * k + 3][e]), hi16(p[4 * k + 3][e])});
      const f32x2_t u = (t[0] + t[1]) + (t[2] + t[3]);
      const unsigned a0 = pk2(u.x, u.y);
      xo[e] = pk2(lo16(xr[e]) + lo16(a0), hi16(xr[e]) + hi16(a0));
      if (L == 0) xo[e] = xo[e] ? xo[e] : 0x8000u;
    }
    ST128(xo, rsY, l * 16, (s * SEG + r) * DM * 2);
    TS(1, s, gr * RW * 64);
    TS(5, s, gr * RW * 64 + (RW - 1) * 64);
    if (L == 0) {
      const unsigned st = row_stats(xo);
      if (l < NR_R) __hip_atomic_store((gu32*)(FL + FL_FR + ((s * 8 + l) * 4 + (r >> 3)) * 32 + (r & 7)), st, RLX_AGENT);
    }
    TS(6, s, gr * RW * 64 + (RW - 1) * 64);
    TS(3, s, gr * RW * 64);
  }
}

constexpr int CUN = 2144;
__device__ __forceinline__ bf16x8 conv_bfrag(const unsigned* up, const unsigned sh) {
  const unsigned d0 = up[0], d1 = up[1], d2 = up[2], d3 = up[3], d4 = up[4];
  const v4u r = {__builtin_amdgcn_alignbit(d1, d0, sh), __builtin_amdgcn_alignbit(d2, d1, sh),
                 __builtin_amdgcn_alignbit(d3, d2, sh), __builtin_amdgcn_alignbit(d4, d3, sh)};
  return BF8(r);
}
__device__ __forceinline__ void role_conv(const MegaArgs& A, char* smem, const int cv) {
  u16* Kr = reinterpret_cast<u16*>(smem);
  u16* Uc = Kr + 8 * 2 * CKN;
  u16* Xr = Uc + 8 * CUN;
  u16* To = Uc + 8 * 2 * CUN;
  u16* Su = To + 32 * 8;
  u16* Sx = Su + 32 * 8;
  const int tid = threadIdx.x;
  const int w = __builtin_amdgcn_readfirstlane(tid >> 6);
  const int l = tid & 63, c = l & 15, g = l >> 4;
  const int d = 8 * cv + w;
  char* ws = A.ws;
  unsigned* FL = (unsigned*)(ws + OFF_FL);
  unsigned* TMO = FL + FL_TMO;
  u16* kr = Kr + w * 2 * CKN;
  float* prm = reinterpret_cast<float*>(kr);
  v4u tx[4];
  {
    const unsigned pa = *reinterpret_cast<const unsigned*>(A.logAre + d * NST + 2 * l);
    const unsigned pb = *reinterpret_cast<const unsigned*>(A.Aim + d * NST + 2 * l);
    const unsigned pc = *reinterpret_cast<const unsigned*>(A.Cre + d * NST + 2 * l);
    const unsigned pd = *reinterpret_cast<const unsigned*>(A.Cim + d * NST + 2 * l);
    const float dt = bfr(expf(bf2f(A.logdt[d])));
    if (w < 2) tx[0] = *reinterpret_cast<const v4u*>(A.x0 + (size_t)tid * DM + 8 * cv);
    if (l < 16) reinterpret_cast<unsigned*>(Uc + w * CUN)[l] = 0u;
    if (l < 32) reinterpret_cast<unsigned*>(Uc + w * CUN + 32 + L_SEQ)[l] = 0u;
    if (l < 32) reinterpret_cast<unsigned*>(Uc + w * CUN + 32 + 128)[l] = 0u;
    if (l < 48) {
      reinterpret_cast<v4u*>(kr + 1824)[l] = v4u{0u, 0u, 0u, 0u};
      reinterpret_cast<v4u*>(kr + CKN + 1824)[l] = v4u{0u, 0u, 0u, 0u};
    }
#pragma unroll
    for (int e = 0; e < 2; ++e) {
      const float ar = -bfr(expf(e ? hi16(pa) : lo16(pa))) * dt, ai = (e ? hi16(pb) : lo16(pb)) * dt;
      const float ex = expf(ar);
      float sn, cs;
      sincosf(ai, &sn, &cs);
      prm[2 * l + e] = ar;
      prm[NST + 2 * l + e] = ai;
      prm[2 * NST + 2 * l + e] = (e ? hi16(pc) : lo16(pc)) * dt;
      prm[3 * NST + 2 * l + e] = (e ? hi16(pd) : lo16(pd)) * dt;
      prm[4 * NST + 2 * l + e] = ex * cs;
      prm[5 * NST + 2 * l + e] = ex * sn;
    }
  }
  const float dsk = bf2f(A.Dskip[d]);
  const u16* ka = kr + (c >> 3) * CKN + 2080 - 16 * (c & 7) + 8 * g;
  const unsigned ush = (c & 1) * 16;
#define KFRAG(D0) (*reinterpret_cast<const bf16x8*>(ka - (D0)))
  f32x4 acc[16], far[9];
  v4u xf[3];
  const unsigned* ub1 = reinterpret_cast<const unsigned*>(Uc + w * CUN + (c & ~1) + 8 * g + 32);
  const __amdgpu_buffer_rsrc_t rz1 = MKRS(ws + OFF_XA, SZ_X);
  const int t0 = 16 * (4 * (g & 1) + (g >> 1)) + c;
  {
    const u16* uc = Uc + w * CUN;
    const unsigned* ub = reinterpret_cast<const unsigned*>(uc + (c & ~1) + 8 * g + 32);
    const __amdgpu_buffer_rsrc_t rz = MKRS(ws + OFF_XA, SZ_X);
    const __amdgpu_buffer_rsrc_t rst = MKRS(ws + OFF_GT, L_SEQ * 4);
#define TILE_OUT(T)                                                                            \
  {                                                                                            \
    const float u0 = bf2f(uc[32 + 128 * (T) + t0]), u1 = bf2f(uc[64 + 128 * (T) + t0]);        \
    const float a0 = bfr(bfr(y0) + bfr(u0 * dsk)), a1 = bfr(bfr(y1) + bfr(u1 * dsk));          \
    u16* xp = Xr + (128 * (T) + t0) * 8 + w;                                                   \
    xp[0] = f2bf(bf2f(xp[0]) + a0);                                                            \
    xp[256] = f2bf(bf2f(xp[256]) + a1);                                                        \
  }
#define CHUNK_ST(ck)                                                                           \
  {                                                                                            \
    const v4u o = *reinterpret_cast<const v4u*>(Xr + (64 * (ck) + l) * 8);                     \
    ST128(o, rz, (l >> 5) * 32768 + (l & 31) * 16, (2 * (ck) * 64 + cv) * 512);                \
  }
#define U_ROW(row)                                                                             \
  {                                                                                            \
    const unsigned st_ = __builtin_amdgcn_raw_buffer_load_b32(rst, (row) * 4, 0, AUX_SC1);     \
    const v4u xr_ = *reinterpret_cast<const v4u*>(Xr + (row) * 8);                             \
    const float mb_ = lo16(st_), ri_ = 1.0f / hi16(st_);                                       \
    _Pragma("unroll") for (int e = 0; e < 4; ++e) {                                            \
      const unsigned t_ = pk2(lo16(xr_[e]) - mb_, hi16(xr_[e]) - mb_);                         \
      const unsigned q_ = pk2(lo16(t_) * ri_, hi16(t_) * ri_);                                 \
      Uc[(2 * e) * CUN + 32 + (row)] = (u16)q_;                                                \
      Uc[(2 * e + 1) * CUN + 32 + (row)] = (u16)(q_ >> 16);                                    \
    }                                                                                          \
  }
    const unsigned* FS = FL + FL_FS + (blockIdx.x & 7) * 32 * 32;
    const int gi = 4 * (g * 8 + (l & 7));
    const int tl = 32 * ((l >> 3) & 1) + 8 * g + (l & 7);
    unsigned khl = 0u;
#pragma unroll 1
    for (int ps = 0; ps < 4; ++ps) {
      float v[32];
      {
        const f32x4 ar = *reinterpret_cast<const f32x4*>(prm + gi), ai = *reinterpret_cast<const f32x4*>(prm + NST + gi);
        const f32x4 cr = *reinterpret_cast<const f32x4*>(prm + 2 * NST + gi), ci = *reinterpret_cast<const f32x4*>(prm + 3 * NST + gi);
        const f32x4 wr = *reinterpret_cast<const f32x4*>(prm + 4 * NST + gi), wi = *reinterpret_cast<const f32x4*>(prm + 5 * NST + gi);
        const float p0 = (float)(64 * ps + 32 * ((l >> 3) & 1));
        float yr0[4], yi0[4];
#pragma unroll
        for (int k = 0; k < 4; ++k) {
          const float e = __expf(ar[k] * p0);
          float sn, cs;
          __sincosf(ai[k] * p0, &sn, &cs);
          const float zr = e * cs, zi = e * sn;
          yr0[k] = cr[k] * zr - ci[k] * zi;
          yi0[k] = cr[k] * zi + ci[k] * zr;
        }
        f32x2_t yra = {yr0[0], yr0[1]}, yia = {yi0[0], yi0[1]}, wra = {wr[0], wr[1]}, wia = {wi[0], wi[1]};
        f32x2_t yrb = {yr0[2], yr0[3]}, yib = {yi0[2], yi0[3]}, wrb = {wr[2], wr[3]}, wib = {wi[2], wi[3]};
#pragma unroll
        for (int i = 0; i < 32; ++i) {
          const f32x2_t sm2 = yra + yrb;
          v[i] = sm2.x + sm2.y;
          const f32x2_t ta = yra * wra - yia * wia, tb = yrb * wrb - yib * wib;
          yia = yra * wia + yia * wra;
          yib = yrb * wib + yib * wrb;
          yra = ta;
          yrb = tb;
        }
      }
#pragma unroll
      for (int i = 0; i < 16; ++i) v[i] = swap_add32(v[i], v[i + 16]);
#pragma unroll
      for (int i = 0; i < 8; ++i) {
        v[i] = swap_add16(v[i], v[i + 8]);
        v[i] += dppf<0xB1>(0.f, v[i]);
        v[i] += dppf<0x4E>(0.f, v[i]);
        v[i] += dppf<0x141>(0.f, v[i]);
      }
      float s0 = v[0], s1 = v[1], s2 = v[2], s3 = v[3], s4 = v[4], s5 = v[5], s6 = v[6], s7 = v[7];
      asm volatile("" : "+v"(s0), "+v"(s1), "+v"(s2), "+v"(s3), "+v"(s4), "+v"(s5), "+v"(s6), "+v"(s7));
      s0 = (l & 4) ? s4 : s0; s1 = (l & 4) ? s5 : s1; s2 = (l & 4) ? s6 : s2; s3 = (l & 4) ? s7 : s3;
      s0 = (l & 2) ? s2 : s0; s1 = (l & 2) ? s3 : s1;
      const float kv = 2.0f * ((l & 1) ? s1 : s0);
      khl = pk2(kv, 0.f);
      khl = pk2(kv, kv - lo16(khl));
      kr[2080 - 64 * ps - tl] = (u16)khl;
      kr[CKN + 2080 - 64 * ps - tl] = (u16)(khl >> 16);
      SST(ps);
      if (ps < 2) {
        if (ps == 0) {
          if (w < 2) {
            reinterpret_cast<v4u*>(Xr)[tid] = tx[0];
            (void)wait_n<2, 8>(FL + FL_F0 + ((blockIdx.x & 7) * 8 + 4 * w) * 32, 32, TMO);
            U_ROW(tid);
          }
          __syncthreads();
        }
        f32x4 a0t = {};
#pragma unroll
        for (int b = -1; b < 4; ++b) a0t = MFMA16(KFRAG(-32 * b), conv_bfrag(ub + 16 * b, ush), a0t);
        const float y0 = swap_add32(a0t[0], a0t[1]), y1 = swap_add32(a0t[2], a0t[3]);
        if ((g & 1) == ps) TILE_OUT(0);
        __syncthreads();
        if (w == ps) {
          CHUNK_ST(w);
          DRAIN();
          if (l < 16) set_flag(FL + FL_FC + (((2 * w + (l >> 3)) * 8 + (l & 7)) * 8 + (cv >> 3)) * 32 + (cv & 7));
        }
        SST(4 + ps);
      }
      if (ps == 2) {
        if (w >= 2) tx[0] = *reinterpret_cast<const v4u*>(A.x0 + (size_t)tid * DM + 8 * cv);
#pragma unroll
        for (int i = 1; i < 4; ++i) tx[i] = *reinterpret_cast<const v4u*>(A.x0 + (size_t)(tid + 512 * i) * DM + 8 * cv);
      }
    }
    if (w >= 2) reinterpret_cast<v4u*>(Xr)[tid] = tx[0];
#pragma unroll
    for (int i = 1; i < 4; ++i) reinterpret_cast<v4u*>(Xr)[tid + 512 * i] = tx[i];
    {
      unsigned spins = 0;
      while (!__all(__hip_atomic_load((gu32*)FS + (w + 8 * ((l >> 3) & 3)) * 32 + (l & 7), RLX_AGENT) != 0u)) {
        if (spin_timeout(++spins, TMO)) break;
        __builtin_amdgcn_s_sleep(4);
      }
      asm volatile("" ::: "memory");
    }
    if (w >= 2) U_ROW(tid);
#pragma unroll
    for (int i = 1; i < 4; ++i) U_ROW(tid + 512 * i);
    SST(6);
    (void)wait_n<8, 1>(FL + FL_KG + cv * 32 + (w >> 2), 1, TMO);
    SST(7);
    {
      const __amdgpu_buffer_rsrc_t rk = MKRS(ws + OFF_KF + (size_t)d * 2 * CKN * 2, 2 * CKN * 2);
      v4u t[8];
#pragma unroll
      for (int i = 0; i < 4; ++i) {
        t[i] = LD128(rk, (64 * i + l) * 16, 0);
        t[4 + i] = LD128(rk, (64 * i + l) * 16, CKN * 2);
      }
#pragma unroll
      for (int i = 0; i < 4; ++i)
        if (64 * i + l < 229) {
          reinterpret_cast<v4u*>(kr)[64 * i + l] = t[i];
          reinterpret_cast<v4u*>(kr + CKN)[64 * i + l] = t[4 + i];
        }
      kr[2080 - 192 - tl] = (u16)khl;
      kr[CKN + 2080 - 192 - tl] = (u16)(khl >> 16);
    }
    __syncthreads();
#define BULK(TA, TB, HOOK)                                                                      \
  {                                                                                            \
    const bf16x8 bm = conv_bfrag(ub - 16, ush);                                                \
    _Pragma("unroll") for (int T = (TA); T < (TB); ++T) acc[T] = MFMA16(KFRAG(128 * T + 32), bm, (f32x4{})); \
    _Pragma("unroll 1") for (int r = 0; r < 4; ++r) {                                          \
      bf16x8 bf[TB];                                                                           \
      _Pragma("unroll") for (int j = 0; j < (TB); ++j) bf[j] = conv_bfrag(ub + 16 * r + 64 * j, ush); \
      _Pragma("unroll") for (int e = 0; e < (TB); ++e) {                                       \
        const bf16x8 a = KFRAG(128 * e - 32 * r);                                              \
        _Pragma("unroll") for (int T = (e > (TA) ? e : (TA)); T < (TB); ++T) acc[T] = MFMA16(a, bf[T - e], acc[T]); \
      }                                                                                        \
      if (r == 0) { HOOK }                                                                     \
    }                                                                                          \
  }
    BULK(1, 4, );
#pragma unroll
    for (int T = 1; T < 4; ++T) {
      const float y0 = swap_add32(acc[T][0], acc[T][1]), y1 = swap_add32(acc[T][2], acc[T][3]);
      TILE_OUT(T);
    }
    __syncthreads();
    if (w < 6) CHUNK_ST(2 + w);
    SST(8);
    BULK(4, 7, if (w < 6) {
      DRAIN();
      if (l < 16) set_flag(FL + FL_FC + (((4 + 2 * w + (l >> 3)) * 8 + (l & 7)) * 8 + (cv >> 3)) * 32 + (cv & 7));
      if (w == 1) (void)(wait_n<SLP_V, 32>(FL + FL_FZ + (blockIdx.x & 7) * 64, 64, TMO));
    });
#pragma unroll
    for (int T = 4; T < 7; ++T) {
      const float y0 = swap_add32(acc[T][0], acc[T][1]), y1 = swap_add32(acc[T][2], acc[T][3]);
      TILE_OUT(T);
    }
    __syncthreads();
    if (w >= 2) {
      CHUNK_ST(6 + w);
#pragma unroll
      for (int i = 0; i < 3; ++i) xf[i] = *reinterpret_cast<const v4u*>(Xr + (64 * (8 + 3 * w + i) + l) * 8);
    }
    SST(10);
    {
      const bf16x8 bm = conv_bfrag(ub - 16, ush);
#pragma unroll
      for (int T = 7; T < 16; ++T) far[T - 7] = MFMA16(KFRAG(128 * T + 32), bm, (f32x4{}));
    }
#define FAR_HALF(r, E0, E1)                                                                     \
  {                                                                                            \
    bf16x8 bf[16];                                                                             \
    _Pragma("unroll") for (int j = (8 - (E1) > 0 ? 8 - (E1) : 0); j < 16 - (E0); ++j) {        \
      bf[j] = conv_bfrag(ub1 + 16 * (r) + 64 * j, ush);                                        \
      if (j == 7 && (E1) - (E0) > 8) __builtin_amdgcn_sched_barrier(0);                        \
    }                                                                                          \
    __builtin_amdgcn_sched_barrier(0);                                                         \
    _Pragma("unroll") for (int e = (E0); e < (E1); ++e) {                                      \
      const bf16x8 a = KFRAG(128 * e - 32 * (r));                                              \
      _Pragma("unroll") for (int T = (e > 7 ? e : 7); T < 16; ++T) far[T - 7] = MFMA16(a, bf[T - e], far[T - 7]); \
    }                                                                                          \
  }
#define FAR_PIECE(p)                                                                            \
  {                                                                                            \
    if ((p) < 8) {                                                                             \
      const int r_ = 2 + ((p) >> 2);                                                           \
      if (((p) & 3) == 0) FAR_HALF(r_, 0, 3) else if (((p) & 3) == 1) FAR_HALF(r_, 3, 6)       \
      else if (((p) & 3) == 2) FAR_HALF(r_, 6, 9) else FAR_HALF(r_, 9, 16)                     \
    } else if ((p) == 8) {                                                                     \
                     \
      u16* ucw = Uc + w * CUN + 32 + t0;                                                       \
      _Pragma("unroll") for (int T = 7; T < 16; ++T) {                                         \
        const float y0 = swap_add32(far[T - 7][0], far[T - 7][1]), y1 = swap_add32(far[T - 7][2], far[T - 7][3]); \
        const float u0 = bf2f(ucw[128 * T]), u1 = bf2f(ucw[128 * T + 32]);                     \
        const unsigned yy = pk2(y0, y1), uu = pk2(u0 * dsk, u1 * dsk);                         \
        const unsigned aa = pk2(lo16(yy) + lo16(uu), hi16(yy) + hi16(uu));                     \
        ucw[128 * T] = (u16)aa;                                                                \
        ucw[128 * T + 32] = (u16)(aa >> 16);                                                   \
      }                                                                                        \
    } else if (w >= 2) {                                                                       \
      int l = tid & 63;                                                                        \
      asm volatile("" : "+v"(l), "+v"(xf[0]), "+v"(xf[1]), "+v"(xf[2]));                       \
      if ((p) == 9) {                                                                          \
        _Pragma("unroll") for (int i = 0; i < 3; ++i) {                                        \
          const int ck = 8 + 3 * w + i;                                                        \
          const u16* ur = Uc + 32 + 64 * ck + l;                                               \
          v4u o;                                                                               \
          _Pragma("unroll") for (int e = 0; e < 4; ++e)                                        \
            o[e] = pk2(lo16(xf[i][e]) + bf2f(ur[(2 * e) * CUN]), hi16(xf[i][e]) + bf2f(ur[(2 * e + 1) * CUN])); \
          ST128(o, rz1, (l >> 5) * 32768 + (l & 31) * 16, (2 * ck * 64 + cv) * 512);           \
        }                                                                                      \
      } else {                                                                                 \
        DRAIN();                                                                               \
        if (l < 48) set_flag(FL + FL_FC + (((16 + 6 * w + (l >> 3)) * 8 + (l & 7)) * 8 + (cv >> 3)) * 32 + (cv & 7)); \
      }                                                                                        \
    }                                                                                          \
  }
#pragma unroll 1
    for (int p = 0; p < 2; ++p) {
      FAR_HALF(p, 0, 16);
      if (p == 0 && w >= 2) {
        DRAIN();
        if (l < 16) set_flag(FL + FL_FC + (((12 + 2 * w + (l >> 3)) * 8 + (l & 7)) * 8 + (cv >> 3)) * 32 + (cv & 7));
      }
    }
#undef BULK
    SST(9);
#undef TILE_OUT
#undef CHUNK_ST
#undef U_ROW
  }
  if (EXP_OFF & 4) {
    for (int p = 0; p < 11; ++p) {
      __syncthreads();
      FAR_PIECE(p);
    }
    return;
  }
  u16* uc = Uc + (8 + w) * CUN;
  const unsigned* ub = reinterpret_cast<const unsigned*>(uc + (c & ~1) + 8 * g + 32);
#pragma unroll
  for (int j = 0; j < 16; ++j) acc[j] = f32x4{};
  const __amdgpu_buffer_rsrc_t rx = MKRS(ws + OFF_X2, SZ_X), rz = MKRS(ws + OFF_X3, SZ_X);
  const __amdgpu_buffer_rsrc_t rf = MKRS(FL + FL_FR, 64 * 32 * 128);
  unsigned st = 0u;
  v2u xx = {0u, 0u};
#define X2_LOAD(ss, far)                                                                                                      \
  {                                                                                                                      \
    xx = LD64(rx, (l >> 1) * (DM * 2) + (l & 1) * 8, (far) ? (int)SZ_X : ((ss) * SEG * DM + 8 * cv) * 2);                 \
    st = __builtin_amdgcn_raw_buffer_load_b32(rf, (l >> 4) * 128 + ((l >> 1) & 7) * 4, ((ss) * 8 + (blockIdx.x & (NR_R - 1))) * 512, AUX_SC1); \
  }
  unsigned tl = now10();
  for (int s = 0; s < NSEG; ++s) {
    if (w == 1) {
      for (unsigned spins = 0; !__all(umin3(xx.x, xx.y, st) != 0u); ++spins) {
        if (spin_timeout(spins, TMO)) break;
        if (GATE_V && spins == 0 && !__any(st != 0u)) gate(tl);
        if (spins && !__all(st != 0u)) __builtin_amdgcn_s_sleep(SLP_V);
        X2_LOAD(s, __builtin_popcountll(__ballot(st != 0u)) < CVTHR);
      }
      tl = now10() + GATE_V;
      TS(0, s, 64);
      const float mb = lo16(st), ri = 1.0f / hi16(st);
      const unsigned t0 = pk2(lo16(xx.x) - mb, hi16(xx.x) - mb), t1 = pk2(lo16(xx.y) - mb, hi16(xx.y) - mb);
      *reinterpret_cast<v2u*>(Su + l * 4) = v2u{pk2(lo16(t0) * ri, hi16(t0) * ri), pk2(lo16(t1) * ri, hi16(t1) * ri)};
      *reinterpret_cast<v2u*>(Sx + l * 4) = xx;
    }
    const int q = s & 3;
    const u16* kq = ka + 32 * q;
    const bf16x8 a0 = *reinterpret_cast<const bf16x8*>(kq - 32), a1 = *reinterpret_cast<const bf16x8*>(kq);
    __syncthreads();
    TS(1, s, 0);
    if (s == 0)
      for (int x = l; x < CUN / 2; x += 64) reinterpret_cast<unsigned*>(uc)[x] = 0u;
    if (l < 32) uc[32 + 32 * s + l] = Su[l * 8 + w];
    asm volatile("s_waitcnt lgkmcnt(0)" ::: "memory");
    const bf16x8 bp = conv_bfrag(ub + 16 * (s - 1), ush);
    acc[0] = MFMA16(a0, bp, (q == 0 ? acc[1] : acc[0]));
    const f32x4 tmp = MFMA16(a1, conv_bfrag(ub + 16 * s, ush), acc[0]);
    float y0 = (q & 1) ? tmp[2] : tmp[0], y1 = (q & 1) ? tmp[3] : tmp[1];
    y0 = xsum32(y0);
    y1 = xsum32(y1);
    if (g == (q >> 1)) {
      const float u0 = bf2f(Su[c * 8 + w]), u1 = bf2f(Su[(16 + c) * 8 + w]);
      const float a0 = bfr(bfr(y0) + bfr(u0 * dsk)), a1 = bfr(bfr(y1) + bfr(u1 * dsk));
      To[c * 8 + w] = f2bf(bf2f(Sx[c * 8 + w]) + a0);
      To[(16 + c) * 8 + w] = f2bf(bf2f(Sx[(16 + c) * 8 + w]) + a1);
    }
    __syncthreads();
    if (w == 0 && l < 32) {
      const v4u o = *reinterpret_cast<const v4u*>(To + l * 8);
      ST128(o, rz, l * 16, (s * 64 + cv) * 512);
    }
    if (w == 1) X2_LOAD(s + 1, CVTHR > 0);
    const int left = 15 - (s >> 2);
#define PUSH(J0, SH)                                                                                              \
  if (left >= (J0)) {                                                                                             \
    _Pragma("unroll") for (int j = (J0); j < (J0) + 4 && j < 16; ++j)                                             \
      acc[j] = MFMA16(*reinterpret_cast<const bf16x8*>(kq - 32 - 128 * j), bp, (j + (SH) < 16 ? acc[(j + (SH)) & 15] : f32x4{})); \
  }
    TS(2, s, 0);
    if (w == 0) {
      DRAIN();
      if (l < NR_C) set_flag(FL + FL_FC + (((64 + s) * 8 + l) * 8 + (cv >> 3)) * 32 + (cv & 7));
    }
    TS(3, s, 0);
    if (q == 0) { PUSH(1, 1) PUSH(5, 1) PUSH(9, 1) PUSH(13, 1) }
    else { PUSH(1, 0) PUSH(5, 0) PUSH(9, 0) PUSH(13, 0) }
#undef PUSH
    MSTAMP();
    if (s >= CVPRE && s < 11 + CVPRE) FAR_PIECE(s - CVPRE);
  }
#undef KFRAG
#undef X2_LOAD
#undef FAR_PIECE
#undef FAR_HALF
}

__device__ __forceinline__ v4u ln_row_packed(const v4u xr) {
  float v[8];
#pragma unroll
  for (int e = 0; e < 4; ++e) { v[2 * e] = lo16(xr[e]); v[2 * e + 1] = hi16(xr[e]); }
  float s = ((v[0] + v[1]) + (v[2] + v[3])) + ((v[4] + v[5]) + (v[6] + v[7]));
  s = wave_sum(s);
  const float mean = s * (1.0f / DM);
  float q = 0.f;
#pragma unroll
  for (int j = 0; j < 8; ++j) { float t = v[j] - mean; q += t * t; }
  q = wave_sum(q);
  const float var = q * (1.0f / DM);
  const float mb = bfr(mean);
  const float sd = bfr(sqrtf(bfr(bfr(var) + 1e-5f)));
  const float ri = 1.0f / sd;
  v4u o;
#pragma unroll
  for (int e = 0; e < 4; ++e) {
    const unsigned t = pk2(v[2 * e] - mb, v[2 * e + 1] - mb);
    const float x0 = lo16(t), x1 = hi16(t);
    float q0 = x0 * ri, q1 = x1 * ri;
    q0 = __builtin_fmaf(__builtin_fmaf(-q0, sd, x0), ri, q0);
    q1 = __builtin_fmaf(__builtin_fmaf(-q1, sd, x1), ri, q1);
    o[e] = pk2(q0, q1);
  }
  return o;
}

__device__ __forceinline__ float bfr1(float x) { return lo16(pk2(x, x)); }
__device__ __forceinline__ void ln4_packed(v4u (&x)[4]) {
  f32x2_t v[16];
#pragma unroll
  for (int e = 0; e < 16; ++e) v[e] = f32x2_t{lo16(x[e >> 2][e & 3]), hi16(x[e >> 2][e & 3])};
  const f32x2_t t = (((v[0] + v[1]) + (v[2] + v[3])) + ((v[4] + v[5]) + (v[6] + v[7]))) +
                    (((v[8] + v[9]) + (v[10] + v[11])) + ((v[12] + v[13]) + (v[14] + v[15])));
  const float mean = red_mod4(t.x + t.y) * (1.0f / DM);
  const float mb = bfr1(mean);
  f32x2_t q0 = {0.f, 0.f}, q1 = {0.f, 0.f};
#pragma unroll
  for (int e = 0; e < 16; e += 2) {
    v[e] -= mb;
    v[e + 1] -= mb;
    q0 += v[e] * v[e];
    q1 += v[e + 1] * v[e + 1];
  }
  q0 += q1;
  const float dm = mean - mb;
  const float var = fmaxf(red_mod4(q0.x + q0.y) * (1.0f / DM) - dm * dm, 0.f);
  const float sd = bfr1(sqrtf(bfr1(bfr1(var) + 1e-5f)));
  const float ri = 1.0f / sd;
#pragma unroll
  for (int e = 0; e < 16; ++e) {
    const unsigned t2 = pk2(v[e].x, v[e].y);
    const f32x2_t q = f32x2_t{lo16(t2), hi16(t2)} * ri;
    x[e >> 2][e & 3] = pk2(q.x, q.y);
  }
}

__device__ __forceinline__ void role_qkv(const MegaArgs& A, char* smem, const int L, const int h) {
  const int w = __builtin_amdgcn_readfirstlane(threadIdx.x >> 6);
  char* ws = A.ws;
  unsigned* FL = (unsigned*)(ws + OFF_FL);
  unsigned* TMO = FL + FL_TMO;
#define LANE_IDS()                         \
  const int l = lane_id();                 \
  const int c = l & 15, g = l >> 4;        \
  (void)c; (void)g;
  unsigned* FC = FL + FL_FC + (L * 64 * 8 + (blockIdx.x & (NR_C - 1))) * 8 * 32;
  const __amdgpu_buffer_rsrc_t rsQ = MKRS(ws + OFF_QK + L * SZ_QK, SZ_QK);
  const __amdgpu_buffer_rsrc_t rsV = MKRS(ws + OFF_VF + L * SZ_O, SZ_O);
  const __amdgpu_buffer_rsrc_t rsM = MKRS(ws + (w >= 4 ? OFF_MEMV : OFF_MEMK) + L * SZ_MEM, SZ_MEM);
  bf16x8 wq[16], wk[16], wv[16];
  {
    LANE_IDS();
    const unsigned sb = lds_base(smem);
    const int q = (l >> 2) & 3, p = l & 3, f = q | ((g & 1) << 2);
    const unsigned ra = sb + (8 * g + q) * 256 + (w ^ f) * 32 + p * 8;
#define W_STAGE(W, hf, VM, NEXT)                                               \
  {                                                                           \
    WAIT_VM(VM);                                                              \
    RAW_BAR();                                                                \
    _Pragma("unroll") for (int t = 0; t < 8; ++t) {                           \
      const unsigned a_ = ra + (hf) * 8192 + t * 16384;                       \
      const v2u r0 = trd(a_), r1 = trd(a_ + 1024);                            \
      W[8 * (hf) + t] = BF8((v4u{r0.x, r0.y, r1.x, r1.y}));                   \
    }                                                                         \
    _Pragma("unroll") for (int t = 0; t < 8; ++t) asm volatile("" : "+v"(W[8 * (hf) + t])); \
    RAW_BAR();                                                                \
    NEXT;                                                                     \
  }
    wqkv_issue<false>(A.Wq, 0, h, w, l, sb);
    wqkv_issue<false>(A.Wq, 1, h, w, l, sb);
    W_STAGE(wq, 0, 8, wqkv_issue<false>(A.Wk, 0, h, w, l, sb));
    SST(4);
    W_STAGE(wq, 1, 8, wqkv_issue<false>(A.Wk, 1, h, w, l, sb));
    SST(5);
    W_STAGE(wk, 0, 8, wqkv_issue<false>(A.Wv, 0, h, w, l, sb));
    W_STAGE(wk, 1, 8, wqkv_issue<false>(A.Wv, 1, h, w, l, sb));
    SST(6);
    W_STAGE(wv, 0, 8, (void)0);
    W_STAGE(wv, 1, 0, (void)0);
#undef W_STAGE
  }
  WSTAMP2();
  f32x4 csq = {}, csk = {};
  float csv;
  {
    const v4u one = {0x3f803f80u, 0x3f803f80u, 0x3f803f80u, 0x3f803f80u};
    f32x4 t = {};
#pragma unroll
    for (int ks = 0; ks < 16; ++ks) {
      csq = MFMA16(wq[ks], BF8(one), csq);
      csk = MFMA16(wk[ks], BF8(one), csk);
      t = MFMA16(BF8(one), wv[ks], t);
    }
    csv = t[0];
  }
  constexpr int Q_ST = 65536;
  const unsigned sb = lds_base(smem);
  const char* Sb = (const char*)(FL + FL_FN + (L * 64 * 8 + (blockIdx.x & (NR_N - 1))) * 64);
#define S_DMA(ss)                                                                                                       \
  {                                                                                                                     \
    unsigned keep_;                                                                                                     \
    asm volatile("s_nop 4\n\ts_mov_b32 %0, m0\n\ts_mov_b32 m0, %3\n\ts_nop 0\n\tglobal_load_lds_dword %1, %2 sc1\n\ts_mov_b32 m0, %0" \
                 : "=&s"(keep_)                                                                                         \
                 : "v"((lane_id() & 31) * 8 + (lane_id() >> 5) * 4), "s"(Sb + (ss) * 2048), "s"(sb + Q_ST + w * 256)                                  \
                 : "memory");                                                                                           \
  }
  const char* Xb = ws + (L ? OFF_X3 : OFF_XA) + w * 4096;
  const unsigned* FCw = FC + w * 32;
  const v4u rsF = mk_srd(FCw, (64 * 64 - (blockIdx.x & (NR_C - 1)) * 8 - w) * 128);
#define X_DMA(ss)                                                                                                       \
  {                                                                                                                     \
    LANE_IDS();                                                                                                         \
    unsigned keep_;                                                                                                     \
    asm volatile(                                                                                                       \
        "s_nop 4\n\ts_mov_b32 %0, m0\n\ts_mov_b32 m0, %3\n\ts_nop 0\n\t"                                               \
        "global_load_lds_dwordx4 %1, %2 sc1\n\tglobal_load_lds_dwordx4 %1, %2 offset:1024 sc1\n\t"                       \
        "global_load_lds_dwordx4 %1, %2 offset:2048 sc1\n\tglobal_load_lds_dwordx4 %1, %2 offset:3072 sc1\n\t"           \
        "s_mov_b32 m0, %0"                                                                                              \
        : "=&s"(keep_)                                                                                                  \
        : "v"(l * 16), "s"(Xb + (size_t)(ss) * 32768), "s"(sb + ((ss) & 1) * 32768 + w * 4096)                           \
        : "memory");                                                                                                    \
    TS(0, ss, 0);                                                                                                       \
  }
#define F_LOAD(r, ss)                                                                                                   \
  asm volatile("s_nop 4\n\tbuffer_load_dword %0, %1, %2, %3 offen sc1"                                                  \
               : "=v"(r)                                                                                                \
               : "v"((lane_id() & 7) * 4), "s"(rsF), "s"((ss) * 8192)                                                   \
               : "memory")
  lu32* cnt = (lu32*)(unsigned long)(sb + Q_ST + 2048);
  if (threadIdx.x < 12) cnt[threadIdx.x] = 0u;
  volatile lu32* hv = cnt + 4;
  const __amdgpu_buffer_rsrc_t rsFQ = MKRS(FL + FL_FQ + (L * 64 * 32 + h) * 32, 64 * 4096 - h * 128);
#define Q_FLAG(ss)                                                                                           \
  {                                                                                                          \
    unsigned old_ = 0u;                                                                                      \
    if (lane_id() == 0) old_ = __hip_atomic_fetch_add(cnt + ((ss) & 1), 1u, __ATOMIC_RELAXED, __HIP_MEMORY_SCOPE_WORKGROUP); \
    __builtin_amdgcn_raw_buffer_store_b32(1u, rsFQ, (old_ & 7u) == 7u ? 0 : 0x7fffffff, (ss) * 4096, AUX_SC1); \
    TS(3, ss, 0);                                                                                            \
    pend = false;                                                                                            \
  }
  __builtin_amdgcn_s_waitcnt(0x0F70);
  bool have = false, pend = false;
  for (int s = 0; s < NSEG; ++s) {
    if (QAHEAD && L == 0 && s >= QAHEAD && (s & 1) == 0) {
      if (pend) {
        DRAIN();
        Q_FLAG(s - 1);
      }
      (void)wait_n<32, 1>(FL + FL_FK + ((L * 64 + s - QAHEAD) * 32 + h) * 32, 1, TMO);
    }
    if (!have) {
      for (unsigned spins = 0;; ++spins) {
        unsigned n;
        F_LOAD(n, s);
        WAIT_VM(0);
        if (pend) Q_FLAG(s - 1);
        if (all_set(n) || spin_timeout(spins, TMO)) break;
        __builtin_amdgcn_s_sleep(SLP_Q);
      }
      X_DMA(s);
      WAIT_VM(0);
    } else if (QHV && pend && !__all(hv[lane_id() & 7] == (unsigned)s)) {
      WAIT_VM(0);
      Q_FLAG(s - 1);
    } else {
      WAIT_VM(4);
    }
    unsigned nf;
    F_LOAD(nf, s + 1);
    RAW_BAR();
    TS(1, s, 0);
    v4u vfrag, qf4, kf4;
    const char* ap;
    {
      LANE_IDS();
      ap = smem + (s & 1) * 32768 + g * 512 + c * 16;
    }
#define AFRAG(i) (*reinterpret_cast<const bf16x8*>(ap + ((i) >> 4) * 256 + 2048 * ((i) & 15)))
    bf16x8 abuf[3];
    abuf[0] = AFRAG(0);
    abuf[1] = AFRAG(1);
    f32x4 aq[2] = {}, ak[2] = {}, av[2] = {};
#pragma unroll
    for (int rt = 0; rt < 2; ++rt) {
#pragma unroll
      for (int ks = 0; ks < 16; ++ks) {
        const int i = 16 * rt + ks;
        if (i + 2 < 32) abuf[(i + 2) % 3] = AFRAG(i + 2);
        const bf16x8 a = abuf[i % 3];
        aq[rt] = MFMA16(wq[ks], a, aq[rt]);
        ak[rt] = MFMA16(wk[ks], a, ak[rt]);
        av[rt] = MFMA16(a, wv[ks], av[rt]);
        __builtin_amdgcn_sched_barrier(0);
      }
      if (rt == 0) {
        WAIT_VM(0);
        if (pend) Q_FLAG(s - 1);
        have = all_set(nf);
        S_DMA(s);
        if (have) X_DMA(s + 1)
        else F_LOAD(nf, s + 1);
      }
    }
    {
      LANE_IDS();
      if (have) WAIT_VM(4);
      else if (QCHK) {
        WAIT_VM(0);
        have = all_set(nf);
        if (have) X_DMA(s + 1)
        else F_LOAD(nf, s + 1);
      } else WAIT_VM(1);
      if (QHV && have) hv[w] = s + 1;
      for (unsigned spins = 0; !__all(*(volatile lu32*)(unsigned long)(sb + Q_ST + w * 256 + l * 4) != 0u); ++spins) {
        if (spin_timeout(spins, TMO)) break;
        if (spins) __builtin_amdgcn_s_sleep(SLP_S);
        S_DMA(s);
        WAIT_VM(0);
      }
      const float* stp = reinterpret_cast<const float*>(smem + Q_ST + w * 256);
#pragma unroll
      for (int rt = 0; rt < 2; ++rt) {
        const float mb = stp[16 * rt + c], ri = stp[32 + 16 * rt + c];
        const f32x4 mv = *reinterpret_cast<const f32x4*>(stp + 16 * rt + 4 * g), rv = *reinterpret_cast<const f32x4*>(stp + 32 + 16 * rt + 4 * g);
        const f32x4 tq = (aq[rt] - mb * csq) * ri, tk = (ak[rt] - mb * csk) * ri, tv = (av[rt] - mv * csv) * rv;
        qf4[2 * rt] = pk2(tq[0], tq[1]); qf4[2 * rt + 1] = pk2(tq[2], tq[3]);
        kf4[2 * rt] = pk2(tk[0], tk[1]); kf4[2 * rt + 1] = pk2(tk[2], tk[3]);
        vfrag[2 * rt] = pk2(tv[0], tv[1]); vfrag[2 * rt + 1] = pk2(tv[2], tv[3]);
      }
    }
    {
      LANE_IDS();
      auto q0 = __builtin_amdgcn_permlane16_swap(qf4.x, qf4.z, false, false);
      auto q1 = __builtin_amdgcn_permlane16_swap(qf4.y, qf4.w, false, false);
      auto k0 = __builtin_amdgcn_permlane16_swap(kf4.x, kf4.z, false, false);
      auto k1 = __builtin_amdgcn_permlane16_swap(kf4.y, kf4.w, false, false);
      const v4u qo = {q0[0], q1[0], q0[1], q1[1]}, ko = {k0[0], k1[0], k0[1], k1[1]};
      const int voff = (c + 16 * (2 * (w & 1) + (g >> 1))) * 16 + 4096 * (g & 1);
      const int soff = ((s * NH + h) * 16 + (w >> 1)) * 1024;
      TS(2, s, 0);
      ST128(ko, rsQ, voff + 8192, soff);
      ST128(qo, rsQ, voff, soff);
      ST128(vfrag, rsV, l * 16, ((s * NH + h) * 512 + w * 64) * 16);
      ST64((v2u{0u, 0u}), rsM, l * 8, (s * NH + h) * 2048 + (w & 3) * 512);
      pend = true;
    }
    if (!have) {
      WAIT_VM(4);
      if (all_set(nf)) {
        X_DMA(s + 1);
        have = true;
        WAIT_VM(4);
        Q_FLAG(s);
        WAIT_VM(1);
      }
    }
    MSTAMP();
  }
  DRAIN();
  if (pend) Q_FLAG(NSEG - 1);
#undef LANE_IDS
#undef X_DMA
#undef S_DMA
#undef F_LOAD
#undef Q_FLAG
#undef AFRAG
}

constexpr int MEGA_WGS = 256;
constexpr size_t MEGA_LDS = (size_t)(32 * 4 * 64 * 8 + 8 * SMS + 15 * QS + 128 + 16 * 64 * 8 + 8 * SOS + 512) * 2;
static_assert(MEGA_LDS <= 163840, "lds");
static_assert(MEGA_LDS >= (size_t)AW_CNT + 64, "lds");
static_assert(MEGA_LDS >= (size_t)(8 * 2 * CKN + 8 * 2 * CUN + 4 * 32 * 8) * 2, "lds");
static_assert(MEGA_LDS >= (size_t)2 * 32 * SMS * 2 && MEGA_LDS >= 131072, "lds");
static_assert(MEGA_LDS >= (size_t)(40 * NST + 32 * 400 + 4 * 392) * 4, "lds");
__global__ __launch_bounds__(512, 2) void mega_kernel(const u16* mem0, const u16* Dskip, char* ws, u16* out, const u16* x0,
                                                      const u16* logAre, const u16* Aim, const u16* Cre, const u16* Cim,
                                                      const u16* logdt, const u16* Wq, const u16* Wk, const u16* Wv,
                                                      const u16* Wo) {
  extern __shared__ __attribute__((aligned(16))) char smem[];
  const int b = __builtin_amdgcn_readfirstlane(role_no());
  const MegaArgs A{mem0, Dskip, ws, out, x0, logAre, Aim, Cre, Cim, logdt, Wq, Wk, Wv, Wo, b};
  {
    const int hi = (b >= 32 && b < 96) ? b - 32 : (b >= 200 && b < 248) ? b - 136 : (b >= 96 && b < 104) ? b + 16 : (b >= 248 ? b - 128 : -1);
    if (hi >= 0) start_kgen(A, smem, hi, b - 248);
  }
  if ((EXP_OFF & 1) && b >= 64 && b < 80) return;
  if ((EXP_OFF & 8) && b >= 200 && b < 232) return;
  if ((EXP_OFF & 16) && b >= 32 && b < 64) return;
  if ((EXP_OFF & 32) && b >= 80 && b < 96) return;
  if (b < 64) role_chain(A, smem, b >> 5, b & 31, 0);
  else if (b < 96) role_aw(A, smem, (b >> 4) & 1, b & 15);
  else if (b < 104) role_red(A, smem, 0, b - 96);
  else if (b < 168) role_conv(A, smem, b - 104);
  else if (b < 232) role_qkv(A, smem, (b - 168) >> 5, (b - 168) & 31);
  else if (b < 240) role_red(A, smem, 0, b - 224);
  else role_red(A, smem, 1, b - 240);
}

extern "C" void kernel_launch(void* const* d_in, const int* in_sizes, int n_in, void* d_out, int out_size, void* d_ws,
                              size_t ws_size, hipStream_t stream) {
  MegaArgs A;
  A.x0 = (const u16*)d_in[0];
  A.logAre = (const u16*)d_in[1];
  A.Aim = (const u16*)d_in[2];
  A.Cre = (const u16*)d_in[3];
  A.Cim = (const u16*)d_in[4];
  A.logdt = (const u16*)d_in[5];
  A.Dskip = (const u16*)d_in[6];
  A.mem0 = (const u16*)d_in[8];
  A.Wq = (const u16*)d_in[9];
  A.Wk = (const u16*)d_in[10];
  A.Wv = (const u16*)d_in[11];
  A.Wo = (const u16*)d_in[12];
  A.ws = (char*)d_ws;
  A.out = (u16*)d_out;

  (void)hipFuncSetAttribute((const void*)mega_kernel, hipFuncAttributeMaxDynamicSharedMemorySize, (int)MEGA_LDS);
  zero_kernel<<<ZBLK, 256, 0, stream>>>((uint4*)(A.ws + OFF_Z));
  mega_kernel<<<MEGA_WGS, 512, MEGA_LDS, stream>>>(A.mem0, A.Dskip, A.ws, A.out, A.x0, A.logAre, A.Aim, A.Cre, A.Cim, A.logdt,
                                                   A.Wq, A.Wk, A.Wv, A.Wo);
}
```

```cpp
#include <hip/hip_runtime.h>
#include <hip/hip_bf16.h>
#include <stdint.h>

constexpr int L_SEQ = 2048;
constexpr int DM    = 512;
constexpr int NST   = 128;
constexpr int NH    = 32;
constexpr int DH    = 128;
constexpr int HD    = NH * DH;
constexpr int MM    = 8;
constexpr int SEG   = 32;
constexpr int NSEG  = L_SEQ / SEG;
constexpr int LK    = MM + SEG;

typedef unsigned short u16;
using bf16x8 = __attribute__((ext_vector_type(8))) short;
using f32x4  = __attribute__((ext_vector_type(4))) float;
using f32x16 = __attribute__((ext_vector_type(16))) float;

static __device__ __forceinline__ u16 f2bf(float f) {
  unsigned u = __float_as_uint(f);
  u += 0x7fffu + ((u >> 16) & 1u);
  return (u16)(u >> 16);
}
static __device__ __forceinline__ float bf2f(u16 h) { return __uint_as_float(((unsigned)h) << 16); }
static __device__ __forceinline__ float bfr(float f) { return bf2f(f2bf(f)); }

constexpr size_t SZ_X      = (size_t)L_SEQ * DM * 2;
constexpr size_t OFF_KF    = 0;
constexpr size_t OFF_UT    = OFF_KF + (size_t)DM * L_SEQ * 4;
constexpr size_t OFF_YT    = OFF_UT + SZ_X;
constexpr size_t OFF_XA    = OFF_YT + SZ_X;
constexpr size_t OFF_H     = OFF_XA + SZ_X;
constexpr size_t OFF_X2    = OFF_H + SZ_X;
constexpr size_t OFF_U2    = OFF_X2 + SZ_X;
constexpr size_t OFF_X3    = OFF_U2 + SZ_X;
constexpr size_t SZ_QK     = (size_t)L_SEQ * 2 * HD * 2;
constexpr size_t OFF_QK    = OFF_X3 + SZ_X;
constexpr size_t SZ_O      = (size_t)L_SEQ * HD * 2;
constexpr size_t OFF_VF    = OFF_QK + 2 * SZ_QK;
constexpr size_t SZ_PW     = (size_t)2 * NSEG * 16 * SEG * DM * 2;
constexpr size_t OFF_PW    = OFF_VF + 2 * SZ_O;
constexpr size_t SZ_MEM    = (size_t)NSEG * MM * HD * 2;
constexpr size_t OFF_MEMK  = OFF_PW + SZ_PW;
constexpr size_t OFF_MEMV  = OFF_MEMK + 2 * SZ_MEM;
constexpr size_t OFF_GT    = OFF_MEMV + 2 * SZ_MEM;
constexpr size_t SZ_ACC    = (size_t)2 * 32768;
constexpr size_t OFF_Z     = OFF_GT + (size_t)64 * DM * 4;
constexpr size_t OFF_ACC   = OFF_Z;
constexpr size_t OFF_FL    = OFF_ACC + 2 * SZ_ACC;
constexpr int FL_FK  = 0;
constexpr int FL_FW  = FL_FK + 2 * 64 * 32 * 32;
constexpr int FL_FR  = FL_FW + 2 * 64 * 32 * 32;
constexpr int FL_FC  = FL_FR + 64 * 32 * 32;
constexpr int FL_FQ  = FL_FC + 2 * 64 * 64 * 32;
constexpr int FL_FS  = FL_FQ + 2 * 64 * 32 * 32;
constexpr int FL_F0  = FL_FS + 8 * 32 * 32;
constexpr int FL_FZ  = FL_F0 + 8 * 8 * 32;
constexpr int FL_FN  = FL_FZ + 16 * 32;
constexpr int FL_KG  = FL_FN + 2 * 64 * 16 * 32;
constexpr int FL_G   = FL_KG + 64 * 32;
constexpr int FL_TK  = FL_G + 2 * 8 * 32;
constexpr int FL_TMO = FL_TK + 2 * 256;
constexpr int FL_END = FL_TMO + 64;
constexpr size_t Z_BYTES   = OFF_FL + (size_t)FL_END * 4 - OFF_Z;
constexpr size_t WS_TOTAL  = OFF_Z + Z_BYTES;
static_assert(Z_BYTES % 16 == 0 && OFF_Z % 256 == 0, "zero region");
static_assert(WS_TOTAL + (4 << 20) <= (size_t)256 * 1024 * 1024, "workspace too large");

constexpr int CKN = 2208;
static_assert((size_t)DM * 2 * CKN * 2 <= (size_t)DM * L_SEQ * 4 + SZ_X, "K buffer");
__device__ __forceinline__ float pos_of_index(int p) {
  if (p < 256) return (float)p;
  if (p < 384) return (float)(256 + 2 * (p - 256));
  if (p < 512) return (float)(512 + 4 * (p - 384));
  if (p < 640) return (float)(1024 + 8 * (p - 512));
  return 2048.0f;
}
__device__ __forceinline__ int index_of_l(int l) {
  float pf = bfr((float)l);
  int p = (int)pf;
  if (p < 256) return p;
  if (p < 512) return 256 + ((p - 256) >> 1);
  if (p < 1024) return 384 + ((p - 512) >> 2);
  if (p < 2048) return 512 + ((p - 1024) >> 3);
  return 640;
}

template <int CTRL> __device__ __forceinline__ float dppr(float x) {
  return __builtin_bit_cast(float, __builtin_amdgcn_update_dpp(0, __builtin_bit_cast(int, x), CTRL, 0xf, 0xf, false));
}
__device__ __forceinline__ float wave_sum(float v) {
  v += dppr<0x121>(v);
  v += dppr<0x122>(v);
  v += dppr<0x124>(v);
  v += dppr<0x128>(v);
  {
    auto r = __builtin_amdgcn_permlane16_swap(__float_as_uint(v), __float_as_uint(v), false, false);
    v = __uint_as_float(r[0]) + __uint_as_float(r[1]);
  }
  {
    auto r = __builtin_amdgcn_permlane32_swap(__float_as_uint(v), __float_as_uint(v), false, false);
    v = __uint_as_float(r[0]) + __uint_as_float(r[1]);
  }
  return v;
}
__device__ __forceinline__ void ln_row8(float (&v)[8]) {
  float s = 0.f;
#pragma unroll
  for (int j = 0; j < 8; ++j) s += v[j];
  s = wave_sum(s);
  const float mean = s * (1.0f / DM);
  float q = 0.f;
#pragma unroll
  for (int j = 0; j < 8; ++j) { float t = v[j] - mean; q += t * t; }
  q = wave_sum(q);
  const float var = q * (1.0f / DM);
  const float mb = bfr(mean);
  const float sd = bfr(sqrtf(bfr(bfr(var) + 1e-5f)));
#pragma unroll
  for (int j = 0; j < 8; ++j) v[j] = bfr(bfr(v[j] - mb) / sd);
}

constexpr int ZBLK = 512;
__global__ __launch_bounds__(256) void zero_kernel(uint4* __restrict__ zero) {
  for (size_t i = (size_t)blockIdx.x * 256 + threadIdx.x; i < Z_BYTES / 16; i += (size_t)ZBLK * 256) zero[i] = uint4{0u, 0u, 0u, 0u};
}

#define RLX_AGENT __ATOMIC_RELAXED, __HIP_MEMORY_SCOPE_AGENT
typedef __attribute__((address_space(1))) unsigned gu32;
typedef unsigned v4u __attribute__((ext_vector_type(4)));
constexpr int AUX_SC1 = 16;
constexpr unsigned SPIN_CAP = 1u << 18;

__device__ __forceinline__ bool wait_flags(const unsigned* flags, int lane, int nfl, unsigned want, unsigned* tmo) {
  for (unsigned spins = 0;; ++spins) {
    unsigned v = want;
    if (lane < nfl) v = __hip_atomic_load((gu32*)flags + lane * 32, RLX_AGENT);
    if (__all(v >= want)) break;
    if (spins > SPIN_CAP) {
      if (lane == 0) __hip_atomic_store((gu32*)tmo, 1u, RLX_AGENT);
      return false;
    }
  }
  __builtin_amdgcn_fence(__ATOMIC_ACQUIRE, "wavefront");
  return true;
}

#define STAMP(i)
#define STAMP_DUMP()
#define WSTAMP(i)
template <int CTRL> __device__ __forceinline__ float dppf(float old, float x) {
  return __builtin_bit_cast(float, __builtin_amdgcn_update_dpp(__builtin_bit_cast(int, old), __builtin_bit_cast(int, x), CTRL, 0xf, 0xf, false));
}
__device__ __forceinline__ float wave_max_dpp(float x) {
  const float ninf = -3e38f;
  x = fmaxf(x, dppf<0x111>(ninf, x));
  x = fmaxf(x, dppf<0x112>(ninf, x));
  x = fmaxf(x, dppf<0x114>(ninf, x));
  x = fmaxf(x, dppf<0x118>(ninf, x));
  x = fmaxf(x, dppf<0x142>(ninf, x));
  x = fmaxf(x, dppf<0x143>(ninf, x));
  return __builtin_bit_cast(float, __builtin_amdgcn_readlane(__builtin_bit_cast(int, x), 63));
}
__device__ __forceinline__ float wave_sum_dpp(float x) {
  x += dppf<0x111>(0.f, x);
  x += dppf<0x112>(0.f, x);
  x += dppf<0x114>(0.f, x);
  x += dppf<0x118>(0.f, x);
  x += dppf<0x142>(0.f, x);
  x += dppf<0x143>(0.f, x);
  return __builtin_bit_cast(float, __builtin_amdgcn_readlane(__builtin_bit_cast(int, x), 63));
}
constexpr int SMS = 528;
constexpr int QS = 144;
constexpr int VS = 80;
constexpr int SOS = 144;
#define VAR_NREP 4
constexpr int NREP = VAR_NREP;
__device__ __forceinline__ float xsum16(float x) {
  auto r = __builtin_amdgcn_permlane16_swap(__float_as_uint(x), __float_as_uint(x), false, false);
  return __uint_as_float(r[0]) + __uint_as_float(r[1]);
}
__device__ __forceinline__ float xsum32(float x) {
  auto r = __builtin_amdgcn_permlane32_swap(__float_as_uint(x), __float_as_uint(x), false, false);
  return __uint_as_float(r[0]) + __uint_as_float(r[1]);
}
__device__ __forceinline__ float xmax16(float x) {
  auto r = __builtin_amdgcn_permlane16_swap(__float_as_uint(x), __float_as_uint(x), false, false);
  return fmaxf(__uint_as_float(r[0]), __uint_as_float(r[1]));
}
__device__ __forceinline__ float xmax32(float x) {
  auto r = __builtin_amdgcn_permlane32_swap(__float_as_uint(x), __float_as_uint(x), false, false);
  return fmaxf(__uint_as_float(r[0]), __uint_as_float(r[1]));
}
__device__ __forceinline__ float red_mod4(float x) {
  x += dppf<0x124>(0.f, x);
  x += dppf<0x128>(0.f, x);
  return xsum32(xsum16(x));
}
typedef __bf16 bf16x2_t __attribute__((ext_vector_type(2)));
typedef float f32x2_t __attribute__((ext_vector_type(2)));
__device__ __forceinline__ unsigned pk2(float a, float b) {
  f32x2_t v = {a, b};
  bf16x2_t r = __builtin_convertvector(v, bf16x2_t);
  return __builtin_bit_cast(unsigned, r);
}
__device__ __forceinline__ float dot2sq(const unsigned x, const float acc) {
  return __builtin_amdgcn_fdot2_f32_bf16(__builtin_bit_cast(bf16x2_t, x), __builtin_bit_cast(bf16x2_t, x), acc, false);
}
__device__ __forceinline__ float lo16(unsigned u) { return __uint_as_float(u << 16); }
__device__ __forceinline__ float hi16(unsigned u) { return __uint_as_float(u & 0xffff0000u); }
__device__ __forceinline__ void scale_pair(float a, float b, float scale, float& oa, float& ob) {
  unsigned u = pk2(a, b);
  unsigned v = pk2(lo16(u) * scale, hi16(u) * scale);
  oa = lo16(v);
  ob = hi16(v);
}

#define XSPLIT 3
__device__ __forceinline__ int role_no() {
  const int b = blockIdx.x, x = b & 7, j = b >> 3;
  if (XSPLIT >= 3) {
    const int y = XSPLIT == 5 ? x & 3 : x >> 1;
    if (XSPLIT == 5 ? x < 4 : !(x & 1)) {
      if (j >= 16) return 104 + 16 * y + j - 16;
      return XSPLIT == 4 ? 16 * y + j : (j & 8) * 4 + 8 * y + (j & 7);
    }
    if (j < 16) return 168 + (j & 8) * 4 + 8 * y + (j & 7);
    const int i = 4 * y + (j & 3);
    return j < 20 ? 64 + i : j < 24 ? (i < 8 ? 96 + i : 224 + i) : j < 28 ? 80 + i : 240 + i;
  }
  const int L = x >> 2, xx = x & 3;
  if (j < 8) return 32 * L + 8 * xx + j;
  if (j < 12) return 64 + 16 * L + 4 * xx + j - 8;
  if (j < 16) { const int i = 4 * xx + j - 12; return L ? 240 + i : i < 8 ? 96 + i : 224 + i; }
  if (j < 24) return 104 + 8 * x + j - 16;
  return 168 + 32 * L + 8 * xx + j - 24;
}
#define MSTAMP()
#define WSTAMP2()
#define SST(k)
#define TSR(rn, k, ss, t)
#define TS(k, ss, t)
#define FSD
#define FS(k)
#define FD(ss)
struct MegaArgs {
  const u16* mem0;
  const u16* Dskip;
  char* ws;
  u16* out;
  const u16* x0;
  const u16 *logAre, *Aim, *Cre, *Cim, *logdt;
  const u16 *Wq, *Wk, *Wv, *Wo;
  int rn;
};

__device__ __forceinline__ int lane_id() {
  int l;
  asm volatile("v_mbcnt_lo_u32_b32 %0, -1, 0\n\tv_mbcnt_hi_u32_b32 %0, -1, %0" : "=v"(l));
  return l;
}
__device__ __forceinline__ bool spin_timeout(unsigned spins, unsigned* tmo) {
  if (spins > SPIN_CAP) {
    if (lane_id() == 0) __hip_atomic_store((gu32*)tmo, 1u, RLX_AGENT);
    return true;
  }
  return false;
}
#define PSWH (h & 2)
#define CD 150
#define GP 230
#define GR 50
#define GM 0
#define ROT (4 * (h & 1))
#define CPRIO 1
#define P1PRIO 3
#define QLEAD 2
#define POLL_SLEEP 12
#define SLP_C POLL_SLEEP
#define SLP_A POLL_SLEEP
#define SLP_W POLL_SLEEP
#define SLP_R POLL_SLEEP
#define SLP_V POLL_SLEEP
#define SLP_Q POLL_SLEEP
#define SLP_M 4
#define AWPRIO 2
#define SLP_S 8
#define SPOS 4
#define CVPRE 2
#define QHV 0
#define QCHK 0
#define LEADN 99
#define GATE_V 160
#define GATE_R 380
#define GATE_N 440
#define AWAHEAD 1
#define GATE_M 150
#define GATE_M2 380
#define GATE_Q 170
#define SLP_Q1 24
#define QPF 3
#define QUP 7
#define QEP 3
#define AWD 11
#define GPER 226
#define GMA 60
#define GMR 60
#define GSTEP 30
#define GATE_RT 200
#define GATE_1 160
#define N1S 20
#define N1E 36
#define PRI_A 3
#define PRI_WA 2
#define PRI_WB 1
#define AWSKIP 3
#define PRI_0 3
#define AWDEF 1
#define SLP_F 2
#define EXP_OFF 0
#define AWSPEC 1
#define CVTHR 48
#define NR_C 8
#define NR_R 8
#define NR_W 8
#define NR_N 2
#define QAHEAD 0
#define NS0 4
#define SLP_N POLL_SLEEP
template <int SLP, int WPL>
__device__ __forceinline__ bool wait_n(const unsigned* f, int n, unsigned* tmo) {
  const int l = lane_id();
  for (unsigned spins = 0;; ++spins) {
    unsigned v = 1u;
    if (l < n) v = __hip_atomic_load((gu32*)f + (l / WPL) * 32 + l % WPL, RLX_AGENT);
    if (__all(v != 0u)) break;
    if (spin_timeout(spins, tmo)) return false;
    __builtin_amdgcn_s_sleep(SLP);
  }
  asm volatile("" ::: "memory");
  return true;
}
__device__ __forceinline__ unsigned now10() { return (unsigned)__builtin_amdgcn_s_memrealtime(); }
__device__ __forceinline__ void gate(const unsigned t) {
  for (int i = 0; i < 64; ++i) {
    const int d = (int)(t - now10());
    if (d <= 0) break;
    if (d > 100) __builtin_amdgcn_s_sleep(32);
    else if (d > 25) __builtin_amdgcn_s_sleep(8);
    else __builtin_amdgcn_s_sleep(2);
  }
}
template <int SLP, int WPL>
__device__ __forceinline__ bool grp_wait(const bool leader, volatile unsigned* sw, const unsigned val, const unsigned* f, int n,
                                         unsigned* tmo, const unsigned t) {
  unsigned spins = 0;
  gate(t);
  if (leader) {
    const int l = lane_id();
    for (;; ++spins) {
      unsigned v = 1u;
      if (l < n) v = __hip_atomic_load((gu32*)f + (l / WPL) * 32 + l % WPL, RLX_AGENT);
      if (__all(v != 0u) || spin_timeout(spins, tmo)) break;
      __builtin_amdgcn_s_sleep(SLP);
    }
    if ((threadIdx.x & 63) == 0) *sw = 2u * val + (spins ? 1u : 0u);
  } else {
    unsigned v;
    for (; (v = __builtin_amdgcn_readfirstlane(*sw)) < 2u * val; ++spins) {
      if (spins > 64u * SPIN_CAP) break;
      __builtin_amdgcn_s_sleep(SLP_F);
    }
    spins = v & 1u;
  }
  asm volatile("" ::: "memory");
  return spins != 0u;
}
template <int SLP, int WPL>
__device__ __forceinline__ bool wg_wait(const unsigned* f, int n, unsigned* tmo) {
  if (threadIdx.x < 64) (void)wait_n<SLP, WPL>(f, n, tmo);
  __syncthreads();
  asm volatile("" ::: "memory");
  return true;
}
__device__ __forceinline__ void set_flag(unsigned* f) { __hip_atomic_store((gu32*)f, 1u, RLX_AGENT); }
#define DRAIN() asm volatile("s_waitcnt vmcnt(0)" ::: "memory")
#define MKRS(ptr, bytes) __builtin_amdgcn_make_buffer_rsrc((void*)(ptr), 0, (int)(bytes), 0x00020000)
typedef unsigned v2u __attribute__((ext_vector_type(2)));
#define LD128(rs, voff, soff) __builtin_amdgcn_raw_buffer_load_b128(rs, voff, soff, AUX_SC1)
#define LD64(rs, voff, soff) __builtin_amdgcn_raw_buffer_load_b64(rs, voff, soff, AUX_SC1)
#define ST128(v, rs, voff, soff)                                              \
  do {                                                                        \
    const v4u st_d_ = (v);                                                    \
    __builtin_amdgcn_raw_buffer_store_b128(st_d_, rs, voff, soff, AUX_SC1);   \
    asm volatile("s_nop 1" ::"v"(st_d_) : "memory");                          \
  } while (0)
#define ST64(v, rs, voff, soff) __builtin_amdgcn_raw_buffer_store_b64(v, rs, voff, soff, AUX_SC1)
#define BF8(x) __builtin_bit_cast(bf16x8, x)
#define MFMA16(a, b, c) __builtin_amdgcn_mfma_f32_16x16x32_bf16(a, b, c, 0, 0, 0)
typedef short bf16x4 __attribute__((ext_vector_type(4)));
#define MFMA16K(a, b, c) \
  __builtin_amdgcn_mfma_f32_16x16x16bf16_1k(__builtin_bit_cast(bf16x4, a), __builtin_bit_cast(bf16x4, b), c, 0, 0, 0)
__device__ __forceinline__ float fmax_nn(float a, float b) { return __builtin_elementwise_maximum(a, b); }
typedef __bf16 bfv8 __attribute__((ext_vector_type(8)));
typedef __bf16 bfv16 __attribute__((ext_vector_type(16)));
typedef unsigned v8u __attribute__((ext_vector_type(8)));
#define SMF(a, b, c, idx) \
  __builtin_amdgcn_smfmac_f32_16x16x64_bf16(__builtin_bit_cast(bfv8, a), __builtin_bit_cast(bfv16, b), c, idx, 0, 0)
__device__ __forceinline__ float swap_add32(float a, float b) {
  auto r = __builtin_amdgcn_permlane32_swap(__float_as_uint(a), __float_as_uint(b), false, false);
  return __uint_as_float(r[0]) + __uint_as_float(r[1]);
}
__device__ __forceinline__ float swap_add16(float a, float b) {
  auto r = __builtin_amdgcn_permlane16_swap(__float_as_uint(a), __float_as_uint(b), false, false);
  return __uint_as_float(r[0]) + __uint_as_float(r[1]);
}
__device__ __forceinline__ v4u smf_fix(const v4u d) {
  auto x = __builtin_amdgcn_permlane16_swap(d.x, d.z, false, false);
  auto y = __builtin_amdgcn_permlane16_swap(d.y, d.w, false, false);
  return v4u{x[0], x[1], y[0], y[1]};
}
__device__ __forceinline__ v8u smf_bfrag(const u16* p) {
  const v2u a = *reinterpret_cast<const v2u*>(p), b = *reinterpret_cast<const v2u*>(p + 8);
  const v2u c = *reinterpret_cast<const v2u*>(p + 32), d = *reinterpret_cast<const v2u*>(p + 40);
  return v8u{a.x, b.x, a.y, b.y, c.x, d.x, c.y, d.y};
}

#define VAR_PPA 0
#define VAR_PPB 0
#define VAR_THR 0
#define VAR_THR2 0
#define CV_LEAD 100
#define VAR_EARLY 1
#define VAR_DRAIN 1
#define VAR_D1 0
#define VAR_D2 0
#define VAR_DR2 1
#define VAR_S1 0
#define VAR_S2 0
typedef __attribute__((address_space(1))) unsigned long long gu64;
typedef __attribute__((address_space(1))) char gch;
__device__ __forceinline__ int cvt_rpi(float x) {
  int r;
  asm("v_cvt_rpi_i32_f32 %0, %1" : "=v"(r) : "v"(x));
  return r;
}
typedef short v4s __attribute__((ext_vector_type(4)));
typedef __attribute__((address_space(3))) v4s* lp4s;
__device__ __forceinline__ void glds16(const void* sbase, const unsigned voff, const unsigned lds) {
  unsigned keep;
  asm volatile("s_nop 4\n\ts_mov_b32 %0, m0\n\ts_mov_b32 m0, %3\n\ts_nop 0\n\tglobal_load_lds_dwordx4 %1, %2\n\ts_mov_b32 m0, %0"
               : "=&s"(keep)
               : "v"(voff), "s"(sbase), "s"(lds)
               : "memory");
}
__device__ __forceinline__ void glds16c(const void* sbase, const unsigned voff, const unsigned lds) {
  unsigned keep;
  asm volatile("s_nop 4\n\ts_mov_b32 %0, m0\n\ts_mov_b32 m0, %3\n\ts_nop 0\n\tglobal_load_lds_dwordx4 %1, %2 sc1\n\ts_mov_b32 m0, %0"
               : "=&s"(keep)
               : "v"(voff), "s"(sbase), "s"(lds)
               : "memory");
}
__device__ __forceinline__ v2u trd(const unsigned a) {
  return __builtin_bit_cast(v2u, __builtin_amdgcn_ds_read_tr16_b64_v4i16((lp4s)(unsigned long)a));
}
__device__ __forceinline__ unsigned lds_base(char* smem) { return (unsigned)(unsigned long)(__attribute__((address_space(3))) char*)smem; }
#define RAW_BAR()                                           \
  do {                                                      \
    asm volatile("s_waitcnt lgkmcnt(0)" ::: "memory");      \
    __builtin_amdgcn_s_barrier();                           \
    asm volatile("" ::: "memory");                          \
  } while (0)
#define WAIT_VM(n) asm volatile("s_waitcnt vmcnt(" #n ")" ::: "memory")
template <bool SP>
__device__ __forceinline__ void wqkv_issue(const u16* Wm, const int hf, const int h, const int w, const int l, const unsigned sb) {
  const char* base = (const char*)Wm + (size_t)(256 * hf + 32 * w) * (HD * 2) + h * (DH * 2);
#pragma unroll
  for (int i = 0; i < 8; ++i) {
    const int r = l >> 4;
    const int f = SP ? ((r & 1) | ((i & 1) << 1) | (((i >> 1) & 1) << 2)) : (r | (((i >> 1) & 1) << 2));
    glds16(base, (unsigned)((4 * i + r) * (HD * 2) + (((l & 15) ^ (f << 1)) << 4)), sb + w * 16384 + hf * 8192 + i * 1024);
  }
}

__device__ __forceinline__ unsigned row_stats(const v4u raw) {
  float v[8];
#pragma unroll
  for (int e = 0; e < 4; ++e) { v[2 * e] = lo16(raw[e]); v[2 * e + 1] = hi16(raw[e]); }
  float s = 0.f;
#pragma unroll
  for (int e = 0; e < 8; ++e) s += v[e];
  const float mean = wave_sum(s) * (1.0f / DM);
  float q = 0.f;
#pragma unroll
  for (int e = 0; e < 8; ++e) { const float t = v[e] - mean; q += t * t; }
  const float var = wave_sum(q) * (1.0f / DM);
  const float sd = bfr(sqrtf(bfr(bfr(var) + 1e-5f)));
  return (unsigned)f2bf(mean) | ((unsigned)f2bf(sd) << 16);
}
__device__ __forceinline__ void start_kgen(const MegaArgs& A, char* smem, const int hi, const int xi) {
  const int tid = threadIdx.x, j = tid >> 7, u = tid & 127, d = 4 * hi + j;
  const int w = __builtin_amdgcn_readfirstlane(tid >> 6), l = tid & 63;
  float* prm = reinterpret_cast<float*>(smem) + j * 10 * NST;
  float* part = reinterpret_cast<float*>(smem) + 40 * NST + j * 8 * 400;
  float* kd = reinterpret_cast<float*>(smem) + 40 * NST + 32 * 400 + j * 392;
  v4u raw[8], ra, rb;
  const int r0 = 16 * xi + 2 * w;
  if (xi >= 0) {
    ra = *reinterpret_cast<const v4u*>(A.x0 + (size_t)r0 * DM + l * 8);
    rb = *reinterpret_cast<const v4u*>(A.x0 + (size_t)(r0 + 1) * DM + l * 8);
  }
  const u16 p0 = A.logdt[d], p1 = A.logAre[d * NST + u], p2 = A.Aim[d * NST + u], p3 = A.Cre[d * NST + u], p4 = A.Cim[d * NST + u];
  if (w >= 6) {
#pragma unroll
    for (int i = 0; i < 8; ++i) raw[i] = *reinterpret_cast<const v4u*>(A.x0 + (size_t)(16 * hi + 8 * (w - 6) + i) * DM + l * 8);
  }
  if (xi >= 0) {
    const unsigned sa = row_stats(ra), sb = row_stats(rb);
    if (l < 2) __hip_atomic_store((gu32*)(A.ws + OFF_GT) + r0 + l, l ? sb : sa, RLX_AGENT);
  }
  {
    const float dt = bfr(expf(bf2f(p0)));
    const float ar = -bfr(expf(bf2f(p1))) * dt, ai = bf2f(p2) * dt;
    prm[u] = ar;
    prm[NST + u] = ai;
    prm[2 * NST + u] = bf2f(p3) * dt;
    prm[3 * NST + u] = bf2f(p4) * dt;
#pragma unroll
    for (int cls = 1; cls < 4; ++cls) {
      const float st = (float)(1 << cls);
      const float e = expf(ar * st);
      float sn, cs;
      sincosf(ai * st, &sn, &cs);
      prm[(2 + 2 * cls) * NST + u] = e * cs;
      prm[(3 + 2 * cls) * NST + u] = e * sn;
    }
  }
  if (xi >= 0) {
    DRAIN();
    if (l < 8) set_flag((unsigned*)(A.ws + OFF_FL) + FL_F0 + (l * 8 + xi) * 32 + w);
    SST(11);
  }
  __syncthreads();
  SST(1);
  if (w >= 6) {
    unsigned stw = 0u;
#pragma unroll
    for (int i = 0; i < 8; ++i) {
      const unsigned t = row_stats(raw[i]);
      if (l == i) stw = t;
    }
    const int sx = 2 * hi + w - 6;
    if (l < 8) __hip_atomic_store((gu32*)(A.ws + OFF_GT) + 8 * sx + l, stw, RLX_AGENT);
    DRAIN();
    if (l < 8) set_flag((unsigned*)(A.ws + OFF_FL) + FL_FS + (l * 32 + (sx >> 3)) * 32 + (sx & 7));
  } else {
    const int jm = tid / 96, um = tid % 96;
    const int r = um % 12, ng = um / 12;
    const float* prm = reinterpret_cast<float*>(smem) + jm * 10 * NST;
    float* part = reinterpret_cast<float*>(smem) + 40 * NST + jm * 8 * 400;
    const float* pw = prm + (4 + 2 * (r >> 2)) * NST;
    const float p0 = pos_of_index(256 + 32 * r);
    f32x2_t acc[33];
#pragma unroll
    for (int i = 0; i < 33; ++i) acc[i] = f32x2_t{0.f, 0.f};
#pragma unroll 1
    for (int bt = 0; bt < 4; ++bt) {
      const int n = 16 * ng + 4 * bt;
      const f32x4 ar = *reinterpret_cast<const f32x4*>(prm + n), ai = *reinterpret_cast<const f32x4*>(prm + NST + n);
      const f32x4 cr = *reinterpret_cast<const f32x4*>(prm + 2 * NST + n), ci = *reinterpret_cast<const f32x4*>(prm + 3 * NST + n);
      const f32x4 wr = *reinterpret_cast<const f32x4*>(pw + n), wi = *reinterpret_cast<const f32x4*>(pw + NST + n);
      float yr0[4], yi0[4];
#pragma unroll
      for (int k = 0; k < 4; ++k) {
        const float e = __expf(ar[k] * p0);
        float sn, cs;
        __sincosf(ai[k] * p0, &sn, &cs);
        const float zr = e * cs, zi = e * sn;
        yr0[k] = cr[k] * zr - ci[k] * zi;
        yi0[k] = cr[k] * zi + ci[k] * zr;
      }
      f32x2_t yra = {yr0[0], yr0[1]}, yia = {yi0[0], yi0[1]}, wra = {wr[0], wr[1]}, wia = {wi[0], wi[1]};
      f32x2_t yrb = {yr0[2], yr0[3]}, yib = {yi0[2], yi0[3]}, wrb = {wr[2], wr[3]}, wib = {wi[2], wi[3]};
#pragma unroll
      for (int i = 0; i < 33; ++i) {
        acc[i] += yra + yrb;
        const f32x2_t ta = yra * wra - yia * wia, tb = yrb * wrb - yib * wib;
        yia = yra * wia + yia * wra;
        yib = yrb * wib + yib * wrb;
        yra = ta;
        yrb = tb;
      }
    }
#pragma unroll
    for (int i = 0; i < 33; ++i) part[ng * 400 + 33 * r + i] = acc[i].x + acc[i].y;
  }
  __syncthreads();
  SST(2);
  for (int i = u; i < 385; i += 128) {
    const int o = i < 384 ? 33 * (i >> 5) + (i & 31) : 33 * 11 + 32;
    float v = 0.f;
#pragma unroll
    for (int g = 0; g < 8; ++g) v += part[g * 400 + o];
    kd[i] = 2.0f * v;
  }
  __syncthreads();
  const __amdgpu_buffer_rsrc_t rs = MKRS(A.ws + OFF_KF, DM * 2 * CKN * 2);
  for (int un = u; un < 229; un += 128) {
    v4u oh, ol;
#pragma unroll
    for (int e = 0; e < 4; ++e) {
      float kv[2];
#pragma unroll
      for (int k = 0; k < 2; ++k) {
        const int lag = 2080 - (8 * un + 2 * e + k);
        kv[k] = (lag >= 256 && lag < L_SEQ) ? kd[index_of_l(lag) - 256] : 0.f;
      }
      oh[e] = pk2(kv[0], kv[1]);
      ol[e] = pk2(kv[0] - lo16(oh[e]), kv[1] - hi16(oh[e]));
    }
    ST128(oh, rs, (d * 2 * CKN + 8 * un) * 2, 0);
    ST128(ol, rs, (d * 2 * CKN + CKN + 8 * un) * 2, 0);
  }
  DRAIN();
  __syncthreads();
  if (tid == 0) set_flag((unsigned*)(A.ws + OFF_FL) + FL_KG + (hi >> 1) * 32 + (hi & 1));
  SST(3);
}

constexpr int C_SM = 131072, C_SO = C_SM + 8192, C_KT = C_SO + 2048, C_X = C_KT + 16384, C_SQ = C_X + 1024;
constexpr int C_SK = C_SQ + (7 * QS + 128) * 2;
static_assert(C_SK + 8 * QS * 2 <= 163840, "lds");
#define PSW 1
#define CHAIN_ASM_ATT \
  "s_mov_b32 s92, 1152\n" \
  "s_mov_b32 s93, 576\n" \
  "s_mov_b32 s94, 2272\n" \
  "s_mov_b32 s95, 272\n" \
  "s_mov_b32 s96, 288\n" \
  "s_mov_b32 s91, 0x40000001\n" \
  "s_mov_b32 s84, 0x4b400000\n" \
  "s_mov_b32 s85, 0x4b400000\n" \
  "s_mov_b32 s78, 0x3e0293ee\n" \
  "s_mov_b32 s79, 0x3fb8aa3b\n" \
  "s_mov_b32 s76, 0xff00ff00\n" \
  "s_mov_b32 s77, 0xff00ff00\n" \
  "s_add_u32 s58, s56, s60\n" \
  "s_addc_u32 s59, s57, 0\n" \
  "s_xor_b32 s38, s35, 0\n" \
  "s_add_u32 s26, s58, s38\n" \
  "s_addc_u32 s27, s59, 0\n" \
  "s_xor_b32 s38, s35, 1024\n" \
  "s_add_u32 s28, s58, s38\n" \
  "s_addc_u32 s29, s59, 0\n" \
  "s_xor_b32 s38, s35, 2048\n" \
  "s_add_u32 s30, s58, s38\n" \
  "s_addc_u32 s31, s59, 0\n" \
  "s_xor_b32 s38, s35, 3072\n" \
  "s_add_u32 s32, s58, s38\n" \
  "s_addc_u32 s33, s59, 0\n" \
  "s_lshl_b32 s98, s73, 10\n" \
  "s_add_u32 s98, s98, s74\n" \
  "s_add_u32 s98, s98, 131072\n" \
  "s_lshl_b32 s97, s73, 5\n" \
  "s_add_u32 s97, s97, s74\n" \
  "s_add_u32 s97, s97, 158720\n" \
  "s_lshl_b32 s99, s73, 10\n" \
  "v_mbcnt_lo_u32_b32 v48, -1, 0\n" \
  "v_mbcnt_hi_u32_b32 v48, -1, v48\n" \
  "v_and_b32 v49, 15, v48\n" \
  "v_lshl_add_u32 v8, v49, 1, s97\n" \
  "v_bfe_u32 v49, v48, 4, 1\n" \
  "v_mad_u32_u24 v8, v49, s92, v8\n" \
  "v_lshrrev_b32 v49, 5, v48\n" \
  "v_mad_u32_u24 v9, v49, s93, v8\n" \
  "v_mad_u32_u24 v8, v49, s94, v8\n" \
  "v_add_u32 v10, s99, v1\n" \
  "s_lshr_b32 s88, s99, 2\n" \
  "s_add_u32 s88, s88, s74\n" \
  "s_add_u32 s88, s88, 139264\n" \
  "v_lshl_add_u32 v7, v49, 10, s88\n" \
  "v_and_b32 v49, 31, v48\n" \
  "v_lshl_add_u32 v7, v49, 3, v7\n" \
  "v_mov_b64 v[12:13], 0\n" \
  "v_mov_b64 v[14:15], 0\n" \
  "v_mov_b64 v[16:17], 0\n" \
  "v_mov_b64 v[18:19], 0\n" \
  "v_mov_b64 v[20:21], 0\n" \
  "v_mov_b64 v[22:23], 0\n" \
  "s_add_u32 s88, s74, 157760\n" \
  "v_mov_b32 v59, s88\n" \
  "v_mov_b32 v24, 0x44870000\n" \
  "v_mul_f32 v24, s72, v24\n" \
  "s_nop 0\n" \
  "v_readfirstlane_b32 s86, v24\n" \
  "s_lshr_b32 s86, s86, 23\n" \
  "s_addk_i32 s86, 0xff83\n" \
  "s_ashr_i32 s86, s86, 1\n" \
  "s_max_i32 s86, s86, -20\n" \
  "s_min_i32 s86, s86, 100\n" \
  "s_mov_b32 s82, s86\n" \
  "s_lshr_b32 s83, s72, 23\n" \
  "s_sub_i32 s83, s83, 242\n" \
  "s_lshl_b32 s88, s73, 10\n" \
  "v_add_u32 v24, s88, v1\n" \
  "ds_read_b128 v[32:35], v24 offset:0\n" \
  "s_waitcnt lgkmcnt(0)\n" \
  "v_mov_b32 v11, 0\n" \
  "v_dot2c_f32_bf16 v11, v32, v32\n" \
  "v_dot2c_f32_bf16 v11, v33, v33\n" \
  "v_dot2c_f32_bf16 v11, v34, v34\n" \
  "v_dot2c_f32_bf16 v11, v35, v35\n" \
  "s_nop 1\n" \
  "s_nop 0\n" \
  "v_cmpx_lt_f32 vcc, 2.0, v11\n" \
  "ds_max_u32 v59, v11\n" \
  "s_mov_b64 exec, -1\n" \
  ".LCA_TOP_%=:\n" \
  "s_waitcnt lgkmcnt(0)\n" \
  "s_barrier\n" \
  "ds_read_b128 v[40:43], v1 offset:0\n" \
  "ds_read_b128 v[44:47], v1 offset:1024\n" \
  "ds_read_b128 v[48:51], v1 offset:2048\n" \
  "ds_read_b128 v[52:55], v1 offset:3072\n" \
  "ds_read_b128 v[56:59], v1 offset:4096\n" \
  "ds_read_b128 v[60:63], v1 offset:5120\n" \
  "s_cmp_lg_u32 s73, 0\n" \
  "s_cbranch_scc1 .LCA_NOZ_%=\n" \
  "s_xor_b32 s88, s60, 0x8000\n" \
  "s_add_u32 s88, s88, s61\n" \
  "buffer_store_dwordx2 v[12:13], v5, s[40:43], s88 offen sc1\n" \
  ".LCA_NOZ_%=:\n" \
  "s_waitcnt lgkmcnt(5)\n" \
  "v_smfmac_f32_16x16x64_bf16 v[20:23], v[40:43], v[192:199], v0\n" \
  "v_smfmac_f32_16x16x64_bf16 v[12:15], v[40:43], v[64:71], v0\n" \
  "v_smfmac_f32_16x16x64_bf16 v[16:19], v[40:43], v[128:135], v0\n" \
  "ds_read_b128 v[40:43], v1 offset:6144\n" \
  "s_waitcnt lgkmcnt(5)\n" \
  "v_smfmac_f32_16x16x64_bf16 v[20:23], v[44:47], v[200:207], v0\n" \
  "v_smfmac_f32_16x16x64_bf16 v[12:15], v[44:47], v[72:79], v0\n" \
  "v_smfmac_f32_16x16x64_bf16 v[16:19], v[44:47], v[136:143], v0\n" \
  "ds_read_b128 v[44:47], v1 offset:7168\n" \
  "s_waitcnt lgkmcnt(5)\n" \
  "v_smfmac_f32_16x16x64_bf16 v[20:23], v[48:51], v[208:215], v0\n" \
  "v_smfmac_f32_16x16x64_bf16 v[12:15], v[48:51], v[80:87], v0\n" \
  "v_smfmac_f32_16x16x64_bf16 v[16:19], v[48:51], v[144:151], v0\n" \
  "ds_read_b128 v[24:27], v1 offset:10240\n" \
  "ds_read_b128 v[28:31], v1 offset:14336\n" \
  "s_waitcnt lgkmcnt(6)\n" \
  "v_smfmac_f32_16x16x64_bf16 v[20:23], v[52:55], v[216:223], v0\n" \
  "v_smfmac_f32_16x16x64_bf16 v[12:15], v[52:55], v[88:95], v0\n" \
  "v_smfmac_f32_16x16x64_bf16 v[16:19], v[52:55], v[152:159], v0\n" \
  "ds_read_b128 v[32:35], v1 offset:11264\n" \
  "ds_read_b128 v[36:39], v1 offset:15360\n" \
  "s_waitcnt lgkmcnt(7)\n" \
  "v_smfmac_f32_16x16x64_bf16 v[20:23], v[56:59], v[224:231], v0\n" \
  "v_smfmac_f32_16x16x64_bf16 v[12:15], v[56:59], v[96:103], v0\n" \
  "v_smfmac_f32_16x16x64_bf16 v[16:19], v[56:59], v[160:167], v0\n" \
  "s_waitcnt lgkmcnt(6)\n" \
  "v_smfmac_f32_16x16x64_bf16 v[20:23], v[60:63], v[232:239], v0\n" \
  "v_smfmac_f32_16x16x64_bf16 v[12:15], v[60:63], v[104:111], v0\n" \
  "v_smfmac_f32_16x16x64_bf16 v[16:19], v[60:63], v[168:175], v0\n" \
  "s_waitcnt lgkmcnt(5)\n" \
  "v_smfmac_f32_16x16x64_bf16 v[20:23], v[40:43], v[240:247], v0\n" \
  "v_smfmac_f32_16x16x64_bf16 v[12:15], v[40:43], v[112:119], v0\n" \
  "v_smfmac_f32_16x16x64_bf16 v[16:19], v[40:43], v[176:183], v0\n" \
  "s_waitcnt lgkmcnt(4)\n" \
  "v_smfmac_f32_16x16x64_bf16 v[20:23], v[44:47], v[248:255], v0\n" \
  "v_smfmac_f32_16x16x64_bf16 v[12:15], v[44:47], v[120:127], v0\n" \
  "v_smfmac_f32_16x16x64_bf16 v[16:19], v[44:47], v[184:191], v0\n" \
  "s_cmp_eq_u32 s71, 0\n" \
  "s_nop 4\n" \
  "v_permlane32_swap_b32 v20, v22\n" \
  "v_permlane32_swap_b32 v21, v23\n" \
  "v_pk_add_f32 v[20:21], v[20:21], v[22:23]\n" \
  "v_permlane32_swap_b32 v12, v16\n" \
  "v_permlane32_swap_b32 v13, v17\n" \
  "v_cvt_pk_bf16_f32 v20, v20, v21\n" \
  "v_pk_add_f32 v[12:13], v[12:13], v[16:17]\n" \
  "v_permlane32_swap_b32 v14, v18\n" \
  "v_permlane32_swap_b32 v15, v19\n" \
  "ds_write_b16 v9, v20 offset:128\n" \
  "v_cvt_pk_bf16_f32 v12, v12, v13\n" \
  "v_pk_add_f32 v[14:15], v[14:15], v[18:19]\n" \
  "ds_write_b16_d16_hi v9, v20 offset:416\n" \
  "ds_write_b16 v8, v12\n" \
  "v_cvt_pk_bf16_f32 v14, v14, v15\n" \
  "ds_write_b16_d16_hi v8, v12 offset:288\n" \
  "ds_write_b16 v8, v14 offset:576\n" \
  "ds_write_b16_d16_hi v8, v14 offset:864\n" \
  "s_setprio 3\n" \
  "s_waitcnt lgkmcnt(0)\n" \
  "s_barrier\n" \
  "s_cbranch_scc1 .LCA_LAST_%=\n" \
  "ds_read_b32 v11, v1 offset:26688\n" \
  "ds_read_b128 v[40:43], v2 offset:0\n" \
  "ds_read_b128 v[44:47], v3 offset:0\n" \
  "ds_read_b128 v[48:51], v2 offset:64\n" \
  "ds_read_b128 v[52:55], v3 offset:64\n" \
  "ds_read_b128 v[56:59], v2 offset:128\n" \
  "ds_read_b128 v[60:63], v3 offset:128\n" \
  "s_waitcnt lgkmcnt(5)\n" \
  "v_mfma_f32_16x16x32_bf16 v[12:15], v[24:27], v[40:43], 0\n" \
  "v_mfma_f32_16x16x32_bf16 v[16:19], v[28:31], v[40:43], 0\n" \
  "v_readfirstlane_b32 s86, v11\n" \
  "ds_read_b128 v[24:27], v1 offset:12288\n" \
  "ds_read_b128 v[28:31], v1 offset:16384\n" \
  "s_waitcnt lgkmcnt(6)\n" \
  "v_mfma_f32_16x16x32_bf16 v[20:23], v[44:47], v[40:43], 0\n" \
  "s_lshr_b32 s86, s86, 23\n" \
  "s_add_i32 s86, s86, s83\n" \
  "ds_read_b128 v[40:43], v2 offset:192\n" \
  "ds_read_b128 v[44:47], v3 offset:192\n" \
  "s_waitcnt lgkmcnt(7)\n" \
  "v_mfma_f32_16x16x32_bf16 v[12:15], v[32:35], v[48:51], v[12:15]\n" \
  "v_mfma_f32_16x16x32_bf16 v[16:19], v[36:39], v[48:51], v[16:19]\n" \
  "s_ashr_i32 s86, s86, 1\n" \
  "s_max_i32 s86, s86, s82\n" \
  "ds_read_b128 v[32:35], v1 offset:13312\n" \
  "ds_read_b128 v[36:39], v1 offset:17408\n" \
  "s_waitcnt lgkmcnt(8)\n" \
  "v_mfma_f32_16x16x32_bf16 v[20:23], v[52:55], v[48:51], v[20:23]\n" \
  "s_min_i32 s86, s86, 100\n" \
  "s_lshl_b32 s86, s86, 23\n" \
  "s_sub_i32 s84, 0x4a800000, s86\n" \
  "ds_read_b128 v[48:51], v10 offset:18432\n" \
  "ds_read_b128 v[52:55], v10 offset:22528\n" \
  "s_waitcnt lgkmcnt(8)\n" \
  "v_mfma_f32_16x16x32_bf16 v[20:23], v[60:63], v[56:59], v[20:23]\n" \
  "s_waitcnt lgkmcnt(6)\n" \
  "v_mfma_f32_16x16x32_bf16 v[12:15], v[24:27], v[56:59], v[12:15]\n" \
  "v_mfma_f32_16x16x32_bf16 v[16:19], v[28:31], v[56:59], v[16:19]\n" \
  "ds_read_b128 v[56:59], v6 offset:0\n" \
  "ds_read_b128 v[60:63], v6 offset:1024\n" \
  "s_waitcnt lgkmcnt(6)\n" \
  "v_mfma_f32_16x16x32_bf16 v[20:23], v[44:47], v[40:43], v[20:23]\n" \
  "ds_read_b128 v[24:27], v6 offset:2048\n" \
  "ds_read_b128 v[28:31], v6 offset:3072\n" \
  "s_waitcnt lgkmcnt(6)\n" \
  "v_mfma_f32_16x16x32_bf16 v[12:15], v[32:35], v[40:43], v[12:15]\n" \
  "v_mfma_f32_16x16x32_bf16 v[16:19], v[36:39], v[40:43], v[16:19]\n" \
  "ds_read_b128 v[32:35], v6 offset:8192\n" \
  "ds_read_b128 v[36:39], v6 offset:9216\n" \
  "s_waitcnt lgkmcnt(6)\n" \
  "v_cndmask_b32 v20, v20, v21, s[76:77]\n" \
  "v_mov_b32 v41, 0\n" \
  "v_mov_b32 v42, 0\n" \
  "s_nop 1\n" \
  "v_cndmask_b32 v12, v12, v16, s[76:77]\n" \
  "v_cndmask_b32 v13, v13, v17, s[76:77]\n" \
  "v_cndmask_b32 v14, v14, v18, s[76:77]\n" \
  "v_cndmask_b32 v15, v15, v19, s[76:77]\n" \
  "v_maximum3_f32 v16, v20, v12, v13\n" \
  "v_maximum3_f32 v16, v16, v14, v15\n" \
  "s_nop 1\n" \
  "v_max_f32_dpp v16, v16, v16 row_ror:8 row_mask:0xf bank_mask:0xf\n" \
  "v_mov_b32 v17, v16\n" \
  "s_nop 1\n" \
  "v_permlane16_swap_b32 v16, v17\n" \
  "v_max_f32 v16, v16, v17\n" \
  "v_mov_b32 v17, v16\n" \
  "s_nop 1\n" \
  "v_permlane32_swap_b32 v16, v17\n" \
  "v_max_f32 v16, v16, v17\n" \
  "v_mul_f32 v16, s78, v16\n" \
  "v_pk_fma_f32 v[12:13], v[12:13], s[78:79], v[16:17] op_sel_hi:[1,0,0] neg_lo:[0,0,1] neg_hi:[0,0,1]\n" \
  "v_pk_fma_f32 v[14:15], v[14:15], s[78:79], v[16:17] op_sel_hi:[1,0,0] neg_lo:[0,0,1] neg_hi:[0,0,1]\n" \
  "v_fma_f32 v20, v20, s78, -v16\n" \
  "v_exp_f32 v12, v12\n" \
  "v_exp_f32 v13, v13\n" \
  "v_exp_f32 v14, v14\n" \
  "v_exp_f32 v15, v15\n" \
  "v_exp_f32 v20, v20\n" \
  "s_barrier\n" \
  "ds_read_b64 v[22:23], v7 offset:0\n" \
  "buffer_load_dword v7, v5, s[16:19], 0 offen sc1\n" \
  "v_cvt_pk_bf16_f32 v44, v12, v13\n" \
  "v_cvt_pk_bf16_f32 v45, v14, v15\n" \
  "v_pk_add_f32 v[8:9], v[12:13], v[14:15]\n" \
  "v_mov_b32_dpp v46, v44 row_ror:8 row_mask:0xf bank_mask:0xf\n" \
  "v_mov_b32_dpp v17, v20 row_ror:8 row_mask:0xf bank_mask:0xf\n" \
  "v_mov_b32_dpp v47, v45 row_ror:8 row_mask:0xf bank_mask:0xf\n" \
  "v_add_f32 v10, v8, v20\n" \
  "v_cvt_pk_bf16_f32 v40, v20, v17\n" \
  "v_add_f32 v10, v10, v9\n" \
  "v_mfma_f32_16x16x32_bf16 v[12:15], v[48:51], v[44:47], 0\n" \
  "v_cvt_pk_bf16_f32 v43, v20, v17\n" \
  "v_add_f32_dpp v10, v10, v10 row_ror:8 row_mask:0xf bank_mask:0xf\n" \
  "v_mfma_f32_16x16x32_bf16 v[16:19], v[52:55], v[44:47], 0\n" \
  "v_mov_b32 v11, v10\n" \
  "s_waitcnt lgkmcnt(0)\n" \
  "v_mfma_f32_16x16x16_bf16 v[12:15], v[22:23], v[40:41], v[12:15]\n" \
  "v_mfma_f32_16x16x16_bf16 v[16:19], v[22:23], v[42:43], v[16:19]\n" \
  "v_permlane16_swap_b32 v10, v11\n" \
  "v_add_f32 v10, v10, v11\n" \
  "ds_read_b128 v[48:51], v6 offset:10240\n" \
  "v_mov_b32 v11, v10\n" \
  "ds_read_b128 v[52:55], v6 offset:11264\n" \
  "v_mov_b64 v[20:21], 0\n" \
  "v_mov_b64 v[22:23], 0\n" \
  "v_permlane32_swap_b32 v10, v11\n" \
  "v_add_f32 v10, v10, v11\n" \
  "v_rcp_f32 v10, v10\n" \
  "s_nop 0\n" \
  "v_pk_mul_f32 v[12:13], v[12:13], v[10:11] op_sel_hi:[1,0]\n" \
  "v_pk_mul_f32 v[14:15], v[14:15], v[10:11] op_sel_hi:[1,0]\n" \
  "v_pk_mul_f32 v[16:17], v[16:17], v[10:11] op_sel_hi:[1,0]\n" \
  "v_pk_mul_f32 v[18:19], v[18:19], v[10:11] op_sel_hi:[1,0]\n" \
  "v_cvt_pk_bf16_f32 v40, v12, v13\n" \
  "v_cvt_pk_bf16_f32 v41, v14, v15\n" \
  "v_cvt_pk_bf16_f32 v42, v16, v17\n" \
  "ds_write_b64 v4, v[40:41]\n" \
  "v_cvt_pk_bf16_f32 v43, v18, v19\n" \
  "ds_write_b64 v4, v[42:43] offset:1024\n" \
  "v_mov_b64 v[12:13], 0\n" \
  "v_mov_b64 v[14:15], 0\n" \
  "v_mov_b64 v[16:17], 0\n" \
  "v_mov_b64 v[18:19], 0\n" \
  "v_mov_b64 v[8:9], 0\n" \
  "v_mov_b64 v[10:11], 0\n" \
  "s_setprio 0\n" \
  "s_waitcnt lgkmcnt(0)\n" \
  "s_barrier\n" \
  "ds_read_b128 v[40:43], v1 offset:8192\n" \
  "ds_read_b128 v[44:47], v1 offset:9216\n" \
  "s_waitcnt lgkmcnt(1)\n" \
  "v_smfmac_f32_16x16x64_bf16 v[12:15], v[40:43], v[56:63], v0\n" \
  "ds_read_b128 v[56:59], v6 offset:4096\n" \
  "ds_read_b128 v[60:63], v6 offset:5120\n" \
  "v_smfmac_f32_16x16x64_bf16 v[16:19], v[40:43], v[24:31], v0\n" \
  "ds_read_b128 v[24:27], v6 offset:6144\n" \
  "ds_read_b128 v[28:31], v6 offset:7168\n" \
  "s_waitcnt lgkmcnt(4)\n" \
  "v_smfmac_f32_16x16x64_bf16 v[12:15], v[44:47], v[32:39], v0\n" \
  "ds_read_b128 v[32:35], v6 offset:12288\n" \
  "ds_read_b128 v[36:39], v6 offset:13312\n" \
  "v_smfmac_f32_16x16x64_bf16 v[16:19], v[44:47], v[48:55], v0\n" \
  "ds_read_b128 v[48:51], v6 offset:14336\n" \
  "ds_read_b128 v[52:55], v6 offset:15360\n" \
  "s_nop 0\n" \
  "s_waitcnt lgkmcnt(6)\n" \
  "v_smfmac_f32_16x16x64_bf16 v[20:23], v[40:43], v[56:63], v0\n" \
  "s_nop 0\n" \
  "s_waitcnt lgkmcnt(4)\n" \
  "v_smfmac_f32_16x16x64_bf16 v[8:11], v[40:43], v[24:31], v0\n" \
  "v_permlane32_swap_b32 v12, v17\n" \
  "v_permlane32_swap_b32 v13, v16\n" \
  "v_permlane32_swap_b32 v14, v19\n" \
  "v_permlane32_swap_b32 v15, v18\n" \
  "s_waitcnt lgkmcnt(2)\n" \
  "v_smfmac_f32_16x16x64_bf16 v[20:23], v[44:47], v[32:39], v0\n" \
  "v_pk_add_f32 v[12:13], v[12:13], v[16:17] op_sel:[0,1] op_sel_hi:[1,0]\n" \
  "v_pk_add_f32 v[14:15], v[14:15], v[18:19] op_sel:[0,1] op_sel_hi:[1,0]\n" \
  "v_pk_fma_f32 v[12:13], v[12:13], s[84:85], s[84:85] op_sel:[0,0,1] op_sel_hi:[1,0,1]\n" \
  "v_pk_fma_f32 v[14:15], v[14:15], s[84:85], s[84:85] op_sel:[0,0,1] op_sel_hi:[1,0,1]\n" \
  "s_waitcnt lgkmcnt(0)\n" \
  "v_smfmac_f32_16x16x64_bf16 v[8:11], v[44:47], v[48:55], v0\n" \
  "v_lshl_add_u32 v12, v12, 6, s91\n" \
  "v_lshl_add_u32 v13, v13, 2, s75\n" \
  "global_atomic_add_x2 v5, v[12:13], s[26:27]\n" \
  "v_lshl_add_u32 v14, v14, 6, s91\n" \
  "v_lshl_add_u32 v15, v15, 2, s75\n" \
  "global_atomic_add_x2 v5, v[14:15], s[30:31]\n" \
  "s_nop 1\n" \
  "v_permlane32_swap_b32 v20, v9\n" \
  "v_permlane32_swap_b32 v21, v8\n" \
  "v_permlane32_swap_b32 v22, v11\n" \
  "v_permlane32_swap_b32 v23, v10\n" \
  "v_pk_add_f32 v[20:21], v[20:21], v[8:9] op_sel:[0,1] op_sel_hi:[1,0]\n" \
  "v_pk_add_f32 v[22:23], v[22:23], v[10:11] op_sel:[0,1] op_sel_hi:[1,0]\n" \
  "v_pk_fma_f32 v[20:21], v[20:21], s[84:85], s[84:85] op_sel:[0,0,1] op_sel_hi:[1,0,1]\n" \
  "v_pk_fma_f32 v[22:23], v[22:23], s[84:85], s[84:85] op_sel:[0,0,1] op_sel_hi:[1,0,1]\n" \
  "v_lshl_add_u32 v20, v20, 6, s91\n" \
  "v_lshl_add_u32 v21, v21, 2, s75\n" \
  "global_atomic_add_x2 v5, v[20:21], s[32:33]\n" \
  "v_lshl_add_u32 v22, v22, 6, s91\n" \
  "v_lshl_add_u32 v23, v23, 2, s75\n" \
  "global_atomic_add_x2 v5, v[22:23], s[28:29]\n" \
  "v_mbcnt_lo_u32_b32 v48, -1, 0\n" \
  "v_mbcnt_hi_u32_b32 v48, -1, v48\n" \
  "v_lshrrev_b32 v49, 3, v48\n" \
  "v_and_b32 v50, 7, v48\n" \
  "v_lshlrev_b32 v50, 4, v50\n" \
  "v_lshl_or_b32 v44, v49, 8, v50\n" \
  "s_add_u32 s20, s20, 32\n" \
  "s_mov_b32 s21, 0x8000\n" \
  "v_mov_b32 v52, s22\n" \
  "v_mov_b32 v54, s20\n" \
  "s_waitcnt vmcnt(4)\n" \
  ".LCA_TKE_%=:\n" \
  "v_readlane_b32 s34, v7, 0\n" \
  "v_readlane_b32 s38, v7, 16\n" \
  "v_readlane_b32 s39, v7, 32\n" \
  "v_readlane_b32 s23, v7, 48\n" \
  "s_max_u32 s34, s34, s38\n" \
  "s_max_u32 s39, s39, s23\n" \
  "v_readlane_b32 s38, v7, 8\n" \
  "s_max_u32 s34, s34, s39\n" \
  "v_readlane_b32 s39, v7, 24\n" \
  "s_add_u32 s34, s34, %[cd]\n" \
  "v_readlane_b32 s23, v7, 40\n" \
  "s_add_u32 s38, s38, s39\n" \
  "v_readlane_b32 s39, v7, 56\n" \
  "s_add_u32 s38, s38, s23\n" \
  "v_mov_b32 v55, s34\n" \
  "s_add_u32 s38, s38, s39\n" \
  "s_cmp_ge_u32 s38, s20\n" \
  "s_cbranch_scc1 .LCA_TKO_%=\n" \
  "s_sub_u32 s21, s21, 1\n" \
  "s_cbranch_scc1 .LCA_TKO_%=\n" \
  "s_sleep 2\n" \
  "buffer_load_dword v7, v5, s[16:19], 0 offen sc1\n" \
  "s_waitcnt vmcnt(0)\n" \
  "s_branch .LCA_TKE_%=\n" \
  ".LCA_TKO_%=:\n" \
  "ds_write_b64 v52, v[54:55]\n" \
  "s_branch .LCA_WAIU_%=\n" \
  ".LCA_WAIS_%=:\n" \
  "s_cmp_lt_i32 s39, -1000\n" \
  "s_cbranch_scc1 .LCA_WAIE_%=\n" \
  "s_sleep 1\n" \
  ".LCA_WAIU_%=:\n" \
  "s_memrealtime s[38:39]\n" \
  "s_waitcnt lgkmcnt(0)\n" \
  "s_sub_i32 s39, s38, s34\n" \
  "s_cmp_lt_i32 s39, -7\n" \
  "s_cbranch_scc1 .LCA_WAIS_%=\n" \
  "s_cmp_lt_i32 s39, 0\n" \
  "s_cbranch_scc1 .LCA_WAIU_%=\n" \
  ".LCA_WAIE_%=:\n" \
  "buffer_load_dwordx4 v[24:27], v44, s[40:43], s60 offen sc1\n" \
  "buffer_load_dwordx4 v[28:31], v44, s[40:43], s60 offen offset:2048 sc1\n" \
  "v_mbcnt_lo_u32_b32 v48, -1, 0\n" \
  "v_mbcnt_hi_u32_b32 v48, -1, v48\n" \
  "v_and_b32 v49, 15, v48\n" \
  "v_lshl_add_u32 v8, v49, 1, s97\n" \
  "v_bfe_u32 v49, v48, 4, 1\n" \
  "v_mad_u32_u24 v8, v49, s92, v8\n" \
  "v_lshrrev_b32 v49, 5, v48\n" \
  "v_mad_u32_u24 v9, v49, s93, v8\n" \
  "v_mad_u32_u24 v8, v49, s94, v8\n" \
  "v_add_u32 v10, s99, v1\n" \
  "s_lshr_b32 s88, s99, 2\n" \
  "s_add_u32 s88, s88, s74\n" \
  "s_add_u32 s88, s88, 139264\n" \
  "v_lshl_add_u32 v7, v49, 10, s88\n" \
  "v_and_b32 v49, 31, v48\n" \
  "v_lshl_add_u32 v7, v49, 3, v7\n" \
  "v_mov_b64 v[12:13], 0\n" \
  "v_mov_b64 v[14:15], 0\n" \
  "v_mov_b64 v[16:17], 0\n" \
  "v_mov_b64 v[18:19], 0\n" \
  "v_mov_b64 v[20:21], 0\n" \
  "v_mov_b64 v[22:23], 0\n" \
  "s_add_u32 s88, s74, 157760\n" \
  "v_mov_b32 v59, s88\n" \
  "v_lshrrev_b32 v49, 3, v48\n" \
  "v_and_b32 v50, 7, v48\n" \
  "v_lshlrev_b32 v50, 4, v50\n" \
  "v_lshl_or_b32 v44, v49, 8, v50\n" \
  "v_and_b32 v50, 3, v48\n" \
  "v_lshl_add_u32 v40, v50, 2, s98\n" \
  "v_lshrrev_b32 v50, 4, v48\n" \
  "v_mad_u32_u24 v40, v50, s95, v40\n" \
  "v_and_b32 v49, 1, v49\n" \
  "v_lshl_add_u32 v40, v49, 6, v40\n" \
  "v_bfe_u32 v49, v48, 2, 1\n" \
  "v_lshl_add_u32 v40, v49, 7, v40\n" \
  "v_xor_b32 v41, 16, v40\n" \
  "v_xor_b32 v42, 32, v40\n" \
  "v_xor_b32 v43, 48, v40\n" \
  "s_add_i32 s80, s86, 0x32800000\n" \
  "s_add_i32 s81, s86, 0x33800000\n" \
  "s_add_i32 s88, s86, 0xb4000000\n" \
  "v_mov_b32 v46, s88\n" \
  "v_mov_b32 v47, 0\n" \
  "v_mov_b32 v11, 0\n" \
  "s_mov_b32 s89, s60\n" \
  "s_xor_b32 s60, s60, 0x80\n" \
  "s_bitcmp0_b32 s60, 7\n" \
  "s_cselect_b32 s88, 0x8000, 0\n" \
  "s_add_u32 s60, s60, s88\n" \
  "s_and_b32 s60, s60, 0xffff\n" \
  "s_add_u32 s58, s56, s60\n" \
  "s_addc_u32 s59, s57, 0\n" \
  "s_xor_b32 s38, s35, 0\n" \
  "s_add_u32 s26, s58, s38\n" \
  "s_addc_u32 s27, s59, 0\n" \
  "s_xor_b32 s38, s35, 1024\n" \
  "s_add_u32 s28, s58, s38\n" \
  "s_addc_u32 s29, s59, 0\n" \
  "s_xor_b32 s38, s35, 2048\n" \
  "s_add_u32 s30, s58, s38\n" \
  "s_addc_u32 s31, s59, 0\n" \
  "s_xor_b32 s38, s35, 3072\n" \
  "s_add_u32 s32, s58, s38\n" \
  "s_addc_u32 s33, s59, 0\n" \
  "s_add_u32 s66, s66, 0x1000\n" \
  "s_addc_u32 s67, s67, 0\n" \
  "s_sub_u32 s71, s71, 1\n" \
  "s_mov_b32 s87, 0x40000\n" \
  "s_branch .LCA_CHK_%=\n" \
  ".LCA_POLL_%=:\n" \
  "buffer_load_dwordx4 v[24:27], v44, s[40:43], s89 offen sc1\n" \
  "buffer_load_dwordx4 v[28:31], v44, s[40:43], s89 offen offset:2048 sc1\n" \
  ".LCA_CHK_%=:\n" \
  "s_sub_u32 s87, s87, 1\n" \
  "s_cbranch_scc1 .LCA_END_%=\n" \
  "s_waitcnt vmcnt(1)\n" \
  "v_bitop3_b32 v45, v24, v26, 32 bitop3:0x80\n" \
  "v_alignbit_b32 v32, v25, v24, 2\n" \
  "v_alignbit_b32 v34, v27, v26, 2\n" \
  "s_waitcnt vmcnt(0)\n" \
  "v_bitop3_b32 v45, v45, v28, v30 bitop3:0x80\n" \
  "v_cmp_eq_u32 vcc, 0, v45\n" \
  "v_cvt_f32_i32 v32, v32\n" \
  "v_cvt_f32_i32 v33, v25\n" \
  "s_cmp_eq_u64 vcc, 0\n" \
  "s_cbranch_scc0 .LCA_POLL_%=\n" \
  "v_cvt_f32_i32 v34, v34\n" \
  "v_cvt_f32_i32 v35, v27\n" \
  "v_pk_fma_f32 v[32:33], v[32:33], s[80:81], v[46:47]\n" \
  "v_alignbit_b32 v36, v29, v28, 2\n" \
  "v_pk_fma_f32 v[34:35], v[34:35], s[80:81], v[46:47]\n" \
  "v_alignbit_b32 v38, v31, v30, 2\n" \
  "v_cvt_pk_bf16_f32 v32, v32, v34\n" \
  "v_cvt_pk_bf16_f32 v33, v33, v35\n" \
  "v_cvt_f32_i32 v36, v36\n" \
  "ds_write_b32 v40, v32\n" \
  "v_cvt_f32_i32 v37, v29\n" \
  "ds_write_b32 v41, v33\n" \
  "v_cvt_f32_i32 v38, v38\n" \
  "v_cvt_f32_i32 v39, v31\n" \
  "v_pk_fma_f32 v[36:37], v[36:37], s[80:81], v[46:47]\n" \
  "v_pk_fma_f32 v[38:39], v[38:39], s[80:81], v[46:47]\n" \
  "v_dot2c_f32_bf16 v11, v32, v32\n" \
  "v_cvt_pk_bf16_f32 v36, v36, v38\n" \
  "v_cvt_pk_bf16_f32 v37, v37, v39\n" \
  "ds_write_b32 v42, v36\n" \
  "ds_write_b32 v43, v37\n" \
  "v_dot2c_f32_bf16 v11, v33, v33\n" \
  "v_dot2c_f32_bf16 v11, v36, v36\n" \
  "v_dot2c_f32_bf16 v11, v37, v37\n" \
  "s_nop 1\n" \
  "s_nop 0\n" \
  "v_cmpx_lt_f32 vcc, 2.0, v11\n" \
  "ds_max_u32 v59, v11\n" \
  "s_mov_b64 exec, -1\n" \
  "s_branch .LCA_TOP_%=\n" \
  ".LCA_LAST_%=:\n" \
  ".LCA_END_%=:\n" \
  "s_endpgm\n"
#define CHAIN_ASM_VO \
  "s_mov_b32 s92, 1152\n" \
  "s_mov_b32 s93, 576\n" \
  "s_mov_b32 s94, 2272\n" \
  "s_mov_b32 s95, 272\n" \
  "s_mov_b32 s96, 288\n" \
  "s_mov_b32 s91, 0x40000001\n" \
  "s_mov_b32 s84, 0x4b400000\n" \
  "s_mov_b32 s85, 0x4b400000\n" \
  "s_mov_b32 s78, 0x3e0293ee\n" \
  "s_mov_b32 s79, 0x3fb8aa3b\n" \
  "s_mov_b32 s76, 0xff00ff00\n" \
  "s_mov_b32 s77, 0xff00ff00\n" \
  "s_add_u32 s58, s56, s60\n" \
  "s_addc_u32 s59, s57, 0\n" \
  "s_xor_b32 s38, s35, 0\n" \
  "s_add_u32 s26, s58, s38\n" \
  "s_addc_u32 s27, s59, 0\n" \
  "s_xor_b32 s38, s35, 1024\n" \
  "s_add_u32 s28, s58, s38\n" \
  "s_addc_u32 s29, s59, 0\n" \
  "s_xor_b32 s38, s35, 2048\n" \
  "s_add_u32 s30, s58, s38\n" \
  "s_addc_u32 s31, s59, 0\n" \
  "s_xor_b32 s38, s35, 3072\n" \
  "s_add_u32 s32, s58, s38\n" \
  "s_addc_u32 s33, s59, 0\n" \
  "s_lshl_b32 s98, s73, 10\n" \
  "s_add_u32 s98, s98, s74\n" \
  "s_add_u32 s98, s98, 131072\n" \
  "s_lshl_b32 s97, s73, 5\n" \
  "s_add_u32 s97, s97, s74\n" \
  "s_add_u32 s97, s97, 160992\n" \
  "s_lshl_b32 s99, s73, 11\n" \
  "s_add_u32 s99, s99, s74\n" \
  "s_add_u32 s99, s99, 133120\n" \
  "v_mbcnt_lo_u32_b32 v48, -1, 0\n" \
  "v_mbcnt_hi_u32_b32 v48, -1, v48\n" \
  "s_sub_u32 s88, s73, 4\n" \
  "v_and_b32 v51, 15, v48\n" \
  "v_lshl_add_u32 v4, v51, 1, s97\n" \
  "v_bfe_u32 v49, v48, 4, 1\n" \
  "v_mad_u32_u24 v4, v49, s92, v4\n" \
  "v_lshrrev_b32 v50, 5, v48\n" \
  "v_mad_u32_u24 v4, v50, s93, v4\n" \
  "v_lshlrev_b32 v8, 3, v49\n" \
  "v_lshl_add_u32 v8, v51, 4, v8\n" \
  "v_lshl_add_u32 v8, v50, 10, v8\n" \
  "s_lshl_b32 s89, s88, 8\n" \
  "s_add_u32 s89, s89, s74\n" \
  "s_add_u32 s89, s89, 139264\n" \
  "v_and_b32 v49, 31, v48\n" \
  "v_lshl_add_u32 v5, v49, 3, s89\n" \
  "v_lshl_add_u32 v5, v50, 10, v5\n" \
  "v_lshrrev_b32 v49, 2, v51\n" \
  "v_and_b32 v50, 5, v51\n" \
  "v_and_or_b32 v49, v49, 2, v50\n" \
  "s_lshl_b32 s89, s88, 6\n" \
  "s_add_u32 s89, s89, s74\n" \
  "s_add_u32 s89, s89, 160992\n" \
  "v_and_b32 v50, 48, v48\n" \
  "v_add_u32 v6, s89, v50\n" \
  "v_mad_u32_u24 v6, v49, s96, v6\n" \
  "v_lshlrev_b32 v7, 3, v50\n" \
  "v_lshl_add_u32 v7, v49, 4, v7\n" \
  "v_and_b32 v49, 2, v51\n" \
  "v_lshl_add_u32 v7, v49, 29, v7\n" \
  "v_mov_b32 v9, 0\n" \
  "v_lshlrev_b32 v10, 4, v48\n" \
  "v_mov_b64 v[12:13], 0\n" \
  "v_mov_b64 v[14:15], 0\n" \
  "v_mov_b64 v[16:17], 0\n" \
  "v_mov_b64 v[18:19], 0\n" \
  "v_mov_b64 v[20:21], 0\n" \
  "v_mov_b64 v[22:23], 0\n" \
  "s_add_u32 s88, s74, 157760\n" \
  "v_mov_b32 v59, s88\n" \
  "v_mov_b32 v24, 0x44870000\n" \
  "v_mul_f32 v24, s72, v24\n" \
  "s_nop 0\n" \
  "v_readfirstlane_b32 s86, v24\n" \
  "s_lshr_b32 s86, s86, 23\n" \
  "s_addk_i32 s86, 0xff83\n" \
  "s_ashr_i32 s86, s86, 1\n" \
  "s_max_i32 s86, s86, -20\n" \
  "s_min_i32 s86, s86, 100\n" \
  "s_mov_b32 s82, s86\n" \
  "s_lshr_b32 s83, s72, 23\n" \
  "s_sub_i32 s83, s83, 242\n" \
  "s_lshl_b32 s88, s73, 10\n" \
  "v_add_u32 v24, s88, v1\n" \
  "ds_read_b128 v[32:35], v24 offset:0\n" \
  "s_waitcnt lgkmcnt(0)\n" \
  "v_mov_b32 v11, 0\n" \
  "v_dot2c_f32_bf16 v11, v32, v32\n" \
  "v_dot2c_f32_bf16 v11, v33, v33\n" \
  "v_dot2c_f32_bf16 v11, v34, v34\n" \
  "v_dot2c_f32_bf16 v11, v35, v35\n" \
  "s_nop 1\n" \
  "s_nop 0\n" \
  "v_cmpx_lt_f32 vcc, 2.0, v11\n" \
  "ds_max_u32 v59, v11\n" \
  "s_mov_b64 exec, -1\n" \
  ".LCV_TOP_%=:\n" \
  "s_waitcnt lgkmcnt(0)\n" \
  "s_barrier\n" \
  "s_setprio 3\n" \
  "buffer_load_dword v58, v9, s[44:47], s68 offen sc1\n" \
  "s_cmp_lg_u32 s73, 7\n" \
  "s_cbranch_scc1 .LCV_NOTK_%=\n" \
  "s_memrealtime s[38:39]\n" \
  "v_mov_b32 v57, 1\n" \
  "s_mov_b64 exec, 1\n" \
  "s_waitcnt lgkmcnt(0)\n" \
  "v_mov_b32 v56, s38\n" \
  "buffer_atomic_umax v56, off, s[16:19], s24\n" \
  "buffer_atomic_add v57, off, s[16:19], s24 offset:64\n" \
  "s_mov_b64 exec, -1\n" \
  ".LCV_NOTK_%=:\n" \
  "ds_read_b128 v[24:27], v1 offset:0\n" \
  "ds_read_b128 v[28:31], v1 offset:1024\n" \
  "ds_read_b128 v[32:35], v1 offset:2048\n" \
  "ds_read_b128 v[36:39], v1 offset:3072\n" \
  "ds_read_b128 v[40:43], v1 offset:4096\n" \
  "ds_read_b128 v[44:47], v1 offset:5120\n" \
  "ds_read_b128 v[48:51], v1 offset:6144\n" \
  "ds_read_b128 v[52:55], v1 offset:7168\n" \
  "s_waitcnt lgkmcnt(7)\n" \
  "v_smfmac_f32_16x16x64_bf16 v[12:15], v[24:27], v[64:71], v0\n" \
  "s_waitcnt lgkmcnt(6)\n" \
  "v_smfmac_f32_16x16x64_bf16 v[12:15], v[28:31], v[72:79], v0\n" \
  "s_waitcnt lgkmcnt(5)\n" \
  "v_smfmac_f32_16x16x64_bf16 v[12:15], v[32:35], v[80:87], v0\n" \
  "s_waitcnt lgkmcnt(4)\n" \
  "v_smfmac_f32_16x16x64_bf16 v[12:15], v[36:39], v[88:95], v0\n" \
  "s_waitcnt lgkmcnt(3)\n" \
  "v_smfmac_f32_16x16x64_bf16 v[12:15], v[40:43], v[96:103], v0\n" \
  "s_waitcnt lgkmcnt(2)\n" \
  "v_smfmac_f32_16x16x64_bf16 v[12:15], v[44:47], v[104:111], v0\n" \
  "s_waitcnt lgkmcnt(1)\n" \
  "v_smfmac_f32_16x16x64_bf16 v[12:15], v[48:51], v[112:119], v0\n" \
  "s_waitcnt lgkmcnt(0)\n" \
  "v_smfmac_f32_16x16x64_bf16 v[12:15], v[52:55], v[120:127], v0\n" \
  "ds_read_b32 v11, v1 offset:26688\n" \
  "s_nop 6\n" \
  "v_permlane32_swap_b32 v12, v14\n" \
  "v_permlane32_swap_b32 v13, v15\n" \
  "v_pk_add_f32 v[12:13], v[12:13], v[14:15]\n" \
  "s_nop 0\n" \
  "v_cvt_pk_bf16_f32 v12, v12, v13\n" \
  "ds_write_b16 v4, v12\n" \
  "ds_write_b16_d16_hi v4, v12 offset:288\n" \
  "s_setprio 0\n" \
  "v_mov_b64 v[12:13], 0\n" \
  "v_mov_b64 v[14:15], 0\n" \
  "s_waitcnt lgkmcnt(2)\n" \
  "v_readfirstlane_b32 s86, v11\n" \
  "s_lshr_b32 s86, s86, 23\n" \
  "s_add_i32 s86, s86, s83\n" \
  "s_ashr_i32 s86, s86, 1\n" \
  "s_max_i32 s86, s86, s82\n" \
  "s_min_i32 s86, s86, 100\n" \
  "s_lshl_b32 s86, s86, 23\n" \
  "s_sub_i32 s84, 0x4a800000, s86\n" \
  "s_cmp_eq_u32 s71, 0\n" \
  "s_waitcnt lgkmcnt(0)\n" \
  "s_barrier\n" \
  "ds_read_b128 v[60:63], v6 offset:0\n" \
  "v_smfmac_f32_16x16x64_bf16 v[16:19], v[24:27], v[128:135], v0\n" \
  "v_smfmac_f32_16x16x64_bf16 v[20:23], v[24:27], v[192:199], v0\n" \
  "v_smfmac_f32_16x16x64_bf16 v[16:19], v[28:31], v[136:143], v0\n" \
  "v_smfmac_f32_16x16x64_bf16 v[20:23], v[28:31], v[200:207], v0\n" \
  "v_smfmac_f32_16x16x64_bf16 v[16:19], v[32:35], v[144:151], v0\n" \
  "v_smfmac_f32_16x16x64_bf16 v[20:23], v[32:35], v[208:215], v0\n" \
  "v_smfmac_f32_16x16x64_bf16 v[16:19], v[36:39], v[152:159], v0\n" \
  "v_smfmac_f32_16x16x64_bf16 v[20:23], v[36:39], v[216:223], v0\n" \
  "v_smfmac_f32_16x16x64_bf16 v[16:19], v[40:43], v[160:167], v0\n" \
  "v_smfmac_f32_16x16x64_bf16 v[20:23], v[40:43], v[224:231], v0\n" \
  "v_smfmac_f32_16x16x64_bf16 v[16:19], v[44:47], v[168:175], v0\n" \
  "v_smfmac_f32_16x16x64_bf16 v[20:23], v[44:47], v[232:239], v0\n" \
  "v_smfmac_f32_16x16x64_bf16 v[16:19], v[48:51], v[176:183], v0\n" \
  "v_smfmac_f32_16x16x64_bf16 v[20:23], v[48:51], v[240:247], v0\n" \
  "v_smfmac_f32_16x16x64_bf16 v[16:19], v[52:55], v[184:191], v0\n" \
  "v_smfmac_f32_16x16x64_bf16 v[20:23], v[52:55], v[248:255], v0\n" \
  "ds_read_b128 v[24:27], v3 offset:0\n" \
  "ds_read_b128 v[28:31], v3 offset:1024\n" \
  "ds_read_b128 v[32:35], v3 offset:2048\n" \
  "ds_read_b128 v[36:39], v3 offset:3072\n" \
  "ds_read_b128 v[40:43], v3 offset:8192\n" \
  "ds_read_b128 v[44:47], v3 offset:9216\n" \
  "s_nop 0\n" \
  "v_permlane32_swap_b32 v16, v18\n" \
  "v_permlane32_swap_b32 v17, v19\n" \
  "v_permlane32_swap_b32 v20, v22\n" \
  "v_permlane32_swap_b32 v21, v23\n" \
  "v_pk_add_f32 v[16:17], v[16:17], v[18:19]\n" \
  "v_pk_add_f32 v[20:21], v[20:21], v[22:23]\n" \
  "v_cvt_pk_bf16_f32 v16, v16, v17\n" \
  "v_cvt_pk_bf16_f32 v17, v20, v21\n" \
  "ds_write_b64 v5, v[16:17]\n" \
  "s_cbranch_scc1 .LCV_LAST_%=\n" \
  "s_cmp_eq_u32 s73, 7\n" \
  "s_waitcnt vmcnt(2)\n" \
  "s_cbranch_scc1 .LCV_TKW_%=\n" \
  "s_waitcnt vmcnt(0)\n" \
  ".LCV_TKW_%=:\n" \
  "v_cmp_eq_u32 vcc, 0, v58\n" \
  "s_cmp_eq_u64 vcc, 0\n" \
  "s_cselect_b32 s90, 1, 0\n" \
  "s_waitcnt lgkmcnt(0)\n" \
  "s_barrier\n" \
  "s_cbranch_scc0 .LCV_NOTOK_%=\n" \
  "s_mov_b32 m0, s99\n" \
  "s_add_u32 s88, s99, 8192\n" \
  "global_load_lds_dwordx4 v10, s[62:63] sc1\n" \
  "global_load_lds_dwordx4 v10, s[62:63] offset:1024 sc1\n" \
  "s_mov_b32 m0, s88\n" \
  "s_nop 0\n" \
  "global_load_lds_dwordx4 v10, s[64:65] sc1\n" \
  "global_load_lds_dwordx4 v10, s[64:65] offset:1024 sc1\n" \
  ".LCV_NOTOK_%=:\n" \
  "ds_write_b32 v59, v12\n" \
  "ds_read_b128 v[48:51], v3 offset:10240\n" \
  "ds_read_b128 v[52:55], v3 offset:11264\n" \
  "v_permlane32_swap_b32 v16, v17\n" \
  "v_max_u32 v60, 1, v60\n" \
  "v_max_u32 v61, 1, v61\n" \
  "v_max_u32 v16, 1, v16\n" \
  "v_max_u32 v17, 1, v17\n" \
  "buffer_store_dwordx2 v[16:17], v8, s[52:55], s70 offen sc1\n" \
  "v_max_u32 v62, 1, v62\n" \
  "v_max_u32 v63, 1, v63\n" \
  "buffer_store_dwordx4 v[60:63], v7, s[48:51], s69 offen sc1\n" \
  "v_mov_b64 v[18:19], 0\n" \
  "v_mov_b64 v[20:21], 0\n" \
  "v_mov_b64 v[22:23], 0\n" \
  "v_mov_b64 v[16:17], 0\n" \
  "ds_read_b128 v[56:59], v3 offset:4096\n" \
  "ds_read_b128 v[60:63], v3 offset:5120\n" \
  "s_waitcnt lgkmcnt(0)\n" \
  "s_barrier\n" \
  "ds_read_b128 v[4:7], v1 offset:8192\n" \
  "ds_read_b128 v[8:11], v1 offset:9216\n" \
  "s_waitcnt lgkmcnt(1)\n" \
  "v_smfmac_f32_16x16x64_bf16 v[12:15], v[4:7], v[24:31], v0\n" \
  "ds_read_b128 v[24:27], v3 offset:6144\n" \
  "ds_read_b128 v[28:31], v3 offset:7168\n" \
  "v_smfmac_f32_16x16x64_bf16 v[16:19], v[4:7], v[32:39], v0\n" \
  "ds_read_b128 v[32:35], v3 offset:12288\n" \
  "ds_read_b128 v[36:39], v3 offset:13312\n" \
  "s_waitcnt lgkmcnt(4)\n" \
  "v_smfmac_f32_16x16x64_bf16 v[12:15], v[8:11], v[40:47], v0\n" \
  "v_smfmac_f32_16x16x64_bf16 v[16:19], v[8:11], v[48:55], v0\n" \
  "v_smfmac_f32_16x16x64_bf16 v[20:23], v[4:7], v[56:63], v0\n" \
  "ds_read_b128 v[56:59], v3 offset:14336\n" \
  "ds_read_b128 v[60:63], v3 offset:15360\n" \
  "v_mov_b64 v[40:41], 0\n" \
  "v_mov_b64 v[42:43], 0\n" \
  "s_waitcnt lgkmcnt(2)\n" \
  "v_smfmac_f32_16x16x64_bf16 v[20:23], v[8:11], v[32:39], v0\n" \
  "s_nop 0\n" \
  "v_permlane32_swap_b32 v12, v17\n" \
  "v_permlane32_swap_b32 v13, v16\n" \
  "v_permlane32_swap_b32 v14, v19\n" \
  "v_permlane32_swap_b32 v15, v18\n" \
  "v_smfmac_f32_16x16x64_bf16 v[40:43], v[4:7], v[24:31], v0\n" \
  "v_pk_add_f32 v[12:13], v[12:13], v[16:17] op_sel:[0,1] op_sel_hi:[1,0]\n" \
  "v_pk_add_f32 v[14:15], v[14:15], v[18:19] op_sel:[0,1] op_sel_hi:[1,0]\n" \
  "v_pk_fma_f32 v[12:13], v[12:13], s[84:85], s[84:85] op_sel:[0,0,1] op_sel_hi:[1,0,1]\n" \
  "v_pk_fma_f32 v[14:15], v[14:15], s[84:85], s[84:85] op_sel:[0,0,1] op_sel_hi:[1,0,1]\n" \
  "s_waitcnt lgkmcnt(0)\n" \
  "v_smfmac_f32_16x16x64_bf16 v[40:43], v[8:11], v[56:63], v0\n" \
  "v_lshl_add_u32 v12, v12, 6, s91\n" \
  "v_lshl_add_u32 v13, v13, 2, s75\n" \
  "global_atomic_add_x2 v2, v[12:13], s[26:27]\n" \
  "v_lshl_add_u32 v14, v14, 6, s91\n" \
  "v_lshl_add_u32 v15, v15, 2, s75\n" \
  "global_atomic_add_x2 v2, v[14:15], s[30:31]\n" \
  "s_nop 1\n" \
  "v_permlane32_swap_b32 v20, v41\n" \
  "v_permlane32_swap_b32 v21, v40\n" \
  "v_permlane32_swap_b32 v22, v43\n" \
  "v_permlane32_swap_b32 v23, v42\n" \
  "v_pk_add_f32 v[20:21], v[20:21], v[40:41] op_sel:[0,1] op_sel_hi:[1,0]\n" \
  "v_pk_add_f32 v[22:23], v[22:23], v[42:43] op_sel:[0,1] op_sel_hi:[1,0]\n" \
  "v_pk_fma_f32 v[20:21], v[20:21], s[84:85], s[84:85] op_sel:[0,0,1] op_sel_hi:[1,0,1]\n" \
  "v_pk_fma_f32 v[22:23], v[22:23], s[84:85], s[84:85] op_sel:[0,0,1] op_sel_hi:[1,0,1]\n" \
  "v_lshl_add_u32 v20, v20, 6, s91\n" \
  "v_lshl_add_u32 v21, v21, 2, s75\n" \
  "global_atomic_add_x2 v2, v[20:21], s[32:33]\n" \
  "v_lshl_add_u32 v22, v22, 6, s91\n" \
  "v_lshl_add_u32 v23, v23, 2, s75\n" \
  "global_atomic_add_x2 v2, v[22:23], s[28:29]\n" \
  "s_add_u32 s20, s20, 32\n" \
  "s_cmp_eq_u32 s90, 1\n" \
  "s_cbranch_scc0 .LCV_NOF_%=\n" \
  "v_mbcnt_lo_u32_b32 v48, -1, 0\n" \
  "v_mbcnt_hi_u32_b32 v48, -1, v48\n" \
  "v_lshrrev_b32 v49, 3, v48\n" \
  "v_and_b32 v50, 7, v48\n" \
  "v_lshlrev_b32 v50, 4, v50\n" \
  "v_lshl_or_b32 v44, v49, 8, v50\n" \
  "s_mov_b32 s21, 0x8000\n" \
  "v_mov_b32 v52, s22\n" \
  "v_mov_b32 v54, s20\n" \
  "s_branch .LCV_TKF_%=\n" \
  ".LCV_TKE_%=:\n" \
  "s_sleep 1\n" \
  ".LCV_TKF_%=:\n" \
  "ds_read_b64 v[54:55], v52\n" \
  "s_sub_u32 s21, s21, 1\n" \
  "s_cbranch_scc1 .LCV_TKO_%=\n" \
  "s_waitcnt lgkmcnt(0)\n" \
  "v_readfirstlane_b32 s38, v54\n" \
  "v_readfirstlane_b32 s34, v55\n" \
  "s_cmp_eq_u32 s38, s20\n" \
  "s_cbranch_scc0 .LCV_TKE_%=\n" \
  ".LCV_TKO_%=:\n" \
  "s_branch .LCV_WAIU_%=\n" \
  ".LCV_WAIS_%=:\n" \
  "s_cmp_lt_i32 s39, -1000\n" \
  "s_cbranch_scc1 .LCV_WAIE_%=\n" \
  "s_sleep 1\n" \
  ".LCV_WAIU_%=:\n" \
  "s_memrealtime s[38:39]\n" \
  "s_waitcnt lgkmcnt(0)\n" \
  "s_sub_i32 s39, s38, s34\n" \
  "s_cmp_lt_i32 s39, -7\n" \
  "s_cbranch_scc1 .LCV_WAIS_%=\n" \
  "s_cmp_lt_i32 s39, 0\n" \
  "s_cbranch_scc1 .LCV_WAIU_%=\n" \
  ".LCV_WAIE_%=:\n" \
  "buffer_load_dwordx4 v[24:27], v44, s[40:43], s60 offen sc1\n" \
  "buffer_load_dwordx4 v[28:31], v44, s[40:43], s60 offen offset:2048 sc1\n" \
  ".LCV_NOF_%=:\n" \
  "v_mbcnt_lo_u32_b32 v48, -1, 0\n" \
  "v_mbcnt_hi_u32_b32 v48, -1, v48\n" \
  "s_sub_u32 s88, s73, 4\n" \
  "v_and_b32 v51, 15, v48\n" \
  "v_lshl_add_u32 v4, v51, 1, s97\n" \
  "v_bfe_u32 v49, v48, 4, 1\n" \
  "v_mad_u32_u24 v4, v49, s92, v4\n" \
  "v_lshrrev_b32 v50, 5, v48\n" \
  "v_mad_u32_u24 v4, v50, s93, v4\n" \
  "v_lshlrev_b32 v8, 3, v49\n" \
  "v_lshl_add_u32 v8, v51, 4, v8\n" \
  "v_lshl_add_u32 v8, v50, 10, v8\n" \
  "s_lshl_b32 s89, s88, 8\n" \
  "s_add_u32 s89, s89, s74\n" \
  "s_add_u32 s89, s89, 139264\n" \
  "v_and_b32 v49, 31, v48\n" \
  "v_lshl_add_u32 v5, v49, 3, s89\n" \
  "v_lshl_add_u32 v5, v50, 10, v5\n" \
  "v_lshrrev_b32 v49, 2, v51\n" \
  "v_and_b32 v50, 5, v51\n" \
  "v_and_or_b32 v49, v49, 2, v50\n" \
  "s_lshl_b32 s89, s88, 6\n" \
  "s_add_u32 s89, s89, s74\n" \
  "s_add_u32 s89, s89, 160992\n" \
  "v_and_b32 v50, 48, v48\n" \
  "v_add_u32 v6, s89, v50\n" \
  "v_mad_u32_u24 v6, v49, s96, v6\n" \
  "v_lshlrev_b32 v7, 3, v50\n" \
  "v_lshl_add_u32 v7, v49, 4, v7\n" \
  "v_and_b32 v49, 2, v51\n" \
  "v_lshl_add_u32 v7, v49, 29, v7\n" \
  "v_mov_b32 v9, 0\n" \
  "v_lshlrev_b32 v10, 4, v48\n" \
  "v_mov_b64 v[12:13], 0\n" \
  "v_mov_b64 v[14:15], 0\n" \
  "v_mov_b64 v[16:17], 0\n" \
  "v_mov_b64 v[18:19], 0\n" \
  "v_mov_b64 v[20:21], 0\n" \
  "v_mov_b64 v[22:23], 0\n" \
  "s_add_u32 s88, s74, 157760\n" \
  "v_mov_b32 v59, s88\n" \
  "s_cmp_eq_u32 s90, 1\n" \
  "s_cbranch_scc1 .LCV_HAVE_%=\n" \
  "s_mov_b32 s87, 0x40000\n" \
  ".LCV_LATE_%=:\n" \
  "buffer_load_dword v58, v9, s[44:47], s68 offen sc1\n" \
  "s_sub_u32 s87, s87, 1\n" \
  "s_cbranch_scc1 .LCV_END_%=\n" \
  "s_waitcnt vmcnt(0)\n" \
  "v_cmp_eq_u32 vcc, 0, v58\n" \
  "s_cmp_eq_u64 vcc, 0\n" \
  "s_cbranch_scc1 .LCV_GOT_%=\n" \
  "s_sleep 12\n" \
  "s_branch .LCV_LATE_%=\n" \
  ".LCV_GOT_%=:\n" \
  "s_mov_b32 m0, s99\n" \
  "s_add_u32 s88, s99, 8192\n" \
  "global_load_lds_dwordx4 v10, s[62:63] sc1\n" \
  "global_load_lds_dwordx4 v10, s[62:63] offset:1024 sc1\n" \
  "s_mov_b32 m0, s88\n" \
  "s_nop 0\n" \
  "global_load_lds_dwordx4 v10, s[64:65] sc1\n" \
  "global_load_lds_dwordx4 v10, s[64:65] offset:1024 sc1\n" \
  ".LCV_HAVE_%=:\n" \
  "v_lshrrev_b32 v49, 3, v48\n" \
  "v_and_b32 v50, 7, v48\n" \
  "v_lshlrev_b32 v50, 4, v50\n" \
  "v_lshl_or_b32 v44, v49, 8, v50\n" \
  "v_and_b32 v50, 3, v48\n" \
  "v_lshl_add_u32 v40, v50, 2, s98\n" \
  "v_lshrrev_b32 v50, 4, v48\n" \
  "v_mad_u32_u24 v40, v50, s95, v40\n" \
  "v_and_b32 v49, 1, v49\n" \
  "v_lshl_add_u32 v40, v49, 6, v40\n" \
  "v_bfe_u32 v49, v48, 2, 1\n" \
  "v_lshl_add_u32 v40, v49, 7, v40\n" \
  "v_xor_b32 v41, 16, v40\n" \
  "v_xor_b32 v42, 32, v40\n" \
  "v_xor_b32 v43, 48, v40\n" \
  "s_add_u32 s62, s62, 0x80000\n" \
  "s_addc_u32 s63, s63, 0\n" \
  "s_add_u32 s64, s64, 0x40000\n" \
  "s_addc_u32 s65, s65, 0\n" \
  "s_add_u32 s68, s68, 0x1000\n" \
  "s_add_u32 s69, s69, 0x10000\n" \
  "s_add_u32 s70, s70, 0x10000\n" \
  "s_add_i32 s80, s86, 0x32800000\n" \
  "s_add_i32 s81, s86, 0x33800000\n" \
  "s_add_i32 s88, s86, 0xb4000000\n" \
  "v_mov_b32 v46, s88\n" \
  "v_mov_b32 v47, 0\n" \
  "v_mov_b32 v11, 0\n" \
  "s_mov_b32 s89, s60\n" \
  "s_xor_b32 s60, s60, 0x80\n" \
  "s_bitcmp0_b32 s60, 7\n" \
  "s_cselect_b32 s88, 0x8000, 0\n" \
  "s_add_u32 s60, s60, s88\n" \
  "s_and_b32 s60, s60, 0xffff\n" \
  "s_add_u32 s58, s56, s60\n" \
  "s_addc_u32 s59, s57, 0\n" \
  "s_xor_b32 s38, s35, 0\n" \
  "s_add_u32 s26, s58, s38\n" \
  "s_addc_u32 s27, s59, 0\n" \
  "s_xor_b32 s38, s35, 1024\n" \
  "s_add_u32 s28, s58, s38\n" \
  "s_addc_u32 s29, s59, 0\n" \
  "s_xor_b32 s38, s35, 2048\n" \
  "s_add_u32 s30, s58, s38\n" \
  "s_addc_u32 s31, s59, 0\n" \
  "s_xor_b32 s38, s35, 3072\n" \
  "s_add_u32 s32, s58, s38\n" \
  "s_addc_u32 s33, s59, 0\n" \
  "s_add_u32 s66, s66, 0x1000\n" \
  "s_addc_u32 s67, s67, 0\n" \
  "s_sub_u32 s71, s71, 1\n" \
  "s_mov_b32 s87, 0x40000\n" \
  "s_cmp_eq_u32 s90, 1\n" \
  "s_cbranch_scc1 .LCV_CHK_%=\n" \
  ".LCV_POLL_%=:\n" \
  "buffer_load_dwordx4 v[24:27], v44, s[40:43], s89 offen sc1\n" \
  "buffer_load_dwordx4 v[28:31], v44, s[40:43], s89 offen offset:2048 sc1\n" \
  ".LCV_CHK_%=:\n" \
  "s_sub_u32 s87, s87, 1\n" \
  "s_cbranch_scc1 .LCV_END_%=\n" \
  "s_waitcnt vmcnt(1)\n" \
  "v_bitop3_b32 v45, v24, v26, 32 bitop3:0x80\n" \
  "v_alignbit_b32 v32, v25, v24, 2\n" \
  "v_alignbit_b32 v34, v27, v26, 2\n" \
  "s_waitcnt vmcnt(0)\n" \
  "v_bitop3_b32 v45, v45, v28, v30 bitop3:0x80\n" \
  "v_cmp_eq_u32 vcc, 0, v45\n" \
  "v_cvt_f32_i32 v32, v32\n" \
  "v_cvt_f32_i32 v33, v25\n" \
  "s_cmp_eq_u64 vcc, 0\n" \
  "s_cbranch_scc0 .LCV_POLL_%=\n" \
  "v_cvt_f32_i32 v34, v34\n" \
  "v_cvt_f32_i32 v35, v27\n" \
  "v_pk_fma_f32 v[32:33], v[32:33], s[80:81], v[46:47]\n" \
  "v_alignbit_b32 v36, v29, v28, 2\n" \
  "v_pk_fma_f32 v[34:35], v[34:35], s[80:81], v[46:47]\n" \
  "v_alignbit_b32 v38, v31, v30, 2\n" \
  "v_cvt_pk_bf16_f32 v32, v32, v34\n" \
  "v_cvt_pk_bf16_f32 v33, v33, v35\n" \
  "v_cvt_f32_i32 v36, v36\n" \
  "ds_write_b32 v40, v32\n" \
  "v_cvt_f32_i32 v37, v29\n" \
  "ds_write_b32 v41, v33\n" \
  "v_cvt_f32_i32 v38, v38\n" \
  "v_cvt_f32_i32 v39, v31\n" \
  "v_pk_fma_f32 v[36:37], v[36:37], s[80:81], v[46:47]\n" \
  "v_pk_fma_f32 v[38:39], v[38:39], s[80:81], v[46:47]\n" \
  "v_dot2c_f32_bf16 v11, v32, v32\n" \
  "v_cvt_pk_bf16_f32 v36, v36, v38\n" \
  "v_cvt_pk_bf16_f32 v37, v37, v39\n" \
  "ds_write_b32 v42, v36\n" \
  "ds_write_b32 v43, v37\n" \
  "v_dot2c_f32_bf16 v11, v33, v33\n" \
  "v_dot2c_f32_bf16 v11, v36, v36\n" \
  "v_dot2c_f32_bf16 v11, v37, v37\n" \
  "s_nop 1\n" \
  "s_nop 0\n" \
  "v_cmpx_lt_f32 vcc, 2.0, v11\n" \
  "ds_max_u32 v59, v11\n" \
  "s_mov_b64 exec, -1\n" \
  "s_branch .LCV_TOP_%=\n" \
  ".LCV_LAST_%=:\n" \
  "s_waitcnt lgkmcnt(0)\n" \
  "s_nop 0\n" \
  "v_permlane32_swap_b32 v16, v17\n" \
  "v_max_u32 v60, 1, v60\n" \
  "v_max_u32 v61, 1, v61\n" \
  "v_max_u32 v16, 1, v16\n" \
  "v_max_u32 v17, 1, v17\n" \
  "buffer_store_dwordx2 v[16:17], v8, s[52:55], s70 offen sc1\n" \
  "v_max_u32 v62, 1, v62\n" \
  "v_max_u32 v63, 1, v63\n" \
  "buffer_store_dwordx4 v[60:63], v7, s[48:51], s69 offen sc1\n" \
  "s_waitcnt vmcnt(0)\n" \
  ".LCV_END_%=:\n" \
  "s_endpgm\n"
__device__ __forceinline__ v4u mk_srd(const void* p, const unsigned bytes) {
  const unsigned long long a = (unsigned long long)p;
  return v4u{(unsigned)a, (unsigned)(a >> 32) & 0xffffu, bytes, 0x00020000u};
}
template <bool ATT>
__device__ __forceinline__ void role_chain_t(const MegaArgs& A, char* smem, const int L, const int h, const int rep) {
  u16* swo = reinterpret_cast<u16*>(smem);
  const int w = __builtin_amdgcn_readfirstlane(threadIdx.x >> 6);
  const int wc = (w + ROT) & 7, psw = PSW ? (PSWH) : 0;
  char* ws = A.ws;
  unsigned* FL = (unsigned*)(ws + OFF_FL);
  unsigned* TMO = FL + FL_TMO;
#define LANE_IDS()                         \
  const int l = lane_id();                 \
  const int c = l & 15, g = l >> 4;        \
  (void)c; (void)g;
  float* sgm = reinterpret_cast<float*>(smem + C_X);
  float* sgf = sgm + 8;
  unsigned* FQ = FL + FL_FQ + L * 64 * 32 * 32;
  v8u w0[8], w1[8], w2[8];
  {
    LANE_IDS();
    const unsigned sb = lds_base(smem);
    const int q = (l >> 2) & 3, p = l & 3;
    const int f = (q & 1) | ((g & 1) << 1) | ((q >> 1) << 2);
    const unsigned ra = sb + (16 * (g >> 1) + 4 * (g & 1) + (q & 1) + 8 * (q >> 1)) * 256 + (w ^ f) * 32 + p * 8;
#define W_READ(W, hf, X)                                                       \
  {                                                                           \
    _Pragma("unroll") for (int t = 0; t < 4; ++t) {                           \
      const unsigned a_ = (ra ^ (X)) + (hf) * 8192 + t * 32768;               \
      const v2u r0 = trd(a_), r1 = trd(a_ + 512), r2 = trd(a_ + 16384), r3 = trd(a_ + 16384 + 512); \
      W[4 * (hf) + t] = v8u{r0.x, r0.y, r1.x, r1.y, r2.x, r2.y, r3.x, r3.y};  \
    }                                                                         \
    _Pragma("unroll") for (int t = 0; t < 4; ++t) asm volatile("" : "+v"(W[4 * (hf) + t])); \
  }
#define W_STAGE(M, hf, VM, NEXT)                                               \
  {                                                                           \
    WAIT_VM(VM);                                                              \
    RAW_BAR();                                                                \
    if ((M) == 0 && ATT) { W_READ(w0, hf, 0); W_READ(w2, hf, 128); }          \
    if ((M) == 1 && ATT) { W_READ(w1, hf, 0); }                               \
    if ((M) == 1 && !ATT) { W_READ(w0, hf, 0); }                              \
    if ((M) == 2 && !ATT) { W_READ(w1, hf, 128); W_READ(w2, hf, 0); }         \
    RAW_BAR();                                                                \
    NEXT;                                                                     \
  }
#define WO_ISSUE(hf)                                                          \
  {                                                                           \
    const char* base_ = (const char*)A.Wo + (size_t)(h * DH + 64 * (hf) + 8 * w) * (DM * 2); \
    _Pragma("unroll") for (int i = 0; i < 8; ++i) {                           \
      const int fd = (i & 1) | (((i >> 2) & 1) << 1) | ((w & 1) << 2);        \
      glds16(base_, (unsigned)(i * 1024 + ((l ^ (fd << 1)) << 4)), sb + w * 16384 + (hf) * 8192 + i * 1024); \
    }                                                                         \
  }
    wqkv_issue<true>(A.Wv, 0, h, w, l, sb);
    wqkv_issue<true>(A.Wv, 1, h, w, l, sb);
    v4u wr[16];
#define WR_LD(i, R) \
  asm volatile("global_load_dwordx4 %0, %1, %2" : "={" R "}"(wr[i]) : "v"(l * 16), "s"((const char*)A.Wo + (size_t)(h * DH + 16 * w + i) * (DM * 2)) : "memory")
    WR_LD(0, "v[64:67]");    WR_LD(1, "v[68:71]");    WR_LD(2, "v[72:75]");    WR_LD(3, "v[76:79]");
    WR_LD(4, "v[80:83]");    WR_LD(5, "v[84:87]");    WR_LD(6, "v[88:91]");    WR_LD(7, "v[92:95]");
    WR_LD(8, "v[96:99]");    WR_LD(9, "v[100:103]");    WR_LD(10, "v[104:107]");    WR_LD(11, "v[108:111]");
    WR_LD(12, "v[112:115]");    WR_LD(13, "v[116:119]");    WR_LD(14, "v[120:123]");    WR_LD(15, "v[124:127]");
#undef WR_LD
    W_STAGE(2, 0, 24, wqkv_issue<true>(A.Wk, 0, h, w, l, sb));
    SST(4);
    W_STAGE(2, 1, 24, wqkv_issue<true>(A.Wk, 1, h, w, l, sb));
    SST(5);
#define DOT2(x, acc) dot2sq(x, acc)
    float* cs = reinterpret_cast<float*>(smem + C_SM);
    {
      float f2 = 0.f;
      if (!ATT) {
#pragma unroll
        for (int t = 0; t < 8; ++t)
#pragma unroll
          for (int e = 0; e < 8; ++e) f2 = DOT2(w2[t][e], DOT2(w1[t][e], f2));
      }
      f2 = wave_sum_dpp(f2);
      if (l == 0) { sgf[w] = f2; sgm[16] = 0.f; sgm[32 + w] = 0.f; }
      WAIT_VM(16);
      f32x4 ca = {}, cb = {};
#pragma unroll
      for (int i = 0; i < 16; ++i) {
        asm volatile("" : "+v"(wr[i]), "+v"(ca), "+v"(cb));
        const f32x4 x = {lo16(wr[i].x), hi16(wr[i].x), lo16(wr[i].y), hi16(wr[i].y)};
        const f32x4 y = {lo16(wr[i].z), hi16(wr[i].z), lo16(wr[i].w), hi16(wr[i].w)};
        ca += x * x;
        cb += y * y;
      }
      *reinterpret_cast<f32x4*>(cs + w * 512 + l * 8) = ca;
      *reinterpret_cast<f32x4*>(cs + w * 512 + l * 8 + 4) = cb;
    }
    W_STAGE(1, 0, 8, wqkv_issue<true>(A.Wq, 0, h, w, l, sb));
    {
      float m2 = 0.f;
#pragma unroll
      for (int i = 0; i < 8; ++i) m2 += cs[i * 512 + w * 64 + l];
      m2 = wave_max_dpp(m2);
      if (l == 0) sgm[w] = m2;
    }
    W_STAGE(1, 1, 8, wqkv_issue<true>(A.Wq, 1, h, w, l, sb));
    if (w == 0) {
      float m2 = 0.f, f2 = 0.f;
#pragma unroll
      for (int i = 0; i < 8; ++i) { m2 = fmaxf(m2, sgm[i]); f2 += sgf[i]; }
      __builtin_amdgcn_raw_buffer_store_b32(__float_as_uint(sqrtf(m2 * f2) * 1.001f) | 1u, MKRS(FL + FL_G + L * 8 * 32, 8 * 128), l * 128 + h * 4, 0, AUX_SC1);
    }
    SST(6);
    W_STAGE(0, 0, 8, WO_ISSUE(0));
    W_STAGE(0, 1, 8, WO_ISSUE(1));
    SST(7);
#undef W_STAGE
#undef W_READ
#undef WO_ISSUE
#undef DOT2
    const unsigned rb = sb + (2 * (g >> 1) + (q >> 1)) * 16384 + (4 * (g & 1) + (q & 1)) * 1024 + p * 8;
#pragma unroll
    for (int hf = 0; hf < 2; ++hf) {
      if (hf == 0) WAIT_VM(8); else WAIT_VM(0);
      RAW_BAR();
      v4u fr[8];
#pragma unroll
      for (int i = 0; i < 8; ++i) {
        const unsigned a_ = rb + hf * 8192 + (i & 1) * 65536 + ((4 * wc + ((i >> 1) ^ psw)) ^ f) * 32;
        const v2u r0 = trd(a_), r1 = trd(a_ + 2048);
        fr[i] = v4u{r0.x, r0.y, r1.x, r1.y};
      }
#pragma unroll
      for (int i = 0; i < 8; ++i) asm volatile("" : "+v"(fr[i]));
      RAW_BAR();
#pragma unroll
      for (int i = 0; i < 8; ++i) *reinterpret_cast<v4u*>(swo + w * 8192 + hf * 4096 + i * 512 + l * 8) = fr[i];
    }
  }
  WSTAMP2();
  v4u m0r;
  {
    LANE_IDS();
    m0r = *reinterpret_cast<const v4u*>(A.mem0 + (c & 7) * DM + 64 * w + 16 * g + 8 * (c >> 3));
  }
  const unsigned sb = lds_base(smem);
  const char* gK = ws + OFF_QK + L * SZ_QK + ((size_t)h * 16 + 2 * w) * 1024;
  const char* gV = ws + OFF_VF + L * SZ_O + ((size_t)h * 8 + 2 * w - 8) * 1024;
  float gw2;
  {
    const unsigned* gp = FL + FL_G + (L * 8 + (blockIdx.x & 7)) * 32;
    unsigned gv;
    for (unsigned spins = 0;; ++spins) {
      gv = __hip_atomic_load((gu32*)gp + (lane_id() & 31), RLX_AGENT);
      if (__all(gv != 0u)) break;
      if (spin_timeout(spins, TMO)) return;
      __builtin_amdgcn_s_sleep(4);
    }
    const float gm = wave_max_dpp(__uint_as_float(gv));
    asm volatile("s_nop 0\n\tv_readfirstlane_b32 %0, %1" : "=s"(gw2) : "v"(gm * gm * 1.06f));
  }
  v8u pv;
  {
    LANE_IDS();
    *reinterpret_cast<v4u*>(smem + C_SM + w * 1024 + l * 16) = m0r;
    const unsigned voff = g * 256 + c * 8, wp = sb + w * 16384 + l * 16;
    pv[0] = (c & 8) ? 0xEEEE : 0x4444;
    pv[1] = sb + C_SM + l * 16;
    pv[2] = ATT ? sb + C_SQ + ((c & 7) * QS + 8 * g) * 2 : voff;
    pv[3] = ATT ? sb + C_SK + ((4 * ((c >> 2) & 1) + 2 * (c >> 3) + (c & 1)) * QS + 8 * g) * 2 : wp;
    pv[4] = sb + (c < 8 ? C_SO : C_SQ) + w * 256 + ((c & 7) + 8 * (g >> 1)) * 16 + 8 * (g & 1);
    pv[5] = voff;
    pv[6] = wp;
    pv[7] = sb + C_SO + (l >> 5) * 1024 + w * 256 + (l & 31) * 8;
  }
  if (!ATT) {
    if (!wait_n<4, 1>(FQ + h * 32, 1, TMO)) return;
    const unsigned lo = lane_id() * 16, d = sb + C_KT + (2 * w - 8) * 1024;
    glds16c(gK, lo, d);
    glds16c(gK + 1024, lo, d + 1024);
    glds16c(gV, lo, d + 8192);
    glds16c(gV + 1024, lo, d + 9216);
  }
  DRAIN();
  SST(8);
  const v4u rsA = mk_srd(ws + OFF_ACC + L * SZ_ACC, SZ_ACC);
  const v4u rsF = mk_srd(FQ, 64 * 32 * 32 * 4);
  const v4u rsMK = mk_srd(ws + OFF_MEMK + L * SZ_MEM, SZ_MEM);
  const v4u rsMV = mk_srd(ws + OFF_MEMV + L * SZ_MEM, SZ_MEM);
  const char* pAB = ws + OFF_ACC + L * SZ_ACC + (wc - w) * 4096;
  const char* pGK = gK + NH * 16 * 1024;
  const char* pGV = gV + NH * 8 * 1024;
  const unsigned* pFK = FL + FL_FK + (L * 64 * 32 + h) * 32 + w;
  unsigned sc[8];
  sc[0] = (32 + h) * 128;
  sc[1] = (h * 4 + w - 4) * 512;
  sc[2] = (h * DH + 16 * (w - 4)) * MM * 2;
  sc[3] = NSEG - 1;
  sc[4] = __float_as_uint(gw2);
  sc[5] = w;
  sc[6] = sb;
  sc[7] = h == 0 ? 0xd4000000u - 2u - (1u << 29) : 0xd4000000u;
  const char* dbg = ws;
  const unsigned tg = (now10() / GR) * GR;
  const v4u rsT = mk_srd(FL + FL_TK + L * 256, 1024);
#define CHAIN_OPS                                                                                                          \
  ::"{v[64:71]}"(w0[0]), "{v[72:79]}"(w0[1]), "{v[80:87]}"(w0[2]), "{v[88:95]}"(w0[3]), "{v[96:103]}"(w0[4]),              \
      "{v[104:111]}"(w0[5]), "{v[112:119]}"(w0[6]), "{v[120:127]}"(w0[7]), "{v[128:135]}"(w1[0]), "{v[136:143]}"(w1[1]),    \
      "{v[144:151]}"(w1[2]), "{v[152:159]}"(w1[3]), "{v[160:167]}"(w1[4]), "{v[168:175]}"(w1[5]), "{v[176:183]}"(w1[6]),    \
      "{v[184:191]}"(w1[7]), "{v[192:199]}"(w2[0]), "{v[200:207]}"(w2[1]), "{v[208:215]}"(w2[2]), "{v[216:223]}"(w2[3]),    \
      "{v[224:231]}"(w2[4]), "{v[232:239]}"(w2[5]), "{v[240:247]}"(w2[6]), "{v[248:255]}"(w2[7]), "{v[0:7]}"(pv),          \
      "{s[40:43]}"(rsA), "{s[44:47]}"(rsF), "{s[48:51]}"(rsMK), "{s[52:55]}"(rsMV), "{s[56:57]}"(pAB), "{s60}"(w * 4096), "{s61}"(h * 1024), \
      "{s[62:63]}"(pGK), "{s[64:65]}"(pGV), "{s[66:67]}"(pFK), "{s68}"(sc[0]), "{s69}"(sc[1]), "{s70}"(sc[2]),    \
      "{s71}"(sc[3]), "{s72}"(sc[4]), "{s73}"(sc[5]), "{s74}"(sc[6]), "{s75}"(sc[7]), "{s[36:37]}"(dbg),                   \
      "{s34}"(tg), "{s35}"(psw * 1536), [gp] "n"(GP), [gr] "n"(GR), [gm] "n"(GM), \
      "{s[16:19]}"(rsT), "{s20}"(0), "{s22}"(sb + C_X + 128 + 8 * (w & 3)), "{s24}"((h & 3) * 256), [cd] "n"(CD) \
      : "memory", "vcc", "scc", "s99", "v63"
  if (ATT) asm volatile("s_mov_b64 exec, -1\ns_nop 4\n" CHAIN_ASM_ATT CHAIN_OPS);
  else asm volatile("s_mov_b64 exec, -1\ns_nop 4\n" CHAIN_ASM_VO CHAIN_OPS);
#undef CHAIN_OPS
#undef LANE_IDS
}

__device__ __forceinline__ void role_chain(const MegaArgs& A, char* smem, const int L, const int h, const int rep) {
  if (__builtin_amdgcn_readfirstlane(threadIdx.x >> 6) < 4) role_chain_t<true>(A, smem, L, h, rep);
  else role_chain_t<false>(A, smem, L, h, rep);
}

constexpr int AW_SO = 0, AW_M = 32768, AW_TK = AW_M + 8192, AW_CNT = AW_TK + 98304;
typedef __attribute__((address_space(3))) unsigned lu32;
__device__ __forceinline__ unsigned umin3(unsigned a, unsigned b, unsigned c) {
  return __builtin_elementwise_min(__builtin_elementwise_min(a, b), c);
}
__device__ __forceinline__ bool all_set(const unsigned r) {
  unsigned long long m;
  asm volatile("v_cmp_eq_u32 %0, 0, %1" : "=s"(m) : "v"(r) : "memory");
  return m == 0ull;
}
__device__ __forceinline__ unsigned long long zero_mask(const unsigned r) {
  unsigned long long m;
  asm volatile("v_cmp_eq_u32 %0, 0, %1" : "=s"(m) : "v"(r) : "memory");
  return m;
}
__device__ __forceinline__ int job_seg(const int n) {
  const int m = n - AWD;
  const int r = (m & 1) ? 64 + (m >> 1) : AWD + (m >> 1);
  return __builtin_amdgcn_readfirstlane(n > 127 ? 128 : (n < AWD || n >= 128 - AWD) ? n : r);
}
template <bool AT>
__device__ __forceinline__ void role_aw_t(const MegaArgs& A, char* smem, const int mode, const int a) {
  const int w = __builtin_amdgcn_readfirstlane(threadIdx.x >> 6);
  const int l = threadIdx.x & 63, c = l & 15, g = l >> 4;
  const int qt = w & 1, hh = (w >> 1) & 1, h = 2 * a + hh;
  char* ws = A.ws;
  unsigned* FL = (unsigned*)(ws + OFF_FL);
  unsigned* TMO = FL + FL_TMO;
  const unsigned sb = lds_base(smem);
  constexpr int NP = AT ? 1 : 3;
  const int cp0 = AT ? w : 3 * w - 8;
  v4u wo[2 * NP][8];
  {
    const int q = (l >> 2) & 3, p = l & 3, f = q | ((g & 1) << 2);
    const unsigned ra = sb + (4 * g + q) * 256 + (p & 1) * 16;
#pragma unroll
    for (int r = 0; r < 2; ++r) {
#pragma unroll
      for (int k = 0; k < 2; ++k) {
        const char* base_ = (const char*)A.Wo + ((size_t)((2 * a + r) * DH + 64 * (w & 1) + 32 * k) * DM + 128 * (w >> 1)) * 2;
#pragma unroll
        for (int i = 0; i < 8; ++i) {
          const int fd = g | ((i & 1) << 2);
          glds16(base_, (unsigned)((4 * i + g) * (DM * 2) + ((c ^ (fd << 1)) << 4)), sb + w * 16384 + k * 8192 + i * 1024);
        }
      }
      WAIT_VM(0);
      RAW_BAR();
#pragma unroll
      for (int ks = 0; ks < 4; ++ks) {
#pragma unroll
        for (int ct = 0; ct < 2 * NP; ++ct) {
          const int cp = cp0 + (ct >> 1);
          const unsigned a_ = ra + (cp >> 2) * 32768 + ks * 8192 + ((2 * (cp & 3) + (p >> 1)) ^ f) * 32 + (ct & 1) * 8;
          const v2u lo = trd(a_), hi = trd(a_ + 4096);
          wo[ct][4 * r + ks] = v4u{lo.x, lo.y, hi.x, hi.y};
        }
#pragma unroll
        for (int ct = 0; ct < 2 * NP; ++ct) asm volatile("" : "+v"(wo[ct][4 * r + ks]));
      }
      RAW_BAR();
    }
  }
  lu32* cnt = (lu32*)(unsigned long)(sb + AW_CNT);
  if (threadIdx.x < 16) cnt[threadIdx.x] = 0u;
  WSTAMP2();
  const __amdgpu_buffer_rsrc_t rsW = MKRS(ws + OFF_PW, SZ_PW);
  const unsigned* FQ = FL + FL_FQ + h * 32;
  const v4u rsF = mk_srd(FQ, (128 * 32 - h) * 128);
  const char* gQ = ws + OFF_QK + ((size_t)h * 16 + 8 * qt) * 1024;
  const char* gV = ws + OFF_VF + ((size_t)h * 8 + 4 * qt) * 1024;
  const char* gM = ws + (qt ? OFF_MEMV : OFF_MEMK) + h * 2048;
  const int NJ = (130 - mode) / 3;
#define AWS(gs) (64 + a + ((gs) >> 6) * 16)
  const int mo = AW_M + hh * 4096;
  const unsigned tq0 = __builtin_amdgcn_readfirstlane(sb + AW_TK + (hh * 24 + 8 * qt) * 1024);
  const unsigned tv0 = __builtin_amdgcn_readfirstlane(sb + AW_TK + (hh * 24 + 16 + 4 * qt) * 1024);
  const float scale = 0.08838834764831845f;
  volatile lu32* cM = cnt + 2 + hh;
#define DMA4(x) "global_load_lds_dwordx4 %1, " x " sc1\n\tglobal_load_lds_dwordx4 %1, " x " offset:1024 sc1\n\t"       \
                "global_load_lds_dwordx4 %1, " x " offset:2048 sc1\n\tglobal_load_lds_dwordx4 %1, " x " offset:3072 sc1\n\t"
#define T_DMA(jj, gg)                                                                                                   \
  {                                                                                                                     \
    const unsigned o_ = ((jj) & 1) ? 49152u : 0u;                                                                       \
    const char* q_ = gQ + (size_t)(gg) * (NH * 16 * 1024);                                                              \
    unsigned keep_;                                                                                                     \
    asm volatile("s_nop 4\n\ts_mov_b32 %0, m0\n\ts_mov_b32 m0, %5\n\ts_nop 0\n\t" DMA4("%2")                           \
                 "s_mov_b32 m0, %6\n\ts_nop 0\n\t" DMA4("%3") "s_mov_b32 m0, %7\n\ts_nop 0\n\t" DMA4("%4") "s_mov_b32 m0, %0" \
                 : "=&s"(keep_)                                                                                         \
                 : "v"(l * 16), "s"(q_), "s"(q_ + 4096), "s"(gV + (size_t)(gg) * (NH * 8 * 1024)), "s"(tq0 + o_),        \
                   "s"(tq0 + o_ + 4096u), "s"(tv0 + o_)                                                                 \
                 : "memory");                                                                                           \
  }
#define T_LAND(jj) { if (l == 0) *(volatile lu32*)(cnt + w) = (jj) + 1; }
#define M_DMA(gg)                                                                                                       \
  {                                                                                                                     \
    unsigned keep_;                                                                                                     \
    asm volatile("s_nop 4\n\ts_mov_b32 %0, m0\n\ts_mov_b32 m0, %3\n\ts_nop 0\n\tglobal_load_lds_dwordx4 %1, %2 sc1\n\t"     \
                 "global_load_lds_dwordx4 %1, %2 offset:1024 sc1\n\ts_mov_b32 m0, %0"                                    \
                 : "=&s"(keep_)                                                                                         \
                 : "v"(l * 16), "s"(gM + (size_t)(gg) * (NH * 2048)), "s"(sb + mo + qt * 2048)                           \
                 : "memory");                                                                                           \
  }
#define M_TEST()                                                                                                        \
  {                                                                                                                     \
    const v4u t_ = *reinterpret_cast<const v4u*>(smem + mo + qt * 2048 + l * 16);                                        \
    const v4u u_ = *reinterpret_cast<const v4u*>(smem + mo + qt * 2048 + 1024 + l * 16);                                 \
    ok = __all(umin3(umin3(t_.x, t_.y, t_.z), umin3(u_.x, u_.y, u_.z), umin3(t_.w, u_.w, 1u)) != 0u);                   \
    if (ok) asm volatile("s_mov_b64 exec, 1\n\tds_add_u32 %0, %1\n\ts_mov_b64 exec, -1" ::"v"(sb + AW_CNT + 8 + 4 * hh), "v"(1u) : "memory"); \
  }
#define F_LOAD(r, gg) \
  asm volatile("s_nop 4\n\tbuffer_load_dword %0, %1, %2, %3 offen sc1" : "=v"(r) : "v"(0), "s"(rsF), "s"((gg) * 4096) : "memory")
#define F_TEST(r) all_set(r)
#define W_FLAG(jj, gg)                                                                                                  \
  {                                                                                                                     \
    unsigned old_;                                                                                                      \
    asm volatile("s_mov_b64 exec, 1\n\tds_add_rtn_u32 %0, %1, %2\n\ts_mov_b64 exec, -1\n\ts_waitcnt lgkmcnt(0)"           \
                 : "=&v"(old_) : "v"(sb + AW_CNT + ((jj) & 1) * 36), "v"(1u) : "memory");                               \
    if ((__builtin_amdgcn_readfirstlane(old_) & 7u) == 7u) {                                                            \
      TSR(AWS(gg), 4, (gg) & 63, threadIdx.x & ~63u);                                                                   \
      if (l < NR_W) set_flag(FL + FL_FW + (((gg) * 8 + l) * 4 + (a >> 2)) * 32 + (a & 3));                              \
    }                                                                                                                   \
    pend = false;                                                                                                       \
  }
#define LDS_U(p) ((unsigned)__builtin_amdgcn_readfirstlane(*(p)))
#define TOK_MIN() __builtin_elementwise_min(LDS_U((volatile lu32*)(cnt + 4 + 2 * hh)), LDS_U((volatile lu32*)(cnt + 5 + 2 * hh)))
  __builtin_amdgcn_s_waitcnt(0x0F70);
  RAW_BAR();
  bool pend = false, asked = false;
  unsigned tb1 = now10() - 130 * GPER, tb2 = tb1;
  int gp = 0, gs = job_seg(mode), g1 = job_seg(mode + 3);
  if (!AT) {
    (void)wait_n<SLP_A, 1>(FQ + gs * 1024, 1, TMO);
    T_DMA(0, gs);
    WAIT_VM(0);
    T_LAND(0);
  }
  FSD;
  for (int s = 0; s < NJ; ++s) {
    char* so = smem + AW_SO + (s & 1) * 16384 + l * 16;
    bool got = g1 == 128, land = got, seen = false;
    if (AT) {
      if (TOK_MIN() < (unsigned)(s + 1)) {
        if (pend) {
          WAIT_VM(0);
          W_FLAG(s - 1, gp);
        }
        for (unsigned spins = 0; TOK_MIN() < (unsigned)(s + 1) && spins < 64u * SPIN_CAP; ++spins) __builtin_amdgcn_s_sleep(1);
      }
      asm volatile("" ::: "memory");
      TSR(AWS(gs), 0, gs & 63, 0);
      FS(0);
      const char* tk = smem + AW_TK + ((s & 1) * 2 + hh) * 24 * 1024 + l * 16;
#define TKF(i) (*reinterpret_cast<const bf16x8*>(tk + (i) * 1024))
      bf16x8 q[4], vt[8];
      float x[12], mx;
      bool ok = false;
      {
        f32x4 s0 = {}, s1 = {};
        bf16x8 kd[4], k0[4];
#pragma unroll
        for (int ks = 0; ks < 4; ++ks) {
          q[ks] = TKF(4 * qt + ks);
          kd[ks] = TKF(8 + 4 * qt + ks);
          k0[ks] = TKF(8 + ks);
        }
#pragma unroll
        for (int i = 0; i < 8; ++i) vt[i] = TKF(16 + i);
        __builtin_amdgcn_sched_barrier(0);
#pragma unroll
        for (int ks = 0; ks < 4; ++ks) {
          s0 = MFMA16(kd[ks], q[ks], s0);
          s1 = MFMA16(k0[ks], q[ks], s1);
        }
        scale_pair(s0[0], s0[1], scale, x[0], x[1]);
        scale_pair(s0[2], s0[3], scale, x[2], x[3]);
        scale_pair(s1[0], s1[1], scale, x[4], x[5]);
        scale_pair(s1[2], s1[3], scale, x[6], x[7]);
#pragma unroll
        for (int i = 0; i < 4; ++i) {
          if (4 * g + i > c) x[i] = -3e38f;
          if (!qt) x[4 + i] = -3e38f;
        }
        mx = fmaxf(fmaxf(fmaxf(x[0], x[1]), fmaxf(x[2], x[3])), fmaxf(fmaxf(x[4], x[5]), fmaxf(x[6], x[7])));
        asm volatile("" : "+v"(mx), "+v"(vt[0]), "+v"(vt[1]), "+v"(vt[2]), "+v"(vt[3]), "+v"(vt[4]), "+v"(vt[5]), "+v"(vt[6]), "+v"(vt[7]));
      }
#undef TKF
      FS(1);
      if (asked) {
        WAIT_VM(1);
        M_TEST();
      }
      FS(2);
      if (!ok) {
        if (pend) {
          WAIT_VM(0);
          W_FLAG(s - 1, gp);
        }
        unsigned tb = gs < 64 ? tb1 : tb2, spins = 0;
        gate(tb + gs * GPER);
        for (; !ok; ++spins) {
          if (spin_timeout(spins, TMO)) break;
          if (spins) __builtin_amdgcn_s_sleep(SLP_M);
          M_DMA(gs);
          WAIT_VM(0);
          M_TEST();
        }
        tb = spins > 1 ? now10() - gs * GPER - GMA : tb - GSTEP;
        if (gs < 64) tb1 = tb; else tb2 = tb;
      }
      FS(3);
      __builtin_amdgcn_s_setprio(PRI_A);
      asked = false;
      for (unsigned spins = 0; LDS_U(cM) < 2u * (s + 1) && spins < 64u * SPIN_CAP; ++spins) __builtin_amdgcn_s_sleep(1);
      v4u km[4];
      v2u vm[8];
#pragma unroll
      for (int ks = 0; ks < 4; ++ks) km[ks] = *reinterpret_cast<const v4u*>(smem + mo + (ks * 32 + g * 8 + (c & 7)) * 16);
#pragma unroll
      for (int i = 0; i < 8; ++i) vm[i] = *reinterpret_cast<const v2u*>(smem + mo + 2048 + ((16 * i + c) * MM + 4 * (g & 1)) * 2);
      TSR(AWS(gs), 1, gs & 63, 0);
      FS(4);
      f32x4 m0 = {};
#pragma unroll
      for (int ks = 0; ks < 4; ++ks) m0 = MFMA16(BF8(km[ks]), q[ks], m0);
      scale_pair(m0[0], m0[1], scale, x[8], x[9]);
      scale_pair(m0[2], m0[3], scale, x[10], x[11]);
#pragma unroll
      for (int i = 0; i < 4; ++i) {
        if (g >= 2) x[8 + i] = -3e38f;
        mx = fmaxf(mx, x[8 + i]);
      }
      mx = xmax32(xmax16(mx));
      float sum = 0.f;
#pragma unroll
      for (int i = 0; i < 12; ++i) {
        x[i] = __expf(x[i] - mx);
        sum += x[i];
      }
      sum = xsum32(xsum16(sum));
      const float inv = 1.0f / sum;
      const v2u pa = {pk2(x[0] * inv, x[1] * inv), pk2(x[2] * inv, x[3] * inv)};
      const v2u pb = {pk2(x[4] * inv, x[5] * inv), pk2(x[6] * inv, x[7] * inv)};
      const v4u p0 = qt ? v4u{pb.x, pb.y, pa.x, pa.y} : v4u{pa.x, pa.y, pb.x, pb.y};
      const v2u p1 = {pk2(x[8] * inv, x[9] * inv), pk2(x[10] * inv, x[11] * inv)};
      FS(5);
#pragma unroll
      for (int k = 0; k < 4; ++k) {
        v4u of;
#pragma unroll
        for (int hf = 0; hf < 2; ++hf) {
          f32x4 ao = {};
          ao = MFMA16(vt[2 * k + hf], BF8(p0), ao);
          ao = MFMA16K(vm[2 * k + hf], p1, ao);
          of[2 * hf] = pk2(ao[0], ao[1]);
          of[2 * hf + 1] = pk2(ao[2], ao[3]);
        }
        *reinterpret_cast<v4u*>(so + ((hh * 2 + qt) * 4 + k) * 1024) = of;
      }
      FS(6);
      seen = g1 >= 64 && g1 < 128 && TOK_MIN() >= (unsigned)(s + 2);
      if (pend) {
        WAIT_VM(0);
        W_FLAG(s - 1, gp);
      }
    } else {
      FS(0);
      unsigned n;
      F_LOAD(n, g1);
      if (pend) {
        WAIT_VM(1);
        W_FLAG(s - 1, gp);
      }
      WAIT_VM(0);
      FS(1);
      if (F_TEST(n)) {
        T_DMA(s + 1, g1);
        got = true;
        if (LDS_U(cM) < 2u * (s + 1)) {
          WAIT_VM(0);
          T_LAND(s + 1);
          land = true;
        }
      }
    }
    FS(7);
    RAW_BAR();
    FS(8);
    __builtin_amdgcn_s_setprio(AT ? PRI_WA : PRI_WB);
    TSR(AWS(gs), 2, gs & 63, 256);
    const int voff = (c >> 1) * 2048 + (c & 1) * 64 + g * 16, soff = (gs * 16 + a) * 32768 + cp0 * 128;
#define OFRAG(i) (*reinterpret_cast<const bf16x8*>(so + (((((i) & 7) >> 2) * 2 + ((i) >> 3)) * 4 + ((i) & 3)) * 1024))
    constexpr int NO = AT ? 16 : 3;
    bf16x8 o[NO];
#pragma unroll
    for (int i = 0; i < (AT ? 16 : 2); ++i) o[i] = OFRAG(i);
    unsigned pr;
    const bool look = seen && g1 >= 128 - AWD;
    if (AT) {
      asm volatile("s_cmp_eq_u32 %3, 0\n\ts_cbranch_scc1 1f\n\ts_nop 4\n\tglobal_load_dword %0, %1, %2 sc1\n1:"
                   : "=v"(pr) : "v"(2044), "s"(gM + (size_t)g1 * (NH * 2048)), "s"(__builtin_amdgcn_readfirstlane((int)look)) : "memory", "scc");
    } else {
      F_LOAD(pr, got ? 128 : g1);
    }
    __builtin_amdgcn_sched_barrier(0);
    FS(9);
#pragma unroll
    for (int rt = 0; rt < 2; ++rt) {
      f32x4 acc[2 * NP] = {};
#pragma unroll
      for (int kk = 0; kk < 8; ++kk) {
        const int i = 8 * rt + kk;
        if (!AT && i + 2 < 16) o[(i + 2) % NO] = OFRAG(i + 2);
#pragma unroll
        for (int ct = 0; ct < 2 * NP; ++ct) acc[ct] = MFMA16(BF8(wo[ct][kk]), o[i % NO], acc[ct]);
        if (!AT) __builtin_amdgcn_sched_barrier(0);
      }
      if (rt == 0) FS(10); else FS(12);
      if (AT && rt == 1 && seen) {
        if (look) {
          WAIT_VM(1);
          seen = F_TEST(pr);
        }
        if (seen) {
          M_DMA(g1);
          asked = true;
        }
      }
      if (!AT && rt == 0) {
        WAIT_VM(0);
        if (got && !land) {
          T_LAND(s + 1);
          land = true;
        }
        if (F_TEST(pr)) {
          T_DMA(s + 1, g1);
          got = true;
        }
      }
      if (!AT && rt == 1 && got && !land) {
        WAIT_VM(3);
        T_LAND(s + 1);
      }
#pragma unroll
      for (int u = 0; u < NP; ++u)
        ST128((v4u{pk2(acc[2 * u][0], acc[2 * u][1]), pk2(acc[2 * u][2], acc[2 * u][3]), pk2(acc[2 * u + 1][0], acc[2 * u + 1][1]),
                   pk2(acc[2 * u + 1][2], acc[2 * u + 1][3])}),
              rsW, voff + u * 128, soff + rt * 16384);
      if (rt == 0) FS(11); else FS(13);
    }
    __builtin_amdgcn_s_setprio(AT ? PRI_0 : 0);
    pend = true;
    FD(s);
    TSR(AWS(gs), 3, gs & 63, 256);
    TSR(AWS(gs), 5, gs & 63, 0);
    if (!AT && !got) {
      WAIT_VM(0);
      W_FLAG(s, gs);
      (void)wait_n<SLP_A, 1>(FQ + g1 * 1024, 1, TMO);
      T_DMA(s + 1, g1);
      WAIT_VM(0);
      T_LAND(s + 1);
    }
    gp = gs;
    gs = g1;
    g1 = job_seg(3 * s + 6 + mode);
  }
  WAIT_VM(0);
  W_FLAG(NJ - 1, gp);
#undef AWS
#undef OFRAG
#undef DMA4
#undef T_DMA
#undef T_LAND
#undef M_DMA
#undef M_TEST
#undef F_LOAD
#undef F_TEST
#undef W_FLAG
#undef LDS_U
#undef TOK_MIN
}
__device__ __forceinline__ void role_aw(const MegaArgs& A, char* smem, const int L, const int a) {
  if (__builtin_amdgcn_readfirstlane(threadIdx.x >> 6) < 4) role_aw_t<true>(A, smem, L, a);
  else role_aw_t<false>(A, smem, L, a);
}

__device__ __forceinline__ void role_red(const MegaArgs& A, char* smem, const int idx) {
  const int w = __builtin_amdgcn_readfirstlane(threadIdx.x >> 6);
  const int l = threadIdx.x & 63;
  const int gr = w >> 1, r = idx * 2 + (w & 1);
  char* ws = A.ws;
  unsigned* FL = (unsigned*)(ws + OFF_FL);
  unsigned* TMO = FL + FL_TMO;
  const __amdgpu_buffer_rsrc_t rsW = MKRS(ws + OFF_PW, SZ_PW);
  const __amdgpu_buffer_rsrc_t rsX = MKRS(ws + OFF_XA, OFF_X3 - OFF_XA + SZ_X);
  volatile unsigned* sw = reinterpret_cast<volatile unsigned*>(smem) + (w >> 1) * 32;
  if (threadIdx.x < 128) reinterpret_cast<volatile unsigned*>(smem)[threadIdx.x] = 0u;
  __syncthreads();
  unsigned tb1 = now10() - 130 * GPER, tb2 = tb1;
  const int rn1 = idx < 8 ? 96 + idx : 224 + idx;
  (void)rn1;
  if (w >= 6) return;
  if (w < 4) {
    const __amdgpu_buffer_rsrc_t rz = MKRS(ws + OFF_X2, SZ_X);
    for (int s = gr; s < NSEG; s += 2) ST128((v4u{0u, 0u, 0u, 0u}), rz, l * 16, (s * SEG + r) * DM * 2);
    DRAIN();
    if (l < 8) set_flag(FL + FL_FZ + (2 * l + (idx >> 3)) * 32 + (idx & 7) * 4 + w);
    if (l == 0) __hip_atomic_fetch_add((lu32*)(unsigned long)(lds_base(smem) + 480), 1u, __ATOMIC_RELAXED, __HIP_MEMORY_SCOPE_WORKGROUP);
  }
  for (unsigned spins = 0; reinterpret_cast<volatile unsigned*>(smem)[120] < 4u && spins < 64u * SPIN_CAP; ++spins) __builtin_amdgcn_s_sleep(2);
  for (int n = gr; n < 128; n += 3) {
    const int gs = job_seg(n), L = gs >> 6, s = gs & 63;
    const unsigned* fw = FL + FL_FW + (gs * 8 + (blockIdx.x & (NR_W - 1))) * 4 * 32;
    unsigned tb = L ? tb2 : tb1;
    tb = grp_wait<SLP_R, 4>((w & 1) == 0, sw, n + 1, fw, 16, TMO, tb + gs * GPER) ? now10() - gs * GPER - GMR : tb - GSTEP;
    if (n >= 125 - AWD) tb = now10() + GATE_RT - (n + 3) * GPER;
    if (L || n >= 125 - AWD) tb2 = tb; else tb1 = tb;
#define RTS(k, t) TSR(L ? A.rn : rn1, k, s, (w & 6) * 64 + (t))
    RTS(0, 0);
    RTS(4, 64);
    v4u p[16];
    const v4u xr = LD128(rsX, l * 512, L * (int)(OFF_X3 - OFF_XA) + s * 32768 + r * 16);
#pragma unroll
    for (int k = 0; k < 16; ++k)
      p[k] = LD128(rsW, (l >> 2) * 128 + (l & 3) * 16, (gs * 16 + k) * 32768 + (r >> 1) * 2048 + (r & 1) * 64);
    v4u xo;
    const unsigned nz = L ? 0u : 0x8000u;
#pragma unroll
    for (int e = 0; e < 4; ++e) {
      f32x2_t t[4];
#pragma unroll
      for (int k = 0; k < 4; ++k)
        t[k] = (f32x2_t{lo16(p[4 * k][e]), hi16(p[4 * k][e])} + f32x2_t{lo16(p[4 * k + 1][e]), hi16(p[4 * k + 1][e])}) +
               (f32x2_t{lo16(p[4 * k + 2][e]), hi16(p[4 * k + 2][e])} + f32x2_t{lo16(p[4 * k + 3][e]), hi16(p[4 * k + 3][e])});
      const f32x2_t u = (t[0] + t[1]) + (t[2] + t[3]);
      const unsigned a0 = pk2(u.x, u.y);
      xo[e] = pk2(lo16(xr[e]) + lo16(a0), hi16(xr[e]) + hi16(a0));
      xo[e] = xo[e] ? xo[e] : nz;
    }
    ST128(xo, MKRS(L ? (char*)A.out : ws + OFF_X2, SZ_X), l * 16, (s * SEG + r) * DM * 2);
    RTS(1, 0);
    RTS(5, 64);
    if (L == 0) {
      const unsigned st = row_stats(xo);
      if (l < NR_R) __hip_atomic_store((gu32*)(FL + FL_FR + ((s * 8 + l) * 4 + (r >> 3)) * 32 + (r & 7)), st, RLX_AGENT);
    }
    RTS(6, 64);
    RTS(3, 0);
#undef RTS
  }
}

constexpr int CUN = 2144;
__device__ __forceinline__ bf16x8 conv_bfrag(const unsigned* up, const unsigned sh) {
  const unsigned d0 = up[0], d1 = up[1], d2 = up[2], d3 = up[3], d4 = up[4];
  const v4u r = {__builtin_amdgcn_alignbit(d1, d0, sh), __builtin_amdgcn_alignbit(d2, d1, sh),
                 __builtin_amdgcn_alignbit(d3, d2, sh), __builtin_amdgcn_alignbit(d4, d3, sh)};
  return BF8(r);
}
__device__ __forceinline__ void role_conv(const MegaArgs& A, char* smem, const int cv) {
  u16* Kr = reinterpret_cast<u16*>(smem);
  u16* Uc = Kr + 8 * 2 * CKN;
  u16* Xr = Uc + 8 * CUN;
  u16* To = Uc + 8 * 2 * CUN;
  u16* Su = To + 32 * 8;
  u16* Sx = Su + 32 * 8;
  const int tid = threadIdx.x;
  const int w = __builtin_amdgcn_readfirstlane(tid >> 6);
  const int l = tid & 63, c = l & 15, g = l >> 4;
  const int d = 8 * cv + w;
  char* ws = A.ws;
  unsigned* FL = (unsigned*)(ws + OFF_FL);
  unsigned* TMO = FL + FL_TMO;
  u16* kr = Kr + w * 2 * CKN;
  float* prm = reinterpret_cast<float*>(kr);
  v4u tx[4];
  {
    const unsigned pa = *reinterpret_cast<const unsigned*>(A.logAre + d * NST + 2 * l);
    const unsigned pb = *reinterpret_cast<const unsigned*>(A.Aim + d * NST + 2 * l);
    const unsigned pc = *reinterpret_cast<const unsigned*>(A.Cre + d * NST + 2 * l);
    const unsigned pd = *reinterpret_cast<const unsigned*>(A.Cim + d * NST + 2 * l);
    const float dt = bfr(expf(bf2f(A.logdt[d])));
    if (w < 2) tx[0] = *reinterpret_cast<const v4u*>(A.x0 + (size_t)tid * DM + 8 * cv);
    if (l < 16) reinterpret_cast<unsigned*>(Uc + w * CUN)[l] = 0u;
    if (l < 32) reinterpret_cast<unsigned*>(Uc + w * CUN + 32 + L_SEQ)[l] = 0u;
    if (l < 32) reinterpret_cast<unsigned*>(Uc + w * CUN + 32 + 128)[l] = 0u;
    if (l < 48) {
      reinterpret_cast<v4u*>(kr + 1824)[l] = v4u{0u, 0u, 0u, 0u};
      reinterpret_cast<v4u*>(kr + CKN + 1824)[l] = v4u{0u, 0u, 0u, 0u};
    }
#pragma unroll
    for (int e = 0; e < 2; ++e) {
      const float ar = -bfr(expf(e ? hi16(pa) : lo16(pa))) * dt, ai = (e ? hi16(pb) : lo16(pb)) * dt;
      const float ex = expf(ar);
      float sn, cs;
      sincosf(ai, &sn, &cs);
      prm[2 * l + e] = ar;
      prm[NST + 2 * l + e] = ai;
      prm[2 * NST + 2 * l + e] = (e ? hi16(pc) : lo16(pc)) * dt;
      prm[3 * NST + 2 * l + e] = (e ? hi16(pd) : lo16(pd)) * dt;
      prm[4 * NST + 2 * l + e] = ex * cs;
      prm[5 * NST + 2 * l + e] = ex * sn;
    }
  }
  const float dsk = bf2f(A.Dskip[d]);
  const u16* ka = kr + (c >> 3) * CKN + 2080 - 16 * (c & 7) + 8 * g;
  const unsigned ush = (c & 1) * 16;
#define KFRAG(D0) (*reinterpret_cast<const bf16x8*>(ka - (D0)))
  f32x4 acc[16], far[9];
  v4u xf[3];
  const unsigned* ub1 = reinterpret_cast<const unsigned*>(Uc + w * CUN + (c & ~1) + 8 * g + 32);
  const __amdgpu_buffer_rsrc_t rz1 = MKRS(ws + OFF_XA, SZ_X);
  const int t0 = 16 * (4 * (g & 1) + (g >> 1)) + c;
  {
    const u16* uc = Uc + w * CUN;
    const unsigned* ub = reinterpret_cast<const unsigned*>(uc + (c & ~1) + 8 * g + 32);
    const __amdgpu_buffer_rsrc_t rz = MKRS(ws + OFF_XA, SZ_X);
    const __amdgpu_buffer_rsrc_t rst = MKRS(ws + OFF_GT, L_SEQ * 4);
#define TILE_OUT(T)                                                                            \
  {                                                                                            \
    const float u0 = bf2f(uc[32 + 128 * (T) + t0]), u1 = bf2f(uc[64 + 128 * (T) + t0]);        \
    const float a0 = bfr(bfr(y0) + bfr(u0 * dsk)), a1 = bfr(bfr(y1) + bfr(u1 * dsk));          \
    u16* xp = Xr + (128 * (T) + t0) * 8 + w;                                                   \
    xp[0] = f2bf(bf2f(xp[0]) + a0);                                                            \
    xp[256] = f2bf(bf2f(xp[256]) + a1);                                                        \
  }
#define CHUNK_ST(ck)                                                                           \
  {                                                                                            \
    const v4u o = *reinterpret_cast<const v4u*>(Xr + (64 * (ck) + l) * 8);                     \
    ST128(o, rz, (l >> 5) * 32768 + (l & 31) * 16, (2 * (ck) * 64 + cv) * 512);                \
  }
#define U_ROW(row)                                                                             \
  {                                                                                            \
    const unsigned st_ = __builtin_amdgcn_raw_buffer_load_b32(rst, (row) * 4, 0, AUX_SC1);     \
    const v4u xr_ = *reinterpret_cast<const v4u*>(Xr + (row) * 8);                             \
    const float mb_ = lo16(st_), ri_ = 1.0f / hi16(st_);                                       \
    _Pragma("unroll") for (int e = 0; e < 4; ++e) {                                            \
      const unsigned t_ = pk2(lo16(xr_[e]) - mb_, hi16(xr_[e]) - mb_);                         \
      const unsigned q_ = pk2(lo16(t_) * ri_, hi16(t_) * ri_);                                 \
      Uc[(2 * e) * CUN + 32 + (row)] = (u16)q_;                                                \
      Uc[(2 * e + 1) * CUN + 32 + (row)] = (u16)(q_ >> 16);                                    \
    }                                                                                          \
  }
    const unsigned* FS = FL + FL_FS + (blockIdx.x & 7) * 32 * 32;
    const int gi = 4 * (g * 8 + (l & 7));
    const int tl = 32 * ((l >> 3) & 1) + 8 * g + (l & 7);
    unsigned khl = 0u;
#pragma unroll 1
    for (int ps = 0; ps < 4; ++ps) {
      float v[32];
      {
        const f32x4 ar = *reinterpret_cast<const f32x4*>(prm + gi), ai = *reinterpret_cast<const f32x4*>(prm + NST + gi);
        const f32x4 cr = *reinterpret_cast<const f32x4*>(prm + 2 * NST + gi), ci = *reinterpret_cast<const f32x4*>(prm + 3 * NST + gi);
        const f32x4 wr = *reinterpret_cast<const f32x4*>(prm + 4 * NST + gi), wi = *reinterpret_cast<const f32x4*>(prm + 5 * NST + gi);
        const float p0 = (float)(64 * ps + 32 * ((l >> 3) & 1));
        float yr0[4], yi0[4];
#pragma unroll
        for (int k = 0; k < 4; ++k) {
          const float e = __expf(ar[k] * p0);
          float sn, cs;
          __sincosf(ai[k] * p0, &sn, &cs);
          const float zr = e * cs, zi = e * sn;
          yr0[k] = cr[k] * zr - ci[k] * zi;
          yi0[k] = cr[k] * zi + ci[k] * zr;
        }
        f32x2_t yra = {yr0[0], yr0[1]}, yia = {yi0[0], yi0[1]}, wra = {wr[0], wr[1]}, wia = {wi[0], wi[1]};
        f32x2_t yrb = {yr0[2], yr0[3]}, yib = {yi0[2], yi0[3]}, wrb = {wr[2], wr[3]}, wib = {wi[2], wi[3]};
#pragma unroll
        for (int i = 0; i < 32; ++i) {
          const f32x2_t sm2 = yra + yrb;
          v[i] = sm2.x + sm2.y;
          const f32x2_t ta = yra * wra - yia * wia, tb = yrb * wrb - yib * wib;
          yia = yra * wia + yia * wra;
          yib = yrb * wib + yib * wrb;
          yra = ta;
          yrb = tb;
        }
      }
#pragma unroll
      for (int i = 0; i < 16; ++i) v[i] = swap_add32(v[i], v[i + 16]);
#pragma unroll
      for (int i = 0; i < 8; ++i) {
        v[i] = swap_add16(v[i], v[i + 8]);
        v[i] += dppf<0xB1>(0.f, v[i]);
        v[i] += dppf<0x4E>(0.f, v[i]);
        v[i] += dppf<0x141>(0.f, v[i]);
      }
      float s0 = v[0], s1 = v[1], s2 = v[2], s3 = v[3], s4 = v[4], s5 = v[5], s6 = v[6], s7 = v[7];
      asm volatile("" : "+v"(s0), "+v"(s1), "+v"(s2), "+v"(s3), "+v"(s4), "+v"(s5), "+v"(s6), "+v"(s7));
      s0 = (l & 4) ? s4 : s0; s1 = (l & 4) ? s5 : s1; s2 = (l & 4) ? s6 : s2; s3 = (l & 4) ? s7 : s3;
      s0 = (l & 2) ? s2 : s0; s1 = (l & 2) ? s3 : s1;
      const float kv = 2.0f * ((l & 1) ? s1 : s0);
      khl = pk2(kv, 0.f);
      khl = pk2(kv, kv - lo16(khl));
      kr[2080 - 64 * ps - tl] = (u16)khl;
      kr[CKN + 2080 - 64 * ps - tl] = (u16)(khl >> 16);
      SST(ps);
      if (ps < 2) {
        if (ps == 0) {
          if (w < 2) {
            reinterpret_cast<v4u*>(Xr)[tid] = tx[0];
            (void)wait_n<2, 8>(FL + FL_F0 + ((blockIdx.x & 7) * 8 + 4 * w) * 32, 32, TMO);
            U_ROW(tid);
          }
          __syncthreads();
        }
        f32x4 a0t = {};
#pragma unroll
        for (int b = -1; b < 4; ++b) a0t = MFMA16(KFRAG(-32 * b), conv_bfrag(ub + 16 * b, ush), a0t);
        const float y0 = swap_add32(a0t[0], a0t[1]), y1 = swap_add32(a0t[2], a0t[3]);
        if ((g & 1) == ps) TILE_OUT(0);
        __syncthreads();
        if (w == ps) {
          CHUNK_ST(w);
          DRAIN();
          if (l < 16) set_flag(FL + FL_FC + (((2 * w + (l >> 3)) * 8 + (l & 7)) * 8 + (cv >> 3)) * 32 + (cv & 7));
        }
        SST(4 + ps);
      }
      if (ps == 2) {
        if (w >= 2) tx[0] = *reinterpret_cast<const v4u*>(A.x0 + (size_t)tid * DM + 8 * cv);
#pragma unroll
        for (int i = 1; i < 4; ++i) tx[i] = *reinterpret_cast<const v4u*>(A.x0 + (size_t)(tid + 512 * i) * DM + 8 * cv);
      }
    }
    if (w >= 2) reinterpret_cast<v4u*>(Xr)[tid] = tx[0];
#pragma unroll
    for (int i = 1; i < 4; ++i) reinterpret_cast<v4u*>(Xr)[tid + 512 * i] = tx[i];
    {
      unsigned spins = 0;
      while (!__all(__hip_atomic_load((gu32*)FS + (w + 8 * ((l >> 3) & 3)) * 32 + (l & 7), RLX_AGENT) != 0u)) {
        if (spin_timeout(++spins, TMO)) break;
        __builtin_amdgcn_s_sleep(4);
      }
      asm volatile("" ::: "memory");
    }
    if (w >= 2) U_ROW(tid);
#pragma unroll
    for (int i = 1; i < 4; ++i) U_ROW(tid + 512 * i);
    SST(6);
    (void)wait_n<8, 1>(FL + FL_KG + cv * 32 + (w >> 2), 1, TMO);
    SST(7);
    {
      const __amdgpu_buffer_rsrc_t rk = MKRS(ws + OFF_KF + (size_t)d * 2 * CKN * 2, 2 * CKN * 2);
      v4u t[8];
#pragma unroll
      for (int i = 0; i < 4; ++i) {
        t[i] = LD128(rk, (64 * i + l) * 16, 0);
        t[4 + i] = LD128(rk, (64 * i + l) * 16, CKN * 2);
      }
#pragma unroll
      for (int i = 0; i < 4; ++i)
        if (64 * i + l < 229) {
          reinterpret_cast<v4u*>(kr)[64 * i + l] = t[i];
          reinterpret_cast<v4u*>(kr + CKN)[64 * i + l] = t[4 + i];
        }
      kr[2080 - 192 - tl] = (u16)khl;
      kr[CKN + 2080 - 192 - tl] = (u16)(khl >> 16);
    }
    __syncthreads();
#define BULK(TA, TB, HOOK)                                                                      \
  {                                                                                            \
    const bf16x8 bm = conv_bfrag(ub - 16, ush);                                                \
    _Pragma("unroll") for (int T = (TA); T < (TB); ++T) acc[T] = MFMA16(KFRAG(128 * T + 32), bm, (f32x4{})); \
    _Pragma("unroll 1") for (int r = 0; r < 4; ++r) {                                          \
      bf16x8 bf[TB];                                                                           \
      _Pragma("unroll") for (int j = 0; j < (TB); ++j) bf[j] = conv_bfrag(ub + 16 * r + 64 * j, ush); \
      _Pragma("unroll") for (int e = 0; e < (TB); ++e) {                                       \
        const bf16x8 a = KFRAG(128 * e - 32 * r);                                              \
        _Pragma("unroll") for (int T = (e > (TA) ? e : (TA)); T < (TB); ++T) acc[T] = MFMA16(a, bf[T - e], acc[T]); \
      }                                                                                        \
      if (r == 0) { HOOK }                                                                     \
    }                                                                                          \
  }
    BULK(1, 4, );
#pragma unroll
    for (int T = 1; T < 4; ++T) {
      const float y0 = swap_add32(acc[T][0], acc[T][1]), y1 = swap_add32(acc[T][2], acc[T][3]);
      TILE_OUT(T);
    }
    __syncthreads();
    if (w < 6) CHUNK_ST(2 + w);
    SST(8);
    BULK(4, 7, if (w < 6) {
      DRAIN();
      if (l < 16) set_flag(FL + FL_FC + (((4 + 2 * w + (l >> 3)) * 8 + (l & 7)) * 8 + (cv >> 3)) * 32 + (cv & 7));
      if (w == 1) (void)(wait_n<SLP_V, 32>(FL + FL_FZ + (blockIdx.x & 7) * 64, 64, TMO));
    });
#pragma unroll
    for (int T = 4; T < 7; ++T) {
      const float y0 = swap_add32(acc[T][0], acc[T][1]), y1 = swap_add32(acc[T][2], acc[T][3]);
      TILE_OUT(T);
    }
    __syncthreads();
    if (w >= 2) {
      CHUNK_ST(6 + w);
#pragma unroll
      for (int i = 0; i < 3; ++i) xf[i] = *reinterpret_cast<const v4u*>(Xr + (64 * (8 + 3 * w + i) + l) * 8);
    }
    SST(10);
    {
      const bf16x8 bm = conv_bfrag(ub - 16, ush);
#pragma unroll
      for (int T = 7; T < 16; ++T) far[T - 7] = MFMA16(KFRAG(128 * T + 32), bm, (f32x4{}));
    }
#define FAR_HALF(r, E0, E1)                                                                     \
  {                                                                                            \
    bf16x8 bf[16];                                                                             \
    _Pragma("unroll") for (int j = (8 - (E1) > 0 ? 8 - (E1) : 0); j < 16 - (E0); ++j) {        \
      bf[j] = conv_bfrag(ub1 + 16 * (r) + 64 * j, ush);                                        \
      if (j == 7 && (E1) - (E0) > 8) __builtin_amdgcn_sched_barrier(0);                        \
    }                                                                                          \
    __builtin_amdgcn_sched_barrier(0);                                                         \
    _Pragma("unroll") for (int e = (E0); e < (E1); ++e) {                                      \
      const bf16x8 a = KFRAG(128 * e - 32 * (r));                                              \
      _Pragma("unroll") for (int T = (e > 7 ? e : 7); T < 16; ++T) far[T - 7] = MFMA16(a, bf[T - e], far[T - 7]); \
    }                                                                                          \
  }
#define FAR_J(r, J0, J1)                                                                       \
  {                                                                                            \
    bf16x8 bf[(J1) - (J0)];                                                                    \
    _Pragma("unroll") for (int j = (J0); j < (J1); ++j) bf[j - (J0)] = conv_bfrag(ub1 + 16 * (r) + 64 * j, ush); \
    _Pragma("unroll") for (int e = (8 - (J1) > 0 ? 8 - (J1) : 0); e < 16 - (J0); ++e) {        \
      const bf16x8 a = KFRAG(128 * e - 32 * (r));                                              \
      _Pragma("unroll") for (int j = (J0); j < (J1); ++j)                                      \
        if (e + j >= 7 && e + j < 16) far[(e + j + 2) % 9] = MFMA16(a, bf[j - (J0)], far[(e + j + 2) % 9]); \
    }                                                                                          \
  }
#define FAR_PIECE(p_)                                                                           \
  {                                                                                            \
    const int p = (p_) - 4;                                                                    \
    if (p < 8) {                                                                               \
      const int r_ = 2 + (p & 1);                                                              \
      if (p < -2) FAR_J(r_, 0, 2) else if (p < 0) FAR_J(r_, 2, 4) else if (p < 2) FAR_J(r_, 4, 6) \
      else if (p < 4) FAR_J(r_, 6, 8) else if (p < 6) FAR_J(r_, 8, 11) else FAR_J(r_, 11, 16)  \
    } else if ((p) == 8) {                                                                     \
                     \
      u16* ucw = Uc + w * CUN + 32 + t0;                                                       \
      _Pragma("unroll") for (int T = 7; T < 16; ++T) {                                         \
        const float y0 = swap_add32(far[T - 7][0], far[T - 7][1]), y1 = swap_add32(far[T - 7][2], far[T - 7][3]); \
        const float u0 = bf2f(ucw[128 * T]), u1 = bf2f(ucw[128 * T + 32]);                     \
        const unsigned yy = pk2(y0, y1), uu = pk2(u0 * dsk, u1 * dsk);                         \
        const unsigned aa = pk2(lo16(yy) + lo16(uu), hi16(yy) + hi16(uu));                     \
        ucw[128 * T] = (u16)aa;                                                                \
        ucw[128 * T + 32] = (u16)(aa >> 16);                                                   \
      }                                                                                        \
    } else if (w >= 2) {                                                                       \
      int l = tid & 63;                                                                        \
      asm volatile("" : "+v"(l), "+v"(xf[0]), "+v"(xf[1]), "+v"(xf[2]));                       \
      if ((p) == 9) {                                                                          \
        _Pragma("unroll") for (int i = 0; i < 3; ++i) {                                        \
          const int ck = 8 + 3 * w + i;                                                        \
          const u16* ur = Uc + 32 + 64 * ck + l;                                               \
          v4u o;                                                                               \
          _Pragma("unroll") for (int e = 0; e < 4; ++e)                                        \
            o[e] = pk2(lo16(xf[i][e]) + bf2f(ur[(2 * e) * CUN]), hi16(xf[i][e]) + bf2f(ur[(2 * e + 1) * CUN])); \
          ST128(o, rz1, (l >> 5) * 32768 + (l & 31) * 16, (2 * ck * 64 + cv) * 512);           \
        }                                                                                      \
      } else {                                                                                 \
        DRAIN();                                                                               \
        if (l < 48) set_flag(FL + FL_FC + (((16 + 6 * w + (l >> 3)) * 8 + (l & 7)) * 8 + (cv >> 3)) * 32 + (cv & 7)); \
      }                                                                                        \
    }                                                                                          \
  }
#pragma unroll 1
    for (int p = 0; p < 2; ++p) {
      FAR_HALF(p, 0, 16);
      if (p == 0 && w >= 2) {
        DRAIN();
        if (l < 16) set_flag(FL + FL_FC + (((12 + 2 * w + (l >> 3)) * 8 + (l & 7)) * 8 + (cv >> 3)) * 32 + (cv & 7));
      }
    }
#undef BULK
    SST(9);
#undef TILE_OUT
#undef CHUNK_ST
#undef U_ROW
  }
  if (EXP_OFF & 4) {
    for (int pp = 0; pp < 15; ++pp) {
      __syncthreads();
      FAR_PIECE(pp);
    }
    return;
  }
  u16* uc = Uc + (8 + w) * CUN;
  const unsigned* ub = reinterpret_cast<const unsigned*>(uc + (c & ~1) + 8 * g + 32);
#pragma unroll
  for (int j = 0; j < 16; ++j) acc[j] = f32x4{};
  const __amdgpu_buffer_rsrc_t rx = MKRS(ws + OFF_X2, SZ_X), rz = MKRS(ws + OFF_X3, SZ_X);
  const __amdgpu_buffer_rsrc_t rf = MKRS(FL + FL_FR, 64 * 32 * 128);
  unsigned st = 0u;
  v2u xx = {0u, 0u};
#define X2_LOAD(ss, far)                                                                                                      \
  {                                                                                                                      \
    xx = LD64(rx, (l >> 1) * (DM * 2) + (l & 1) * 8, (far) ? (int)SZ_X : ((ss) * SEG * DM + 8 * cv) * 2);                 \
    st = __builtin_amdgcn_raw_buffer_load_b32(rf, (l >> 4) * 128 + ((l >> 1) & 7) * 4, ((ss) * 8 + (blockIdx.x & (NR_R - 1))) * 512, AUX_SC1); \
  }
  unsigned tl = now10();
  for (int s = 0; s < NSEG; ++s) {
    if (w == 1) {
      for (unsigned spins = 0; !__all(umin3(xx.x, xx.y, st) != 0u); ++spins) {
        if (spin_timeout(spins, TMO)) break;
        if (GATE_V && spins == 0 && !__any(st != 0u)) gate(tl);
        if (spins && !__all(st != 0u)) __builtin_amdgcn_s_sleep(SLP_V);
        X2_LOAD(s, __builtin_popcountll(__ballot(st != 0u)) < CVTHR);
      }
      tl = now10() + GATE_V;
      TS(0, s, 64);
      const float mb = lo16(st), ri = 1.0f / hi16(st);
      const unsigned t0 = pk2(lo16(xx.x) - mb, hi16(xx.x) - mb), t1 = pk2(lo16(xx.y) - mb, hi16(xx.y) - mb);
      *reinterpret_cast<v2u*>(Su + l * 4) = v2u{pk2(lo16(t0) * ri, hi16(t0) * ri), pk2(lo16(t1) * ri, hi16(t1) * ri)};
      *reinterpret_cast<v2u*>(Sx + l * 4) = xx;
    }
    const int q = s & 3;
    const u16* kq = ka + 32 * q;
    const bf16x8 a0 = *reinterpret_cast<const bf16x8*>(kq - 32), a1 = *reinterpret_cast<const bf16x8*>(kq);
    __syncthreads();
    TS(1, s, 0);
    if (s == 0)
      for (int x = l; x < CUN / 2; x += 64) reinterpret_cast<unsigned*>(uc)[x] = 0u;
    if (l < 32) uc[32 + 32 * s + l] = Su[l * 8 + w];
    asm volatile("s_waitcnt lgkmcnt(0)" ::: "memory");
    const bf16x8 bp = conv_bfrag(ub + 16 * (s - 1), ush);
    acc[0] = MFMA16(a0, bp, (q == 0 ? acc[1] : acc[0]));
    const f32x4 tmp = MFMA16(a1, conv_bfrag(ub + 16 * s, ush), acc[0]);
    float y0 = (q & 1) ? tmp[2] : tmp[0], y1 = (q & 1) ? tmp[3] : tmp[1];
    y0 = xsum32(y0);
    y1 = xsum32(y1);
    if (g == (q >> 1)) {
      const float u0 = bf2f(Su[c * 8 + w]), u1 = bf2f(Su[(16 + c) * 8 + w]);
      const float a0 = bfr(bfr(y0) + bfr(u0 * dsk)), a1 = bfr(bfr(y1) + bfr(u1 * dsk));
      To[c * 8 + w] = f2bf(bf2f(Sx[c * 8 + w]) + a0);
      To[(16 + c) * 8 + w] = f2bf(bf2f(Sx[(16 + c) * 8 + w]) + a1);
    }
    __syncthreads();
    if (w == 0 && l < 32) {
      const v4u o = *reinterpret_cast<const v4u*>(To + l * 8);
      ST128(o, rz, l * 16, (s * 64 + cv) * 512);
    }
    if (w == 1) X2_LOAD(s + 1, CVTHR > 0);
    const int left = 15 - (s >> 2);
#define PUSH(J0, SH)                                                                                              \
  if (left >= (J0)) {                                                                                             \
    _Pragma("unroll") for (int j = (J0); j < (J0) + 4 && j < 16; ++j)                                             \
      acc[j] = MFMA16(*reinterpret_cast<const bf16x8*>(kq - 32 - 128 * j), bp, (j + (SH) < 16 ? acc[(j + (SH)) & 15] : f32x4{})); \
  }
    TS(2, s, 0);
    if (w == 0) {
      DRAIN();
      if (l < NR_C) set_flag(FL + FL_FC + (((64 + s) * 8 + l) * 8 + (cv >> 3)) * 32 + (cv & 7));
    }
    TS(3, s, 0);
    if (q == 0) { PUSH(1, 1) PUSH(5, 1) PUSH(9, 1) PUSH(13, 1) }
    else { PUSH(1, 0) PUSH(5, 0) PUSH(9, 0) PUSH(13, 0) }
#undef PUSH
    MSTAMP();
    if (s >= CVPRE && s < 15 + CVPRE) FAR_PIECE(s - CVPRE);
  }
#undef KFRAG
#undef X2_LOAD
#undef FAR_PIECE
#undef FAR_HALF
#undef FAR_J
}

__device__ __forceinline__ v4u ln_row_packed(const v4u xr) {
  float v[8];
#pragma unroll
  for (int e = 0; e < 4; ++e) { v[2 * e] = lo16(xr[e]); v[2 * e + 1] = hi16(xr[e]); }
  float s = ((v[0] + v[1]) + (v[2] + v[3])) + ((v[4] + v[5]) + (v[6] + v[7]));
  s = wave_sum(s);
  const float mean = s * (1.0f / DM);
  float q = 0.f;
#pragma unroll
  for (int j = 0; j < 8; ++j) { float t = v[j] - mean; q += t * t; }
  q = wave_sum(q);
  const float var = q * (1.0f / DM);
  const float mb = bfr(mean);
  const float sd = bfr(sqrtf(bfr(bfr(var) + 1e-5f)));
  const float ri = 1.0f / sd;
  v4u o;
#pragma unroll
  for (int e = 0; e < 4; ++e) {
    const unsigned t = pk2(v[2 * e] - mb, v[2 * e + 1] - mb);
    const float x0 = lo16(t), x1 = hi16(t);
    float q0 = x0 * ri, q1 = x1 * ri;
    q0 = __builtin_fmaf(__builtin_fmaf(-q0, sd, x0), ri, q0);
    q1 = __builtin_fmaf(__builtin_fmaf(-q1, sd, x1), ri, q1);
    o[e] = pk2(q0, q1);
  }
  return o;
}

__device__ __forceinline__ float bfr1(float x) { return lo16(pk2(x, x)); }
__device__ __forceinline__ void ln4_packed(v4u (&x)[4]) {
  f32x2_t v[16];
#pragma unroll
  for (int e = 0; e < 16; ++e) v[e] = f32x2_t{lo16(x[e >> 2][e & 3]), hi16(x[e >> 2][e & 3])};
  const f32x2_t t = (((v[0] + v[1]) + (v[2] + v[3])) + ((v[4] + v[5]) + (v[6] + v[7]))) +
                    (((v[8] + v[9]) + (v[10] + v[11])) + ((v[12] + v[13]) + (v[14] + v[15])));
  const float mean = red_mod4(t.x + t.y) * (1.0f / DM);
  const float mb = bfr1(mean);
  f32x2_t q0 = {0.f, 0.f}, q1 = {0.f, 0.f};
#pragma unroll
  for (int e = 0; e < 16; e += 2) {
    v[e] -= mb;
    v[e + 1] -= mb;
    q0 += v[e] * v[e];
    q1 += v[e + 1] * v[e + 1];
  }
  q0 += q1;
  const float dm = mean - mb;
  const float var = fmaxf(red_mod4(q0.x + q0.y) * (1.0f / DM) - dm * dm, 0.f);
  const float sd = bfr1(sqrtf(bfr1(bfr1(var) + 1e-5f)));
  const float ri = 1.0f / sd;
#pragma unroll
  for (int e = 0; e < 16; ++e) {
    const unsigned t2 = pk2(v[e].x, v[e].y);
    const f32x2_t q = f32x2_t{lo16(t2), hi16(t2)} * ri;
    x[e >> 2][e & 3] = pk2(q.x, q.y);
  }
}

extern "C" __device__ float mul1(float, float) __asm("llvm.amdgcn.fmul.legacy");
template <bool G0>
__device__ __forceinline__ void role_qkv_t(const MegaArgs& A, char* smem, const int L, const int h) {
  const int w = __builtin_amdgcn_readfirstlane(threadIdx.x >> 6);
  char* ws = A.ws;
  unsigned* FL = (unsigned*)(ws + OFF_FL);
  unsigned* TMO = FL + FL_TMO;
#define LANE_IDS()                         \
  const int l = lane_id();                 \
  const int c = l & 15, g = l >> 4;        \
  (void)c; (void)g;
  unsigned* FC = FL + FL_FC + (L * 64 * 8 + (blockIdx.x & (NR_C - 1))) * 8 * 32;
  const __amdgpu_buffer_rsrc_t rsQ = MKRS(ws + OFF_QK + L * SZ_QK, SZ_QK);
  const __amdgpu_buffer_rsrc_t rsV = MKRS(ws + OFF_VF + L * SZ_O, SZ_O);
  const __amdgpu_buffer_rsrc_t rsM = MKRS(ws + (G0 ? OFF_MEMK : OFF_MEMV) + L * SZ_MEM, SZ_MEM);
  bf16x8 wq[16], wk[16], wv[16];
  {
    LANE_IDS();
    const unsigned sb = lds_base(smem);
    const int q = (l >> 2) & 3, p = l & 3, f = q | ((g & 1) << 2);
    const unsigned ra = sb + (8 * g + q) * 256 + (w ^ f) * 32 + p * 8;
#define W_STAGE(W, hf, VM, NEXT)                                               \
  {                                                                           \
    WAIT_VM(VM);                                                              \
    RAW_BAR();                                                                \
    _Pragma("unroll") for (int t = 0; t < 8; ++t) {                           \
      const unsigned a_ = ra + (hf) * 8192 + t * 16384;                       \
      const v2u r0 = trd(a_), r1 = trd(a_ + 1024);                            \
      W[8 * (hf) + t] = BF8((v4u{r0.x, r0.y, r1.x, r1.y}));                   \
    }                                                                         \
    _Pragma("unroll") for (int t = 0; t < 8; ++t) asm volatile("" : "+v"(W[8 * (hf) + t])); \
    RAW_BAR();                                                                \
    NEXT;                                                                     \
  }
    wqkv_issue<false>(A.Wq, 0, h, w, l, sb);
    wqkv_issue<false>(A.Wq, 1, h, w, l, sb);
    W_STAGE(wq, 0, 8, wqkv_issue<false>(A.Wk, 0, h, w, l, sb));
    SST(4);
    W_STAGE(wq, 1, 8, wqkv_issue<false>(A.Wk, 1, h, w, l, sb));
    SST(5);
    W_STAGE(wk, 0, 8, wqkv_issue<false>(A.Wv, 0, h, w, l, sb));
    W_STAGE(wk, 1, 8, wqkv_issue<false>(A.Wv, 1, h, w, l, sb));
    SST(6);
    W_STAGE(wv, 0, 8, (void)0);
    W_STAGE(wv, 1, 0, (void)0);
#undef W_STAGE
  }
  WSTAMP2();
  constexpr int Q_X = 98304;
  const unsigned sb = lds_base(smem);
  v2u eq, ek, ev;
  {
    LANE_IDS();
    const v4u one = {0x3f803f80u, 0x3f803f80u, 0x3f803f80u, 0x3f803f80u};
    f32x4 t[3] = {};
#pragma unroll
    for (int ks = 0; ks < 16; ++ks) {
      t[0] = MFMA16(BF8(one), wq[ks], t[0]);
      t[1] = MFMA16(BF8(one), wk[ks], t[1]);
      t[2] = MFMA16(BF8(one), wv[ks], t[2]);
    }
    v2u e[3];
#pragma unroll
    for (int i = 0; i < 3; ++i) {
      const float x = g ? 0.f : t[i][0], x1 = x - bfr1(x);
      e[i] = v2u{pk2(x, x1), pk2(x1 - bfr1(x1), 0.f)};
    }
    eq = e[0]; ek = e[1]; ev = e[2];
    if (l < 32) *reinterpret_cast<v4u*>(smem + Q_X + w * 2048 + (l >> 4) * 16384 + c * 16) = one;
  }
  constexpr int Q_CNT = Q_X + 11 * 2048 + 256;
  lu32* cnt = (lu32*)(unsigned long)(sb + Q_CNT);
  if (threadIdx.x < 16) cnt[threadIdx.x] = 0u;
#define C_ADD(i) asm volatile("s_mov_b64 exec, 1\n\tds_add_u32 %0, %1\n\ts_mov_b64 exec, -1" ::"v"(sb + Q_CNT + 4 * (i)), "v"(1u) : "memory")
#define C_RD(i) (*(volatile lu32*)(cnt + (i)))
#define C_GET(i) ((unsigned)__builtin_amdgcn_readfirstlane(C_RD(i)))
#define C_WAIT(i, n)                                                                                                    \
  {                                                                                                                     \
    for (unsigned spins = 0; C_GET(i) < (n) && spins < 64u * SPIN_CAP; ++spins) __builtin_amdgcn_s_sleep(1);             \
    asm volatile("" ::: "memory");                                                                                      \
  }
  const char* Xb = ws + (L ? OFF_X3 : OFF_XA) + w * 8192;
  const v4u rsF = mk_srd(FC + w * 64, (64 * 64 - (blockIdx.x & (NR_C - 1)) * 8 - 2 * w) * 128);
  unsigned l16, vo, fo;
  {
    LANE_IDS();
    l16 = l * 16;
    vo = (c + 16 * (2 * (w & 1) + (g >> 1))) * 16 + 4096 * (g & 1);
    fo = g * 512 + c * 16;
  }
#define DMA4(x) "global_load_lds_dwordx4 %1, " x " sc1\n\tglobal_load_lds_dwordx4 %1, " x " offset:1024 sc1\n\t"       \
                "global_load_lds_dwordx4 %1, " x " offset:2048 sc1\n\tglobal_load_lds_dwordx4 %1, " x " offset:3072 sc1\n\t"
#define X_DMA(ss, bb)                                                                                                   \
  {                                                                                                                     \
    unsigned keep_;                                                                                                     \
    const char* x_ = Xb + (size_t)(ss) * 32768;                                                                         \
    const unsigned d_ = sb + (bb) * 32768 + w * 8192;                                                                   \
    asm volatile("s_nop 4\n\ts_mov_b32 %0, m0\n\ts_mov_b32 m0, %4\n\ts_nop 0\n\t" DMA4("%2")                           \
                 "s_mov_b32 m0, %5\n\ts_nop 0\n\t" DMA4("%3") "s_mov_b32 m0, %0"                                         \
                 : "=&s"(keep_)                                                                                         \
                 : "v"(l16), "s"(x_), "s"(x_ + 4096), "s"(d_), "s"(d_ + 4096u)                                           \
                 : "memory");                                                                                           \
    TS(0, ss, 0);                                                                                                       \
  }
#define X_DMA1(on, x, d, off)                                                                                           \
  {                                                                                                                     \
    unsigned keep_;                                                                                                     \
    asm volatile("s_cmp_eq_u32 %4, 0\n\ts_cbranch_scc1 1f\n\ts_mov_b32 %0, m0\n\ts_mov_b32 m0, %3\n\ts_nop 0\n\t"       \
                 "global_load_lds_dwordx4 %1, %2 offset:%5 sc1\n\ts_mov_b32 m0, %0\n1:"                                  \
                 : "=&s"(keep_)                                                                                         \
                 : "v"(l16), "s"(x), "s"(d), "s"(on), "n"(off)                                                          \
                 : "memory", "scc");                                                                                    \
  }
#define F_LOAD(r, ss)                                                                                                   \
  asm volatile("s_nop 4\n\tbuffer_load_dword %0, %1, %2, %3 offen sc1"                                                  \
               : "=v"(r)                                                                                                \
               : "v"(((l16 >> 2) & 28) + (l16 & 128)), "s"(rsF), "s"((ss) * 8192)                                       \
               : "memory")
  unsigned* FQ = FL + FL_FQ + (L * 64 * 32 + h) * 32;
#define Q_FLAG(ss)                                                                                                      \
  {                                                                                                                     \
    unsigned old_;                                                                                                      \
    asm volatile("s_mov_b64 exec, 1\n\tds_add_rtn_u32 %0, %1, %2\n\ts_mov_b64 exec, -1\n\ts_waitcnt lgkmcnt(0)"           \
                 : "=&v"(old_) : "v"(sb + Q_CNT + ((ss) & 1) * 4), "v"(1u) : "memory");                                 \
    if ((__builtin_amdgcn_readfirstlane(old_) & 7u) == 7u) {                                                            \
      asm volatile("s_mov_b64 exec, 1\n\tglobal_store_dword %0, %1, %2 sc1\n\ts_mov_b64 exec, -1"                        \
                   ::"v"(0), "v"(1u), "s"(FQ + (ss) * 1024) : "memory");                                                \
      TS(3, ss, threadIdx.x & ~63u);                                                                                    \
    }                                                                                                                   \
    pend = false;                                                                                                       \
  }
  __builtin_amdgcn_s_waitcnt(0x0F70);
  RAW_BAR();
  bool have = false, pend = false;
  unsigned tl = now10();
  int b = 0;
  unsigned k4 = 4u;
  FSD;
  for (int s = 0; s < NSEG; ++s) {
    FS(0);
    const int b1 = b == 2 ? 0 : b + 1;
    const unsigned u1 = 2u * (b1 ? k4 - 4u : k4);
    unsigned nf;
    if (G0) {
      if (!have) {
        for (unsigned spins = 0;; ++spins) {
          unsigned n;
          F_LOAD(n, s);
          WAIT_VM(0);
          if (pend) Q_FLAG(s - 1);
          if (all_set(n) || spin_timeout(spins, TMO)) break;
          if (GATE_Q && spins == 0) gate(tl);
          else if (L) __builtin_amdgcn_s_sleep(SLP_Q);
          else __builtin_amdgcn_s_sleep(SLP_Q1);
        }
        tl = now10() + GATE_Q;
        C_WAIT(8 + b, 2u * k4 - 8u);
        X_DMA(s, b);
        WAIT_VM(0);
        C_ADD(4 + b);
      }
      const unsigned c0 = C_RD(4 + b);
      F_LOAD(nf, s + 1);
      if ((unsigned)__builtin_amdgcn_readfirstlane(c0) < k4) C_WAIT(4 + b, k4);
    } else {
      if (C_GET(4 + b) < k4) {
        if (pend) {
          WAIT_VM(0);
          Q_FLAG(s - 1);
        }
        C_WAIT(4 + b, k4);
      }
      FS(1);
      const char* sp = smem + ((l16 & 128) ? Q_X + ((w + 1) & 1) * 128 : b * 32768 + fo + (w & 3) * 128);
      f32x4 gm = {};
      __builtin_amdgcn_s_setprio(3);
      {
        bf16x8 a[10];
#pragma unroll
        for (int ks = 0; ks < 10; ++ks) a[ks] = *reinterpret_cast<const bf16x8*>(sp + 2048 * ks);
        __builtin_amdgcn_sched_barrier(0);
#pragma unroll
        for (int ks = 0; ks < 16; ++ks) {
          gm = MFMA16(a[ks % 10], a[ks % 10], gm);
          if (ks < 6) a[ks] = *reinterpret_cast<const bf16x8*>(sp + 2048 * (10 + ks));
          __builtin_amdgcn_sched_barrier(0);
        }
      }
      float q2 = (l16 & 16) ? gm[1] : gm[0], q3 = (l16 & 16) ? gm[3] : gm[2];
      asm volatile("" : "+v"(q2), "+v"(q3));
      q2 = (l16 & 32) ? q3 : q2;
      auto sw = __builtin_amdgcn_permlane32_swap(__float_as_uint(q2), __float_as_uint(q2), false, false);
      const float mean = __uint_as_float(sw[1]) * (-1.0f / DM);
      const float var = fmaxf(__builtin_fmaf(-mean, mean, q2 * (1.0f / DM)), 0.f);
      const float sd = bfr1(__builtin_amdgcn_sqrtf(bfr1(bfr1(var) + 1e-5f)));
      asm volatile("s_mov_b64 exec, 0xf0000f\n\tds_write2_b32 %0, %1, %2 offset1:32\n\ts_mov_b64 exec, -1"
                   ::"v"(sb + Q_X + (8 + b) * 2048 + 256 + (w & 3) * 32 + ((l16 >> 2) & 28)), "v"(pk2(mean, mean)),
                   "v"(__builtin_amdgcn_rcpf(sd)) : "memory");
      C_ADD(12 + b);
      FS(2);
      if (pend) {
        WAIT_VM(0);
        Q_FLAG(s - 1);
      }
    }
    TS(G0 ? 1 : 5, s, G0 ? 0 : 256);
    FS(3);
    const char* ap = smem + b * 32768 + fo;
#define AFRAG(i) (*reinterpret_cast<const bf16x8*>(ap + ((i) >> 4) * 256 + 2048 * ((i) & 15)))
    bf16x8 abuf[QPF + 1];
#pragma unroll
    for (int i = 0; i < QPF; ++i) abuf[i] = AFRAG(i);
    f32x4 aq[2] = {}, ak[2] = {}, av[2] = {};
    unsigned cx = 0u;
    int on = 0;
    const char* xn = Xb + (size_t)(s + 1) * 32768;
    const unsigned dn = sb + b1 * 32768 + w * 8192;
    __builtin_amdgcn_s_setprio(G0 ? 1 : 0);
#pragma unroll
    for (int rt = 0; rt < 2; ++rt) {
#pragma unroll
      for (int ks = 0; ks < 16; ++ks) {
        const int i = 16 * rt + ks;
        if (i + QPF < 32) abuf[(i + QPF) % (QPF + 1)] = AFRAG(i + QPF);
        if (G0 && i == 11) cx = C_RD(8 + b1);
        if (i == 28) cx = C_RD(12 + b);
        const bf16x8 a = abuf[i % (QPF + 1)];
        aq[rt] = MFMA16(wq[ks], a, aq[rt]);
        ak[rt] = MFMA16(wk[ks], a, ak[rt]);
        av[rt] = MFMA16(a, wv[ks], av[rt]);
        if (i == QUP) { __builtin_amdgcn_s_setprio(G0 ? 3 : 2); FS(4); }
        __builtin_amdgcn_sched_barrier(0);
        if (G0 && rt == 1 && ks < 8) {
          X_DMA1(on, xn + (ks >> 2) * 4096, dn + (ks >> 2) * 4096, (ks & 3) * 1024);
          __builtin_amdgcn_sched_barrier(0);
        }
      }
      asm volatile("" : "+v"(aq[rt]), "+v"(ak[rt]), "+v"(av[rt]));
      if (rt == 0) FS(5); else FS(7);
      if (G0 && rt == 0) {
        WAIT_VM(0);
        if (pend) Q_FLAG(s - 1);
        have = all_set(nf) && (unsigned)__builtin_amdgcn_readfirstlane(cx) >= u1;
        F_LOAD(nf, have ? NSEG : s + 1);
        if (have) TS(0, s + 1, 0);
        on = __builtin_amdgcn_readfirstlane((int)have);
        FS(6);
      }
    }
    if ((unsigned)__builtin_amdgcn_readfirstlane(cx) < k4) C_WAIT(12 + b, k4);
    FS(8);
    v4u vfrag, qf4, kf4;
    {
      const char* stp = smem + Q_X + (8 + b) * 2048 + 256;
      unsigned nm[2];
      float ri[2];
      f32x4 rv[2];
#pragma unroll
      for (int rt = 0; rt < 2; ++rt) {
        nm[rt] = *reinterpret_cast<const unsigned*>(stp + 64 * rt + ((l16 >> 2) & 60));
        ri[rt] = *reinterpret_cast<const float*>(stp + 128 + 64 * rt + ((l16 >> 2) & 60));
        rv[rt] = *reinterpret_cast<const f32x4*>(stp + 128 + 64 * rt + ((l16 >> 4) & 48));
      }
#pragma unroll
      for (int rt = 0; rt < 2; ++rt) {
        const v2u xm = {nm[rt], nm[rt]};
        aq[rt] = MFMA16K(eq, xm, aq[rt]);
        ak[rt] = MFMA16K(ek, xm, ak[rt]);
        av[rt] = MFMA16K(xm, ev, av[rt]);
      }
      __builtin_amdgcn_s_setprio(QEP);
#pragma unroll
      for (int rt = 0; rt < 2; ++rt) {
        f32x4 tq, tk, tv;
#pragma unroll
        for (int e = 0; e < 4; ++e) {
          tq[e] = mul1(aq[rt][e], ri[rt]);
          tk[e] = mul1(ak[rt][e], ri[rt]);
          tv[e] = mul1(av[rt][e], rv[rt][e]);
        }
        qf4[2 * rt] = pk2(tq[0], tq[1]); qf4[2 * rt + 1] = pk2(tq[2], tq[3]);
        kf4[2 * rt] = pk2(tk[0], tk[1]); kf4[2 * rt + 1] = pk2(tk[2], tk[3]);
        vfrag[2 * rt] = pk2(tv[0], tv[1]); vfrag[2 * rt + 1] = pk2(tv[2], tv[3]);
      }
    }
    {
      auto q0 = __builtin_amdgcn_permlane16_swap(qf4.x, qf4.z, false, false);
      auto q1 = __builtin_amdgcn_permlane16_swap(qf4.y, qf4.w, false, false);
      auto k0 = __builtin_amdgcn_permlane16_swap(kf4.x, kf4.z, false, false);
      auto k1 = __builtin_amdgcn_permlane16_swap(kf4.y, kf4.w, false, false);
      v4u qo = {q0[0], q1[0], q0[1], q1[1]}, ko = {k0[0], k1[0], k0[1], k1[1]};
      const int soff = ((s * NH + h) * 16 + (w >> 1)) * 1024;
      asm volatile("" : "+v"(qo), "+v"(ko), "+v"(vfrag));
      FS(9);
      C_ADD(8 + b);
      if (G0 && have) {
        WAIT_VM(0);
        C_ADD(4 + b1);
      }
      TS(G0 ? 2 : 6, s, G0 ? 0 : 256);
      FS(10);
      ST128(ko, rsQ, vo, soff + 8192);
      ST128(qo, rsQ, vo, soff);
      ST128(vfrag, rsV, l16, ((s * NH + h) * 512 + w * 64) * 16);
      ST64((v2u{0u, 0u}), rsM, l16 >> 1, (s * NH + h) * 2048 + (w & 3) * 512);
      pend = true;
      FS(11);
    }
    if (G0) {
      WAIT_VM(4);
      if (all_set(nf) && C_GET(8 + b1) >= u1) {
        X_DMA(s + 1, b1);
        have = true;
        WAIT_VM(8);
        Q_FLAG(s);
        WAIT_VM(0);
        C_ADD(4 + b1);
      }
    }
    MSTAMP();
    FD(s);
    b = b1;
    if (b == 0) k4 += 4u;
  }
  DRAIN();
  if (pend) Q_FLAG(NSEG - 1);
#undef LANE_IDS
#undef DMA4
#undef X_DMA
#undef X_DMA1
#undef F_LOAD
#undef Q_FLAG
#undef AFRAG
#undef C_ADD
#undef C_RD
#undef C_GET
#undef C_WAIT
}
__device__ __forceinline__ void role_qkv(const MegaArgs& A, char* smem, const int L, const int h) {
  if (__builtin_amdgcn_readfirstlane(threadIdx.x >> 6) < 4) role_qkv_t<true>(A, smem, L, h);
  else role_qkv_t<false>(A, smem, L, h);
}

constexpr int MEGA_WGS = 256;
constexpr size_t MEGA_LDS = (size_t)(32 * 4 * 64 * 8 + 8 * SMS + 15 * QS + 128 + 16 * 64 * 8 + 8 * SOS + 512) * 2;
static_assert(MEGA_LDS <= 163840, "lds");
static_assert(MEGA_LDS >= (size_t)AW_CNT + 64, "lds");
static_assert(MEGA_LDS >= (size_t)(8 * 2 * CKN + 8 * 2 * CUN + 4 * 32 * 8) * 2, "lds");
static_assert(MEGA_LDS >= (size_t)2 * 32 * SMS * 2 && MEGA_LDS >= 131072, "lds");
static_assert(MEGA_LDS >= (size_t)(40 * NST + 32 * 400 + 4 * 392) * 4, "lds");
__global__ __launch_bounds__(512, 2) void mega_kernel(const u16* mem0, const u16* Dskip, char* ws, u16* out, const u16* x0,
                                                      const u16* logAre, const u16* Aim, const u16* Cre, const u16* Cim,
                                                      const u16* logdt, const u16* Wq, const u16* Wk, const u16* Wv,
                                                      const u16* Wo) {
  extern __shared__ __attribute__((aligned(16))) char smem[];
  const int b = __builtin_amdgcn_readfirstlane(role_no());
  const MegaArgs A{mem0, Dskip, ws, out, x0, logAre, Aim, Cre, Cim, logdt, Wq, Wk, Wv, Wo, b};
  {
    const int hi = (b >= 32 && b < 96) ? b - 32 : (b >= 200 && b < 248) ? b - 136 : (b >= 96 && b < 104) ? b + 16 : (b >= 248 ? b - 128 : -1);
    if (hi >= 0) start_kgen(A, smem, hi, b - 248);
  }
  if ((EXP_OFF & 1) && b >= 64 && b < 80) return;
  if ((EXP_OFF & 8) && b >= 200 && b < 232) return;
  if ((EXP_OFF & 16) && b >= 32 && b < 64) return;
  if ((EXP_OFF & 32) && b >= 80 && b < 96) return;
  if (b < 64) role_chain(A, smem, b >> 5, b & 31, 0);
  else if (b < 104 || (b >= 232 && b < 240)) role_aw(A, smem, b < 80 ? 0 : b < 96 ? 2 : 1, b & 15);
  else if (b < 168) role_conv(A, smem, b - 104);
  else if (b < 232) role_qkv(A, smem, (b - 168) >> 5, (b - 168) & 31);
  else role_red(A, smem, b - 240);
}

extern "C" void kernel_launch(void* const* d_in, const int* in_sizes, int n_in, void* d_out, int out_size, void* d_ws,
                              size_t ws_size, hipStream_t stream) {
  MegaArgs A;
  A.x0 = (const u16*)d_in[0];
  A.logAre = (const u16*)d_in[1];
  A.Aim = (const u16*)d_in[2];
  A.Cre = (const u16*)d_in[3];
  A.Cim = (const u16*)d_in[4];
  A.logdt = (const u16*)d_in[5];
  A.Dskip = (const u16*)d_in[6];
  A.mem0 = (const u16*)d_in[8];
  A.Wq = (const u16*)d_in[9];
  A.Wk = (const u16*)d_in[10];
  A.Wv = (const u16*)d_in[11];
  A.Wo = (const u16*)d_in[12];
  A.ws = (char*)d_ws;
  A.out = (u16*)d_out;

  (void)hipFuncSetAttribute((const void*)mega_kernel, hipFuncAttributeMaxDynamicSharedMemorySize, (int)MEGA_LDS);
  zero_kernel<<<ZBLK, 256, 0, stream>>>((uint4*)(A.ws + OFF_Z));
  mega_kernel<<<MEGA_WGS, 512, MEGA_LDS, stream>>>(A.mem0, A.Dskip, A.ws, A.out, A.x0, A.logAre, A.Aim, A.Cre, A.Cim, A.logdt,
                                                   A.Wq, A.Wk, A.Wv, A.Wo);
}
```

```cpp
#include <hip/hip_runtime.h>
#include <hip/hip_bf16.h>
#include <stdint.h>

constexpr int L_SEQ = 2048;
constexpr int DM    = 512;
constexpr int NST   = 128;
constexpr int NH    = 32;
constexpr int DH    = 128;
constexpr int HD    = NH * DH;
constexpr int MM    = 8;
constexpr int SEG   = 32;
constexpr int NSEG  = L_SEQ / SEG;
constexpr int LK    = MM + SEG;

typedef unsigned short u16;
using bf16x8 = __attribute__((ext_vector_type(8))) short;
using f32x4  = __attribute__((ext_vector_type(4))) float;
using f32x16 = __attribute__((ext_vector_type(16))) float;

static __device__ __forceinline__ u16 f2bf(float f) {
  unsigned u = __float_as_uint(f);
  u += 0x7fffu + ((u >> 16) & 1u);
  return (u16)(u >> 16);
}
static __device__ __forceinline__ float bf2f(u16 h) { return __uint_as_float(((unsigned)h) << 16); }
static __device__ __forceinline__ float bfr(float f) { return bf2f(f2bf(f)); }

constexpr size_t SZ_X      = (size_t)L_SEQ * DM * 2;
constexpr size_t OFF_KF    = 0;
constexpr size_t OFF_UT    = OFF_KF + (size_t)DM * L_SEQ * 4;
constexpr size_t OFF_YT    = OFF_UT + SZ_X;
constexpr size_t OFF_XA    = OFF_YT + SZ_X;
constexpr size_t OFF_H     = OFF_XA + SZ_X;
constexpr size_t OFF_X2    = OFF_H + SZ_X;
constexpr size_t OFF_U2    = OFF_X2 + SZ_X;
constexpr size_t OFF_X3    = OFF_U2 + SZ_X;
constexpr size_t SZ_QK     = (size_t)L_SEQ * 2 * HD * 2;
constexpr size_t OFF_QK    = OFF_X3 + SZ_X;
constexpr size_t SZ_O      = (size_t)L_SEQ * HD * 2;
constexpr size_t OFF_VF    = OFF_QK + 2 * SZ_QK;
constexpr size_t SZ_PW     = (size_t)2 * NSEG * 16 * SEG * DM * 2;
constexpr size_t OFF_PW    = OFF_VF + 2 * SZ_O;
constexpr size_t SZ_MEM    = (size_t)NSEG * MM * HD * 2;
constexpr size_t OFF_MEMK  = OFF_PW + SZ_PW;
constexpr size_t OFF_MEMV  = OFF_MEMK + 2 * SZ_MEM;
constexpr size_t OFF_GT    = OFF_MEMV + 2 * SZ_MEM;
constexpr size_t SZ_ACC    = (size_t)2 * 32768;
constexpr size_t OFF_Z     = OFF_GT + (size_t)64 * DM * 4;
constexpr size_t OFF_ACC   = OFF_Z;
constexpr size_t OFF_FL    = OFF_ACC + 2 * SZ_ACC;
constexpr int FL_FW  = 0;
constexpr int FL_FR  = FL_FW + 2 * 64 * 16 * 32;
constexpr int FL_FC  = FL_FR + 64 * 16 * 32;
constexpr int FL_FQ  = FL_FC + 2 * 64 * 32 * 32;
constexpr int FL_FS  = FL_FQ + 2 * 64 * 32 * 32;
constexpr int FL_FZ  = FL_FS + 4 * 32 * 32;
constexpr int FL_KG  = FL_FZ + 8 * 32;
constexpr int FL_G   = FL_KG + 64 * 32;
constexpr int FL_TK  = FL_G + 2 * 4 * 32;
constexpr int FL_TMO = FL_TK + 2 * 256;
constexpr int FL_END = FL_TMO + 64;
constexpr size_t Z_BYTES   = OFF_FL + (size_t)FL_END * 4 - OFF_Z;
constexpr size_t OFF_TAG   = OFF_Z + Z_BYTES;
constexpr unsigned long long TAG_STEP = 0x9e3779b97f4a7c15ull;
constexpr size_t OFF_G0    = OFF_TAG + 8 * 1024 + 256;
constexpr size_t WS_TOTAL  = OFF_G0 + 8192;
static_assert(Z_BYTES % 256 == 0 && OFF_Z % 256 == 0 && Z_BYTES <= 2 * 128 * 8192, "zero region");
#define REP ((blockIdx.x >> 1) & 3)
static_assert(WS_TOTAL + (4 << 20) <= (size_t)256 * 1024 * 1024, "workspace too large");

constexpr int CKN = 2208;
static_assert((size_t)DM * 2 * CKN * 2 <= (size_t)DM * L_SEQ * 4 + SZ_X, "K buffer");
__device__ __forceinline__ float pos_of_index(int p) {
  if (p < 256) return (float)p;
  if (p < 384) return (float)(256 + 2 * (p - 256));
  if (p < 512) return (float)(512 + 4 * (p - 384));
  if (p < 640) return (float)(1024 + 8 * (p - 512));
  return 2048.0f;
}
__device__ __forceinline__ int index_of_l(int l) {
  float pf = bfr((float)l);
  int p = (int)pf;
  if (p < 256) return p;
  if (p < 512) return 256 + ((p - 256) >> 1);
  if (p < 1024) return 384 + ((p - 512) >> 2);
  if (p < 2048) return 512 + ((p - 1024) >> 3);
  return 640;
}

template <int CTRL> __device__ __forceinline__ float dppr(float x) {
  return __builtin_bit_cast(float, __builtin_amdgcn_update_dpp(0, __builtin_bit_cast(int, x), CTRL, 0xf, 0xf, false));
}
__device__ __forceinline__ float wave_sum(float v) {
  v += dppr<0x121>(v);
  v += dppr<0x122>(v);
  v += dppr<0x124>(v);
  v += dppr<0x128>(v);
  {
    auto r = __builtin_amdgcn_permlane16_swap(__float_as_uint(v), __float_as_uint(v), false, false);
    v = __uint_as_float(r[0]) + __uint_as_float(r[1]);
  }
  {
    auto r = __builtin_amdgcn_permlane32_swap(__float_as_uint(v), __float_as_uint(v), false, false);
    v = __uint_as_float(r[0]) + __uint_as_float(r[1]);
  }
  return v;
}
__device__ __forceinline__ void ln_row8(float (&v)[8]) {
  float s = 0.f;
#pragma unroll
  for (int j = 0; j < 8; ++j) s += v[j];
  s = wave_sum(s);
  const float mean = s * (1.0f / DM);
  float q = 0.f;
#pragma unroll
  for (int j = 0; j < 8; ++j) { float t = v[j] - mean; q += t * t; }
  q = wave_sum(q);
  const float var = q * (1.0f / DM);
  const float mb = bfr(mean);
  const float sd = bfr(sqrtf(bfr(bfr(var) + 1e-5f)));
#pragma unroll
  for (int j = 0; j < 8; ++j) v[j] = bfr(bfr(v[j] - mb) / sd);
}

#define RLX_AGENT __ATOMIC_RELAXED, __HIP_MEMORY_SCOPE_AGENT
typedef __attribute__((address_space(1))) unsigned gu32;
typedef unsigned v4u __attribute__((ext_vector_type(4)));
constexpr int AUX_SC1 = 16;
constexpr unsigned SPIN_CAP = 1u << 18;

__device__ __forceinline__ bool wait_flags(const unsigned* flags, int lane, int nfl, unsigned want, unsigned* tmo) {
  for (unsigned spins = 0;; ++spins) {
    unsigned v = want;
    if (lane < nfl) v = __hip_atomic_load((gu32*)flags + lane * 32, RLX_AGENT);
    if (__all(v >= want)) break;
    if (spins > SPIN_CAP) {
      if (lane == 0) __hip_atomic_store((gu32*)tmo, 1u, RLX_AGENT);
      return false;
    }
  }
  __builtin_amdgcn_fence(__ATOMIC_ACQUIRE, "wavefront");
  return true;
}

#define STAMP(i)
#define STAMP_DUMP()
#define WSTAMP(i)
template <int CTRL> __device__ __forceinline__ float dppf(float old, float x) {
  return __builtin_bit_cast(float, __builtin_amdgcn_update_dpp(__builtin_bit_cast(int, old), __builtin_bit_cast(int, x), CTRL, 0xf, 0xf, false));
}
__device__ __forceinline__ float wave_max_dpp(float x) {
  const float ninf = -3e38f;
  x = fmaxf(x, dppf<0x111>(ninf, x));
  x = fmaxf(x, dppf<0x112>(ninf, x));
  x = fmaxf(x, dppf<0x114>(ninf, x));
  x = fmaxf(x, dppf<0x118>(ninf, x));
  x = fmaxf(x, dppf<0x142>(ninf, x));
  x = fmaxf(x, dppf<0x143>(ninf, x));
  return __builtin_bit_cast(float, __builtin_amdgcn_readlane(__builtin_bit_cast(int, x), 63));
}
__device__ __forceinline__ float wave_sum_dpp(float x) {
  x += dppf<0x111>(0.f, x);
  x += dppf<0x112>(0.f, x);
  x += dppf<0x114>(0.f, x);
  x += dppf<0x118>(0.f, x);
  x += dppf<0x142>(0.f, x);
  x += dppf<0x143>(0.f, x);
  return __builtin_bit_cast(float, __builtin_amdgcn_readlane(__builtin_bit_cast(int, x), 63));
}
constexpr int SMS = 528;
constexpr int QS = 144;
constexpr int VS = 80;
constexpr int SOS = 144;
#define VAR_NREP 4
constexpr int NREP = VAR_NREP;
__device__ __forceinline__ float xsum16(float x) {
  auto r = __builtin_amdgcn_permlane16_swap(__float_as_uint(x), __float_as_uint(x), false, false);
  return __uint_as_float(r[0]) + __uint_as_float(r[1]);
}
__device__ __forceinline__ float xsum32(float x) {
  auto r = __builtin_amdgcn_permlane32_swap(__float_as_uint(x), __float_as_uint(x), false, false);
  return __uint_as_float(r[0]) + __uint_as_float(r[1]);
}
__device__ __forceinline__ float xmax16(float x) {
  auto r = __builtin_amdgcn_permlane16_swap(__float_as_uint(x), __float_as_uint(x), false, false);
  return fmaxf(__uint_as_float(r[0]), __uint_as_float(r[1]));
}
__device__ __forceinline__ float xmax32(float x) {
  auto r = __builtin_amdgcn_permlane32_swap(__float_as_uint(x), __float_as_uint(x), false, false);
  return fmaxf(__uint_as_float(r[0]), __uint_as_float(r[1]));
}
__device__ __forceinline__ float red_mod4(float x) {
  x += dppf<0x124>(0.f, x);
  x += dppf<0x128>(0.f, x);
  return xsum32(xsum16(x));
}
typedef __bf16 bf16x2_t __attribute__((ext_vector_type(2)));
typedef float f32x2_t __attribute__((ext_vector_type(2)));
__device__ __forceinline__ unsigned pk2(float a, float b) {
  f32x2_t v = {a, b};
  bf16x2_t r = __builtin_convertvector(v, bf16x2_t);
  return __builtin_bit_cast(unsigned, r);
}
__device__ __forceinline__ float dot2sq(const unsigned x, const float acc) {
  return __builtin_amdgcn_fdot2_f32_bf16(__builtin_bit_cast(bf16x2_t, x), __builtin_bit_cast(bf16x2_t, x), acc, false);
}
__device__ __forceinline__ float lo16(unsigned u) { return __uint_as_float(u << 16); }
__device__ __forceinline__ float hi16(unsigned u) { return __uint_as_float(u & 0xffff0000u); }
__device__ __forceinline__ void scale_pair(float a, float b, float scale, float& oa, float& ob) {
  unsigned u = pk2(a, b);
  unsigned v = pk2(lo16(u) * scale, hi16(u) * scale);
  oa = lo16(v);
  ob = hi16(v);
}

#define XSPLIT 3
__device__ __forceinline__ int role_no() {
  const int b = blockIdx.x, x = b & 7, j = b >> 3;
  if (XSPLIT >= 3) {
    const int y = XSPLIT == 5 ? x & 3 : x >> 1;
    if (XSPLIT == 5 ? x < 4 : !(x & 1)) {
      if (j >= 16) return 104 + 16 * y + j - 16;
      return XSPLIT == 4 ? 16 * y + j : (j & 8) * 4 + 8 * y + (j & 7);
    }
    if (j < 16) return 168 + (j & 8) * 4 + 8 * y + (j & 7);
    const int i = 4 * y + (j & 3);
    return j < 20 ? 64 + i : j < 24 ? (i < 8 ? 96 + i : 224 + i) : j < 28 ? 80 + i : 240 + i;
  }
  const int L = x >> 2, xx = x & 3;
  if (j < 8) return 32 * L + 8 * xx + j;
  if (j < 12) return 64 + 16 * L + 4 * xx + j - 8;
  if (j < 16) { const int i = 4 * xx + j - 12; return L ? 240 + i : i < 8 ? 96 + i : 224 + i; }
  if (j < 24) return 104 + 8 * x + j - 16;
  return 168 + 32 * L + 8 * xx + j - 24;
}
#define MSTAMP()
#define WSTAMP2()
#define SST(k)
#define TSR(rn, k, ss, t)
#define TS(k, ss, t)
#define FSD
#define FS(k)
#define FD(ss)
struct MegaArgs {
  const u16* mem0;
  const u16* Dskip;
  char* ws;
  u16* out;
  const u16* x0;
  const u16 *logAre, *Aim, *Cre, *Cim, *logdt;
  const u16 *Wq, *Wk, *Wv, *Wo;
  int rn;
};

__device__ __forceinline__ int lane_id() {
  int l;
  asm volatile("v_mbcnt_lo_u32_b32 %0, -1, 0\n\tv_mbcnt_hi_u32_b32 %0, -1, %0" : "=v"(l));
  return l;
}
__device__ __forceinline__ bool spin_timeout(unsigned spins, unsigned* tmo) {
  if (spins > SPIN_CAP) {
    if (lane_id() == 0) __hip_atomic_store((gu32*)tmo, 1u, RLX_AGENT);
    return true;
  }
  return false;
}
#define PSWH (h & 2)
#define CD 146
#define GP 230
#define GR 50
#define GM 0
#define ROT (4 * (h & 1))
#define CPRIO 1
#define P1PRIO 3
#define QLEAD 2
#define POLL_SLEEP 12
#define SLP_C POLL_SLEEP
#define SLP_A POLL_SLEEP
#define SLP_W POLL_SLEEP
#define SLP_R POLL_SLEEP
#define SLP_V POLL_SLEEP
#define SLP_Q POLL_SLEEP
#define SLP_M 4
#define AWPRIO 2
#define SLP_S 8
#define SPOS 4
#define CVPRE 2
#define QHV 0
#define QCHK 0
#define LEADN 99
#define GATE_V 160
#define GATE_R 380
#define GATE_N 440
#define AWAHEAD 1
#define GATE_M 150
#define GATE_M2 380
#define QPACE 0
#define QPS 6
#define QPL 100
#define GATE_Q 170
#define SLP_Q1 24
#define QPF 3
#define QUP 7
#define QEP 3
#define AWD 11
#define GPER 226
#define GMA 60
#define GMR 60
#define GSTEP 30
#define GATE_RT 200
#define GATE_1 160
#define N1S 20
#define N1E 36
#define PRI_A 3
#define PRI_WA 2
#define PRI_WB 1
#define AWSKIP 3
#define PRI_0 3
#define AWDEF 1
#define SLP_F 2
#define EXP_OFF 0
#define AWSPEC 1
#define CVTHR 48
#define NR_C 8
#define NR_R 8
#define NR_W 8
#define NR_N 2
#define QAHEAD 0
#define NS0 4
#define SLP_N POLL_SLEEP
template <int SLP, int WPL>
__device__ __forceinline__ bool wait_n(const unsigned* f, int n, unsigned* tmo) {
  const int l = lane_id();
  for (unsigned spins = 0;; ++spins) {
    unsigned v = 1u;
    if (l < n) v = __hip_atomic_load((gu32*)f + (l / WPL) * 32 + l % WPL, RLX_AGENT);
    if (__all(v != 0u)) break;
    if (spin_timeout(spins, tmo)) return false;
    __builtin_amdgcn_s_sleep(SLP);
  }
  asm volatile("" ::: "memory");
  return true;
}
__device__ __forceinline__ unsigned now10() { return (unsigned)__builtin_amdgcn_s_memrealtime(); }
__device__ __forceinline__ void gate(const unsigned t) {
  for (int i = 0; i < 64; ++i) {
    const int d = (int)(t - now10());
    if (d <= 0) break;
    if (d > 100) __builtin_amdgcn_s_sleep(32);
    else if (d > 25) __builtin_amdgcn_s_sleep(8);
    else __builtin_amdgcn_s_sleep(2);
  }
}
template <int SLP, int WPL>
__device__ __forceinline__ bool grp_wait(const bool leader, volatile unsigned* sw, const unsigned val, const unsigned* f, int n,
                                         unsigned* tmo, const unsigned t) {
  unsigned spins = 0;
  gate(t);
  if (leader) {
    const int l = lane_id();
    for (;; ++spins) {
      unsigned v = 1u;
      if (l < n) v = __hip_atomic_load((gu32*)f + (l / WPL) * 32 + l % WPL, RLX_AGENT);
      if (__all(v != 0u) || spin_timeout(spins, tmo)) break;
      __builtin_amdgcn_s_sleep(SLP);
    }
    if ((threadIdx.x & 63) == 0) *sw = 2u * val + (spins ? 1u : 0u);
  } else {
    unsigned v;
    for (; (v = __builtin_amdgcn_readfirstlane(*sw)) < 2u * val; ++spins) {
      if (spins > 64u * SPIN_CAP) break;
      __builtin_amdgcn_s_sleep(SLP_F);
    }
    spins = v & 1u;
  }
  asm volatile("" ::: "memory");
  return spins != 0u;
}
template <int SLP, int WPL>
__device__ __forceinline__ bool wg_wait(const unsigned* f, int n, unsigned* tmo) {
  if (threadIdx.x < 64) (void)wait_n<SLP, WPL>(f, n, tmo);
  __syncthreads();
  asm volatile("" ::: "memory");
  return true;
}
__device__ __forceinline__ void set_flag(unsigned* f) { __hip_atomic_store((gu32*)f, 1u, RLX_AGENT); }
#define DRAIN() asm volatile("s_waitcnt vmcnt(0)" ::: "memory")
#define MKRS(ptr, bytes) __builtin_amdgcn_make_buffer_rsrc((void*)(ptr), 0, (int)(bytes), 0x00020000)
typedef unsigned v2u __attribute__((ext_vector_type(2)));
#define LD128(rs, voff, soff) __builtin_amdgcn_raw_buffer_load_b128(rs, voff, soff, AUX_SC1)
#define LD64(rs, voff, soff) __builtin_amdgcn_raw_buffer_load_b64(rs, voff, soff, AUX_SC1)
#define ST128(v, rs, voff, soff)                                              \
  do {                                                                        \
    const v4u st_d_ = (v);                                                    \
    __builtin_amdgcn_raw_buffer_store_b128(st_d_, rs, voff, soff, AUX_SC1);   \
    asm volatile("s_nop 1" ::"v"(st_d_) : "memory");                          \
  } while (0)
#define ST64(v, rs, voff, soff) __builtin_amdgcn_raw_buffer_store_b64(v, rs, voff, soff, AUX_SC1)
#define BF8(x) __builtin_bit_cast(bf16x8, x)
#define MFMA16(a, b, c) __builtin_amdgcn_mfma_f32_16x16x32_bf16(a, b, c, 0, 0, 0)
typedef short bf16x4 __attribute__((ext_vector_type(4)));
#define MFMA16K(a, b, c) \
  __builtin_amdgcn_mfma_f32_16x16x16bf16_1k(__builtin_bit_cast(bf16x4, a), __builtin_bit_cast(bf16x4, b), c, 0, 0, 0)
__device__ __forceinline__ float fmax_nn(float a, float b) { return __builtin_elementwise_maximum(a, b); }
typedef __bf16 bfv8 __attribute__((ext_vector_type(8)));
typedef __bf16 bfv16 __attribute__((ext_vector_type(16)));
typedef unsigned v8u __attribute__((ext_vector_type(8)));
#define SMF(a, b, c, idx) \
  __builtin_amdgcn_smfmac_f32_16x16x64_bf16(__builtin_bit_cast(bfv8, a), __builtin_bit_cast(bfv16, b), c, idx, 0, 0)
__device__ __forceinline__ float swap_add32(float a, float b) {
  auto r = __builtin_amdgcn_permlane32_swap(__float_as_uint(a), __float_as_uint(b), false, false);
  return __uint_as_float(r[0]) + __uint_as_float(r[1]);
}
__device__ __forceinline__ float swap_add16(float a, float b) {
  auto r = __builtin_amdgcn_permlane16_swap(__float_as_uint(a), __float_as_uint(b), false, false);
  return __uint_as_float(r[0]) + __uint_as_float(r[1]);
}
__device__ __forceinline__ v4u smf_fix(const v4u d) {
  auto x = __builtin_amdgcn_permlane16_swap(d.x, d.z, false, false);
  auto y = __builtin_amdgcn_permlane16_swap(d.y, d.w, false, false);
  return v4u{x[0], x[1], y[0], y[1]};
}
__device__ __forceinline__ v8u smf_bfrag(const u16* p) {
  const v2u a = *reinterpret_cast<const v2u*>(p), b = *reinterpret_cast<const v2u*>(p + 8);
  const v2u c = *reinterpret_cast<const v2u*>(p + 32), d = *reinterpret_cast<const v2u*>(p + 40);
  return v8u{a.x, b.x, a.y, b.y, c.x, d.x, c.y, d.y};
}

#define VAR_PPA 0
#define VAR_PPB 0
#define VAR_THR 0
#define VAR_THR2 0
#define CV_LEAD 100
#define VAR_EARLY 1
#define VAR_DRAIN 1
#define VAR_D1 0
#define VAR_D2 0
#define VAR_DR2 1
#define VAR_S1 0
#define VAR_S2 0
typedef __attribute__((address_space(1))) unsigned long long gu64;
typedef __attribute__((address_space(1))) char gch;
__device__ __forceinline__ int cvt_rpi(float x) {
  int r;
  asm("v_cvt_rpi_i32_f32 %0, %1" : "=v"(r) : "v"(x));
  return r;
}
typedef short v4s __attribute__((ext_vector_type(4)));
typedef __attribute__((address_space(3))) v4s* lp4s;
__device__ __forceinline__ void glds16(const void* sbase, const unsigned voff, const unsigned lds) {
  unsigned keep;
  asm volatile("s_nop 4\n\ts_mov_b32 %0, m0\n\ts_mov_b32 m0, %3\n\ts_nop 0\n\tglobal_load_lds_dwordx4 %1, %2\n\ts_mov_b32 m0, %0"
               : "=&s"(keep)
               : "v"(voff), "s"(sbase), "s"(lds)
               : "memory");
}
__device__ __forceinline__ void glds16c(const void* sbase, const unsigned voff, const unsigned lds) {
  unsigned keep;
  asm volatile("s_nop 4\n\ts_mov_b32 %0, m0\n\ts_mov_b32 m0, %3\n\ts_nop 0\n\tglobal_load_lds_dwordx4 %1, %2 sc1\n\ts_mov_b32 m0, %0"
               : "=&s"(keep)
               : "v"(voff), "s"(sbase), "s"(lds)
               : "memory");
}
__device__ __forceinline__ v2u trd(const unsigned a) {
  return __builtin_bit_cast(v2u, __builtin_amdgcn_ds_read_tr16_b64_v4i16((lp4s)(unsigned long)a));
}
__device__ __forceinline__ unsigned lds_base(char* smem) { return (unsigned)(unsigned long)(__attribute__((address_space(3))) char*)smem; }
#define RAW_BAR()                                           \
  do {                                                      \
    asm volatile("s_waitcnt lgkmcnt(0)" ::: "memory");      \
    __builtin_amdgcn_s_barrier();                           \
    asm volatile("" ::: "memory");                          \
  } while (0)
#define WAIT_VM(n) asm volatile("s_waitcnt vmcnt(" #n ")" ::: "memory")
template <bool SP>
__device__ __forceinline__ void wqkv_issue(const u16* Wm, const int hf, const int h, const int w, const int l, const unsigned sb) {
  const char* base = (const char*)Wm + (size_t)(256 * hf + 32 * w) * (HD * 2) + h * (DH * 2);
#pragma unroll
  for (int i = 0; i < 8; ++i) {
    const int r = l >> 4;
    const int f = SP ? ((r & 1) | ((i & 1) << 1) | (((i >> 1) & 1) << 2)) : (r | (((i >> 1) & 1) << 2));
    glds16(base, (unsigned)((4 * i + r) * (HD * 2) + (((l & 15) ^ (f << 1)) << 4)), sb + w * 16384 + hf * 8192 + i * 1024);
  }
}

__device__ __forceinline__ bool zero_seen(const v4u t, const v2u e) {
  const unsigned long long m = (((unsigned long long)e.y << 32) | e.x) + TAG_STEP;
  const unsigned lo = (unsigned)m, hi = (unsigned)(m >> 32);
  return __all(((t.x ^ lo) | (t.y ^ hi) | (t.z ^ lo) | (t.w ^ hi)) == 0u);
}
__device__ __forceinline__ void zero_wait(char* ws) {
  const __amdgpu_buffer_rsrc_t rs = MKRS(ws + OFF_TAG, 8192 + 8);
  for (unsigned spins = 0; spins < 16u * SPIN_CAP; ++spins) {
    const v4u t = LD128(rs, lane_id() * 16, (blockIdx.x & 7) * 1024);
    const v2u e = LD64(rs, 0, 8192);
    if (zero_seen(t, e)) break;
    asm volatile("s_sleep 4" ::: "memory");
  }
  asm volatile("" ::: "memory");
}
__device__ __forceinline__ unsigned row_stats(const v4u raw) {
  float v[8];
#pragma unroll
  for (int e = 0; e < 4; ++e) { v[2 * e] = lo16(raw[e]); v[2 * e + 1] = hi16(raw[e]); }
  float s = 0.f;
#pragma unroll
  for (int e = 0; e < 8; ++e) s += v[e];
  const float mean = wave_sum(s) * (1.0f / DM);
  float q = 0.f;
#pragma unroll
  for (int e = 0; e < 8; ++e) { const float t = v[e] - mean; q += t * t; }
  const float var = wave_sum(q) * (1.0f / DM);
  const float sd = bfr(sqrtf(bfr(bfr(var) + 1e-5f)));
  return (unsigned)f2bf(mean) | ((unsigned)f2bf(sd) << 16);
}
__device__ __forceinline__ void start_kgen(const MegaArgs& A, char* smem, const int hi, const int xi) {
  const int tid = threadIdx.x, j = tid >> 7, u = tid & 127, d = 4 * hi + j;
  const int w = __builtin_amdgcn_readfirstlane(tid >> 6), l = tid & 63;
  float* prm = reinterpret_cast<float*>(smem) + j * 10 * NST;
  float* part = reinterpret_cast<float*>(smem) + 40 * NST + j * 8 * 400;
  float* kd = reinterpret_cast<float*>(smem) + 40 * NST + 32 * 400 + j * 392;
  v4u raw[8], ra, rb;
  const int r0 = 16 * xi + 2 * w;
  if (xi >= 0) {
    ra = *reinterpret_cast<const v4u*>(A.x0 + (size_t)r0 * DM + l * 8);
    rb = *reinterpret_cast<const v4u*>(A.x0 + (size_t)(r0 + 1) * DM + l * 8);
  }
  const u16 p0 = A.logdt[d], p1 = A.logAre[d * NST + u], p2 = A.Aim[d * NST + u], p3 = A.Cre[d * NST + u], p4 = A.Cim[d * NST + u];
  if (w >= 6) {
#pragma unroll
    for (int i = 0; i < 8; ++i) raw[i] = *reinterpret_cast<const v4u*>(A.x0 + (size_t)(16 * hi + 8 * (w - 6) + i) * DM + l * 8);
  }
  const v2u en = LD64(MKRS(A.ws + OFF_TAG, 8192 + 8), 0, 8192);
  {
    const __amdgpu_buffer_rsrc_t rz = MKRS(A.ws + OFF_Z, Z_BYTES);
    ST128((v4u{0u, 0u, 0u, 0u}), rz, tid * 16, hi * 8192);
    ST128((v4u{0u, 0u, 0u, 0u}), rz, tid * 16, (128 + hi) * 8192);
  }
  if (xi >= 0) {
    const unsigned sa = row_stats(ra), sb = row_stats(rb), st = (l & 1) ? sb : sa;
    const unsigned long long m = (((unsigned long long)en.y << 32) | en.x) + TAG_STEP;
    ST128((v4u{st, (unsigned)m, st, (unsigned)(m >> 32)}), MKRS(A.ws + OFF_G0, 8192), (l >> 1) * 2048 + (l & 1) * 16, r0 * 16);
    SST(11);
  }
  {
    const float dt = bfr(expf(bf2f(p0)));
    const float ar = -bfr(expf(bf2f(p1))) * dt, ai = bf2f(p2) * dt;
    prm[u] = ar;
    prm[NST + u] = ai;
    prm[2 * NST + u] = bf2f(p3) * dt;
    prm[3 * NST + u] = bf2f(p4) * dt;
#pragma unroll
    for (int cls = 1; cls < 4; ++cls) {
      const float st = (float)(1 << cls);
      const float e = expf(ar * st);
      float sn, cs;
      sincosf(ai * st, &sn, &cs);
      prm[(2 + 2 * cls) * NST + u] = e * cs;
      prm[(3 + 2 * cls) * NST + u] = e * sn;
    }
  }
  DRAIN();
  __syncthreads();
  SST(1);
  if (w >= 6) {
    if (w == 7 && l < 8)
      __hip_atomic_store((gu64*)(A.ws + OFF_TAG) + l * 128 + hi, (((unsigned long long)en.y << 32) | en.x) + TAG_STEP, RLX_AGENT);
    unsigned stw = 0u;
#pragma unroll
    for (int i = 0; i < 8; ++i) {
      const unsigned t = row_stats(raw[i]);
      if (l == i) stw = t;
    }
    const int sx = 2 * hi + w - 6;
    if (l < 8) __hip_atomic_store((gu32*)(A.ws + OFF_GT) + 8 * sx + l, stw, RLX_AGENT);
    zero_wait(A.ws);
    DRAIN();
    if (l < 4) set_flag((unsigned*)(A.ws + OFF_FL) + FL_FS + (l * 32 + (sx >> 3)) * 32 + (sx & 7));
  } else {
    const int jm = tid / 96, um = tid % 96;
    const int r = um % 12, ng = um / 12;
    const float* prm = reinterpret_cast<float*>(smem) + jm * 10 * NST;
    float* part = reinterpret_cast<float*>(smem) + 40 * NST + jm * 8 * 400;
    const float* pw = prm + (4 + 2 * (r >> 2)) * NST;
    const float p0 = pos_of_index(256 + 32 * r);
    f32x2_t acc[33];
#pragma unroll
    for (int i = 0; i < 33; ++i) acc[i] = f32x2_t{0.f, 0.f};
#pragma unroll 1
    for (int bt = 0; bt < 4; ++bt) {
      const int n = 16 * ng + 4 * bt;
      const f32x4 ar = *reinterpret_cast<const f32x4*>(prm + n), ai = *reinterpret_cast<const f32x4*>(prm + NST + n);
      const f32x4 cr = *reinterpret_cast<const f32x4*>(prm + 2 * NST + n), ci = *reinterpret_cast<const f32x4*>(prm + 3 * NST + n);
      const f32x4 wr = *reinterpret_cast<const f32x4*>(pw + n), wi = *reinterpret_cast<const f32x4*>(pw + NST + n);
      float yr0[4], yi0[4];
#pragma unroll
      for (int k = 0; k < 4; ++k) {
        const float e = __expf(ar[k] * p0);
        float sn, cs;
        __sincosf(ai[k] * p0, &sn, &cs);
        const float zr = e * cs, zi = e * sn;
        yr0[k] = cr[k] * zr - ci[k] * zi;
        yi0[k] = cr[k] * zi + ci[k] * zr;
      }
      f32x2_t yra = {yr0[0], yr0[1]}, yia = {yi0[0], yi0[1]}, wra = {wr[0], wr[1]}, wia = {wi[0], wi[1]};
      f32x2_t yrb = {yr0[2], yr0[3]}, yib = {yi0[2], yi0[3]}, wrb = {wr[2], wr[3]}, wib = {wi[2], wi[3]};
#pragma unroll
      for (int i = 0; i < 33; ++i) {
        acc[i] += yra + yrb;
        const f32x2_t ta = yra * wra - yia * wia, tb = yrb * wrb - yib * wib;
        yia = yra * wia + yia * wra;
        yib = yrb * wib + yib * wrb;
        yra = ta;
        yrb = tb;
      }
    }
#pragma unroll
    for (int i = 0; i < 33; ++i) part[ng * 400 + 33 * r + i] = acc[i].x + acc[i].y;
  }
  __syncthreads();
  SST(2);
  for (int i = u; i < 385; i += 128) {
    const int o = i < 384 ? 33 * (i >> 5) + (i & 31) : 33 * 11 + 32;
    float v = 0.f;
#pragma unroll
    for (int g = 0; g < 8; ++g) v += part[g * 400 + o];
    kd[i] = 2.0f * v;
  }
  __syncthreads();
  const __amdgpu_buffer_rsrc_t rs = MKRS(A.ws + OFF_KF, DM * 2 * CKN * 2);
  for (int un = u; un < 229; un += 128) {
    v4u oh, ol;
#pragma unroll
    for (int e = 0; e < 4; ++e) {
      float kv[2];
#pragma unroll
      for (int k = 0; k < 2; ++k) {
        const int lag = 2080 - (8 * un + 2 * e + k);
        kv[k] = (lag >= 256 && lag < L_SEQ) ? kd[index_of_l(lag) - 256] : 0.f;
      }
      oh[e] = pk2(kv[0], kv[1]);
      ol[e] = pk2(kv[0] - lo16(oh[e]), kv[1] - hi16(oh[e]));
    }
    ST128(oh, rs, (d * 2 * CKN + 8 * un) * 2, 0);
    ST128(ol, rs, (d * 2 * CKN + CKN + 8 * un) * 2, 0);
  }
  DRAIN();
  __syncthreads();
  if (tid == 0) set_flag((unsigned*)(A.ws + OFF_FL) + FL_KG + (hi >> 1) * 32 + (hi & 1));
  SST(3);
}

constexpr int C_SM = 131072, C_SO = C_SM + 8192, C_KT = C_SO + 2048, C_X = C_KT + 16384, C_SQ = C_X + 1024;
constexpr int C_SK = C_SQ + (7 * QS + 128) * 2;
static_assert(C_SK + 8 * QS * 2 <= 163840, "lds");
#define PSW 1
#define CHAIN_ASM_ATT \
  "s_mov_b32 s92, 1152\n" \
  "s_mov_b32 s93, 576\n" \
  "s_mov_b32 s94, 2272\n" \
  "s_mov_b32 s95, 272\n" \
  "s_mov_b32 s96, 288\n" \
  "s_mov_b32 s91, 0x40000001\n" \
  "s_mov_b32 s84, 0x4b400000\n" \
  "s_mov_b32 s85, 0x4b400000\n" \
  "s_mov_b32 s78, 0x3e0293ee\n" \
  "s_mov_b32 s79, 0x3fb8aa3b\n" \
  "s_mov_b32 s76, 0xff00ff00\n" \
  "s_mov_b32 s77, 0xff00ff00\n" \
  "s_add_u32 s58, s56, s60\n" \
  "s_addc_u32 s59, s57, 0\n" \
  "s_xor_b32 s38, s35, 0\n" \
  "s_add_u32 s26, s58, s38\n" \
  "s_addc_u32 s27, s59, 0\n" \
  "s_xor_b32 s38, s35, 1024\n" \
  "s_add_u32 s28, s58, s38\n" \
  "s_addc_u32 s29, s59, 0\n" \
  "s_xor_b32 s38, s35, 2048\n" \
  "s_add_u32 s30, s58, s38\n" \
  "s_addc_u32 s31, s59, 0\n" \
  "s_xor_b32 s38, s35, 3072\n" \
  "s_add_u32 s32, s58, s38\n" \
  "s_addc_u32 s33, s59, 0\n" \
  "s_lshl_b32 s98, s73, 10\n" \
  "s_add_u32 s98, s98, s74\n" \
  "s_add_u32 s98, s98, 131072\n" \
  "s_lshl_b32 s97, s73, 5\n" \
  "s_add_u32 s97, s97, s74\n" \
  "s_add_u32 s97, s97, 158720\n" \
  "s_lshl_b32 s99, s73, 10\n" \
  "v_mbcnt_lo_u32_b32 v48, -1, 0\n" \
  "v_mbcnt_hi_u32_b32 v48, -1, v48\n" \
  "v_and_b32 v49, 15, v48\n" \
  "v_lshl_add_u32 v8, v49, 1, s97\n" \
  "v_bfe_u32 v49, v48, 4, 1\n" \
  "v_mad_u32_u24 v8, v49, s92, v8\n" \
  "v_lshrrev_b32 v49, 5, v48\n" \
  "v_mad_u32_u24 v9, v49, s93, v8\n" \
  "v_mad_u32_u24 v8, v49, s94, v8\n" \
  "v_add_u32 v10, s99, v1\n" \
  "s_lshr_b32 s88, s99, 2\n" \
  "s_add_u32 s88, s88, s74\n" \
  "s_add_u32 s88, s88, 139264\n" \
  "v_lshl_add_u32 v7, v49, 10, s88\n" \
  "v_and_b32 v49, 31, v48\n" \
  "v_lshl_add_u32 v7, v49, 3, v7\n" \
  "v_mov_b64 v[12:13], 0\n" \
  "v_mov_b64 v[14:15], 0\n" \
  "v_mov_b64 v[16:17], 0\n" \
  "v_mov_b64 v[18:19], 0\n" \
  "v_mov_b64 v[20:21], 0\n" \
  "v_mov_b64 v[22:23], 0\n" \
  "s_add_u32 s88, s74, 157760\n" \
  "v_mov_b32 v59, s88\n" \
  "v_mov_b32 v24, 0x44870000\n" \
  "v_mul_f32 v24, s72, v24\n" \
  "s_nop 0\n" \
  "v_readfirstlane_b32 s86, v24\n" \
  "s_lshr_b32 s86, s86, 23\n" \
  "s_addk_i32 s86, 0xff83\n" \
  "s_ashr_i32 s86, s86, 1\n" \
  "s_max_i32 s86, s86, -20\n" \
  "s_min_i32 s86, s86, 100\n" \
  "s_mov_b32 s82, s86\n" \
  "s_lshr_b32 s83, s72, 23\n" \
  "s_sub_i32 s83, s83, 242\n" \
  "s_lshl_b32 s88, s73, 10\n" \
  "v_add_u32 v24, s88, v1\n" \
  "ds_read_b128 v[32:35], v24 offset:0\n" \
  "s_waitcnt lgkmcnt(0)\n" \
  "v_mov_b32 v11, 0\n" \
  "v_dot2c_f32_bf16 v11, v32, v32\n" \
  "v_dot2c_f32_bf16 v11, v33, v33\n" \
  "v_dot2c_f32_bf16 v11, v34, v34\n" \
  "v_dot2c_f32_bf16 v11, v35, v35\n" \
  "s_nop 1\n" \
  "s_nop 0\n" \
  "v_cmpx_lt_f32 vcc, 2.0, v11\n" \
  "ds_max_u32 v59, v11\n" \
  "s_mov_b64 exec, -1\n" \
  ".LCA_TOP_%=:\n" \
  "s_waitcnt lgkmcnt(0)\n" \
  "s_barrier\n" \
  "ds_read_b128 v[40:43], v1 offset:0\n" \
  "ds_read_b128 v[44:47], v1 offset:1024\n" \
  "ds_read_b128 v[48:51], v1 offset:2048\n" \
  "ds_read_b128 v[52:55], v1 offset:3072\n" \
  "ds_read_b128 v[56:59], v1 offset:4096\n" \
  "ds_read_b128 v[60:63], v1 offset:5120\n" \
  "s_cmp_lg_u32 s73, 0\n" \
  "s_cbranch_scc1 .LCA_NOZ_%=\n" \
  "s_xor_b32 s88, s60, 0x8000\n" \
  "s_add_u32 s88, s88, s61\n" \
  "buffer_store_dwordx2 v[12:13], v5, s[40:43], s88 offen sc1\n" \
  ".LCA_NOZ_%=:\n" \
  "s_waitcnt lgkmcnt(5)\n" \
  "v_smfmac_f32_16x16x64_bf16 v[20:23], v[40:43], v[192:199], v0\n" \
  "v_smfmac_f32_16x16x64_bf16 v[12:15], v[40:43], v[64:71], v0\n" \
  "v_smfmac_f32_16x16x64_bf16 v[16:19], v[40:43], v[128:135], v0\n" \
  "ds_read_b128 v[40:43], v1 offset:6144\n" \
  "s_waitcnt lgkmcnt(5)\n" \
  "v_smfmac_f32_16x16x64_bf16 v[20:23], v[44:47], v[200:207], v0\n" \
  "v_smfmac_f32_16x16x64_bf16 v[12:15], v[44:47], v[72:79], v0\n" \
  "v_smfmac_f32_16x16x64_bf16 v[16:19], v[44:47], v[136:143], v0\n" \
  "ds_read_b128 v[44:47], v1 offset:7168\n" \
  "s_waitcnt lgkmcnt(5)\n" \
  "v_smfmac_f32_16x16x64_bf16 v[20:23], v[48:51], v[208:215], v0\n" \
  "v_smfmac_f32_16x16x64_bf16 v[12:15], v[48:51], v[80:87], v0\n" \
  "v_smfmac_f32_16x16x64_bf16 v[16:19], v[48:51], v[144:151], v0\n" \
  "ds_read_b128 v[24:27], v1 offset:10240\n" \
  "ds_read_b128 v[28:31], v1 offset:14336\n" \
  "s_waitcnt lgkmcnt(6)\n" \
  "v_smfmac_f32_16x16x64_bf16 v[20:23], v[52:55], v[216:223], v0\n" \
  "v_smfmac_f32_16x16x64_bf16 v[12:15], v[52:55], v[88:95], v0\n" \
  "v_smfmac_f32_16x16x64_bf16 v[16:19], v[52:55], v[152:159], v0\n" \
  "ds_read_b128 v[32:35], v1 offset:11264\n" \
  "ds_read_b128 v[36:39], v1 offset:15360\n" \
  "s_waitcnt lgkmcnt(7)\n" \
  "v_smfmac_f32_16x16x64_bf16 v[20:23], v[56:59], v[224:231], v0\n" \
  "v_smfmac_f32_16x16x64_bf16 v[12:15], v[56:59], v[96:103], v0\n" \
  "v_smfmac_f32_16x16x64_bf16 v[16:19], v[56:59], v[160:167], v0\n" \
  "s_waitcnt lgkmcnt(6)\n" \
  "v_smfmac_f32_16x16x64_bf16 v[20:23], v[60:63], v[232:239], v0\n" \
  "v_smfmac_f32_16x16x64_bf16 v[12:15], v[60:63], v[104:111], v0\n" \
  "v_smfmac_f32_16x16x64_bf16 v[16:19], v[60:63], v[168:175], v0\n" \
  "s_waitcnt lgkmcnt(5)\n" \
  "v_smfmac_f32_16x16x64_bf16 v[20:23], v[40:43], v[240:247], v0\n" \
  "v_smfmac_f32_16x16x64_bf16 v[12:15], v[40:43], v[112:119], v0\n" \
  "v_smfmac_f32_16x16x64_bf16 v[16:19], v[40:43], v[176:183], v0\n" \
  "s_waitcnt lgkmcnt(4)\n" \
  "v_smfmac_f32_16x16x64_bf16 v[20:23], v[44:47], v[248:255], v0\n" \
  "v_smfmac_f32_16x16x64_bf16 v[12:15], v[44:47], v[120:127], v0\n" \
  "v_smfmac_f32_16x16x64_bf16 v[16:19], v[44:47], v[184:191], v0\n" \
  "s_cmp_eq_u32 s71, 0\n" \
  "s_nop 4\n" \
  "v_permlane32_swap_b32 v20, v22\n" \
  "v_permlane32_swap_b32 v21, v23\n" \
  "v_pk_add_f32 v[20:21], v[20:21], v[22:23]\n" \
  "v_permlane32_swap_b32 v12, v16\n" \
  "v_permlane32_swap_b32 v13, v17\n" \
  "v_cvt_pk_bf16_f32 v20, v20, v21\n" \
  "v_pk_add_f32 v[12:13], v[12:13], v[16:17]\n" \
  "v_permlane32_swap_b32 v14, v18\n" \
  "v_permlane32_swap_b32 v15, v19\n" \
  "ds_write_b16 v9, v20 offset:128\n" \
  "v_cvt_pk_bf16_f32 v12, v12, v13\n" \
  "v_pk_add_f32 v[14:15], v[14:15], v[18:19]\n" \
  "ds_write_b16_d16_hi v9, v20 offset:416\n" \
  "ds_write_b16 v8, v12\n" \
  "v_cvt_pk_bf16_f32 v14, v14, v15\n" \
  "ds_write_b16_d16_hi v8, v12 offset:288\n" \
  "ds_write_b16 v8, v14 offset:576\n" \
  "ds_write_b16_d16_hi v8, v14 offset:864\n" \
  "s_setprio 3\n" \
  "s_waitcnt lgkmcnt(0)\n" \
  "s_barrier\n" \
  "s_cbranch_scc1 .LCA_LAST_%=\n" \
  "ds_read_b32 v11, v1 offset:26688\n" \
  "ds_read_b128 v[40:43], v2 offset:0\n" \
  "ds_read_b128 v[44:47], v3 offset:0\n" \
  "ds_read_b128 v[48:51], v2 offset:64\n" \
  "ds_read_b128 v[52:55], v3 offset:64\n" \
  "ds_read_b128 v[56:59], v2 offset:128\n" \
  "ds_read_b128 v[60:63], v3 offset:128\n" \
  "s_waitcnt lgkmcnt(5)\n" \
  "v_mfma_f32_16x16x32_bf16 v[12:15], v[24:27], v[40:43], 0\n" \
  "v_mfma_f32_16x16x32_bf16 v[16:19], v[28:31], v[40:43], 0\n" \
  "v_readfirstlane_b32 s86, v11\n" \
  "ds_read_b128 v[24:27], v1 offset:12288\n" \
  "ds_read_b128 v[28:31], v1 offset:16384\n" \
  "s_waitcnt lgkmcnt(6)\n" \
  "v_mfma_f32_16x16x32_bf16 v[20:23], v[44:47], v[40:43], 0\n" \
  "s_lshr_b32 s86, s86, 23\n" \
  "s_add_i32 s86, s86, s83\n" \
  "ds_read_b128 v[40:43], v2 offset:192\n" \
  "ds_read_b128 v[44:47], v3 offset:192\n" \
  "s_waitcnt lgkmcnt(7)\n" \
  "v_mfma_f32_16x16x32_bf16 v[12:15], v[32:35], v[48:51], v[12:15]\n" \
  "v_mfma_f32_16x16x32_bf16 v[16:19], v[36:39], v[48:51], v[16:19]\n" \
  "s_ashr_i32 s86, s86, 1\n" \
  "s_max_i32 s86, s86, s82\n" \
  "ds_read_b128 v[32:35], v1 offset:13312\n" \
  "ds_read_b128 v[36:39], v1 offset:17408\n" \
  "s_waitcnt lgkmcnt(8)\n" \
  "v_mfma_f32_16x16x32_bf16 v[20:23], v[52:55], v[48:51], v[20:23]\n" \
  "s_min_i32 s86, s86, 100\n" \
  "s_lshl_b32 s86, s86, 23\n" \
  "s_sub_i32 s84, 0x4a800000, s86\n" \
  "ds_read_b128 v[48:51], v10 offset:18432\n" \
  "ds_read_b128 v[52:55], v10 offset:22528\n" \
  "s_waitcnt lgkmcnt(8)\n" \
  "v_mfma_f32_16x16x32_bf16 v[20:23], v[60:63], v[56:59], v[20:23]\n" \
  "s_waitcnt lgkmcnt(6)\n" \
  "v_mfma_f32_16x16x32_bf16 v[12:15], v[24:27], v[56:59], v[12:15]\n" \
  "v_mfma_f32_16x16x32_bf16 v[16:19], v[28:31], v[56:59], v[16:19]\n" \
  "ds_read_b128 v[56:59], v6 offset:0\n" \
  "ds_read_b128 v[60:63], v6 offset:1024\n" \
  "s_waitcnt lgkmcnt(6)\n" \
  "v_mfma_f32_16x16x32_bf16 v[20:23], v[44:47], v[40:43], v[20:23]\n" \
  "ds_read_b128 v[24:27], v6 offset:2048\n" \
  "ds_read_b128 v[28:31], v6 offset:3072\n" \
  "s_waitcnt lgkmcnt(6)\n" \
  "v_mfma_f32_16x16x32_bf16 v[12:15], v[32:35], v[40:43], v[12:15]\n" \
  "v_mfma_f32_16x16x32_bf16 v[16:19], v[36:39], v[40:43], v[16:19]\n" \
  "ds_read_b128 v[32:35], v6 offset:8192\n" \
  "ds_read_b128 v[36:39], v6 offset:9216\n" \
  "s_waitcnt lgkmcnt(6)\n" \
  "v_cndmask_b32 v20, v20, v21, s[76:77]\n" \
  "v_mov_b32 v41, 0\n" \
  "v_mov_b32 v42, 0\n" \
  "s_nop 1\n" \
  "v_cndmask_b32 v12, v12, v16, s[76:77]\n" \
  "v_cndmask_b32 v13, v13, v17, s[76:77]\n" \
  "v_cndmask_b32 v14, v14, v18, s[76:77]\n" \
  "v_cndmask_b32 v15, v15, v19, s[76:77]\n" \
  "v_maximum3_f32 v16, v20, v12, v13\n" \
  "v_maximum3_f32 v16, v16, v14, v15\n" \
  "s_nop 1\n" \
  "v_max_f32_dpp v16, v16, v16 row_ror:8 row_mask:0xf bank_mask:0xf\n" \
  "v_mov_b32 v17, v16\n" \
  "s_nop 1\n" \
  "v_permlane16_swap_b32 v16, v17\n" \
  "v_max_f32 v16, v16, v17\n" \
  "v_mov_b32 v17, v16\n" \
  "s_nop 1\n" \
  "v_permlane32_swap_b32 v16, v17\n" \
  "v_max_f32 v16, v16, v17\n" \
  "v_mul_f32 v16, s78, v16\n" \
  "v_pk_fma_f32 v[12:13], v[12:13], s[78:79], v[16:17] op_sel_hi:[1,0,0] neg_lo:[0,0,1] neg_hi:[0,0,1]\n" \
  "v_pk_fma_f32 v[14:15], v[14:15], s[78:79], v[16:17] op_sel_hi:[1,0,0] neg_lo:[0,0,1] neg_hi:[0,0,1]\n" \
  "v_fma_f32 v20, v20, s78, -v16\n" \
  "v_exp_f32 v12, v12\n" \
  "v_exp_f32 v13, v13\n" \
  "v_exp_f32 v14, v14\n" \
  "v_exp_f32 v15, v15\n" \
  "v_exp_f32 v20, v20\n" \
  "s_barrier\n" \
  "ds_read_b64 v[22:23], v7 offset:0\n" \
  "buffer_load_dword v7, v5, s[16:19], 0 offen sc1\n" \
  "v_cvt_pk_bf16_f32 v44, v12, v13\n" \
  "v_cvt_pk_bf16_f32 v45, v14, v15\n" \
  "v_pk_add_f32 v[8:9], v[12:13], v[14:15]\n" \
  "v_mov_b32_dpp v46, v44 row_ror:8 row_mask:0xf bank_mask:0xf\n" \
  "v_mov_b32_dpp v17, v20 row_ror:8 row_mask:0xf bank_mask:0xf\n" \
  "v_mov_b32_dpp v47, v45 row_ror:8 row_mask:0xf bank_mask:0xf\n" \
  "v_add_f32 v10, v8, v20\n" \
  "v_cvt_pk_bf16_f32 v40, v20, v17\n" \
  "v_add_f32 v10, v10, v9\n" \
  "v_mfma_f32_16x16x32_bf16 v[12:15], v[48:51], v[44:47], 0\n" \
  "v_cvt_pk_bf16_f32 v43, v20, v17\n" \
  "v_add_f32_dpp v10, v10, v10 row_ror:8 row_mask:0xf bank_mask:0xf\n" \
  "v_mfma_f32_16x16x32_bf16 v[16:19], v[52:55], v[44:47], 0\n" \
  "v_mov_b32 v11, v10\n" \
  "s_waitcnt lgkmcnt(0)\n" \
  "v_mfma_f32_16x16x16_bf16 v[12:15], v[22:23], v[40:41], v[12:15]\n" \
  "v_mfma_f32_16x16x16_bf16 v[16:19], v[22:23], v[42:43], v[16:19]\n" \
  "v_permlane16_swap_b32 v10, v11\n" \
  "v_add_f32 v10, v10, v11\n" \
  "ds_read_b128 v[48:51], v6 offset:10240\n" \
  "v_mov_b32 v11, v10\n" \
  "ds_read_b128 v[52:55], v6 offset:11264\n" \
  "v_mov_b64 v[20:21], 0\n" \
  "v_mov_b64 v[22:23], 0\n" \
  "v_permlane32_swap_b32 v10, v11\n" \
  "v_add_f32 v10, v10, v11\n" \
  "v_rcp_f32 v10, v10\n" \
  "s_nop 0\n" \
  "v_pk_mul_f32 v[12:13], v[12:13], v[10:11] op_sel_hi:[1,0]\n" \
  "v_pk_mul_f32 v[14:15], v[14:15], v[10:11] op_sel_hi:[1,0]\n" \
  "v_pk_mul_f32 v[16:17], v[16:17], v[10:11] op_sel_hi:[1,0]\n" \
  "v_pk_mul_f32 v[18:19], v[18:19], v[10:11] op_sel_hi:[1,0]\n" \
  "v_cvt_pk_bf16_f32 v40, v12, v13\n" \
  "v_cvt_pk_bf16_f32 v41, v14, v15\n" \
  "v_cvt_pk_bf16_f32 v42, v16, v17\n" \
  "ds_write_b64 v4, v[40:41]\n" \
  "v_cvt_pk_bf16_f32 v43, v18, v19\n" \
  "ds_write_b64 v4, v[42:43] offset:1024\n" \
  "v_mov_b64 v[12:13], 0\n" \
  "v_mov_b64 v[14:15], 0\n" \
  "v_mov_b64 v[16:17], 0\n" \
  "v_mov_b64 v[18:19], 0\n" \
  "v_mov_b64 v[8:9], 0\n" \
  "v_mov_b64 v[10:11], 0\n" \
  "s_setprio 0\n" \
  "s_waitcnt lgkmcnt(0)\n" \
  "s_barrier\n" \
  "ds_read_b128 v[40:43], v1 offset:8192\n" \
  "ds_read_b128 v[44:47], v1 offset:9216\n" \
  "s_waitcnt lgkmcnt(1)\n" \
  "v_smfmac_f32_16x16x64_bf16 v[12:15], v[40:43], v[56:63], v0\n" \
  "ds_read_b128 v[56:59], v6 offset:4096\n" \
  "ds_read_b128 v[60:63], v6 offset:5120\n" \
  "v_smfmac_f32_16x16x64_bf16 v[16:19], v[40:43], v[24:31], v0\n" \
  "ds_read_b128 v[24:27], v6 offset:6144\n" \
  "ds_read_b128 v[28:31], v6 offset:7168\n" \
  "s_waitcnt lgkmcnt(4)\n" \
  "v_smfmac_f32_16x16x64_bf16 v[12:15], v[44:47], v[32:39], v0\n" \
  "ds_read_b128 v[32:35], v6 offset:12288\n" \
  "ds_read_b128 v[36:39], v6 offset:13312\n" \
  "v_smfmac_f32_16x16x64_bf16 v[16:19], v[44:47], v[48:55], v0\n" \
  "ds_read_b128 v[48:51], v6 offset:14336\n" \
  "ds_read_b128 v[52:55], v6 offset:15360\n" \
  "s_nop 0\n" \
  "s_waitcnt lgkmcnt(6)\n" \
  "v_smfmac_f32_16x16x64_bf16 v[20:23], v[40:43], v[56:63], v0\n" \
  "s_nop 0\n" \
  "s_waitcnt lgkmcnt(4)\n" \
  "v_smfmac_f32_16x16x64_bf16 v[8:11], v[40:43], v[24:31], v0\n" \
  "v_permlane32_swap_b32 v12, v17\n" \
  "v_permlane32_swap_b32 v13, v16\n" \
  "v_permlane32_swap_b32 v14, v19\n" \
  "v_permlane32_swap_b32 v15, v18\n" \
  "s_waitcnt lgkmcnt(2)\n" \
  "v_smfmac_f32_16x16x64_bf16 v[20:23], v[44:47], v[32:39], v0\n" \
  "v_pk_add_f32 v[12:13], v[12:13], v[16:17] op_sel:[0,1] op_sel_hi:[1,0]\n" \
  "v_pk_add_f32 v[14:15], v[14:15], v[18:19] op_sel:[0,1] op_sel_hi:[1,0]\n" \
  "v_pk_fma_f32 v[12:13], v[12:13], s[84:85], s[84:85] op_sel:[0,0,1] op_sel_hi:[1,0,1]\n" \
  "v_pk_fma_f32 v[14:15], v[14:15], s[84:85], s[84:85] op_sel:[0,0,1] op_sel_hi:[1,0,1]\n" \
  "s_waitcnt lgkmcnt(0)\n" \
  "v_smfmac_f32_16x16x64_bf16 v[8:11], v[44:47], v[48:55], v0\n" \
  "v_lshl_add_u32 v12, v12, 6, s91\n" \
  "v_lshl_add_u32 v13, v13, 2, s75\n" \
  "global_atomic_add_x2 v5, v[12:13], s[26:27]\n" \
  "v_lshl_add_u32 v14, v14, 6, s91\n" \
  "v_lshl_add_u32 v15, v15, 2, s75\n" \
  "global_atomic_add_x2 v5, v[14:15], s[30:31]\n" \
  "s_nop 1\n" \
  "v_permlane32_swap_b32 v20, v9\n" \
  "v_permlane32_swap_b32 v21, v8\n" \
  "v_permlane32_swap_b32 v22, v11\n" \
  "v_permlane32_swap_b32 v23, v10\n" \
  "v_pk_add_f32 v[20:21], v[20:21], v[8:9] op_sel:[0,1] op_sel_hi:[1,0]\n" \
  "v_pk_add_f32 v[22:23], v[22:23], v[10:11] op_sel:[0,1] op_sel_hi:[1,0]\n" \
  "v_pk_fma_f32 v[20:21], v[20:21], s[84:85], s[84:85] op_sel:[0,0,1] op_sel_hi:[1,0,1]\n" \
  "v_pk_fma_f32 v[22:23], v[22:23], s[84:85], s[84:85] op_sel:[0,0,1] op_sel_hi:[1,0,1]\n" \
  "v_lshl_add_u32 v20, v20, 6, s91\n" \
  "v_lshl_add_u32 v21, v21, 2, s75\n" \
  "global_atomic_add_x2 v5, v[20:21], s[32:33]\n" \
  "v_lshl_add_u32 v22, v22, 6, s91\n" \
  "v_lshl_add_u32 v23, v23, 2, s75\n" \
  "global_atomic_add_x2 v5, v[22:23], s[28:29]\n" \
  "v_mbcnt_lo_u32_b32 v48, -1, 0\n" \
  "v_mbcnt_hi_u32_b32 v48, -1, v48\n" \
  "v_lshrrev_b32 v49, 3, v48\n" \
  "v_and_b32 v50, 7, v48\n" \
  "v_lshlrev_b32 v50, 4, v50\n" \
  "v_lshl_or_b32 v44, v49, 8, v50\n" \
  "s_add_u32 s20, s20, 32\n" \
  "s_mov_b32 s21, 0x8000\n" \
  "v_mov_b32 v52, s22\n" \
  "v_mov_b32 v54, s20\n" \
  "s_waitcnt vmcnt(4)\n" \
  ".LCA_TKE_%=:\n" \
  "v_readlane_b32 s34, v7, 0\n" \
  "v_readlane_b32 s38, v7, 16\n" \
  "v_readlane_b32 s39, v7, 32\n" \
  "v_readlane_b32 s23, v7, 48\n" \
  "s_max_u32 s34, s34, s38\n" \
  "s_max_u32 s39, s39, s23\n" \
  "v_readlane_b32 s38, v7, 8\n" \
  "s_max_u32 s34, s34, s39\n" \
  "v_readlane_b32 s39, v7, 24\n" \
  "s_add_u32 s34, s34, %[cd]\n" \
  "v_readlane_b32 s23, v7, 40\n" \
  "s_add_u32 s38, s38, s39\n" \
  "v_readlane_b32 s39, v7, 56\n" \
  "s_add_u32 s38, s38, s23\n" \
  "v_mov_b32 v55, s34\n" \
  "s_add_u32 s38, s38, s39\n" \
  "s_cmp_ge_u32 s38, s20\n" \
  "s_cbranch_scc1 .LCA_TKO_%=\n" \
  "s_sub_u32 s21, s21, 1\n" \
  "s_cbranch_scc1 .LCA_TKO_%=\n" \
  "s_sleep 2\n" \
  "buffer_load_dword v7, v5, s[16:19], 0 offen sc1\n" \
  "s_waitcnt vmcnt(0)\n" \
  "s_branch .LCA_TKE_%=\n" \
  ".LCA_TKO_%=:\n" \
  "ds_write_b64 v52, v[54:55]\n" \
  "s_branch .LCA_WAIU_%=\n" \
  ".LCA_WAIS_%=:\n" \
  "s_cmp_lt_i32 s39, -1000\n" \
  "s_cbranch_scc1 .LCA_WAIE_%=\n" \
  "s_sleep 1\n" \
  ".LCA_WAIU_%=:\n" \
  "s_memrealtime s[38:39]\n" \
  "s_waitcnt lgkmcnt(0)\n" \
  "s_sub_i32 s39, s38, s34\n" \
  "s_cmp_lt_i32 s39, -7\n" \
  "s_cbranch_scc1 .LCA_WAIS_%=\n" \
  "s_cmp_lt_i32 s39, 0\n" \
  "s_cbranch_scc1 .LCA_WAIU_%=\n" \
  ".LCA_WAIE_%=:\n" \
  "buffer_load_dwordx4 v[24:27], v44, s[40:43], s60 offen sc1\n" \
  "buffer_load_dwordx4 v[28:31], v44, s[40:43], s60 offen offset:2048 sc1\n" \
  "v_mbcnt_lo_u32_b32 v48, -1, 0\n" \
  "v_mbcnt_hi_u32_b32 v48, -1, v48\n" \
  "v_and_b32 v49, 15, v48\n" \
  "v_lshl_add_u32 v8, v49, 1, s97\n" \
  "v_bfe_u32 v49, v48, 4, 1\n" \
  "v_mad_u32_u24 v8, v49, s92, v8\n" \
  "v_lshrrev_b32 v49, 5, v48\n" \
  "v_mad_u32_u24 v9, v49, s93, v8\n" \
  "v_mad_u32_u24 v8, v49, s94, v8\n" \
  "v_add_u32 v10, s99, v1\n" \
  "s_lshr_b32 s88, s99, 2\n" \
  "s_add_u32 s88, s88, s74\n" \
  "s_add_u32 s88, s88, 139264\n" \
  "v_lshl_add_u32 v7, v49, 10, s88\n" \
  "v_and_b32 v49, 31, v48\n" \
  "v_lshl_add_u32 v7, v49, 3, v7\n" \
  "v_mov_b64 v[12:13], 0\n" \
  "v_mov_b64 v[14:15], 0\n" \
  "v_mov_b64 v[16:17], 0\n" \
  "v_mov_b64 v[18:19], 0\n" \
  "v_mov_b64 v[20:21], 0\n" \
  "v_mov_b64 v[22:23], 0\n" \
  "s_add_u32 s88, s74, 157760\n" \
  "v_mov_b32 v59, s88\n" \
  "v_lshrrev_b32 v49, 3, v48\n" \
  "v_and_b32 v50, 7, v48\n" \
  "v_lshlrev_b32 v50, 4, v50\n" \
  "v_lshl_or_b32 v44, v49, 8, v50\n" \
  "v_and_b32 v50, 3, v48\n" \
  "v_lshl_add_u32 v40, v50, 2, s98\n" \
  "v_lshrrev_b32 v50, 4, v48\n" \
  "v_mad_u32_u24 v40, v50, s95, v40\n" \
  "v_and_b32 v49, 1, v49\n" \
  "v_lshl_add_u32 v40, v49, 6, v40\n" \
  "v_bfe_u32 v49, v48, 2, 1\n" \
  "v_lshl_add_u32 v40, v49, 7, v40\n" \
  "v_xor_b32 v41, 16, v40\n" \
  "v_xor_b32 v42, 32, v40\n" \
  "v_xor_b32 v43, 48, v40\n" \
  "s_add_i32 s80, s86, 0x32800000\n" \
  "s_add_i32 s81, s86, 0x33800000\n" \
  "s_add_i32 s88, s86, 0xb4000000\n" \
  "v_mov_b32 v46, s88\n" \
  "v_mov_b32 v47, 0\n" \
  "v_mov_b32 v11, 0\n" \
  "s_mov_b32 s89, s60\n" \
  "s_xor_b32 s60, s60, 0x80\n" \
  "s_bitcmp0_b32 s60, 7\n" \
  "s_cselect_b32 s88, 0x8000, 0\n" \
  "s_add_u32 s60, s60, s88\n" \
  "s_and_b32 s60, s60, 0xffff\n" \
  "s_add_u32 s58, s56, s60\n" \
  "s_addc_u32 s59, s57, 0\n" \
  "s_xor_b32 s38, s35, 0\n" \
  "s_add_u32 s26, s58, s38\n" \
  "s_addc_u32 s27, s59, 0\n" \
  "s_xor_b32 s38, s35, 1024\n" \
  "s_add_u32 s28, s58, s38\n" \
  "s_addc_u32 s29, s59, 0\n" \
  "s_xor_b32 s38, s35, 2048\n" \
  "s_add_u32 s30, s58, s38\n" \
  "s_addc_u32 s31, s59, 0\n" \
  "s_xor_b32 s38, s35, 3072\n" \
  "s_add_u32 s32, s58, s38\n" \
  "s_addc_u32 s33, s59, 0\n" \
  "s_add_u32 s66, s66, 0x1000\n" \
  "s_addc_u32 s67, s67, 0\n" \
  "s_sub_u32 s71, s71, 1\n" \
  "s_mov_b32 s87, 0x40000\n" \
  "s_branch .LCA_CHK_%=\n" \
  ".LCA_POLL_%=:\n" \
  "buffer_load_dwordx4 v[24:27], v44, s[40:43], s89 offen sc1\n" \
  "buffer_load_dwordx4 v[28:31], v44, s[40:43], s89 offen offset:2048 sc1\n" \
  ".LCA_CHK_%=:\n" \
  "s_sub_u32 s87, s87, 1\n" \
  "s_cbranch_scc1 .LCA_END_%=\n" \
  "s_waitcnt vmcnt(1)\n" \
  "v_bitop3_b32 v45, v24, v26, 32 bitop3:0x80\n" \
  "v_alignbit_b32 v32, v25, v24, 2\n" \
  "v_alignbit_b32 v34, v27, v26, 2\n" \
  "s_waitcnt vmcnt(0)\n" \
  "v_bitop3_b32 v45, v45, v28, v30 bitop3:0x80\n" \
  "v_cmp_eq_u32 vcc, 0, v45\n" \
  "v_cvt_f32_i32 v32, v32\n" \
  "v_cvt_f32_i32 v33, v25\n" \
  "s_cmp_eq_u64 vcc, 0\n" \
  "s_cbranch_scc0 .LCA_POLL_%=\n" \
  "v_cvt_f32_i32 v34, v34\n" \
  "v_cvt_f32_i32 v35, v27\n" \
  "v_pk_fma_f32 v[32:33], v[32:33], s[80:81], v[46:47]\n" \
  "v_alignbit_b32 v36, v29, v28, 2\n" \
  "v_pk_fma_f32 v[34:35], v[34:35], s[80:81], v[46:47]\n" \
  "v_alignbit_b32 v38, v31, v30, 2\n" \
  "v_cvt_pk_bf16_f32 v32, v32, v34\n" \
  "v_cvt_pk_bf16_f32 v33, v33, v35\n" \
  "v_cvt_f32_i32 v36, v36\n" \
  "ds_write_b32 v40, v32\n" \
  "v_cvt_f32_i32 v37, v29\n" \
  "ds_write_b32 v41, v33\n" \
  "v_cvt_f32_i32 v38, v38\n" \
  "v_cvt_f32_i32 v39, v31\n" \
  "v_pk_fma_f32 v[36:37], v[36:37], s[80:81], v[46:47]\n" \
  "v_pk_fma_f32 v[38:39], v[38:39], s[80:81], v[46:47]\n" \
  "v_dot2c_f32_bf16 v11, v32, v32\n" \
  "v_cvt_pk_bf16_f32 v36, v36, v38\n" \
  "v_cvt_pk_bf16_f32 v37, v37, v39\n" \
  "ds_write_b32 v42, v36\n" \
  "ds_write_b32 v43, v37\n" \
  "v_dot2c_f32_bf16 v11, v33, v33\n" \
  "v_dot2c_f32_bf16 v11, v36, v36\n" \
  "v_dot2c_f32_bf16 v11, v37, v37\n" \
  "s_nop 1\n" \
  "s_nop 0\n" \
  "v_cmpx_lt_f32 vcc, 2.0, v11\n" \
  "ds_max_u32 v59, v11\n" \
  "s_mov_b64 exec, -1\n" \
  "s_branch .LCA_TOP_%=\n" \
  ".LCA_LAST_%=:\n" \
  ".LCA_END_%=:\n" \
  "s_endpgm\n"
#define CHAIN_ASM_VO \
  "s_mov_b32 s92, 1152\n" \
  "s_mov_b32 s93, 576\n" \
  "s_mov_b32 s94, 2272\n" \
  "s_mov_b32 s95, 272\n" \
  "s_mov_b32 s96, 288\n" \
  "s_mov_b32 s91, 0x40000001\n" \
  "s_mov_b32 s84, 0x4b400000\n" \
  "s_mov_b32 s85, 0x4b400000\n" \
  "s_mov_b32 s78, 0x3e0293ee\n" \
  "s_mov_b32 s79, 0x3fb8aa3b\n" \
  "s_mov_b32 s76, 0xff00ff00\n" \
  "s_mov_b32 s77, 0xff00ff00\n" \
  "s_add_u32 s58, s56, s60\n" \
  "s_addc_u32 s59, s57, 0\n" \
  "s_xor_b32 s38, s35, 0\n" \
  "s_add_u32 s26, s58, s38\n" \
  "s_addc_u32 s27, s59, 0\n" \
  "s_xor_b32 s38, s35, 1024\n" \
  "s_add_u32 s28, s58, s38\n" \
  "s_addc_u32 s29, s59, 0\n" \
  "s_xor_b32 s38, s35, 2048\n" \
  "s_add_u32 s30, s58, s38\n" \
  "s_addc_u32 s31, s59, 0\n" \
  "s_xor_b32 s38, s35, 3072\n" \
  "s_add_u32 s32, s58, s38\n" \
  "s_addc_u32 s33, s59, 0\n" \
  "s_lshl_b32 s98, s73, 10\n" \
  "s_add_u32 s98, s98, s74\n" \
  "s_add_u32 s98, s98, 131072\n" \
  "s_lshl_b32 s97, s73, 5\n" \
  "s_add_u32 s97, s97, s74\n" \
  "s_add_u32 s97, s97, 160992\n" \
  "s_lshl_b32 s99, s73, 11\n" \
  "s_add_u32 s99, s99, s74\n" \
  "s_add_u32 s99, s99, 133120\n" \
  "v_mbcnt_lo_u32_b32 v48, -1, 0\n" \
  "v_mbcnt_hi_u32_b32 v48, -1, v48\n" \
  "s_sub_u32 s88, s73, 4\n" \
  "v_and_b32 v51, 15, v48\n" \
  "v_lshl_add_u32 v4, v51, 1, s97\n" \
  "v_bfe_u32 v49, v48, 4, 1\n" \
  "v_mad_u32_u24 v4, v49, s92, v4\n" \
  "v_lshrrev_b32 v50, 5, v48\n" \
  "v_mad_u32_u24 v4, v50, s93, v4\n" \
  "v_lshlrev_b32 v8, 3, v49\n" \
  "v_lshl_add_u32 v8, v51, 4, v8\n" \
  "v_lshl_add_u32 v8, v50, 10, v8\n" \
  "s_lshl_b32 s89, s88, 8\n" \
  "s_add_u32 s89, s89, s74\n" \
  "s_add_u32 s89, s89, 139264\n" \
  "v_and_b32 v49, 31, v48\n" \
  "v_lshl_add_u32 v5, v49, 3, s89\n" \
  "v_lshl_add_u32 v5, v50, 10, v5\n" \
  "v_lshrrev_b32 v49, 2, v51\n" \
  "v_and_b32 v50, 5, v51\n" \
  "v_and_or_b32 v49, v49, 2, v50\n" \
  "s_lshl_b32 s89, s88, 6\n" \
  "s_add_u32 s89, s89, s74\n" \
  "s_add_u32 s89, s89, 160992\n" \
  "v_and_b32 v50, 48, v48\n" \
  "v_add_u32 v6, s89, v50\n" \
  "v_mad_u32_u24 v6, v49, s96, v6\n" \
  "v_lshlrev_b32 v7, 3, v50\n" \
  "v_lshl_add_u32 v7, v49, 4, v7\n" \
  "v_and_b32 v49, 2, v51\n" \
  "v_lshl_add_u32 v7, v49, 29, v7\n" \
  "v_mov_b32 v9, 0\n" \
  "v_lshlrev_b32 v10, 4, v48\n" \
  "v_mov_b64 v[12:13], 0\n" \
  "v_mov_b64 v[14:15], 0\n" \
  "v_mov_b64 v[16:17], 0\n" \
  "v_mov_b64 v[18:19], 0\n" \
  "v_mov_b64 v[20:21], 0\n" \
  "v_mov_b64 v[22:23], 0\n" \
  "s_add_u32 s88, s74, 157760\n" \
  "v_mov_b32 v59, s88\n" \
  "v_mov_b32 v24, 0x44870000\n" \
  "v_mul_f32 v24, s72, v24\n" \
  "s_nop 0\n" \
  "v_readfirstlane_b32 s86, v24\n" \
  "s_lshr_b32 s86, s86, 23\n" \
  "s_addk_i32 s86, 0xff83\n" \
  "s_ashr_i32 s86, s86, 1\n" \
  "s_max_i32 s86, s86, -20\n" \
  "s_min_i32 s86, s86, 100\n" \
  "s_mov_b32 s82, s86\n" \
  "s_lshr_b32 s83, s72, 23\n" \
  "s_sub_i32 s83, s83, 242\n" \
  "s_lshl_b32 s88, s73, 10\n" \
  "v_add_u32 v24, s88, v1\n" \
  "ds_read_b128 v[32:35], v24 offset:0\n" \
  "s_waitcnt lgkmcnt(0)\n" \
  "v_mov_b32 v11, 0\n" \
  "v_dot2c_f32_bf16 v11, v32, v32\n" \
  "v_dot2c_f32_bf16 v11, v33, v33\n" \
  "v_dot2c_f32_bf16 v11, v34, v34\n" \
  "v_dot2c_f32_bf16 v11, v35, v35\n" \
  "s_nop 1\n" \
  "s_nop 0\n" \
  "v_cmpx_lt_f32 vcc, 2.0, v11\n" \
  "ds_max_u32 v59, v11\n" \
  "s_mov_b64 exec, -1\n" \
  ".LCV_TOP_%=:\n" \
  "s_waitcnt lgkmcnt(0)\n" \
  "s_barrier\n" \
  "s_setprio 3\n" \
  "buffer_load_dword v58, v9, s[44:47], s68 offen sc1\n" \
  "s_cmp_lg_u32 s73, 7\n" \
  "s_cbranch_scc1 .LCV_NOTK_%=\n" \
  "s_memrealtime s[38:39]\n" \
  "v_mov_b32 v57, 1\n" \
  "s_mov_b64 exec, 1\n" \
  "s_waitcnt lgkmcnt(0)\n" \
  "v_mov_b32 v56, s38\n" \
  "buffer_atomic_umax v56, off, s[16:19], s24\n" \
  "buffer_atomic_add v57, off, s[16:19], s24 offset:64\n" \
  "s_mov_b64 exec, -1\n" \
  ".LCV_NOTK_%=:\n" \
  "ds_read_b128 v[24:27], v1 offset:0\n" \
  "ds_read_b128 v[28:31], v1 offset:1024\n" \
  "ds_read_b128 v[32:35], v1 offset:2048\n" \
  "ds_read_b128 v[36:39], v1 offset:3072\n" \
  "ds_read_b128 v[40:43], v1 offset:4096\n" \
  "ds_read_b128 v[44:47], v1 offset:5120\n" \
  "ds_read_b128 v[48:51], v1 offset:6144\n" \
  "ds_read_b128 v[52:55], v1 offset:7168\n" \
  "s_waitcnt lgkmcnt(7)\n" \
  "v_smfmac_f32_16x16x64_bf16 v[12:15], v[24:27], v[64:71], v0\n" \
  "s_waitcnt lgkmcnt(6)\n" \
  "v_smfmac_f32_16x16x64_bf16 v[12:15], v[28:31], v[72:79], v0\n" \
  "s_waitcnt lgkmcnt(5)\n" \
  "v_smfmac_f32_16x16x64_bf16 v[12:15], v[32:35], v[80:87], v0\n" \
  "s_waitcnt lgkmcnt(4)\n" \
  "v_smfmac_f32_16x16x64_bf16 v[12:15], v[36:39], v[88:95], v0\n" \
  "s_waitcnt lgkmcnt(3)\n" \
  "v_smfmac_f32_16x16x64_bf16 v[12:15], v[40:43], v[96:103], v0\n" \
  "s_waitcnt lgkmcnt(2)\n" \
  "v_smfmac_f32_16x16x64_bf16 v[12:15], v[44:47], v[104:111], v0\n" \
  "s_waitcnt lgkmcnt(1)\n" \
  "v_smfmac_f32_16x16x64_bf16 v[12:15], v[48:51], v[112:119], v0\n" \
  "s_waitcnt lgkmcnt(0)\n" \
  "v_smfmac_f32_16x16x64_bf16 v[12:15], v[52:55], v[120:127], v0\n" \
  "ds_read_b32 v11, v1 offset:26688\n" \
  "s_nop 6\n" \
  "v_permlane32_swap_b32 v12, v14\n" \
  "v_permlane32_swap_b32 v13, v15\n" \
  "v_pk_add_f32 v[12:13], v[12:13], v[14:15]\n" \
  "s_nop 0\n" \
  "v_cvt_pk_bf16_f32 v12, v12, v13\n" \
  "ds_write_b16 v4, v12\n" \
  "ds_write_b16_d16_hi v4, v12 offset:288\n" \
  "s_setprio 0\n" \
  "v_mov_b64 v[12:13], 0\n" \
  "v_mov_b64 v[14:15], 0\n" \
  "s_waitcnt lgkmcnt(2)\n" \
  "v_readfirstlane_b32 s86, v11\n" \
  "s_lshr_b32 s86, s86, 23\n" \
  "s_add_i32 s86, s86, s83\n" \
  "s_ashr_i32 s86, s86, 1\n" \
  "s_max_i32 s86, s86, s82\n" \
  "s_min_i32 s86, s86, 100\n" \
  "s_lshl_b32 s86, s86, 23\n" \
  "s_sub_i32 s84, 0x4a800000, s86\n" \
  "s_cmp_eq_u32 s71, 0\n" \
  "s_waitcnt lgkmcnt(0)\n" \
  "s_barrier\n" \
  "ds_read_b128 v[60:63], v6 offset:0\n" \
  "v_smfmac_f32_16x16x64_bf16 v[16:19], v[24:27], v[128:135], v0\n" \
  "v_smfmac_f32_16x16x64_bf16 v[20:23], v[24:27], v[192:199], v0\n" \
  "v_smfmac_f32_16x16x64_bf16 v[16:19], v[28:31], v[136:143], v0\n" \
  "v_smfmac_f32_16x16x64_bf16 v[20:23], v[28:31], v[200:207], v0\n" \
  "v_smfmac_f32_16x16x64_bf16 v[16:19], v[32:35], v[144:151], v0\n" \
  "v_smfmac_f32_16x16x64_bf16 v[20:23], v[32:35], v[208:215], v0\n" \
  "v_smfmac_f32_16x16x64_bf16 v[16:19], v[36:39], v[152:159], v0\n" \
  "v_smfmac_f32_16x16x64_bf16 v[20:23], v[36:39], v[216:223], v0\n" \
  "v_smfmac_f32_16x16x64_bf16 v[16:19], v[40:43], v[160:167], v0\n" \
  "v_smfmac_f32_16x16x64_bf16 v[20:23], v[40:43], v[224:231], v0\n" \
  "v_smfmac_f32_16x16x64_bf16 v[16:19], v[44:47], v[168:175], v0\n" \
  "v_smfmac_f32_16x16x64_bf16 v[20:23], v[44:47], v[232:239], v0\n" \
  "v_smfmac_f32_16x16x64_bf16 v[16:19], v[48:51], v[176:183], v0\n" \
  "v_smfmac_f32_16x16x64_bf16 v[20:23], v[48:51], v[240:247], v0\n" \
  "v_smfmac_f32_16x16x64_bf16 v[16:19], v[52:55], v[184:191], v0\n" \
  "v_smfmac_f32_16x16x64_bf16 v[20:23], v[52:55], v[248:255], v0\n" \
  "ds_read_b128 v[24:27], v3 offset:0\n" \
  "ds_read_b128 v[28:31], v3 offset:1024\n" \
  "ds_read_b128 v[32:35], v3 offset:2048\n" \
  "ds_read_b128 v[36:39], v3 offset:3072\n" \
  "ds_read_b128 v[40:43], v3 offset:8192\n" \
  "ds_read_b128 v[44:47], v3 offset:9216\n" \
  "s_nop 0\n" \
  "v_permlane32_swap_b32 v16, v18\n" \
  "v_permlane32_swap_b32 v17, v19\n" \
  "v_permlane32_swap_b32 v20, v22\n" \
  "v_permlane32_swap_b32 v21, v23\n" \
  "v_pk_add_f32 v[16:17], v[16:17], v[18:19]\n" \
  "v_pk_add_f32 v[20:21], v[20:21], v[22:23]\n" \
  "v_cvt_pk_bf16_f32 v16, v16, v17\n" \
  "v_cvt_pk_bf16_f32 v17, v20, v21\n" \
  "ds_write_b64 v5, v[16:17]\n" \
  "s_cbranch_scc1 .LCV_LAST_%=\n" \
  "s_cmp_eq_u32 s73, 7\n" \
  "s_waitcnt vmcnt(2)\n" \
  "s_cbranch_scc1 .LCV_TKW_%=\n" \
  "s_waitcnt vmcnt(0)\n" \
  ".LCV_TKW_%=:\n" \
  "v_cmp_eq_u32 vcc, 0, v58\n" \
  "s_cmp_eq_u64 vcc, 0\n" \
  "s_cselect_b32 s90, 1, 0\n" \
  "s_waitcnt lgkmcnt(0)\n" \
  "s_barrier\n" \
  "s_cbranch_scc0 .LCV_NOTOK_%=\n" \
  "s_mov_b32 m0, s99\n" \
  "s_add_u32 s88, s99, 8192\n" \
  "global_load_lds_dwordx4 v10, s[62:63] sc1\n" \
  "global_load_lds_dwordx4 v10, s[62:63] offset:1024 sc1\n" \
  "s_mov_b32 m0, s88\n" \
  "s_nop 0\n" \
  "global_load_lds_dwordx4 v10, s[64:65] sc1\n" \
  "global_load_lds_dwordx4 v10, s[64:65] offset:1024 sc1\n" \
  ".LCV_NOTOK_%=:\n" \
  "ds_write_b32 v59, v12\n" \
  "ds_read_b128 v[48:51], v3 offset:10240\n" \
  "ds_read_b128 v[52:55], v3 offset:11264\n" \
  "v_permlane32_swap_b32 v16, v17\n" \
  "v_max_u32 v60, 1, v60\n" \
  "v_max_u32 v61, 1, v61\n" \
  "v_max_u32 v16, 1, v16\n" \
  "v_max_u32 v17, 1, v17\n" \
  "buffer_store_dwordx2 v[16:17], v8, s[52:55], s70 offen sc1\n" \
  "v_max_u32 v62, 1, v62\n" \
  "v_max_u32 v63, 1, v63\n" \
  "buffer_store_dwordx4 v[60:63], v7, s[48:51], s69 offen sc1\n" \
  "v_mov_b64 v[18:19], 0\n" \
  "v_mov_b64 v[20:21], 0\n" \
  "v_mov_b64 v[22:23], 0\n" \
  "v_mov_b64 v[16:17], 0\n" \
  "ds_read_b128 v[56:59], v3 offset:4096\n" \
  "ds_read_b128 v[60:63], v3 offset:5120\n" \
  "s_waitcnt lgkmcnt(0)\n" \
  "s_barrier\n" \
  "ds_read_b128 v[4:7], v1 offset:8192\n" \
  "ds_read_b128 v[8:11], v1 offset:9216\n" \
  "s_waitcnt lgkmcnt(1)\n" \
  "v_smfmac_f32_16x16x64_bf16 v[12:15], v[4:7], v[24:31], v0\n" \
  "ds_read_b128 v[24:27], v3 offset:6144\n" \
  "ds_read_b128 v[28:31], v3 offset:7168\n" \
  "v_smfmac_f32_16x16x64_bf16 v[16:19], v[4:7], v[32:39], v0\n" \
  "ds_read_b128 v[32:35], v3 offset:12288\n" \
  "ds_read_b128 v[36:39], v3 offset:13312\n" \
  "s_waitcnt lgkmcnt(4)\n" \
  "v_smfmac_f32_16x16x64_bf16 v[12:15], v[8:11], v[40:47], v0\n" \
  "v_smfmac_f32_16x16x64_bf16 v[16:19], v[8:11], v[48:55], v0\n" \
  "v_smfmac_f32_16x16x64_bf16 v[20:23], v[4:7], v[56:63], v0\n" \
  "ds_read_b128 v[56:59], v3 offset:14336\n" \
  "ds_read_b128 v[60:63], v3 offset:15360\n" \
  "v_mov_b64 v[40:41], 0\n" \
  "v_mov_b64 v[42:43], 0\n" \
  "s_waitcnt lgkmcnt(2)\n" \
  "v_smfmac_f32_16x16x64_bf16 v[20:23], v[8:11], v[32:39], v0\n" \
  "s_nop 0\n" \
  "v_permlane32_swap_b32 v12, v17\n" \
  "v_permlane32_swap_b32 v13, v16\n" \
  "v_permlane32_swap_b32 v14, v19\n" \
  "v_permlane32_swap_b32 v15, v18\n" \
  "v_smfmac_f32_16x16x64_bf16 v[40:43], v[4:7], v[24:31], v0\n" \
  "v_pk_add_f32 v[12:13], v[12:13], v[16:17] op_sel:[0,1] op_sel_hi:[1,0]\n" \
  "v_pk_add_f32 v[14:15], v[14:15], v[18:19] op_sel:[0,1] op_sel_hi:[1,0]\n" \
  "v_pk_fma_f32 v[12:13], v[12:13], s[84:85], s[84:85] op_sel:[0,0,1] op_sel_hi:[1,0,1]\n" \
  "v_pk_fma_f32 v[14:15], v[14:15], s[84:85], s[84:85] op_sel:[0,0,1] op_sel_hi:[1,0,1]\n" \
  "s_waitcnt lgkmcnt(0)\n" \
  "v_smfmac_f32_16x16x64_bf16 v[40:43], v[8:11], v[56:63], v0\n" \
  "v_lshl_add_u32 v12, v12, 6, s91\n" \
  "v_lshl_add_u32 v13, v13, 2, s75\n" \
  "global_atomic_add_x2 v2, v[12:13], s[26:27]\n" \
  "v_lshl_add_u32 v14, v14, 6, s91\n" \
  "v_lshl_add_u32 v15, v15, 2, s75\n" \
  "global_atomic_add_x2 v2, v[14:15], s[30:31]\n" \
  "s_nop 1\n" \
  "v_permlane32_swap_b32 v20, v41\n" \
  "v_permlane32_swap_b32 v21, v40\n" \
  "v_permlane32_swap_b32 v22, v43\n" \
  "v_permlane32_swap_b32 v23, v42\n" \
  "v_pk_add_f32 v[20:21], v[20:21], v[40:41] op_sel:[0,1] op_sel_hi:[1,0]\n" \
  "v_pk_add_f32 v[22:23], v[22:23], v[42:43] op_sel:[0,1] op_sel_hi:[1,0]\n" \
  "v_pk_fma_f32 v[20:21], v[20:21], s[84:85], s[84:85] op_sel:[0,0,1] op_sel_hi:[1,0,1]\n" \
  "v_pk_fma_f32 v[22:23], v[22:23], s[84:85], s[84:85] op_sel:[0,0,1] op_sel_hi:[1,0,1]\n" \
  "v_lshl_add_u32 v20, v20, 6, s91\n" \
  "v_lshl_add_u32 v21, v21, 2, s75\n" \
  "global_atomic_add_x2 v2, v[20:21], s[32:33]\n" \
  "v_lshl_add_u32 v22, v22, 6, s91\n" \
  "v_lshl_add_u32 v23, v23, 2, s75\n" \
  "global_atomic_add_x2 v2, v[22:23], s[28:29]\n" \
  "v_mbcnt_lo_u32_b32 v48, -1, 0\n" \
  "v_mbcnt_hi_u32_b32 v48, -1, v48\n" \
  "s_add_u32 s20, s20, 32\n" \
  "s_cmp_eq_u32 s90, 1\n" \
  "s_cbranch_scc1 .LCV_HAVE_%=\n" \
  "s_mov_b32 s87, 0x40000\n" \
  "v_mov_b32 v9, 0\n" \
  "v_lshlrev_b32 v10, 4, v48\n" \
  ".LCV_LATE_%=:\n" \
  "buffer_load_dword v58, v9, s[44:47], s68 offen sc1\n" \
  "s_sub_u32 s87, s87, 1\n" \
  "s_cbranch_scc1 .LCV_END_%=\n" \
  "s_waitcnt vmcnt(0)\n" \
  "v_cmp_eq_u32 vcc, 0, v58\n" \
  "s_cmp_eq_u64 vcc, 0\n" \
  "s_cbranch_scc1 .LCV_GOT_%=\n" \
  "s_sleep 12\n" \
  "s_branch .LCV_LATE_%=\n" \
  ".LCV_GOT_%=:\n" \
  "s_mov_b32 m0, s99\n" \
  "s_add_u32 s88, s99, 8192\n" \
  "global_load_lds_dwordx4 v10, s[62:63] sc1\n" \
  "global_load_lds_dwordx4 v10, s[62:63] offset:1024 sc1\n" \
  "s_mov_b32 m0, s88\n" \
  "s_nop 0\n" \
  "global_load_lds_dwordx4 v10, s[64:65] sc1\n" \
  "global_load_lds_dwordx4 v10, s[64:65] offset:1024 sc1\n" \
  ".LCV_HAVE_%=:\n" \
  "v_lshrrev_b32 v49, 3, v48\n" \
  "v_and_b32 v50, 7, v48\n" \
  "v_lshlrev_b32 v50, 4, v50\n" \
  "v_lshl_or_b32 v44, v49, 8, v50\n" \
  "s_mov_b32 s21, 0x8000\n" \
  "v_mov_b32 v52, s22\n" \
  "v_mov_b32 v54, s20\n" \
  "s_branch .LCV_TKF_%=\n" \
  ".LCV_TKE_%=:\n" \
  "s_sleep 1\n" \
  ".LCV_TKF_%=:\n" \
  "ds_read_b64 v[54:55], v52\n" \
  "s_sub_u32 s21, s21, 1\n" \
  "s_cbranch_scc1 .LCV_TKO_%=\n" \
  "s_waitcnt lgkmcnt(0)\n" \
  "v_readfirstlane_b32 s38, v54\n" \
  "v_readfirstlane_b32 s34, v55\n" \
  "s_cmp_eq_u32 s38, s20\n" \
  "s_cbranch_scc0 .LCV_TKE_%=\n" \
  ".LCV_TKO_%=:\n" \
  "s_branch .LCV_WAIU_%=\n" \
  ".LCV_WAIS_%=:\n" \
  "s_cmp_lt_i32 s39, -1000\n" \
  "s_cbranch_scc1 .LCV_WAIE_%=\n" \
  "s_sleep 1\n" \
  ".LCV_WAIU_%=:\n" \
  "s_memrealtime s[38:39]\n" \
  "s_waitcnt lgkmcnt(0)\n" \
  "s_sub_i32 s39, s38, s34\n" \
  "s_cmp_lt_i32 s39, -7\n" \
  "s_cbranch_scc1 .LCV_WAIS_%=\n" \
  "s_cmp_lt_i32 s39, 0\n" \
  "s_cbranch_scc1 .LCV_WAIU_%=\n" \
  ".LCV_WAIE_%=:\n" \
  "buffer_load_dwordx4 v[24:27], v44, s[40:43], s60 offen sc1\n" \
  "buffer_load_dwordx4 v[28:31], v44, s[40:43], s60 offen offset:2048 sc1\n" \
  "v_mbcnt_lo_u32_b32 v48, -1, 0\n" \
  "v_mbcnt_hi_u32_b32 v48, -1, v48\n" \
  "s_sub_u32 s88, s73, 4\n" \
  "v_and_b32 v51, 15, v48\n" \
  "v_lshl_add_u32 v4, v51, 1, s97\n" \
  "v_bfe_u32 v49, v48, 4, 1\n" \
  "v_mad_u32_u24 v4, v49, s92, v4\n" \
  "v_lshrrev_b32 v50, 5, v48\n" \
  "v_mad_u32_u24 v4, v50, s93, v4\n" \
  "v_lshlrev_b32 v8, 3, v49\n" \
  "v_lshl_add_u32 v8, v51, 4, v8\n" \
  "v_lshl_add_u32 v8, v50, 10, v8\n" \
  "s_lshl_b32 s89, s88, 8\n" \
  "s_add_u32 s89, s89, s74\n" \
  "s_add_u32 s89, s89, 139264\n" \
  "v_and_b32 v49, 31, v48\n" \
  "v_lshl_add_u32 v5, v49, 3, s89\n" \
  "v_lshl_add_u32 v5, v50, 10, v5\n" \
  "v_lshrrev_b32 v49, 2, v51\n" \
  "v_and_b32 v50, 5, v51\n" \
  "v_and_or_b32 v49, v49, 2, v50\n" \
  "s_lshl_b32 s89, s88, 6\n" \
  "s_add_u32 s89, s89, s74\n" \
  "s_add_u32 s89, s89, 160992\n" \
  "v_and_b32 v50, 48, v48\n" \
  "v_add_u32 v6, s89, v50\n" \
  "v_mad_u32_u24 v6, v49, s96, v6\n" \
  "v_lshlrev_b32 v7, 3, v50\n" \
  "v_lshl_add_u32 v7, v49, 4, v7\n" \
  "v_and_b32 v49, 2, v51\n" \
  "v_lshl_add_u32 v7, v49, 29, v7\n" \
  "v_mov_b32 v9, 0\n" \
  "v_lshlrev_b32 v10, 4, v48\n" \
  "v_mov_b64 v[12:13], 0\n" \
  "v_mov_b64 v[14:15], 0\n" \
  "v_mov_b64 v[16:17], 0\n" \
  "v_mov_b64 v[18:19], 0\n" \
  "v_mov_b64 v[20:21], 0\n" \
  "v_mov_b64 v[22:23], 0\n" \
  "s_add_u32 s88, s74, 157760\n" \
  "v_mov_b32 v59, s88\n" \
  "v_lshrrev_b32 v49, 3, v48\n" \
  "v_and_b32 v50, 7, v48\n" \
  "v_lshlrev_b32 v50, 4, v50\n" \
  "v_lshl_or_b32 v44, v49, 8, v50\n" \
  "v_and_b32 v50, 3, v48\n" \
  "v_lshl_add_u32 v40, v50, 2, s98\n" \
  "v_lshrrev_b32 v50, 4, v48\n" \
  "v_mad_u32_u24 v40, v50, s95, v40\n" \
  "v_and_b32 v49, 1, v49\n" \
  "v_lshl_add_u32 v40, v49, 6, v40\n" \
  "v_bfe_u32 v49, v48, 2, 1\n" \
  "v_lshl_add_u32 v40, v49, 7, v40\n" \
  "v_xor_b32 v41, 16, v40\n" \
  "v_xor_b32 v42, 32, v40\n" \
  "v_xor_b32 v43, 48, v40\n" \
  "s_add_u32 s62, s62, 0x80000\n" \
  "s_addc_u32 s63, s63, 0\n" \
  "s_add_u32 s64, s64, 0x40000\n" \
  "s_addc_u32 s65, s65, 0\n" \
  "s_add_u32 s68, s68, 0x1000\n" \
  "s_add_u32 s69, s69, 0x10000\n" \
  "s_add_u32 s70, s70, 0x10000\n" \
  "s_add_i32 s80, s86, 0x32800000\n" \
  "s_add_i32 s81, s86, 0x33800000\n" \
  "s_add_i32 s88, s86, 0xb4000000\n" \
  "v_mov_b32 v46, s88\n" \
  "v_mov_b32 v47, 0\n" \
  "v_mov_b32 v11, 0\n" \
  "s_mov_b32 s89, s60\n" \
  "s_xor_b32 s60, s60, 0x80\n" \
  "s_bitcmp0_b32 s60, 7\n" \
  "s_cselect_b32 s88, 0x8000, 0\n" \
  "s_add_u32 s60, s60, s88\n" \
  "s_and_b32 s60, s60, 0xffff\n" \
  "s_add_u32 s58, s56, s60\n" \
  "s_addc_u32 s59, s57, 0\n" \
  "s_xor_b32 s38, s35, 0\n" \
  "s_add_u32 s26, s58, s38\n" \
  "s_addc_u32 s27, s59, 0\n" \
  "s_xor_b32 s38, s35, 1024\n" \
  "s_add_u32 s28, s58, s38\n" \
  "s_addc_u32 s29, s59, 0\n" \
  "s_xor_b32 s38, s35, 2048\n" \
  "s_add_u32 s30, s58, s38\n" \
  "s_addc_u32 s31, s59, 0\n" \
  "s_xor_b32 s38, s35, 3072\n" \
  "s_add_u32 s32, s58, s38\n" \
  "s_addc_u32 s33, s59, 0\n" \
  "s_add_u32 s66, s66, 0x1000\n" \
  "s_addc_u32 s67, s67, 0\n" \
  "s_sub_u32 s71, s71, 1\n" \
  "s_mov_b32 s87, 0x40000\n" \
  "s_branch .LCV_CHK_%=\n" \
  ".LCV_POLL_%=:\n" \
  "buffer_load_dwordx4 v[24:27], v44, s[40:43], s89 offen sc1\n" \
  "buffer_load_dwordx4 v[28:31], v44, s[40:43], s89 offen offset:2048 sc1\n" \
  ".LCV_CHK_%=:\n" \
  "s_sub_u32 s87, s87, 1\n" \
  "s_cbranch_scc1 .LCV_END_%=\n" \
  "s_waitcnt vmcnt(1)\n" \
  "v_bitop3_b32 v45, v24, v26, 32 bitop3:0x80\n" \
  "v_alignbit_b32 v32, v25, v24, 2\n" \
  "v_alignbit_b32 v34, v27, v26, 2\n" \
  "s_waitcnt vmcnt(0)\n" \
  "v_bitop3_b32 v45, v45, v28, v30 bitop3:0x80\n" \
  "v_cmp_eq_u32 vcc, 0, v45\n" \
  "v_cvt_f32_i32 v32, v32\n" \
  "v_cvt_f32_i32 v33, v25\n" \
  "s_cmp_eq_u64 vcc, 0\n" \
  "s_cbranch_scc0 .LCV_POLL_%=\n" \
  "v_cvt_f32_i32 v34, v34\n" \
  "v_cvt_f32_i32 v35, v27\n" \
  "v_pk_fma_f32 v[32:33], v[32:33], s[80:81], v[46:47]\n" \
  "v_alignbit_b32 v36, v29, v28, 2\n" \
  "v_pk_fma_f32 v[34:35], v[34:35], s[80:81], v[46:47]\n" \
  "v_alignbit_b32 v38, v31, v30, 2\n" \
  "v_cvt_pk_bf16_f32 v32, v32, v34\n" \
  "v_cvt_pk_bf16_f32 v33, v33, v35\n" \
  "v_cvt_f32_i32 v36, v36\n" \
  "ds_write_b32 v40, v32\n" \
  "v_cvt_f32_i32 v37, v29\n" \
  "ds_write_b32 v41, v33\n" \
  "v_cvt_f32_i32 v38, v38\n" \
  "v_cvt_f32_i32 v39, v31\n" \
  "v_pk_fma_f32 v[36:37], v[36:37], s[80:81], v[46:47]\n" \
  "v_pk_fma_f32 v[38:39], v[38:39], s[80:81], v[46:47]\n" \
  "v_dot2c_f32_bf16 v11, v32, v32\n" \
  "v_cvt_pk_bf16_f32 v36, v36, v38\n" \
  "v_cvt_pk_bf16_f32 v37, v37, v39\n" \
  "ds_write_b32 v42, v36\n" \
  "ds_write_b32 v43, v37\n" \
  "v_dot2c_f32_bf16 v11, v33, v33\n" \
  "v_dot2c_f32_bf16 v11, v36, v36\n" \
  "v_dot2c_f32_bf16 v11, v37, v37\n" \
  "s_nop 1\n" \
  "s_nop 0\n" \
  "v_cmpx_lt_f32 vcc, 2.0, v11\n" \
  "ds_max_u32 v59, v11\n" \
  "s_mov_b64 exec, -1\n" \
  "s_branch .LCV_TOP_%=\n" \
  ".LCV_LAST_%=:\n" \
  "s_waitcnt lgkmcnt(0)\n" \
  "s_nop 0\n" \
  "v_permlane32_swap_b32 v16, v17\n" \
  "v_max_u32 v60, 1, v60\n" \
  "v_max_u32 v61, 1, v61\n" \
  "v_max_u32 v16, 1, v16\n" \
  "v_max_u32 v17, 1, v17\n" \
  "buffer_store_dwordx2 v[16:17], v8, s[52:55], s70 offen sc1\n" \
  "v_max_u32 v62, 1, v62\n" \
  "v_max_u32 v63, 1, v63\n" \
  "buffer_store_dwordx4 v[60:63], v7, s[48:51], s69 offen sc1\n" \
  "s_waitcnt vmcnt(0)\n" \
  ".LCV_END_%=:\n" \
  "s_endpgm\n"
__device__ __forceinline__ v4u mk_srd(const void* p, const unsigned bytes) {
  const unsigned long long a = (unsigned long long)p;
  return v4u{(unsigned)a, (unsigned)(a >> 32) & 0xffffu, bytes, 0x00020000u};
}
template <bool ATT>
__device__ __forceinline__ void role_chain_t(const MegaArgs& A, char* smem, const int L, const int h, const int rep) {
  __builtin_amdgcn_s_waitcnt(0x0F70);
  u16* swo = reinterpret_cast<u16*>(smem);
  const int w = __builtin_amdgcn_readfirstlane(threadIdx.x >> 6);
  const int wc = (w + ROT) & 7, psw = PSW ? (PSWH) : 0;
  char* ws = A.ws;
  unsigned* FL = (unsigned*)(ws + OFF_FL);
  unsigned* TMO = FL + FL_TMO;
#define LANE_IDS()                         \
  const int l = lane_id();                 \
  const int c = l & 15, g = l >> 4;        \
  (void)c; (void)g;
  float* sgm = reinterpret_cast<float*>(smem + C_X);
  float* sgf = sgm + 8;
  unsigned* FQ = FL + FL_FQ + L * 64 * 32 * 32;
  v8u w0[8], w1[8], w2[8];
  {
    LANE_IDS();
    const unsigned sb = lds_base(smem);
    const int q = (l >> 2) & 3, p = l & 3;
    const int f = (q & 1) | ((g & 1) << 1) | ((q >> 1) << 2);
    const unsigned ra = sb + (16 * (g >> 1) + 4 * (g & 1) + (q & 1) + 8 * (q >> 1)) * 256 + (w ^ f) * 32 + p * 8;
#define W_READ(W, hf, X)                                                       \
  {                                                                           \
    _Pragma("unroll") for (int t = 0; t < 4; ++t) {                           \
      const unsigned a_ = (ra ^ (X)) + (hf) * 8192 + t * 32768;               \
      const v2u r0 = trd(a_), r1 = trd(a_ + 512), r2 = trd(a_ + 16384), r3 = trd(a_ + 16384 + 512); \
      W[4 * (hf) + t] = v8u{r0.x, r0.y, r1.x, r1.y, r2.x, r2.y, r3.x, r3.y};  \
    }                                                                         \
    _Pragma("unroll") for (int t = 0; t < 4; ++t) asm volatile("" : "+v"(W[4 * (hf) + t])); \
  }
#define W_STAGE(M, hf, VM, NEXT)                                               \
  {                                                                           \
    WAIT_VM(VM);                                                              \
    RAW_BAR();                                                                \
    if ((M) == 0 && ATT) { W_READ(w0, hf, 0); W_READ(w2, hf, 128); }          \
    if ((M) == 1 && ATT) { W_READ(w1, hf, 0); }                               \
    if ((M) == 1 && !ATT) { W_READ(w0, hf, 0); }                              \
    if ((M) == 2 && !ATT) { W_READ(w1, hf, 128); W_READ(w2, hf, 0); }         \
    RAW_BAR();                                                                \
    NEXT;                                                                     \
  }
#define WO_ISSUE(hf)                                                          \
  {                                                                           \
    const char* base_ = (const char*)A.Wo + (size_t)(h * DH + 64 * (hf) + 8 * w) * (DM * 2); \
    _Pragma("unroll") for (int i = 0; i < 8; ++i) {                           \
      const int fd = (i & 1) | (((i >> 2) & 1) << 1) | ((w & 1) << 2);        \
      glds16(base_, (unsigned)(i * 1024 + ((l ^ (fd << 1)) << 4)), sb + w * 16384 + (hf) * 8192 + i * 1024); \
    }                                                                         \
  }
    wqkv_issue<true>(A.Wv, 0, h, w, l, sb);
    wqkv_issue<true>(A.Wv, 1, h, w, l, sb);
    v4u wr[16];
#define WR_LD(i, R) \
  asm volatile("global_load_dwordx4 %0, %1, %2" : "={" R "}"(wr[i]) : "v"(l * 16), "s"((const char*)A.Wo + (size_t)(h * DH + 16 * w + i) * (DM * 2)) : "memory")
    WR_LD(0, "v[64:67]");    WR_LD(1, "v[68:71]");    WR_LD(2, "v[72:75]");    WR_LD(3, "v[76:79]");
    WR_LD(4, "v[80:83]");    WR_LD(5, "v[84:87]");    WR_LD(6, "v[88:91]");    WR_LD(7, "v[92:95]");
    WR_LD(8, "v[96:99]");    WR_LD(9, "v[100:103]");    WR_LD(10, "v[104:107]");    WR_LD(11, "v[108:111]");
    WR_LD(12, "v[112:115]");    WR_LD(13, "v[116:119]");    WR_LD(14, "v[120:123]");    WR_LD(15, "v[124:127]");
#undef WR_LD
    W_STAGE(2, 0, 24, wqkv_issue<true>(A.Wk, 0, h, w, l, sb));
    SST(4);
    W_STAGE(2, 1, 24, wqkv_issue<true>(A.Wk, 1, h, w, l, sb));
    SST(5);
#define DOT2(x, acc) dot2sq(x, acc)
    float* cs = reinterpret_cast<float*>(smem + C_SM);
    {
      float f2 = 0.f;
      if (!ATT) {
#pragma unroll
        for (int t = 0; t < 8; ++t)
#pragma unroll
          for (int e = 0; e < 8; ++e) f2 = DOT2(w2[t][e], DOT2(w1[t][e], f2));
      }
      f2 = wave_sum_dpp(f2);
      if (l == 0) { sgf[w] = f2; sgm[16] = 0.f; sgm[32 + w] = 0.f; }
      WAIT_VM(16);
      f32x4 ca = {}, cb = {};
#pragma unroll
      for (int i = 0; i < 16; ++i) {
        asm volatile("" : "+v"(wr[i]), "+v"(ca), "+v"(cb));
        const f32x4 x = {lo16(wr[i].x), hi16(wr[i].x), lo16(wr[i].y), hi16(wr[i].y)};
        const f32x4 y = {lo16(wr[i].z), hi16(wr[i].z), lo16(wr[i].w), hi16(wr[i].w)};
        ca += x * x;
        cb += y * y;
      }
      *reinterpret_cast<f32x4*>(cs + w * 512 + l * 8) = ca;
      *reinterpret_cast<f32x4*>(cs + w * 512 + l * 8 + 4) = cb;
    }
    v4u zt;
    v2u ze;
#define Z_LOAD()                                                                                                        \
  asm volatile("global_load_dwordx4 %0, %2, %3 sc1\n\tglobal_load_dwordx2 %1, %4, %5 sc1"                              \
               : "=&v"(zt), "=&v"(ze)                                                                                   \
               : "v"(l * 16), "s"(ws + OFF_TAG + (blockIdx.x & 7) * 1024), "v"(0), "s"(ws + OFF_TAG + 8192)              \
               : "memory")
    W_STAGE(1, 0, 8, Z_LOAD(); wqkv_issue<true>(A.Wq, 0, h, w, l, sb));
#undef Z_LOAD
    {
      float m2 = 0.f;
#pragma unroll
      for (int i = 0; i < 8; ++i) m2 += cs[i * 512 + w * 64 + l];
      m2 = wave_max_dpp(m2);
      if (l == 0) sgm[w] = m2;
    }
    W_STAGE(1, 1, 8, wqkv_issue<true>(A.Wq, 1, h, w, l, sb));
    asm volatile("" : "+v"(zt), "+v"(ze));
    if (w == 0) {
      if (!zero_seen(zt, ze)) zero_wait(ws);
      float m2 = 0.f, f2 = 0.f;
#pragma unroll
      for (int i = 0; i < 8; ++i) { m2 = fmaxf(m2, sgm[i]); f2 += sgf[i]; }
      __builtin_amdgcn_raw_buffer_store_b32(__float_as_uint(sqrtf(m2 * f2) * 1.001f) | 1u, MKRS(FL + FL_G + L * 4 * 32, 4 * 128), l * 128 + h * 4, 0, AUX_SC1);
    }
    SST(6);
    W_STAGE(0, 0, 8, WO_ISSUE(0));
    W_STAGE(0, 1, 8, WO_ISSUE(1));
    SST(7);
#undef W_STAGE
#undef W_READ
#undef WO_ISSUE
#undef DOT2
    const unsigned rb = sb + (2 * (g >> 1) + (q >> 1)) * 16384 + (4 * (g & 1) + (q & 1)) * 1024 + p * 8;
#pragma unroll
    for (int hf = 0; hf < 2; ++hf) {
      if (hf == 0) WAIT_VM(8); else WAIT_VM(0);
      RAW_BAR();
      v4u fr[8];
#pragma unroll
      for (int i = 0; i < 8; ++i) {
        const unsigned a_ = rb + hf * 8192 + (i & 1) * 65536 + ((4 * wc + ((i >> 1) ^ psw)) ^ f) * 32;
        const v2u r0 = trd(a_), r1 = trd(a_ + 2048);
        fr[i] = v4u{r0.x, r0.y, r1.x, r1.y};
      }
#pragma unroll
      for (int i = 0; i < 8; ++i) asm volatile("" : "+v"(fr[i]));
      RAW_BAR();
#pragma unroll
      for (int i = 0; i < 8; ++i) *reinterpret_cast<v4u*>(swo + w * 8192 + hf * 4096 + i * 512 + l * 8) = fr[i];
    }
  }
  WSTAMP2();
  v4u m0r;
  {
    LANE_IDS();
    m0r = *reinterpret_cast<const v4u*>(A.mem0 + (c & 7) * DM + 64 * w + 16 * g + 8 * (c >> 3));
  }
  const unsigned sb = lds_base(smem);
  const char* gK = ws + OFF_QK + L * SZ_QK + ((size_t)h * 16 + 2 * w) * 1024;
  const char* gV = ws + OFF_VF + L * SZ_O + ((size_t)h * 8 + 2 * w - 8) * 1024;
  float gw2;
  {
    const unsigned* gp = FL + FL_G + (L * 4 + REP) * 32;
    unsigned gv;
    for (unsigned spins = 0;; ++spins) {
      gv = __hip_atomic_load((gu32*)gp + (lane_id() & 31), RLX_AGENT);
      if (__all(gv != 0u)) break;
      if (spin_timeout(spins, TMO)) return;
      __builtin_amdgcn_s_sleep(4);
    }
    const float gm = wave_max_dpp(__uint_as_float(gv));
    asm volatile("s_nop 0\n\tv_readfirstlane_b32 %0, %1" : "=s"(gw2) : "v"(gm * gm * 1.06f));
  }
  v8u pv;
  {
    LANE_IDS();
    *reinterpret_cast<v4u*>(smem + C_SM + w * 1024 + l * 16) = m0r;
    const unsigned voff = g * 256 + c * 8, wp = sb + w * 16384 + l * 16;
    pv[0] = (c & 8) ? 0xEEEE : 0x4444;
    pv[1] = sb + C_SM + l * 16;
    pv[2] = ATT ? sb + C_SQ + ((c & 7) * QS + 8 * g) * 2 : voff;
    pv[3] = ATT ? sb + C_SK + ((4 * ((c >> 2) & 1) + 2 * (c >> 3) + (c & 1)) * QS + 8 * g) * 2 : wp;
    pv[4] = sb + (c < 8 ? C_SO : C_SQ) + w * 256 + ((c & 7) + 8 * (g >> 1)) * 16 + 8 * (g & 1);
    pv[5] = voff;
    pv[6] = wp;
    pv[7] = sb + C_SO + (l >> 5) * 1024 + w * 256 + (l & 31) * 8;
  }
  if (!ATT) {
    if (!wait_n<4, 1>(FQ + h * 32, 1, TMO)) return;
    const unsigned lo = lane_id() * 16, d = sb + C_KT + (2 * w - 8) * 1024;
    glds16c(gK, lo, d);
    glds16c(gK + 1024, lo, d + 1024);
    glds16c(gV, lo, d + 8192);
    glds16c(gV + 1024, lo, d + 9216);
  }
  DRAIN();
  SST(8);
  const v4u rsA = mk_srd(ws + OFF_ACC + L * SZ_ACC, SZ_ACC);
  const v4u rsF = mk_srd(FQ, 64 * 32 * 32 * 4);
  const v4u rsMK = mk_srd(ws + OFF_MEMK + L * SZ_MEM, SZ_MEM);
  const v4u rsMV = mk_srd(ws + OFF_MEMV + L * SZ_MEM, SZ_MEM);
  const char* pAB = ws + OFF_ACC + L * SZ_ACC + (wc - w) * 4096;
  const char* pGK = gK + NH * 16 * 1024;
  const char* pGV = gV + NH * 8 * 1024;
  const unsigned* pFK = FL;
  unsigned sc[8];
  sc[0] = (32 + h) * 128;
  sc[1] = (h * 4 + w - 4) * 512;
  sc[2] = (h * DH + 16 * (w - 4)) * MM * 2;
  sc[3] = NSEG - 1;
  sc[4] = __float_as_uint(gw2);
  sc[5] = w;
  sc[6] = sb;
  sc[7] = h == 0 ? 0xd4000000u - 2u - (1u << 29) : 0xd4000000u;
  const char* dbg = ws;
  const unsigned tg = (now10() / GR) * GR;
  const v4u rsT = mk_srd(FL + FL_TK + L * 256, 1024);
#define CHAIN_OPS                                                                                                          \
  ::"{v[64:71]}"(w0[0]), "{v[72:79]}"(w0[1]), "{v[80:87]}"(w0[2]), "{v[88:95]}"(w0[3]), "{v[96:103]}"(w0[4]),              \
      "{v[104:111]}"(w0[5]), "{v[112:119]}"(w0[6]), "{v[120:127]}"(w0[7]), "{v[128:135]}"(w1[0]), "{v[136:143]}"(w1[1]),    \
      "{v[144:151]}"(w1[2]), "{v[152:159]}"(w1[3]), "{v[160:167]}"(w1[4]), "{v[168:175]}"(w1[5]), "{v[176:183]}"(w1[6]),    \
      "{v[184:191]}"(w1[7]), "{v[192:199]}"(w2[0]), "{v[200:207]}"(w2[1]), "{v[208:215]}"(w2[2]), "{v[216:223]}"(w2[3]),    \
      "{v[224:231]}"(w2[4]), "{v[232:239]}"(w2[5]), "{v[240:247]}"(w2[6]), "{v[248:255]}"(w2[7]), "{v[0:7]}"(pv),          \
      "{s[40:43]}"(rsA), "{s[44:47]}"(rsF), "{s[48:51]}"(rsMK), "{s[52:55]}"(rsMV), "{s[56:57]}"(pAB), "{s60}"(w * 4096), "{s61}"(h * 1024), \
      "{s[62:63]}"(pGK), "{s[64:65]}"(pGV), "{s[66:67]}"(pFK), "{s68}"(sc[0]), "{s69}"(sc[1]), "{s70}"(sc[2]),    \
      "{s71}"(sc[3]), "{s72}"(sc[4]), "{s73}"(sc[5]), "{s74}"(sc[6]), "{s75}"(sc[7]), "{s[36:37]}"(dbg),                   \
      "{s34}"(tg), "{s35}"(psw * 1536), [gp] "n"(GP), [gr] "n"(GR), [gm] "n"(GM), \
      "{s[16:19]}"(rsT), "{s20}"(0), "{s22}"(sb + C_X + 128 + 8 * (w & 3)), "{s24}"((h & 3) * 256), [cd] "n"(CD) \
      : "memory", "vcc", "scc", "s99", "v63"
  if (ATT) asm volatile("s_mov_b64 exec, -1\ns_nop 4\n" CHAIN_ASM_ATT CHAIN_OPS);
  else asm volatile("s_mov_b64 exec, -1\ns_nop 4\n" CHAIN_ASM_VO CHAIN_OPS);
#undef CHAIN_OPS
#undef LANE_IDS
}

__device__ __forceinline__ void role_chain(const MegaArgs& A, char* smem, const int L, const int h, const int rep) {
  if (__builtin_amdgcn_readfirstlane(threadIdx.x >> 6) < 4) role_chain_t<true>(A, smem, L, h, rep);
  else role_chain_t<false>(A, smem, L, h, rep);
}

constexpr int AW_SO = 0, AW_M = 32768, AW_TK = AW_M + 8192, AW_CNT = AW_TK + 98304;
typedef __attribute__((address_space(3))) unsigned lu32;
__device__ __forceinline__ unsigned umin3(unsigned a, unsigned b, unsigned c) {
  return __builtin_elementwise_min(__builtin_elementwise_min(a, b), c);
}
__device__ __forceinline__ bool all_set(const unsigned r) {
  unsigned long long m;
  asm volatile("v_cmp_eq_u32 %0, 0, %1" : "=s"(m) : "v"(r) : "memory");
  return m == 0ull;
}
__device__ __forceinline__ unsigned long long zero_mask(const unsigned r) {
  unsigned long long m;
  asm volatile("v_cmp_eq_u32 %0, 0, %1" : "=s"(m) : "v"(r) : "memory");
  return m;
}
__device__ __forceinline__ int job_seg(const int n) {
  const int m = n - AWD;
  const int r = (m & 1) ? 64 + (m >> 1) : AWD + (m >> 1);
  return __builtin_amdgcn_readfirstlane(n > 127 ? 128 : (n < AWD || n >= 128 - AWD) ? n : r);
}
template <bool AT>
__device__ __forceinline__ void role_aw_t(const MegaArgs& A, char* smem, const int mode, const int a) {
  __builtin_amdgcn_s_waitcnt(0x0F70);
  const int w = __builtin_amdgcn_readfirstlane(threadIdx.x >> 6);
  const int l = threadIdx.x & 63, c = l & 15, g = l >> 4;
  const int qt = w & 1, hh = (w >> 1) & 1, h = 2 * a + hh;
  char* ws = A.ws;
  unsigned* FL = (unsigned*)(ws + OFF_FL);
  unsigned* TMO = FL + FL_TMO;
  const unsigned sb = lds_base(smem);
  constexpr int NP = AT ? 1 : 3;
  const int cp0 = AT ? w : 3 * w - 8;
  v4u wo[2 * NP][8];
  {
    const int q = (l >> 2) & 3, p = l & 3, f = q | ((g & 1) << 2);
    const unsigned ra = sb + (4 * g + q) * 256 + (p & 1) * 16;
#pragma unroll
    for (int r = 0; r < 2; ++r) {
#pragma unroll
      for (int k = 0; k < 2; ++k) {
        const char* base_ = (const char*)A.Wo + ((size_t)((2 * a + r) * DH + 64 * (w & 1) + 32 * k) * DM + 128 * (w >> 1)) * 2;
#pragma unroll
        for (int i = 0; i < 8; ++i) {
          const int fd = g | ((i & 1) << 2);
          glds16(base_, (unsigned)((4 * i + g) * (DM * 2) + ((c ^ (fd << 1)) << 4)), sb + w * 16384 + k * 8192 + i * 1024);
        }
      }
      WAIT_VM(0);
      RAW_BAR();
#pragma unroll
      for (int ks = 0; ks < 4; ++ks) {
#pragma unroll
        for (int ct = 0; ct < 2 * NP; ++ct) {
          const int cp = cp0 + (ct >> 1);
          const unsigned a_ = ra + (cp >> 2) * 32768 + ks * 8192 + ((2 * (cp & 3) + (p >> 1)) ^ f) * 32 + (ct & 1) * 8;
          const v2u lo = trd(a_), hi = trd(a_ + 4096);
          wo[ct][4 * r + ks] = v4u{lo.x, lo.y, hi.x, hi.y};
        }
#pragma unroll
        for (int ct = 0; ct < 2 * NP; ++ct) asm volatile("" : "+v"(wo[ct][4 * r + ks]));
      }
      RAW_BAR();
    }
  }
  lu32* cnt = (lu32*)(unsigned long)(sb + AW_CNT);
  if (threadIdx.x < 16) cnt[threadIdx.x] = 0u;
  WSTAMP2();
  const __amdgpu_buffer_rsrc_t rsW = MKRS(ws + OFF_PW, SZ_PW);
  const unsigned* FQ = FL + FL_FQ + h * 32;
  const v4u rsF = mk_srd(FQ, (128 * 32 - h) * 128);
  const char* gQ = ws + OFF_QK + ((size_t)h * 16 + 8 * qt) * 1024;
  const char* gV = ws + OFF_VF + ((size_t)h * 8 + 4 * qt) * 1024;
  const char* gM = ws + (qt ? OFF_MEMV : OFF_MEMK) + h * 2048;
  const int NJ = (130 - mode) / 3;
#define AWS(gs) (64 + a + ((gs) >> 6) * 16)
  const int mo = AW_M + hh * 4096;
  const unsigned tq0 = __builtin_amdgcn_readfirstlane(sb + AW_TK + (hh * 24 + 8 * qt) * 1024);
  const unsigned tv0 = __builtin_amdgcn_readfirstlane(sb + AW_TK + (hh * 24 + 16 + 4 * qt) * 1024);
  const float scale = 0.08838834764831845f;
  volatile lu32* cM = cnt + 2 + hh;
#define DMA4(x) "global_load_lds_dwordx4 %1, " x " sc1\n\tglobal_load_lds_dwordx4 %1, " x " offset:1024 sc1\n\t"       \
                "global_load_lds_dwordx4 %1, " x " offset:2048 sc1\n\tglobal_load_lds_dwordx4 %1, " x " offset:3072 sc1\n\t"
#define T_DMA(jj, gg)                                                                                                   \
  {                                                                                                                     \
    const unsigned o_ = ((jj) & 1) ? 49152u : 0u;                                                                       \
    const char* q_ = gQ + (size_t)(gg) * (NH * 16 * 1024);                                                              \
    unsigned keep_;                                                                                                     \
    asm volatile("s_nop 4\n\ts_mov_b32 %0, m0\n\ts_mov_b32 m0, %5\n\ts_nop 0\n\t" DMA4("%2")                           \
                 "s_mov_b32 m0, %6\n\ts_nop 0\n\t" DMA4("%3") "s_mov_b32 m0, %7\n\ts_nop 0\n\t" DMA4("%4") "s_mov_b32 m0, %0" \
                 : "=&s"(keep_)                                                                                         \
                 : "v"(l * 16), "s"(q_), "s"(q_ + 4096), "s"(gV + (size_t)(gg) * (NH * 8 * 1024)), "s"(tq0 + o_),        \
                   "s"(tq0 + o_ + 4096u), "s"(tv0 + o_)                                                                 \
                 : "memory");                                                                                           \
  }
#define T_LAND(jj) { if (l == 0) *(volatile lu32*)(cnt + w) = (jj) + 1; }
#define M_DMA(gg)                                                                                                       \
  {                                                                                                                     \
    unsigned keep_;                                                                                                     \
    asm volatile("s_nop 4\n\ts_mov_b32 %0, m0\n\ts_mov_b32 m0, %3\n\ts_nop 0\n\tglobal_load_lds_dwordx4 %1, %2 sc1\n\t"     \
                 "global_load_lds_dwordx4 %1, %2 offset:1024 sc1\n\ts_mov_b32 m0, %0"                                    \
                 : "=&s"(keep_)                                                                                         \
                 : "v"(l * 16), "s"(gM + (size_t)(gg) * (NH * 2048)), "s"(sb + mo + qt * 2048)                           \
                 : "memory");                                                                                           \
  }
#define M_TEST()                                                                                                        \
  {                                                                                                                     \
    const v4u t_ = *reinterpret_cast<const v4u*>(smem + mo + qt * 2048 + l * 16);                                        \
    const v4u u_ = *reinterpret_cast<const v4u*>(smem + mo + qt * 2048 + 1024 + l * 16);                                 \
    ok = __all(umin3(umin3(t_.x, t_.y, t_.z), umin3(u_.x, u_.y, u_.z), umin3(t_.w, u_.w, 1u)) != 0u);                   \
    if (ok) asm volatile("s_mov_b64 exec, 1\n\tds_add_u32 %0, %1\n\ts_mov_b64 exec, -1" ::"v"(sb + AW_CNT + 8 + 4 * hh), "v"(1u) : "memory"); \
  }
#define F_LOAD(r, gg) \
  asm volatile("s_nop 4\n\tbuffer_load_dword %0, %1, %2, %3 offen sc1" : "=v"(r) : "v"(0), "s"(rsF), "s"((gg) * 4096) : "memory")
#define F_TEST(r) all_set(r)
#define W_FLAG(jj, gg)                                                                                                  \
  {                                                                                                                     \
    unsigned old_;                                                                                                      \
    asm volatile("s_mov_b64 exec, 1\n\tds_add_rtn_u32 %0, %1, %2\n\ts_mov_b64 exec, -1\n\ts_waitcnt lgkmcnt(0)"           \
                 : "=&v"(old_) : "v"(sb + AW_CNT + ((jj) & 1) * 36), "v"(1u) : "memory");                               \
    if ((__builtin_amdgcn_readfirstlane(old_) & 7u) == 7u) {                                                            \
      TSR(AWS(gg), 4, (gg) & 63, threadIdx.x & ~63u);                                                                   \
      if (l < 4) set_flag(FL + FL_FW + (((gg) * 4 + l) * 4 + (a >> 2)) * 32 + (a & 3));                              \
    }                                                                                                                   \
    pend = false;                                                                                                       \
  }
#define LDS_U(p) ((unsigned)__builtin_amdgcn_readfirstlane(*(p)))
#define TOK_MIN() __builtin_elementwise_min(LDS_U((volatile lu32*)(cnt + 4 + 2 * hh)), LDS_U((volatile lu32*)(cnt + 5 + 2 * hh)))
  __builtin_amdgcn_s_waitcnt(0x0F70);
  RAW_BAR();
  bool pend = false, asked = false;
  unsigned tb1 = now10() - 130 * GPER, tb2 = tb1;
  int gp = 0, gs = job_seg(mode), g1 = job_seg(mode + 3);
  if (!AT) {
    (void)wait_n<SLP_A, 1>(FQ + gs * 1024, 1, TMO);
    T_DMA(0, gs);
    WAIT_VM(0);
    T_LAND(0);
  }
  FSD;
  for (int s = 0; s < NJ; ++s) {
    char* so = smem + AW_SO + (s & 1) * 16384 + l * 16;
    bool got = g1 == 128, land = got, seen = false;
    if (AT) {
      if (TOK_MIN() < (unsigned)(s + 1)) {
        if (pend) {
          WAIT_VM(0);
          W_FLAG(s - 1, gp);
        }
        for (unsigned spins = 0; TOK_MIN() < (unsigned)(s + 1) && spins < 64u * SPIN_CAP; ++spins) __builtin_amdgcn_s_sleep(1);
      }
      asm volatile("" ::: "memory");
      TSR(AWS(gs), 0, gs & 63, 0);
      FS(0);
      const char* tk = smem + AW_TK + ((s & 1) * 2 + hh) * 24 * 1024 + l * 16;
#define TKF(i) (*reinterpret_cast<const bf16x8*>(tk + (i) * 1024))
      bf16x8 q[4], vt[8];
      float x[12], mx;
      bool ok = false;
      {
        f32x4 s0 = {}, s1 = {};
        bf16x8 kd[4], k0[4];
#pragma unroll
        for (int ks = 0; ks < 4; ++ks) {
          q[ks] = TKF(4 * qt + ks);
          kd[ks] = TKF(8 + 4 * qt + ks);
          k0[ks] = TKF(8 + ks);
        }
#pragma unroll
        for (int i = 0; i < 8; ++i) vt[i] = TKF(16 + i);
        __builtin_amdgcn_sched_barrier(0);
#pragma unroll
        for (int ks = 0; ks < 4; ++ks) {
          s0 = MFMA16(kd[ks], q[ks], s0);
          s1 = MFMA16(k0[ks], q[ks], s1);
        }
        scale_pair(s0[0], s0[1], scale, x[0], x[1]);
        scale_pair(s0[2], s0[3], scale, x[2], x[3]);
        scale_pair(s1[0], s1[1], scale, x[4], x[5]);
        scale_pair(s1[2], s1[3], scale, x[6], x[7]);
#pragma unroll
        for (int i = 0; i < 4; ++i) {
          if (4 * g + i > c) x[i] = -3e38f;
          if (!qt) x[4 + i] = -3e38f;
        }
        mx = fmaxf(fmaxf(fmaxf(x[0], x[1]), fmaxf(x[2], x[3])), fmaxf(fmaxf(x[4], x[5]), fmaxf(x[6], x[7])));
        asm volatile("" : "+v"(mx), "+v"(vt[0]), "+v"(vt[1]), "+v"(vt[2]), "+v"(vt[3]), "+v"(vt[4]), "+v"(vt[5]), "+v"(vt[6]), "+v"(vt[7]));
      }
#undef TKF
      FS(1);
      if (asked) {
        WAIT_VM(1);
        M_TEST();
      }
      FS(2);
      if (!ok) {
        if (pend) {
          WAIT_VM(0);
          W_FLAG(s - 1, gp);
        }
        unsigned tb = gs < 64 ? tb1 : tb2, spins = 0;
        gate(tb + gs * GPER);
        for (; !ok; ++spins) {
          if (spin_timeout(spins, TMO)) break;
          if (spins) __builtin_amdgcn_s_sleep(SLP_M);
          M_DMA(gs);
          WAIT_VM(0);
          M_TEST();
        }
        tb = spins > 1 ? now10() - gs * GPER - GMA : tb - GSTEP;
        if (gs < 64) tb1 = tb; else tb2 = tb;
      }
      FS(3);
      __builtin_amdgcn_s_setprio(PRI_A);
      asked = false;
      for (unsigned spins = 0; LDS_U(cM) < 2u * (s + 1) && spins < 64u * SPIN_CAP; ++spins) __builtin_amdgcn_s_sleep(1);
      v4u km[4];
      v2u vm[8];
#pragma unroll
      for (int ks = 0; ks < 4; ++ks) km[ks] = *reinterpret_cast<const v4u*>(smem + mo + (ks * 32 + g * 8 + (c & 7)) * 16);
#pragma unroll
      for (int i = 0; i < 8; ++i) vm[i] = *reinterpret_cast<const v2u*>(smem + mo + 2048 + ((16 * i + c) * MM + 4 * (g & 1)) * 2);
      TSR(AWS(gs), 1, gs & 63, 0);
      FS(4);
      f32x4 m0 = {};
#pragma unroll
      for (int ks = 0; ks < 4; ++ks) m0 = MFMA16(BF8(km[ks]), q[ks], m0);
      scale_pair(m0[0], m0[1], scale, x[8], x[9]);
      scale_pair(m0[2], m0[3], scale, x[10], x[11]);
#pragma unroll
      for (int i = 0; i < 4; ++i) {
        if (g >= 2) x[8 + i] = -3e38f;
        mx = fmaxf(mx, x[8 + i]);
      }
      mx = xmax32(xmax16(mx));
      float sum = 0.f;
#pragma unroll
      for (int i = 0; i < 12; ++i) {
        x[i] = __expf(x[i] - mx);
        sum += x[i];
      }
      sum = xsum32(xsum16(sum));
      const float inv = 1.0f / sum;
      const v2u pa = {pk2(x[0] * inv, x[1] * inv), pk2(x[2] * inv, x[3] * inv)};
      const v2u pb = {pk2(x[4] * inv, x[5] * inv), pk2(x[6] * inv, x[7] * inv)};
      const v4u p0 = qt ? v4u{pb.x, pb.y, pa.x, pa.y} : v4u{pa.x, pa.y, pb.x, pb.y};
      const v2u p1 = {pk2(x[8] * inv, x[9] * inv), pk2(x[10] * inv, x[11] * inv)};
      FS(5);
#pragma unroll
      for (int k = 0; k < 4; ++k) {
        v4u of;
#pragma unroll
        for (int hf = 0; hf < 2; ++hf) {
          f32x4 ao = {};
          ao = MFMA16(vt[2 * k + hf], BF8(p0), ao);
          ao = MFMA16K(vm[2 * k + hf], p1, ao);
          of[2 * hf] = pk2(ao[0], ao[1]);
          of[2 * hf + 1] = pk2(ao[2], ao[3]);
        }
        *reinterpret_cast<v4u*>(so + ((hh * 2 + qt) * 4 + k) * 1024) = of;
      }
      FS(6);
      seen = g1 >= 64 && g1 < 128 && TOK_MIN() >= (unsigned)(s + 2);
      if (pend) {
        WAIT_VM(0);
        W_FLAG(s - 1, gp);
      }
    } else {
      FS(0);
      unsigned n;
      F_LOAD(n, g1);
      if (pend) {
        WAIT_VM(1);
        W_FLAG(s - 1, gp);
      }
      WAIT_VM(0);
      FS(1);
      if (F_TEST(n)) {
        T_DMA(s + 1, g1);
        got = true;
        if (LDS_U(cM) < 2u * (s + 1)) {
          WAIT_VM(0);
          T_LAND(s + 1);
          land = true;
        }
      }
    }
    FS(7);
    RAW_BAR();
    FS(8);
    __builtin_amdgcn_s_setprio(AT ? PRI_WA : PRI_WB);
    TSR(AWS(gs), 2, gs & 63, 256);
    const int voff = (c >> 1) * 2048 + (c & 1) * 64 + g * 16, soff = (gs * 16 + a) * 32768 + cp0 * 128;
#define OFRAG(i) (*reinterpret_cast<const bf16x8*>(so + (((((i) & 7) >> 2) * 2 + ((i) >> 3)) * 4 + ((i) & 3)) * 1024))
    constexpr int NO = AT ? 16 : 3;
    bf16x8 o[NO];
#pragma unroll
    for (int i = 0; i < (AT ? 16 : 2); ++i) o[i] = OFRAG(i);
    unsigned pr;
    const bool look = seen && g1 >= 128 - AWD;
    if (AT) {
      asm volatile("s_cmp_eq_u32 %3, 0\n\ts_cbranch_scc1 1f\n\ts_nop 4\n\tglobal_load_dword %0, %1, %2 sc1\n1:"
                   : "=v"(pr) : "v"(2044), "s"(gM + (size_t)g1 * (NH * 2048)), "s"(__builtin_amdgcn_readfirstlane((int)look)) : "memory", "scc");
    } else {
      F_LOAD(pr, got ? 128 : g1);
    }
    __builtin_amdgcn_sched_barrier(0);
    FS(9);
#pragma unroll
    for (int rt = 0; rt < 2; ++rt) {
      f32x4 acc[2 * NP] = {};
#pragma unroll
      for (int kk = 0; kk < 8; ++kk) {
        const int i = 8 * rt + kk;
        if (!AT && i + 2 < 16) o[(i + 2) % NO] = OFRAG(i + 2);
#pragma unroll
        for (int ct = 0; ct < 2 * NP; ++ct) acc[ct] = MFMA16(BF8(wo[ct][kk]), o[i % NO], acc[ct]);
        if (!AT) __builtin_amdgcn_sched_barrier(0);
      }
      if (rt == 0) FS(10); else FS(12);
      if (AT && rt == 1 && seen) {
        if (look) {
          WAIT_VM(1);
          seen = F_TEST(pr);
        }
        if (seen) {
          M_DMA(g1);
          asked = true;
        }
      }
      if (!AT && rt == 0) {
        WAIT_VM(0);
        if (got && !land) {
          T_LAND(s + 1);
          land = true;
        }
        if (F_TEST(pr)) {
          T_DMA(s + 1, g1);
          got = true;
        }
      }
      if (!AT && rt == 1 && got && !land) {
        WAIT_VM(3);
        T_LAND(s + 1);
      }
#pragma unroll
      for (int u = 0; u < NP; ++u)
        ST128((v4u{pk2(acc[2 * u][0], acc[2 * u][1]), pk2(acc[2 * u][2], acc[2 * u][3]), pk2(acc[2 * u + 1][0], acc[2 * u + 1][1]),
                   pk2(acc[2 * u + 1][2], acc[2 * u + 1][3])}),
              rsW, voff + u * 128, soff + rt * 16384);
      if (rt == 0) FS(11); else FS(13);
    }
    __builtin_amdgcn_s_setprio(AT ? PRI_0 : 0);
    pend = true;
    FD(s);
    TSR(AWS(gs), 3, gs & 63, 256);
    TSR(AWS(gs), 5, gs & 63, 0);
    if (!AT && !got) {
      WAIT_VM(0);
      W_FLAG(s, gs);
      (void)wait_n<SLP_A, 1>(FQ + g1 * 1024, 1, TMO);
      T_DMA(s + 1, g1);
      WAIT_VM(0);
      T_LAND(s + 1);
    }
    gp = gs;
    gs = g1;
    g1 = job_seg(3 * s + 6 + mode);
  }
  WAIT_VM(0);
  W_FLAG(NJ - 1, gp);
#undef AWS
#undef OFRAG
#undef DMA4
#undef T_DMA
#undef T_LAND
#undef M_DMA
#undef M_TEST
#undef F_LOAD
#undef F_TEST
#undef W_FLAG
#undef LDS_U
#undef TOK_MIN
}
__device__ __forceinline__ void role_aw(const MegaArgs& A, char* smem, const int L, const int a) {
  if (__builtin_amdgcn_readfirstlane(threadIdx.x >> 6) < 4) role_aw_t<true>(A, smem, L, a);
  else role_aw_t<false>(A, smem, L, a);
}

__device__ __forceinline__ void role_red(const MegaArgs& A, char* smem, const int idx) {
  const int w = __builtin_amdgcn_readfirstlane(threadIdx.x >> 6);
  const int l = threadIdx.x & 63;
  const int gr = w >> 1, r = idx * 2 + (w & 1);
  char* ws = A.ws;
  unsigned* FL = (unsigned*)(ws + OFF_FL);
  unsigned* TMO = FL + FL_TMO;
  const __amdgpu_buffer_rsrc_t rsW = MKRS(ws + OFF_PW, SZ_PW);
  const __amdgpu_buffer_rsrc_t rsX = MKRS(ws + OFF_XA, OFF_X3 - OFF_XA + SZ_X);
  volatile unsigned* sw = reinterpret_cast<volatile unsigned*>(smem) + (w >> 1) * 32;
  if (threadIdx.x < 128) reinterpret_cast<volatile unsigned*>(smem)[threadIdx.x] = 0u;
  __syncthreads();
  unsigned tb1 = now10() - 130 * GPER, tb2 = tb1;
  const int rn1 = idx < 8 ? 96 + idx : 224 + idx;
  (void)rn1;
  if (w >= 6) return;
  if (w < 4) {
    const __amdgpu_buffer_rsrc_t rz = MKRS(ws + OFF_X2, SZ_X);
    for (int s = gr; s < NSEG; s += 2) ST128((v4u{0u, 0u, 0u, 0u}), rz, l * 16, (s * SEG + r) * DM * 2);
    DRAIN();
    if (l < 4) set_flag(FL + FL_FZ + (2 * l + (idx >> 3)) * 32 + (idx & 7) * 4 + w);
    if (l == 0) __hip_atomic_fetch_add((lu32*)(unsigned long)(lds_base(smem) + 480), 1u, __ATOMIC_RELAXED, __HIP_MEMORY_SCOPE_WORKGROUP);
  }
  for (unsigned spins = 0; reinterpret_cast<volatile unsigned*>(smem)[120] < 4u && spins < 64u * SPIN_CAP; ++spins) __builtin_amdgcn_s_sleep(2);
  for (int n = gr; n < 128; n += 3) {
    const int gs = job_seg(n), L = gs >> 6, s = gs & 63;
    const unsigned* fw = FL + FL_FW + (gs * 4 + REP) * 4 * 32;
    unsigned tb = L ? tb2 : tb1;
    tb = grp_wait<SLP_R, 4>((w & 1) == 0, sw, n + 1, fw, 16, TMO, tb + gs * GPER) ? now10() - gs * GPER - GMR : tb - GSTEP;
    if (n >= 125 - AWD) tb = now10() + GATE_RT - (n + 3) * GPER;
    if (L || n >= 125 - AWD) tb2 = tb; else tb1 = tb;
#define RTS(k, t) TSR(L ? A.rn : rn1, k, s, (w & 6) * 64 + (t))
    RTS(0, 0);
    RTS(4, 64);
    v4u p[16];
    const v4u xr = LD128(rsX, l * 512, L * (int)(OFF_X3 - OFF_XA) + s * 32768 + r * 16);
#pragma unroll
    for (int k = 0; k < 16; ++k)
      p[k] = LD128(rsW, (l >> 2) * 128 + (l & 3) * 16, (gs * 16 + k) * 32768 + (r >> 1) * 2048 + (r & 1) * 64);
    v4u xo;
    const unsigned nz = L ? 0u : 0x8000u;
#pragma unroll
    for (int e = 0; e < 4; ++e) {
      f32x2_t t[4];
#pragma unroll
      for (int k = 0; k < 4; ++k)
        t[k] = (f32x2_t{lo16(p[4 * k][e]), hi16(p[4 * k][e])} + f32x2_t{lo16(p[4 * k + 1][e]), hi16(p[4 * k + 1][e])}) +
               (f32x2_t{lo16(p[4 * k + 2][e]), hi16(p[4 * k + 2][e])} + f32x2_t{lo16(p[4 * k + 3][e]), hi16(p[4 * k + 3][e])});
      const f32x2_t u = (t[0] + t[1]) + (t[2] + t[3]);
      const unsigned a0 = pk2(u.x, u.y);
      xo[e] = pk2(lo16(xr[e]) + lo16(a0), hi16(xr[e]) + hi16(a0));
      xo[e] = xo[e] ? xo[e] : nz;
    }
    ST128(xo, MKRS(L ? (char*)A.out : ws + OFF_X2, SZ_X), l * 16, (s * SEG + r) * DM * 2);
    RTS(1, 0);
    RTS(5, 64);
    if (L == 0) {
      const unsigned st = row_stats(xo);
      if (l < 4) __hip_atomic_store((gu32*)(FL + FL_FR + ((s * 4 + l) * 4 + (r >> 3)) * 32 + (r & 7)), st, RLX_AGENT);
    }
    RTS(6, 64);
    RTS(3, 0);
#undef RTS
  }
  if (idx == 0 && threadIdx.x == 128) {
    gu64* en = (gu64*)(ws + OFF_TAG + 8192);
    __hip_atomic_store(en, __hip_atomic_load(en, RLX_AGENT) + TAG_STEP, RLX_AGENT);
  }
}

constexpr int CUN = 2144;
__device__ __forceinline__ bf16x8 conv_bfrag(const unsigned* up, const unsigned sh) {
  const unsigned d0 = up[0], d1 = up[1], d2 = up[2], d3 = up[3], d4 = up[4];
  const v4u r = {__builtin_amdgcn_alignbit(d1, d0, sh), __builtin_amdgcn_alignbit(d2, d1, sh),
                 __builtin_amdgcn_alignbit(d3, d2, sh), __builtin_amdgcn_alignbit(d4, d3, sh)};
  return BF8(r);
}
__device__ __forceinline__ void role_conv(const MegaArgs& A, char* smem, const int cv) {
  __builtin_amdgcn_s_waitcnt(0x0F70);
  u16* Kr = reinterpret_cast<u16*>(smem);
  u16* Uc = Kr + 8 * 2 * CKN;
  u16* Xr = Uc + 8 * CUN;
  u16* To = Uc + 8 * 2 * CUN;
  u16* Su = To + 32 * 8;
  u16* Sx = Su + 32 * 8;
  const int tid = threadIdx.x;
  const int w = __builtin_amdgcn_readfirstlane(tid >> 6);
  const int l = tid & 63, c = l & 15, g = l >> 4;
  const int d = 8 * cv + w;
  char* ws = A.ws;
  unsigned* FL = (unsigned*)(ws + OFF_FL);
  unsigned* TMO = FL + FL_TMO;
  u16* kr = Kr + w * 2 * CKN;
  float* prm = reinterpret_cast<float*>(kr);
  v4u tx[4];
  const v2u en = LD64(MKRS(ws + OFF_TAG, 8192 + 8), 0, 8192);
  {
    const unsigned pa = *reinterpret_cast<const unsigned*>(A.logAre + d * NST + 2 * l);
    const unsigned pb = *reinterpret_cast<const unsigned*>(A.Aim + d * NST + 2 * l);
    const unsigned pc = *reinterpret_cast<const unsigned*>(A.Cre + d * NST + 2 * l);
    const unsigned pd = *reinterpret_cast<const unsigned*>(A.Cim + d * NST + 2 * l);
    const float dt = bfr(expf(bf2f(A.logdt[d])));
    if (w < 2) tx[0] = *reinterpret_cast<const v4u*>(A.x0 + (size_t)tid * DM + 8 * cv);
    if (l < 16) reinterpret_cast<unsigned*>(Uc + w * CUN)[l] = 0u;
    if (l < 32) reinterpret_cast<unsigned*>(Uc + w * CUN + 32 + L_SEQ)[l] = 0u;
    if (l < 32) reinterpret_cast<unsigned*>(Uc + w * CUN + 32 + 128)[l] = 0u;
    if (l < 48) {
      reinterpret_cast<v4u*>(kr + 1824)[l] = v4u{0u, 0u, 0u, 0u};
      reinterpret_cast<v4u*>(kr + CKN + 1824)[l] = v4u{0u, 0u, 0u, 0u};
    }
#pragma unroll
    for (int e = 0; e < 2; ++e) {
      const float ar = -bfr(expf(e ? hi16(pa) : lo16(pa))) * dt, ai = (e ? hi16(pb) : lo16(pb)) * dt;
      const float ex = expf(ar);
      float sn, cs;
      sincosf(ai, &sn, &cs);
      prm[2 * l + e] = ar;
      prm[NST + 2 * l + e] = ai;
      prm[2 * NST + 2 * l + e] = (e ? hi16(pc) : lo16(pc)) * dt;
      prm[3 * NST + 2 * l + e] = (e ? hi16(pd) : lo16(pd)) * dt;
      prm[4 * NST + 2 * l + e] = ex * cs;
      prm[5 * NST + 2 * l + e] = ex * sn;
    }
  }
  const float dsk = bf2f(A.Dskip[d]);
  const u16* ka = kr + (c >> 3) * CKN + 2080 - 16 * (c & 7) + 8 * g;
  const unsigned ush = (c & 1) * 16;
#define KFRAG(D0) (*reinterpret_cast<const bf16x8*>(ka - (D0)))
  f32x4 acc[16], far[9];
  v4u xf[3];
  const unsigned* ub1 = reinterpret_cast<const unsigned*>(Uc + w * CUN + (c & ~1) + 8 * g + 32);
  const __amdgpu_buffer_rsrc_t rz1 = MKRS(ws + OFF_XA, SZ_X);
  const int t0 = 16 * (4 * (g & 1) + (g >> 1)) + c;
  {
    const u16* uc = Uc + w * CUN;
    const unsigned* ub = reinterpret_cast<const unsigned*>(uc + (c & ~1) + 8 * g + 32);
    const __amdgpu_buffer_rsrc_t rz = MKRS(ws + OFF_XA, SZ_X);
    const __amdgpu_buffer_rsrc_t rst = MKRS(ws + OFF_GT, L_SEQ * 4);
#define TILE_OUT(T)                                                                            \
  {                                                                                            \
    const float u0 = bf2f(uc[32 + 128 * (T) + t0]), u1 = bf2f(uc[64 + 128 * (T) + t0]);        \
    const float a0 = bfr(bfr(y0) + bfr(u0 * dsk)), a1 = bfr(bfr(y1) + bfr(u1 * dsk));          \
    u16* xp = Xr + (128 * (T) + t0) * 8 + w;                                                   \
    xp[0] = f2bf(bf2f(xp[0]) + a0);                                                            \
    xp[256] = f2bf(bf2f(xp[256]) + a1);                                                        \
  }
#define CHUNK_ST(ck)                                                                           \
  {                                                                                            \
    const v4u o = *reinterpret_cast<const v4u*>(Xr + (64 * (ck) + l) * 8);                     \
    ST128(o, rz, (l >> 5) * 32768 + (l & 31) * 16, (2 * (ck) * 64 + cv) * 512);                \
  }
#define U_ROW(row) U_ROW2(row, __builtin_amdgcn_raw_buffer_load_b32(rst, (row) * 4, 0, AUX_SC1))
#define U_ROW2(row, stv)                                                                       \
  {                                                                                            \
    const unsigned st_ = (stv);                                                                \
    const v4u xr_ = *reinterpret_cast<const v4u*>(Xr + (row) * 8);                             \
    const float mb_ = lo16(st_), ri_ = 1.0f / hi16(st_);                                       \
    _Pragma("unroll") for (int e = 0; e < 4; ++e) {                                            \
      const unsigned t_ = pk2(lo16(xr_[e]) - mb_, hi16(xr_[e]) - mb_);                         \
      const unsigned q_ = pk2(lo16(t_) * ri_, hi16(t_) * ri_);                                 \
      Uc[(2 * e) * CUN + 32 + (row)] = (u16)q_;                                                \
      Uc[(2 * e + 1) * CUN + 32 + (row)] = (u16)(q_ >> 16);                                    \
    }                                                                                          \
  }
    const unsigned* FS = FL + FL_FS + REP * 32 * 32;
    const int gi = 4 * (g * 8 + (l & 7));
    const int tl = 32 * ((l >> 3) & 1) + 8 * g + (l & 7);
    unsigned khl = 0u;
#pragma unroll 1
    for (int ps = 0; ps < 4; ++ps) {
      float v[32];
      {
        const f32x4 ar = *reinterpret_cast<const f32x4*>(prm + gi), ai = *reinterpret_cast<const f32x4*>(prm + NST + gi);
        const f32x4 cr = *reinterpret_cast<const f32x4*>(prm + 2 * NST + gi), ci = *reinterpret_cast<const f32x4*>(prm + 3 * NST + gi);
        const f32x4 wr = *reinterpret_cast<const f32x4*>(prm + 4 * NST + gi), wi = *reinterpret_cast<const f32x4*>(prm + 5 * NST + gi);
        const float p0 = (float)(64 * ps + 32 * ((l >> 3) & 1));
        float yr0[4], yi0[4];
#pragma unroll
        for (int k = 0; k < 4; ++k) {
          const float e = __expf(ar[k] * p0);
          float sn, cs;
          __sincosf(ai[k] * p0, &sn, &cs);
          const float zr = e * cs, zi = e * sn;
          yr0[k] = cr[k] * zr - ci[k] * zi;
          yi0[k] = cr[k] * zi + ci[k] * zr;
        }
        f32x2_t yra = {yr0[0], yr0[1]}, yia = {yi0[0], yi0[1]}, wra = {wr[0], wr[1]}, wia = {wi[0], wi[1]};
        f32x2_t yrb = {yr0[2], yr0[3]}, yib = {yi0[2], yi0[3]}, wrb = {wr[2], wr[3]}, wib = {wi[2], wi[3]};
#pragma unroll
        for (int i = 0; i < 32; ++i) {
          const f32x2_t sm2 = yra + yrb;
          v[i] = sm2.x + sm2.y;
          const f32x2_t ta = yra * wra - yia * wia, tb = yrb * wrb - yib * wib;
          yia = yra * wia + yia * wra;
          yib = yrb * wib + yib * wrb;
          yra = ta;
          yrb = tb;
        }
      }
#pragma unroll
      for (int i = 0; i < 16; ++i) v[i] = swap_add32(v[i], v[i + 16]);
#pragma unroll
      for (int i = 0; i < 8; ++i) {
        v[i] = swap_add16(v[i], v[i + 8]);
        v[i] += dppf<0xB1>(0.f, v[i]);
        v[i] += dppf<0x4E>(0.f, v[i]);
        v[i] += dppf<0x141>(0.f, v[i]);
      }
      float s0 = v[0], s1 = v[1], s2 = v[2], s3 = v[3], s4 = v[4], s5 = v[5], s6 = v[6], s7 = v[7];
      asm volatile("" : "+v"(s0), "+v"(s1), "+v"(s2), "+v"(s3), "+v"(s4), "+v"(s5), "+v"(s6), "+v"(s7));
      s0 = (l & 4) ? s4 : s0; s1 = (l & 4) ? s5 : s1; s2 = (l & 4) ? s6 : s2; s3 = (l & 4) ? s7 : s3;
      s0 = (l & 2) ? s2 : s0; s1 = (l & 2) ? s3 : s1;
      const float kv = 2.0f * ((l & 1) ? s1 : s0);
      khl = pk2(kv, 0.f);
      khl = pk2(kv, kv - lo16(khl));
      kr[2080 - 64 * ps - tl] = (u16)khl;
      kr[CKN + 2080 - 64 * ps - tl] = (u16)(khl >> 16);
      SST(ps);
      if (ps < 2) {
        if (ps == 0) {
          if (w < 2) {
            reinterpret_cast<v4u*>(Xr)[tid] = tx[0];
            const unsigned long long m = (((unsigned long long)en.y << 32) | en.x) + TAG_STEP;
            v4u g0;
            for (unsigned spins = 0; spins < 16u * SPIN_CAP; ++spins) {
              g0 = LD128(MKRS(ws + OFF_G0, 8192), tid * 16, REP * 2048);
              if (__all(((g0.x ^ g0.z) | (g0.y ^ (unsigned)m) | (g0.w ^ (unsigned)(m >> 32))) == 0u)) break;
              asm volatile("s_sleep 2" ::: "memory");
            }
            U_ROW2(tid, g0.x);
          }
          __syncthreads();
        }
        f32x4 a0t = {};
#pragma unroll
        for (int b = -1; b < 4; ++b) a0t = MFMA16(KFRAG(-32 * b), conv_bfrag(ub + 16 * b, ush), a0t);
        const float y0 = swap_add32(a0t[0], a0t[1]), y1 = swap_add32(a0t[2], a0t[3]);
        if ((g & 1) == ps) TILE_OUT(0);
        __syncthreads();
        if (w == ps) {
          CHUNK_ST(w);
          if (ps == 0) zero_wait(ws);
          DRAIN();
          if (l < 8) set_flag(FL + FL_FC + ((2 * w * 4 + l) * 8 + (cv >> 3)) * 32 + (cv & 7));
        }
        SST(4 + ps);
      }
      if (ps == 2) {
        if (w >= 2) tx[0] = *reinterpret_cast<const v4u*>(A.x0 + (size_t)tid * DM + 8 * cv);
#pragma unroll
        for (int i = 1; i < 4; ++i) tx[i] = *reinterpret_cast<const v4u*>(A.x0 + (size_t)(tid + 512 * i) * DM + 8 * cv);
      }
    }
    if (w >= 2) reinterpret_cast<v4u*>(Xr)[tid] = tx[0];
#pragma unroll
    for (int i = 1; i < 4; ++i) reinterpret_cast<v4u*>(Xr)[tid + 512 * i] = tx[i];
    {
      unsigned spins = 0;
      while (!__all(__hip_atomic_load((gu32*)FS + (w + 8 * ((l >> 3) & 3)) * 32 + (l & 7), RLX_AGENT) != 0u)) {
        if (spin_timeout(++spins, TMO)) break;
        __builtin_amdgcn_s_sleep(4);
      }
      asm volatile("" ::: "memory");
    }
    if (w >= 2) U_ROW(tid);
#pragma unroll
    for (int i = 1; i < 4; ++i) U_ROW(tid + 512 * i);
    SST(6);
    (void)wait_n<8, 1>(FL + FL_KG + cv * 32 + (w >> 2), 1, TMO);
    SST(7);
    {
      const __amdgpu_buffer_rsrc_t rk = MKRS(ws + OFF_KF + (size_t)d * 2 * CKN * 2, 2 * CKN * 2);
      v4u t[8];
#pragma unroll
      for (int i = 0; i < 4; ++i) {
        t[i] = LD128(rk, (64 * i + l) * 16, 0);
        t[4 + i] = LD128(rk, (64 * i + l) * 16, CKN * 2);
      }
#pragma unroll
      for (int i = 0; i < 4; ++i)
        if (64 * i + l < 229) {
          reinterpret_cast<v4u*>(kr)[64 * i + l] = t[i];
          reinterpret_cast<v4u*>(kr + CKN)[64 * i + l] = t[4 + i];
        }
      kr[2080 - 192 - tl] = (u16)khl;
      kr[CKN + 2080 - 192 - tl] = (u16)(khl >> 16);
    }
    __syncthreads();
#define BULK(TA, TB, HOOK)                                                                      \
  {                                                                                            \
    const bf16x8 bm = conv_bfrag(ub - 16, ush);                                                \
    _Pragma("unroll") for (int T = (TA); T < (TB); ++T) acc[T] = MFMA16(KFRAG(128 * T + 32), bm, (f32x4{})); \
    _Pragma("unroll 1") for (int r = 0; r < 4; ++r) {                                          \
      bf16x8 bf[TB];                                                                           \
      _Pragma("unroll") for (int j = 0; j < (TB); ++j) bf[j] = conv_bfrag(ub + 16 * r + 64 * j, ush); \
      _Pragma("unroll") for (int e = 0; e < (TB); ++e) {                                       \
        const bf16x8 a = KFRAG(128 * e - 32 * r);                                              \
        _Pragma("unroll") for (int T = (e > (TA) ? e : (TA)); T < (TB); ++T) acc[T] = MFMA16(a, bf[T - e], acc[T]); \
      }                                                                                        \
      if (r == 0) { HOOK }                                                                     \
    }                                                                                          \
  }
    BULK(1, 4, );
#pragma unroll
    for (int T = 1; T < 4; ++T) {
      const float y0 = swap_add32(acc[T][0], acc[T][1]), y1 = swap_add32(acc[T][2], acc[T][3]);
      TILE_OUT(T);
    }
    __syncthreads();
    if (w < 6) CHUNK_ST(2 + w);
    SST(8);
    BULK(4, 7, if (w < 6) {
      DRAIN();
      if (l < 8) set_flag(FL + FL_FC + (((4 + 2 * w) * 4 + l) * 8 + (cv >> 3)) * 32 + (cv & 7));
      if (w == 1) (void)(wait_n<SLP_V, 32>(FL + FL_FZ + REP * 64, 64, TMO));
    });
#pragma unroll
    for (int T = 4; T < 7; ++T) {
      const float y0 = swap_add32(acc[T][0], acc[T][1]), y1 = swap_add32(acc[T][2], acc[T][3]);
      TILE_OUT(T);
    }
    __syncthreads();
    if (w >= 2) {
      CHUNK_ST(6 + w);
#pragma unroll
      for (int i = 0; i < 3; ++i) xf[i] = *reinterpret_cast<const v4u*>(Xr + (64 * (8 + 3 * w + i) + l) * 8);
    }
    SST(10);
    {
      const bf16x8 bm = conv_bfrag(ub - 16, ush);
#pragma unroll
      for (int T = 7; T < 16; ++T) far[T - 7] = MFMA16(KFRAG(128 * T + 32), bm, (f32x4{}));
    }
#define FAR_HALF(r, E0, E1)                                                                     \
  {                                                                                            \
    bf16x8 bf[16];                                                                             \
    _Pragma("unroll") for (int j = (8 - (E1) > 0 ? 8 - (E1) : 0); j < 16 - (E0); ++j) {        \
      bf[j] = conv_bfrag(ub1 + 16 * (r) + 64 * j, ush);                                        \
      if (j == 7 && (E1) - (E0) > 8) __builtin_amdgcn_sched_barrier(0);                        \
    }                                                                                          \
    __builtin_amdgcn_sched_barrier(0);                                                         \
    _Pragma("unroll") for (int e = (E0); e < (E1); ++e) {                                      \
      const bf16x8 a = KFRAG(128 * e - 32 * (r));                                              \
      _Pragma("unroll") for (int T = (e > 7 ? e : 7); T < 16; ++T) far[T - 7] = MFMA16(a, bf[T - e], far[T - 7]); \
    }                                                                                          \
  }
#define FAR_J(r, J0, J1)                                                                       \
  {                                                                                            \
    bf16x8 bf[(J1) - (J0)];                                                                    \
    _Pragma("unroll") for (int j = (J0); j < (J1); ++j) bf[j - (J0)] = conv_bfrag(ub1 + 16 * (r) + 64 * j, ush); \
    _Pragma("unroll") for (int e = (8 - (J1) > 0 ? 8 - (J1) : 0); e < 16 - (J0); ++e) {        \
      const bf16x8 a = KFRAG(128 * e - 32 * (r));                                              \
      _Pragma("unroll") for (int j = (J0); j < (J1); ++j)                                      \
        if (e + j >= 7 && e + j < 16) far[(e + j + 2) % 9] = MFMA16(a, bf[j - (J0)], far[(e + j + 2) % 9]); \
    }                                                                                          \
  }
#define FAR_PIECE(p_)                                                                           \
  {                                                                                            \
    const int p = (p_) - 4;                                                                    \
    if (p < 8) {                                                                               \
      const int r_ = 2 + (p & 1);                                                              \
      if (p < -2) FAR_J(r_, 0, 2) else if (p < 0) FAR_J(r_, 2, 4) else if (p < 2) FAR_J(r_, 4, 6) \
      else if (p < 4) FAR_J(r_, 6, 8) else if (p < 6) FAR_J(r_, 8, 11) else FAR_J(r_, 11, 16)  \
    } else if ((p) == 8) {                                                                     \
                     \
      u16* ucw = Uc + w * CUN + 32 + t0;                                                       \
      _Pragma("unroll") for (int T = 7; T < 16; ++T) {                                         \
        const float y0 = swap_add32(far[T - 7][0], far[T - 7][1]), y1 = swap_add32(far[T - 7][2], far[T - 7][3]); \
        const float u0 = bf2f(ucw[128 * T]), u1 = bf2f(ucw[128 * T + 32]);                     \
        const unsigned yy = pk2(y0, y1), uu = pk2(u0 * dsk, u1 * dsk);                         \
        const unsigned aa = pk2(lo16(yy) + lo16(uu), hi16(yy) + hi16(uu));                     \
        ucw[128 * T] = (u16)aa;                                                                \
        ucw[128 * T + 32] = (u16)(aa >> 16);                                                   \
      }                                                                                        \
    } else if (w >= 2) {                                                                       \
      int l = tid & 63;                                                                        \
      asm volatile("" : "+v"(l), "+v"(xf[0]), "+v"(xf[1]), "+v"(xf[2]));                       \
      if ((p) == 9) {                                                                          \
        _Pragma("unroll") for (int i = 0; i < 3; ++i) {                                        \
          const int ck = 8 + 3 * w + i;                                                        \
          const u16* ur = Uc + 32 + 64 * ck + l;                                               \
          v4u o;                                                                               \
          _Pragma("unroll") for (int e = 0; e < 4; ++e)                                        \
            o[e] = pk2(lo16(xf[i][e]) + bf2f(ur[(2 * e) * CUN]), hi16(xf[i][e]) + bf2f(ur[(2 * e + 1) * CUN])); \
          ST128(o, rz1, (l >> 5) * 32768 + (l & 31) * 16, (2 * ck * 64 + cv) * 512);           \
        }                                                                                      \
      } else {                                                                                 \
        DRAIN();                                                                               \
        if (l < 24) set_flag(FL + FL_FC + (((16 + 6 * w) * 4 + l) * 8 + (cv >> 3)) * 32 + (cv & 7)); \
      }                                                                                        \
    }                                                                                          \
  }
#pragma unroll 1
    for (int p = 0; p < 2; ++p) {
      FAR_HALF(p, 0, 16);
      if (p == 0 && w >= 2) {
        DRAIN();
        if (l < 8) set_flag(FL + FL_FC + (((12 + 2 * w) * 4 + l) * 8 + (cv >> 3)) * 32 + (cv & 7));
      }
    }
#undef BULK
    SST(9);
#undef TILE_OUT
#undef CHUNK_ST
#undef U_ROW
#undef U_ROW2
  }
  if (EXP_OFF & 4) {
    for (int pp = 0; pp < 15; ++pp) {
      __syncthreads();
      FAR_PIECE(pp);
    }
    return;
  }
  u16* uc = Uc + (8 + w) * CUN;
  const unsigned* ub = reinterpret_cast<const unsigned*>(uc + (c & ~1) + 8 * g + 32);
#pragma unroll
  for (int j = 0; j < 16; ++j) acc[j] = f32x4{};
  const __amdgpu_buffer_rsrc_t rx = MKRS(ws + OFF_X2, SZ_X), rz = MKRS(ws + OFF_X3, SZ_X);
  const __amdgpu_buffer_rsrc_t rf = MKRS(FL + FL_FR, 64 * 16 * 128);
  unsigned st = 0u;
  v2u xx = {0u, 0u};
#define X2_LOAD(ss, far)                                                                                                      \
  {                                                                                                                      \
    xx = LD64(rx, (l >> 1) * (DM * 2) + (l & 1) * 8, (far) ? (int)SZ_X : ((ss) * SEG * DM + 8 * cv) * 2);                 \
    st = __builtin_amdgcn_raw_buffer_load_b32(rf, (l >> 4) * 128 + ((l >> 1) & 7) * 4, ((ss) * 4 + REP) * 512, AUX_SC1); \
  }
  unsigned tl = now10();
  for (int s = 0; s < NSEG; ++s) {
    if (w == 1) {
      for (unsigned spins = 0; !__all(umin3(xx.x, xx.y, st) != 0u); ++spins) {
        if (spin_timeout(spins, TMO)) break;
        if (GATE_V && spins == 0 && !__any(st != 0u)) gate(tl);
        if (spins && !__all(st != 0u)) __builtin_amdgcn_s_sleep(SLP_V);
        X2_LOAD(s, __builtin_popcountll(__ballot(st != 0u)) < CVTHR);
      }
      tl = now10() + GATE_V;
      TS(0, s, 64);
      const float mb = lo16(st), ri = 1.0f / hi16(st);
      const unsigned t0 = pk2(lo16(xx.x) - mb, hi16(xx.x) - mb), t1 = pk2(lo16(xx.y) - mb, hi16(xx.y) - mb);
      *reinterpret_cast<v2u*>(Su + l * 4) = v2u{pk2(lo16(t0) * ri, hi16(t0) * ri), pk2(lo16(t1) * ri, hi16(t1) * ri)};
      *reinterpret_cast<v2u*>(Sx + l * 4) = xx;
    }
    const int q = s & 3;
    const u16* kq = ka + 32 * q;
    const bf16x8 a0 = *reinterpret_cast<const bf16x8*>(kq - 32), a1 = *reinterpret_cast<const bf16x8*>(kq);
    __syncthreads();
    TS(1, s, 0);
    if (s == 0)
      for (int x = l; x < CUN / 2; x += 64) reinterpret_cast<unsigned*>(uc)[x] = 0u;
    if (l < 32) uc[32 + 32 * s + l] = Su[l * 8 + w];
    asm volatile("s_waitcnt lgkmcnt(0)" ::: "memory");
    const bf16x8 bp = conv_bfrag(ub + 16 * (s - 1), ush);
    acc[0] = MFMA16(a0, bp, (q == 0 ? acc[1] : acc[0]));
    const f32x4 tmp = MFMA16(a1, conv_bfrag(ub + 16 * s, ush), acc[0]);
    float y0 = (q & 1) ? tmp[2] : tmp[0], y1 = (q & 1) ? tmp[3] : tmp[1];
    y0 = xsum32(y0);
    y1 = xsum32(y1);
    if (g == (q >> 1)) {
      const float u0 = bf2f(Su[c * 8 + w]), u1 = bf2f(Su[(16 + c) * 8 + w]);
      const float a0 = bfr(bfr(y0) + bfr(u0 * dsk)), a1 = bfr(bfr(y1) + bfr(u1 * dsk));
      To[c * 8 + w] = f2bf(bf2f(Sx[c * 8 + w]) + a0);
      To[(16 + c) * 8 + w] = f2bf(bf2f(Sx[(16 + c) * 8 + w]) + a1);
    }
    __syncthreads();
    if (w == 0 && l < 32) {
      const v4u o = *reinterpret_cast<const v4u*>(To + l * 8);
      ST128(o, rz, l * 16, (s * 64 + cv) * 512);
    }
    if (w == 1) X2_LOAD(s + 1, CVTHR > 0);
    const int left = 15 - (s >> 2);
#define PUSH(J0, SH)                                                                                              \
  if (left >= (J0)) {                                                                                             \
    _Pragma("unroll") for (int j = (J0); j < (J0) + 4 && j < 16; ++j)                                             \
      acc[j] = MFMA16(*reinterpret_cast<const bf16x8*>(kq - 32 - 128 * j), bp, (j + (SH) < 16 ? acc[(j + (SH)) & 15] : f32x4{})); \
  }
    TS(2, s, 0);
    if (w == 0) {
      DRAIN();
      if (l < 4) set_flag(FL + FL_FC + (((64 + s) * 4 + l) * 8 + (cv >> 3)) * 32 + (cv & 7));
    }
    TS(3, s, 0);
    if (q == 0) { PUSH(1, 1) PUSH(5, 1) PUSH(9, 1) PUSH(13, 1) }
    else { PUSH(1, 0) PUSH(5, 0) PUSH(9, 0) PUSH(13, 0) }
#undef PUSH
    MSTAMP();
    if (s >= CVPRE && s < 15 + CVPRE) FAR_PIECE(s - CVPRE);
  }
#undef KFRAG
#undef X2_LOAD
#undef FAR_PIECE
#undef FAR_HALF
#undef FAR_J
}

__device__ __forceinline__ v4u ln_row_packed(const v4u xr) {
  float v[8];
#pragma unroll
  for (int e = 0; e < 4; ++e) { v[2 * e] = lo16(xr[e]); v[2 * e + 1] = hi16(xr[e]); }
  float s = ((v[0] + v[1]) + (v[2] + v[3])) + ((v[4] + v[5]) + (v[6] + v[7]));
  s = wave_sum(s);
  const float mean = s * (1.0f / DM);
  float q = 0.f;
#pragma unroll
  for (int j = 0; j < 8; ++j) { float t = v[j] - mean; q += t * t; }
  q = wave_sum(q);
  const float var = q * (1.0f / DM);
  const float mb = bfr(mean);
  const float sd = bfr(sqrtf(bfr(bfr(var) + 1e-5f)));
  const float ri = 1.0f / sd;
  v4u o;
#pragma unroll
  for (int e = 0; e < 4; ++e) {
    const unsigned t = pk2(v[2 * e] - mb, v[2 * e + 1] - mb);
    const float x0 = lo16(t), x1 = hi16(t);
    float q0 = x0 * ri, q1 = x1 * ri;
    q0 = __builtin_fmaf(__builtin_fmaf(-q0, sd, x0), ri, q0);
    q1 = __builtin_fmaf(__builtin_fmaf(-q1, sd, x1), ri, q1);
    o[e] = pk2(q0, q1);
  }
  return o;
}

__device__ __forceinline__ float bfr1(float x) { return lo16(pk2(x, x)); }
__device__ __forceinline__ void ln4_packed(v4u (&x)[4]) {
  f32x2_t v[16];
#pragma unroll
  for (int e = 0; e < 16; ++e) v[e] = f32x2_t{lo16(x[e >> 2][e & 3]), hi16(x[e >> 2][e & 3])};
  const f32x2_t t = (((v[0] + v[1]) + (v[2] + v[3])) + ((v[4] + v[5]) + (v[6] + v[7]))) +
                    (((v[8] + v[9]) + (v[10] + v[11])) + ((v[12] + v[13]) + (v[14] + v[15])));
  const float mean = red_mod4(t.x + t.y) * (1.0f / DM);
  const float mb = bfr1(mean);
  f32x2_t q0 = {0.f, 0.f}, q1 = {0.f, 0.f};
#pragma unroll
  for (int e = 0; e < 16; e += 2) {
    v[e] -= mb;
    v[e + 1] -= mb;
    q0 += v[e] * v[e];
    q1 += v[e + 1] * v[e + 1];
  }
  q0 += q1;
  const float dm = mean - mb;
  const float var = fmaxf(red_mod4(q0.x + q0.y) * (1.0f / DM) - dm * dm, 0.f);
  const float sd = bfr1(sqrtf(bfr1(bfr1(var) + 1e-5f)));
  const float ri = 1.0f / sd;
#pragma unroll
  for (int e = 0; e < 16; ++e) {
    const unsigned t2 = pk2(v[e].x, v[e].y);
    const f32x2_t q = f32x2_t{lo16(t2), hi16(t2)} * ri;
    x[e >> 2][e & 3] = pk2(q.x, q.y);
  }
}

extern "C" __device__ float mul1(float, float) __asm("llvm.amdgcn.fmul.legacy");
template <bool G0>
__device__ __forceinline__ void role_qkv_t(const MegaArgs& A, char* smem, const int L, const int h) {
  __builtin_amdgcn_s_waitcnt(0x0F70);
  const int w = __builtin_amdgcn_readfirstlane(threadIdx.x >> 6);
  char* ws = A.ws;
  unsigned* FL = (unsigned*)(ws + OFF_FL);
  unsigned* TMO = FL + FL_TMO;
#define LANE_IDS()                         \
  const int l = lane_id();                 \
  const int c = l & 15, g = l >> 4;        \
  (void)c; (void)g;
  unsigned* FC = FL + FL_FC + (L * 64 * 4 + REP) * 8 * 32;
  const __amdgpu_buffer_rsrc_t rsQ = MKRS(ws + OFF_QK + L * SZ_QK, SZ_QK);
  const __amdgpu_buffer_rsrc_t rsV = MKRS(ws + OFF_VF + L * SZ_O, SZ_O);
  const __amdgpu_buffer_rsrc_t rsM = MKRS(ws + (G0 ? OFF_MEMK : OFF_MEMV) + L * SZ_MEM, SZ_MEM);
  bf16x8 wq[16], wk[16], wv[16];
  {
    LANE_IDS();
    const unsigned sb = lds_base(smem);
    const int q = (l >> 2) & 3, p = l & 3, f = q | ((g & 1) << 2);
    const unsigned ra = sb + (8 * g + q) * 256 + (w ^ f) * 32 + p * 8;
#define W_STAGE(W, hf, VM, NEXT)                                               \
  {                                                                           \
    WAIT_VM(VM);                                                              \
    RAW_BAR();                                                                \
    _Pragma("unroll") for (int t = 0; t < 8; ++t) {                           \
      const unsigned a_ = ra + (hf) * 8192 + t * 16384;                       \
      const v2u r0 = trd(a_), r1 = trd(a_ + 1024);                            \
      W[8 * (hf) + t] = BF8((v4u{r0.x, r0.y, r1.x, r1.y}));                   \
    }                                                                         \
    _Pragma("unroll") for (int t = 0; t < 8; ++t) asm volatile("" : "+v"(W[8 * (hf) + t])); \
    RAW_BAR();                                                                \
    NEXT;                                                                     \
  }
    wqkv_issue<false>(A.Wq, 0, h, w, l, sb);
    wqkv_issue<false>(A.Wq, 1, h, w, l, sb);
    W_STAGE(wq, 0, 8, wqkv_issue<false>(A.Wk, 0, h, w, l, sb));
    SST(4);
    W_STAGE(wq, 1, 8, wqkv_issue<false>(A.Wk, 1, h, w, l, sb));
    SST(5);
    W_STAGE(wk, 0, 8, wqkv_issue<false>(A.Wv, 0, h, w, l, sb));
    W_STAGE(wk, 1, 8, wqkv_issue<false>(A.Wv, 1, h, w, l, sb));
    SST(6);
    W_STAGE(wv, 0, 8, (void)0);
    W_STAGE(wv, 1, 0, (void)0);
#undef W_STAGE
  }
  WSTAMP2();
  constexpr int Q_X = 98304;
  const unsigned sb = lds_base(smem);
  v2u eq, ek, ev;
  {
    LANE_IDS();
    const v4u zt = LD128(MKRS(ws + OFF_TAG, 8192 + 8), l * 16, (blockIdx.x & 7) * 1024);
    const v2u ze = LD64(MKRS(ws + OFF_TAG, 8192 + 8), 0, 8192);
    const v4u one = {0x3f803f80u, 0x3f803f80u, 0x3f803f80u, 0x3f803f80u};
    f32x4 t[3] = {};
#pragma unroll
    for (int ks = 0; ks < 16; ++ks) {
      t[0] = MFMA16(BF8(one), wq[ks], t[0]);
      t[1] = MFMA16(BF8(one), wk[ks], t[1]);
      t[2] = MFMA16(BF8(one), wv[ks], t[2]);
    }
    v2u e[3];
#pragma unroll
    for (int i = 0; i < 3; ++i) {
      const float x = g ? 0.f : t[i][0], x1 = x - bfr1(x);
      e[i] = v2u{pk2(x, x1), pk2(x1 - bfr1(x1), 0.f)};
    }
    eq = e[0]; ek = e[1]; ev = e[2];
    asm volatile("" : "+v"(eq), "+v"(ek), "+v"(ev));
    if (!zero_seen(zt, ze)) zero_wait(ws);
    if (l < 32) *reinterpret_cast<v4u*>(smem + Q_X + w * 2048 + (l >> 4) * 16384 + c * 16) = one;
  }
  constexpr int Q_CNT = Q_X + 11 * 2048 + 256;
  lu32* cnt = (lu32*)(unsigned long)(sb + Q_CNT);
  if (threadIdx.x < 16) cnt[threadIdx.x] = 0u;
#define C_ADD(i) asm volatile("s_mov_b64 exec, 1\n\tds_add_u32 %0, %1\n\ts_mov_b64 exec, -1" ::"v"(sb + Q_CNT + 4 * (i)), "v"(1u) : "memory")
#define C_RD(i) (*(volatile lu32*)(cnt + (i)))
#define C_GET(i) ((unsigned)__builtin_amdgcn_readfirstlane(C_RD(i)))
#define C_WAIT(i, n)                                                                                                    \
  {                                                                                                                     \
    for (unsigned spins = 0; C_GET(i) < (n) && spins < 64u * SPIN_CAP; ++spins) __builtin_amdgcn_s_sleep(1);             \
    asm volatile("" ::: "memory");                                                                                      \
  }
  const char* Xb = ws + (L ? OFF_X3 : OFF_XA) + w * 8192;
  const v4u rsF = mk_srd(FC + w * 64, (64 * 32 - REP * 8 - 2 * w) * 128);
  unsigned l16, vo, fo;
  {
    LANE_IDS();
    l16 = l * 16;
    vo = (c + 16 * (2 * (w & 1) + (g >> 1))) * 16 + 4096 * (g & 1);
    fo = g * 512 + c * 16;
  }
#define DMA4(x) "global_load_lds_dwordx4 %1, " x " sc1\n\tglobal_load_lds_dwordx4 %1, " x " offset:1024 sc1\n\t"       \
                "global_load_lds_dwordx4 %1, " x " offset:2048 sc1\n\tglobal_load_lds_dwordx4 %1, " x " offset:3072 sc1\n\t"
#define X_DMA(ss, bb)                                                                                                   \
  {                                                                                                                     \
    unsigned keep_;                                                                                                     \
    const char* x_ = Xb + (size_t)(ss) * 32768;                                                                         \
    const unsigned d_ = sb + (bb) * 32768 + w * 8192;                                                                   \
    asm volatile("s_nop 4\n\ts_mov_b32 %0, m0\n\ts_mov_b32 m0, %4\n\ts_nop 0\n\t" DMA4("%2")                           \
                 "s_mov_b32 m0, %5\n\ts_nop 0\n\t" DMA4("%3") "s_mov_b32 m0, %0"                                         \
                 : "=&s"(keep_)                                                                                         \
                 : "v"(l16), "s"(x_), "s"(x_ + 4096), "s"(d_), "s"(d_ + 4096u)                                           \
                 : "memory");                                                                                           \
    TS(0, ss, 0);                                                                                                       \
  }
#define X_DMA1(on, x, d, off)                                                                                           \
  {                                                                                                                     \
    unsigned keep_;                                                                                                     \
    asm volatile("s_cmp_eq_u32 %4, 0\n\ts_cbranch_scc1 1f\n\ts_mov_b32 %0, m0\n\ts_mov_b32 m0, %3\n\ts_nop 0\n\t"       \
                 "global_load_lds_dwordx4 %1, %2 offset:%5 sc1\n\ts_mov_b32 m0, %0\n1:"                                  \
                 : "=&s"(keep_)                                                                                         \
                 : "v"(l16), "s"(x), "s"(d), "s"(on), "n"(off)                                                          \
                 : "memory", "scc");                                                                                    \
  }
#define F_LOAD(r, ss)                                                                                                   \
  asm volatile("s_nop 4\n\tbuffer_load_dword %0, %1, %2, %3 offen sc1"                                                  \
               : "=v"(r)                                                                                                \
               : "v"(((l16 >> 2) & 28) + (l16 & 128)), "s"(rsF), "s"((ss) * 4096)                                       \
               : "memory")
  unsigned* FQ = FL + FL_FQ + (L * 64 * 32 + h) * 32;
#define Q_FLAG(ss)                                                                                                      \
  {                                                                                                                     \
    unsigned old_;                                                                                                      \
    asm volatile("s_mov_b64 exec, 1\n\tds_add_rtn_u32 %0, %1, %2\n\ts_mov_b64 exec, -1\n\ts_waitcnt lgkmcnt(0)"           \
                 : "=&v"(old_) : "v"(sb + Q_CNT + ((ss) & 1) * 4), "v"(1u) : "memory");                                 \
    if ((__builtin_amdgcn_readfirstlane(old_) & 7u) == 7u) {                                                            \
      asm volatile("s_mov_b64 exec, 1\n\tglobal_store_dword %0, %1, %2 sc1\n\ts_mov_b64 exec, -1"                        \
                   ::"v"(0), "v"(1u), "s"(FQ + (ss) * 1024) : "memory");                                                \
      TS(3, ss, threadIdx.x & ~63u);                                                                                    \
    }                                                                                                                   \
    pend = false;                                                                                                       \
  }
  __builtin_amdgcn_s_waitcnt(0x0F70);
  RAW_BAR();
  bool have = false, pend = false;
  unsigned tl = now10();
  const unsigned tq = tl - QPL;
  int b = 0;
  unsigned k4 = 4u;
  FSD;
  for (int s = 0; s < NSEG; ++s) {
    FS(0);
    const int b1 = b == 2 ? 0 : b + 1;
    const unsigned u1 = 2u * (b1 ? k4 - 4u : k4);
    unsigned nf;
    if (QPACE && L == 0 && s >= QPS && (int)(tq + s * QPACE - now10()) > 0) {
      if (pend) {
        WAIT_VM(0);
        Q_FLAG(s - 1);
      }
      gate(tq + s * QPACE);
    }
    if (G0) {
      if (!have) {
        for (unsigned spins = 0;; ++spins) {
          unsigned n;
          F_LOAD(n, s);
          WAIT_VM(0);
          if (pend) Q_FLAG(s - 1);
          if (all_set(n) || spin_timeout(spins, TMO)) break;
          if (GATE_Q && spins == 0) gate(tl);
          else if (L) __builtin_amdgcn_s_sleep(SLP_Q);
          else __builtin_amdgcn_s_sleep(SLP_Q1);
        }
        tl = now10() + GATE_Q;
        C_WAIT(8 + b, 2u * k4 - 8u);
        X_DMA(s, b);
        WAIT_VM(0);
        C_ADD(4 + b);
      }
      const unsigned c0 = C_RD(4 + b);
      F_LOAD(nf, s + 1);
      if ((unsigned)__builtin_amdgcn_readfirstlane(c0) < k4) C_WAIT(4 + b, k4);
    } else {
      if (C_GET(4 + b) < k4) {
        if (pend) {
          WAIT_VM(0);
          Q_FLAG(s - 1);
        }
        C_WAIT(4 + b, k4);
      }
      FS(1);
      const char* sp = smem + ((l16 & 128) ? Q_X + ((w + 1) & 1) * 128 : b * 32768 + fo + (w & 3) * 128);
      f32x4 gm = {};
      __builtin_amdgcn_s_setprio(3);
      {
        bf16x8 a[10];
#pragma unroll
        for (int ks = 0; ks < 10; ++ks) a[ks] = *reinterpret_cast<const bf16x8*>(sp + 2048 * ks);
        __builtin_amdgcn_sched_barrier(0);
#pragma unroll
        for (int ks = 0; ks < 16; ++ks) {
          gm = MFMA16(a[ks % 10], a[ks % 10], gm);
          if (ks < 6) a[ks] = *reinterpret_cast<const bf16x8*>(sp + 2048 * (10 + ks));
          __builtin_amdgcn_sched_barrier(0);
        }
      }
      float q2 = (l16 & 16) ? gm[1] : gm[0], q3 = (l16 & 16) ? gm[3] : gm[2];
      asm volatile("" : "+v"(q2), "+v"(q3));
      q2 = (l16 & 32) ? q3 : q2;
      auto sw = __builtin_amdgcn_permlane32_swap(__float_as_uint(q2), __float_as_uint(q2), false, false);
      const float mean = __uint_as_float(sw[1]) * (-1.0f / DM);
      const float var = fmaxf(__builtin_fmaf(-mean, mean, q2 * (1.0f / DM)), 0.f);
      const float sd = bfr1(__builtin_amdgcn_sqrtf(bfr1(bfr1(var) + 1e-5f)));
      asm volatile("s_mov_b64 exec, 0xf0000f\n\tds_write2_b32 %0, %1, %2 offset1:32\n\ts_mov_b64 exec, -1"
                   ::"v"(sb + Q_X + (8 + b) * 2048 + 256 + (w & 3) * 32 + ((l16 >> 2) & 28)), "v"(pk2(mean, mean)),
                   "v"(__builtin_amdgcn_rcpf(sd)) : "memory");
      C_ADD(12 + b);
      FS(2);
      if (pend) {
        WAIT_VM(0);
        Q_FLAG(s - 1);
      }
    }
    TS(G0 ? 1 : 5, s, G0 ? 0 : 256);
    FS(3);
    const char* ap = smem + b * 32768 + fo;
#define AFRAG(i) (*reinterpret_cast<const bf16x8*>(ap + ((i) >> 4) * 256 + 2048 * ((i) & 15)))
    bf16x8 abuf[QPF + 1];
#pragma unroll
    for (int i = 0; i < QPF; ++i) abuf[i] = AFRAG(i);
    f32x4 aq[2] = {}, ak[2] = {}, av[2] = {};
    unsigned cx = 0u;
    int on = 0;
    const char* xn = Xb + (size_t)(s + 1) * 32768;
    const unsigned dn = sb + b1 * 32768 + w * 8192;
    __builtin_amdgcn_s_setprio(G0 ? 1 : 0);
#pragma unroll
    for (int rt = 0; rt < 2; ++rt) {
#pragma unroll
      for (int ks = 0; ks < 16; ++ks) {
        const int i = 16 * rt + ks;
        if (i + QPF < 32) abuf[(i + QPF) % (QPF + 1)] = AFRAG(i + QPF);
        if (G0 && i == 11) cx = C_RD(8 + b1);
        if (i == 28) cx = C_RD(12 + b);
        const bf16x8 a = abuf[i % (QPF + 1)];
        aq[rt] = MFMA16(wq[ks], a, aq[rt]);
        ak[rt] = MFMA16(wk[ks], a, ak[rt]);
        av[rt] = MFMA16(a, wv[ks], av[rt]);
        if (i == QUP) { __builtin_amdgcn_s_setprio(G0 ? 3 : 2); FS(4); }
        __builtin_amdgcn_sched_barrier(0);
        if (G0 && rt == 1 && ks < 8) {
          X_DMA1(on, xn + (ks >> 2) * 4096, dn + (ks >> 2) * 4096, (ks & 3) * 1024);
          __builtin_amdgcn_sched_barrier(0);
        }
      }
      asm volatile("" : "+v"(aq[rt]), "+v"(ak[rt]), "+v"(av[rt]));
      if (rt == 0) FS(5); else FS(7);
      if (G0 && rt == 0) {
        WAIT_VM(0);
        if (pend) Q_FLAG(s - 1);
        have = all_set(nf) && (unsigned)__builtin_amdgcn_readfirstlane(cx) >= u1;
        F_LOAD(nf, have ? NSEG : s + 1);
        if (have) TS(0, s + 1, 0);
        on = __builtin_amdgcn_readfirstlane((int)have);
        FS(6);
      }
    }
    if ((unsigned)__builtin_amdgcn_readfirstlane(cx) < k4) C_WAIT(12 + b, k4);
    FS(8);
    v4u vfrag, qf4, kf4;
    {
      const char* stp = smem + Q_X + (8 + b) * 2048 + 256;
      unsigned nm[2];
      float ri[2];
      f32x4 rv[2];
#pragma unroll
      for (int rt = 0; rt < 2; ++rt) {
        nm[rt] = *reinterpret_cast<const unsigned*>(stp + 64 * rt + ((l16 >> 2) & 60));
        ri[rt] = *reinterpret_cast<const float*>(stp + 128 + 64 * rt + ((l16 >> 2) & 60));
        rv[rt] = *reinterpret_cast<const f32x4*>(stp + 128 + 64 * rt + ((l16 >> 4) & 48));
      }
#pragma unroll
      for (int rt = 0; rt < 2; ++rt) {
        const v2u xm = {nm[rt], nm[rt]};
        aq[rt] = MFMA16K(eq, xm, aq[rt]);
        ak[rt] = MFMA16K(ek, xm, ak[rt]);
        av[rt] = MFMA16K(xm, ev, av[rt]);
      }
      __builtin_amdgcn_s_setprio(QEP);
#pragma unroll
      for (int rt = 0; rt < 2; ++rt) {
        f32x4 tq, tk, tv;
#pragma unroll
        for (int e = 0; e < 4; ++e) {
          tq[e] = mul1(aq[rt][e], ri[rt]);
          tk[e] = mul1(ak[rt][e], ri[rt]);
          tv[e] = mul1(av[rt][e], rv[rt][e]);
        }
        qf4[2 * rt] = pk2(tq[0], tq[1]); qf4[2 * rt + 1] = pk2(tq[2], tq[3]);
        kf4[2 * rt] = pk2(tk[0], tk[1]); kf4[2 * rt + 1] = pk2(tk[2], tk[3]);
        vfrag[2 * rt] = pk2(tv[0], tv[1]); vfrag[2 * rt + 1] = pk2(tv[2], tv[3]);
      }
    }
    {
      auto q0 = __builtin_amdgcn_permlane16_swap(qf4.x, qf4.z, false, false);
      auto q1 = __builtin_amdgcn_permlane16_swap(qf4.y, qf4.w, false, false);
      auto k0 = __builtin_amdgcn_permlane16_swap(kf4.x, kf4.z, false, false);
      auto k1 = __builtin_amdgcn_permlane16_swap(kf4.y, kf4.w, false, false);
      v4u qo = {q0[0], q1[0], q0[1], q1[1]}, ko = {k0[0], k1[0], k0[1], k1[1]};
      const int soff = ((s * NH + h) * 16 + (w >> 1)) * 1024;
      asm volatile("" : "+v"(qo), "+v"(ko), "+v"(vfrag));
      FS(9);
      C_ADD(8 + b);
      if (G0 && have) {
        WAIT_VM(0);
        C_ADD(4 + b1);
      }
      TS(G0 ? 2 : 6, s, G0 ? 0 : 256);
      FS(10);
      ST128(ko, rsQ, vo, soff + 8192);
      ST128(qo, rsQ, vo, soff);
      ST128(vfrag, rsV, l16, ((s * NH + h) * 512 + w * 64) * 16);
      ST64((v2u{0u, 0u}), rsM, l16 >> 1, (s * NH + h) * 2048 + (w & 3) * 512);
      pend = true;
      FS(11);
    }
    if (G0) {
      WAIT_VM(4);
      if (all_set(nf) && C_GET(8 + b1) >= u1) {
        X_DMA(s + 1, b1);
        have = true;
        WAIT_VM(8);
        Q_FLAG(s);
        WAIT_VM(0);
        C_ADD(4 + b1);
      }
    }
    MSTAMP();
    FD(s);
    b = b1;
    if (b == 0) k4 += 4u;
  }
  DRAIN();
  if (pend) Q_FLAG(NSEG - 1);
#undef LANE_IDS
#undef DMA4
#undef X_DMA
#undef X_DMA1
#undef F_LOAD
#undef Q_FLAG
#undef AFRAG
#undef C_ADD
#undef C_RD
#undef C_GET
#undef C_WAIT
}
__device__ __forceinline__ void role_qkv(const MegaArgs& A, char* smem, const int L, const int h) {
  if (__builtin_amdgcn_readfirstlane(threadIdx.x >> 6) < 4) role_qkv_t<true>(A, smem, L, h);
  else role_qkv_t<false>(A, smem, L, h);
}

constexpr int MEGA_WGS = 256;
constexpr size_t MEGA_LDS = (size_t)(32 * 4 * 64 * 8 + 8 * SMS + 15 * QS + 128 + 16 * 64 * 8 + 8 * SOS + 512) * 2;
static_assert(MEGA_LDS <= 163840, "lds");
static_assert(MEGA_LDS >= (size_t)AW_CNT + 64, "lds");
static_assert(MEGA_LDS >= (size_t)(8 * 2 * CKN + 8 * 2 * CUN + 4 * 32 * 8) * 2, "lds");
static_assert(MEGA_LDS >= (size_t)2 * 32 * SMS * 2 && MEGA_LDS >= 131072, "lds");
static_assert(MEGA_LDS >= (size_t)(40 * NST + 32 * 400 + 4 * 392) * 4, "lds");
__global__ __launch_bounds__(512, 2) void mega_kernel(const u16* mem0, const u16* Dskip, char* ws, u16* out, const u16* x0,
                                                      const u16* logAre, const u16* Aim, const u16* Cre, const u16* Cim,
                                                      const u16* logdt, const u16* Wq, const u16* Wk, const u16* Wv,
                                                      const u16* Wo) {
  extern __shared__ __attribute__((aligned(16))) char smem[];
  const int b = __builtin_amdgcn_readfirstlane(role_no());
  const MegaArgs A{mem0, Dskip, ws, out, x0, logAre, Aim, Cre, Cim, logdt, Wq, Wk, Wv, Wo, b};
  {
    const int hi = (b >= 32 && b < 96) ? b - 32 : (b >= 200 && b < 248) ? b - 136 : (b >= 96 && b < 104) ? b + 16 : (b >= 248 ? b - 128 : -1);
    if (hi >= 0) start_kgen(A, smem, hi, b - 248);
  }
  if ((EXP_OFF & 1) && b >= 64 && b < 80) return;
  if ((EXP_OFF & 8) && b >= 200 && b < 232) return;
  if ((EXP_OFF & 16) && b >= 32 && b < 64) return;
  if ((EXP_OFF & 32) && b >= 80 && b < 96) return;
  if (b < 64) role_chain(A, smem, b >> 5, b & 31, 0);
  else if (b < 104 || (b >= 232 && b < 240)) role_aw(A, smem, b < 80 ? 0 : b < 96 ? 2 : 1, b & 15);
  else if (b < 168) role_conv(A, smem, b - 104);
  else if (b < 232) role_qkv(A, smem, (b - 168) >> 5, (b - 168) & 31);
  else role_red(A, smem, b - 240);
}

extern "C" void kernel_launch(void* const* d_in, const int* in_sizes, int n_in, void* d_out, int out_size, void* d_ws,
                              size_t ws_size, hipStream_t stream) {
  MegaArgs A;
  A.x0 = (const u16*)d_in[0];
  A.logAre = (const u16*)d_in[1];
  A.Aim = (const u16*)d_in[2];
  A.Cre = (const u16*)d_in[3];
  A.Cim = (const u16*)d_in[4];
  A.logdt = (const u16*)d_in[5];
  A.Dskip = (const u16*)d_in[6];
  A.mem0 = (const u16*)d_in[8];
  A.Wq = (const u16*)d_in[9];
  A.Wk = (const u16*)d_in[10];
  A.Wv = (const u16*)d_in[11];
  A.Wo = (const u16*)d_in[12];
  A.ws = (char*)d_ws;
  A.out = (u16*)d_out;

  (void)hipFuncSetAttribute((const void*)mega_kernel, hipFuncAttributeMaxDynamicSharedMemorySize, (int)MEGA_LDS);
  mega_kernel<<<MEGA_WGS, 512, MEGA_LDS, stream>>>(A.mem0, A.Dskip, A.ws, A.out, A.x0, A.logAre, A.Aim, A.Cre, A.Cim, A.logdt,
                                                   A.Wq, A.Wk, A.Wv, A.Wo);
}
```

```cpp
#include <hip/hip_runtime.h>
#include <hip/hip_bf16.h>
#include <stdint.h>

constexpr int L_SEQ = 2048;
constexpr int DM    = 512;
constexpr int NST   = 128;
constexpr int NH    = 32;
constexpr int DH    = 128;
constexpr int HD    = NH * DH;
constexpr int MM    = 8;
constexpr int SEG   = 32;
constexpr int NSEG  = L_SEQ / SEG;
constexpr int LK    = MM + SEG;

typedef unsigned short u16;
using bf16x8 = __attribute__((ext_vector_type(8))) short;
using f32x4  = __attribute__((ext_vector_type(4))) float;
using f32x16 = __attribute__((ext_vector_type(16))) float;

static __device__ __forceinline__ u16 f2bf(float f) {
  unsigned u = __float_as_uint(f);
  u += 0x7fffu + ((u >> 16) & 1u);
  return (u16)(u >> 16);
}
static __device__ __forceinline__ float bf2f(u16 h) { return __uint_as_float(((unsigned)h) << 16); }
static __device__ __forceinline__ float bfr(float f) { return bf2f(f2bf(f)); }

constexpr size_t SZ_X      = (size_t)L_SEQ * DM * 2;
constexpr size_t OFF_KF    = 0;
constexpr size_t OFF_UT    = OFF_KF + (size_t)DM * L_SEQ * 4;
constexpr size_t OFF_YT    = OFF_UT + SZ_X;
constexpr size_t OFF_XA    = OFF_YT + SZ_X;
constexpr size_t OFF_H     = OFF_XA + SZ_X;
constexpr size_t OFF_X2    = OFF_H + SZ_X;
constexpr size_t OFF_U2    = OFF_X2 + SZ_X;
constexpr size_t OFF_X3    = OFF_U2 + SZ_X;
constexpr size_t SZ_QK     = (size_t)L_SEQ * 2 * HD * 2;
constexpr size_t OFF_QK    = OFF_X3 + SZ_X;
constexpr size_t SZ_O      = (size_t)L_SEQ * HD * 2;
constexpr size_t OFF_VF    = OFF_QK + 2 * SZ_QK;
constexpr size_t SZ_PW     = (size_t)2 * NSEG * 16 * SEG * DM * 2;
constexpr size_t OFF_PW    = OFF_VF + 2 * SZ_O;
constexpr size_t SZ_MEM    = (size_t)NSEG * MM * HD * 2;
constexpr size_t OFF_MEMK  = OFF_PW + SZ_PW;
constexpr size_t OFF_MEMV  = OFF_MEMK + 2 * SZ_MEM;
constexpr size_t OFF_GT    = OFF_MEMV + 2 * SZ_MEM;
constexpr size_t SZ_ACC    = (size_t)2 * 32768;
constexpr size_t OFF_Z     = OFF_GT + (size_t)64 * DM * 4;
constexpr size_t OFF_ACC   = OFF_Z;
constexpr size_t OFF_FL    = OFF_ACC + 2 * SZ_ACC;
constexpr int FL_FW  = 0;
constexpr int FL_FR  = FL_FW + 2 * 64 * 16 * 32;
constexpr int FL_FC  = FL_FR + 64 * 16 * 32;
constexpr int FL_FQ  = FL_FC + 2 * 64 * 32 * 32;
constexpr int FL_FS  = FL_FQ + 2 * 64 * 32 * 32;
constexpr int FL_FZ  = FL_FS + 4 * 32 * 32;
constexpr int FL_KG  = FL_FZ + 8 * 32;
constexpr int FL_G   = FL_KG + 64 * 32;
constexpr int FL_TK  = FL_G + 4 * 4 * 32;
constexpr int FL_TMO = FL_TK + 2 * 256;
constexpr int FL_END = FL_TMO + 64;
constexpr size_t Z_BYTES   = OFF_FL + (size_t)FL_END * 4 - OFF_Z;
constexpr size_t OFF_TAG   = OFF_Z + Z_BYTES;
constexpr unsigned long long TAG_STEP = 0x9e3779b97f4a7c15ull;
constexpr size_t OFF_G0    = OFF_TAG + 8 * 1024 + 256;
constexpr size_t WS_TOTAL  = OFF_G0 + 8192;
static_assert(Z_BYTES % 256 == 0 && OFF_Z % 256 == 0 && Z_BYTES <= 2 * 128 * 8192, "zero region");
#define REP ((blockIdx.x >> 1) & 3)
static_assert(WS_TOTAL + (4 << 20) <= (size_t)256 * 1024 * 1024, "workspace too large");

constexpr int CKN = 2208;
static_assert((size_t)DM * 2 * CKN * 2 <= (size_t)DM * L_SEQ * 4 + SZ_X, "K buffer");
__device__ __forceinline__ float pos_of_index(int p) {
  if (p < 256) return (float)p;
  if (p < 384) return (float)(256 + 2 * (p - 256));
  if (p < 512) return (float)(512 + 4 * (p - 384));
  if (p < 640) return (float)(1024 + 8 * (p - 512));
  return 2048.0f;
}
__device__ __forceinline__ int index_of_l(int l) {
  float pf = bfr((float)l);
  int p = (int)pf;
  if (p < 256) return p;
  if (p < 512) return 256 + ((p - 256) >> 1);
  if (p < 1024) return 384 + ((p - 512) >> 2);
  if (p < 2048) return 512 + ((p - 1024) >> 3);
  return 640;
}

template <int CTRL> __device__ __forceinline__ float dppr(float x) {
  return __builtin_bit_cast(float, __builtin_amdgcn_update_dpp(0, __builtin_bit_cast(int, x), CTRL, 0xf, 0xf, false));
}
__device__ __forceinline__ float wave_sum(float v) {
  v += dppr<0x121>(v);
  v += dppr<0x122>(v);
  v += dppr<0x124>(v);
  v += dppr<0x128>(v);
  {
    auto r = __builtin_amdgcn_permlane16_swap(__float_as_uint(v), __float_as_uint(v), false, false);
    v = __uint_as_float(r[0]) + __uint_as_float(r[1]);
  }
  {
    auto r = __builtin_amdgcn_permlane32_swap(__float_as_uint(v), __float_as_uint(v), false, false);
    v = __uint_as_float(r[0]) + __uint_as_float(r[1]);
  }
  return v;
}
__device__ __forceinline__ void ln_row8(float (&v)[8]) {
  float s = 0.f;
#pragma unroll
  for (int j = 0; j < 8; ++j) s += v[j];
  s = wave_sum(s);
  const float mean = s * (1.0f / DM);
  float q = 0.f;
#pragma unroll
  for (int j = 0; j < 8; ++j) { float t = v[j] - mean; q += t * t; }
  q = wave_sum(q);
  const float var = q * (1.0f / DM);
  const float mb = bfr(mean);
  const float sd = bfr(sqrtf(bfr(bfr(var) + 1e-5f)));
#pragma unroll
  for (int j = 0; j < 8; ++j) v[j] = bfr(bfr(v[j] - mb) / sd);
}

#define RLX_AGENT __ATOMIC_RELAXED, __HIP_MEMORY_SCOPE_AGENT
typedef __attribute__((address_space(1))) unsigned gu32;
typedef unsigned v4u __attribute__((ext_vector_type(4)));
constexpr int AUX_SC1 = 16;
constexpr unsigned SPIN_CAP = 1u << 18;

__device__ __forceinline__ bool wait_flags(const unsigned* flags, int lane, int nfl, unsigned want, unsigned* tmo) {
  for (unsigned spins = 0;; ++spins) {
    unsigned v = want;
    if (lane < nfl) v = __hip_atomic_load((gu32*)flags + lane * 32, RLX_AGENT);
    if (__all(v >= want)) break;
    if (spins > SPIN_CAP) {
      if (lane == 0) __hip_atomic_store((gu32*)tmo, 1u, RLX_AGENT);
      return false;
    }
  }
  __builtin_amdgcn_fence(__ATOMIC_ACQUIRE, "wavefront");
  return true;
}

#define STAMP(i)
#define STAMP_DUMP()
#define WSTAMP(i)
template <int CTRL> __device__ __forceinline__ float dppf(float old, float x) {
  return __builtin_bit_cast(float, __builtin_amdgcn_update_dpp(__builtin_bit_cast(int, old), __builtin_bit_cast(int, x), CTRL, 0xf, 0xf, false));
}
__device__ __forceinline__ float wave_max_dpp(float x) {
  const float ninf = -3e38f;
  x = fmaxf(x, dppf<0x111>(ninf, x));
  x = fmaxf(x, dppf<0x112>(ninf, x));
  x = fmaxf(x, dppf<0x114>(ninf, x));
  x = fmaxf(x, dppf<0x118>(ninf, x));
  x = fmaxf(x, dppf<0x142>(ninf, x));
  x = fmaxf(x, dppf<0x143>(ninf, x));
  return __builtin_bit_cast(float, __builtin_amdgcn_readlane(__builtin_bit_cast(int, x), 63));
}
__device__ __forceinline__ float wave_sum_dpp(float x) {
  x += dppf<0x111>(0.f, x);
  x += dppf<0x112>(0.f, x);
  x += dppf<0x114>(0.f, x);
  x += dppf<0x118>(0.f, x);
  x += dppf<0x142>(0.f, x);
  x += dppf<0x143>(0.f, x);
  return __builtin_bit_cast(float, __builtin_amdgcn_readlane(__builtin_bit_cast(int, x), 63));
}
constexpr int SMS = 528;
constexpr int QS = 144;
constexpr int VS = 80;
constexpr int SOS = 144;
#define VAR_NREP 4
constexpr int NREP = VAR_NREP;
__device__ __forceinline__ float xsum16(float x) {
  auto r = __builtin_amdgcn_permlane16_swap(__float_as_uint(x), __float_as_uint(x), false, false);
  return __uint_as_float(r[0]) + __uint_as_float(r[1]);
}
__device__ __forceinline__ float xsum32(float x) {
  auto r = __builtin_amdgcn_permlane32_swap(__float_as_uint(x), __float_as_uint(x), false, false);
  return __uint_as_float(r[0]) + __uint_as_float(r[1]);
}
__device__ __forceinline__ float xmax16(float x) {
  auto r = __builtin_amdgcn_permlane16_swap(__float_as_uint(x), __float_as_uint(x), false, false);
  return fmaxf(__uint_as_float(r[0]), __uint_as_float(r[1]));
}
__device__ __forceinline__ float xmax32(float x) {
  auto r = __builtin_amdgcn_permlane32_swap(__float_as_uint(x), __float_as_uint(x), false, false);
  return fmaxf(__uint_as_float(r[0]), __uint_as_float(r[1]));
}
__device__ __forceinline__ float red_mod4(float x) {
  x += dppf<0x124>(0.f, x);
  x += dppf<0x128>(0.f, x);
  return xsum32(xsum16(x));
}
typedef __bf16 bf16x2_t __attribute__((ext_vector_type(2)));
typedef float f32x2_t __attribute__((ext_vector_type(2)));
__device__ __forceinline__ unsigned pk2(float a, float b) {
  f32x2_t v = {a, b};
  bf16x2_t r = __builtin_convertvector(v, bf16x2_t);
  return __builtin_bit_cast(unsigned, r);
}
__device__ __forceinline__ float dot2sq(const unsigned x, const float acc) {
  return __builtin_amdgcn_fdot2_f32_bf16(__builtin_bit_cast(bf16x2_t, x), __builtin_bit_cast(bf16x2_t, x), acc, false);
}
__device__ __forceinline__ float lo16(unsigned u) { return __uint_as_float(u << 16); }
__device__ __forceinline__ float hi16(unsigned u) { return __uint_as_float(u & 0xffff0000u); }
__device__ __forceinline__ void scale_pair(float a, float b, float scale, float& oa, float& ob) {
  unsigned u = pk2(a, b);
  unsigned v = pk2(lo16(u) * scale, hi16(u) * scale);
  oa = lo16(v);
  ob = hi16(v);
}

#define XSPLIT 3
__device__ __forceinline__ int role_no() {
  const int b = blockIdx.x, x = b & 7, j = b >> 3;
  if (XSPLIT >= 3) {
    const int y = XSPLIT == 5 ? x & 3 : x >> 1;
    if (XSPLIT == 5 ? x < 4 : !(x & 1)) {
      if (j >= 16) return 104 + 16 * y + j - 16;
      return XSPLIT == 4 ? 16 * y + j : (j & 8) * 4 + 8 * y + (j & 7);
    }
    if (j < 16) return 168 + (j & 8) * 4 + 8 * y + (j & 7);
    const int i = 4 * y + (j & 3);
    return j < 20 ? 64 + i : j < 24 ? (i < 8 ? 96 + i : 224 + i) : j < 28 ? 80 + i : 240 + i;
  }
  const int L = x >> 2, xx = x & 3;
  if (j < 8) return 32 * L + 8 * xx + j;
  if (j < 12) return 64 + 16 * L + 4 * xx + j - 8;
  if (j < 16) { const int i = 4 * xx + j - 12; return L ? 240 + i : i < 8 ? 96 + i : 224 + i; }
  if (j < 24) return 104 + 8 * x + j - 16;
  return 168 + 32 * L + 8 * xx + j - 24;
}
#define MSTAMP()
#define WSTAMP2()
#define SST(k)
#define TSR(rn, k, ss, t)
#define TS(k, ss, t)
#define FSD
#define FS(k)
#define FD(ss)
struct MegaArgs {
  const u16* mem0;
  const u16* Dskip;
  char* ws;
  u16* out;
  const u16* x0;
  const u16 *logAre, *Aim, *Cre, *Cim, *logdt;
  const u16 *Wq, *Wk, *Wv, *Wo;
  int rn;
};

__device__ __forceinline__ int lane_id() {
  int l;
  asm volatile("v_mbcnt_lo_u32_b32 %0, -1, 0\n\tv_mbcnt_hi_u32_b32 %0, -1, %0" : "=v"(l));
  return l;
}
__device__ __forceinline__ bool spin_timeout(unsigned spins, unsigned* tmo) {
  if (spins > SPIN_CAP) {
    if (lane_id() == 0) __hip_atomic_store((gu32*)tmo, 1u, RLX_AGENT);
    return true;
  }
  return false;
}
#define PSWH (h & 2)
#define CD 146
#define GP 230
#define GR 50
#define GM 0
#define ROT (4 * (h & 1))
#define CPRIO 1
#define P1PRIO 3
#define QLEAD 2
#define POLL_SLEEP 12
#define SLP_C POLL_SLEEP
#define SLP_A POLL_SLEEP
#define SLP_W POLL_SLEEP
#define SLP_R POLL_SLEEP
#define SLP_V POLL_SLEEP
#define SLP_Q POLL_SLEEP
#define SLP_M 4
#define AWPRIO 2
#define SLP_S 8
#define SPOS 4
#define CVPRE 0
#define QHV 0
#define QCHK 0
#define LEADN 99
#define GATE_V 160
#define GATE_R 380
#define GATE_N 440
#define AWAHEAD 1
#define GATE_M 150
#define GATE_M2 380
#define QPACE 0
#define QPS 6
#define QPL 100
#define GATE_Q 170
#define SLP_Q1 24
#define QPF 3
#define QUP 7
#define QEP 3
#define AWD 11
#define GPER 226
#define GMA 60
#define GMR 60
#define GSTEP 30
#define GATE_RT 200
#define GATE_1 160
#define N1S 20
#define N1E 36
#define PRI_A 3
#define PRI_WA 2
#define PRI_WB 1
#define AWSKIP 3
#define PRI_0 3
#define AWDEF 1
#define SLP_F 2
#define EXP_OFF 0
#define AWSPEC 1
#define CVTHR 48
#define NR_C 8
#define NR_R 8
#define NR_W 8
#define NR_N 2
#define QAHEAD 0
#define NS0 4
#define SLP_N POLL_SLEEP
template <int SLP, int WPL>
__device__ __forceinline__ bool wait_n(const unsigned* f, int n, unsigned* tmo) {
  const int l = lane_id();
  for (unsigned spins = 0;; ++spins) {
    unsigned v = 1u;
    if (l < n) v = __hip_atomic_load((gu32*)f + (l / WPL) * 32 + l % WPL, RLX_AGENT);
    if (__all(v != 0u)) break;
    if (spin_timeout(spins, tmo)) return false;
    __builtin_amdgcn_s_sleep(SLP);
  }
  asm volatile("" ::: "memory");
  return true;
}
__device__ __forceinline__ unsigned now10() { return (unsigned)__builtin_amdgcn_s_memrealtime(); }
__device__ __forceinline__ void gate(const unsigned t) {
  for (int i = 0; i < 64; ++i) {
    const int d = (int)(t - now10());
    if (d <= 0) break;
    if (d > 100) __builtin_amdgcn_s_sleep(32);
    else if (d > 25) __builtin_amdgcn_s_sleep(8);
    else __builtin_amdgcn_s_sleep(2);
  }
}
template <int SLP, int WPL>
__device__ __forceinline__ bool grp_wait(const bool leader, volatile unsigned* sw, const unsigned val, const unsigned* f, int n,
                                         unsigned* tmo, const unsigned t) {
  unsigned spins = 0;
  gate(t);
  if (leader) {
    const int l = lane_id();
    for (;; ++spins) {
      unsigned v = 1u;
      if (l < n) v = __hip_atomic_load((gu32*)f + (l / WPL) * 32 + l % WPL, RLX_AGENT);
      if (__all(v != 0u) || spin_timeout(spins, tmo)) break;
      __builtin_amdgcn_s_sleep(SLP);
    }
    if ((threadIdx.x & 63) == 0) *sw = 2u * val + (spins ? 1u : 0u);
  } else {
    unsigned v;
    for (; (v = __builtin_amdgcn_readfirstlane(*sw)) < 2u * val; ++spins) {
      if (spins > 64u * SPIN_CAP) break;
      __builtin_amdgcn_s_sleep(SLP_F);
    }
    spins = v & 1u;
  }
  asm volatile("" ::: "memory");
  return spins != 0u;
}
template <int SLP, int WPL>
__device__ __forceinline__ bool wg_wait(const unsigned* f, int n, unsigned* tmo) {
  if (threadIdx.x < 64) (void)wait_n<SLP, WPL>(f, n, tmo);
  __syncthreads();
  asm volatile("" ::: "memory");
  return true;
}
__device__ __forceinline__ void set_flag(unsigned* f) { __hip_atomic_store((gu32*)f, 1u, RLX_AGENT); }
#define DRAIN() asm volatile("s_waitcnt vmcnt(0)" ::: "memory")
#define MKRS(ptr, bytes) __builtin_amdgcn_make_buffer_rsrc((void*)(ptr), 0, (int)(bytes), 0x00020000)
typedef unsigned v2u __attribute__((ext_vector_type(2)));
#define LD128(rs, voff, soff) __builtin_amdgcn_raw_buffer_load_b128(rs, voff, soff, AUX_SC1)
#define LD64(rs, voff, soff) __builtin_amdgcn_raw_buffer_load_b64(rs, voff, soff, AUX_SC1)
#define ST128(v, rs, voff, soff)                                              \
  do {                                                                        \
    const v4u st_d_ = (v);                                                    \
    __builtin_amdgcn_raw_buffer_store_b128(st_d_, rs, voff, soff, AUX_SC1);   \
    asm volatile("s_nop 1" ::"v"(st_d_) : "memory");                          \
  } while (0)
#define ST64(v, rs, voff, soff) __builtin_amdgcn_raw_buffer_store_b64(v, rs, voff, soff, AUX_SC1)
#define BF8(x) __builtin_bit_cast(bf16x8, x)
#define MFMA16(a, b, c) __builtin_amdgcn_mfma_f32_16x16x32_bf16(a, b, c, 0, 0, 0)
typedef short bf16x4 __attribute__((ext_vector_type(4)));
#define MFMA16K(a, b, c) \
  __builtin_amdgcn_mfma_f32_16x16x16bf16_1k(__builtin_bit_cast(bf16x4, a), __builtin_bit_cast(bf16x4, b), c, 0, 0, 0)
__device__ __forceinline__ float fmax_nn(float a, float b) { return __builtin_elementwise_maximum(a, b); }
typedef __bf16 bfv8 __attribute__((ext_vector_type(8)));
typedef __bf16 bfv16 __attribute__((ext_vector_type(16)));
typedef unsigned v8u __attribute__((ext_vector_type(8)));
#define SMF(a, b, c, idx) \
  __builtin_amdgcn_smfmac_f32_16x16x64_bf16(__builtin_bit_cast(bfv8, a), __builtin_bit_cast(bfv16, b), c, idx, 0, 0)
__device__ __forceinline__ float swap_add32(float a, float b) {
  auto r = __builtin_amdgcn_permlane32_swap(__float_as_uint(a), __float_as_uint(b), false, false);
  return __uint_as_float(r[0]) + __uint_as_float(r[1]);
}
__device__ __forceinline__ float swap_add16(float a, float b) {
  auto r = __builtin_amdgcn_permlane16_swap(__float_as_uint(a), __float_as_uint(b), false, false);
  return __uint_as_float(r[0]) + __uint_as_float(r[1]);
}
__device__ __forceinline__ v4u smf_fix(const v4u d) {
  auto x = __builtin_amdgcn_permlane16_swap(d.x, d.z, false, false);
  auto y = __builtin_amdgcn_permlane16_swap(d.y, d.w, false, false);
  return v4u{x[0], x[1], y[0], y[1]};
}
__device__ __forceinline__ v8u smf_bfrag(const u16* p) {
  const v2u a = *reinterpret_cast<const v2u*>(p), b = *reinterpret_cast<const v2u*>(p + 8);
  const v2u c = *reinterpret_cast<const v2u*>(p + 32), d = *reinterpret_cast<const v2u*>(p + 40);
  return v8u{a.x, b.x, a.y, b.y, c.x, d.x, c.y, d.y};
}

#define VAR_PPA 0
#define VAR_PPB 0
#define VAR_THR 0
#define VAR_THR2 0
#define CV_LEAD 100
#define VAR_EARLY 1
#define VAR_DRAIN 1
#define VAR_D1 0
#define VAR_D2 0
#define VAR_DR2 1
#define VAR_S1 0
#define VAR_S2 0
typedef __attribute__((address_space(1))) unsigned long long gu64;
typedef __attribute__((address_space(1))) char gch;
__device__ __forceinline__ int cvt_rpi(float x) {
  int r;
  asm("v_cvt_rpi_i32_f32 %0, %1" : "=v"(r) : "v"(x));
  return r;
}
typedef short v4s __attribute__((ext_vector_type(4)));
typedef __attribute__((address_space(3))) v4s* lp4s;
__device__ __forceinline__ void glds16(const void* sbase, const unsigned voff, const unsigned lds) {
  unsigned keep;
  asm volatile("s_nop 4\n\ts_mov_b32 %0, m0\n\ts_mov_b32 m0, %3\n\ts_nop 0\n\tglobal_load_lds_dwordx4 %1, %2\n\ts_mov_b32 m0, %0"
               : "=&s"(keep)
               : "v"(voff), "s"(sbase), "s"(lds)
               : "memory");
}
__device__ __forceinline__ void glds16c(const void* sbase, const unsigned voff, const unsigned lds) {
  unsigned keep;
  asm volatile("s_nop 4\n\ts_mov_b32 %0, m0\n\ts_mov_b32 m0, %3\n\ts_nop 0\n\tglobal_load_lds_dwordx4 %1, %2 sc1\n\ts_mov_b32 m0, %0"
               : "=&s"(keep)
               : "v"(voff), "s"(sbase), "s"(lds)
               : "memory");
}
__device__ __forceinline__ v2u trd(const unsigned a) {
  return __builtin_bit_cast(v2u, __builtin_amdgcn_ds_read_tr16_b64_v4i16((lp4s)(unsigned long)a));
}
__device__ __forceinline__ unsigned lds_base(char* smem) { return (unsigned)(unsigned long)(__attribute__((address_space(3))) char*)smem; }
#define RAW_BAR()                                           \
  do {                                                      \
    asm volatile("s_waitcnt lgkmcnt(0)" ::: "memory");      \
    __builtin_amdgcn_s_barrier();                           \
    asm volatile("" ::: "memory");                          \
  } while (0)
#define WAIT_VM(n) asm volatile("s_waitcnt vmcnt(" #n ")" ::: "memory")
template <bool SP>
__device__ __forceinline__ void wqkv_issue(const u16* Wm, const int hf, const int h, const int w, const int l, const unsigned sb) {
  const char* base = (const char*)Wm + (size_t)(256 * hf + 32 * w) * (HD * 2) + h * (DH * 2);
#pragma unroll
  for (int i = 0; i < 8; ++i) {
    const int r = l >> 4;
    const int f = SP ? ((r & 1) | ((i & 1) << 1) | (((i >> 1) & 1) << 2)) : (r | (((i >> 1) & 1) << 2));
    glds16(base, (unsigned)((4 * i + r) * (HD * 2) + (((l & 15) ^ (f << 1)) << 4)), sb + w * 16384 + hf * 8192 + i * 1024);
  }
}

#define PFW 1
__device__ __forceinline__ void pull_weights(const MegaArgs& A, char* smem, const int slot, const int slots) {
  if (!PFW) return;
  const int x = blockIdx.x & 7, j = blockIdx.x >> 3, h = 8 * (x >> 1) + (j & 7), k = (j >> 3) - 1, l = lane_id();
  const bool odd = x & 1, wo = !odd && k == 1;
  const unsigned voff = wo ? l * 64 : (l >> 2) * (HD * 2) + (l & 3) * 64;
  const unsigned dst = lds_base(smem) + 162816;
  unsigned long long pk = (unsigned long long)A.Wk, pq = (unsigned long long)A.Wq, pv = (unsigned long long)A.Wv, po = (unsigned long long)A.Wo;
  asm volatile("" : "+s"(pk), "+s"(pq), "+s"(pv), "+s"(po));
  for (int i = odd ? slot * 3 + k : slot; (PFW & 1) && i < (odd ? 64 : 32); i += odd ? 3 * slots : slots) {
    const unsigned long long m = odd ? (i < 32 ? pk : pv) : (k == 0 ? pq : pk);
    const unsigned long long b = wo ? po + h * (DH * DM * 2) + i * 4096 : m + (i & 31) * (16 * HD * 2) + h * (DH * 2);
    unsigned keep;
    asm volatile("s_nop 4\n\ts_mov_b32 %0, m0\n\ts_mov_b32 m0, %3\n\ts_nop 0\n\tglobal_load_lds_dword %1, %2\n\ts_mov_b32 m0, %0"
                 : "=&s"(keep)
                 : "v"(voff), "s"(b), "s"(dst)
                 : "memory");
  }
  if (PFW & 2) {
    const unsigned long long b = po + (size_t)h * (DH * DM * 2);
    for (int i = slot * 3 + k; i < 32; i += 3 * slots) {
      unsigned keep;
      asm volatile("s_nop 4\n\ts_mov_b32 %0, m0\n\ts_mov_b32 m0, %3\n\ts_nop 0\n\tglobal_load_lds_dword %1, %2\n\ts_mov_b32 m0, %0"
                   : "=&s"(keep)
                   : "v"(l * 64), "s"(b + i * 4096), "s"(dst)
                   : "memory");
    }
  }
}
__device__ __forceinline__ void wo_issue(const u16* Wo, const int a, const int st, const unsigned sb) {
  const int w = __builtin_amdgcn_readfirstlane(threadIdx.x >> 6), l = lane_id(), c = l & 15, g = l >> 4;
  const char* base = (const char*)Wo + ((size_t)((2 * a + (st >> 1)) * DH + 64 * (w & 1) + 32 * (st & 1)) * DM + 128 * (w >> 1)) * 2;
#pragma unroll
  for (int i = 0; i < 8; ++i) {
    const int fd = g | ((i & 1) << 2);
    glds16(base, (unsigned)((4 * i + g) * (DM * 2) + ((c ^ (fd << 1)) << 4)), sb + (st & 1) * 65536 + w * 8192 + i * 1024);
  }
}
__device__ __forceinline__ bool zero_seen(const v4u t, const v2u e) {
  const unsigned long long m = (((unsigned long long)e.y << 32) | e.x) + TAG_STEP;
  const unsigned lo = (unsigned)m, hi = (unsigned)(m >> 32);
  return __all(((t.x ^ lo) | (t.y ^ hi) | (t.z ^ lo) | (t.w ^ hi)) == 0u);
}
__device__ __forceinline__ void zero_wait(char* ws) {
  const __amdgpu_buffer_rsrc_t rs = MKRS(ws + OFF_TAG, 8192 + 8);
  for (unsigned spins = 0; spins < 16u * SPIN_CAP; ++spins) {
    const v4u t = LD128(rs, lane_id() * 16, (blockIdx.x & 7) * 1024);
    const v2u e = LD64(rs, 0, 8192);
    if (zero_seen(t, e)) break;
    asm volatile("s_sleep 4" ::: "memory");
  }
  asm volatile("" ::: "memory");
}
__device__ __forceinline__ unsigned row_stats(const v4u raw) {
  float v[8];
#pragma unroll
  for (int e = 0; e < 4; ++e) { v[2 * e] = lo16(raw[e]); v[2 * e + 1] = hi16(raw[e]); }
  float s = 0.f;
#pragma unroll
  for (int e = 0; e < 8; ++e) s += v[e];
  const float mean = wave_sum(s) * (1.0f / DM);
  float q = 0.f;
#pragma unroll
  for (int e = 0; e < 8; ++e) { const float t = v[e] - mean; q += t * t; }
  const float var = wave_sum(q) * (1.0f / DM);
  const float sd = bfr(sqrtf(bfr(bfr(var) + 1e-5f)));
  return (unsigned)f2bf(mean) | ((unsigned)f2bf(sd) << 16);
}
__device__ __forceinline__ void start_kgen(const MegaArgs& A, char* smem, const int hi, const int xi, const int aw) {
  const int tid = threadIdx.x, j = tid >> 7, u = tid & 127, d = 4 * hi + j;
  const int w = __builtin_amdgcn_readfirstlane(tid >> 6), l = tid & 63;
  float* prm = reinterpret_cast<float*>(smem) + j * 10 * NST;
  float* part = reinterpret_cast<float*>(smem) + 40 * NST + j * 8 * 400;
  unsigned* kd = reinterpret_cast<unsigned*>(smem + 131072) + j * 392;
  v4u raw[8], ra, rb;
  const int r0 = 16 * xi + 2 * w;
  if (xi >= 0) {
    ra = *reinterpret_cast<const v4u*>(A.x0 + (size_t)r0 * DM + l * 8);
    rb = *reinterpret_cast<const v4u*>(A.x0 + (size_t)(r0 + 1) * DM + l * 8);
  }
  const u16 p0 = A.logdt[d], p1 = A.logAre[d * NST + u], p2 = A.Aim[d * NST + u], p3 = A.Cre[d * NST + u], p4 = A.Cim[d * NST + u];
  if (w >= 6) {
#pragma unroll
    for (int i = 0; i < 8; ++i) raw[i] = *reinterpret_cast<const v4u*>(A.x0 + (size_t)(16 * hi + 8 * (w - 6) + i) * DM + l * 8);
  }
  const v2u en = LD64(MKRS(A.ws + OFF_TAG, 8192 + 8), 0, 8192);
  {
    const __amdgpu_buffer_rsrc_t rz = MKRS(A.ws + OFF_Z, Z_BYTES);
    ST128((v4u{0u, 0u, 0u, 0u}), rz, tid * 16, hi * 8192);
    ST128((v4u{0u, 0u, 0u, 0u}), rz, tid * 16, (128 + hi) * 8192);
  }
  if (xi >= 0) {
    const unsigned sa = row_stats(ra), sb = row_stats(rb), st = (l & 1) ? sb : sa;
    const unsigned long long m = (((unsigned long long)en.y << 32) | en.x) + TAG_STEP;
    ST128((v4u{st, (unsigned)m, st, (unsigned)(m >> 32)}), MKRS(A.ws + OFF_G0, 8192), (l >> 1) * 2048 + (l & 1) * 16, r0 * 16);
    SST(11);
  }
  {
    const float dt = bfr(expf(bf2f(p0)));
    const float ar = -bfr(expf(bf2f(p1))) * dt, ai = bf2f(p2) * dt;
    prm[u] = ar;
    prm[NST + u] = ai;
    prm[2 * NST + u] = bf2f(p3) * dt;
    prm[3 * NST + u] = bf2f(p4) * dt;
#pragma unroll
    for (int cls = 1; cls < 4; ++cls) {
      const float st = (float)(1 << cls);
      const float e = expf(ar * st);
      float sn, cs;
      sincosf(ai * st, &sn, &cs);
      prm[(2 + 2 * cls) * NST + u] = e * cs;
      prm[(3 + 2 * cls) * NST + u] = e * sn;
    }
  }
  __builtin_amdgcn_s_waitcnt(0x0F70);
  __syncthreads();
  SST(1);
  if (w >= 6) {
    if (w == 7 && l < 8)
      __hip_atomic_store((gu64*)(A.ws + OFF_TAG) + l * 128 + hi, (((unsigned long long)en.y << 32) | en.x) + TAG_STEP, RLX_AGENT);
    __builtin_amdgcn_s_setprio(3);
    if (hi >= 32) pull_weights(A, smem, w - 6, 2);
    v4u wr[32];
    const u16* wop = A.Wo + (size_t)(hi * DH + 64 * (w - 6)) * DM + l * 8;
    if (hi < 32) {
#pragma unroll
      for (int i = 0; i < 32; ++i) wr[i] = *reinterpret_cast<const v4u*>(wop + i * DM);
    }
    unsigned stw = 0u;
#pragma unroll
    for (int i = 0; i < 8; ++i) {
      const unsigned t = row_stats(raw[i]);
      if (l == i) stw = t;
    }
    const int sx = 2 * hi + w - 6;
    if (l < 8) __hip_atomic_store((gu32*)(A.ws + OFF_GT) + 8 * sx + l, stw, RLX_AGENT);
    zero_wait(A.ws);
    DRAIN();
    if (l < 4) set_flag((unsigned*)(A.ws + OFF_FL) + FL_FS + (l * 32 + (sx >> 3)) * 32 + (sx & 7));
    if (hi < 32) {
      float c0 = 0.f, c1 = 0.f, c2 = 0.f, c3 = 0.f;
#pragma unroll
      for (int i = 0; i < 64; ++i) {
        const v4u x = wr[i & 31];
        if (i < 32) wr[i] = *reinterpret_cast<const v4u*>(wop + (32 + i) * DM);
        c0 = dot2sq(x.x, c0); c1 = dot2sq(x.y, c1); c2 = dot2sq(x.z, c2); c3 = dot2sq(x.w, c3);
      }
      const float m = wave_max_dpp(fmaxf(fmaxf(c0, c1), fmaxf(c2, c3)));
      if (l < 4) __hip_atomic_store((gu32*)(A.ws + OFF_FL) + FL_G + (l * 4 + w - 5) * 32 + hi, __float_as_uint(m) | 1u, RLX_AGENT);
    }
    __builtin_amdgcn_s_setprio(0);
  } else {
    const int jm = tid / 96, um = tid % 96;
    const int r = um % 12, ng = um / 12;
    const float* prm = reinterpret_cast<float*>(smem) + jm * 10 * NST;
    float* part = reinterpret_cast<float*>(smem) + 40 * NST + jm * 8 * 400;
    const float* pw = prm + (4 + 2 * (r >> 2)) * NST;
    const float p0 = pos_of_index(256 + 32 * r);
    f32x2_t acc[33];
#pragma unroll
    for (int i = 0; i < 33; ++i) acc[i] = f32x2_t{0.f, 0.f};
#pragma unroll 1
    for (int bt = 0; bt < 4; ++bt) {
      const int n = 16 * ng + 4 * bt;
      const f32x4 ar = *reinterpret_cast<const f32x4*>(prm + n), ai = *reinterpret_cast<const f32x4*>(prm + NST + n);
      const f32x4 cr = *reinterpret_cast<const f32x4*>(prm + 2 * NST + n), ci = *reinterpret_cast<const f32x4*>(prm + 3 * NST + n);
      const f32x4 wr = *reinterpret_cast<const f32x4*>(pw + n), wi = *reinterpret_cast<const f32x4*>(pw + NST + n);
      float yr0[4], yi0[4];
#pragma unroll
      for (int k = 0; k < 4; ++k) {
        const float e = __expf(ar[k] * p0);
        float sn, cs;
        __sincosf(ai[k] * p0, &sn, &cs);
        const float zr = e * cs, zi = e * sn;
        yr0[k] = cr[k] * zr - ci[k] * zi;
        yi0[k] = cr[k] * zi + ci[k] * zr;
      }
      f32x2_t yra = {yr0[0], yr0[1]}, yia = {yi0[0], yi0[1]}, wra = {wr[0], wr[1]}, wia = {wi[0], wi[1]};
      f32x2_t yrb = {yr0[2], yr0[3]}, yib = {yi0[2], yi0[3]}, wrb = {wr[2], wr[3]}, wib = {wi[2], wi[3]};
#pragma unroll
      for (int i = 0; i < 33; ++i) {
        acc[i] += yra + yrb;
        const f32x2_t ta = yra * wra - yia * wia, tb = yrb * wrb - yib * wib;
        yia = yra * wia + yia * wra;
        yib = yrb * wib + yib * wrb;
        yra = ta;
        yrb = tb;
      }
    }
#pragma unroll
    for (int i = 0; i < 33; ++i) part[ng * 400 + 33 * r + i] = acc[i].x + acc[i].y;
  }
  __syncthreads();
  SST(2);
  for (int i = u; i < 385; i += 128) {
    const int o = i < 384 ? 33 * (i >> 5) + (i & 31) : 33 * 11 + 32;
    float v = 0.f;
#pragma unroll
    for (int g = 0; g < 8; ++g) v += part[g * 400 + o];
    v *= 2.0f;
    kd[i] = pk2(v, v - lo16(pk2(v, v)));
  }
  if (u == 1) kd[385] = 0u;
  __syncthreads();
  if (aw >= 0) {
    wo_issue(A.Wo, aw, 0, lds_base(smem));
    wo_issue(A.Wo, aw, 1, lds_base(smem));
  }
  const __amdgpu_buffer_rsrc_t rs = MKRS(A.ws + OFF_KF, DM * 2 * CKN * 2);
  for (int un = u; un < 229; un += 128) {
    v4u oh, ol;
#pragma unroll
    for (int e = 0; e < 4; ++e) {
      const int lag = 2080 - (8 * un + 2 * e);
      const unsigned pb = pk2((float)lag, (float)(lag - 1));
      const unsigned k0 = kd[(unsigned)(lag - 256) < L_SEQ - 256u ? (pb & 0xffffu) - 0x4380u : 385u];
      const unsigned k1 = kd[(unsigned)(lag - 257) < L_SEQ - 256u ? (pb >> 16) - 0x4380u : 385u];
      oh[e] = __builtin_amdgcn_perm(k1, k0, 0x05040100u);
      ol[e] = __builtin_amdgcn_perm(k1, k0, 0x07060302u);
    }
    ST128(oh, rs, (d * 2 * CKN + 8 * un) * 2, 0);
    ST128(ol, rs, (d * 2 * CKN + CKN + 8 * un) * 2, 0);
  }
  DRAIN();
  __syncthreads();
  if (tid == 0) set_flag((unsigned*)(A.ws + OFF_FL) + FL_KG + (hi >> 1) * 32 + (hi & 1));
  SST(3);
}

constexpr int C_SM = 131072, C_SO = C_SM + 8192, C_KT = C_SO + 2048, C_X = C_KT + 16384, C_SQ = C_X + 1024;
constexpr int C_SK = C_SQ + (7 * QS + 128) * 2;
static_assert(C_SK + 8 * QS * 2 <= 163840, "lds");
#define PSW 1
#define CHAIN_ASM_ATT \
  "s_mov_b32 s92, 1152\n" \
  "s_mov_b32 s93, 576\n" \
  "s_mov_b32 s94, 2272\n" \
  "s_mov_b32 s95, 272\n" \
  "s_mov_b32 s96, 288\n" \
  "s_mov_b32 s91, 0x40000001\n" \
  "s_mov_b32 s84, 0x4b400000\n" \
  "s_mov_b32 s85, 0x4b400000\n" \
  "s_mov_b32 s78, 0x3e0293ee\n" \
  "s_mov_b32 s79, 0x3fb8aa3b\n" \
  "s_mov_b32 s76, 0xff00ff00\n" \
  "s_mov_b32 s77, 0xff00ff00\n" \
  "s_add_u32 s58, s56, s60\n" \
  "s_addc_u32 s59, s57, 0\n" \
  "s_xor_b32 s38, s35, 0\n" \
  "s_add_u32 s26, s58, s38\n" \
  "s_addc_u32 s27, s59, 0\n" \
  "s_xor_b32 s38, s35, 1024\n" \
  "s_add_u32 s28, s58, s38\n" \
  "s_addc_u32 s29, s59, 0\n" \
  "s_xor_b32 s38, s35, 2048\n" \
  "s_add_u32 s30, s58, s38\n" \
  "s_addc_u32 s31, s59, 0\n" \
  "s_xor_b32 s38, s35, 3072\n" \
  "s_add_u32 s32, s58, s38\n" \
  "s_addc_u32 s33, s59, 0\n" \
  "s_lshl_b32 s98, s73, 10\n" \
  "s_add_u32 s98, s98, s74\n" \
  "s_add_u32 s98, s98, 131072\n" \
  "s_lshl_b32 s97, s73, 5\n" \
  "s_add_u32 s97, s97, s74\n" \
  "s_add_u32 s97, s97, 158720\n" \
  "s_lshl_b32 s99, s73, 10\n" \
  "v_mbcnt_lo_u32_b32 v48, -1, 0\n" \
  "v_mbcnt_hi_u32_b32 v48, -1, v48\n" \
  "v_and_b32 v49, 15, v48\n" \
  "v_lshl_add_u32 v8, v49, 1, s97\n" \
  "v_bfe_u32 v49, v48, 4, 1\n" \
  "v_mad_u32_u24 v8, v49, s92, v8\n" \
  "v_lshrrev_b32 v49, 5, v48\n" \
  "v_mad_u32_u24 v9, v49, s93, v8\n" \
  "v_mad_u32_u24 v8, v49, s94, v8\n" \
  "v_add_u32 v10, s99, v1\n" \
  "s_lshr_b32 s88, s99, 2\n" \
  "s_add_u32 s88, s88, s74\n" \
  "s_add_u32 s88, s88, 139264\n" \
  "v_lshl_add_u32 v7, v49, 10, s88\n" \
  "v_and_b32 v49, 31, v48\n" \
  "v_lshl_add_u32 v7, v49, 3, v7\n" \
  "v_mov_b64 v[12:13], 0\n" \
  "v_mov_b64 v[14:15], 0\n" \
  "v_mov_b64 v[16:17], 0\n" \
  "v_mov_b64 v[18:19], 0\n" \
  "v_mov_b64 v[20:21], 0\n" \
  "v_mov_b64 v[22:23], 0\n" \
  "s_add_u32 s88, s74, 157760\n" \
  "v_mov_b32 v59, s88\n" \
  "v_mov_b32 v24, 0x44870000\n" \
  "v_mul_f32 v24, s72, v24\n" \
  "s_nop 0\n" \
  "v_readfirstlane_b32 s86, v24\n" \
  "s_lshr_b32 s86, s86, 23\n" \
  "s_addk_i32 s86, 0xff83\n" \
  "s_ashr_i32 s86, s86, 1\n" \
  "s_max_i32 s86, s86, -20\n" \
  "s_min_i32 s86, s86, 100\n" \
  "s_mov_b32 s82, s86\n" \
  "s_lshr_b32 s83, s72, 23\n" \
  "s_sub_i32 s83, s83, 242\n" \
  "s_lshl_b32 s88, s73, 10\n" \
  "v_add_u32 v24, s88, v1\n" \
  "ds_read_b128 v[32:35], v24 offset:0\n" \
  "s_waitcnt lgkmcnt(0)\n" \
  "v_mov_b32 v11, 0\n" \
  "v_dot2c_f32_bf16 v11, v32, v32\n" \
  "v_dot2c_f32_bf16 v11, v33, v33\n" \
  "v_dot2c_f32_bf16 v11, v34, v34\n" \
  "v_dot2c_f32_bf16 v11, v35, v35\n" \
  "s_nop 1\n" \
  "s_nop 0\n" \
  "v_cmpx_lt_f32 vcc, 2.0, v11\n" \
  "ds_max_u32 v59, v11\n" \
  "s_mov_b64 exec, -1\n" \
  ".LCA_TOP_%=:\n" \
  "s_waitcnt lgkmcnt(0)\n" \
  "s_barrier\n" \
  "ds_read_b128 v[40:43], v1 offset:0\n" \
  "ds_read_b128 v[44:47], v1 offset:1024\n" \
  "ds_read_b128 v[48:51], v1 offset:2048\n" \
  "ds_read_b128 v[52:55], v1 offset:3072\n" \
  "ds_read_b128 v[56:59], v1 offset:4096\n" \
  "ds_read_b128 v[60:63], v1 offset:5120\n" \
  "s_cmp_lg_u32 s73, 0\n" \
  "s_cbranch_scc1 .LCA_NOZ_%=\n" \
  "s_xor_b32 s88, s60, 0x8000\n" \
  "s_add_u32 s88, s88, s61\n" \
  "buffer_store_dwordx2 v[12:13], v5, s[40:43], s88 offen sc1\n" \
  ".LCA_NOZ_%=:\n" \
  "s_waitcnt lgkmcnt(5)\n" \
  "v_smfmac_f32_16x16x64_bf16 v[20:23], v[40:43], v[192:199], v0\n" \
  "v_smfmac_f32_16x16x64_bf16 v[12:15], v[40:43], v[64:71], v0\n" \
  "v_smfmac_f32_16x16x64_bf16 v[16:19], v[40:43], v[128:135], v0\n" \
  "ds_read_b128 v[40:43], v1 offset:6144\n" \
  "s_waitcnt lgkmcnt(5)\n" \
  "v_smfmac_f32_16x16x64_bf16 v[20:23], v[44:47], v[200:207], v0\n" \
  "v_smfmac_f32_16x16x64_bf16 v[12:15], v[44:47], v[72:79], v0\n" \
  "v_smfmac_f32_16x16x64_bf16 v[16:19], v[44:47], v[136:143], v0\n" \
  "ds_read_b128 v[44:47], v1 offset:7168\n" \
  "s_waitcnt lgkmcnt(5)\n" \
  "v_smfmac_f32_16x16x64_bf16 v[20:23], v[48:51], v[208:215], v0\n" \
  "v_smfmac_f32_16x16x64_bf16 v[12:15], v[48:51], v[80:87], v0\n" \
  "v_smfmac_f32_16x16x64_bf16 v[16:19], v[48:51], v[144:151], v0\n" \
  "ds_read_b128 v[24:27], v1 offset:10240\n" \
  "ds_read_b128 v[28:31], v1 offset:14336\n" \
  "s_waitcnt lgkmcnt(6)\n" \
  "v_smfmac_f32_16x16x64_bf16 v[20:23], v[52:55], v[216:223], v0\n" \
  "v_smfmac_f32_16x16x64_bf16 v[12:15], v[52:55], v[88:95], v0\n" \
  "v_smfmac_f32_16x16x64_bf16 v[16:19], v[52:55], v[152:159], v0\n" \
  "ds_read_b128 v[32:35], v1 offset:11264\n" \
  "ds_read_b128 v[36:39], v1 offset:15360\n" \
  "s_waitcnt lgkmcnt(7)\n" \
  "v_smfmac_f32_16x16x64_bf16 v[20:23], v[56:59], v[224:231], v0\n" \
  "v_smfmac_f32_16x16x64_bf16 v[12:15], v[56:59], v[96:103], v0\n" \
  "v_smfmac_f32_16x16x64_bf16 v[16:19], v[56:59], v[160:167], v0\n" \
  "s_waitcnt lgkmcnt(6)\n" \
  "v_smfmac_f32_16x16x64_bf16 v[20:23], v[60:63], v[232:239], v0\n" \
  "v_smfmac_f32_16x16x64_bf16 v[12:15], v[60:63], v[104:111], v0\n" \
  "v_smfmac_f32_16x16x64_bf16 v[16:19], v[60:63], v[168:175], v0\n" \
  "s_waitcnt lgkmcnt(5)\n" \
  "v_smfmac_f32_16x16x64_bf16 v[20:23], v[40:43], v[240:247], v0\n" \
  "v_smfmac_f32_16x16x64_bf16 v[12:15], v[40:43], v[112:119], v0\n" \
  "v_smfmac_f32_16x16x64_bf16 v[16:19], v[40:43], v[176:183], v0\n" \
  "s_waitcnt lgkmcnt(4)\n" \
  "v_smfmac_f32_16x16x64_bf16 v[20:23], v[44:47], v[248:255], v0\n" \
  "v_smfmac_f32_16x16x64_bf16 v[12:15], v[44:47], v[120:127], v0\n" \
  "v_smfmac_f32_16x16x64_bf16 v[16:19], v[44:47], v[184:191], v0\n" \
  "s_cmp_eq_u32 s71, 0\n" \
  "s_nop 4\n" \
  "v_permlane32_swap_b32 v20, v22\n" \
  "v_permlane32_swap_b32 v21, v23\n" \
  "v_pk_add_f32 v[20:21], v[20:21], v[22:23]\n" \
  "v_permlane32_swap_b32 v12, v16\n" \
  "v_permlane32_swap_b32 v13, v17\n" \
  "v_cvt_pk_bf16_f32 v20, v20, v21\n" \
  "v_pk_add_f32 v[12:13], v[12:13], v[16:17]\n" \
  "v_permlane32_swap_b32 v14, v18\n" \
  "v_permlane32_swap_b32 v15, v19\n" \
  "ds_write_b16 v9, v20 offset:128\n" \
  "v_cvt_pk_bf16_f32 v12, v12, v13\n" \
  "v_pk_add_f32 v[14:15], v[14:15], v[18:19]\n" \
  "ds_write_b16_d16_hi v9, v20 offset:416\n" \
  "ds_write_b16 v8, v12\n" \
  "v_cvt_pk_bf16_f32 v14, v14, v15\n" \
  "ds_write_b16_d16_hi v8, v12 offset:288\n" \
  "ds_write_b16 v8, v14 offset:576\n" \
  "ds_write_b16_d16_hi v8, v14 offset:864\n" \
  "s_setprio 3\n" \
  "s_waitcnt lgkmcnt(0)\n" \
  "s_barrier\n" \
  "s_cbranch_scc1 .LCA_LAST_%=\n" \
  "ds_read_b32 v11, v1 offset:26688\n" \
  "ds_read_b128 v[40:43], v2 offset:0\n" \
  "ds_read_b128 v[44:47], v3 offset:0\n" \
  "ds_read_b128 v[48:51], v2 offset:64\n" \
  "ds_read_b128 v[52:55], v3 offset:64\n" \
  "ds_read_b128 v[56:59], v2 offset:128\n" \
  "ds_read_b128 v[60:63], v3 offset:128\n" \
  "s_waitcnt lgkmcnt(5)\n" \
  "v_mfma_f32_16x16x32_bf16 v[12:15], v[24:27], v[40:43], 0\n" \
  "v_mfma_f32_16x16x32_bf16 v[16:19], v[28:31], v[40:43], 0\n" \
  "v_readfirstlane_b32 s86, v11\n" \
  "ds_read_b128 v[24:27], v1 offset:12288\n" \
  "ds_read_b128 v[28:31], v1 offset:16384\n" \
  "s_waitcnt lgkmcnt(6)\n" \
  "v_mfma_f32_16x16x32_bf16 v[20:23], v[44:47], v[40:43], 0\n" \
  "s_lshr_b32 s86, s86, 23\n" \
  "s_add_i32 s86, s86, s83\n" \
  "ds_read_b128 v[40:43], v2 offset:192\n" \
  "ds_read_b128 v[44:47], v3 offset:192\n" \
  "s_waitcnt lgkmcnt(7)\n" \
  "v_mfma_f32_16x16x32_bf16 v[12:15], v[32:35], v[48:51], v[12:15]\n" \
  "v_mfma_f32_16x16x32_bf16 v[16:19], v[36:39], v[48:51], v[16:19]\n" \
  "s_ashr_i32 s86, s86, 1\n" \
  "s_max_i32 s86, s86, s82\n" \
  "ds_read_b128 v[32:35], v1 offset:13312\n" \
  "ds_read_b128 v[36:39], v1 offset:17408\n" \
  "s_waitcnt lgkmcnt(8)\n" \
  "v_mfma_f32_16x16x32_bf16 v[20:23], v[52:55], v[48:51], v[20:23]\n" \
  "s_min_i32 s86, s86, 100\n" \
  "s_lshl_b32 s86, s86, 23\n" \
  "s_sub_i32 s84, 0x4a800000, s86\n" \
  "ds_read_b128 v[48:51], v10 offset:18432\n" \
  "ds_read_b128 v[52:55], v10 offset:22528\n" \
  "s_waitcnt lgkmcnt(8)\n" \
  "v_mfma_f32_16x16x32_bf16 v[20:23], v[60:63], v[56:59], v[20:23]\n" \
  "s_waitcnt lgkmcnt(6)\n" \
  "v_mfma_f32_16x16x32_bf16 v[12:15], v[24:27], v[56:59], v[12:15]\n" \
  "v_mfma_f32_16x16x32_bf16 v[16:19], v[28:31], v[56:59], v[16:19]\n" \
  "ds_read_b128 v[56:59], v6 offset:0\n" \
  "ds_read_b128 v[60:63], v6 offset:1024\n" \
  "s_waitcnt lgkmcnt(6)\n" \
  "v_mfma_f32_16x16x32_bf16 v[20:23], v[44:47], v[40:43], v[20:23]\n" \
  "ds_read_b128 v[24:27], v6 offset:2048\n" \
  "ds_read_b128 v[28:31], v6 offset:3072\n" \
  "s_waitcnt lgkmcnt(6)\n" \
  "v_mfma_f32_16x16x32_bf16 v[12:15], v[32:35], v[40:43], v[12:15]\n" \
  "v_mfma_f32_16x16x32_bf16 v[16:19], v[36:39], v[40:43], v[16:19]\n" \
  "ds_read_b128 v[32:35], v6 offset:8192\n" \
  "ds_read_b128 v[36:39], v6 offset:9216\n" \
  "s_waitcnt lgkmcnt(6)\n" \
  "v_cndmask_b32 v20, v20, v21, s[76:77]\n" \
  "v_mov_b32 v41, 0\n" \
  "v_mov_b32 v42, 0\n" \
  "s_nop 1\n" \
  "v_cndmask_b32 v12, v12, v16, s[76:77]\n" \
  "v_cndmask_b32 v13, v13, v17, s[76:77]\n" \
  "v_cndmask_b32 v14, v14, v18, s[76:77]\n" \
  "v_cndmask_b32 v15, v15, v19, s[76:77]\n" \
  "v_maximum3_f32 v16, v20, v12, v13\n" \
  "v_maximum3_f32 v16, v16, v14, v15\n" \
  "s_nop 1\n" \
  "v_max_f32_dpp v16, v16, v16 row_ror:8 row_mask:0xf bank_mask:0xf\n" \
  "v_mov_b32 v17, v16\n" \
  "s_nop 1\n" \
  "v_permlane16_swap_b32 v16, v17\n" \
  "v_max_f32 v16, v16, v17\n" \
  "v_mov_b32 v17, v16\n" \
  "s_nop 1\n" \
  "v_permlane32_swap_b32 v16, v17\n" \
  "v_max_f32 v16, v16, v17\n" \
  "v_mul_f32 v16, s78, v16\n" \
  "v_pk_fma_f32 v[12:13], v[12:13], s[78:79], v[16:17] op_sel_hi:[1,0,0] neg_lo:[0,0,1] neg_hi:[0,0,1]\n" \
  "v_pk_fma_f32 v[14:15], v[14:15], s[78:79], v[16:17] op_sel_hi:[1,0,0] neg_lo:[0,0,1] neg_hi:[0,0,1]\n" \
  "v_fma_f32 v20, v20, s78, -v16\n" \
  "v_exp_f32 v12, v12\n" \
  "v_exp_f32 v13, v13\n" \
  "v_exp_f32 v14, v14\n" \
  "v_exp_f32 v15, v15\n" \
  "v_exp_f32 v20, v20\n" \
  "s_barrier\n" \
  "ds_read_b64 v[22:23], v7 offset:0\n" \
  "buffer_load_dword v7, v5, s[16:19], 0 offen sc1\n" \
  "v_cvt_pk_bf16_f32 v44, v12, v13\n" \
  "v_cvt_pk_bf16_f32 v45, v14, v15\n" \
  "v_pk_add_f32 v[8:9], v[12:13], v[14:15]\n" \
  "v_mov_b32_dpp v46, v44 row_ror:8 row_mask:0xf bank_mask:0xf\n" \
  "v_mov_b32_dpp v17, v20 row_ror:8 row_mask:0xf bank_mask:0xf\n" \
  "v_mov_b32_dpp v47, v45 row_ror:8 row_mask:0xf bank_mask:0xf\n" \
  "v_add_f32 v10, v8, v20\n" \
  "v_cvt_pk_bf16_f32 v40, v20, v17\n" \
  "v_add_f32 v10, v10, v9\n" \
  "v_mfma_f32_16x16x32_bf16 v[12:15], v[48:51], v[44:47], 0\n" \
  "v_cvt_pk_bf16_f32 v43, v20, v17\n" \
  "v_add_f32_dpp v10, v10, v10 row_ror:8 row_mask:0xf bank_mask:0xf\n" \
  "v_mfma_f32_16x16x32_bf16 v[16:19], v[52:55], v[44:47], 0\n" \
  "v_mov_b32 v11, v10\n" \
  "s_waitcnt lgkmcnt(0)\n" \
  "v_mfma_f32_16x16x16_bf16 v[12:15], v[22:23], v[40:41], v[12:15]\n" \
  "v_mfma_f32_16x16x16_bf16 v[16:19], v[22:23], v[42:43], v[16:19]\n" \
  "v_permlane16_swap_b32 v10, v11\n" \
  "v_add_f32 v10, v10, v11\n" \
  "ds_read_b128 v[48:51], v6 offset:10240\n" \
  "v_mov_b32 v11, v10\n" \
  "ds_read_b128 v[52:55], v6 offset:11264\n" \
  "v_mov_b64 v[20:21], 0\n" \
  "v_mov_b64 v[22:23], 0\n" \
  "v_permlane32_swap_b32 v10, v11\n" \
  "v_add_f32 v10, v10, v11\n" \
  "v_rcp_f32 v10, v10\n" \
  "s_nop 0\n" \
  "v_pk_mul_f32 v[12:13], v[12:13], v[10:11] op_sel_hi:[1,0]\n" \
  "v_pk_mul_f32 v[14:15], v[14:15], v[10:11] op_sel_hi:[1,0]\n" \
  "v_pk_mul_f32 v[16:17], v[16:17], v[10:11] op_sel_hi:[1,0]\n" \
  "v_pk_mul_f32 v[18:19], v[18:19], v[10:11] op_sel_hi:[1,0]\n" \
  "v_cvt_pk_bf16_f32 v40, v12, v13\n" \
  "v_cvt_pk_bf16_f32 v41, v14, v15\n" \
  "v_cvt_pk_bf16_f32 v42, v16, v17\n" \
  "ds_write_b64 v4, v[40:41]\n" \
  "v_cvt_pk_bf16_f32 v43, v18, v19\n" \
  "ds_write_b64 v4, v[42:43] offset:1024\n" \
  "v_mov_b64 v[12:13], 0\n" \
  "v_mov_b64 v[14:15], 0\n" \
  "v_mov_b64 v[16:17], 0\n" \
  "v_mov_b64 v[18:19], 0\n" \
  "v_mov_b64 v[8:9], 0\n" \
  "v_mov_b64 v[10:11], 0\n" \
  "s_setprio 0\n" \
  "s_waitcnt lgkmcnt(0)\n" \
  "s_barrier\n" \
  "ds_read_b128 v[40:43], v1 offset:8192\n" \
  "ds_read_b128 v[44:47], v1 offset:9216\n" \
  "s_waitcnt lgkmcnt(1)\n" \
  "v_smfmac_f32_16x16x64_bf16 v[12:15], v[40:43], v[56:63], v0\n" \
  "ds_read_b128 v[56:59], v6 offset:4096\n" \
  "ds_read_b128 v[60:63], v6 offset:5120\n" \
  "v_smfmac_f32_16x16x64_bf16 v[16:19], v[40:43], v[24:31], v0\n" \
  "ds_read_b128 v[24:27], v6 offset:6144\n" \
  "ds_read_b128 v[28:31], v6 offset:7168\n" \
  "s_waitcnt lgkmcnt(4)\n" \
  "v_smfmac_f32_16x16x64_bf16 v[12:15], v[44:47], v[32:39], v0\n" \
  "ds_read_b128 v[32:35], v6 offset:12288\n" \
  "ds_read_b128 v[36:39], v6 offset:13312\n" \
  "v_smfmac_f32_16x16x64_bf16 v[16:19], v[44:47], v[48:55], v0\n" \
  "ds_read_b128 v[48:51], v6 offset:14336\n" \
  "ds_read_b128 v[52:55], v6 offset:15360\n" \
  "s_nop 0\n" \
  "s_waitcnt lgkmcnt(6)\n" \
  "v_smfmac_f32_16x16x64_bf16 v[20:23], v[40:43], v[56:63], v0\n" \
  "s_nop 0\n" \
  "s_waitcnt lgkmcnt(4)\n" \
  "v_smfmac_f32_16x16x64_bf16 v[8:11], v[40:43], v[24:31], v0\n" \
  "v_permlane32_swap_b32 v12, v17\n" \
  "v_permlane32_swap_b32 v13, v16\n" \
  "v_permlane32_swap_b32 v14, v19\n" \
  "v_permlane32_swap_b32 v15, v18\n" \
  "s_waitcnt lgkmcnt(2)\n" \
  "v_smfmac_f32_16x16x64_bf16 v[20:23], v[44:47], v[32:39], v0\n" \
  "v_pk_add_f32 v[12:13], v[12:13], v[16:17] op_sel:[0,1] op_sel_hi:[1,0]\n" \
  "v_pk_add_f32 v[14:15], v[14:15], v[18:19] op_sel:[0,1] op_sel_hi:[1,0]\n" \
  "v_pk_fma_f32 v[12:13], v[12:13], s[84:85], s[84:85] op_sel:[0,0,1] op_sel_hi:[1,0,1]\n" \
  "v_pk_fma_f32 v[14:15], v[14:15], s[84:85], s[84:85] op_sel:[0,0,1] op_sel_hi:[1,0,1]\n" \
  "s_waitcnt lgkmcnt(0)\n" \
  "v_smfmac_f32_16x16x64_bf16 v[8:11], v[44:47], v[48:55], v0\n" \
  "v_lshl_add_u32 v12, v12, 6, s91\n" \
  "v_lshl_add_u32 v13, v13, 2, s75\n" \
  "global_atomic_add_x2 v5, v[12:13], s[26:27]\n" \
  "v_lshl_add_u32 v14, v14, 6, s91\n" \
  "v_lshl_add_u32 v15, v15, 2, s75\n" \
  "global_atomic_add_x2 v5, v[14:15], s[30:31]\n" \
  "s_nop 1\n" \
  "v_permlane32_swap_b32 v20, v9\n" \
  "v_permlane32_swap_b32 v21, v8\n" \
  "v_permlane32_swap_b32 v22, v11\n" \
  "v_permlane32_swap_b32 v23, v10\n" \
  "v_pk_add_f32 v[20:21], v[20:21], v[8:9] op_sel:[0,1] op_sel_hi:[1,0]\n" \
  "v_pk_add_f32 v[22:23], v[22:23], v[10:11] op_sel:[0,1] op_sel_hi:[1,0]\n" \
  "v_pk_fma_f32 v[20:21], v[20:21], s[84:85], s[84:85] op_sel:[0,0,1] op_sel_hi:[1,0,1]\n" \
  "v_pk_fma_f32 v[22:23], v[22:23], s[84:85], s[84:85] op_sel:[0,0,1] op_sel_hi:[1,0,1]\n" \
  "v_lshl_add_u32 v20, v20, 6, s91\n" \
  "v_lshl_add_u32 v21, v21, 2, s75\n" \
  "global_atomic_add_x2 v5, v[20:21], s[32:33]\n" \
  "v_lshl_add_u32 v22, v22, 6, s91\n" \
  "v_lshl_add_u32 v23, v23, 2, s75\n" \
  "global_atomic_add_x2 v5, v[22:23], s[28:29]\n" \
  "v_mbcnt_lo_u32_b32 v48, -1, 0\n" \
  "v_mbcnt_hi_u32_b32 v48, -1, v48\n" \
  "v_lshrrev_b32 v49, 3, v48\n" \
  "v_and_b32 v50, 7, v48\n" \
  "v_lshlrev_b32 v50, 4, v50\n" \
  "v_lshl_or_b32 v44, v49, 8, v50\n" \
  "s_add_u32 s20, s20, 32\n" \
  "s_mov_b32 s21, 0x8000\n" \
  "v_mov_b32 v52, s22\n" \
  "v_mov_b32 v54, s20\n" \
  "s_waitcnt vmcnt(4)\n" \
  ".LCA_TKE_%=:\n" \
  "v_readlane_b32 s34, v7, 0\n" \
  "v_readlane_b32 s38, v7, 16\n" \
  "v_readlane_b32 s39, v7, 32\n" \
  "v_readlane_b32 s23, v7, 48\n" \
  "s_max_u32 s34, s34, s38\n" \
  "s_max_u32 s39, s39, s23\n" \
  "v_readlane_b32 s38, v7, 8\n" \
  "s_max_u32 s34, s34, s39\n" \
  "v_readlane_b32 s39, v7, 24\n" \
  "s_add_u32 s34, s34, %[cd]\n" \
  "v_readlane_b32 s23, v7, 40\n" \
  "s_add_u32 s38, s38, s39\n" \
  "v_readlane_b32 s39, v7, 56\n" \
  "s_add_u32 s38, s38, s23\n" \
  "v_mov_b32 v55, s34\n" \
  "s_add_u32 s38, s38, s39\n" \
  "s_cmp_ge_u32 s38, s20\n" \
  "s_cbranch_scc1 .LCA_TKO_%=\n" \
  "s_sub_u32 s21, s21, 1\n" \
  "s_cbranch_scc1 .LCA_TKO_%=\n" \
  "s_sleep 2\n" \
  "buffer_load_dword v7, v5, s[16:19], 0 offen sc1\n" \
  "s_waitcnt vmcnt(0)\n" \
  "s_branch .LCA_TKE_%=\n" \
  ".LCA_TKO_%=:\n" \
  "ds_write_b64 v52, v[54:55]\n" \
  "s_branch .LCA_WAIU_%=\n" \
  ".LCA_WAIS_%=:\n" \
  "s_cmp_lt_i32 s39, -1000\n" \
  "s_cbranch_scc1 .LCA_WAIE_%=\n" \
  "s_sleep 1\n" \
  ".LCA_WAIU_%=:\n" \
  "s_memrealtime s[38:39]\n" \
  "s_waitcnt lgkmcnt(0)\n" \
  "s_sub_i32 s39, s38, s34\n" \
  "s_cmp_lt_i32 s39, -7\n" \
  "s_cbranch_scc1 .LCA_WAIS_%=\n" \
  "s_cmp_lt_i32 s39, 0\n" \
  "s_cbranch_scc1 .LCA_WAIU_%=\n" \
  ".LCA_WAIE_%=:\n" \
  "buffer_load_dwordx4 v[24:27], v44, s[40:43], s60 offen sc1\n" \
  "buffer_load_dwordx4 v[28:31], v44, s[40:43], s60 offen offset:2048 sc1\n" \
  "v_mbcnt_lo_u32_b32 v48, -1, 0\n" \
  "v_mbcnt_hi_u32_b32 v48, -1, v48\n" \
  "v_and_b32 v49, 15, v48\n" \
  "v_lshl_add_u32 v8, v49, 1, s97\n" \
  "v_bfe_u32 v49, v48, 4, 1\n" \
  "v_mad_u32_u24 v8, v49, s92, v8\n" \
  "v_lshrrev_b32 v49, 5, v48\n" \
  "v_mad_u32_u24 v9, v49, s93, v8\n" \
  "v_mad_u32_u24 v8, v49, s94, v8\n" \
  "v_add_u32 v10, s99, v1\n" \
  "s_lshr_b32 s88, s99, 2\n" \
  "s_add_u32 s88, s88, s74\n" \
  "s_add_u32 s88, s88, 139264\n" \
  "v_lshl_add_u32 v7, v49, 10, s88\n" \
  "v_and_b32 v49, 31, v48\n" \
  "v_lshl_add_u32 v7, v49, 3, v7\n" \
  "v_mov_b64 v[12:13], 0\n" \
  "v_mov_b64 v[14:15], 0\n" \
  "v_mov_b64 v[16:17], 0\n" \
  "v_mov_b64 v[18:19], 0\n" \
  "v_mov_b64 v[20:21], 0\n" \
  "v_mov_b64 v[22:23], 0\n" \
  "s_add_u32 s88, s74, 157760\n" \
  "v_mov_b32 v59, s88\n" \
  "v_lshrrev_b32 v49, 3, v48\n" \
  "v_and_b32 v50, 7, v48\n" \
  "v_lshlrev_b32 v50, 4, v50\n" \
  "v_lshl_or_b32 v44, v49, 8, v50\n" \
  "v_and_b32 v50, 3, v48\n" \
  "v_lshl_add_u32 v40, v50, 2, s98\n" \
  "v_lshrrev_b32 v50, 4, v48\n" \
  "v_mad_u32_u24 v40, v50, s95, v40\n" \
  "v_and_b32 v49, 1, v49\n" \
  "v_lshl_add_u32 v40, v49, 6, v40\n" \
  "v_bfe_u32 v49, v48, 2, 1\n" \
  "v_lshl_add_u32 v40, v49, 7, v40\n" \
  "v_xor_b32 v41, 16, v40\n" \
  "v_xor_b32 v42, 32, v40\n" \
  "v_xor_b32 v43, 48, v40\n" \
  "s_add_i32 s80, s86, 0x32800000\n" \
  "s_add_i32 s81, s86, 0x33800000\n" \
  "s_add_i32 s88, s86, 0xb4000000\n" \
  "v_mov_b32 v46, s88\n" \
  "v_mov_b32 v47, 0\n" \
  "v_mov_b32 v11, 0\n" \
  "s_mov_b32 s89, s60\n" \
  "s_xor_b32 s60, s60, 0x80\n" \
  "s_bitcmp0_b32 s60, 7\n" \
  "s_cselect_b32 s88, 0x8000, 0\n" \
  "s_add_u32 s60, s60, s88\n" \
  "s_and_b32 s60, s60, 0xffff\n" \
  "s_add_u32 s58, s56, s60\n" \
  "s_addc_u32 s59, s57, 0\n" \
  "s_xor_b32 s38, s35, 0\n" \
  "s_add_u32 s26, s58, s38\n" \
  "s_addc_u32 s27, s59, 0\n" \
  "s_xor_b32 s38, s35, 1024\n" \
  "s_add_u32 s28, s58, s38\n" \
  "s_addc_u32 s29, s59, 0\n" \
  "s_xor_b32 s38, s35, 2048\n" \
  "s_add_u32 s30, s58, s38\n" \
  "s_addc_u32 s31, s59, 0\n" \
  "s_xor_b32 s38, s35, 3072\n" \
  "s_add_u32 s32, s58, s38\n" \
  "s_addc_u32 s33, s59, 0\n" \
  "s_add_u32 s66, s66, 0x1000\n" \
  "s_addc_u32 s67, s67, 0\n" \
  "s_sub_u32 s71, s71, 1\n" \
  "s_mov_b32 s87, 0x40000\n" \
  "s_branch .LCA_CHK_%=\n" \
  ".LCA_POLL_%=:\n" \
  "buffer_load_dwordx4 v[24:27], v44, s[40:43], s89 offen sc1\n" \
  "buffer_load_dwordx4 v[28:31], v44, s[40:43], s89 offen offset:2048 sc1\n" \
  ".LCA_CHK_%=:\n" \
  "s_sub_u32 s87, s87, 1\n" \
  "s_cbranch_scc1 .LCA_END_%=\n" \
  "s_waitcnt vmcnt(1)\n" \
  "v_bitop3_b32 v45, v24, v26, 32 bitop3:0x80\n" \
  "v_alignbit_b32 v32, v25, v24, 2\n" \
  "v_alignbit_b32 v34, v27, v26, 2\n" \
  "s_waitcnt vmcnt(0)\n" \
  "v_bitop3_b32 v45, v45, v28, v30 bitop3:0x80\n" \
  "v_cmp_eq_u32 vcc, 0, v45\n" \
  "v_cvt_f32_i32 v32, v32\n" \
  "v_cvt_f32_i32 v33, v25\n" \
  "s_cmp_eq_u64 vcc, 0\n" \
  "s_cbranch_scc0 .LCA_POLL_%=\n" \
  "v_cvt_f32_i32 v34, v34\n" \
  "v_cvt_f32_i32 v35, v27\n" \
  "v_pk_fma_f32 v[32:33], v[32:33], s[80:81], v[46:47]\n" \
  "v_alignbit_b32 v36, v29, v28, 2\n" \
  "v_pk_fma_f32 v[34:35], v[34:35], s[80:81], v[46:47]\n" \
  "v_alignbit_b32 v38, v31, v30, 2\n" \
  "v_cvt_pk_bf16_f32 v32, v32, v34\n" \
  "v_cvt_pk_bf16_f32 v33, v33, v35\n" \
  "v_cvt_f32_i32 v36, v36\n" \
  "ds_write_b32 v40, v32\n" \
  "v_cvt_f32_i32 v37, v29\n" \
  "ds_write_b32 v41, v33\n" \
  "v_cvt_f32_i32 v38, v38\n" \
  "v_cvt_f32_i32 v39, v31\n" \
  "v_pk_fma_f32 v[36:37], v[36:37], s[80:81], v[46:47]\n" \
  "v_pk_fma_f32 v[38:39], v[38:39], s[80:81], v[46:47]\n" \
  "v_dot2c_f32_bf16 v11, v32, v32\n" \
  "v_cvt_pk_bf16_f32 v36, v36, v38\n" \
  "v_cvt_pk_bf16_f32 v37, v37, v39\n" \
  "ds_write_b32 v42, v36\n" \
  "ds_write_b32 v43, v37\n" \
  "v_dot2c_f32_bf16 v11, v33, v33\n" \
  "v_dot2c_f32_bf16 v11, v36, v36\n" \
  "v_dot2c_f32_bf16 v11, v37, v37\n" \
  "s_nop 1\n" \
  "s_nop 0\n" \
  "v_cmpx_lt_f32 vcc, 2.0, v11\n" \
  "ds_max_u32 v59, v11\n" \
  "s_mov_b64 exec, -1\n" \
  "s_branch .LCA_TOP_%=\n" \
  ".LCA_LAST_%=:\n" \
  ".LCA_END_%=:\n" \
  "s_endpgm\n"
#define CHAIN_ASM_VO \
  "s_mov_b32 s92, 1152\n" \
  "s_mov_b32 s93, 576\n" \
  "s_mov_b32 s94, 2272\n" \
  "s_mov_b32 s95, 272\n" \
  "s_mov_b32 s96, 288\n" \
  "s_mov_b32 s91, 0x40000001\n" \
  "s_mov_b32 s84, 0x4b400000\n" \
  "s_mov_b32 s85, 0x4b400000\n" \
  "s_mov_b32 s78, 0x3e0293ee\n" \
  "s_mov_b32 s79, 0x3fb8aa3b\n" \
  "s_mov_b32 s76, 0xff00ff00\n" \
  "s_mov_b32 s77, 0xff00ff00\n" \
  "s_add_u32 s58, s56, s60\n" \
  "s_addc_u32 s59, s57, 0\n" \
  "s_xor_b32 s38, s35, 0\n" \
  "s_add_u32 s26, s58, s38\n" \
  "s_addc_u32 s27, s59, 0\n" \
  "s_xor_b32 s38, s35, 1024\n" \
  "s_add_u32 s28, s58, s38\n" \
  "s_addc_u32 s29, s59, 0\n" \
  "s_xor_b32 s38, s35, 2048\n" \
  "s_add_u32 s30, s58, s38\n" \
  "s_addc_u32 s31, s59, 0\n" \
  "s_xor_b32 s38, s35, 3072\n" \
  "s_add_u32 s32, s58, s38\n" \
  "s_addc_u32 s33, s59, 0\n" \
  "s_lshl_b32 s98, s73, 10\n" \
  "s_add_u32 s98, s98, s74\n" \
  "s_add_u32 s98, s98, 131072\n" \
  "s_lshl_b32 s97, s73, 5\n" \
  "s_add_u32 s97, s97, s74\n" \
  "s_add_u32 s97, s97, 160992\n" \
  "s_lshl_b32 s99, s73, 11\n" \
  "s_add_u32 s99, s99, s74\n" \
  "s_add_u32 s99, s99, 133120\n" \
  "v_mbcnt_lo_u32_b32 v48, -1, 0\n" \
  "v_mbcnt_hi_u32_b32 v48, -1, v48\n" \
  "s_sub_u32 s88, s73, 4\n" \
  "v_and_b32 v51, 15, v48\n" \
  "v_lshl_add_u32 v4, v51, 1, s97\n" \
  "v_bfe_u32 v49, v48, 4, 1\n" \
  "v_mad_u32_u24 v4, v49, s92, v4\n" \
  "v_lshrrev_b32 v50, 5, v48\n" \
  "v_mad_u32_u24 v4, v50, s93, v4\n" \
  "v_lshlrev_b32 v8, 3, v49\n" \
  "v_lshl_add_u32 v8, v51, 4, v8\n" \
  "v_lshl_add_u32 v8, v50, 10, v8\n" \
  "s_lshl_b32 s89, s88, 8\n" \
  "s_add_u32 s89, s89, s74\n" \
  "s_add_u32 s89, s89, 139264\n" \
  "v_and_b32 v49, 31, v48\n" \
  "v_lshl_add_u32 v5, v49, 3, s89\n" \
  "v_lshl_add_u32 v5, v50, 10, v5\n" \
  "v_lshrrev_b32 v49, 2, v51\n" \
  "v_and_b32 v50, 5, v51\n" \
  "v_and_or_b32 v49, v49, 2, v50\n" \
  "s_lshl_b32 s89, s88, 6\n" \
  "s_add_u32 s89, s89, s74\n" \
  "s_add_u32 s89, s89, 160992\n" \
  "v_and_b32 v50, 48, v48\n" \
  "v_add_u32 v6, s89, v50\n" \
  "v_mad_u32_u24 v6, v49, s96, v6\n" \
  "v_lshlrev_b32 v7, 3, v50\n" \
  "v_lshl_add_u32 v7, v49, 4, v7\n" \
  "v_and_b32 v49, 2, v51\n" \
  "v_lshl_add_u32 v7, v49, 29, v7\n" \
  "v_mov_b32 v9, 0\n" \
  "v_lshlrev_b32 v10, 4, v48\n" \
  "v_mov_b64 v[12:13], 0\n" \
  "v_mov_b64 v[14:15], 0\n" \
  "v_mov_b64 v[16:17], 0\n" \
  "v_mov_b64 v[18:19], 0\n" \
  "v_mov_b64 v[20:21], 0\n" \
  "v_mov_b64 v[22:23], 0\n" \
  "s_add_u32 s88, s74, 157760\n" \
  "v_mov_b32 v59, s88\n" \
  "v_mov_b32 v24, 0x44870000\n" \
  "v_mul_f32 v24, s72, v24\n" \
  "s_nop 0\n" \
  "v_readfirstlane_b32 s86, v24\n" \
  "s_lshr_b32 s86, s86, 23\n" \
  "s_addk_i32 s86, 0xff83\n" \
  "s_ashr_i32 s86, s86, 1\n" \
  "s_max_i32 s86, s86, -20\n" \
  "s_min_i32 s86, s86, 100\n" \
  "s_mov_b32 s82, s86\n" \
  "s_lshr_b32 s83, s72, 23\n" \
  "s_sub_i32 s83, s83, 242\n" \
  "s_lshl_b32 s88, s73, 10\n" \
  "v_add_u32 v24, s88, v1\n" \
  "ds_read_b128 v[32:35], v24 offset:0\n" \
  "s_waitcnt lgkmcnt(0)\n" \
  "v_mov_b32 v11, 0\n" \
  "v_dot2c_f32_bf16 v11, v32, v32\n" \
  "v_dot2c_f32_bf16 v11, v33, v33\n" \
  "v_dot2c_f32_bf16 v11, v34, v34\n" \
  "v_dot2c_f32_bf16 v11, v35, v35\n" \
  "s_nop 1\n" \
  "s_nop 0\n" \
  "v_cmpx_lt_f32 vcc, 2.0, v11\n" \
  "ds_max_u32 v59, v11\n" \
  "s_mov_b64 exec, -1\n" \
  ".LCV_TOP_%=:\n" \
  "s_waitcnt lgkmcnt(0)\n" \
  "s_barrier\n" \
  "s_setprio 3\n" \
  "buffer_load_dword v58, v9, s[44:47], s68 offen sc1\n" \
  "s_cmp_lg_u32 s73, 7\n" \
  "s_cbranch_scc1 .LCV_NOTK_%=\n" \
  "s_memrealtime s[38:39]\n" \
  "v_mov_b32 v57, 1\n" \
  "s_mov_b64 exec, 1\n" \
  "s_waitcnt lgkmcnt(0)\n" \
  "v_mov_b32 v56, s38\n" \
  "buffer_atomic_umax v56, off, s[16:19], s24\n" \
  "buffer_atomic_add v57, off, s[16:19], s24 offset:64\n" \
  "s_mov_b64 exec, -1\n" \
  ".LCV_NOTK_%=:\n" \
  "ds_read_b128 v[24:27], v1 offset:0\n" \
  "ds_read_b128 v[28:31], v1 offset:1024\n" \
  "ds_read_b128 v[32:35], v1 offset:2048\n" \
  "ds_read_b128 v[36:39], v1 offset:3072\n" \
  "ds_read_b128 v[40:43], v1 offset:4096\n" \
  "ds_read_b128 v[44:47], v1 offset:5120\n" \
  "ds_read_b128 v[48:51], v1 offset:6144\n" \
  "ds_read_b128 v[52:55], v1 offset:7168\n" \
  "s_waitcnt lgkmcnt(7)\n" \
  "v_smfmac_f32_16x16x64_bf16 v[12:15], v[24:27], v[64:71], v0\n" \
  "s_waitcnt lgkmcnt(6)\n" \
  "v_smfmac_f32_16x16x64_bf16 v[12:15], v[28:31], v[72:79], v0\n" \
  "s_waitcnt lgkmcnt(5)\n" \
  "v_smfmac_f32_16x16x64_bf16 v[12:15], v[32:35], v[80:87], v0\n" \
  "s_waitcnt lgkmcnt(4)\n" \
  "v_smfmac_f32_16x16x64_bf16 v[12:15], v[36:39], v[88:95], v0\n" \
  "s_waitcnt lgkmcnt(3)\n" \
  "v_smfmac_f32_16x16x64_bf16 v[12:15], v[40:43], v[96:103], v0\n" \
  "s_waitcnt lgkmcnt(2)\n" \
  "v_smfmac_f32_16x16x64_bf16 v[12:15], v[44:47], v[104:111], v0\n" \
  "s_waitcnt lgkmcnt(1)\n" \
  "v_smfmac_f32_16x16x64_bf16 v[12:15], v[48:51], v[112:119], v0\n" \
  "s_waitcnt lgkmcnt(0)\n" \
  "v_smfmac_f32_16x16x64_bf16 v[12:15], v[52:55], v[120:127], v0\n" \
  "ds_read_b32 v11, v1 offset:26688\n" \
  "s_nop 6\n" \
  "v_permlane32_swap_b32 v12, v14\n" \
  "v_permlane32_swap_b32 v13, v15\n" \
  "v_pk_add_f32 v[12:13], v[12:13], v[14:15]\n" \
  "s_nop 0\n" \
  "v_cvt_pk_bf16_f32 v12, v12, v13\n" \
  "ds_write_b16 v4, v12\n" \
  "ds_write_b16_d16_hi v4, v12 offset:288\n" \
  "s_setprio 0\n" \
  "v_mov_b64 v[12:13], 0\n" \
  "v_mov_b64 v[14:15], 0\n" \
  "s_waitcnt lgkmcnt(2)\n" \
  "v_readfirstlane_b32 s86, v11\n" \
  "s_lshr_b32 s86, s86, 23\n" \
  "s_add_i32 s86, s86, s83\n" \
  "s_ashr_i32 s86, s86, 1\n" \
  "s_max_i32 s86, s86, s82\n" \
  "s_min_i32 s86, s86, 100\n" \
  "s_lshl_b32 s86, s86, 23\n" \
  "s_sub_i32 s84, 0x4a800000, s86\n" \
  "s_cmp_eq_u32 s71, 0\n" \
  "s_waitcnt lgkmcnt(0)\n" \
  "s_barrier\n" \
  "ds_read_b128 v[60:63], v6 offset:0\n" \
  "v_smfmac_f32_16x16x64_bf16 v[16:19], v[24:27], v[128:135], v0\n" \
  "v_smfmac_f32_16x16x64_bf16 v[20:23], v[24:27], v[192:199], v0\n" \
  "v_smfmac_f32_16x16x64_bf16 v[16:19], v[28:31], v[136:143], v0\n" \
  "v_smfmac_f32_16x16x64_bf16 v[20:23], v[28:31], v[200:207], v0\n" \
  "v_smfmac_f32_16x16x64_bf16 v[16:19], v[32:35], v[144:151], v0\n" \
  "v_smfmac_f32_16x16x64_bf16 v[20:23], v[32:35], v[208:215], v0\n" \
  "v_smfmac_f32_16x16x64_bf16 v[16:19], v[36:39], v[152:159], v0\n" \
  "v_smfmac_f32_16x16x64_bf16 v[20:23], v[36:39], v[216:223], v0\n" \
  "v_smfmac_f32_16x16x64_bf16 v[16:19], v[40:43], v[160:167], v0\n" \
  "v_smfmac_f32_16x16x64_bf16 v[20:23], v[40:43], v[224:231], v0\n" \
  "v_smfmac_f32_16x16x64_bf16 v[16:19], v[44:47], v[168:175], v0\n" \
  "v_smfmac_f32_16x16x64_bf16 v[20:23], v[44:47], v[232:239], v0\n" \
  "v_smfmac_f32_16x16x64_bf16 v[16:19], v[48:51], v[176:183], v0\n" \
  "v_smfmac_f32_16x16x64_bf16 v[20:23], v[48:51], v[240:247], v0\n" \
  "v_smfmac_f32_16x16x64_bf16 v[16:19], v[52:55], v[184:191], v0\n" \
  "v_smfmac_f32_16x16x64_bf16 v[20:23], v[52:55], v[248:255], v0\n" \
  "ds_read_b128 v[24:27], v3 offset:0\n" \
  "ds_read_b128 v[28:31], v3 offset:1024\n" \
  "ds_read_b128 v[32:35], v3 offset:2048\n" \
  "ds_read_b128 v[36:39], v3 offset:3072\n" \
  "ds_read_b128 v[40:43], v3 offset:8192\n" \
  "ds_read_b128 v[44:47], v3 offset:9216\n" \
  "s_nop 0\n" \
  "v_permlane32_swap_b32 v16, v18\n" \
  "v_permlane32_swap_b32 v17, v19\n" \
  "v_permlane32_swap_b32 v20, v22\n" \
  "v_permlane32_swap_b32 v21, v23\n" \
  "v_pk_add_f32 v[16:17], v[16:17], v[18:19]\n" \
  "v_pk_add_f32 v[20:21], v[20:21], v[22:23]\n" \
  "v_cvt_pk_bf16_f32 v16, v16, v17\n" \
  "v_cvt_pk_bf16_f32 v17, v20, v21\n" \
  "ds_write_b64 v5, v[16:17]\n" \
  "s_cbranch_scc1 .LCV_LAST_%=\n" \
  "s_cmp_eq_u32 s73, 7\n" \
  "s_waitcnt vmcnt(2)\n" \
  "s_cbranch_scc1 .LCV_TKW_%=\n" \
  "s_waitcnt vmcnt(0)\n" \
  ".LCV_TKW_%=:\n" \
  "v_cmp_eq_u32 vcc, 0, v58\n" \
  "s_cmp_eq_u64 vcc, 0\n" \
  "s_cselect_b32 s90, 1, 0\n" \
  "s_waitcnt lgkmcnt(0)\n" \
  "s_barrier\n" \
  "s_cbranch_scc0 .LCV_NOTOK_%=\n" \
  "s_mov_b32 m0, s99\n" \
  "s_add_u32 s88, s99, 8192\n" \
  "global_load_lds_dwordx4 v10, s[62:63] sc1\n" \
  "global_load_lds_dwordx4 v10, s[62:63] offset:1024 sc1\n" \
  "s_mov_b32 m0, s88\n" \
  "s_nop 0\n" \
  "global_load_lds_dwordx4 v10, s[64:65] sc1\n" \
  "global_load_lds_dwordx4 v10, s[64:65] offset:1024 sc1\n" \
  ".LCV_NOTOK_%=:\n" \
  "ds_write_b32 v59, v12\n" \
  "ds_read_b128 v[48:51], v3 offset:10240\n" \
  "ds_read_b128 v[52:55], v3 offset:11264\n" \
  "v_permlane32_swap_b32 v16, v17\n" \
  "v_max_u32 v60, 1, v60\n" \
  "v_max_u32 v61, 1, v61\n" \
  "v_max_u32 v16, 1, v16\n" \
  "v_max_u32 v17, 1, v17\n" \
  "buffer_store_dwordx2 v[16:17], v8, s[52:55], s70 offen sc1\n" \
  "v_max_u32 v62, 1, v62\n" \
  "v_max_u32 v63, 1, v63\n" \
  "buffer_store_dwordx4 v[60:63], v7, s[48:51], s69 offen sc1\n" \
  "v_mov_b64 v[18:19], 0\n" \
  "v_mov_b64 v[20:21], 0\n" \
  "v_mov_b64 v[22:23], 0\n" \
  "v_mov_b64 v[16:17], 0\n" \
  "ds_read_b128 v[56:59], v3 offset:4096\n" \
  "ds_read_b128 v[60:63], v3 offset:5120\n" \
  "s_waitcnt lgkmcnt(0)\n" \
  "s_barrier\n" \
  "ds_read_b128 v[4:7], v1 offset:8192\n" \
  "ds_read_b128 v[8:11], v1 offset:9216\n" \
  "s_waitcnt lgkmcnt(1)\n" \
  "v_smfmac_f32_16x16x64_bf16 v[12:15], v[4:7], v[24:31], v0\n" \
  "ds_read_b128 v[24:27], v3 offset:6144\n" \
  "ds_read_b128 v[28:31], v3 offset:7168\n" \
  "v_smfmac_f32_16x16x64_bf16 v[16:19], v[4:7], v[32:39], v0\n" \
  "ds_read_b128 v[32:35], v3 offset:12288\n" \
  "ds_read_b128 v[36:39], v3 offset:13312\n" \
  "s_waitcnt lgkmcnt(4)\n" \
  "v_smfmac_f32_16x16x64_bf16 v[12:15], v[8:11], v[40:47], v0\n" \
  "v_smfmac_f32_16x16x64_bf16 v[16:19], v[8:11], v[48:55], v0\n" \
  "v_smfmac_f32_16x16x64_bf16 v[20:23], v[4:7], v[56:63], v0\n" \
  "ds_read_b128 v[56:59], v3 offset:14336\n" \
  "ds_read_b128 v[60:63], v3 offset:15360\n" \
  "v_mov_b64 v[40:41], 0\n" \
  "v_mov_b64 v[42:43], 0\n" \
  "s_waitcnt lgkmcnt(2)\n" \
  "v_smfmac_f32_16x16x64_bf16 v[20:23], v[8:11], v[32:39], v0\n" \
  "s_nop 0\n" \
  "v_permlane32_swap_b32 v12, v17\n" \
  "v_permlane32_swap_b32 v13, v16\n" \
  "v_permlane32_swap_b32 v14, v19\n" \
  "v_permlane32_swap_b32 v15, v18\n" \
  "v_smfmac_f32_16x16x64_bf16 v[40:43], v[4:7], v[24:31], v0\n" \
  "v_pk_add_f32 v[12:13], v[12:13], v[16:17] op_sel:[0,1] op_sel_hi:[1,0]\n" \
  "v_pk_add_f32 v[14:15], v[14:15], v[18:19] op_sel:[0,1] op_sel_hi:[1,0]\n" \
  "v_pk_fma_f32 v[12:13], v[12:13], s[84:85], s[84:85] op_sel:[0,0,1] op_sel_hi:[1,0,1]\n" \
  "v_pk_fma_f32 v[14:15], v[14:15], s[84:85], s[84:85] op_sel:[0,0,1] op_sel_hi:[1,0,1]\n" \
  "s_waitcnt lgkmcnt(0)\n" \
  "v_smfmac_f32_16x16x64_bf16 v[40:43], v[8:11], v[56:63], v0\n" \
  "v_lshl_add_u32 v12, v12, 6, s91\n" \
  "v_lshl_add_u32 v13, v13, 2, s75\n" \
  "global_atomic_add_x2 v2, v[12:13], s[26:27]\n" \
  "v_lshl_add_u32 v14, v14, 6, s91\n" \
  "v_lshl_add_u32 v15, v15, 2, s75\n" \
  "global_atomic_add_x2 v2, v[14:15], s[30:31]\n" \
  "s_nop 1\n" \
  "v_permlane32_swap_b32 v20, v41\n" \
  "v_permlane32_swap_b32 v21, v40\n" \
  "v_permlane32_swap_b32 v22, v43\n" \
  "v_permlane32_swap_b32 v23, v42\n" \
  "v_pk_add_f32 v[20:21], v[20:21], v[40:41] op_sel:[0,1] op_sel_hi:[1,0]\n" \
  "v_pk_add_f32 v[22:23], v[22:23], v[42:43] op_sel:[0,1] op_sel_hi:[1,0]\n" \
  "v_pk_fma_f32 v[20:21], v[20:21], s[84:85], s[84:85] op_sel:[0,0,1] op_sel_hi:[1,0,1]\n" \
  "v_pk_fma_f32 v[22:23], v[22:23], s[84:85], s[84:85] op_sel:[0,0,1] op_sel_hi:[1,0,1]\n" \
  "v_lshl_add_u32 v20, v20, 6, s91\n" \
  "v_lshl_add_u32 v21, v21, 2, s75\n" \
  "global_atomic_add_x2 v2, v[20:21], s[32:33]\n" \
  "v_lshl_add_u32 v22, v22, 6, s91\n" \
  "v_lshl_add_u32 v23, v23, 2, s75\n" \
  "global_atomic_add_x2 v2, v[22:23], s[28:29]\n" \
  "v_mbcnt_lo_u32_b32 v48, -1, 0\n" \
  "v_mbcnt_hi_u32_b32 v48, -1, v48\n" \
  "s_add_u32 s20, s20, 32\n" \
  "s_cmp_eq_u32 s90, 1\n" \
  "s_cbranch_scc1 .LCV_HAVE_%=\n" \
  "s_mov_b32 s87, 0x40000\n" \
  "v_mov_b32 v9, 0\n" \
  "v_lshlrev_b32 v10, 4, v48\n" \
  ".LCV_LATE_%=:\n" \
  "buffer_load_dword v58, v9, s[44:47], s68 offen sc1\n" \
  "s_sub_u32 s87, s87, 1\n" \
  "s_cbranch_scc1 .LCV_END_%=\n" \
  "s_waitcnt vmcnt(0)\n" \
  "v_cmp_eq_u32 vcc, 0, v58\n" \
  "s_cmp_eq_u64 vcc, 0\n" \
  "s_cbranch_scc1 .LCV_GOT_%=\n" \
  "s_sleep 12\n" \
  "s_branch .LCV_LATE_%=\n" \
  ".LCV_GOT_%=:\n" \
  "s_mov_b32 m0, s99\n" \
  "s_add_u32 s88, s99, 8192\n" \
  "global_load_lds_dwordx4 v10, s[62:63] sc1\n" \
  "global_load_lds_dwordx4 v10, s[62:63] offset:1024 sc1\n" \
  "s_mov_b32 m0, s88\n" \
  "s_nop 0\n" \
  "global_load_lds_dwordx4 v10, s[64:65] sc1\n" \
  "global_load_lds_dwordx4 v10, s[64:65] offset:1024 sc1\n" \
  ".LCV_HAVE_%=:\n" \
  "v_lshrrev_b32 v49, 3, v48\n" \
  "v_and_b32 v50, 7, v48\n" \
  "v_lshlrev_b32 v50, 4, v50\n" \
  "v_lshl_or_b32 v44, v49, 8, v50\n" \
  "s_mov_b32 s21, 0x8000\n" \
  "v_mov_b32 v52, s22\n" \
  "v_mov_b32 v54, s20\n" \
  "s_branch .LCV_TKF_%=\n" \
  ".LCV_TKE_%=:\n" \
  "s_sleep 1\n" \
  ".LCV_TKF_%=:\n" \
  "ds_read_b64 v[54:55], v52\n" \
  "s_sub_u32 s21, s21, 1\n" \
  "s_cbranch_scc1 .LCV_TKO_%=\n" \
  "s_waitcnt lgkmcnt(0)\n" \
  "v_readfirstlane_b32 s38, v54\n" \
  "v_readfirstlane_b32 s34, v55\n" \
  "s_cmp_eq_u32 s38, s20\n" \
  "s_cbranch_scc0 .LCV_TKE_%=\n" \
  ".LCV_TKO_%=:\n" \
  "s_branch .LCV_WAIU_%=\n" \
  ".LCV_WAIS_%=:\n" \
  "s_cmp_lt_i32 s39, -1000\n" \
  "s_cbranch_scc1 .LCV_WAIE_%=\n" \
  "s_sleep 1\n" \
  ".LCV_WAIU_%=:\n" \
  "s_memrealtime s[38:39]\n" \
  "s_waitcnt lgkmcnt(0)\n" \
  "s_sub_i32 s39, s38, s34\n" \
  "s_cmp_lt_i32 s39, -7\n" \
  "s_cbranch_scc1 .LCV_WAIS_%=\n" \
  "s_cmp_lt_i32 s39, 0\n" \
  "s_cbranch_scc1 .LCV_WAIU_%=\n" \
  ".LCV_WAIE_%=:\n" \
  "buffer_load_dwordx4 v[24:27], v44, s[40:43], s60 offen sc1\n" \
  "buffer_load_dwordx4 v[28:31], v44, s[40:43], s60 offen offset:2048 sc1\n" \
  "v_mbcnt_lo_u32_b32 v48, -1, 0\n" \
  "v_mbcnt_hi_u32_b32 v48, -1, v48\n" \
  "s_sub_u32 s88, s73, 4\n" \
  "v_and_b32 v51, 15, v48\n" \
  "v_lshl_add_u32 v4, v51, 1, s97\n" \
  "v_bfe_u32 v49, v48, 4, 1\n" \
  "v_mad_u32_u24 v4, v49, s92, v4\n" \
  "v_lshrrev_b32 v50, 5, v48\n" \
  "v_mad_u32_u24 v4, v50, s93, v4\n" \
  "v_lshlrev_b32 v8, 3, v49\n" \
  "v_lshl_add_u32 v8, v51, 4, v8\n" \
  "v_lshl_add_u32 v8, v50, 10, v8\n" \
  "s_lshl_b32 s89, s88, 8\n" \
  "s_add_u32 s89, s89, s74\n" \
  "s_add_u32 s89, s89, 139264\n" \
  "v_and_b32 v49, 31, v48\n" \
  "v_lshl_add_u32 v5, v49, 3, s89\n" \
  "v_lshl_add_u32 v5, v50, 10, v5\n" \
  "v_lshrrev_b32 v49, 2, v51\n" \
  "v_and_b32 v50, 5, v51\n" \
  "v_and_or_b32 v49, v49, 2, v50\n" \
  "s_lshl_b32 s89, s88, 6\n" \
  "s_add_u32 s89, s89, s74\n" \
  "s_add_u32 s89, s89, 160992\n" \
  "v_and_b32 v50, 48, v48\n" \
  "v_add_u32 v6, s89, v50\n" \
  "v_mad_u32_u24 v6, v49, s96, v6\n" \
  "v_lshlrev_b32 v7, 3, v50\n" \
  "v_lshl_add_u32 v7, v49, 4, v7\n" \
  "v_and_b32 v49, 2, v51\n" \
  "v_lshl_add_u32 v7, v49, 29, v7\n" \
  "v_mov_b32 v9, 0\n" \
  "v_lshlrev_b32 v10, 4, v48\n" \
  "v_mov_b64 v[12:13], 0\n" \
  "v_mov_b64 v[14:15], 0\n" \
  "v_mov_b64 v[16:17], 0\n" \
  "v_mov_b64 v[18:19], 0\n" \
  "v_mov_b64 v[20:21], 0\n" \
  "v_mov_b64 v[22:23], 0\n" \
  "s_add_u32 s88, s74, 157760\n" \
  "v_mov_b32 v59, s88\n" \
  "v_lshrrev_b32 v49, 3, v48\n" \
  "v_and_b32 v50, 7, v48\n" \
  "v_lshlrev_b32 v50, 4, v50\n" \
  "v_lshl_or_b32 v44, v49, 8, v50\n" \
  "v_and_b32 v50, 3, v48\n" \
  "v_lshl_add_u32 v40, v50, 2, s98\n" \
  "v_lshrrev_b32 v50, 4, v48\n" \
  "v_mad_u32_u24 v40, v50, s95, v40\n" \
  "v_and_b32 v49, 1, v49\n" \
  "v_lshl_add_u32 v40, v49, 6, v40\n" \
  "v_bfe_u32 v49, v48, 2, 1\n" \
  "v_lshl_add_u32 v40, v49, 7, v40\n" \
  "v_xor_b32 v41, 16, v40\n" \
  "v_xor_b32 v42, 32, v40\n" \
  "v_xor_b32 v43, 48, v40\n" \
  "s_add_u32 s62, s62, 0x80000\n" \
  "s_addc_u32 s63, s63, 0\n" \
  "s_add_u32 s64, s64, 0x40000\n" \
  "s_addc_u32 s65, s65, 0\n" \
  "s_add_u32 s68, s68, 0x1000\n" \
  "s_add_u32 s69, s69, 0x10000\n" \
  "s_add_u32 s70, s70, 0x10000\n" \
  "s_add_i32 s80, s86, 0x32800000\n" \
  "s_add_i32 s81, s86, 0x33800000\n" \
  "s_add_i32 s88, s86, 0xb4000000\n" \
  "v_mov_b32 v46, s88\n" \
  "v_mov_b32 v47, 0\n" \
  "v_mov_b32 v11, 0\n" \
  "s_mov_b32 s89, s60\n" \
  "s_xor_b32 s60, s60, 0x80\n" \
  "s_bitcmp0_b32 s60, 7\n" \
  "s_cselect_b32 s88, 0x8000, 0\n" \
  "s_add_u32 s60, s60, s88\n" \
  "s_and_b32 s60, s60, 0xffff\n" \
  "s_add_u32 s58, s56, s60\n" \
  "s_addc_u32 s59, s57, 0\n" \
  "s_xor_b32 s38, s35, 0\n" \
  "s_add_u32 s26, s58, s38\n" \
  "s_addc_u32 s27, s59, 0\n" \
  "s_xor_b32 s38, s35, 1024\n" \
  "s_add_u32 s28, s58, s38\n" \
  "s_addc_u32 s29, s59, 0\n" \
  "s_xor_b32 s38, s35, 2048\n" \
  "s_add_u32 s30, s58, s38\n" \
  "s_addc_u32 s31, s59, 0\n" \
  "s_xor_b32 s38, s35, 3072\n" \
  "s_add_u32 s32, s58, s38\n" \
  "s_addc_u32 s33, s59, 0\n" \
  "s_add_u32 s66, s66, 0x1000\n" \
  "s_addc_u32 s67, s67, 0\n" \
  "s_sub_u32 s71, s71, 1\n" \
  "s_mov_b32 s87, 0x40000\n" \
  "s_branch .LCV_CHK_%=\n" \
  ".LCV_POLL_%=:\n" \
  "buffer_load_dwordx4 v[24:27], v44, s[40:43], s89 offen sc1\n" \
  "buffer_load_dwordx4 v[28:31], v44, s[40:43], s89 offen offset:2048 sc1\n" \
  ".LCV_CHK_%=:\n" \
  "s_sub_u32 s87, s87, 1\n" \
  "s_cbranch_scc1 .LCV_END_%=\n" \
  "s_waitcnt vmcnt(1)\n" \
  "v_bitop3_b32 v45, v24, v26, 32 bitop3:0x80\n" \
  "v_alignbit_b32 v32, v25, v24, 2\n" \
  "v_alignbit_b32 v34, v27, v26, 2\n" \
  "s_waitcnt vmcnt(0)\n" \
  "v_bitop3_b32 v45, v45, v28, v30 bitop3:0x80\n" \
  "v_cmp_eq_u32 vcc, 0, v45\n" \
  "v_cvt_f32_i32 v32, v32\n" \
  "v_cvt_f32_i32 v33, v25\n" \
  "s_cmp_eq_u64 vcc, 0\n" \
  "s_cbranch_scc0 .LCV_POLL_%=\n" \
  "v_cvt_f32_i32 v34, v34\n" \
  "v_cvt_f32_i32 v35, v27\n" \
  "v_pk_fma_f32 v[32:33], v[32:33], s[80:81], v[46:47]\n" \
  "v_alignbit_b32 v36, v29, v28, 2\n" \
  "v_pk_fma_f32 v[34:35], v[34:35], s[80:81], v[46:47]\n" \
  "v_alignbit_b32 v38, v31, v30, 2\n" \
  "v_cvt_pk_bf16_f32 v32, v32, v34\n" \
  "v_cvt_pk_bf16_f32 v33, v33, v35\n" \
  "v_cvt_f32_i32 v36, v36\n" \
  "ds_write_b32 v40, v32\n" \
  "v_cvt_f32_i32 v37, v29\n" \
  "ds_write_b32 v41, v33\n" \
  "v_cvt_f32_i32 v38, v38\n" \
  "v_cvt_f32_i32 v39, v31\n" \
  "v_pk_fma_f32 v[36:37], v[36:37], s[80:81], v[46:47]\n" \
  "v_pk_fma_f32 v[38:39], v[38:39], s[80:81], v[46:47]\n" \
  "v_dot2c_f32_bf16 v11, v32, v32\n" \
  "v_cvt_pk_bf16_f32 v36, v36, v38\n" \
  "v_cvt_pk_bf16_f32 v37, v37, v39\n" \
  "ds_write_b32 v42, v36\n" \
  "ds_write_b32 v43, v37\n" \
  "v_dot2c_f32_bf16 v11, v33, v33\n" \
  "v_dot2c_f32_bf16 v11, v36, v36\n" \
  "v_dot2c_f32_bf16 v11, v37, v37\n" \
  "s_nop 1\n" \
  "s_nop 0\n" \
  "v_cmpx_lt_f32 vcc, 2.0, v11\n" \
  "ds_max_u32 v59, v11\n" \
  "s_mov_b64 exec, -1\n" \
  "s_branch .LCV_TOP_%=\n" \
  ".LCV_LAST_%=:\n" \
  "s_waitcnt lgkmcnt(0)\n" \
  "s_nop 0\n" \
  "v_permlane32_swap_b32 v16, v17\n" \
  "v_max_u32 v60, 1, v60\n" \
  "v_max_u32 v61, 1, v61\n" \
  "v_max_u32 v16, 1, v16\n" \
  "v_max_u32 v17, 1, v17\n" \
  "buffer_store_dwordx2 v[16:17], v8, s[52:55], s70 offen sc1\n" \
  "v_max_u32 v62, 1, v62\n" \
  "v_max_u32 v63, 1, v63\n" \
  "buffer_store_dwordx4 v[60:63], v7, s[48:51], s69 offen sc1\n" \
  "s_waitcnt vmcnt(0)\n" \
  ".LCV_END_%=:\n" \
  "s_endpgm\n"
__device__ __forceinline__ v4u mk_srd(const void* p, const unsigned bytes) {
  const unsigned long long a = (unsigned long long)p;
  return v4u{(unsigned)a, (unsigned)(a >> 32) & 0xffffu, bytes, 0x00020000u};
}
template <bool ATT>
__device__ __forceinline__ void role_chain_t(const MegaArgs& A, char* smem, const int L, const int h, const int rep) {
  __builtin_amdgcn_s_waitcnt(0x0F70);
  u16* swo = reinterpret_cast<u16*>(smem);
  const int w = __builtin_amdgcn_readfirstlane(threadIdx.x >> 6);
  const int wc = (w + ROT) & 7, psw = PSW ? (PSWH) : 0;
  char* ws = A.ws;
  unsigned* FL = (unsigned*)(ws + OFF_FL);
  unsigned* TMO = FL + FL_TMO;
#define LANE_IDS()                         \
  const int l = lane_id();                 \
  const int c = l & 15, g = l >> 4;        \
  (void)c; (void)g;
  float* sgm = reinterpret_cast<float*>(smem + C_X);
  float* sgf = sgm + 8;
  unsigned* FQ = FL + FL_FQ + L * 64 * 32 * 32;
  v8u w0[8], w1[8], w2[8];
  {
    LANE_IDS();
    v4u m0r;
    const unsigned sb = lds_base(smem);
    const int q = (l >> 2) & 3, p = l & 3;
    const int f = (q & 1) | ((g & 1) << 1) | ((q >> 1) << 2);
    const unsigned ra = sb + (16 * (g >> 1) + 4 * (g & 1) + (q & 1) + 8 * (q >> 1)) * 256 + (w ^ f) * 32 + p * 8;
#define W_READ(W, hf, X)                                                       \
  {                                                                           \
    _Pragma("unroll") for (int t = 0; t < 4; ++t) {                           \
      const unsigned a_ = (ra ^ (X)) + (hf) * 8192 + t * 32768;               \
      const v2u r0 = trd(a_), r1 = trd(a_ + 512), r2 = trd(a_ + 16384), r3 = trd(a_ + 16384 + 512); \
      W[4 * (hf) + t] = v8u{r0.x, r0.y, r1.x, r1.y, r2.x, r2.y, r3.x, r3.y};  \
    }                                                                         \
    _Pragma("unroll") for (int t = 0; t < 4; ++t) asm volatile("" : "+v"(W[4 * (hf) + t])); \
  }
#define W_STAGE(M, hf, VM, NEXT)                                               \
  {                                                                           \
    WAIT_VM(VM);                                                              \
    RAW_BAR();                                                                \
    if ((M) == 0 && ATT) { W_READ(w0, hf, 0); W_READ(w2, hf, 128); }          \
    if ((M) == 1 && ATT) { W_READ(w1, hf, 0); }                               \
    if ((M) == 1 && !ATT) { W_READ(w0, hf, 0); }                              \
    if ((M) == 2 && !ATT) { W_READ(w1, hf, 128); W_READ(w2, hf, 0); }         \
    RAW_BAR();                                                                \
    NEXT;                                                                     \
  }
#define WO_ISSUE(hf)                                                          \
  {                                                                           \
    const char* base_ = (const char*)A.Wo + (size_t)(h * DH + 64 * (hf) + 8 * w) * (DM * 2); \
    _Pragma("unroll") for (int i = 0; i < 8; ++i) {                           \
      const int fd = (i & 1) | (((i >> 2) & 1) << 1) | ((w & 1) << 2);        \
      glds16(base_, (unsigned)(i * 1024 + ((l ^ (fd << 1)) << 4)), sb + w * 16384 + (hf) * 8192 + i * 1024); \
    }                                                                         \
  }
    wqkv_issue<true>(A.Wv, 0, h, w, l, sb);
    wqkv_issue<true>(A.Wv, 1, h, w, l, sb);
    asm volatile("global_load_dwordx4 %0, %1, %2" : "=v"(m0r) : "v"(((c & 7) * DM + 64 * w + 16 * g + 8 * (c >> 3)) * 2), "s"(A.mem0) : "memory");
    W_STAGE(2, 0, 9, wqkv_issue<true>(A.Wk, 0, h, w, l, sb));
    SST(4);
    W_STAGE(2, 1, 8, wqkv_issue<true>(A.Wk, 1, h, w, l, sb));
    SST(5);
    asm volatile("" : "+v"(m0r));
    *reinterpret_cast<v4u*>(smem + C_SM + w * 1024 + l * 16) = m0r;
    {
      float f2[4] = {};
      if (!ATT) {
#pragma unroll
        for (int t = 0; t < 8; ++t)
#pragma unroll
          for (int e = 0; e < 8; ++e) {
            f2[e & 1] = dot2sq(w1[t][e], f2[e & 1]);
            f2[2 + (e & 1)] = dot2sq(w2[t][e], f2[2 + (e & 1)]);
          }
      }
      const float f = wave_sum_dpp((f2[0] + f2[1]) + (f2[2] + f2[3]));
      if (l == 0) { sgf[w] = f; sgm[16] = 0.f; sgm[32 + w] = 0.f; }
    }
    v4u zt;
    v2u ze;
#define Z_LOAD()                                                                                                        \
  asm volatile("global_load_dwordx4 %0, %2, %3 sc1\n\tglobal_load_dwordx2 %1, %4, %5 sc1"                              \
               : "=&v"(zt), "=&v"(ze)                                                                                   \
               : "v"(l * 16), "s"(ws + OFF_TAG + (blockIdx.x & 7) * 1024), "v"(0), "s"(ws + OFF_TAG + 8192)              \
               : "memory")
    W_STAGE(1, 0, 8, Z_LOAD(); wqkv_issue<true>(A.Wq, 0, h, w, l, sb));
#undef Z_LOAD
    W_STAGE(1, 1, 8, wqkv_issue<true>(A.Wq, 1, h, w, l, sb));
    asm volatile("" : "+v"(zt), "+v"(ze));
    if (w == 0) {
      if (!zero_seen(zt, ze)) zero_wait(ws);
      const float f2 = (sgf[4] + sgf[5]) + (sgf[6] + sgf[7]);
      __builtin_amdgcn_raw_buffer_store_b32(__float_as_uint(f2) | 1u, MKRS(FL + FL_G, L ? 0 : 4 * 512), l * 512 + h * 4, 0, AUX_SC1);
    }
    SST(6);
    W_STAGE(0, 0, 8, WO_ISSUE(0));
    W_STAGE(0, 1, 8, WO_ISSUE(1));
    SST(7);
#undef W_STAGE
#undef W_READ
#undef WO_ISSUE
    const unsigned rb = sb + (2 * (g >> 1) + (q >> 1)) * 16384 + (4 * (g & 1) + (q & 1)) * 1024 + p * 8;
#pragma unroll
    for (int hf = 0; hf < 2; ++hf) {
      if (hf == 0) WAIT_VM(8); else WAIT_VM(0);
      RAW_BAR();
      v4u fr[8];
#pragma unroll
      for (int i = 0; i < 8; ++i) {
        const unsigned a_ = rb + hf * 8192 + (i & 1) * 65536 + ((4 * wc + ((i >> 1) ^ psw)) ^ f) * 32;
        const v2u r0 = trd(a_), r1 = trd(a_ + 2048);
        fr[i] = v4u{r0.x, r0.y, r1.x, r1.y};
      }
#pragma unroll
      for (int i = 0; i < 8; ++i) asm volatile("" : "+v"(fr[i]));
      RAW_BAR();
#pragma unroll
      for (int i = 0; i < 8; ++i) *reinterpret_cast<v4u*>(swo + w * 8192 + hf * 4096 + i * 512 + l * 8) = fr[i];
    }
  }
  WSTAMP2();
  const unsigned sb = lds_base(smem);
  const char* gK = ws + OFF_QK + L * SZ_QK + ((size_t)h * 16 + 2 * w) * 1024;
  const char* gV = ws + OFF_VF + L * SZ_O + ((size_t)h * 8 + 2 * w - 8) * 1024;
  float gw2;
  {
    const gu32* gp = (gu32*)(FL + FL_G + REP * 128) + (lane_id() & 31);
    unsigned g0, g1, g2;
    for (unsigned spins = 0;; ++spins) {
      g0 = __hip_atomic_load(gp, RLX_AGENT);
      g1 = __hip_atomic_load(gp + 32, RLX_AGENT);
      g2 = __hip_atomic_load(gp + 64, RLX_AGENT);
      if (__all(g0 != 0u && g1 != 0u && g2 != 0u)) break;
      if (spin_timeout(spins, TMO)) return;
      __builtin_amdgcn_s_sleep(4);
    }
    const float gm = wave_max_dpp(__uint_as_float(g0) * (__uint_as_float(g1) + __uint_as_float(g2)));
    asm volatile("s_nop 0\n\tv_readfirstlane_b32 %0, %1" : "=s"(gw2) : "v"(gm * 1.07f));
  }
  v8u pv;
  {
    LANE_IDS();
    const unsigned voff = g * 256 + c * 8, wp = sb + w * 16384 + l * 16;
    pv[0] = (c & 8) ? 0xEEEE : 0x4444;
    pv[1] = sb + C_SM + l * 16;
    pv[2] = ATT ? sb + C_SQ + ((c & 7) * QS + 8 * g) * 2 : voff;
    pv[3] = ATT ? sb + C_SK + ((4 * ((c >> 2) & 1) + 2 * (c >> 3) + (c & 1)) * QS + 8 * g) * 2 : wp;
    pv[4] = sb + (c < 8 ? C_SO : C_SQ) + w * 256 + ((c & 7) + 8 * (g >> 1)) * 16 + 8 * (g & 1);
    pv[5] = voff;
    pv[6] = wp;
    pv[7] = sb + C_SO + (l >> 5) * 1024 + w * 256 + (l & 31) * 8;
  }
  if (!ATT) {
    if (!wait_n<4, 1>(FQ + h * 32, 1, TMO)) return;
    const unsigned lo = lane_id() * 16, d = sb + C_KT + (2 * w - 8) * 1024;
    glds16c(gK, lo, d);
    glds16c(gK + 1024, lo, d + 1024);
    glds16c(gV, lo, d + 8192);
    glds16c(gV + 1024, lo, d + 9216);
  }
  DRAIN();
  SST(8);
  const v4u rsA = mk_srd(ws + OFF_ACC + L * SZ_ACC, SZ_ACC);
  const v4u rsF = mk_srd(FQ, 64 * 32 * 32 * 4);
  const v4u rsMK = mk_srd(ws + OFF_MEMK + L * SZ_MEM, SZ_MEM);
  const v4u rsMV = mk_srd(ws + OFF_MEMV + L * SZ_MEM, SZ_MEM);
  const char* pAB = ws + OFF_ACC + L * SZ_ACC + (wc - w) * 4096;
  const char* pGK = gK + NH * 16 * 1024;
  const char* pGV = gV + NH * 8 * 1024;
  const unsigned* pFK = FL;
  unsigned sc[8];
  sc[0] = (32 + h) * 128;
  sc[1] = (h * 4 + w - 4) * 512;
  sc[2] = (h * DH + 16 * (w - 4)) * MM * 2;
  sc[3] = NSEG - 1;
  sc[4] = __float_as_uint(gw2);
  sc[5] = w;
  sc[6] = sb;
  sc[7] = h == 0 ? 0xd4000000u - 2u - (1u << 29) : 0xd4000000u;
  const char* dbg = ws;
  const unsigned tg = (now10() / GR) * GR;
  const v4u rsT = mk_srd(FL + FL_TK + L * 256, 1024);
#define CHAIN_OPS                                                                                                          \
  ::"{v[64:71]}"(w0[0]), "{v[72:79]}"(w0[1]), "{v[80:87]}"(w0[2]), "{v[88:95]}"(w0[3]), "{v[96:103]}"(w0[4]),              \
      "{v[104:111]}"(w0[5]), "{v[112:119]}"(w0[6]), "{v[120:127]}"(w0[7]), "{v[128:135]}"(w1[0]), "{v[136:143]}"(w1[1]),    \
      "{v[144:151]}"(w1[2]), "{v[152:159]}"(w1[3]), "{v[160:167]}"(w1[4]), "{v[168:175]}"(w1[5]), "{v[176:183]}"(w1[6]),    \
      "{v[184:191]}"(w1[7]), "{v[192:199]}"(w2[0]), "{v[200:207]}"(w2[1]), "{v[208:215]}"(w2[2]), "{v[216:223]}"(w2[3]),    \
      "{v[224:231]}"(w2[4]), "{v[232:239]}"(w2[5]), "{v[240:247]}"(w2[6]), "{v[248:255]}"(w2[7]), "{v[0:7]}"(pv),          \
      "{s[40:43]}"(rsA), "{s[44:47]}"(rsF), "{s[48:51]}"(rsMK), "{s[52:55]}"(rsMV), "{s[56:57]}"(pAB), "{s60}"(w * 4096), "{s61}"(h * 1024), \
      "{s[62:63]}"(pGK), "{s[64:65]}"(pGV), "{s[66:67]}"(pFK), "{s68}"(sc[0]), "{s69}"(sc[1]), "{s70}"(sc[2]),    \
      "{s71}"(sc[3]), "{s72}"(sc[4]), "{s73}"(sc[5]), "{s74}"(sc[6]), "{s75}"(sc[7]), "{s[36:37]}"(dbg),                   \
      "{s34}"(tg), "{s35}"(psw * 1536), [gp] "n"(GP), [gr] "n"(GR), [gm] "n"(GM), \
      "{s[16:19]}"(rsT), "{s20}"(0), "{s22}"(sb + C_X + 128 + 8 * (w & 3)), "{s24}"((h & 3) * 256), [cd] "n"(CD) \
      : "memory", "vcc", "scc", "s99", "v63"
  if (ATT) asm volatile("s_mov_b64 exec, -1\ns_nop 4\n" CHAIN_ASM_ATT CHAIN_OPS);
  else asm volatile("s_mov_b64 exec, -1\ns_nop 4\n" CHAIN_ASM_VO CHAIN_OPS);
#undef CHAIN_OPS
#undef LANE_IDS
}

__device__ __forceinline__ void role_chain(const MegaArgs& A, char* smem, const int L, const int h, const int rep) {
  if (__builtin_amdgcn_readfirstlane(threadIdx.x >> 6) < 4) role_chain_t<true>(A, smem, L, h, rep);
  else role_chain_t<false>(A, smem, L, h, rep);
}

constexpr int AW_TK = 0, AW_SO = 98304, AW_M = AW_SO + 32768, AW_CNT = AW_M + 8192;
typedef __attribute__((address_space(3))) unsigned lu32;
__device__ __forceinline__ unsigned umin3(unsigned a, unsigned b, unsigned c) {
  return __builtin_elementwise_min(__builtin_elementwise_min(a, b), c);
}
__device__ __forceinline__ bool all_set(const unsigned r) {
  unsigned long long m;
  asm volatile("v_cmp_eq_u32 %0, 0, %1" : "=s"(m) : "v"(r) : "memory");
  return m == 0ull;
}
__device__ __forceinline__ unsigned long long zero_mask(const unsigned r) {
  unsigned long long m;
  asm volatile("v_cmp_eq_u32 %0, 0, %1" : "=s"(m) : "v"(r) : "memory");
  return m;
}
__device__ __forceinline__ int job_seg(const int n) {
  const int m = n - AWD;
  const int r = (m & 1) ? 64 + (m >> 1) : AWD + (m >> 1);
  return __builtin_amdgcn_readfirstlane(n > 127 ? 128 : (n < AWD || n >= 128 - AWD) ? n : r);
}
template <bool AT>
__device__ __forceinline__ void role_aw_t(const MegaArgs& A, char* smem, const int mode, const int a) {
  __builtin_amdgcn_s_waitcnt(0x0F70);
  const int w = __builtin_amdgcn_readfirstlane(threadIdx.x >> 6);
  const int l = threadIdx.x & 63, c = l & 15, g = l >> 4;
  const int qt = w & 1, hh = (w >> 1) & 1, h = 2 * a + hh;
  char* ws = A.ws;
  unsigned* FL = (unsigned*)(ws + OFF_FL);
  unsigned* TMO = FL + FL_TMO;
  const unsigned sb = lds_base(smem);
  constexpr int NP = AT ? 1 : 3;
  const int cp0 = AT ? w : 3 * w - 8;
  v4u wo[2 * NP][8];
  lu32* cnt = (lu32*)(unsigned long)(sb + AW_CNT);
  if (threadIdx.x < 16) cnt[threadIdx.x] = 0u;
  const __amdgpu_buffer_rsrc_t rsW = MKRS(ws + OFF_PW, SZ_PW);
  const unsigned* FQ = FL + FL_FQ + h * 32;
  const v4u rsF = mk_srd(FQ, (128 * 32 - h) * 128);
  const char* gQ = ws + OFF_QK + ((size_t)h * 16 + 8 * qt) * 1024;
  const char* gV = ws + OFF_VF + ((size_t)h * 8 + 4 * qt) * 1024;
  const char* gM = ws + (qt ? OFF_MEMV : OFF_MEMK) + h * 2048;
  const int NJ = (130 - mode) / 3;
#define AWS(gs) (64 + a + ((gs) >> 6) * 16)
  const int mo = AW_M + hh * 4096;
  const unsigned tq0 = __builtin_amdgcn_readfirstlane(sb + AW_TK + (hh * 24 + 8 * qt) * 1024);
  const unsigned tv0 = __builtin_amdgcn_readfirstlane(sb + AW_TK + (hh * 24 + 16 + 4 * qt) * 1024);
  const float scale = 0.08838834764831845f;
  volatile lu32* cM = cnt + 2 + hh;
#define DMA4(x) "global_load_lds_dwordx4 %1, " x " sc1\n\tglobal_load_lds_dwordx4 %1, " x " offset:1024 sc1\n\t"       \
                "global_load_lds_dwordx4 %1, " x " offset:2048 sc1\n\tglobal_load_lds_dwordx4 %1, " x " offset:3072 sc1\n\t"
#define T_DMA(jj, gg)                                                                                                   \
  {                                                                                                                     \
    const unsigned o_ = ((jj) & 1) ? 49152u : 0u;                                                                       \
    const char* q_ = gQ + (size_t)(gg) * (NH * 16 * 1024);                                                              \
    unsigned keep_;                                                                                                     \
    asm volatile("s_nop 4\n\ts_mov_b32 %0, m0\n\ts_mov_b32 m0, %5\n\ts_nop 0\n\t" DMA4("%2")                           \
                 "s_mov_b32 m0, %6\n\ts_nop 0\n\t" DMA4("%3") "s_mov_b32 m0, %7\n\ts_nop 0\n\t" DMA4("%4") "s_mov_b32 m0, %0" \
                 : "=&s"(keep_)                                                                                         \
                 : "v"(l * 16), "s"(q_), "s"(q_ + 4096), "s"(gV + (size_t)(gg) * (NH * 8 * 1024)), "s"(tq0 + o_),        \
                   "s"(tq0 + o_ + 4096u), "s"(tv0 + o_)                                                                 \
                 : "memory");                                                                                           \
  }
#define T_LAND(jj) { if (l == 0) *(volatile lu32*)(cnt + w) = (jj) + 1; }
#define M_DMA(gg)                                                                                                       \
  {                                                                                                                     \
    unsigned keep_;                                                                                                     \
    asm volatile("s_nop 4\n\ts_mov_b32 %0, m0\n\ts_mov_b32 m0, %3\n\ts_nop 0\n\tglobal_load_lds_dwordx4 %1, %2 sc1\n\t"     \
                 "global_load_lds_dwordx4 %1, %2 offset:1024 sc1\n\ts_mov_b32 m0, %0"                                    \
                 : "=&s"(keep_)                                                                                         \
                 : "v"(l * 16), "s"(gM + (size_t)(gg) * (NH * 2048)), "s"(sb + mo + qt * 2048)                           \
                 : "memory");                                                                                           \
  }
#define M_TEST()                                                                                                        \
  {                                                                                                                     \
    const v4u t_ = *reinterpret_cast<const v4u*>(smem + mo + qt * 2048 + l * 16);                                        \
    const v4u u_ = *reinterpret_cast<const v4u*>(smem + mo + qt * 2048 + 1024 + l * 16);                                 \
    ok = __all(umin3(umin3(t_.x, t_.y, t_.z), umin3(u_.x, u_.y, u_.z), umin3(t_.w, u_.w, 1u)) != 0u);                   \
    if (ok) asm volatile("s_mov_b64 exec, 1\n\tds_add_u32 %0, %1\n\ts_mov_b64 exec, -1" ::"v"(sb + AW_CNT + 8 + 4 * hh), "v"(1u) : "memory"); \
  }
#define F_LOAD(r, gg) \
  asm volatile("s_nop 4\n\tbuffer_load_dword %0, %1, %2, %3 offen sc1" : "=v"(r) : "v"(0), "s"(rsF), "s"((gg) * 4096) : "memory")
#define F_TEST(r) all_set(r)
#define W_FLAG(jj, gg)                                                                                                  \
  {                                                                                                                     \
    unsigned old_;                                                                                                      \
    asm volatile("s_mov_b64 exec, 1\n\tds_add_rtn_u32 %0, %1, %2\n\ts_mov_b64 exec, -1\n\ts_waitcnt lgkmcnt(0)"           \
                 : "=&v"(old_) : "v"(sb + AW_CNT + ((jj) & 1) * 36), "v"(1u) : "memory");                               \
    if ((__builtin_amdgcn_readfirstlane(old_) & 7u) == 7u) {                                                            \
      TSR(AWS(gg), 4, (gg) & 63, threadIdx.x & ~63u);                                                                   \
      if (l < 4) set_flag(FL + FL_FW + (((gg) * 4 + l) * 4 + (a >> 2)) * 32 + (a & 3));                              \
    }                                                                                                                   \
    pend = false;                                                                                                       \
  }
#define LDS_U(p) ((unsigned)__builtin_amdgcn_readfirstlane(*(p)))
#define TOK_MIN() __builtin_elementwise_min(LDS_U((volatile lu32*)(cnt + 4 + 2 * hh)), LDS_U((volatile lu32*)(cnt + 5 + 2 * hh)))
  int gp = 0, gs = job_seg(mode), g1 = job_seg(mode + 3);
  {
    const int q = (l >> 2) & 3, p = l & 3, f = q | ((g & 1) << 2);
    const unsigned ra = sb + (4 * g + q) * 256 + (p & 1) * 16;
    unsigned n0 = 0u;
    bool got = false;
#pragma unroll
    for (int st = 0; st < 4; ++st) {
      if (st == 3 && got) WAIT_VM(12); else if (st == 3) WAIT_VM(0); else WAIT_VM(8);
      RAW_BAR();
#pragma unroll
      for (int k2 = 0; k2 < 2; ++k2) {
        const int ks = 4 * (st >> 1) + 2 * k2 + (st & 1);
#pragma unroll
        for (int ct = 0; ct < 2 * NP; ++ct) {
          const int cp = cp0 + (ct >> 1);
          const unsigned a_ = ra + (st & 1) * 65536 + (cp >> 2) * 16384 + k2 * 8192 + ((2 * (cp & 3) + (p >> 1)) ^ f) * 32 + (ct & 1) * 8;
          const v2u lo = trd(a_), hi = trd(a_ + 4096);
          wo[ct][ks] = v4u{lo.x, lo.y, hi.x, hi.y};
        }
#pragma unroll
        for (int ct = 0; ct < 2 * NP; ++ct) asm volatile("" : "+v"(wo[ct][ks]));
      }
      RAW_BAR();
      if (!AT && st == 1) F_LOAD(n0, gs);
      if (st < 2) wo_issue(A.Wo, a, st + 2, sb);
      if (!AT && st == 2 && F_TEST(n0)) {
        T_DMA(0, gs);
        got = true;
      }
    }
    if (!AT) {
      if (!got) {
        (void)wait_n<SLP_A, 1>(FQ + gs * 1024, 1, TMO);
        T_DMA(0, gs);
      }
      WAIT_VM(0);
      T_LAND(0);
    }
  }
  __builtin_amdgcn_s_waitcnt(0x0F70);
  bool pend = false, asked = false;
  unsigned tb1 = now10() - 130 * GPER, tb2 = tb1;
  FSD;
  for (int s = 0; s < NJ; ++s) {
    char* so = smem + AW_SO + (s & 1) * 16384 + l * 16;
    bool got = g1 == 128, land = got, seen = false;
    if (AT) {
      if (TOK_MIN() < (unsigned)(s + 1)) {
        if (pend) {
          WAIT_VM(0);
          W_FLAG(s - 1, gp);
        }
        for (unsigned spins = 0; TOK_MIN() < (unsigned)(s + 1) && spins < 64u * SPIN_CAP; ++spins) __builtin_amdgcn_s_sleep(1);
      }
      asm volatile("" ::: "memory");
      TSR(AWS(gs), 0, gs & 63, 0);
      FS(0);
      const char* tk = smem + AW_TK + ((s & 1) * 2 + hh) * 24 * 1024 + l * 16;
#define TKF(i) (*reinterpret_cast<const bf16x8*>(tk + (i) * 1024))
      bf16x8 q[4], vt[8];
      float x[12], mx;
      bool ok = false;
      {
        f32x4 s0 = {}, s1 = {};
        bf16x8 kd[4], k0[4];
#pragma unroll
        for (int ks = 0; ks < 4; ++ks) {
          q[ks] = TKF(4 * qt + ks);
          kd[ks] = TKF(8 + 4 * qt + ks);
          k0[ks] = TKF(8 + ks);
        }
#pragma unroll
        for (int i = 0; i < 8; ++i) vt[i] = TKF(16 + i);
        __builtin_amdgcn_sched_barrier(0);
#pragma unroll
        for (int ks = 0; ks < 4; ++ks) {
          s0 = MFMA16(kd[ks], q[ks], s0);
          s1 = MFMA16(k0[ks], q[ks], s1);
        }
        scale_pair(s0[0], s0[1], scale, x[0], x[1]);
        scale_pair(s0[2], s0[3], scale, x[2], x[3]);
        scale_pair(s1[0], s1[1], scale, x[4], x[5]);
        scale_pair(s1[2], s1[3], scale, x[6], x[7]);
#pragma unroll
        for (int i = 0; i < 4; ++i) {
          if (4 * g + i > c) x[i] = -3e38f;
          if (!qt) x[4 + i] = -3e38f;
        }
        mx = fmaxf(fmaxf(fmaxf(x[0], x[1]), fmaxf(x[2], x[3])), fmaxf(fmaxf(x[4], x[5]), fmaxf(x[6], x[7])));
        asm volatile("" : "+v"(mx), "+v"(vt[0]), "+v"(vt[1]), "+v"(vt[2]), "+v"(vt[3]), "+v"(vt[4]), "+v"(vt[5]), "+v"(vt[6]), "+v"(vt[7]));
      }
#undef TKF
      FS(1);
      if (asked) {
        WAIT_VM(1);
        M_TEST();
      }
      FS(2);
      if (!ok) {
        if (pend) {
          WAIT_VM(0);
          W_FLAG(s - 1, gp);
        }
        unsigned tb = gs < 64 ? tb1 : tb2, spins = 0;
        gate(tb + gs * GPER);
        for (; !ok; ++spins) {
          if (spin_timeout(spins, TMO)) break;
          if (spins) __builtin_amdgcn_s_sleep(SLP_M);
          M_DMA(gs);
          WAIT_VM(0);
          M_TEST();
        }
        tb = spins > 1 ? now10() - gs * GPER - GMA : tb - GSTEP;
        if (gs < 64) tb1 = tb; else tb2 = tb;
      }
      FS(3);
      __builtin_amdgcn_s_setprio(PRI_A);
      asked = false;
      for (unsigned spins = 0; LDS_U(cM) < 2u * (s + 1) && spins < 64u * SPIN_CAP; ++spins) __builtin_amdgcn_s_sleep(1);
      v4u km[4];
      v2u vm[8];
#pragma unroll
      for (int ks = 0; ks < 4; ++ks) km[ks] = *reinterpret_cast<const v4u*>(smem + mo + (ks * 32 + g * 8 + (c & 7)) * 16);
#pragma unroll
      for (int i = 0; i < 8; ++i) vm[i] = *reinterpret_cast<const v2u*>(smem + mo + 2048 + ((16 * i + c) * MM + 4 * (g & 1)) * 2);
      TSR(AWS(gs), 1, gs & 63, 0);
      FS(4);
      f32x4 m0 = {};
#pragma unroll
      for (int ks = 0; ks < 4; ++ks) m0 = MFMA16(BF8(km[ks]), q[ks], m0);
      scale_pair(m0[0], m0[1], scale, x[8], x[9]);
      scale_pair(m0[2], m0[3], scale, x[10], x[11]);
#pragma unroll
      for (int i = 0; i < 4; ++i) {
        if (g >= 2) x[8 + i] = -3e38f;
        mx = fmaxf(mx, x[8 + i]);
      }
      mx = xmax32(xmax16(mx));
      float sum = 0.f;
#pragma unroll
      for (int i = 0; i < 12; ++i) {
        x[i] = __expf(x[i] - mx);
        sum += x[i];
      }
      sum = xsum32(xsum16(sum));
      const float inv = 1.0f / sum;
      const v2u pa = {pk2(x[0] * inv, x[1] * inv), pk2(x[2] * inv, x[3] * inv)};
      const v2u pb = {pk2(x[4] * inv, x[5] * inv), pk2(x[6] * inv, x[7] * inv)};
      const v4u p0 = qt ? v4u{pb.x, pb.y, pa.x, pa.y} : v4u{pa.x, pa.y, pb.x, pb.y};
      const v2u p1 = {pk2(x[8] * inv, x[9] * inv), pk2(x[10] * inv, x[11] * inv)};
      FS(5);
#pragma unroll
      for (int k = 0; k < 4; ++k) {
        v4u of;
#pragma unroll
        for (int hf = 0; hf < 2; ++hf) {
          f32x4 ao = {};
          ao = MFMA16(vt[2 * k + hf], BF8(p0), ao);
          ao = MFMA16K(vm[2 * k + hf], p1, ao);
          of[2 * hf] = pk2(ao[0], ao[1]);
          of[2 * hf + 1] = pk2(ao[2], ao[3]);
        }
        *reinterpret_cast<v4u*>(so + ((hh * 2 + qt) * 4 + k) * 1024) = of;
      }
      FS(6);
      seen = g1 >= 64 && g1 < 128 && TOK_MIN() >= (unsigned)(s + 2);
      if (pend) {
        WAIT_VM(0);
        W_FLAG(s - 1, gp);
      }
    } else {
      FS(0);
      unsigned n;
      F_LOAD(n, g1);
      if (pend) {
        WAIT_VM(1);
        W_FLAG(s - 1, gp);
      }
      WAIT_VM(0);
      FS(1);
      if (F_TEST(n)) {
        T_DMA(s + 1, g1);
        got = true;
        if (LDS_U(cM) < 2u * (s + 1)) {
          WAIT_VM(0);
          T_LAND(s + 1);
          land = true;
        }
      }
    }
    FS(7);
    RAW_BAR();
    FS(8);
    __builtin_amdgcn_s_setprio(AT ? PRI_WA : PRI_WB);
    TSR(AWS(gs), 2, gs & 63, 256);
    const int voff = (c >> 1) * 2048 + (c & 1) * 64 + g * 16, soff = (gs * 16 + a) * 32768 + cp0 * 128;
#define OFRAG(i) (*reinterpret_cast<const bf16x8*>(so + (((((i) & 7) >> 2) * 2 + ((i) >> 3)) * 4 + ((i) & 3)) * 1024))
    constexpr int NO = AT ? 16 : 3;
    bf16x8 o[NO];
#pragma unroll
    for (int i = 0; i < (AT ? 16 : 2); ++i) o[i] = OFRAG(i);
    unsigned pr;
    const bool look = seen && g1 >= 128 - AWD;
    if (AT) {
      asm volatile("s_cmp_eq_u32 %3, 0\n\ts_cbranch_scc1 1f\n\ts_nop 4\n\tglobal_load_dword %0, %1, %2 sc1\n1:"
                   : "=v"(pr) : "v"(2044), "s"(gM + (size_t)g1 * (NH * 2048)), "s"(__builtin_amdgcn_readfirstlane((int)look)) : "memory", "scc");
    } else {
      F_LOAD(pr, got ? 128 : g1);
    }
    __builtin_amdgcn_sched_barrier(0);
    FS(9);
#pragma unroll
    for (int rt = 0; rt < 2; ++rt) {
      f32x4 acc[2 * NP] = {};
#pragma unroll
      for (int kk = 0; kk < 8; ++kk) {
        const int i = 8 * rt + kk;
        if (!AT && i + 2 < 16) o[(i + 2) % NO] = OFRAG(i + 2);
#pragma unroll
        for (int ct = 0; ct < 2 * NP; ++ct) acc[ct] = MFMA16(BF8(wo[ct][kk]), o[i % NO], acc[ct]);
        if (!AT) __builtin_amdgcn_sched_barrier(0);
      }
      if (rt == 0) FS(10); else FS(12);
      if (AT && rt == 1 && seen) {
        if (look) {
          WAIT_VM(1);
          seen = F_TEST(pr);
        }
        if (seen) {
          M_DMA(g1);
          asked = true;
        }
      }
      if (!AT && rt == 0) {
        WAIT_VM(0);
        if (got && !land) {
          T_LAND(s + 1);
          land = true;
        }
        if (F_TEST(pr)) {
          T_DMA(s + 1, g1);
          got = true;
        }
      }
      if (!AT && rt == 1 && got && !land) {
        WAIT_VM(3);
        T_LAND(s + 1);
      }
#pragma unroll
      for (int u = 0; u < NP; ++u)
        ST128((v4u{pk2(acc[2 * u][0], acc[2 * u][1]), pk2(acc[2 * u][2], acc[2 * u][3]), pk2(acc[2 * u + 1][0], acc[2 * u + 1][1]),
                   pk2(acc[2 * u + 1][2], acc[2 * u + 1][3])}),
              rsW, voff + u * 128, soff + rt * 16384);
      if (rt == 0) FS(11); else FS(13);
    }
    __builtin_amdgcn_s_setprio(AT ? PRI_0 : 0);
    pend = true;
    FD(s);
    TSR(AWS(gs), 3, gs & 63, 256);
    TSR(AWS(gs), 5, gs & 63, 0);
    if (!AT && !got) {
      WAIT_VM(0);
      W_FLAG(s, gs);
      (void)wait_n<SLP_A, 1>(FQ + g1 * 1024, 1, TMO);
      T_DMA(s + 1, g1);
      WAIT_VM(0);
      T_LAND(s + 1);
    }
    gp = gs;
    gs = g1;
    g1 = job_seg(3 * s + 6 + mode);
  }
  WAIT_VM(0);
  W_FLAG(NJ - 1, gp);
#undef AWS
#undef OFRAG
#undef DMA4
#undef T_DMA
#undef T_LAND
#undef M_DMA
#undef M_TEST
#undef F_LOAD
#undef F_TEST
#undef W_FLAG
#undef LDS_U
#undef TOK_MIN
}
__device__ __forceinline__ void role_aw(const MegaArgs& A, char* smem, const int L, const int a) {
  if (__builtin_amdgcn_readfirstlane(threadIdx.x >> 6) < 4) role_aw_t<true>(A, smem, L, a);
  else role_aw_t<false>(A, smem, L, a);
}

__device__ __forceinline__ void role_red(const MegaArgs& A, char* smem, const int idx) {
  const int w = __builtin_amdgcn_readfirstlane(threadIdx.x >> 6);
  const int l = threadIdx.x & 63;
  const int gr = w >> 1, r = idx * 2 + (w & 1);
  char* ws = A.ws;
  unsigned* FL = (unsigned*)(ws + OFF_FL);
  unsigned* TMO = FL + FL_TMO;
  const __amdgpu_buffer_rsrc_t rsW = MKRS(ws + OFF_PW, SZ_PW);
  const __amdgpu_buffer_rsrc_t rsX = MKRS(ws + OFF_XA, OFF_X3 - OFF_XA + SZ_X);
  volatile unsigned* sw = reinterpret_cast<volatile unsigned*>(smem) + (w >> 1) * 32;
  if (threadIdx.x < 128) reinterpret_cast<volatile unsigned*>(smem)[threadIdx.x] = 0u;
  __syncthreads();
  unsigned tb1 = now10() - 130 * GPER, tb2 = tb1;
  const int rn1 = idx < 8 ? 96 + idx : 224 + idx;
  (void)rn1;
  if (w >= 6) return;
  if (w < 4) {
    const __amdgpu_buffer_rsrc_t rz = MKRS(ws + OFF_X2, SZ_X);
    for (int s = gr; s < NSEG; s += 2) ST128((v4u{0u, 0u, 0u, 0u}), rz, l * 16, (s * SEG + r) * DM * 2);
    DRAIN();
    if (l < 4) set_flag(FL + FL_FZ + (2 * l + (idx >> 3)) * 32 + (idx & 7) * 4 + w);
    if (l == 0) __hip_atomic_fetch_add((lu32*)(unsigned long)(lds_base(smem) + 480), 1u, __ATOMIC_RELAXED, __HIP_MEMORY_SCOPE_WORKGROUP);
  }
  for (unsigned spins = 0; reinterpret_cast<volatile unsigned*>(smem)[120] < 4u && spins < 64u * SPIN_CAP; ++spins) __builtin_amdgcn_s_sleep(2);
  for (int n = gr; n < 128; n += 3) {
    const int gs = job_seg(n), L = gs >> 6, s = gs & 63;
    const unsigned* fw = FL + FL_FW + (gs * 4 + REP) * 4 * 32;
    unsigned tb = L ? tb2 : tb1;
    tb = grp_wait<SLP_R, 4>((w & 1) == 0, sw, n + 1, fw, 16, TMO, tb + gs * GPER) ? now10() - gs * GPER - GMR : tb - GSTEP;
    if (n >= 125 - AWD) tb = now10() + GATE_RT - (n + 3) * GPER;
    if (L || n >= 125 - AWD) tb2 = tb; else tb1 = tb;
#define RTS(k, t) TSR(L ? A.rn : rn1, k, s, (w & 6) * 64 + (t))
    RTS(0, 0);
    RTS(4, 64);
    v4u p[16];
    const v4u xr = LD128(rsX, l * 512, L * (int)(OFF_X3 - OFF_XA) + s * 32768 + r * 16);
#pragma unroll
    for (int k = 0; k < 16; ++k)
      p[k] = LD128(rsW, (l >> 2) * 128 + (l & 3) * 16, (gs * 16 + k) * 32768 + (r >> 1) * 2048 + (r & 1) * 64);
    v4u xo;
    const unsigned nz = L ? 0u : 0x8000u;
#pragma unroll
    for (int e = 0; e < 4; ++e) {
      f32x2_t t[4];
#pragma unroll
      for (int k = 0; k < 4; ++k)
        t[k] = (f32x2_t{lo16(p[4 * k][e]), hi16(p[4 * k][e])} + f32x2_t{lo16(p[4 * k + 1][e]), hi16(p[4 * k + 1][e])}) +
               (f32x2_t{lo16(p[4 * k + 2][e]), hi16(p[4 * k + 2][e])} + f32x2_t{lo16(p[4 * k + 3][e]), hi16(p[4 * k + 3][e])});
      const f32x2_t u = (t[0] + t[1]) + (t[2] + t[3]);
      const unsigned a0 = pk2(u.x, u.y);
      xo[e] = pk2(lo16(xr[e]) + lo16(a0), hi16(xr[e]) + hi16(a0));
      xo[e] = xo[e] ? xo[e] : nz;
    }
    ST128(xo, MKRS(L ? (char*)A.out : ws + OFF_X2, SZ_X), l * 16, (s * SEG + r) * DM * 2);
    RTS(1, 0);
    RTS(5, 64);
    if (L == 0) {
      const unsigned st = row_stats(xo);
      if (l < 4) __hip_atomic_store((gu32*)(FL + FL_FR + ((s * 4 + l) * 4 + (r >> 3)) * 32 + (r & 7)), st, RLX_AGENT);
    }
    RTS(6, 64);
    RTS(3, 0);
#undef RTS
  }
  if (idx == 0 && threadIdx.x == 128) {
    gu64* en = (gu64*)(ws + OFF_TAG + 8192);
    __hip_atomic_store(en, __hip_atomic_load(en, RLX_AGENT) + TAG_STEP, RLX_AGENT);
  }
}

constexpr int CUN = 2144;
__device__ __forceinline__ bf16x8 conv_bfrag(const unsigned* up, const unsigned sh) {
  const unsigned d0 = up[0], d1 = up[1], d2 = up[2], d3 = up[3], d4 = up[4];
  const v4u r = {__builtin_amdgcn_alignbit(d1, d0, sh), __builtin_amdgcn_alignbit(d2, d1, sh),
                 __builtin_amdgcn_alignbit(d3, d2, sh), __builtin_amdgcn_alignbit(d4, d3, sh)};
  return BF8(r);
}
__device__ __forceinline__ void role_conv(const MegaArgs& A, char* smem, const int cv) {
  __builtin_amdgcn_s_waitcnt(0x0F70);
  u16* Kr = reinterpret_cast<u16*>(smem);
  u16* Uc = Kr + 8 * 2 * CKN;
  u16* Xr = Uc + 8 * CUN;
  u16* To = Uc + 8 * 2 * CUN;
  u16* Su = To + 32 * 8;
  u16* Sx = Su + 32 * 8;
  const int tid = threadIdx.x;
  const int w = __builtin_amdgcn_readfirstlane(tid >> 6);
  const int l = tid & 63, c = l & 15, g = l >> 4;
  const int d = 8 * cv + w;
  char* ws = A.ws;
  unsigned* FL = (unsigned*)(ws + OFF_FL);
  unsigned* TMO = FL + FL_TMO;
  u16* kr = Kr + w * 2 * CKN;
  float* prm = reinterpret_cast<float*>(kr);
  v4u tx[4];
  const v2u en = LD64(MKRS(ws + OFF_TAG, 8192 + 8), 0, 8192);
  {
    const unsigned pa = *reinterpret_cast<const unsigned*>(A.logAre + d * NST + 2 * l);
    const unsigned pb = *reinterpret_cast<const unsigned*>(A.Aim + d * NST + 2 * l);
    const unsigned pc = *reinterpret_cast<const unsigned*>(A.Cre + d * NST + 2 * l);
    const unsigned pd = *reinterpret_cast<const unsigned*>(A.Cim + d * NST + 2 * l);
    const float dt = bfr(expf(bf2f(A.logdt[d])));
    if (w < 2) tx[0] = *reinterpret_cast<const v4u*>(A.x0 + (size_t)tid * DM + 8 * cv);
    if (l < 16) reinterpret_cast<unsigned*>(Uc + w * CUN)[l] = 0u;
    if (l < 32) reinterpret_cast<unsigned*>(Uc + w * CUN + 32 + L_SEQ)[l] = 0u;
    if (l < 32) reinterpret_cast<unsigned*>(Uc + w * CUN + 32 + 128)[l] = 0u;
    if (l < 50) {
      reinterpret_cast<v4u*>(kr + 1808)[l] = v4u{0u, 0u, 0u, 0u};
      reinterpret_cast<v4u*>(kr + CKN + 1808)[l] = v4u{0u, 0u, 0u, 0u};
    }
#pragma unroll
    for (int e = 0; e < 2; ++e) {
      const float ar = -bfr(expf(e ? hi16(pa) : lo16(pa))) * dt, ai = (e ? hi16(pb) : lo16(pb)) * dt;
      const float ex = expf(ar);
      float sn, cs;
      sincosf(ai, &sn, &cs);
      prm[2 * l + e] = ar;
      prm[NST + 2 * l + e] = ai;
      prm[2 * NST + 2 * l + e] = (e ? hi16(pc) : lo16(pc)) * dt;
      prm[3 * NST + 2 * l + e] = (e ? hi16(pd) : lo16(pd)) * dt;
      prm[4 * NST + 2 * l + e] = ex * cs;
      prm[5 * NST + 2 * l + e] = ex * sn;
    }
  }
  const float dsk = bf2f(A.Dskip[d]);
  const u16* ka = kr + (c >> 3) * CKN + 2080 - 16 * (c & 7) + 8 * g;
  const unsigned ush = (c & 1) * 16;
#define KFRAG(D0) (*reinterpret_cast<const bf16x8*>(ka - (D0)))
  f32x4 acc[16], far[5];
  v4u xf[2];
  const unsigned* ub1 = reinterpret_cast<const unsigned*>(Uc + w * CUN + (c & ~1) + 8 * g + 32);
  const __amdgpu_buffer_rsrc_t rz1 = MKRS(ws + OFF_XA, SZ_X);
  const int t0 = 16 * (4 * (g & 1) + (g >> 1)) + c;
  {
    const u16* uc = Uc + w * CUN;
    const unsigned* ub = reinterpret_cast<const unsigned*>(uc + (c & ~1) + 8 * g + 32);
    const __amdgpu_buffer_rsrc_t rz = MKRS(ws + OFF_XA, SZ_X);
    const __amdgpu_buffer_rsrc_t rst = MKRS(ws + OFF_GT, L_SEQ * 4);
#define TILE_OUT(T)                                                                            \
  {                                                                                            \
    const float u0 = bf2f(uc[32 + 128 * (T) + t0]), u1 = bf2f(uc[64 + 128 * (T) + t0]);        \
    u16* xp = Xr + (128 * (T) + t0) * 8 + w;                                                   \
    const unsigned yy = pk2(y0, y1), uu = pk2(u0 * dsk, u1 * dsk);                             \
    const unsigned aa = pk2(lo16(yy) + lo16(uu), hi16(yy) + hi16(uu));                         \
    const unsigned oo = pk2(bf2f(xp[0]) + lo16(aa), bf2f(xp[256]) + hi16(aa));                 \
    xp[0] = (u16)oo;                                                                           \
    xp[256] = (u16)(oo >> 16);                                                                 \
  }
#define CHUNK_ST(ck)                                                                           \
  {                                                                                            \
    const v4u o = *reinterpret_cast<const v4u*>(Xr + (64 * (ck) + l) * 8);                     \
    ST128(o, rz, (l >> 5) * 32768 + (l & 31) * 16, (2 * (ck) * 64 + cv) * 512);                \
  }
#define U_ROW(row) U_ROW2(row, __builtin_amdgcn_raw_buffer_load_b32(rst, (row) * 4, 0, AUX_SC1))
#define U_ROW2(row, stv)                                                                       \
  {                                                                                            \
    const unsigned st_ = (stv);                                                                \
    const v4u xr_ = *reinterpret_cast<const v4u*>(Xr + (row) * 8);                             \
    const float mb_ = lo16(st_), ri_ = 1.0f / hi16(st_);                                       \
    _Pragma("unroll") for (int e = 0; e < 4; ++e) {                                            \
      const unsigned t_ = pk2(lo16(xr_[e]) - mb_, hi16(xr_[e]) - mb_);                         \
      const unsigned q_ = pk2(lo16(t_) * ri_, hi16(t_) * ri_);                                 \
      Uc[(2 * e) * CUN + 32 + (row)] = (u16)q_;                                                \
      Uc[(2 * e + 1) * CUN + 32 + (row)] = (u16)(q_ >> 16);                                    \
    }                                                                                          \
  }
    const unsigned* FS = FL + FL_FS + REP * 32 * 32;
    const int gi = 4 * (g * 8 + (l & 7));
    const int tl = 32 * ((l >> 3) & 1) + 8 * g + (l & 7);
    unsigned khl = 0u;
#pragma unroll 1
    for (int ps = 0; ps < 4; ++ps) {
      float v[32];
      {
        const f32x4 ar = *reinterpret_cast<const f32x4*>(prm + gi), ai = *reinterpret_cast<const f32x4*>(prm + NST + gi);
        const f32x4 cr = *reinterpret_cast<const f32x4*>(prm + 2 * NST + gi), ci = *reinterpret_cast<const f32x4*>(prm + 3 * NST + gi);
        const f32x4 wr = *reinterpret_cast<const f32x4*>(prm + 4 * NST + gi), wi = *reinterpret_cast<const f32x4*>(prm + 5 * NST + gi);
        const float p0 = (float)(64 * ps + 32 * ((l >> 3) & 1));
        float yr0[4], yi0[4];
#pragma unroll
        for (int k = 0; k < 4; ++k) {
          const float e = __expf(ar[k] * p0);
          float sn, cs;
          __sincosf(ai[k] * p0, &sn, &cs);
          const float zr = e * cs, zi = e * sn;
          yr0[k] = cr[k] * zr - ci[k] * zi;
          yi0[k] = cr[k] * zi + ci[k] * zr;
        }
        f32x2_t yra = {yr0[0], yr0[1]}, yia = {yi0[0], yi0[1]}, wra = {wr[0], wr[1]}, wia = {wi[0], wi[1]};
        f32x2_t yrb = {yr0[2], yr0[3]}, yib = {yi0[2], yi0[3]}, wrb = {wr[2], wr[3]}, wib = {wi[2], wi[3]};
#pragma unroll
        for (int i = 0; i < 32; ++i) {
          const f32x2_t sm2 = yra + yrb;
          v[i] = sm2.x + sm2.y;
          const f32x2_t ta = yra * wra - yia * wia, tb = yrb * wrb - yib * wib;
          yia = yra * wia + yia * wra;
          yib = yrb * wib + yib * wrb;
          yra = ta;
          yrb = tb;
        }
      }
#pragma unroll
      for (int i = 0; i < 16; ++i) v[i] = swap_add32(v[i], v[i + 16]);
#pragma unroll
      for (int i = 0; i < 8; ++i) {
        v[i] = swap_add16(v[i], v[i + 8]);
        v[i] += dppf<0xB1>(0.f, v[i]);
        v[i] += dppf<0x4E>(0.f, v[i]);
        v[i] += dppf<0x141>(0.f, v[i]);
      }
      float s0 = v[0], s1 = v[1], s2 = v[2], s3 = v[3], s4 = v[4], s5 = v[5], s6 = v[6], s7 = v[7];
      asm volatile("" : "+v"(s0), "+v"(s1), "+v"(s2), "+v"(s3), "+v"(s4), "+v"(s5), "+v"(s6), "+v"(s7));
      s0 = (l & 4) ? s4 : s0; s1 = (l & 4) ? s5 : s1; s2 = (l & 4) ? s6 : s2; s3 = (l & 4) ? s7 : s3;
      s0 = (l & 2) ? s2 : s0; s1 = (l & 2) ? s3 : s1;
      const float kv = 2.0f * ((l & 1) ? s1 : s0);
      khl = pk2(kv, 0.f);
      khl = pk2(kv, kv - lo16(khl));
      kr[2080 - 64 * ps - tl] = (u16)khl;
      kr[CKN + 2080 - 64 * ps - tl] = (u16)(khl >> 16);
      SST(ps);
      if (ps < 2) {
        if (ps == 0) {
          if (w < 2) {
            reinterpret_cast<v4u*>(Xr)[tid] = tx[0];
            const unsigned long long m = (((unsigned long long)en.y << 32) | en.x) + TAG_STEP;
            v4u g0;
            for (unsigned spins = 0; spins < 16u * SPIN_CAP; ++spins) {
              g0 = LD128(MKRS(ws + OFF_G0, 8192), tid * 16, REP * 2048);
              if (__all(((g0.x ^ g0.z) | (g0.y ^ (unsigned)m) | (g0.w ^ (unsigned)(m >> 32))) == 0u)) break;
              asm volatile("s_sleep 2" ::: "memory");
            }
            U_ROW2(tid, g0.x);
          }
          __syncthreads();
        }
        f32x4 a0t = {};
#pragma unroll
        for (int b = -1; b < 4; ++b) a0t = MFMA16(KFRAG(-32 * b), conv_bfrag(ub + 16 * b, ush), a0t);
        const float y0 = swap_add32(a0t[0], a0t[1]), y1 = swap_add32(a0t[2], a0t[3]);
        if ((g & 1) == ps) TILE_OUT(0);
        __syncthreads();
        if (w == ps) {
          CHUNK_ST(w);
          if (ps == 0) zero_wait(ws);
          DRAIN();
          if (l < 8) set_flag(FL + FL_FC + ((2 * w * 4 + l) * 8 + (cv >> 3)) * 32 + (cv & 7));
        }
        SST(4 + ps);
      }
      if (ps == 2) {
        if (w >= 2) tx[0] = *reinterpret_cast<const v4u*>(A.x0 + (size_t)tid * DM + 8 * cv);
#pragma unroll
        for (int i = 1; i < 4; ++i) tx[i] = *reinterpret_cast<const v4u*>(A.x0 + (size_t)(tid + 512 * i) * DM + 8 * cv);
      }
    }
    if (w >= 2) reinterpret_cast<v4u*>(Xr)[tid] = tx[0];
#pragma unroll
    for (int i = 1; i < 4; ++i) reinterpret_cast<v4u*>(Xr)[tid + 512 * i] = tx[i];
    {
      unsigned spins = 0;
      while (!__all(__hip_atomic_load((gu32*)FS + (w + 8 * ((l >> 3) & 3)) * 32 + (l & 7), RLX_AGENT) != 0u)) {
        if (spin_timeout(++spins, TMO)) break;
        __builtin_amdgcn_s_sleep(4);
      }
      asm volatile("" ::: "memory");
    }
    if (w >= 2) U_ROW(tid);
#pragma unroll
    for (int i = 1; i < 4; ++i) U_ROW(tid + 512 * i);
    SST(6);
    __syncthreads();
    {
      f32x4 a1t = {};
#pragma unroll
      for (int b = -1; b < 8; ++b) a1t = MFMA16(KFRAG(128 - 32 * b), conv_bfrag(ub + 16 * b, ush), a1t);
      const float y0 = swap_add32(a1t[0], a1t[1]), y1 = swap_add32(a1t[2], a1t[3]);
      TILE_OUT(1);
    }
    __syncthreads();
    if (w < 2) {
      CHUNK_ST(2 + w);
      DRAIN();
      if (l < 8) set_flag(FL + FL_FC + (((4 + 2 * w) * 4 + l) * 8 + (cv >> 3)) * 32 + (cv & 7));
    }
    SST(8);
    (void)wait_n<8, 1>(FL + FL_KG + cv * 32 + (w >> 2), 1, TMO);
    SST(7);
    {
      const __amdgpu_buffer_rsrc_t rk = MKRS(ws + OFF_KF + (size_t)d * 2 * CKN * 2, 2 * CKN * 2);
      v4u t[8];
#pragma unroll
      for (int i = 0; i < 4; ++i) {
        t[i] = LD128(rk, (64 * i + l) * 16, 0);
        t[4 + i] = LD128(rk, (64 * i + l) * 16, CKN * 2);
      }
#pragma unroll
      for (int i = 0; i < 4; ++i)
        if (64 * i + l < 229) {
          reinterpret_cast<v4u*>(kr)[64 * i + l] = t[i];
          reinterpret_cast<v4u*>(kr + CKN)[64 * i + l] = t[4 + i];
        }
      kr[2080 - 192 - tl] = (u16)khl;
      kr[CKN + 2080 - 192 - tl] = (u16)(khl >> 16);
    }
    asm volatile("" ::: "memory");
#define BULK(TA, TB, HOOK)                                                                      \
  {                                                                                            \
    const bf16x8 bm = conv_bfrag(ub - 16, ush);                                                \
    _Pragma("unroll") for (int T = (TA); T < (TB); ++T) acc[T] = MFMA16(KFRAG(128 * T + 32), bm, (f32x4{})); \
    _Pragma("unroll") for (int T = 11; T < 16; ++T) far[T - 11] = MFMA16(KFRAG(128 * T + 32), bm, (f32x4{})); \
    _Pragma("unroll 1") for (int r = 0; r < 4; ++r) {                                          \
      bf16x8 bf[TB];                                                                           \
      _Pragma("unroll") for (int j = 0; j < (TB); ++j) bf[j] = conv_bfrag(ub + 16 * r + 64 * j, ush); \
      _Pragma("unroll") for (int e = 0; e < (TB); ++e) {                                       \
        const bf16x8 a = KFRAG(128 * e - 32 * r);                                              \
        _Pragma("unroll") for (int T = (e > (TA) ? e : (TA)); T < (TB); ++T) acc[T] = MFMA16(a, bf[T - e], acc[T]); \
      }                                                                                        \
      if (r == 0) { HOOK }                                                                     \
    }                                                                                          \
  }
    BULK(2, 11, if (w == 1) (void)(wait_n<SLP_V, 32>(FL + FL_FZ + REP * 64, 64, TMO)););
    SST(10);
#pragma unroll
    for (int T = 2; T < 11; ++T) {
      const float y0 = swap_add32(acc[T][0], acc[T][1]), y1 = swap_add32(acc[T][2], acc[T][3]);
      TILE_OUT(T);
    }
    __syncthreads();
    if (w >= 2) {
#pragma unroll
      for (int i = 0; i < 3; ++i) CHUNK_ST(3 * w - 2 + i);
#pragma unroll
      for (int i = 0; i < 2; ++i) xf[i] = *reinterpret_cast<const v4u*>(Xr + (64 * ((18 + 2 * w + i) & 31) + l) * 8);
      DRAIN();
      if (l < 24) set_flag(FL + FL_FC + ((6 * w - 4) * 4 + l) * 8 * 32 + (cv >> 3) * 32 + (cv & 7));
    }
#define FAR_PIECE(p)                                                                           \
  {                                                                                            \
    if ((p) < 24) {                                                                            \
      const int o_ = 256 * ((p) >> 2) + 32 * ((p) & 3);                                        \
      const bf16x8 b0 = conv_bfrag(ub1 + (o_ >> 1), ush), b1 = conv_bfrag(ub1 + (o_ >> 1) + 64, ush); \
      _Pragma("unroll") for (int e = 10; e < 16; ++e) {                                        \
        const bf16x8 a = KFRAG(128 * e - o_);                                                  \
        if (e > 10) far[e - 11] = MFMA16(a, b0, far[e - 11]);                                  \
        if (e < 15) far[e - 10] = MFMA16(a, b1, far[e - 10]);                                  \
      }                                                                                        \
    } else if ((p) < 28) {                                                                     \
      bf16x8 bf[4];                                                                            \
      _Pragma("unroll") for (int j = 0; j < 4; ++j) bf[j] = conv_bfrag(ub1 + 16 * ((p) & 3) + 64 * (12 + j), ush); \
      _Pragma("unroll") for (int e = 0; e < 4; ++e) {                                          \
        const bf16x8 a = KFRAG(128 * e - 32 * ((p) & 3));                                      \
        _Pragma("unroll") for (int j = 0; j + e < 4; ++j) far[1 + e + j] = MFMA16(a, bf[j], far[1 + e + j]); \
      }                                                                                        \
    } else if ((p) == 28) {                                                                    \
                     \
      u16* ucw = Uc + w * CUN + 32 + t0;                                                       \
      _Pragma("unroll") for (int T = 11; T < 16; ++T) {                                        \
        const float y0 = swap_add32(far[T - 11][0], far[T - 11][1]), y1 = swap_add32(far[T - 11][2], far[T - 11][3]); \
        const float u0 = bf2f(ucw[128 * T]), u1 = bf2f(ucw[128 * T + 32]);                     \
        const unsigned yy = pk2(y0, y1), uu = pk2(u0 * dsk, u1 * dsk);                         \
        const unsigned aa = pk2(lo16(yy) + lo16(uu), hi16(yy) + hi16(uu));                     \
        ucw[128 * T] = (u16)aa;                                                                \
        ucw[128 * T + 32] = (u16)(aa >> 16);                                                   \
      }                                                                                        \
    } else if (w >= 2 && w < 7) {                                                              \
      int l = tid & 63;                                                                        \
      asm volatile("" : "+v"(l), "+v"(xf[0]), "+v"(xf[1]));                                    \
      if ((p) == 29) {                                                                         \
        _Pragma("unroll") for (int i = 0; i < 2; ++i) {                                        \
          const int ck = 18 + 2 * w + i;                                                       \
          const u16* ur = Uc + 32 + 64 * ck + l;                                               \
          v4u o;                                                                               \
          _Pragma("unroll") for (int e = 0; e < 4; ++e)                                        \
            o[e] = pk2(lo16(xf[i][e]) + bf2f(ur[(2 * e) * CUN]), hi16(xf[i][e]) + bf2f(ur[(2 * e + 1) * CUN])); \
          ST128(o, rz1, (l >> 5) * 32768 + (l & 31) * 16, (2 * ck * 64 + cv) * 512);           \
        }                                                                                      \
      } else {                                                                                 \
        DRAIN();                                                                               \
        if (l < 16) set_flag(FL + FL_FC + (((36 + 4 * w) * 4 + l) * 8 + (cv >> 3)) * 32 + (cv & 7)); \
      }                                                                                        \
    }                                                                                          \
  }
#undef BULK
    SST(9);
#undef TILE_OUT
#undef CHUNK_ST
#undef U_ROW
#undef U_ROW2
  }
  u16* uc = Uc + (8 + w) * CUN;
  const unsigned* ub = reinterpret_cast<const unsigned*>(uc + (c & ~1) + 8 * g + 32);
#pragma unroll
  for (int j = 0; j < 16; ++j) acc[j] = f32x4{};
  const __amdgpu_buffer_rsrc_t rx = MKRS(ws + OFF_X2, SZ_X), rz = MKRS(ws + OFF_X3, SZ_X);
  const __amdgpu_buffer_rsrc_t rf = MKRS(FL + FL_FR, 64 * 16 * 128);
  unsigned st = 0u;
  v2u xx = {0u, 0u};
#define X2_LOAD(ss, far)                                                                                                      \
  {                                                                                                                      \
    xx = LD64(rx, (l >> 1) * (DM * 2) + (l & 1) * 8, (far) ? (int)SZ_X : ((ss) * SEG * DM + 8 * cv) * 2);                 \
    st = __builtin_amdgcn_raw_buffer_load_b32(rf, (l >> 4) * 128 + ((l >> 1) & 7) * 4, ((ss) * 4 + REP) * 512, AUX_SC1); \
  }
  unsigned tl = now10();
  for (int s = 0; s < NSEG; ++s) {
    if (w == 1) {
      for (unsigned spins = 0; !__all(umin3(xx.x, xx.y, st) != 0u); ++spins) {
        if (spin_timeout(spins, TMO)) break;
        if (GATE_V && spins == 0 && !__any(st != 0u)) gate(tl);
        if (spins && !__all(st != 0u)) __builtin_amdgcn_s_sleep(SLP_V);
        X2_LOAD(s, __builtin_popcountll(__ballot(st != 0u)) < CVTHR);
      }
      tl = now10() + GATE_V;
      TS(0, s, 64);
      const float mb = lo16(st), ri = 1.0f / hi16(st);
      const unsigned t0 = pk2(lo16(xx.x) - mb, hi16(xx.x) - mb), t1 = pk2(lo16(xx.y) - mb, hi16(xx.y) - mb);
      *reinterpret_cast<v2u*>(Su + l * 4) = v2u{pk2(lo16(t0) * ri, hi16(t0) * ri), pk2(lo16(t1) * ri, hi16(t1) * ri)};
      *reinterpret_cast<v2u*>(Sx + l * 4) = xx;
    }
    const int q = s & 3;
    const u16* kq = ka + 32 * q;
    const bf16x8 a0 = *reinterpret_cast<const bf16x8*>(kq - 32), a1 = *reinterpret_cast<const bf16x8*>(kq);
    __syncthreads();
    TS(1, s, 0);
    if (s == 0)
      for (int x = l; x < CUN / 2; x += 64) reinterpret_cast<unsigned*>(uc)[x] = 0u;
    if (l < 32) uc[32 + 32 * s + l] = Su[l * 8 + w];
    asm volatile("s_waitcnt lgkmcnt(0)" ::: "memory");
    const bf16x8 bp = conv_bfrag(ub + 16 * (s - 1), ush);
    acc[0] = MFMA16(a0, bp, (q == 0 ? acc[1] : acc[0]));
    const f32x4 tmp = MFMA16(a1, conv_bfrag(ub + 16 * s, ush), acc[0]);
    float y0 = (q & 1) ? tmp[2] : tmp[0], y1 = (q & 1) ? tmp[3] : tmp[1];
    y0 = xsum32(y0);
    y1 = xsum32(y1);
    if (g == (q >> 1)) {
      const float u0 = bf2f(Su[c * 8 + w]), u1 = bf2f(Su[(16 + c) * 8 + w]);
      const float a0 = bfr(bfr(y0) + bfr(u0 * dsk)), a1 = bfr(bfr(y1) + bfr(u1 * dsk));
      To[c * 8 + w] = f2bf(bf2f(Sx[c * 8 + w]) + a0);
      To[(16 + c) * 8 + w] = f2bf(bf2f(Sx[(16 + c) * 8 + w]) + a1);
    }
    __syncthreads();
    if (w == 0 && l < 32) {
      const v4u o = *reinterpret_cast<const v4u*>(To + l * 8);
      ST128(o, rz, l * 16, (s * 64 + cv) * 512);
    }
    if (w == 1) X2_LOAD(s + 1, CVTHR > 0);
    const int left = 15 - (s >> 2);
#define PUSH(J0, SH)                                                                                              \
  if (left >= (J0)) {                                                                                             \
    _Pragma("unroll") for (int j = (J0); j < (J0) + 4 && j < 16; ++j)                                             \
      acc[j] = MFMA16(*reinterpret_cast<const bf16x8*>(kq - 32 - 128 * j), bp, (j + (SH) < 16 ? acc[(j + (SH)) & 15] : f32x4{})); \
  }
    TS(2, s, 0);
    if (w == 0) {
      DRAIN();
      if (l < 4) set_flag(FL + FL_FC + (((64 + s) * 4 + l) * 8 + (cv >> 3)) * 32 + (cv & 7));
    }
    TS(3, s, 0);
    if (q == 0) { PUSH(1, 1) PUSH(5, 1) PUSH(9, 1) PUSH(13, 1) }
    else { PUSH(1, 0) PUSH(5, 0) PUSH(9, 0) PUSH(13, 0) }
#undef PUSH
    MSTAMP();
    if (s >= CVPRE && s < 31 + CVPRE) FAR_PIECE(s - CVPRE);
  }
#undef KFRAG
#undef X2_LOAD
#undef FAR_PIECE
}

__device__ __forceinline__ v4u ln_row_packed(const v4u xr) {
  float v[8];
#pragma unroll
  for (int e = 0; e < 4; ++e) { v[2 * e] = lo16(xr[e]); v[2 * e + 1] = hi16(xr[e]); }
  float s = ((v[0] + v[1]) + (v[2] + v[3])) + ((v[4] + v[5]) + (v[6] + v[7]));
  s = wave_sum(s);
  const float mean = s * (1.0f / DM);
  float q = 0.f;
#pragma unroll
  for (int j = 0; j < 8; ++j) { float t = v[j] - mean; q += t * t; }
  q = wave_sum(q);
  const float var = q * (1.0f / DM);
  const float mb = bfr(mean);
  const float sd = bfr(sqrtf(bfr(bfr(var) + 1e-5f)));
  const float ri = 1.0f / sd;
  v4u o;
#pragma unroll
  for (int e = 0; e < 4; ++e) {
    const unsigned t = pk2(v[2 * e] - mb, v[2 * e + 1] - mb);
    const float x0 = lo16(t), x1 = hi16(t);
    float q0 = x0 * ri, q1 = x1 * ri;
    q0 = __builtin_fmaf(__builtin_fmaf(-q0, sd, x0), ri, q0);
    q1 = __builtin_fmaf(__builtin_fmaf(-q1, sd, x1), ri, q1);
    o[e] = pk2(q0, q1);
  }
  return o;
}

__device__ __forceinline__ float bfr1(float x) { return lo16(pk2(x, x)); }
__device__ __forceinline__ void ln4_packed(v4u (&x)[4]) {
  f32x2_t v[16];
#pragma unroll
  for (int e = 0; e < 16; ++e) v[e] = f32x2_t{lo16(x[e >> 2][e & 3]), hi16(x[e >> 2][e & 3])};
  const f32x2_t t = (((v[0] + v[1]) + (v[2] + v[3])) + ((v[4] + v[5]) + (v[6] + v[7]))) +
                    (((v[8] + v[9]) + (v[10] + v[11])) + ((v[12] + v[13]) + (v[14] + v[15])));
  const float mean = red_mod4(t.x + t.y) * (1.0f / DM);
  const float mb = bfr1(mean);
  f32x2_t q0 = {0.f, 0.f}, q1 = {0.f, 0.f};
#pragma unroll
  for (int e = 0; e < 16; e += 2) {
    v[e] -= mb;
    v[e + 1] -= mb;
    q0 += v[e] * v[e];
    q1 += v[e + 1] * v[e + 1];
  }
  q0 += q1;
  const float dm = mean - mb;
  const float var = fmaxf(red_mod4(q0.x + q0.y) * (1.0f / DM) - dm * dm, 0.f);
  const float sd = bfr1(sqrtf(bfr1(bfr1(var) + 1e-5f)));
  const float ri = 1.0f / sd;
#pragma unroll
  for (int e = 0; e < 16; ++e) {
    const unsigned t2 = pk2(v[e].x, v[e].y);
    const f32x2_t q = f32x2_t{lo16(t2), hi16(t2)} * ri;
    x[e >> 2][e & 3] = pk2(q.x, q.y);
  }
}

extern "C" __device__ float mul1(float, float) __asm("llvm.amdgcn.fmul.legacy");
template <bool G0>
__device__ __forceinline__ void role_qkv_t(const MegaArgs& A, char* smem, const int L, const int h) {
  __builtin_amdgcn_s_waitcnt(0x0F70);
  const int w = __builtin_amdgcn_readfirstlane(threadIdx.x >> 6);
  char* ws = A.ws;
  unsigned* FL = (unsigned*)(ws + OFF_FL);
  unsigned* TMO = FL + FL_TMO;
#define LANE_IDS()                         \
  const int l = lane_id();                 \
  const int c = l & 15, g = l >> 4;        \
  (void)c; (void)g;
  unsigned* FC = FL + FL_FC + (L * 64 * 4 + REP) * 8 * 32;
  const __amdgpu_buffer_rsrc_t rsQ = MKRS(ws + OFF_QK + L * SZ_QK, SZ_QK);
  const __amdgpu_buffer_rsrc_t rsV = MKRS(ws + OFF_VF + L * SZ_O, SZ_O);
  const __amdgpu_buffer_rsrc_t rsM = MKRS(ws + (G0 ? OFF_MEMK : OFF_MEMV) + L * SZ_MEM, SZ_MEM);
  bf16x8 wq[16], wk[16], wv[16];
  {
    LANE_IDS();
    const unsigned sb = lds_base(smem);
    const int q = (l >> 2) & 3, p = l & 3, f = q | ((g & 1) << 2);
    const unsigned ra = sb + (8 * g + q) * 256 + (w ^ f) * 32 + p * 8;
#define W_STAGE(W, hf, VM, NEXT)                                               \
  {                                                                           \
    WAIT_VM(VM);                                                              \
    RAW_BAR();                                                                \
    _Pragma("unroll") for (int t = 0; t < 8; ++t) {                           \
      const unsigned a_ = ra + (hf) * 8192 + t * 16384;                       \
      const v2u r0 = trd(a_), r1 = trd(a_ + 1024);                            \
      W[8 * (hf) + t] = BF8((v4u{r0.x, r0.y, r1.x, r1.y}));                   \
    }                                                                         \
    _Pragma("unroll") for (int t = 0; t < 8; ++t) asm volatile("" : "+v"(W[8 * (hf) + t])); \
    RAW_BAR();                                                                \
    NEXT;                                                                     \
  }
    wqkv_issue<false>(A.Wq, 0, h, w, l, sb);
    wqkv_issue<false>(A.Wq, 1, h, w, l, sb);
    W_STAGE(wq, 0, 8, wqkv_issue<false>(A.Wk, 0, h, w, l, sb));
    SST(4);
    W_STAGE(wq, 1, 8, wqkv_issue<false>(A.Wk, 1, h, w, l, sb));
    SST(5);
    W_STAGE(wk, 0, 8, wqkv_issue<false>(A.Wv, 0, h, w, l, sb));
    W_STAGE(wk, 1, 8, wqkv_issue<false>(A.Wv, 1, h, w, l, sb));
    SST(6);
    W_STAGE(wv, 0, 8, (void)0);
    W_STAGE(wv, 1, 0, (void)0);
#undef W_STAGE
  }
  WSTAMP2();
  constexpr int Q_X = 98304;
  const unsigned sb = lds_base(smem);
  v2u eq, ek, ev;
  {
    LANE_IDS();
    const v4u zt = LD128(MKRS(ws + OFF_TAG, 8192 + 8), l * 16, (blockIdx.x & 7) * 1024);
    const v2u ze = LD64(MKRS(ws + OFF_TAG, 8192 + 8), 0, 8192);
    const v4u one = {0x3f803f80u, 0x3f803f80u, 0x3f803f80u, 0x3f803f80u};
    f32x4 t[3] = {};
#pragma unroll
    for (int ks = 0; ks < 16; ++ks) {
      t[0] = MFMA16(BF8(one), wq[ks], t[0]);
      t[1] = MFMA16(BF8(one), wk[ks], t[1]);
      t[2] = MFMA16(BF8(one), wv[ks], t[2]);
    }
    v2u e[3];
#pragma unroll
    for (int i = 0; i < 3; ++i) {
      const float x = g ? 0.f : t[i][0], x1 = x - bfr1(x);
      e[i] = v2u{pk2(x, x1), pk2(x1 - bfr1(x1), 0.f)};
    }
    eq = e[0]; ek = e[1]; ev = e[2];
    asm volatile("" : "+v"(eq), "+v"(ek), "+v"(ev));
    if (!zero_seen(zt, ze)) zero_wait(ws);
    if (l < 32) *reinterpret_cast<v4u*>(smem + Q_X + w * 2048 + (l >> 4) * 16384 + c * 16) = one;
  }
  constexpr int Q_CNT = Q_X + 11 * 2048 + 256;
  lu32* cnt = (lu32*)(unsigned long)(sb + Q_CNT);
  if (threadIdx.x < 16) cnt[threadIdx.x] = 0u;
#define C_ADD(i) asm volatile("s_mov_b64 exec, 1\n\tds_add_u32 %0, %1\n\ts_mov_b64 exec, -1" ::"v"(sb + Q_CNT + 4 * (i)), "v"(1u) : "memory")
#define C_RD(i) (*(volatile lu32*)(cnt + (i)))
#define C_GET(i) ((unsigned)__builtin_amdgcn_readfirstlane(C_RD(i)))
#define C_WAIT(i, n)                                                                                                    \
  {                                                                                                                     \
    for (unsigned spins = 0; C_GET(i) < (n) && spins < 64u * SPIN_CAP; ++spins) __builtin_amdgcn_s_sleep(1);             \
    asm volatile("" ::: "memory");                                                                                      \
  }
  const char* Xb = ws + (L ? OFF_X3 : OFF_XA) + w * 8192;
  const v4u rsF = mk_srd(FC + w * 64, (64 * 32 - REP * 8 - 2 * w) * 128);
  unsigned l16, vo, fo;
  {
    LANE_IDS();
    l16 = l * 16;
    vo = (c + 16 * (2 * (w & 1) + (g >> 1))) * 16 + 4096 * (g & 1);
    fo = g * 512 + c * 16;
  }
#define DMA4(x) "global_load_lds_dwordx4 %1, " x " sc1\n\tglobal_load_lds_dwordx4 %1, " x " offset:1024 sc1\n\t"       \
                "global_load_lds_dwordx4 %1, " x " offset:2048 sc1\n\tglobal_load_lds_dwordx4 %1, " x " offset:3072 sc1\n\t"
#define X_DMA(ss, bb)                                                                                                   \
  {                                                                                                                     \
    unsigned keep_;                                                                                                     \
    const char* x_ = Xb + (size_t)(ss) * 32768;                                                                         \
    const unsigned d_ = sb + (bb) * 32768 + w * 8192;                                                                   \
    asm volatile("s_nop 4\n\ts_mov_b32 %0, m0\n\ts_mov_b32 m0, %4\n\ts_nop 0\n\t" DMA4("%2")                           \
                 "s_mov_b32 m0, %5\n\ts_nop 0\n\t" DMA4("%3") "s_mov_b32 m0, %0"                                         \
                 : "=&s"(keep_)                                                                                         \
                 : "v"(l16), "s"(x_), "s"(x_ + 4096), "s"(d_), "s"(d_ + 4096u)                                           \
                 : "memory");                                                                                           \
    TS(0, ss, 0);                                                                                                       \
  }
#define X_DMA1(on, x, d, off)                                                                                           \
  {                                                                                                                     \
    unsigned keep_;                                                                                                     \
    asm volatile("s_cmp_eq_u32 %4, 0\n\ts_cbranch_scc1 1f\n\ts_mov_b32 %0, m0\n\ts_mov_b32 m0, %3\n\ts_nop 0\n\t"       \
                 "global_load_lds_dwordx4 %1, %2 offset:%5 sc1\n\ts_mov_b32 m0, %0\n1:"                                  \
                 : "=&s"(keep_)                                                                                         \
                 : "v"(l16), "s"(x), "s"(d), "s"(on), "n"(off)                                                          \
                 : "memory", "scc");                                                                                    \
  }
#define F_LOAD(r, ss)                                                                                                   \
  asm volatile("s_nop 4\n\tbuffer_load_dword %0, %1, %2, %3 offen sc1"                                                  \
               : "=v"(r)                                                                                                \
               : "v"(((l16 >> 2) & 28) + (l16 & 128)), "s"(rsF), "s"((ss) * 4096)                                       \
               : "memory")
  unsigned* FQ = FL + FL_FQ + (L * 64 * 32 + h) * 32;
#define Q_FLAG(ss)                                                                                                      \
  {                                                                                                                     \
    unsigned old_;                                                                                                      \
    asm volatile("s_mov_b64 exec, 1\n\tds_add_rtn_u32 %0, %1, %2\n\ts_mov_b64 exec, -1\n\ts_waitcnt lgkmcnt(0)"           \
                 : "=&v"(old_) : "v"(sb + Q_CNT + ((ss) & 1) * 4), "v"(1u) : "memory");                                 \
    if ((__builtin_amdgcn_readfirstlane(old_) & 7u) == 7u) {                                                            \
      asm volatile("s_mov_b64 exec, 1\n\tglobal_store_dword %0, %1, %2 sc1\n\ts_mov_b64 exec, -1"                        \
                   ::"v"(0), "v"(1u), "s"(FQ + (ss) * 1024) : "memory");                                                \
      TS(3, ss, threadIdx.x & ~63u);                                                                                    \
    }                                                                                                                   \
    pend = false;                                                                                                       \
  }
  __builtin_amdgcn_s_waitcnt(0x0F70);
  RAW_BAR();
  bool have = false, pend = false;
  unsigned tl = now10();
  const unsigned tq = tl - QPL;
  int b = 0;
  unsigned k4 = 4u;
  FSD;
  for (int s = 0; s < NSEG; ++s) {
    FS(0);
    const int b1 = b == 2 ? 0 : b + 1;
    const unsigned u1 = 2u * (b1 ? k4 - 4u : k4);
    unsigned nf;
    if (QPACE && L == 0 && s >= QPS && (int)(tq + s * QPACE - now10()) > 0) {
      if (pend) {
        WAIT_VM(0);
        Q_FLAG(s - 1);
      }
      gate(tq + s * QPACE);
    }
    if (G0) {
      if (!have) {
        for (unsigned spins = 0;; ++spins) {
          unsigned n;
          F_LOAD(n, s);
          WAIT_VM(0);
          if (pend) Q_FLAG(s - 1);
          if (all_set(n) || spin_timeout(spins, TMO)) break;
          if (GATE_Q && spins == 0) gate(tl);
          else if (L) __builtin_amdgcn_s_sleep(SLP_Q);
          else __builtin_amdgcn_s_sleep(SLP_Q1);
        }
        tl = now10() + GATE_Q;
        C_WAIT(8 + b, 2u * k4 - 8u);
        X_DMA(s, b);
        WAIT_VM(0);
        C_ADD(4 + b);
      }
      const unsigned c0 = C_RD(4 + b);
      F_LOAD(nf, s + 1);
      if ((unsigned)__builtin_amdgcn_readfirstlane(c0) < k4) C_WAIT(4 + b, k4);
    } else {
      if (C_GET(4 + b) < k4) {
        if (pend) {
          WAIT_VM(0);
          Q_FLAG(s - 1);
        }
        C_WAIT(4 + b, k4);
      }
      FS(1);
      const char* sp = smem + ((l16 & 128) ? Q_X + ((w + 1) & 1) * 128 : b * 32768 + fo + (w & 3) * 128);
      f32x4 gm = {};
      __builtin_amdgcn_s_setprio(3);
      {
        bf16x8 a[10];
#pragma unroll
        for (int ks = 0; ks < 10; ++ks) a[ks] = *reinterpret_cast<const bf16x8*>(sp + 2048 * ks);
        __builtin_amdgcn_sched_barrier(0);
#pragma unroll
        for (int ks = 0; ks < 16; ++ks) {
          gm = MFMA16(a[ks % 10], a[ks % 10], gm);
          if (ks < 6) a[ks] = *reinterpret_cast<const bf16x8*>(sp + 2048 * (10 + ks));
          __builtin_amdgcn_sched_barrier(0);
        }
      }
      float q2 = (l16 & 16) ? gm[1] : gm[0], q3 = (l16 & 16) ? gm[3] : gm[2];
      asm volatile("" : "+v"(q2), "+v"(q3));
      q2 = (l16 & 32) ? q3 : q2;
      auto sw = __builtin_amdgcn_permlane32_swap(__float_as_uint(q2), __float_as_uint(q2), false, false);
      const float mean = __uint_as_float(sw[1]) * (-1.0f / DM);
      const float var = fmaxf(__builtin_fmaf(-mean, mean, q2 * (1.0f / DM)), 0.f);
      const float sd = bfr1(__builtin_amdgcn_sqrtf(bfr1(bfr1(var) + 1e-5f)));
      asm volatile("s_mov_b64 exec, 0xf0000f\n\tds_write2_b32 %0, %1, %2 offset1:32\n\ts_mov_b64 exec, -1"
                   ::"v"(sb + Q_X + (8 + b) * 2048 + 256 + (w & 3) * 32 + ((l16 >> 2) & 28)), "v"(pk2(mean, mean)),
                   "v"(__builtin_amdgcn_rcpf(sd)) : "memory");
      C_ADD(12 + b);
      FS(2);
      if (pend) {
        WAIT_VM(0);
        Q_FLAG(s - 1);
      }
    }
    TS(G0 ? 1 : 5, s, G0 ? 0 : 256);
    FS(3);
    const char* ap = smem + b * 32768 + fo;
#define AFRAG(i) (*reinterpret_cast<const bf16x8*>(ap + ((i) >> 4) * 256 + 2048 * ((i) & 15)))
    bf16x8 abuf[QPF + 1];
#pragma unroll
    for (int i = 0; i < QPF; ++i) abuf[i] = AFRAG(i);
    f32x4 aq[2] = {}, ak[2] = {}, av[2] = {};
    unsigned cx = 0u;
    int on = 0;
    const char* xn = Xb + (size_t)(s + 1) * 32768;
    const unsigned dn = sb + b1 * 32768 + w * 8192;
    __builtin_amdgcn_s_setprio(G0 ? 1 : 0);
#pragma unroll
    for (int rt = 0; rt < 2; ++rt) {
#pragma unroll
      for (int ks = 0; ks < 16; ++ks) {
        const int i = 16 * rt + ks;
        if (i + QPF < 32) abuf[(i + QPF) % (QPF + 1)] = AFRAG(i + QPF);
        if (G0 && i == 11) cx = C_RD(8 + b1);
        if (i == 28) cx = C_RD(12 + b);
        const bf16x8 a = abuf[i % (QPF + 1)];
        aq[rt] = MFMA16(wq[ks], a, aq[rt]);
        ak[rt] = MFMA16(wk[ks], a, ak[rt]);
        av[rt] = MFMA16(a, wv[ks], av[rt]);
        if (i == QUP) { __builtin_amdgcn_s_setprio(G0 ? 3 : 2); FS(4); }
        __builtin_amdgcn_sched_barrier(0);
        if (G0 && rt == 1 && ks < 8) {
          X_DMA1(on, xn + (ks >> 2) * 4096, dn + (ks >> 2) * 4096, (ks & 3) * 1024);
          __builtin_amdgcn_sched_barrier(0);
        }
      }
      asm volatile("" : "+v"(aq[rt]), "+v"(ak[rt]), "+v"(av[rt]));
      if (rt == 0) FS(5); else FS(7);
      if (G0 && rt == 0) {
        WAIT_VM(0);
        if (pend) Q_FLAG(s - 1);
        have = all_set(nf) && (unsigned)__builtin_amdgcn_readfirstlane(cx) >= u1;
        F_LOAD(nf, have ? NSEG : s + 1);
        if (have) TS(0, s + 1, 0);
        on = __builtin_amdgcn_readfirstlane((int)have);
        FS(6);
      }
    }
    if ((unsigned)__builtin_amdgcn_readfirstlane(cx) < k4) C_WAIT(12 + b, k4);
    FS(8);
    v4u vfrag, qf4, kf4;
    {
      const char* stp = smem + Q_X + (8 + b) * 2048 + 256;
      unsigned nm[2];
      float ri[2];
      f32x4 rv[2];
#pragma unroll
      for (int rt = 0; rt < 2; ++rt) {
        nm[rt] = *reinterpret_cast<const unsigned*>(stp + 64 * rt + ((l16 >> 2) & 60));
        ri[rt] = *reinterpret_cast<const float*>(stp + 128 + 64 * rt + ((l16 >> 2) & 60));
        rv[rt] = *reinterpret_cast<const f32x4*>(stp + 128 + 64 * rt + ((l16 >> 4) & 48));
      }
#pragma unroll
      for (int rt = 0; rt < 2; ++rt) {
        const v2u xm = {nm[rt], nm[rt]};
        aq[rt] = MFMA16K(eq, xm, aq[rt]);
        ak[rt] = MFMA16K(ek, xm, ak[rt]);
        av[rt] = MFMA16K(xm, ev, av[rt]);
      }
      __builtin_amdgcn_s_setprio(QEP);
#pragma unroll
      for (int rt = 0; rt < 2; ++rt) {
        f32x4 tq, tk, tv;
#pragma unroll
        for (int e = 0; e < 4; ++e) {
          tq[e] = mul1(aq[rt][e], ri[rt]);
          tk[e] = mul1(ak[rt][e], ri[rt]);
          tv[e] = mul1(av[rt][e], rv[rt][e]);
        }
        qf4[2 * rt] = pk2(tq[0], tq[1]); qf4[2 * rt + 1] = pk2(tq[2], tq[3]);
        kf4[2 * rt] = pk2(tk[0], tk[1]); kf4[2 * rt + 1] = pk2(tk[2], tk[3]);
        vfrag[2 * rt] = pk2(tv[0], tv[1]); vfrag[2 * rt + 1] = pk2(tv[2], tv[3]);
      }
    }
    {
      auto q0 = __builtin_amdgcn_permlane16_swap(qf4.x, qf4.z, false, false);
      auto q1 = __builtin_amdgcn_permlane16_swap(qf4.y, qf4.w, false, false);
      auto k0 = __builtin_amdgcn_permlane16_swap(kf4.x, kf4.z, false, false);
      auto k1 = __builtin_amdgcn_permlane16_swap(kf4.y, kf4.w, false, false);
      v4u qo = {q0[0], q1[0], q0[1], q1[1]}, ko = {k0[0], k1[0], k0[1], k1[1]};
      const int soff = ((s * NH + h) * 16 + (w >> 1)) * 1024;
      asm volatile("" : "+v"(qo), "+v"(ko), "+v"(vfrag));
      FS(9);
      C_ADD(8 + b);
      if (G0 && have) {
        WAIT_VM(0);
        C_ADD(4 + b1);
      }
      TS(G0 ? 2 : 6, s, G0 ? 0 : 256);
      FS(10);
      ST128(ko, rsQ, vo, soff + 8192);
      ST128(qo, rsQ, vo, soff);
      ST128(vfrag, rsV, l16, ((s * NH + h) * 512 + w * 64) * 16);
      ST64((v2u{0u, 0u}), rsM, l16 >> 1, (s * NH + h) * 2048 + (w & 3) * 512);
      pend = true;
      FS(11);
    }
    if (G0) {
      WAIT_VM(4);
      if (all_set(nf) && C_GET(8 + b1) >= u1) {
        X_DMA(s + 1, b1);
        have = true;
        WAIT_VM(8);
        Q_FLAG(s);
        WAIT_VM(0);
        C_ADD(4 + b1);
      }
    }
    MSTAMP();
    FD(s);
    b = b1;
    if (b == 0) k4 += 4u;
  }
  DRAIN();
  if (pend) Q_FLAG(NSEG - 1);
#undef LANE_IDS
#undef DMA4
#undef X_DMA
#undef X_DMA1
#undef F_LOAD
#undef Q_FLAG
#undef AFRAG
#undef C_ADD
#undef C_RD
#undef C_GET
#undef C_WAIT
}
__device__ __forceinline__ void role_qkv(const MegaArgs& A, char* smem, const int L, const int h) {
  if (__builtin_amdgcn_readfirstlane(threadIdx.x >> 6) < 4) role_qkv_t<true>(A, smem, L, h);
  else role_qkv_t<false>(A, smem, L, h);
}

constexpr int MEGA_WGS = 256;
constexpr size_t MEGA_LDS = (size_t)(32 * 4 * 64 * 8 + 8 * SMS + 15 * QS + 128 + 16 * 64 * 8 + 8 * SOS + 512) * 2;
static_assert(MEGA_LDS <= 163840 && MEGA_LDS >= 162816 + 256, "lds");
static_assert(MEGA_LDS >= (size_t)AW_CNT + 64, "lds");
static_assert(MEGA_LDS >= (size_t)(8 * 2 * CKN + 8 * 2 * CUN + 4 * 32 * 8) * 2, "lds");
static_assert(MEGA_LDS >= (size_t)2 * 32 * SMS * 2 && MEGA_LDS >= 131072, "lds");
static_assert(131072 >= (size_t)(40 * NST + 32 * 400) * 4 && 131072 + 4 * 392 * 4 <= AW_CNT, "lds");
__global__ __launch_bounds__(512, 2) void mega_kernel(const u16* mem0, const u16* Dskip, char* ws, u16* out, const u16* x0,
                                                      const u16* logAre, const u16* Aim, const u16* Cre, const u16* Cim,
                                                      const u16* logdt, const u16* Wq, const u16* Wk, const u16* Wv,
                                                      const u16* Wo) {
  extern __shared__ __attribute__((aligned(16))) char smem[];
  const int b = __builtin_amdgcn_readfirstlane(role_no());
  const MegaArgs A{mem0, Dskip, ws, out, x0, logAre, Aim, Cre, Cim, logdt, Wq, Wk, Wv, Wo, b};
  {
    const int hi = (b >= 32 && b < 96) ? b - 32 : (b >= 200 && b < 248) ? b - 136 : (b >= 96 && b < 104) ? b + 16 : (b >= 248 ? b - 128 : -1);
    if (hi >= 0) start_kgen(A, smem, hi, b - 248, (b >= 64 && b < 104) || (b >= 232 && b < 240) ? b & 15 : -1);
  }
  if ((EXP_OFF & 1) && b >= 64 && b < 80) return;
  if ((EXP_OFF & 8) && b >= 200 && b < 232) return;
  if ((EXP_OFF & 16) && b >= 32 && b < 64) return;
  if ((EXP_OFF & 32) && b >= 80 && b < 96) return;
  if (b < 64) role_chain(A, smem, b >> 5, b & 31, 0);
  else if (b < 104 || (b >= 232 && b < 240)) role_aw(A, smem, b < 80 ? 0 : b < 96 ? 2 : 1, b & 15);
  else if (b < 168) role_conv(A, smem, b - 104);
  else if (b < 232) role_qkv(A, smem, (b - 168) >> 5, (b - 168) & 31);
  else role_red(A, smem, b - 240);
}

extern "C" void kernel_launch(void* const* d_in, const int* in_sizes, int n_in, void* d_out, int out_size, void* d_ws,
                              size_t ws_size, hipStream_t stream) {
  MegaArgs A;
  A.x0 = (const u16*)d_in[0];
  A.logAre = (const u16*)d_in[1];
  A.Aim = (const u16*)d_in[2];
  A.Cre = (const u16*)d_in[3];
  A.Cim = (const u16*)d_in[4];
  A.logdt = (const u16*)d_in[5];
  A.Dskip = (const u16*)d_in[6];
  A.mem0 = (const u16*)d_in[8];
  A.Wq = (const u16*)d_in[9];
  A.Wk = (const u16*)d_in[10];
  A.Wv = (const u16*)d_in[11];
  A.Wo = (const u16*)d_in[12];
  A.ws = (char*)d_ws;
  A.out = (u16*)d_out;

  (void)hipFuncSetAttribute((const void*)mega_kernel, hipFuncAttributeMaxDynamicSharedMemorySize, (int)MEGA_LDS);
  mega_kernel<<<MEGA_WGS, 512, MEGA_LDS, stream>>>(A.mem0, A.Dskip, A.ws, A.out, A.x0, A.logAre, A.Aim, A.Cre, A.Cim, A.logdt,
                                                   A.Wq, A.Wk, A.Wv, A.Wo);
}
```

```cpp
#include <hip/hip_runtime.h>
#include <hip/hip_bf16.h>
#include <stdint.h>

constexpr int L_SEQ = 2048;
constexpr int DM    = 512;
constexpr int NST   = 128;
constexpr int NH    = 32;
constexpr int DH    = 128;
constexpr int HD    = NH * DH;
constexpr int MM    = 8;
constexpr int SEG   = 32;
constexpr int NSEG  = L_SEQ / SEG;
constexpr int LK    = MM + SEG;

typedef unsigned short u16;
using bf16x8 = __attribute__((ext_vector_type(8))) short;
using f32x4  = __attribute__((ext_vector_type(4))) float;
using f32x16 = __attribute__((ext_vector_type(16))) float;

static __device__ __forceinline__ u16 f2bf(float f) {
  unsigned u = __float_as_uint(f);
  u += 0x7fffu + ((u >> 16) & 1u);
  return (u16)(u >> 16);
}
static __device__ __forceinline__ float bf2f(u16 h) { return __uint_as_float(((unsigned)h) << 16); }
static __device__ __forceinline__ float bfr(float f) { return bf2f(f2bf(f)); }

constexpr size_t SZ_X      = (size_t)L_SEQ * DM * 2;
constexpr size_t OFF_KF    = 0;
constexpr size_t OFF_UT    = OFF_KF + (size_t)DM * L_SEQ * 4;
constexpr size_t OFF_YT    = OFF_UT + SZ_X;
constexpr size_t OFF_XA    = OFF_YT + SZ_X;
constexpr size_t OFF_H     = OFF_XA + SZ_X;
constexpr size_t OFF_X2    = OFF_H + SZ_X;
constexpr size_t OFF_U2    = OFF_X2 + SZ_X;
constexpr size_t OFF_X3    = OFF_U2 + SZ_X;
constexpr size_t SZ_QK     = (size_t)L_SEQ * 2 * HD * 2;
constexpr size_t OFF_QK    = OFF_X3 + SZ_X;
constexpr size_t SZ_O      = (size_t)L_SEQ * HD * 2;
constexpr size_t OFF_VF    = OFF_QK + 2 * SZ_QK;
constexpr size_t SZ_PW     = (size_t)2 * NSEG * 16 * SEG * DM * 2;
constexpr size_t OFF_PW    = OFF_VF + 2 * SZ_O;
constexpr size_t SZ_MEM    = (size_t)NSEG * MM * HD * 2;
constexpr size_t OFF_MEMK  = OFF_PW + SZ_PW;
constexpr size_t OFF_MEMV  = OFF_MEMK + 2 * SZ_MEM;
constexpr size_t OFF_GT    = OFF_MEMV + 2 * SZ_MEM;
constexpr size_t SZ_ACC    = (size_t)2 * 32768;
constexpr size_t OFF_Z     = OFF_GT + (size_t)64 * DM * 4;
constexpr size_t OFF_ACC   = OFF_Z;
constexpr size_t OFF_FL    = OFF_ACC + 2 * SZ_ACC;
constexpr int FL_FW  = 0;
constexpr int FL_FR  = FL_FW + 2 * 64 * 16 * 32;
constexpr int FL_FC  = FL_FR + 64 * 16 * 32;
constexpr int FL_FQ  = FL_FC + 2 * 64 * 32 * 32;
constexpr int FL_FS  = FL_FQ + 2 * 64 * 32 * 32;
constexpr int FL_FZ  = FL_FS + 4 * 32 * 32;
constexpr int FL_KG  = FL_FZ + 8 * 32;
constexpr int FL_G   = FL_KG + 64 * 32;
constexpr int FL_TK  = FL_G + 4 * 4 * 32;
constexpr int FL_TMO = FL_TK + 2 * 256;
constexpr int FL_END = FL_TMO + 64;
constexpr size_t Z_BYTES   = OFF_FL + (size_t)FL_END * 4 - OFF_Z;
constexpr size_t OFF_TAG   = OFF_Z + Z_BYTES;
constexpr unsigned long long TAG_STEP = 0x9e3779b97f4a7c15ull;
constexpr size_t OFF_G0    = OFF_TAG + 8 * 1024 + 256;
constexpr size_t WS_TOTAL  = OFF_G0 + 8192;
static_assert(Z_BYTES % 256 == 0 && OFF_Z % 256 == 0 && Z_BYTES <= 2 * 128 * 8192, "zero region");
#define REP ((blockIdx.x >> 1) & 3)
static_assert(WS_TOTAL + (4 << 20) <= (size_t)256 * 1024 * 1024, "workspace too large");

constexpr int CKN = 2208;
static_assert((size_t)DM * 2 * CKN * 2 <= (size_t)DM * L_SEQ * 4 + SZ_X, "K buffer");
__device__ __forceinline__ float pos_of_index(int p) {
  if (p < 256) return (float)p;
  if (p < 384) return (float)(256 + 2 * (p - 256));
  if (p < 512) return (float)(512 + 4 * (p - 384));
  if (p < 640) return (float)(1024 + 8 * (p - 512));
  return 2048.0f;
}
__device__ __forceinline__ int index_of_l(int l) {
  float pf = bfr((float)l);
  int p = (int)pf;
  if (p < 256) return p;
  if (p < 512) return 256 + ((p - 256) >> 1);
  if (p < 1024) return 384 + ((p - 512) >> 2);
  if (p < 2048) return 512 + ((p - 1024) >> 3);
  return 640;
}

template <int CTRL> __device__ __forceinline__ float dppr(float x) {
  return __builtin_bit_cast(float, __builtin_amdgcn_update_dpp(0, __builtin_bit_cast(int, x), CTRL, 0xf, 0xf, false));
}
__device__ __forceinline__ float wave_sum(float v) {
  v += dppr<0x121>(v);
  v += dppr<0x122>(v);
  v += dppr<0x124>(v);
  v += dppr<0x128>(v);
  {
    auto r = __builtin_amdgcn_permlane16_swap(__float_as_uint(v), __float_as_uint(v), false, false);
    v = __uint_as_float(r[0]) + __uint_as_float(r[1]);
  }
  {
    auto r = __builtin_amdgcn_permlane32_swap(__float_as_uint(v), __float_as_uint(v), false, false);
    v = __uint_as_float(r[0]) + __uint_as_float(r[1]);
  }
  return v;
}
__device__ __forceinline__ void ln_row8(float (&v)[8]) {
  float s = 0.f;
#pragma unroll
  for (int j = 0; j < 8; ++j) s += v[j];
  s = wave_sum(s);
  const float mean = s * (1.0f / DM);
  float q = 0.f;
#pragma unroll
  for (int j = 0; j < 8; ++j) { float t = v[j] - mean; q += t * t; }
  q = wave_sum(q);
  const float var = q * (1.0f / DM);
  const float mb = bfr(mean);
  const float sd = bfr(sqrtf(bfr(bfr(var) + 1e-5f)));
#pragma unroll
  for (int j = 0; j < 8; ++j) v[j] = bfr(bfr(v[j] - mb) / sd);
}

#define RLX_AGENT __ATOMIC_RELAXED, __HIP_MEMORY_SCOPE_AGENT
typedef __attribute__((address_space(1))) unsigned gu32;
typedef unsigned v4u __attribute__((ext_vector_type(4)));
constexpr int AUX_SC1 = 16;
constexpr unsigned SPIN_CAP = 1u << 18;

__device__ __forceinline__ bool wait_flags(const unsigned* flags, int lane, int nfl, unsigned want, unsigned* tmo) {
  for (unsigned spins = 0;; ++spins) {
    unsigned v = want;
    if (lane < nfl) v = __hip_atomic_load((gu32*)flags + lane * 32, RLX_AGENT);
    if (__all(v >= want)) break;
    if (spins > SPIN_CAP) {
      if (lane == 0) __hip_atomic_store((gu32*)tmo, 1u, RLX_AGENT);
      return false;
    }
  }
  __builtin_amdgcn_fence(__ATOMIC_ACQUIRE, "wavefront");
  return true;
}

#define STAMP(i)
#define STAMP_DUMP()
#define WSTAMP(i)
template <int CTRL> __device__ __forceinline__ float dppf(float old, float x) {
  return __builtin_bit_cast(float, __builtin_amdgcn_update_dpp(__builtin_bit_cast(int, old), __builtin_bit_cast(int, x), CTRL, 0xf, 0xf, false));
}
__device__ __forceinline__ float wave_max_dpp(float x) {
  const float ninf = -3e38f;
  x = fmaxf(x, dppf<0x111>(ninf, x));
  x = fmaxf(x, dppf<0x112>(ninf, x));
  x = fmaxf(x, dppf<0x114>(ninf, x));
  x = fmaxf(x, dppf<0x118>(ninf, x));
  x = fmaxf(x, dppf<0x142>(ninf, x));
  x = fmaxf(x, dppf<0x143>(ninf, x));
  return __builtin_bit_cast(float, __builtin_amdgcn_readlane(__builtin_bit_cast(int, x), 63));
}
__device__ __forceinline__ float wave_sum_dpp(float x) {
  x += dppf<0x111>(0.f, x);
  x += dppf<0x112>(0.f, x);
  x += dppf<0x114>(0.f, x);
  x += dppf<0x118>(0.f, x);
  x += dppf<0x142>(0.f, x);
  x += dppf<0x143>(0.f, x);
  return __builtin_bit_cast(float, __builtin_amdgcn_readlane(__builtin_bit_cast(int, x), 63));
}
constexpr int SMS = 528;
constexpr int QS = 144;
constexpr int VS = 80;
constexpr int SOS = 144;
#define VAR_NREP 4
constexpr int NREP = VAR_NREP;
__device__ __forceinline__ float xsum16(float x) {
  auto r = __builtin_amdgcn_permlane16_swap(__float_as_uint(x), __float_as_uint(x), false, false);
  return __uint_as_float(r[0]) + __uint_as_float(r[1]);
}
__device__ __forceinline__ float xsum32(float x) {
  auto r = __builtin_amdgcn_permlane32_swap(__float_as_uint(x), __float_as_uint(x), false, false);
  return __uint_as_float(r[0]) + __uint_as_float(r[1]);
}
__device__ __forceinline__ float xmax16(float x) {
  auto r = __builtin_amdgcn_permlane16_swap(__float_as_uint(x), __float_as_uint(x), false, false);
  return fmaxf(__uint_as_float(r[0]), __uint_as_float(r[1]));
}
__device__ __forceinline__ float xmax32(float x) {
  auto r = __builtin_amdgcn_permlane32_swap(__float_as_uint(x), __float_as_uint(x), false, false);
  return fmaxf(__uint_as_float(r[0]), __uint_as_float(r[1]));
}
__device__ __forceinline__ float red_mod4(float x) {
  x += dppf<0x124>(0.f, x);
  x += dppf<0x128>(0.f, x);
  return xsum32(xsum16(x));
}
typedef __bf16 bf16x2_t __attribute__((ext_vector_type(2)));
typedef float f32x2_t __attribute__((ext_vector_type(2)));
__device__ __forceinline__ unsigned pk2(float a, float b) {
  f32x2_t v = {a, b};
  bf16x2_t r = __builtin_convertvector(v, bf16x2_t);
  return __builtin_bit_cast(unsigned, r);
}
__device__ __forceinline__ float dot2sq(const unsigned x, const float acc) {
  return __builtin_amdgcn_fdot2_f32_bf16(__builtin_bit_cast(bf16x2_t, x), __builtin_bit_cast(bf16x2_t, x), acc, false);
}
__device__ __forceinline__ float lo16(unsigned u) { return __uint_as_float(u << 16); }
__device__ __forceinline__ float hi16(unsigned u) { return __uint_as_float(u & 0xffff0000u); }
__device__ __forceinline__ void scale_pair(float a, float b, float scale, float& oa, float& ob) {
  unsigned u = pk2(a, b);
  unsigned v = pk2(lo16(u) * scale, hi16(u) * scale);
  oa = lo16(v);
  ob = hi16(v);
}

#define XSPLIT 3
__device__ __forceinline__ int role_no() {
  const int b = blockIdx.x, x = b & 7, j = b >> 3;
  if (XSPLIT >= 3) {
    const int y = XSPLIT == 5 ? x & 3 : x >> 1;
    if (XSPLIT == 5 ? x < 4 : !(x & 1)) {
      if (j >= 16) return 104 + 16 * y + j - 16;
      return XSPLIT == 4 ? 16 * y + j : (j & 8) * 4 + 8 * y + (j & 7);
    }
    if (j < 16) return 168 + (j & 8) * 4 + 8 * y + (j & 7);
    const int i = 4 * y + (j & 3);
    return j < 20 ? 64 + i : j < 24 ? (i < 8 ? 96 + i : 224 + i) : j < 28 ? 80 + i : 240 + i;
  }
  const int L = x >> 2, xx = x & 3;
  if (j < 8) return 32 * L + 8 * xx + j;
  if (j < 12) return 64 + 16 * L + 4 * xx + j - 8;
  if (j < 16) { const int i = 4 * xx + j - 12; return L ? 240 + i : i < 8 ? 96 + i : 224 + i; }
  if (j < 24) return 104 + 8 * x + j - 16;
  return 168 + 32 * L + 8 * xx + j - 24;
}
#define MSTAMP()
#define WSTAMP2()
#define SST(k)
#define TSR(rn, k, ss, t)
#define TS(k, ss, t)
#define FSD
#define FS(k)
#define FD(ss)
struct MegaArgs {
  const u16* mem0;
  const u16* Dskip;
  char* ws;
  u16* out;
  const u16* x0;
  const u16 *logAre, *Aim, *Cre, *Cim, *logdt;
  const u16 *Wq, *Wk, *Wv, *Wo;
  int rn;
};

__device__ __forceinline__ int lane_id() {
  int l;
  asm volatile("v_mbcnt_lo_u32_b32 %0, -1, 0\n\tv_mbcnt_hi_u32_b32 %0, -1, %0" : "=v"(l));
  return l;
}
__device__ __forceinline__ bool spin_timeout(unsigned spins, unsigned* tmo) {
  if (spins > SPIN_CAP) {
    if (lane_id() == 0) __hip_atomic_store((gu32*)tmo, 1u, RLX_AGENT);
    return true;
  }
  return false;
}
#define PSWH (h & 2)
#define RR 0
#define RQM 63
#define RPM 63
#define CD0 144
#define APN 8
#define APL 300
#define APA 130
#define APB 90
#define APP 232
#define CD 15
#define GP 230
#define GR 50
#define GM 0
#define ROT (4 * (h & 1))
#define CPRIO 1
#define P1PRIO 3
#define QLEAD 2
#define POLL_SLEEP 12
#define SLP_C POLL_SLEEP
#define SLP_A POLL_SLEEP
#define SLP_W POLL_SLEEP
#define SLP_R 4
#define SLP_V 4
#define SLP_Q 4
#define SLP_M 4
#define AWPRIO 2
#define SLP_S 8
#define SPOS 4
#define CVPRE 0
#define QHV 0
#define QCHK 0
#define LEADN 99
#define GATE_V 160
#define GATE_R 380
#define GATE_N 440
#define AWAHEAD 1
#define GATE_M 150
#define GATE_M2 380
#define QPACE 0
#define QPS 6
#define QPL 100
#define GATE_Q 170
#define SLP_Q1 24
#define QPF 3
#define QUP 7
#define QEP 3
#define AWD 11
#define GPER 226
#define GMA 60
#define GMR 60
#define GSTEP 30
#define GATE_RT 200
#define GATE_1 160
#define N1S 20
#define N1E 36
#define PRI_A 3
#define PRI_WA 2
#define PRI_WB 1
#define AWSKIP 3
#define PRI_0 3
#define AWDEF 1
#define SLP_F 2
#define EXP_OFF 0
#define AWSPEC 1
#define CVTHR 48
#define NR_C 8
#define NR_R 8
#define NR_W 8
#define NR_N 2
#define QAHEAD 0
#define NS0 4
#define SLP_N POLL_SLEEP
template <int SLP, int WPL>
__device__ __forceinline__ bool wait_n(const unsigned* f, int n, unsigned* tmo) {
  const int l = lane_id();
  for (unsigned spins = 0;; ++spins) {
    unsigned v = 1u;
    if (l < n) v = __hip_atomic_load((gu32*)f + (l / WPL) * 32 + l % WPL, RLX_AGENT);
    if (__all(v != 0u)) break;
    if (spin_timeout(spins, tmo)) return false;
    __builtin_amdgcn_s_sleep(SLP);
  }
  asm volatile("" ::: "memory");
  return true;
}
__device__ __forceinline__ unsigned now10() { return (unsigned)__builtin_amdgcn_s_memrealtime(); }
__device__ __forceinline__ void gate(const unsigned t) {
  for (int i = 0; i < 64; ++i) {
    const int d = (int)(t - now10());
    if (d <= 0) break;
    if (d > 100) __builtin_amdgcn_s_sleep(32);
    else if (d > 25) __builtin_amdgcn_s_sleep(8);
    else __builtin_amdgcn_s_sleep(2);
  }
}
template <int SLP, int WPL>
__device__ __forceinline__ bool grp_wait(const bool leader, volatile unsigned* sw, const unsigned val, const unsigned* f, int n,
                                         unsigned* tmo, const unsigned t) {
  unsigned spins = 0;
  gate(t);
  if (leader) {
    const int l = lane_id();
    for (;; ++spins) {
      unsigned v = 1u;
      if (l < n) v = __hip_atomic_load((gu32*)f + (l / WPL) * 32 + l % WPL, RLX_AGENT);
      if (__all(v != 0u) || spin_timeout(spins, tmo)) break;
      __builtin_amdgcn_s_sleep(SLP);
    }
    if ((threadIdx.x & 63) == 0) *sw = 2u * val + (spins ? 1u : 0u);
  } else {
    unsigned v;
    for (; (v = __builtin_amdgcn_readfirstlane(*sw)) < 2u * val; ++spins) {
      if (spins > 64u * SPIN_CAP) break;
      __builtin_amdgcn_s_sleep(SLP_F);
    }
    spins = v & 1u;
  }
  asm volatile("" ::: "memory");
  return spins != 0u;
}
template <int SLP, int WPL>
__device__ __forceinline__ bool wg_wait(const unsigned* f, int n, unsigned* tmo) {
  if (threadIdx.x < 64) (void)wait_n<SLP, WPL>(f, n, tmo);
  __syncthreads();
  asm volatile("" ::: "memory");
  return true;
}
__device__ __forceinline__ void set_flag(unsigned* f) { __hip_atomic_store((gu32*)f, 1u, RLX_AGENT); }
#define DRAIN() asm volatile("s_waitcnt vmcnt(0)" ::: "memory")
#define MKRS(ptr, bytes) __builtin_amdgcn_make_buffer_rsrc((void*)(ptr), 0, (int)(bytes), 0x00020000)
typedef unsigned v2u __attribute__((ext_vector_type(2)));
#define LD128(rs, voff, soff) __builtin_amdgcn_raw_buffer_load_b128(rs, voff, soff, AUX_SC1)
#define LD64(rs, voff, soff) __builtin_amdgcn_raw_buffer_load_b64(rs, voff, soff, AUX_SC1)
#define ST128(v, rs, voff, soff)                                              \
  do {                                                                        \
    const v4u st_d_ = (v);                                                    \
    __builtin_amdgcn_raw_buffer_store_b128(st_d_, rs, voff, soff, AUX_SC1);   \
    asm volatile("s_nop 1" ::"v"(st_d_) : "memory");                          \
  } while (0)
#define ST64(v, rs, voff, soff) __builtin_amdgcn_raw_buffer_store_b64(v, rs, voff, soff, AUX_SC1)
#define BF8(x) __builtin_bit_cast(bf16x8, x)
#define MFMA16(a, b, c) __builtin_amdgcn_mfma_f32_16x16x32_bf16(a, b, c, 0, 0, 0)
typedef short bf16x4 __attribute__((ext_vector_type(4)));
#define MFMA16K(a, b, c) \
  __builtin_amdgcn_mfma_f32_16x16x16bf16_1k(__builtin_bit_cast(bf16x4, a), __builtin_bit_cast(bf16x4, b), c, 0, 0, 0)
__device__ __forceinline__ float fmax_nn(float a, float b) { return __builtin_elementwise_maximum(a, b); }
typedef __bf16 bfv8 __attribute__((ext_vector_type(8)));
typedef __bf16 bfv16 __attribute__((ext_vector_type(16)));
typedef unsigned v8u __attribute__((ext_vector_type(8)));
#define SMF(a, b, c, idx) \
  __builtin_amdgcn_smfmac_f32_16x16x64_bf16(__builtin_bit_cast(bfv8, a), __builtin_bit_cast(bfv16, b), c, idx, 0, 0)
__device__ __forceinline__ float swap_add32(float a, float b) {
  auto r = __builtin_amdgcn_permlane32_swap(__float_as_uint(a), __float_as_uint(b), false, false);
  return __uint_as_float(r[0]) + __uint_as_float(r[1]);
}
__device__ __forceinline__ float swap_add16(float a, float b) {
  auto r = __builtin_amdgcn_permlane16_swap(__float_as_uint(a), __float_as_uint(b), false, false);
  return __uint_as_float(r[0]) + __uint_as_float(r[1]);
}
__device__ __forceinline__ v4u smf_fix(const v4u d) {
  auto x = __builtin_amdgcn_permlane16_swap(d.x, d.z, false, false);
  auto y = __builtin_amdgcn_permlane16_swap(d.y, d.w, false, false);
  return v4u{x[0], x[1], y[0], y[1]};
}
__device__ __forceinline__ v8u smf_bfrag(const u16* p) {
  const v2u a = *reinterpret_cast<const v2u*>(p), b = *reinterpret_cast<const v2u*>(p + 8);
  const v2u c = *reinterpret_cast<const v2u*>(p + 32), d = *reinterpret_cast<const v2u*>(p + 40);
  return v8u{a.x, b.x, a.y, b.y, c.x, d.x, c.y, d.y};
}

#define VAR_PPA 0
#define VAR_PPB 0
#define VAR_THR 0
#define VAR_THR2 0
#define CV_LEAD 100
#define VAR_EARLY 1
#define VAR_DRAIN 1
#define VAR_D1 0
#define VAR_D2 0
#define VAR_DR2 1
#define VAR_S1 0
#define VAR_S2 0
typedef __attribute__((address_space(1))) unsigned long long gu64;
typedef __attribute__((address_space(1))) char gch;
__device__ __forceinline__ int cvt_rpi(float x) {
  int r;
  asm("v_cvt_rpi_i32_f32 %0, %1" : "=v"(r) : "v"(x));
  return r;
}
typedef short v4s __attribute__((ext_vector_type(4)));
typedef __attribute__((address_space(3))) v4s* lp4s;
__device__ __forceinline__ void glds16(const void* sbase, const unsigned voff, const unsigned lds) {
  unsigned keep;
  asm volatile("s_nop 4\n\ts_mov_b32 %0, m0\n\ts_mov_b32 m0, %3\n\ts_nop 0\n\tglobal_load_lds_dwordx4 %1, %2\n\ts_mov_b32 m0, %0"
               : "=&s"(keep)
               : "v"(voff), "s"(sbase), "s"(lds)
               : "memory");
}
__device__ __forceinline__ void glds16c(const void* sbase, const unsigned voff, const unsigned lds) {
  unsigned keep;
  asm volatile("s_nop 4\n\ts_mov_b32 %0, m0\n\ts_mov_b32 m0, %3\n\ts_nop 0\n\tglobal_load_lds_dwordx4 %1, %2 sc1\n\ts_mov_b32 m0, %0"
               : "=&s"(keep)
               : "v"(voff), "s"(sbase), "s"(lds)
               : "memory");
}
__device__ __forceinline__ v2u trd(const unsigned a) {
  return __builtin_bit_cast(v2u, __builtin_amdgcn_ds_read_tr16_b64_v4i16((lp4s)(unsigned long)a));
}
__device__ __forceinline__ unsigned lds_base(char* smem) { return (unsigned)(unsigned long)(__attribute__((address_space(3))) char*)smem; }
#define RAW_BAR()                                           \
  do {                                                      \
    asm volatile("s_waitcnt lgkmcnt(0)" ::: "memory");      \
    __builtin_amdgcn_s_barrier();                           \
    asm volatile("" ::: "memory");                          \
  } while (0)
#define WAIT_VM(n) asm volatile("s_waitcnt vmcnt(" #n ")" ::: "memory")
template <bool SP>
__device__ __forceinline__ void wqkv_issue(const u16* Wm, const int hf, const int h, const int w, const int l, const unsigned sb) {
  const char* base = (const char*)Wm + (size_t)(256 * hf + 32 * w) * (HD * 2) + h * (DH * 2);
#pragma unroll
  for (int i = 0; i < 8; ++i) {
    const int r = l >> 4;
    const int f = SP ? ((r & 1) | ((i & 1) << 1) | (((i >> 1) & 1) << 2)) : (r | (((i >> 1) & 1) << 2));
    glds16(base, (unsigned)((4 * i + r) * (HD * 2) + (((l & 15) ^ (f << 1)) << 4)), sb + w * 16384 + hf * 8192 + i * 1024);
  }
}

#define PFW 1
__device__ __forceinline__ void pull_weights(const MegaArgs& A, char* smem, const int slot, const int slots) {
  if (!PFW) return;
  const int x = blockIdx.x & 7, j = blockIdx.x >> 3, h = 8 * (x >> 1) + (j & 7), k = (j >> 3) - 1, l = lane_id();
  const bool odd = x & 1, wo = !odd && k == 1;
  const unsigned voff = wo ? l * 64 : (l >> 2) * (HD * 2) + (l & 3) * 64;
  const unsigned dst = lds_base(smem) + 162816;
  unsigned long long pk = (unsigned long long)A.Wk, pq = (unsigned long long)A.Wq, pv = (unsigned long long)A.Wv, po = (unsigned long long)A.Wo;
  asm volatile("" : "+s"(pk), "+s"(pq), "+s"(pv), "+s"(po));
  for (int i = odd ? slot * 3 + k : slot; (PFW & 1) && i < (odd ? 64 : 32); i += odd ? 3 * slots : slots) {
    const unsigned long long m = odd ? (i < 32 ? pk : pv) : (k == 0 ? pq : pk);
    const unsigned long long b = wo ? po + h * (DH * DM * 2) + i * 4096 : m + (i & 31) * (16 * HD * 2) + h * (DH * 2);
    unsigned keep;
    asm volatile("s_nop 4\n\ts_mov_b32 %0, m0\n\ts_mov_b32 m0, %3\n\ts_nop 0\n\tglobal_load_lds_dword %1, %2\n\ts_mov_b32 m0, %0"
                 : "=&s"(keep)
                 : "v"(voff), "s"(b), "s"(dst)
                 : "memory");
  }
  if (PFW & 2) {
    const unsigned long long b = po + (size_t)h * (DH * DM * 2);
    for (int i = slot * 3 + k; i < 32; i += 3 * slots) {
      unsigned keep;
      asm volatile("s_nop 4\n\ts_mov_b32 %0, m0\n\ts_mov_b32 m0, %3\n\ts_nop 0\n\tglobal_load_lds_dword %1, %2\n\ts_mov_b32 m0, %0"
                   : "=&s"(keep)
                   : "v"(l * 64), "s"(b + i * 4096), "s"(dst)
                   : "memory");
    }
  }
}
__device__ __forceinline__ void wo_issue(const u16* Wo, const int a, const int st, const unsigned sb) {
  const int w = __builtin_amdgcn_readfirstlane(threadIdx.x >> 6), l = lane_id(), c = l & 15, g = l >> 4;
  const char* base = (const char*)Wo + ((size_t)((2 * a + (st >> 1)) * DH + 64 * (w & 1) + 32 * (st & 1)) * DM + 128 * (w >> 1)) * 2;
#pragma unroll
  for (int i = 0; i < 8; ++i) {
    const int fd = g | ((i & 1) << 2);
    glds16(base, (unsigned)((4 * i + g) * (DM * 2) + ((c ^ (fd << 1)) << 4)), sb + (st & 1) * 65536 + w * 8192 + i * 1024);
  }
}
__device__ __forceinline__ bool zero_seen(const v4u t, const v2u e) {
  const unsigned long long m = (((unsigned long long)e.y << 32) | e.x) + TAG_STEP;
  const unsigned lo = (unsigned)m, hi = (unsigned)(m >> 32);
  return __all(((t.x ^ lo) | (t.y ^ hi) | (t.z ^ lo) | (t.w ^ hi)) == 0u);
}
__device__ __forceinline__ void zero_wait(char* ws) {
  const __amdgpu_buffer_rsrc_t rs = MKRS(ws + OFF_TAG, 8192 + 8);
  for (unsigned spins = 0; spins < 16u * SPIN_CAP; ++spins) {
    const v4u t = LD128(rs, lane_id() * 16, (blockIdx.x & 7) * 1024);
    const v2u e = LD64(rs, 0, 8192);
    if (zero_seen(t, e)) break;
    asm volatile("s_sleep 4" ::: "memory");
  }
  asm volatile("" ::: "memory");
}
__device__ __forceinline__ unsigned row_stats(const v4u raw) {
  float v[8];
#pragma unroll
  for (int e = 0; e < 4; ++e) { v[2 * e] = lo16(raw[e]); v[2 * e + 1] = hi16(raw[e]); }
  float s = 0.f;
#pragma unroll
  for (int e = 0; e < 8; ++e) s += v[e];
  const float mean = wave_sum(s) * (1.0f / DM);
  float q = 0.f;
#pragma unroll
  for (int e = 0; e < 8; ++e) { const float t = v[e] - mean; q += t * t; }
  const float var = wave_sum(q) * (1.0f / DM);
  const float sd = bfr(sqrtf(bfr(bfr(var) + 1e-5f)));
  return (unsigned)f2bf(mean) | ((unsigned)f2bf(sd) << 16);
}
__device__ __forceinline__ void start_kgen(const MegaArgs& A, char* smem, const int hi, const int xi, const int aw) {
  const int tid = threadIdx.x, j = tid >> 7, u = tid & 127, d = 4 * hi + j;
  const int w = __builtin_amdgcn_readfirstlane(tid >> 6), l = tid & 63;
  float* prm = reinterpret_cast<float*>(smem) + j * 10 * NST;
  float* part = reinterpret_cast<float*>(smem) + 40 * NST + j * 8 * 400;
  unsigned* kd = reinterpret_cast<unsigned*>(smem + 131072) + j * 392;
  v4u raw[8], ra, rb;
  const int r0 = 16 * xi + 2 * w;
  if (xi >= 0) {
    ra = *reinterpret_cast<const v4u*>(A.x0 + (size_t)r0 * DM + l * 8);
    rb = *reinterpret_cast<const v4u*>(A.x0 + (size_t)(r0 + 1) * DM + l * 8);
  }
  const u16 p0 = A.logdt[d], p1 = A.logAre[d * NST + u], p2 = A.Aim[d * NST + u], p3 = A.Cre[d * NST + u], p4 = A.Cim[d * NST + u];
  if (w >= 6) {
#pragma unroll
    for (int i = 0; i < 8; ++i) raw[i] = *reinterpret_cast<const v4u*>(A.x0 + (size_t)(16 * hi + 8 * (w - 6) + i) * DM + l * 8);
  }
  const v2u en = LD64(MKRS(A.ws + OFF_TAG, 8192 + 8), 0, 8192);
  {
    const __amdgpu_buffer_rsrc_t rz = MKRS(A.ws + OFF_Z, Z_BYTES);
    ST128((v4u{0u, 0u, 0u, 0u}), rz, tid * 16, hi * 8192);
    ST128((v4u{0u, 0u, 0u, 0u}), rz, tid * 16, (128 + hi) * 8192);
  }
  if (xi >= 0) {
    const unsigned sa = row_stats(ra), sb = row_stats(rb), st = (l & 1) ? sb : sa;
    const unsigned long long m = (((unsigned long long)en.y << 32) | en.x) + TAG_STEP;
    ST128((v4u{st, (unsigned)m, st, (unsigned)(m >> 32)}), MKRS(A.ws + OFF_G0, 8192), (l >> 1) * 2048 + (l & 1) * 16, r0 * 16);
    SST(11);
  }
  {
    const float dt = bfr(expf(bf2f(p0)));
    const float ar = -bfr(expf(bf2f(p1))) * dt, ai = bf2f(p2) * dt;
    prm[u] = ar;
    prm[NST + u] = ai;
    prm[2 * NST + u] = bf2f(p3) * dt;
    prm[3 * NST + u] = bf2f(p4) * dt;
#pragma unroll
    for (int cls = 1; cls < 4; ++cls) {
      const float st = (float)(1 << cls);
      const float e = expf(ar * st);
      float sn, cs;
      sincosf(ai * st, &sn, &cs);
      prm[(2 + 2 * cls) * NST + u] = e * cs;
      prm[(3 + 2 * cls) * NST + u] = e * sn;
    }
  }
  __builtin_amdgcn_s_waitcnt(0x0F70);
  __syncthreads();
  SST(1);
  if (w >= 6) {
    if (w == 7 && l < 8)
      __hip_atomic_store((gu64*)(A.ws + OFF_TAG) + l * 128 + hi, (((unsigned long long)en.y << 32) | en.x) + TAG_STEP, RLX_AGENT);
    __builtin_amdgcn_s_setprio(3);
    if (hi >= 32) pull_weights(A, smem, w - 6, 2);
    v4u wr[32];
    const u16* wop = A.Wo + (size_t)(hi * DH + 64 * (w - 6)) * DM + l * 8;
    if (hi < 32) {
#pragma unroll
      for (int i = 0; i < 32; ++i) wr[i] = *reinterpret_cast<const v4u*>(wop + i * DM);
    }
    unsigned stw = 0u;
#pragma unroll
    for (int i = 0; i < 8; ++i) {
      const unsigned t = row_stats(raw[i]);
      if (l == i) stw = t;
    }
    const int sx = 2 * hi + w - 6;
    if (l < 8) __hip_atomic_store((gu32*)(A.ws + OFF_GT) + 8 * sx + l, stw, RLX_AGENT);
    zero_wait(A.ws);
    DRAIN();
    if (l < 4) set_flag((unsigned*)(A.ws + OFF_FL) + FL_FS + (l * 32 + (sx >> 3)) * 32 + (sx & 7));
    if (hi < 32) {
      float c0 = 0.f, c1 = 0.f, c2 = 0.f, c3 = 0.f;
#pragma unroll
      for (int i = 0; i < 64; ++i) {
        const v4u x = wr[i & 31];
        if (i < 32) wr[i] = *reinterpret_cast<const v4u*>(wop + (32 + i) * DM);
        c0 = dot2sq(x.x, c0); c1 = dot2sq(x.y, c1); c2 = dot2sq(x.z, c2); c3 = dot2sq(x.w, c3);
      }
      const float m = wave_max_dpp(fmaxf(fmaxf(c0, c1), fmaxf(c2, c3)));
      if (l < 4) __hip_atomic_store((gu32*)(A.ws + OFF_FL) + FL_G + (l * 4 + w - 5) * 32 + hi, __float_as_uint(m) | 1u, RLX_AGENT);
    }
    __builtin_amdgcn_s_setprio(0);
  } else {
    const int jm = tid / 96, um = tid % 96;
    const int r = um % 12, ng = um / 12;
    const float* prm = reinterpret_cast<float*>(smem) + jm * 10 * NST;
    float* part = reinterpret_cast<float*>(smem) + 40 * NST + jm * 8 * 400;
    const float* pw = prm + (4 + 2 * (r >> 2)) * NST;
    const float p0 = pos_of_index(256 + 32 * r);
    f32x2_t acc[33];
#pragma unroll
    for (int i = 0; i < 33; ++i) acc[i] = f32x2_t{0.f, 0.f};
#pragma unroll 1
    for (int bt = 0; bt < 4; ++bt) {
      const int n = 16 * ng + 4 * bt;
      const f32x4 ar = *reinterpret_cast<const f32x4*>(prm + n), ai = *reinterpret_cast<const f32x4*>(prm + NST + n);
      const f32x4 cr = *reinterpret_cast<const f32x4*>(prm + 2 * NST + n), ci = *reinterpret_cast<const f32x4*>(prm + 3 * NST + n);
      const f32x4 wr = *reinterpret_cast<const f32x4*>(pw + n), wi = *reinterpret_cast<const f32x4*>(pw + NST + n);
      float yr0[4], yi0[4];
#pragma unroll
      for (int k = 0; k < 4; ++k) {
        const float e = __expf(ar[k] * p0);
        float sn, cs;
        __sincosf(ai[k] * p0, &sn, &cs);
        const float zr = e * cs, zi = e * sn;
        yr0[k] = cr[k] * zr - ci[k] * zi;
        yi0[k] = cr[k] * zi + ci[k] * zr;
      }
      f32x2_t yra = {yr0[0], yr0[1]}, yia = {yi0[0], yi0[1]}, wra = {wr[0], wr[1]}, wia = {wi[0], wi[1]};
      f32x2_t yrb = {yr0[2], yr0[3]}, yib = {yi0[2], yi0[3]}, wrb = {wr[2], wr[3]}, wib = {wi[2], wi[3]};
#pragma unroll
      for (int i = 0; i < 33; ++i) {
        acc[i] += yra + yrb;
        const f32x2_t ta = yra * wra - yia * wia, tb = yrb * wrb - yib * wib;
        yia = yra * wia + yia * wra;
        yib = yrb * wib + yib * wrb;
        yra = ta;
        yrb = tb;
      }
    }
#pragma unroll
    for (int i = 0; i < 33; ++i) part[ng * 400 + 33 * r + i] = acc[i].x + acc[i].y;
  }
  __syncthreads();
  SST(2);
  for (int i = u; i < 385; i += 128) {
    const int o = i < 384 ? 33 * (i >> 5) + (i & 31) : 33 * 11 + 32;
    float v = 0.f;
#pragma unroll
    for (int g = 0; g < 8; ++g) v += part[g * 400 + o];
    v *= 2.0f;
    kd[i] = pk2(v, v - lo16(pk2(v, v)));
  }
  if (u == 1) kd[385] = 0u;
  __syncthreads();
  if (aw >= 0) {
    wo_issue(A.Wo, aw, 0, lds_base(smem));
    wo_issue(A.Wo, aw, 1, lds_base(smem));
  }
  const __amdgpu_buffer_rsrc_t rs = MKRS(A.ws + OFF_KF, DM * 2 * CKN * 2);
  for (int un = u; un < 229; un += 128) {
    v4u oh, ol;
#pragma unroll
    for (int e = 0; e < 4; ++e) {
      const int lag = 2080 - (8 * un + 2 * e);
      const unsigned pb = pk2((float)lag, (float)(lag - 1));
      const unsigned k0 = kd[(unsigned)(lag - 256) < L_SEQ - 256u ? (pb & 0xffffu) - 0x4380u : 385u];
      const unsigned k1 = kd[(unsigned)(lag - 257) < L_SEQ - 256u ? (pb >> 16) - 0x4380u : 385u];
      oh[e] = __builtin_amdgcn_perm(k1, k0, 0x05040100u);
      ol[e] = __builtin_amdgcn_perm(k1, k0, 0x07060302u);
    }
    ST128(oh, rs, (d * 2 * CKN + 8 * un) * 2, 0);
    ST128(ol, rs, (d * 2 * CKN + CKN + 8 * un) * 2, 0);
  }
  DRAIN();
  __syncthreads();
  if (tid == 0) set_flag((unsigned*)(A.ws + OFF_FL) + FL_KG + (hi >> 1) * 32 + (hi & 1));
  SST(3);
}

constexpr int C_SM = 131072, C_SO = C_SM + 8192, C_KT = C_SO + 2048, C_X = C_KT + 16384, C_SQ = C_X + 1024;
constexpr int C_SK = C_SQ + (7 * QS + 128) * 2;
static_assert(C_SK + 8 * QS * 2 <= 163840, "lds");
#define PSW 1
#define CHAIN_ASM_ATT \
  "s_mov_b32 s66, %[d0]\n" \
  "s_mov_b32 s92, 1152\n" \
  "s_mov_b32 s93, 576\n" \
  "s_mov_b32 s94, 2272\n" \
  "s_mov_b32 s95, 272\n" \
  "s_mov_b32 s96, 288\n" \
  "s_mov_b32 s91, 0x40000001\n" \
  "s_mov_b32 s84, 0x4b400000\n" \
  "s_mov_b32 s85, 0x4b400000\n" \
  "s_mov_b32 s78, 0x3e0293ee\n" \
  "s_mov_b32 s79, 0x3fb8aa3b\n" \
  "s_mov_b32 s76, 0xff00ff00\n" \
  "s_mov_b32 s77, 0xff00ff00\n" \
  "s_add_u32 s58, s56, s60\n" \
  "s_addc_u32 s59, s57, 0\n" \
  "s_xor_b32 s38, s35, 0\n" \
  "s_add_u32 s26, s58, s38\n" \
  "s_addc_u32 s27, s59, 0\n" \
  "s_xor_b32 s38, s35, 1024\n" \
  "s_add_u32 s28, s58, s38\n" \
  "s_addc_u32 s29, s59, 0\n" \
  "s_xor_b32 s38, s35, 2048\n" \
  "s_add_u32 s30, s58, s38\n" \
  "s_addc_u32 s31, s59, 0\n" \
  "s_xor_b32 s38, s35, 3072\n" \
  "s_add_u32 s32, s58, s38\n" \
  "s_addc_u32 s33, s59, 0\n" \
  "s_lshr_b32 s98, s60, 2\n" \
  "s_add_u32 s98, s98, s74\n" \
  "s_add_u32 s98, s98, 131072\n" \
  "s_lshl_b32 s97, s73, 5\n" \
  "s_add_u32 s97, s97, s74\n" \
  "s_add_u32 s97, s97, 158720\n" \
  "s_lshl_b32 s99, s73, 10\n" \
  "v_mbcnt_lo_u32_b32 v48, -1, 0\n" \
  "v_mbcnt_hi_u32_b32 v48, -1, v48\n" \
  "v_and_b32 v49, 15, v48\n" \
  "v_lshl_add_u32 v8, v49, 1, s97\n" \
  "v_bfe_u32 v49, v48, 4, 1\n" \
  "v_mad_u32_u24 v8, v49, s92, v8\n" \
  "v_lshrrev_b32 v49, 5, v48\n" \
  "v_mad_u32_u24 v9, v49, s93, v8\n" \
  "v_mad_u32_u24 v8, v49, s94, v8\n" \
  "v_add_u32 v10, s99, v1\n" \
  "s_lshr_b32 s88, s99, 2\n" \
  "s_add_u32 s88, s88, s74\n" \
  "s_add_u32 s88, s88, 139264\n" \
  "v_lshl_add_u32 v7, v49, 10, s88\n" \
  "v_and_b32 v49, 31, v48\n" \
  "v_lshl_add_u32 v7, v49, 3, v7\n" \
  "v_mov_b64 v[12:13], 0\n" \
  "v_mov_b64 v[14:15], 0\n" \
  "v_mov_b64 v[16:17], 0\n" \
  "v_mov_b64 v[18:19], 0\n" \
  "v_mov_b64 v[20:21], 0\n" \
  "v_mov_b64 v[22:23], 0\n" \
  "s_add_u32 s88, s74, 157760\n" \
  "v_mov_b32 v59, s88\n" \
  "v_mov_b32 v24, 0x44870000\n" \
  "v_mul_f32 v24, s72, v24\n" \
  "s_nop 0\n" \
  "v_readfirstlane_b32 s86, v24\n" \
  "s_lshr_b32 s86, s86, 23\n" \
  "s_addk_i32 s86, 0xff83\n" \
  "s_ashr_i32 s86, s86, 1\n" \
  "s_max_i32 s86, s86, -20\n" \
  "s_min_i32 s86, s86, 100\n" \
  "s_mov_b32 s82, s86\n" \
  "s_lshr_b32 s83, s72, 23\n" \
  "s_sub_i32 s83, s83, 242\n" \
  "s_lshl_b32 s88, s73, 10\n" \
  "v_add_u32 v24, s88, v1\n" \
  "ds_read_b128 v[32:35], v24 offset:0\n" \
  "s_waitcnt lgkmcnt(0)\n" \
  "v_mov_b32 v11, 0\n" \
  "v_dot2c_f32_bf16 v11, v32, v32\n" \
  "v_dot2c_f32_bf16 v11, v33, v33\n" \
  "v_dot2c_f32_bf16 v11, v34, v34\n" \
  "v_dot2c_f32_bf16 v11, v35, v35\n" \
  "s_nop 1\n" \
  "s_nop 0\n" \
  "v_cmpx_lt_f32 vcc, 2.0, v11\n" \
  "ds_max_u32 v59, v11\n" \
  "s_mov_b64 exec, -1\n" \
  ".LCA_TOP_%=:\n" \
  "s_waitcnt lgkmcnt(0)\n" \
  "s_barrier\n" \
  "ds_read_b128 v[40:43], v1 offset:0\n" \
  "ds_read_b128 v[44:47], v1 offset:1024\n" \
  "ds_read_b128 v[48:51], v1 offset:2048\n" \
  "ds_read_b128 v[52:55], v1 offset:3072\n" \
  "ds_read_b128 v[56:59], v1 offset:4096\n" \
  "ds_read_b128 v[60:63], v1 offset:5120\n" \
  "s_cmp_lg_u32 s73, 0\n" \
  "s_cbranch_scc1 .LCA_NOZ_%=\n" \
  "s_xor_b32 s88, s60, 0x8000\n" \
  "s_add_u32 s88, s88, s61\n" \
  "buffer_store_dwordx2 v[12:13], v5, s[40:43], s88 offen sc1\n" \
  ".LCA_NOZ_%=:\n" \
  "s_waitcnt lgkmcnt(5)\n" \
  "v_smfmac_f32_16x16x64_bf16 v[20:23], v[40:43], v[192:199], v0\n" \
  "v_smfmac_f32_16x16x64_bf16 v[12:15], v[40:43], v[64:71], v0\n" \
  "v_smfmac_f32_16x16x64_bf16 v[16:19], v[40:43], v[128:135], v0\n" \
  "ds_read_b128 v[40:43], v1 offset:6144\n" \
  "s_waitcnt lgkmcnt(5)\n" \
  "v_smfmac_f32_16x16x64_bf16 v[20:23], v[44:47], v[200:207], v0\n" \
  "v_smfmac_f32_16x16x64_bf16 v[12:15], v[44:47], v[72:79], v0\n" \
  "v_smfmac_f32_16x16x64_bf16 v[16:19], v[44:47], v[136:143], v0\n" \
  "ds_read_b128 v[44:47], v1 offset:7168\n" \
  "s_waitcnt lgkmcnt(5)\n" \
  "v_smfmac_f32_16x16x64_bf16 v[20:23], v[48:51], v[208:215], v0\n" \
  "v_smfmac_f32_16x16x64_bf16 v[12:15], v[48:51], v[80:87], v0\n" \
  "v_smfmac_f32_16x16x64_bf16 v[16:19], v[48:51], v[144:151], v0\n" \
  "ds_read_b128 v[24:27], v1 offset:10240\n" \
  "ds_read_b128 v[28:31], v1 offset:14336\n" \
  "s_waitcnt lgkmcnt(6)\n" \
  "v_smfmac_f32_16x16x64_bf16 v[20:23], v[52:55], v[216:223], v0\n" \
  "v_smfmac_f32_16x16x64_bf16 v[12:15], v[52:55], v[88:95], v0\n" \
  "v_smfmac_f32_16x16x64_bf16 v[16:19], v[52:55], v[152:159], v0\n" \
  "ds_read_b128 v[32:35], v1 offset:11264\n" \
  "ds_read_b128 v[36:39], v1 offset:15360\n" \
  "s_waitcnt lgkmcnt(7)\n" \
  "v_smfmac_f32_16x16x64_bf16 v[20:23], v[56:59], v[224:231], v0\n" \
  "v_smfmac_f32_16x16x64_bf16 v[12:15], v[56:59], v[96:103], v0\n" \
  "v_smfmac_f32_16x16x64_bf16 v[16:19], v[56:59], v[160:167], v0\n" \
  "s_waitcnt lgkmcnt(6)\n" \
  "v_smfmac_f32_16x16x64_bf16 v[20:23], v[60:63], v[232:239], v0\n" \
  "v_smfmac_f32_16x16x64_bf16 v[12:15], v[60:63], v[104:111], v0\n" \
  "v_smfmac_f32_16x16x64_bf16 v[16:19], v[60:63], v[168:175], v0\n" \
  "s_waitcnt lgkmcnt(5)\n" \
  "v_smfmac_f32_16x16x64_bf16 v[20:23], v[40:43], v[240:247], v0\n" \
  "v_smfmac_f32_16x16x64_bf16 v[12:15], v[40:43], v[112:119], v0\n" \
  "v_smfmac_f32_16x16x64_bf16 v[16:19], v[40:43], v[176:183], v0\n" \
  "s_waitcnt lgkmcnt(4)\n" \
  "v_smfmac_f32_16x16x64_bf16 v[20:23], v[44:47], v[248:255], v0\n" \
  "v_smfmac_f32_16x16x64_bf16 v[12:15], v[44:47], v[120:127], v0\n" \
  "v_smfmac_f32_16x16x64_bf16 v[16:19], v[44:47], v[184:191], v0\n" \
  "s_cmp_eq_u32 s71, 0\n" \
  "s_nop 4\n" \
  "v_permlane32_swap_b32 v20, v22\n" \
  "v_permlane32_swap_b32 v21, v23\n" \
  "v_pk_add_f32 v[20:21], v[20:21], v[22:23]\n" \
  "v_permlane32_swap_b32 v12, v16\n" \
  "v_permlane32_swap_b32 v13, v17\n" \
  "v_cvt_pk_bf16_f32 v20, v20, v21\n" \
  "v_pk_add_f32 v[12:13], v[12:13], v[16:17]\n" \
  "v_permlane32_swap_b32 v14, v18\n" \
  "v_permlane32_swap_b32 v15, v19\n" \
  "ds_write_b16 v9, v20 offset:128\n" \
  "v_cvt_pk_bf16_f32 v12, v12, v13\n" \
  "v_pk_add_f32 v[14:15], v[14:15], v[18:19]\n" \
  "ds_write_b16_d16_hi v9, v20 offset:416\n" \
  "ds_write_b16 v8, v12\n" \
  "v_cvt_pk_bf16_f32 v14, v14, v15\n" \
  "ds_write_b16_d16_hi v8, v12 offset:288\n" \
  "ds_write_b16 v8, v14 offset:576\n" \
  "ds_write_b16_d16_hi v8, v14 offset:864\n" \
  "s_setprio 3\n" \
  "s_waitcnt lgkmcnt(0)\n" \
  "s_barrier\n" \
  "s_cbranch_scc1 .LCA_LAST_%=\n" \
  "ds_read_b32 v11, v1 offset:26688\n" \
  "ds_read_b128 v[40:43], v2 offset:0\n" \
  "ds_read_b128 v[44:47], v3 offset:0\n" \
  "ds_read_b128 v[48:51], v2 offset:64\n" \
  "ds_read_b128 v[52:55], v3 offset:64\n" \
  "ds_read_b128 v[56:59], v2 offset:128\n" \
  "ds_read_b128 v[60:63], v3 offset:128\n" \
  "s_waitcnt lgkmcnt(5)\n" \
  "v_mfma_f32_16x16x32_bf16 v[12:15], v[24:27], v[40:43], 0\n" \
  "v_mfma_f32_16x16x32_bf16 v[16:19], v[28:31], v[40:43], 0\n" \
  "v_readfirstlane_b32 s86, v11\n" \
  "ds_read_b128 v[24:27], v1 offset:12288\n" \
  "ds_read_b128 v[28:31], v1 offset:16384\n" \
  "s_waitcnt lgkmcnt(6)\n" \
  "v_mfma_f32_16x16x32_bf16 v[20:23], v[44:47], v[40:43], 0\n" \
  "s_lshr_b32 s86, s86, 23\n" \
  "s_add_i32 s86, s86, s83\n" \
  "ds_read_b128 v[40:43], v2 offset:192\n" \
  "ds_read_b128 v[44:47], v3 offset:192\n" \
  "s_waitcnt lgkmcnt(7)\n" \
  "v_mfma_f32_16x16x32_bf16 v[12:15], v[32:35], v[48:51], v[12:15]\n" \
  "v_mfma_f32_16x16x32_bf16 v[16:19], v[36:39], v[48:51], v[16:19]\n" \
  "s_ashr_i32 s86, s86, 1\n" \
  "s_max_i32 s86, s86, s82\n" \
  "ds_read_b128 v[32:35], v1 offset:13312\n" \
  "ds_read_b128 v[36:39], v1 offset:17408\n" \
  "s_waitcnt lgkmcnt(8)\n" \
  "v_mfma_f32_16x16x32_bf16 v[20:23], v[52:55], v[48:51], v[20:23]\n" \
  "s_min_i32 s86, s86, 100\n" \
  "s_lshl_b32 s86, s86, 23\n" \
  "s_sub_i32 s84, 0x4a800000, s86\n" \
  "ds_read_b128 v[48:51], v10 offset:18432\n" \
  "ds_read_b128 v[52:55], v10 offset:22528\n" \
  "s_waitcnt lgkmcnt(8)\n" \
  "v_mfma_f32_16x16x32_bf16 v[20:23], v[60:63], v[56:59], v[20:23]\n" \
  "s_waitcnt lgkmcnt(6)\n" \
  "v_mfma_f32_16x16x32_bf16 v[12:15], v[24:27], v[56:59], v[12:15]\n" \
  "v_mfma_f32_16x16x32_bf16 v[16:19], v[28:31], v[56:59], v[16:19]\n" \
  "ds_read_b128 v[56:59], v6 offset:0\n" \
  "ds_read_b128 v[60:63], v6 offset:1024\n" \
  "s_waitcnt lgkmcnt(6)\n" \
  "v_mfma_f32_16x16x32_bf16 v[20:23], v[44:47], v[40:43], v[20:23]\n" \
  "ds_read_b128 v[24:27], v6 offset:2048\n" \
  "ds_read_b128 v[28:31], v6 offset:3072\n" \
  "s_waitcnt lgkmcnt(6)\n" \
  "v_mfma_f32_16x16x32_bf16 v[12:15], v[32:35], v[40:43], v[12:15]\n" \
  "v_mfma_f32_16x16x32_bf16 v[16:19], v[36:39], v[40:43], v[16:19]\n" \
  "ds_read_b128 v[32:35], v6 offset:8192\n" \
  "ds_read_b128 v[36:39], v6 offset:9216\n" \
  "s_waitcnt lgkmcnt(6)\n" \
  "v_cndmask_b32 v20, v20, v21, s[76:77]\n" \
  "v_mov_b32 v41, 0\n" \
  "v_mov_b32 v42, 0\n" \
  "s_nop 1\n" \
  "v_cndmask_b32 v12, v12, v16, s[76:77]\n" \
  "v_cndmask_b32 v13, v13, v17, s[76:77]\n" \
  "v_cndmask_b32 v14, v14, v18, s[76:77]\n" \
  "v_cndmask_b32 v15, v15, v19, s[76:77]\n" \
  "v_maximum3_f32 v16, v20, v12, v13\n" \
  "v_maximum3_f32 v16, v16, v14, v15\n" \
  "s_nop 1\n" \
  "v_max_f32_dpp v16, v16, v16 row_ror:8 row_mask:0xf bank_mask:0xf\n" \
  "v_mov_b32 v17, v16\n" \
  "s_nop 1\n" \
  "v_permlane16_swap_b32 v16, v17\n" \
  "v_max_f32 v16, v16, v17\n" \
  "v_mov_b32 v17, v16\n" \
  "s_nop 1\n" \
  "v_permlane32_swap_b32 v16, v17\n" \
  "v_max_f32 v16, v16, v17\n" \
  "v_mul_f32 v16, s78, v16\n" \
  "v_pk_fma_f32 v[12:13], v[12:13], s[78:79], v[16:17] op_sel_hi:[1,0,0] neg_lo:[0,0,1] neg_hi:[0,0,1]\n" \
  "v_pk_fma_f32 v[14:15], v[14:15], s[78:79], v[16:17] op_sel_hi:[1,0,0] neg_lo:[0,0,1] neg_hi:[0,0,1]\n" \
  "v_fma_f32 v20, v20, s78, -v16\n" \
  "v_exp_f32 v12, v12\n" \
  "v_exp_f32 v13, v13\n" \
  "v_exp_f32 v14, v14\n" \
  "v_exp_f32 v15, v15\n" \
  "v_exp_f32 v20, v20\n" \
  "s_barrier\n" \
  "ds_read_b64 v[22:23], v7 offset:0\n" \
  "buffer_load_dword v7, v5, s[16:19], s24 offen sc1\n" \
  "v_cvt_pk_bf16_f32 v44, v12, v13\n" \
  "v_cvt_pk_bf16_f32 v45, v14, v15\n" \
  "v_pk_add_f32 v[8:9], v[12:13], v[14:15]\n" \
  "v_mov_b32_dpp v46, v44 row_ror:8 row_mask:0xf bank_mask:0xf\n" \
  "v_mov_b32_dpp v17, v20 row_ror:8 row_mask:0xf bank_mask:0xf\n" \
  "v_mov_b32_dpp v47, v45 row_ror:8 row_mask:0xf bank_mask:0xf\n" \
  "v_add_f32 v10, v8, v20\n" \
  "v_cvt_pk_bf16_f32 v40, v20, v17\n" \
  "v_add_f32 v10, v10, v9\n" \
  "v_mfma_f32_16x16x32_bf16 v[12:15], v[48:51], v[44:47], 0\n" \
  "v_cvt_pk_bf16_f32 v43, v20, v17\n" \
  "v_add_f32_dpp v10, v10, v10 row_ror:8 row_mask:0xf bank_mask:0xf\n" \
  "v_mfma_f32_16x16x32_bf16 v[16:19], v[52:55], v[44:47], 0\n" \
  "v_mov_b32 v11, v10\n" \
  "s_waitcnt lgkmcnt(0)\n" \
  "v_mfma_f32_16x16x16_bf16 v[12:15], v[22:23], v[40:41], v[12:15]\n" \
  "v_mfma_f32_16x16x16_bf16 v[16:19], v[22:23], v[42:43], v[16:19]\n" \
  "v_permlane16_swap_b32 v10, v11\n" \
  "v_add_f32 v10, v10, v11\n" \
  "ds_read_b128 v[48:51], v6 offset:10240\n" \
  "v_mov_b32 v11, v10\n" \
  "ds_read_b128 v[52:55], v6 offset:11264\n" \
  "v_mov_b64 v[20:21], 0\n" \
  "v_mov_b64 v[22:23], 0\n" \
  "v_permlane32_swap_b32 v10, v11\n" \
  "v_add_f32 v10, v10, v11\n" \
  "v_rcp_f32 v10, v10\n" \
  "s_nop 0\n" \
  "v_pk_mul_f32 v[12:13], v[12:13], v[10:11] op_sel_hi:[1,0]\n" \
  "v_pk_mul_f32 v[14:15], v[14:15], v[10:11] op_sel_hi:[1,0]\n" \
  "v_pk_mul_f32 v[16:17], v[16:17], v[10:11] op_sel_hi:[1,0]\n" \
  "v_pk_mul_f32 v[18:19], v[18:19], v[10:11] op_sel_hi:[1,0]\n" \
  "v_cvt_pk_bf16_f32 v40, v12, v13\n" \
  "v_cvt_pk_bf16_f32 v41, v14, v15\n" \
  "v_cvt_pk_bf16_f32 v42, v16, v17\n" \
  "ds_write_b64 v4, v[40:41]\n" \
  "v_cvt_pk_bf16_f32 v43, v18, v19\n" \
  "ds_write_b64 v4, v[42:43] offset:1024\n" \
  "v_mov_b64 v[12:13], 0\n" \
  "v_mov_b64 v[14:15], 0\n" \
  "v_mov_b64 v[16:17], 0\n" \
  "v_mov_b64 v[18:19], 0\n" \
  "v_mov_b64 v[8:9], 0\n" \
  "v_mov_b64 v[10:11], 0\n" \
  "s_setprio 0\n" \
  "s_waitcnt lgkmcnt(0)\n" \
  "s_barrier\n" \
  "ds_read_b128 v[40:43], v1 offset:8192\n" \
  "ds_read_b128 v[44:47], v1 offset:9216\n" \
  "s_waitcnt lgkmcnt(1)\n" \
  "v_smfmac_f32_16x16x64_bf16 v[12:15], v[40:43], v[56:63], v0\n" \
  "ds_read_b128 v[56:59], v6 offset:4096\n" \
  "ds_read_b128 v[60:63], v6 offset:5120\n" \
  "v_smfmac_f32_16x16x64_bf16 v[16:19], v[40:43], v[24:31], v0\n" \
  "ds_read_b128 v[24:27], v6 offset:6144\n" \
  "ds_read_b128 v[28:31], v6 offset:7168\n" \
  "s_waitcnt lgkmcnt(4)\n" \
  "v_smfmac_f32_16x16x64_bf16 v[12:15], v[44:47], v[32:39], v0\n" \
  "ds_read_b128 v[32:35], v6 offset:12288\n" \
  "ds_read_b128 v[36:39], v6 offset:13312\n" \
  "v_smfmac_f32_16x16x64_bf16 v[16:19], v[44:47], v[48:55], v0\n" \
  "ds_read_b128 v[48:51], v6 offset:14336\n" \
  "ds_read_b128 v[52:55], v6 offset:15360\n" \
  "s_nop 0\n" \
  "s_waitcnt lgkmcnt(6)\n" \
  "v_smfmac_f32_16x16x64_bf16 v[20:23], v[40:43], v[56:63], v0\n" \
  "s_nop 0\n" \
  "s_waitcnt lgkmcnt(4)\n" \
  "v_smfmac_f32_16x16x64_bf16 v[8:11], v[40:43], v[24:31], v0\n" \
  "v_permlane32_swap_b32 v12, v17\n" \
  "v_permlane32_swap_b32 v13, v16\n" \
  "v_permlane32_swap_b32 v14, v19\n" \
  "v_permlane32_swap_b32 v15, v18\n" \
  "s_waitcnt lgkmcnt(2)\n" \
  "v_smfmac_f32_16x16x64_bf16 v[20:23], v[44:47], v[32:39], v0\n" \
  "v_pk_add_f32 v[12:13], v[12:13], v[16:17] op_sel:[0,1] op_sel_hi:[1,0]\n" \
  "v_pk_add_f32 v[14:15], v[14:15], v[18:19] op_sel:[0,1] op_sel_hi:[1,0]\n" \
  "v_pk_fma_f32 v[12:13], v[12:13], s[84:85], s[84:85] op_sel:[0,0,1] op_sel_hi:[1,0,1]\n" \
  "v_pk_fma_f32 v[14:15], v[14:15], s[84:85], s[84:85] op_sel:[0,0,1] op_sel_hi:[1,0,1]\n" \
  "s_waitcnt lgkmcnt(0)\n" \
  "v_smfmac_f32_16x16x64_bf16 v[8:11], v[44:47], v[48:55], v0\n" \
  "v_lshl_add_u32 v12, v12, 6, s91\n" \
  "v_lshl_add_u32 v13, v13, 2, s75\n" \
  "global_atomic_add_x2 v5, v[12:13], s[26:27]\n" \
  "v_lshl_add_u32 v14, v14, 6, s91\n" \
  "v_lshl_add_u32 v15, v15, 2, s75\n" \
  "global_atomic_add_x2 v5, v[14:15], s[30:31]\n" \
  "s_nop 1\n" \
  "v_permlane32_swap_b32 v20, v9\n" \
  "v_permlane32_swap_b32 v21, v8\n" \
  "v_permlane32_swap_b32 v22, v11\n" \
  "v_permlane32_swap_b32 v23, v10\n" \
  "v_pk_add_f32 v[20:21], v[20:21], v[8:9] op_sel:[0,1] op_sel_hi:[1,0]\n" \
  "v_pk_add_f32 v[22:23], v[22:23], v[10:11] op_sel:[0,1] op_sel_hi:[1,0]\n" \
  "v_pk_fma_f32 v[20:21], v[20:21], s[84:85], s[84:85] op_sel:[0,0,1] op_sel_hi:[1,0,1]\n" \
  "v_pk_fma_f32 v[22:23], v[22:23], s[84:85], s[84:85] op_sel:[0,0,1] op_sel_hi:[1,0,1]\n" \
  "v_lshl_add_u32 v20, v20, 6, s91\n" \
  "v_lshl_add_u32 v21, v21, 2, s75\n" \
  "global_atomic_add_x2 v5, v[20:21], s[32:33]\n" \
  "v_lshl_add_u32 v22, v22, 6, s91\n" \
  "v_lshl_add_u32 v23, v23, 2, s75\n" \
  "global_atomic_add_x2 v5, v[22:23], s[28:29]\n" \
  "v_mbcnt_lo_u32_b32 v48, -1, 0\n" \
  "v_mbcnt_hi_u32_b32 v48, -1, v48\n" \
  "v_lshrrev_b32 v49, 3, v48\n" \
  "v_and_b32 v50, 7, v48\n" \
  "v_lshlrev_b32 v50, 4, v50\n" \
  "v_lshl_or_b32 v44, v49, 8, v50\n" \
  "s_add_u32 s20, s20, 32\n" \
  "s_mov_b32 s21, 0x8000\n" \
  "v_mov_b32 v52, s22\n" \
  "v_mov_b32 v54, s20\n" \
  "s_mul_i32 s88, s71, %[pp]\n" \
  "s_lshl_b32 s89, s71, 10\n" \
  "s_sub_u32 s88, s88, s25\n" \
  "s_mul_i32 s38, s24, %[an]\n" \
  "s_cmp_gt_u32 s89, s38\n" \
  "s_cselect_b32 s89, 0x1000, 0\n" \
  "s_waitcnt vmcnt(4)\n" \
  ".LCA_TKE_%=:\n" \
  "v_readlane_b32 s34, v7, 0\n" \
  "v_readlane_b32 s38, v7, 16\n" \
  "v_readlane_b32 s39, v7, 32\n" \
  "v_readlane_b32 s23, v7, 48\n" \
  "s_max_u32 s34, s34, s38\n" \
  "s_max_u32 s39, s39, s23\n" \
  "v_readlane_b32 s38, v7, 8\n" \
  "s_max_u32 s34, s34, s39\n" \
  "v_readlane_b32 s39, v7, 24\n" \
  "v_readlane_b32 s23, v7, 1\n" \
  "s_sub_u32 s23, s23, s34\n" \
  "s_sub_u32 s23, s23, s88\n" \
  "s_cmp_lt_u32 s23, s89\n" \
  "s_cselect_b32 s23, s23, 0\n" \
  "s_min_u32 s23, s23, %[ka]\n" \
  "s_add_u32 s34, s34, s23\n" \
  "s_add_u32 s34, s34, %[cd]\n" \
  "v_readlane_b32 s23, v7, 40\n" \
  "s_add_u32 s38, s38, s39\n" \
  "v_readlane_b32 s39, v7, 56\n" \
  "s_add_u32 s38, s38, s23\n" \
  "v_mov_b32 v55, s34\n" \
  "s_add_u32 s38, s38, s39\n" \
  "s_cmp_ge_u32 s38, s20\n" \
  "s_cbranch_scc1 .LCA_TKO_%=\n" \
  "s_sub_u32 s21, s21, 1\n" \
  "s_cbranch_scc1 .LCA_TKO_%=\n" \
  "s_sleep 2\n" \
  "buffer_load_dword v7, v5, s[16:19], s24 offen sc1\n" \
  "s_waitcnt vmcnt(0)\n" \
  "s_branch .LCA_TKE_%=\n" \
  ".LCA_TKO_%=:\n" \
  "ds_write_b64 v52, v[54:55]\n" \
  "s_branch .LCA_WAIU_%=\n" \
  ".LCA_WAIS_%=:\n" \
  "s_cmp_lt_i32 s39, -1000\n" \
  "s_cbranch_scc1 .LCA_WAIE_%=\n" \
  "s_sleep 1\n" \
  ".LCA_WAIU_%=:\n" \
  "s_memrealtime s[38:39]\n" \
  "s_waitcnt lgkmcnt(0)\n" \
  "s_sub_i32 s39, s38, s34\n" \
  "s_cmp_lt_i32 s39, -7\n" \
  "s_cbranch_scc1 .LCA_WAIS_%=\n" \
  "s_cmp_lt_i32 s39, 0\n" \
  "s_cbranch_scc1 .LCA_WAIU_%=\n" \
  ".LCA_WAIE_%=:\n" \
  "buffer_load_dwordx4 v[24:27], v44, s[40:43], s60 offen sc1\n" \
  "buffer_load_dwordx4 v[28:31], v44, s[40:43], s60 offen offset:2048 sc1\n" \
  "v_mbcnt_lo_u32_b32 v48, -1, 0\n" \
  "v_mbcnt_hi_u32_b32 v48, -1, v48\n" \
  "v_and_b32 v49, 15, v48\n" \
  "v_lshl_add_u32 v8, v49, 1, s97\n" \
  "v_bfe_u32 v49, v48, 4, 1\n" \
  "v_mad_u32_u24 v8, v49, s92, v8\n" \
  "v_lshrrev_b32 v49, 5, v48\n" \
  "v_mad_u32_u24 v9, v49, s93, v8\n" \
  "v_mad_u32_u24 v8, v49, s94, v8\n" \
  "v_add_u32 v10, s99, v1\n" \
  "s_lshr_b32 s88, s99, 2\n" \
  "s_add_u32 s88, s88, s74\n" \
  "s_add_u32 s88, s88, 139264\n" \
  "v_lshl_add_u32 v7, v49, 10, s88\n" \
  "v_and_b32 v49, 31, v48\n" \
  "v_lshl_add_u32 v7, v49, 3, v7\n" \
  "v_mov_b64 v[12:13], 0\n" \
  "v_mov_b64 v[14:15], 0\n" \
  "v_mov_b64 v[16:17], 0\n" \
  "v_mov_b64 v[18:19], 0\n" \
  "v_mov_b64 v[20:21], 0\n" \
  "v_mov_b64 v[22:23], 0\n" \
  "s_add_u32 s88, s74, 157760\n" \
  "v_mov_b32 v59, s88\n" \
  "v_lshrrev_b32 v49, 3, v48\n" \
  "v_and_b32 v50, 7, v48\n" \
  "v_lshlrev_b32 v50, 4, v50\n" \
  "v_lshl_or_b32 v44, v49, 8, v50\n" \
  "v_and_b32 v50, 3, v48\n" \
  "v_lshl_add_u32 v40, v50, 2, s98\n" \
  "v_lshrrev_b32 v50, 4, v48\n" \
  "v_mad_u32_u24 v40, v50, s95, v40\n" \
  "v_and_b32 v49, 1, v49\n" \
  "v_lshl_add_u32 v40, v49, 6, v40\n" \
  "v_bfe_u32 v49, v48, 2, 1\n" \
  "v_lshl_add_u32 v40, v49, 7, v40\n" \
  "v_xor_b32 v41, 16, v40\n" \
  "v_xor_b32 v42, 32, v40\n" \
  "v_xor_b32 v43, 48, v40\n" \
  "s_add_i32 s80, s86, 0x32800000\n" \
  "s_add_i32 s81, s86, 0x33800000\n" \
  "s_add_i32 s88, s86, 0xb4000000\n" \
  "v_mov_b32 v46, s88\n" \
  "v_mov_b32 v47, 0\n" \
  "v_mov_b32 v11, 0\n" \
  "s_mov_b32 s89, s60\n" \
  "s_xor_b32 s60, s60, 0x80\n" \
  "s_bitcmp0_b32 s60, 7\n" \
  "s_cselect_b32 s88, 0x8000, 0\n" \
  "s_add_u32 s60, s60, s88\n" \
  "s_and_b32 s60, s60, 0xffff\n" \
  "s_add_u32 s58, s56, s60\n" \
  "s_addc_u32 s59, s57, 0\n" \
  "s_xor_b32 s38, s35, 0\n" \
  "s_add_u32 s26, s58, s38\n" \
  "s_addc_u32 s27, s59, 0\n" \
  "s_xor_b32 s38, s35, 1024\n" \
  "s_add_u32 s28, s58, s38\n" \
  "s_addc_u32 s29, s59, 0\n" \
  "s_xor_b32 s38, s35, 2048\n" \
  "s_add_u32 s30, s58, s38\n" \
  "s_addc_u32 s31, s59, 0\n" \
  "s_xor_b32 s38, s35, 3072\n" \
  "s_add_u32 s32, s58, s38\n" \
  "s_addc_u32 s33, s59, 0\n" \
  "s_sub_u32 s71, s71, 1\n" \
  "s_mov_b32 s87, 0x40000\n" \
  "s_branch .LCA_CHK_%=\n" \
  ".LCA_POLL_%=:\n" \
  "buffer_load_dwordx4 v[24:27], v44, s[40:43], s89 offen sc1\n" \
  "buffer_load_dwordx4 v[28:31], v44, s[40:43], s89 offen offset:2048 sc1\n" \
  ".LCA_CHK_%=:\n" \
  "s_sub_u32 s87, s87, 1\n" \
  "s_cbranch_scc1 .LCA_END_%=\n" \
  "s_waitcnt vmcnt(1)\n" \
  "v_bitop3_b32 v45, v24, v26, 32 bitop3:0x80\n" \
  "v_alignbit_b32 v32, v25, v24, 2\n" \
  "v_alignbit_b32 v34, v27, v26, 2\n" \
  "s_waitcnt vmcnt(0)\n" \
  "v_bitop3_b32 v45, v45, v28, v30 bitop3:0x80\n" \
  "v_cmp_eq_u32 vcc, 0, v45\n" \
  "v_cvt_f32_i32 v32, v32\n" \
  "v_cvt_f32_i32 v33, v25\n" \
  "s_cmp_eq_u64 vcc, 0\n" \
  "s_cbranch_scc0 .LCA_POLL_%=\n" \
  "v_cvt_f32_i32 v34, v34\n" \
  "v_cvt_f32_i32 v35, v27\n" \
  "v_pk_fma_f32 v[32:33], v[32:33], s[80:81], v[46:47]\n" \
  "v_alignbit_b32 v36, v29, v28, 2\n" \
  "v_pk_fma_f32 v[34:35], v[34:35], s[80:81], v[46:47]\n" \
  "v_alignbit_b32 v38, v31, v30, 2\n" \
  "v_cvt_pk_bf16_f32 v32, v32, v34\n" \
  "v_cvt_pk_bf16_f32 v33, v33, v35\n" \
  "v_cvt_f32_i32 v36, v36\n" \
  "ds_write_b32 v40, v32\n" \
  "v_cvt_f32_i32 v37, v29\n" \
  "ds_write_b32 v41, v33\n" \
  "v_cvt_f32_i32 v38, v38\n" \
  "v_cvt_f32_i32 v39, v31\n" \
  "v_pk_fma_f32 v[36:37], v[36:37], s[80:81], v[46:47]\n" \
  "v_pk_fma_f32 v[38:39], v[38:39], s[80:81], v[46:47]\n" \
  "v_dot2c_f32_bf16 v11, v32, v32\n" \
  "v_cvt_pk_bf16_f32 v36, v36, v38\n" \
  "v_cvt_pk_bf16_f32 v37, v37, v39\n" \
  "ds_write_b32 v42, v36\n" \
  "ds_write_b32 v43, v37\n" \
  "v_dot2c_f32_bf16 v11, v33, v33\n" \
  "v_dot2c_f32_bf16 v11, v36, v36\n" \
  "v_dot2c_f32_bf16 v11, v37, v37\n" \
  "s_nop 1\n" \
  "s_nop 0\n" \
  "v_cmpx_lt_f32 vcc, 2.0, v11\n" \
  "ds_max_u32 v59, v11\n" \
  "s_mov_b64 exec, -1\n" \
  "s_branch .LCA_TOP_%=\n" \
  ".LCA_LAST_%=:\n" \
  ".LCA_END_%=:\n" \
  "s_endpgm\n"
#define CHAIN_ASM_VO \
  "s_mov_b32 s66, %[d0]\n" \
  "s_mov_b32 s90, 1\n" \
  "v_mov_b32 v60, 0\n" \
  "s_mov_b32 s92, 1152\n" \
  "s_mov_b32 s93, 576\n" \
  "s_mov_b32 s94, 2272\n" \
  "s_mov_b32 s95, 272\n" \
  "s_mov_b32 s96, 288\n" \
  "s_mov_b32 s91, 0x40000001\n" \
  "s_mov_b32 s84, 0x4b400000\n" \
  "s_mov_b32 s85, 0x4b400000\n" \
  "s_mov_b32 s78, 0x3e0293ee\n" \
  "s_mov_b32 s79, 0x3fb8aa3b\n" \
  "s_mov_b32 s76, 0xff00ff00\n" \
  "s_mov_b32 s77, 0xff00ff00\n" \
  "s_add_u32 s58, s56, s60\n" \
  "s_addc_u32 s59, s57, 0\n" \
  "s_xor_b32 s38, s35, 0\n" \
  "s_add_u32 s26, s58, s38\n" \
  "s_addc_u32 s27, s59, 0\n" \
  "s_xor_b32 s38, s35, 1024\n" \
  "s_add_u32 s28, s58, s38\n" \
  "s_addc_u32 s29, s59, 0\n" \
  "s_xor_b32 s38, s35, 2048\n" \
  "s_add_u32 s30, s58, s38\n" \
  "s_addc_u32 s31, s59, 0\n" \
  "s_xor_b32 s38, s35, 3072\n" \
  "s_add_u32 s32, s58, s38\n" \
  "s_addc_u32 s33, s59, 0\n" \
  "s_lshr_b32 s98, s60, 2\n" \
  "s_add_u32 s98, s98, s74\n" \
  "s_add_u32 s98, s98, 131072\n" \
  "s_lshl_b32 s97, s73, 5\n" \
  "s_add_u32 s97, s97, s74\n" \
  "s_add_u32 s97, s97, 160992\n" \
  "s_lshl_b32 s99, s73, 11\n" \
  "s_add_u32 s99, s99, s74\n" \
  "s_add_u32 s99, s99, 133120\n" \
  "v_mbcnt_lo_u32_b32 v48, -1, 0\n" \
  "v_mbcnt_hi_u32_b32 v48, -1, v48\n" \
  "s_sub_u32 s88, s73, 4\n" \
  "v_and_b32 v51, 15, v48\n" \
  "v_lshl_add_u32 v4, v51, 1, s97\n" \
  "v_bfe_u32 v49, v48, 4, 1\n" \
  "v_mad_u32_u24 v4, v49, s92, v4\n" \
  "v_lshrrev_b32 v50, 5, v48\n" \
  "v_mad_u32_u24 v4, v50, s93, v4\n" \
  "v_lshlrev_b32 v8, 3, v49\n" \
  "v_lshl_add_u32 v8, v51, 4, v8\n" \
  "v_lshl_add_u32 v8, v50, 10, v8\n" \
  "s_lshl_b32 s89, s88, 8\n" \
  "s_add_u32 s89, s89, s74\n" \
  "s_add_u32 s89, s89, 139264\n" \
  "v_and_b32 v49, 31, v48\n" \
  "v_lshl_add_u32 v5, v49, 3, s89\n" \
  "v_lshl_add_u32 v5, v50, 10, v5\n" \
  "v_lshrrev_b32 v49, 2, v51\n" \
  "v_and_b32 v50, 5, v51\n" \
  "v_and_or_b32 v49, v49, 2, v50\n" \
  "s_lshl_b32 s89, s88, 6\n" \
  "s_add_u32 s89, s89, s74\n" \
  "s_add_u32 s89, s89, 160992\n" \
  "v_and_b32 v50, 48, v48\n" \
  "v_add_u32 v6, s89, v50\n" \
  "v_mad_u32_u24 v6, v49, s96, v6\n" \
  "v_lshlrev_b32 v7, 3, v50\n" \
  "v_lshl_add_u32 v7, v49, 4, v7\n" \
  "v_and_b32 v49, 2, v51\n" \
  "v_lshl_add_u32 v7, v49, 29, v7\n" \
  "v_mov_b32 v9, 0\n" \
  "v_lshlrev_b32 v10, 4, v48\n" \
  "v_mov_b64 v[12:13], 0\n" \
  "v_mov_b64 v[14:15], 0\n" \
  "v_mov_b64 v[16:17], 0\n" \
  "v_mov_b64 v[18:19], 0\n" \
  "v_mov_b64 v[20:21], 0\n" \
  "v_mov_b64 v[22:23], 0\n" \
  "s_add_u32 s88, s74, 157760\n" \
  "v_mov_b32 v59, s88\n" \
  "v_mov_b32 v24, 0x44870000\n" \
  "v_mul_f32 v24, s72, v24\n" \
  "s_nop 0\n" \
  "v_readfirstlane_b32 s86, v24\n" \
  "s_lshr_b32 s86, s86, 23\n" \
  "s_addk_i32 s86, 0xff83\n" \
  "s_ashr_i32 s86, s86, 1\n" \
  "s_max_i32 s86, s86, -20\n" \
  "s_min_i32 s86, s86, 100\n" \
  "s_mov_b32 s82, s86\n" \
  "s_lshr_b32 s83, s72, 23\n" \
  "s_sub_i32 s83, s83, 242\n" \
  "s_lshl_b32 s88, s73, 10\n" \
  "v_add_u32 v24, s88, v1\n" \
  "ds_read_b128 v[32:35], v24 offset:0\n" \
  "s_waitcnt lgkmcnt(0)\n" \
  "v_mov_b32 v11, 0\n" \
  "v_dot2c_f32_bf16 v11, v32, v32\n" \
  "v_dot2c_f32_bf16 v11, v33, v33\n" \
  "v_dot2c_f32_bf16 v11, v34, v34\n" \
  "v_dot2c_f32_bf16 v11, v35, v35\n" \
  "s_nop 1\n" \
  "s_nop 0\n" \
  "v_cmpx_lt_f32 vcc, 2.0, v11\n" \
  "ds_max_u32 v59, v11\n" \
  "s_mov_b64 exec, -1\n" \
  ".LCV_TOP_%=:\n" \
  "s_waitcnt lgkmcnt(0)\n" \
  "s_barrier\n" \
  "s_setprio 3\n" \
  "buffer_load_dword v58, v9, s[44:47], s68 offen sc1\n" \
  "s_cmp_lg_u32 s73, 7\n" \
  "s_cbranch_scc1 .LCV_NOTK_%=\n" \
  "s_memrealtime s[38:39]\n" \
  "v_mov_b32 v57, 1\n" \
  "s_mul_i32 s23, s71, %[pp]\n" \
  "s_lshr_b32 s76, s66, 4\n" \
  "s_mov_b64 exec, 1\n" \
  "s_waitcnt lgkmcnt(0)\n" \
  "s_mov_b32 s67, s38\n" \
  "s_add_u32 s38, s38, s76\n" \
  "v_mov_b32 v56, s38\n" \
  "buffer_atomic_umax v56, off, s[16:19], s24\n" \
  "buffer_atomic_add v57, off, s[16:19], s24 offset:64\n" \
  "v_add_u32 v60, s23, v56\n" \
  "buffer_store_dword v60, off, s[16:19], s25 sc1\n" \
  "s_mov_b64 exec, -1\n" \
  ".LCV_NOTK_%=:\n" \
  "ds_read_b128 v[24:27], v1 offset:0\n" \
  "ds_read_b128 v[28:31], v1 offset:1024\n" \
  "ds_read_b128 v[32:35], v1 offset:2048\n" \
  "ds_read_b128 v[36:39], v1 offset:3072\n" \
  "ds_read_b128 v[40:43], v1 offset:4096\n" \
  "ds_read_b128 v[44:47], v1 offset:5120\n" \
  "ds_read_b128 v[48:51], v1 offset:6144\n" \
  "ds_read_b128 v[52:55], v1 offset:7168\n" \
  "s_waitcnt lgkmcnt(7)\n" \
  "v_smfmac_f32_16x16x64_bf16 v[12:15], v[24:27], v[64:71], v0\n" \
  "s_waitcnt lgkmcnt(6)\n" \
  "v_smfmac_f32_16x16x64_bf16 v[12:15], v[28:31], v[72:79], v0\n" \
  "s_waitcnt lgkmcnt(5)\n" \
  "v_smfmac_f32_16x16x64_bf16 v[12:15], v[32:35], v[80:87], v0\n" \
  "s_waitcnt lgkmcnt(4)\n" \
  "v_smfmac_f32_16x16x64_bf16 v[12:15], v[36:39], v[88:95], v0\n" \
  "s_waitcnt lgkmcnt(3)\n" \
  "v_smfmac_f32_16x16x64_bf16 v[12:15], v[40:43], v[96:103], v0\n" \
  "s_waitcnt lgkmcnt(2)\n" \
  "v_smfmac_f32_16x16x64_bf16 v[12:15], v[44:47], v[104:111], v0\n" \
  "s_waitcnt lgkmcnt(1)\n" \
  "v_smfmac_f32_16x16x64_bf16 v[12:15], v[48:51], v[112:119], v0\n" \
  "s_waitcnt lgkmcnt(0)\n" \
  "v_smfmac_f32_16x16x64_bf16 v[12:15], v[52:55], v[120:127], v0\n" \
  "ds_read_b32 v11, v1 offset:26688\n" \
  "s_nop 6\n" \
  "v_permlane32_swap_b32 v12, v14\n" \
  "v_permlane32_swap_b32 v13, v15\n" \
  "v_pk_add_f32 v[12:13], v[12:13], v[14:15]\n" \
  "s_nop 0\n" \
  "v_cvt_pk_bf16_f32 v12, v12, v13\n" \
  "ds_write_b16 v4, v12\n" \
  "ds_write_b16_d16_hi v4, v12 offset:288\n" \
  "s_setprio 0\n" \
  "v_mov_b64 v[12:13], 0\n" \
  "v_mov_b64 v[14:15], 0\n" \
  "s_waitcnt lgkmcnt(2)\n" \
  "v_readfirstlane_b32 s86, v11\n" \
  "s_lshr_b32 s86, s86, 23\n" \
  "s_add_i32 s86, s86, s83\n" \
  "s_ashr_i32 s86, s86, 1\n" \
  "s_max_i32 s86, s86, s82\n" \
  "s_min_i32 s86, s86, 100\n" \
  "s_lshl_b32 s86, s86, 23\n" \
  "s_sub_i32 s84, 0x4a800000, s86\n" \
  "s_cmp_eq_u32 s71, 0\n" \
  "s_waitcnt lgkmcnt(0)\n" \
  "s_barrier\n" \
  "ds_read_b128 v[60:63], v6 offset:0\n" \
  "v_smfmac_f32_16x16x64_bf16 v[16:19], v[24:27], v[128:135], v0\n" \
  "v_smfmac_f32_16x16x64_bf16 v[20:23], v[24:27], v[192:199], v0\n" \
  "v_smfmac_f32_16x16x64_bf16 v[16:19], v[28:31], v[136:143], v0\n" \
  "v_smfmac_f32_16x16x64_bf16 v[20:23], v[28:31], v[200:207], v0\n" \
  "v_smfmac_f32_16x16x64_bf16 v[16:19], v[32:35], v[144:151], v0\n" \
  "v_smfmac_f32_16x16x64_bf16 v[20:23], v[32:35], v[208:215], v0\n" \
  "v_smfmac_f32_16x16x64_bf16 v[16:19], v[36:39], v[152:159], v0\n" \
  "v_smfmac_f32_16x16x64_bf16 v[20:23], v[36:39], v[216:223], v0\n" \
  "v_smfmac_f32_16x16x64_bf16 v[16:19], v[40:43], v[160:167], v0\n" \
  "v_smfmac_f32_16x16x64_bf16 v[20:23], v[40:43], v[224:231], v0\n" \
  "v_smfmac_f32_16x16x64_bf16 v[16:19], v[44:47], v[168:175], v0\n" \
  "v_smfmac_f32_16x16x64_bf16 v[20:23], v[44:47], v[232:239], v0\n" \
  "v_smfmac_f32_16x16x64_bf16 v[16:19], v[48:51], v[176:183], v0\n" \
  "v_smfmac_f32_16x16x64_bf16 v[20:23], v[48:51], v[240:247], v0\n" \
  "v_smfmac_f32_16x16x64_bf16 v[16:19], v[52:55], v[184:191], v0\n" \
  "v_smfmac_f32_16x16x64_bf16 v[20:23], v[52:55], v[248:255], v0\n" \
  "ds_read_b128 v[24:27], v3 offset:0\n" \
  "ds_read_b128 v[28:31], v3 offset:1024\n" \
  "ds_read_b128 v[32:35], v3 offset:2048\n" \
  "ds_read_b128 v[36:39], v3 offset:3072\n" \
  "ds_read_b128 v[40:43], v3 offset:8192\n" \
  "ds_read_b128 v[44:47], v3 offset:9216\n" \
  "s_nop 0\n" \
  "v_permlane32_swap_b32 v16, v18\n" \
  "v_permlane32_swap_b32 v17, v19\n" \
  "v_permlane32_swap_b32 v20, v22\n" \
  "v_permlane32_swap_b32 v21, v23\n" \
  "v_pk_add_f32 v[16:17], v[16:17], v[18:19]\n" \
  "v_pk_add_f32 v[20:21], v[20:21], v[22:23]\n" \
  "v_cvt_pk_bf16_f32 v16, v16, v17\n" \
  "v_cvt_pk_bf16_f32 v17, v20, v21\n" \
  "ds_write_b64 v5, v[16:17]\n" \
  "s_cbranch_scc1 .LCV_LAST_%=\n" \
  "s_cmp_eq_u32 s73, 7\n" \
  "s_waitcnt vmcnt(3)\n" \
  "s_cbranch_scc1 .LCV_TKW_%=\n" \
  "s_waitcnt vmcnt(0)\n" \
  ".LCV_TKW_%=:\n" \
  "v_cmp_eq_u32 vcc, 0, v58\n" \
  "s_cmp_eq_u64 vcc, 0\n" \
  "s_cselect_b32 s90, 1, 0\n" \
  "s_waitcnt lgkmcnt(0)\n" \
  "s_barrier\n" \
  "s_cbranch_scc0 .LCV_NOTOK_%=\n" \
  "s_mov_b32 m0, s99\n" \
  "s_add_u32 s88, s99, 8192\n" \
  "global_load_lds_dwordx4 v10, s[62:63] sc1\n" \
  "global_load_lds_dwordx4 v10, s[62:63] offset:1024 sc1\n" \
  "s_mov_b32 m0, s88\n" \
  "s_nop 0\n" \
  "global_load_lds_dwordx4 v10, s[64:65] sc1\n" \
  "global_load_lds_dwordx4 v10, s[64:65] offset:1024 sc1\n" \
  ".LCV_NOTOK_%=:\n" \
  "ds_write_b32 v59, v12\n" \
  "ds_read_b128 v[48:51], v3 offset:10240\n" \
  "ds_read_b128 v[52:55], v3 offset:11264\n" \
  "v_permlane32_swap_b32 v16, v17\n" \
  "v_max_u32 v60, 1, v60\n" \
  "v_max_u32 v61, 1, v61\n" \
  "v_max_u32 v16, 1, v16\n" \
  "v_max_u32 v17, 1, v17\n" \
  "buffer_store_dwordx2 v[16:17], v8, s[52:55], s70 offen sc1\n" \
  "v_max_u32 v62, 1, v62\n" \
  "v_max_u32 v63, 1, v63\n" \
  "buffer_store_dwordx4 v[60:63], v7, s[48:51], s69 offen sc1\n" \
  "v_mov_b64 v[18:19], 0\n" \
  "v_mov_b64 v[20:21], 0\n" \
  "v_mov_b64 v[22:23], 0\n" \
  "v_mov_b64 v[16:17], 0\n" \
  "ds_read_b128 v[56:59], v3 offset:4096\n" \
  "ds_read_b128 v[60:63], v3 offset:5120\n" \
  "s_waitcnt lgkmcnt(0)\n" \
  "s_barrier\n" \
  "ds_read_b128 v[4:7], v1 offset:8192\n" \
  "ds_read_b128 v[8:11], v1 offset:9216\n" \
  "s_waitcnt lgkmcnt(1)\n" \
  "v_smfmac_f32_16x16x64_bf16 v[12:15], v[4:7], v[24:31], v0\n" \
  "ds_read_b128 v[24:27], v3 offset:6144\n" \
  "ds_read_b128 v[28:31], v3 offset:7168\n" \
  "v_smfmac_f32_16x16x64_bf16 v[16:19], v[4:7], v[32:39], v0\n" \
  "ds_read_b128 v[32:35], v3 offset:12288\n" \
  "ds_read_b128 v[36:39], v3 offset:13312\n" \
  "s_waitcnt lgkmcnt(4)\n" \
  "v_smfmac_f32_16x16x64_bf16 v[12:15], v[8:11], v[40:47], v0\n" \
  "v_smfmac_f32_16x16x64_bf16 v[16:19], v[8:11], v[48:55], v0\n" \
  "s_cmp_eq_u32 s90, 1\n" \
  "s_cbranch_scc1 .LCV_NMT_%=\n" \
  "buffer_load_dword v55, off, s[44:47], s68 sc1\n" \
  ".LCV_NMT_%=:\n" \
  "v_smfmac_f32_16x16x64_bf16 v[20:23], v[4:7], v[56:63], v0\n" \
  "ds_read_b128 v[56:59], v3 offset:14336\n" \
  "ds_read_b128 v[60:63], v3 offset:15360\n" \
  "v_mov_b64 v[40:41], 0\n" \
  "v_mov_b64 v[42:43], 0\n" \
  "s_waitcnt lgkmcnt(2)\n" \
  "v_smfmac_f32_16x16x64_bf16 v[20:23], v[8:11], v[32:39], v0\n" \
  "s_nop 0\n" \
  "v_permlane32_swap_b32 v12, v17\n" \
  "v_permlane32_swap_b32 v13, v16\n" \
  "v_permlane32_swap_b32 v14, v19\n" \
  "v_permlane32_swap_b32 v15, v18\n" \
  "v_smfmac_f32_16x16x64_bf16 v[40:43], v[4:7], v[24:31], v0\n" \
  "v_pk_add_f32 v[12:13], v[12:13], v[16:17] op_sel:[0,1] op_sel_hi:[1,0]\n" \
  "v_pk_add_f32 v[14:15], v[14:15], v[18:19] op_sel:[0,1] op_sel_hi:[1,0]\n" \
  "v_pk_fma_f32 v[12:13], v[12:13], s[84:85], s[84:85] op_sel:[0,0,1] op_sel_hi:[1,0,1]\n" \
  "v_pk_fma_f32 v[14:15], v[14:15], s[84:85], s[84:85] op_sel:[0,0,1] op_sel_hi:[1,0,1]\n" \
  "s_waitcnt lgkmcnt(0)\n" \
  "v_smfmac_f32_16x16x64_bf16 v[40:43], v[8:11], v[56:63], v0\n" \
  "v_lshl_add_u32 v12, v12, 6, s91\n" \
  "v_lshl_add_u32 v13, v13, 2, s75\n" \
  "global_atomic_add_x2 v2, v[12:13], s[26:27]\n" \
  "v_lshl_add_u32 v14, v14, 6, s91\n" \
  "v_lshl_add_u32 v15, v15, 2, s75\n" \
  "global_atomic_add_x2 v2, v[14:15], s[30:31]\n" \
  "s_nop 1\n" \
  "v_permlane32_swap_b32 v20, v41\n" \
  "v_permlane32_swap_b32 v21, v40\n" \
  "v_permlane32_swap_b32 v22, v43\n" \
  "v_permlane32_swap_b32 v23, v42\n" \
  "v_pk_add_f32 v[20:21], v[20:21], v[40:41] op_sel:[0,1] op_sel_hi:[1,0]\n" \
  "v_pk_add_f32 v[22:23], v[22:23], v[42:43] op_sel:[0,1] op_sel_hi:[1,0]\n" \
  "v_pk_fma_f32 v[20:21], v[20:21], s[84:85], s[84:85] op_sel:[0,0,1] op_sel_hi:[1,0,1]\n" \
  "v_pk_fma_f32 v[22:23], v[22:23], s[84:85], s[84:85] op_sel:[0,0,1] op_sel_hi:[1,0,1]\n" \
  "v_lshl_add_u32 v20, v20, 6, s91\n" \
  "v_lshl_add_u32 v21, v21, 2, s75\n" \
  "global_atomic_add_x2 v2, v[20:21], s[32:33]\n" \
  "v_lshl_add_u32 v22, v22, 6, s91\n" \
  "v_lshl_add_u32 v23, v23, 2, s75\n" \
  "global_atomic_add_x2 v2, v[22:23], s[28:29]\n" \
  "s_memrealtime s[76:77]\n" \
  "v_mbcnt_lo_u32_b32 v48, -1, 0\n" \
  "v_mbcnt_hi_u32_b32 v48, -1, v48\n" \
  "s_add_u32 s20, s20, 32\n" \
  "s_cmp_eq_u32 s90, 1\n" \
  "s_cbranch_scc1 .LCV_HAVE_%=\n" \
  "s_mov_b32 s87, 0x40000\n" \
  "v_mov_b32 v9, 0\n" \
  "v_lshlrev_b32 v10, 4, v48\n" \
  "s_waitcnt vmcnt(4)\n" \
  "v_cmp_eq_u32 vcc, 0, v55\n" \
  "s_cmp_eq_u64 vcc, 0\n" \
  "s_cbranch_scc1 .LCV_GOT_%=\n" \
  ".LCV_LATE_%=:\n" \
  "buffer_load_dword v58, v9, s[44:47], s68 offen sc1\n" \
  "s_sub_u32 s87, s87, 1\n" \
  "s_cbranch_scc1 .LCV_END_%=\n" \
  "s_waitcnt vmcnt(0)\n" \
  "v_cmp_eq_u32 vcc, 0, v58\n" \
  "s_cmp_eq_u64 vcc, 0\n" \
  "s_cbranch_scc1 .LCV_GOT_%=\n" \
  "s_sleep 12\n" \
  "s_branch .LCV_LATE_%=\n" \
  ".LCV_GOT_%=:\n" \
  "s_mov_b32 m0, s99\n" \
  "s_add_u32 s88, s99, 8192\n" \
  "global_load_lds_dwordx4 v10, s[62:63] sc1\n" \
  "global_load_lds_dwordx4 v10, s[62:63] offset:1024 sc1\n" \
  "s_mov_b32 m0, s88\n" \
  "s_nop 0\n" \
  "global_load_lds_dwordx4 v10, s[64:65] sc1\n" \
  "global_load_lds_dwordx4 v10, s[64:65] offset:1024 sc1\n" \
  ".LCV_HAVE_%=:\n" \
  "v_lshrrev_b32 v49, 3, v48\n" \
  "v_and_b32 v50, 7, v48\n" \
  "v_lshlrev_b32 v50, 4, v50\n" \
  "v_lshl_or_b32 v44, v49, 8, v50\n" \
  "s_mov_b32 s21, 0x8000\n" \
  "v_mov_b32 v52, s22\n" \
  "v_mov_b32 v54, s20\n" \
  "s_branch .LCV_TKF_%=\n" \
  ".LCV_TKE_%=:\n" \
  "s_sleep 1\n" \
  ".LCV_TKF_%=:\n" \
  "ds_read_b64 v[54:55], v52\n" \
  "s_sub_u32 s21, s21, 1\n" \
  "s_cbranch_scc1 .LCV_TKO_%=\n" \
  "s_waitcnt lgkmcnt(0)\n" \
  "v_readfirstlane_b32 s38, v54\n" \
  "v_readfirstlane_b32 s34, v55\n" \
  "s_cmp_eq_u32 s38, s20\n" \
  "s_cbranch_scc0 .LCV_TKE_%=\n" \
  ".LCV_TKO_%=:\n" \
  "s_branch .LCV_WAIU_%=\n" \
  ".LCV_WAIS_%=:\n" \
  "s_cmp_lt_i32 s39, -1000\n" \
  "s_cbranch_scc1 .LCV_WAIE_%=\n" \
  "s_sleep 1\n" \
  ".LCV_WAIU_%=:\n" \
  "s_memrealtime s[38:39]\n" \
  "s_waitcnt lgkmcnt(0)\n" \
  "s_sub_i32 s39, s38, s34\n" \
  "s_cmp_lt_i32 s39, -7\n" \
  "s_cbranch_scc1 .LCV_WAIS_%=\n" \
  "s_cmp_lt_i32 s39, 0\n" \
  "s_cbranch_scc1 .LCV_WAIU_%=\n" \
  ".LCV_WAIE_%=:\n" \
  "buffer_load_dwordx4 v[24:27], v44, s[40:43], s60 offen sc1\n" \
  "buffer_load_dwordx4 v[28:31], v44, s[40:43], s60 offen offset:2048 sc1\n" \
  "s_sub_u32 s76, s76, s67\n" \
  "s_lshl_b32 s76, s76, 4\n" \
  "s_sub_i32 s76, s76, s66\n" \
  "s_min_i32 s76, s76, 128\n" \
  "s_max_i32 s76, s76, -256\n" \
  "s_ashr_i32 s76, s76, 2\n" \
  "s_add_i32 s66, s66, s76\n" \
  "v_mbcnt_lo_u32_b32 v48, -1, 0\n" \
  "v_mbcnt_hi_u32_b32 v48, -1, v48\n" \
  "s_sub_u32 s88, s73, 4\n" \
  "v_and_b32 v51, 15, v48\n" \
  "v_lshl_add_u32 v4, v51, 1, s97\n" \
  "v_bfe_u32 v49, v48, 4, 1\n" \
  "v_mad_u32_u24 v4, v49, s92, v4\n" \
  "v_lshrrev_b32 v50, 5, v48\n" \
  "v_mad_u32_u24 v4, v50, s93, v4\n" \
  "v_lshlrev_b32 v8, 3, v49\n" \
  "v_lshl_add_u32 v8, v51, 4, v8\n" \
  "v_lshl_add_u32 v8, v50, 10, v8\n" \
  "s_lshl_b32 s89, s88, 8\n" \
  "s_add_u32 s89, s89, s74\n" \
  "s_add_u32 s89, s89, 139264\n" \
  "v_and_b32 v49, 31, v48\n" \
  "v_lshl_add_u32 v5, v49, 3, s89\n" \
  "v_lshl_add_u32 v5, v50, 10, v5\n" \
  "v_lshrrev_b32 v49, 2, v51\n" \
  "v_and_b32 v50, 5, v51\n" \
  "v_and_or_b32 v49, v49, 2, v50\n" \
  "s_lshl_b32 s89, s88, 6\n" \
  "s_add_u32 s89, s89, s74\n" \
  "s_add_u32 s89, s89, 160992\n" \
  "v_and_b32 v50, 48, v48\n" \
  "v_add_u32 v6, s89, v50\n" \
  "v_mad_u32_u24 v6, v49, s96, v6\n" \
  "v_lshlrev_b32 v7, 3, v50\n" \
  "v_lshl_add_u32 v7, v49, 4, v7\n" \
  "v_and_b32 v49, 2, v51\n" \
  "v_lshl_add_u32 v7, v49, 29, v7\n" \
  "v_mov_b32 v9, 0\n" \
  "v_lshlrev_b32 v10, 4, v48\n" \
  "v_mov_b64 v[12:13], 0\n" \
  "v_mov_b64 v[14:15], 0\n" \
  "v_mov_b64 v[16:17], 0\n" \
  "v_mov_b64 v[18:19], 0\n" \
  "v_mov_b64 v[20:21], 0\n" \
  "v_mov_b64 v[22:23], 0\n" \
  "s_add_u32 s88, s74, 157760\n" \
  "v_mov_b32 v59, s88\n" \
  "v_lshrrev_b32 v49, 3, v48\n" \
  "v_and_b32 v50, 7, v48\n" \
  "v_lshlrev_b32 v50, 4, v50\n" \
  "v_lshl_or_b32 v44, v49, 8, v50\n" \
  "v_and_b32 v50, 3, v48\n" \
  "v_lshl_add_u32 v40, v50, 2, s98\n" \
  "v_lshrrev_b32 v50, 4, v48\n" \
  "v_mad_u32_u24 v40, v50, s95, v40\n" \
  "v_and_b32 v49, 1, v49\n" \
  "v_lshl_add_u32 v40, v49, 6, v40\n" \
  "v_bfe_u32 v49, v48, 2, 1\n" \
  "v_lshl_add_u32 v40, v49, 7, v40\n" \
  "v_xor_b32 v41, 16, v40\n" \
  "v_xor_b32 v42, 32, v40\n" \
  "v_xor_b32 v43, 48, v40\n" \
  "s_add_u32 s62, s62, 0x80000\n" \
  "s_addc_u32 s63, s63, 0\n" \
  "s_add_u32 s64, s64, 0x40000\n" \
  "s_addc_u32 s65, s65, 0\n" \
  "s_add_u32 s68, s68, 0x1000\n" \
  "s_add_u32 s69, s69, 0x10000\n" \
  "s_add_u32 s70, s70, 0x10000\n" \
  "s_add_i32 s80, s86, 0x32800000\n" \
  "s_add_i32 s81, s86, 0x33800000\n" \
  "s_add_i32 s88, s86, 0xb4000000\n" \
  "v_mov_b32 v46, s88\n" \
  "v_mov_b32 v47, 0\n" \
  "v_mov_b32 v11, 0\n" \
  "s_mov_b32 s89, s60\n" \
  "s_xor_b32 s60, s60, 0x80\n" \
  "s_bitcmp0_b32 s60, 7\n" \
  "s_cselect_b32 s88, 0x8000, 0\n" \
  "s_add_u32 s60, s60, s88\n" \
  "s_and_b32 s60, s60, 0xffff\n" \
  "s_add_u32 s58, s56, s60\n" \
  "s_addc_u32 s59, s57, 0\n" \
  "s_xor_b32 s38, s35, 0\n" \
  "s_add_u32 s26, s58, s38\n" \
  "s_addc_u32 s27, s59, 0\n" \
  "s_xor_b32 s38, s35, 1024\n" \
  "s_add_u32 s28, s58, s38\n" \
  "s_addc_u32 s29, s59, 0\n" \
  "s_xor_b32 s38, s35, 2048\n" \
  "s_add_u32 s30, s58, s38\n" \
  "s_addc_u32 s31, s59, 0\n" \
  "s_xor_b32 s38, s35, 3072\n" \
  "s_add_u32 s32, s58, s38\n" \
  "s_addc_u32 s33, s59, 0\n" \
  "s_sub_u32 s71, s71, 1\n" \
  "s_mov_b32 s87, 0x40000\n" \
  "s_branch .LCV_CHK_%=\n" \
  ".LCV_POLL_%=:\n" \
  "buffer_load_dwordx4 v[24:27], v44, s[40:43], s89 offen sc1\n" \
  "buffer_load_dwordx4 v[28:31], v44, s[40:43], s89 offen offset:2048 sc1\n" \
  ".LCV_CHK_%=:\n" \
  "s_sub_u32 s87, s87, 1\n" \
  "s_cbranch_scc1 .LCV_END_%=\n" \
  "s_waitcnt vmcnt(1)\n" \
  "v_bitop3_b32 v45, v24, v26, 32 bitop3:0x80\n" \
  "v_alignbit_b32 v32, v25, v24, 2\n" \
  "v_alignbit_b32 v34, v27, v26, 2\n" \
  "s_waitcnt vmcnt(0)\n" \
  "v_bitop3_b32 v45, v45, v28, v30 bitop3:0x80\n" \
  "v_cmp_eq_u32 vcc, 0, v45\n" \
  "v_cvt_f32_i32 v32, v32\n" \
  "v_cvt_f32_i32 v33, v25\n" \
  "s_cmp_eq_u64 vcc, 0\n" \
  "s_cbranch_scc0 .LCV_POLL_%=\n" \
  "v_cvt_f32_i32 v34, v34\n" \
  "v_cvt_f32_i32 v35, v27\n" \
  "v_pk_fma_f32 v[32:33], v[32:33], s[80:81], v[46:47]\n" \
  "v_alignbit_b32 v36, v29, v28, 2\n" \
  "v_pk_fma_f32 v[34:35], v[34:35], s[80:81], v[46:47]\n" \
  "v_alignbit_b32 v38, v31, v30, 2\n" \
  "v_cvt_pk_bf16_f32 v32, v32, v34\n" \
  "v_cvt_pk_bf16_f32 v33, v33, v35\n" \
  "v_cvt_f32_i32 v36, v36\n" \
  "ds_write_b32 v40, v32\n" \
  "v_cvt_f32_i32 v37, v29\n" \
  "ds_write_b32 v41, v33\n" \
  "v_cvt_f32_i32 v38, v38\n" \
  "v_cvt_f32_i32 v39, v31\n" \
  "v_pk_fma_f32 v[36:37], v[36:37], s[80:81], v[46:47]\n" \
  "v_pk_fma_f32 v[38:39], v[38:39], s[80:81], v[46:47]\n" \
  "v_dot2c_f32_bf16 v11, v32, v32\n" \
  "v_cvt_pk_bf16_f32 v36, v36, v38\n" \
  "v_cvt_pk_bf16_f32 v37, v37, v39\n" \
  "ds_write_b32 v42, v36\n" \
  "ds_write_b32 v43, v37\n" \
  "v_dot2c_f32_bf16 v11, v33, v33\n" \
  "v_dot2c_f32_bf16 v11, v36, v36\n" \
  "v_dot2c_f32_bf16 v11, v37, v37\n" \
  "s_nop 1\n" \
  "s_nop 0\n" \
  "v_cmpx_lt_f32 vcc, 2.0, v11\n" \
  "ds_max_u32 v59, v11\n" \
  "s_mov_b64 exec, -1\n" \
  "s_branch .LCV_TOP_%=\n" \
  ".LCV_LAST_%=:\n" \
  "s_waitcnt lgkmcnt(0)\n" \
  "s_nop 0\n" \
  "v_permlane32_swap_b32 v16, v17\n" \
  "v_max_u32 v60, 1, v60\n" \
  "v_max_u32 v61, 1, v61\n" \
  "v_max_u32 v16, 1, v16\n" \
  "v_max_u32 v17, 1, v17\n" \
  "buffer_store_dwordx2 v[16:17], v8, s[52:55], s70 offen sc1\n" \
  "v_max_u32 v62, 1, v62\n" \
  "v_max_u32 v63, 1, v63\n" \
  "buffer_store_dwordx4 v[60:63], v7, s[48:51], s69 offen sc1\n" \
  "s_waitcnt vmcnt(0)\n" \
  ".LCV_END_%=:\n" \
  "s_endpgm\n"
__device__ __forceinline__ v4u mk_srd(const void* p, const unsigned bytes) {
  const unsigned long long a = (unsigned long long)p;
  return v4u{(unsigned)a, (unsigned)(a >> 32) & 0xffffu, bytes, 0x00020000u};
}
template <bool ATT>
__device__ __forceinline__ void role_chain_t(const MegaArgs& A, char* smem, const int L, const int h, const int rep) {
  __builtin_amdgcn_s_waitcnt(0x0F70);
  u16* swo = reinterpret_cast<u16*>(smem);
  const int w = __builtin_amdgcn_readfirstlane(threadIdx.x >> 6);
  const int wc = (w + ROT) & 7, psw = PSW ? (PSWH) : 0;
  char* ws = A.ws;
  unsigned* FL = (unsigned*)(ws + OFF_FL);
  unsigned* TMO = FL + FL_TMO;
#define LANE_IDS()                         \
  const int l = lane_id();                 \
  const int c = l & 15, g = l >> 4;        \
  (void)c; (void)g;
  float* sgm = reinterpret_cast<float*>(smem + C_X);
  float* sgf = sgm + 8;
  unsigned* FQ = FL + FL_FQ + L * 64 * 32 * 32;
  v8u w0[8], w1[8], w2[8];
  {
    LANE_IDS();
    v4u m0r;
    const unsigned sb = lds_base(smem);
    const int q = (l >> 2) & 3, p = l & 3;
    const int f = (q & 1) | ((g & 1) << 1) | ((q >> 1) << 2);
    const unsigned ra = sb + (16 * (g >> 1) + 4 * (g & 1) + (q & 1) + 8 * (q >> 1)) * 256 + (w ^ f) * 32 + p * 8;
#define W_READ(W, hf, X)                                                       \
  {                                                                           \
    _Pragma("unroll") for (int t = 0; t < 4; ++t) {                           \
      const unsigned a_ = (ra ^ (X)) + (hf) * 8192 + t * 32768;               \
      const v2u r0 = trd(a_), r1 = trd(a_ + 512), r2 = trd(a_ + 16384), r3 = trd(a_ + 16384 + 512); \
      W[4 * (hf) + t] = v8u{r0.x, r0.y, r1.x, r1.y, r2.x, r2.y, r3.x, r3.y};  \
    }                                                                         \
    _Pragma("unroll") for (int t = 0; t < 4; ++t) asm volatile("" : "+v"(W[4 * (hf) + t])); \
  }
#define W_STAGE(M, hf, VM, NEXT)                                               \
  {                                                                           \
    WAIT_VM(VM);                                                              \
    RAW_BAR();                                                                \
    if ((M) == 0 && ATT) { W_READ(w0, hf, 0); W_READ(w2, hf, 128); }          \
    if ((M) == 1 && ATT) { W_READ(w1, hf, 0); }                               \
    if ((M) == 1 && !ATT) { W_READ(w0, hf, 0); }                              \
    if ((M) == 2 && !ATT) { W_READ(w1, hf, 128); W_READ(w2, hf, 0); }         \
    RAW_BAR();                                                                \
    NEXT;                                                                     \
  }
#define WO_ISSUE(hf)                                                          \
  {                                                                           \
    const char* base_ = (const char*)A.Wo + (size_t)(h * DH + 64 * (hf) + 8 * w) * (DM * 2); \
    _Pragma("unroll") for (int i = 0; i < 8; ++i) {                           \
      const int fd = (i & 1) | (((i >> 2) & 1) << 1) | ((w & 1) << 2);        \
      glds16(base_, (unsigned)(i * 1024 + ((l ^ (fd << 1)) << 4)), sb + w * 16384 + (hf) * 8192 + i * 1024); \
    }                                                                         \
  }
    wqkv_issue<true>(A.Wv, 0, h, w, l, sb);
    wqkv_issue<true>(A.Wv, 1, h, w, l, sb);
    asm volatile("global_load_dwordx4 %0, %1, %2" : "=v"(m0r) : "v"(((c & 7) * DM + 64 * w + 16 * g + 8 * (c >> 3)) * 2), "s"(A.mem0) : "memory");
    W_STAGE(2, 0, 9, wqkv_issue<true>(A.Wk, 0, h, w, l, sb));
    SST(4);
    W_STAGE(2, 1, 8, wqkv_issue<true>(A.Wk, 1, h, w, l, sb));
    SST(5);
    asm volatile("" : "+v"(m0r));
    *reinterpret_cast<v4u*>(smem + C_SM + w * 1024 + l * 16) = m0r;
    {
      float f2[4] = {};
      if (!ATT) {
#pragma unroll
        for (int t = 0; t < 8; ++t)
#pragma unroll
          for (int e = 0; e < 8; ++e) {
            f2[e & 1] = dot2sq(w1[t][e], f2[e & 1]);
            f2[2 + (e & 1)] = dot2sq(w2[t][e], f2[2 + (e & 1)]);
          }
      }
      const float f = wave_sum_dpp((f2[0] + f2[1]) + (f2[2] + f2[3]));
      if (l == 0) { sgf[w] = f; sgm[16] = 0.f; sgm[32 + w] = 0.f; }
    }
    v4u zt;
    v2u ze;
#define Z_LOAD()                                                                                                        \
  asm volatile("global_load_dwordx4 %0, %2, %3 sc1\n\tglobal_load_dwordx2 %1, %4, %5 sc1"                              \
               : "=&v"(zt), "=&v"(ze)                                                                                   \
               : "v"(l * 16), "s"(ws + OFF_TAG + (blockIdx.x & 7) * 1024), "v"(0), "s"(ws + OFF_TAG + 8192)              \
               : "memory")
    W_STAGE(1, 0, 8, Z_LOAD(); wqkv_issue<true>(A.Wq, 0, h, w, l, sb));
#undef Z_LOAD
    W_STAGE(1, 1, 8, wqkv_issue<true>(A.Wq, 1, h, w, l, sb));
    asm volatile("" : "+v"(zt), "+v"(ze));
    if (w == 0) {
      if (!zero_seen(zt, ze)) zero_wait(ws);
      const float f2 = (sgf[4] + sgf[5]) + (sgf[6] + sgf[7]);
      __builtin_amdgcn_raw_buffer_store_b32(__float_as_uint(f2) | 1u, MKRS(FL + FL_G, L ? 0 : 4 * 512), l * 512 + h * 4, 0, AUX_SC1);
    }
    SST(6);
    W_STAGE(0, 0, 8, WO_ISSUE(0));
    W_STAGE(0, 1, 8, WO_ISSUE(1));
    SST(7);
#undef W_STAGE
#undef W_READ
#undef WO_ISSUE
    const unsigned rb = sb + (2 * (g >> 1) + (q >> 1)) * 16384 + (4 * (g & 1) + (q & 1)) * 1024 + p * 8;
#pragma unroll
    for (int hf = 0; hf < 2; ++hf) {
      if (hf == 0) WAIT_VM(8); else WAIT_VM(0);
      RAW_BAR();
      v4u fr[8];
#pragma unroll
      for (int i = 0; i < 8; ++i) {
        const unsigned a_ = rb + hf * 8192 + (i & 1) * 65536 + ((4 * wc + ((i >> 1) ^ psw)) ^ f) * 32;
        const v2u r0 = trd(a_), r1 = trd(a_ + 2048);
        fr[i] = v4u{r0.x, r0.y, r1.x, r1.y};
      }
#pragma unroll
      for (int i = 0; i < 8; ++i) asm volatile("" : "+v"(fr[i]));
      RAW_BAR();
#pragma unroll
      for (int i = 0; i < 8; ++i) *reinterpret_cast<v4u*>(swo + w * 8192 + hf * 4096 + i * 512 + l * 8) = fr[i];
    }
  }
  WSTAMP2();
  const unsigned sb = lds_base(smem);
  const char* gK = ws + OFF_QK + L * SZ_QK + ((size_t)h * 16 + 2 * w) * 1024;
  const char* gV = ws + OFF_VF + L * SZ_O + ((size_t)h * 8 + 2 * w - 8) * 1024;
  float gw2;
  {
    const gu32* gp = (gu32*)(FL + FL_G + REP * 128) + (lane_id() & 31);
    unsigned g0, g1, g2;
    for (unsigned spins = 0;; ++spins) {
      g0 = __hip_atomic_load(gp, RLX_AGENT);
      g1 = __hip_atomic_load(gp + 32, RLX_AGENT);
      g2 = __hip_atomic_load(gp + 64, RLX_AGENT);
      if (__all(g0 != 0u && g1 != 0u && g2 != 0u)) break;
      if (spin_timeout(spins, TMO)) return;
      __builtin_amdgcn_s_sleep(4);
    }
    const float gm = wave_max_dpp(__uint_as_float(g0) * (__uint_as_float(g1) + __uint_as_float(g2)));
    asm volatile("s_nop 0\n\tv_readfirstlane_b32 %0, %1" : "=s"(gw2) : "v"(gm * 1.07f));
  }
  v8u pv;
  {
    LANE_IDS();
    const unsigned voff = g * 256 + c * 8, wp = sb + w * 16384 + l * 16;
    pv[0] = (c & 8) ? 0xEEEE : 0x4444;
    pv[1] = sb + C_SM + l * 16;
    pv[2] = ATT ? sb + C_SQ + ((c & 7) * QS + 8 * g) * 2 : voff;
    pv[3] = ATT ? sb + C_SK + ((4 * ((c >> 2) & 1) + 2 * (c >> 3) + (c & 1)) * QS + 8 * g) * 2 : wp;
    pv[4] = sb + (c < 8 ? C_SO : C_SQ) + w * 256 + ((c & 7) + 8 * (g >> 1)) * 16 + 8 * (g & 1);
    pv[5] = voff;
    pv[6] = wp;
    pv[7] = sb + C_SO + (l >> 5) * 1024 + w * 256 + (l & 31) * 8;
  }
  if (!ATT) {
    if (!wait_n<4, 1>(FQ + h * 32, 1, TMO)) return;
    const unsigned lo = lane_id() * 16, d = sb + C_KT + (2 * w - 8) * 1024;
    glds16c(gK, lo, d);
    glds16c(gK + 1024, lo, d + 1024);
    glds16c(gV, lo, d + 8192);
    glds16c(gV + 1024, lo, d + 9216);
  }
  DRAIN();
  SST(8);
  const v4u rsA = mk_srd(ws + OFF_ACC + L * SZ_ACC, SZ_ACC);
  const v4u rsF = mk_srd(FQ, 64 * 32 * 32 * 4);
  const v4u rsMK = mk_srd(ws + OFF_MEMK + L * SZ_MEM, SZ_MEM);
  const v4u rsMV = mk_srd(ws + OFF_MEMV + L * SZ_MEM, SZ_MEM);
  const int wr = (w + RR) & 7;
  const char* pAB = ws + OFF_ACC + L * SZ_ACC + (wc - wr) * 4096;
  const char* pGK = gK + NH * 16 * 1024;
  const char* pGV = gV + NH * 8 * 1024;
  const unsigned* pFK = FL;
  unsigned sc[8];
  sc[0] = (32 + h) * 128;
  sc[1] = (h * 4 + w - 4) * 512;
  sc[2] = (h * DH + 16 * (w - 4)) * MM * 2;
  sc[3] = NSEG - 1;
  sc[4] = __float_as_uint(gw2);
  sc[5] = w;
  sc[6] = sb;
  sc[7] = h == 0 ? 0xd4000000u - 2u - (1u << 29) : 0xd4000000u;
  const char* dbg = ws;
  const unsigned tg = (now10() / GR) * GR;
  const v4u rsT = mk_srd(FL + FL_TK, 2048);
#define CHAIN_OPS                                                                                                          \
  ::"{v[64:71]}"(w0[0]), "{v[72:79]}"(w0[1]), "{v[80:87]}"(w0[2]), "{v[88:95]}"(w0[3]), "{v[96:103]}"(w0[4]),              \
      "{v[104:111]}"(w0[5]), "{v[112:119]}"(w0[6]), "{v[120:127]}"(w0[7]), "{v[128:135]}"(w1[0]), "{v[136:143]}"(w1[1]),    \
      "{v[144:151]}"(w1[2]), "{v[152:159]}"(w1[3]), "{v[160:167]}"(w1[4]), "{v[168:175]}"(w1[5]), "{v[176:183]}"(w1[6]),    \
      "{v[184:191]}"(w1[7]), "{v[192:199]}"(w2[0]), "{v[200:207]}"(w2[1]), "{v[208:215]}"(w2[2]), "{v[216:223]}"(w2[3]),    \
      "{v[224:231]}"(w2[4]), "{v[232:239]}"(w2[5]), "{v[240:247]}"(w2[6]), "{v[248:255]}"(w2[7]), "{v[0:7]}"(pv),          \
      "{s[40:43]}"(rsA), "{s[44:47]}"(rsF), "{s[48:51]}"(rsMK), "{s[52:55]}"(rsMV), "{s[56:57]}"(pAB), "{s60}"(wr * 4096), "{s61}"(h * 1024 - wr * 4096), \
      "{s[62:63]}"(pGK), "{s[64:65]}"(pGV), "{s[66:67]}"(pFK), "{s68}"(sc[0]), "{s69}"(sc[1]), "{s70}"(sc[2]),    \
      "{s71}"(sc[3]), "{s72}"(sc[4]), "{s73}"(sc[5]), "{s74}"(sc[6]), "{s75}"(sc[7]), "{s[36:37]}"(dbg),                   \
      "{s34}"(tg), "{s35}"(psw * 1536), [gp] "n"(GP), [gr] "n"(GR), [gm] "n"(GM), \
      "{s[16:19]}"(rsT), "{s20}"(0), "{s22}"(sb + C_X + 128 + 8 * (w & 3)), "{s24}"(L * 1024 + (ATT ? 0 : (h & 3) * 256)), [cd] "n"(CD), \
      "{s25}"(ATT ? APA - (APN + 1) * APP + L * (APB - APA + (2 * APN + 1) * APP) : 1032 - L * 1024 + h * 4096), [ka] "n"(APL), [an] "n"(APN), [d0] "n"(16 * CD0), [rq] "n"(RQM), [pp] "n"(APP) \
      : "memory", "vcc", "scc", "s99", "v63"
  if (ATT) asm volatile("s_mov_b64 exec, -1\ns_nop 4\n" CHAIN_ASM_ATT CHAIN_OPS);
  else asm volatile("s_mov_b64 exec, -1\ns_nop 4\n" CHAIN_ASM_VO CHAIN_OPS);
#undef CHAIN_OPS
#undef LANE_IDS
}

__device__ __forceinline__ void role_chain(const MegaArgs& A, char* smem, const int L, const int h, const int rep) {
  if (__builtin_amdgcn_readfirstlane(threadIdx.x >> 6) < 4) role_chain_t<true>(A, smem, L, h, rep);
  else role_chain_t<false>(A, smem, L, h, rep);
}

constexpr int AW_TK = 0, AW_SO = 98304, AW_M = AW_SO + 32768, AW_CNT = AW_M + 8192;
typedef __attribute__((address_space(3))) unsigned lu32;
__device__ __forceinline__ unsigned umin3(unsigned a, unsigned b, unsigned c) {
  return __builtin_elementwise_min(__builtin_elementwise_min(a, b), c);
}
__device__ __forceinline__ bool all_set(const unsigned r) {
  unsigned long long m;
  asm volatile("v_cmp_eq_u32 %0, 0, %1" : "=s"(m) : "v"(r) : "memory");
  return m == 0ull;
}
__device__ __forceinline__ unsigned long long zero_mask(const unsigned r) {
  unsigned long long m;
  asm volatile("v_cmp_eq_u32 %0, 0, %1" : "=s"(m) : "v"(r) : "memory");
  return m;
}
__device__ __forceinline__ int job_seg(const int n) {
  const int m = n - AWD;
  const int r = (m & 1) ? 64 + (m >> 1) : AWD + (m >> 1);
  return __builtin_amdgcn_readfirstlane(n > 127 ? 128 : (n < AWD || n >= 128 - AWD) ? n : r);
}
template <bool AT>
__device__ __forceinline__ void role_aw_t(const MegaArgs& A, char* smem, const int mode, const int a) {
  __builtin_amdgcn_s_waitcnt(0x0F70);
  const int w = __builtin_amdgcn_readfirstlane(threadIdx.x >> 6);
  const int l = threadIdx.x & 63, c = l & 15, g = l >> 4;
  const int qt = w & 1, hh = (w >> 1) & 1, h = 2 * a + hh;
  char* ws = A.ws;
  unsigned* FL = (unsigned*)(ws + OFF_FL);
  unsigned* TMO = FL + FL_TMO;
  const unsigned sb = lds_base(smem);
  constexpr int NP = AT ? 1 : 3;
  const int cp0 = AT ? w : 3 * w - 8;
  v4u wo[2 * NP][8];
  lu32* cnt = (lu32*)(unsigned long)(sb + AW_CNT);
  if (threadIdx.x < 16) cnt[threadIdx.x] = 0u;
  const __amdgpu_buffer_rsrc_t rsW = MKRS(ws + OFF_PW, SZ_PW);
  const unsigned* FQ = FL + FL_FQ + h * 32;
  const v4u rsF = mk_srd(FQ, (128 * 32 - h) * 128);
  const char* gQ = ws + OFF_QK + ((size_t)h * 16 + 8 * qt) * 1024;
  const char* gV = ws + OFF_VF + ((size_t)h * 8 + 4 * qt) * 1024;
  const char* gM = ws + (qt ? OFF_MEMV : OFF_MEMK) + h * 2048;
  const int NJ = (130 - mode) / 3;
#define AWS(gs) (64 + a + ((gs) >> 6) * 16)
  const int mo = AW_M + hh * 4096;
  const unsigned tq0 = __builtin_amdgcn_readfirstlane(sb + AW_TK + (hh * 24 + 8 * qt) * 1024);
  const unsigned tv0 = __builtin_amdgcn_readfirstlane(sb + AW_TK + (hh * 24 + 16 + 4 * qt) * 1024);
  const float scale = 0.08838834764831845f;
  volatile lu32* cM = cnt + 2 + hh;
#define DMA4(x) "global_load_lds_dwordx4 %1, " x " sc1\n\tglobal_load_lds_dwordx4 %1, " x " offset:1024 sc1\n\t"       \
                "global_load_lds_dwordx4 %1, " x " offset:2048 sc1\n\tglobal_load_lds_dwordx4 %1, " x " offset:3072 sc1\n\t"
#define T_DMA(jj, gg)                                                                                                   \
  {                                                                                                                     \
    const unsigned o_ = ((jj) & 1) ? 49152u : 0u;                                                                       \
    const char* q_ = gQ + (size_t)(gg) * (NH * 16 * 1024);                                                              \
    unsigned keep_;                                                                                                     \
    asm volatile("s_nop 4\n\ts_mov_b32 %0, m0\n\ts_mov_b32 m0, %5\n\ts_nop 0\n\t" DMA4("%2")                           \
                 "s_mov_b32 m0, %6\n\ts_nop 0\n\t" DMA4("%3") "s_mov_b32 m0, %7\n\ts_nop 0\n\t" DMA4("%4") "s_mov_b32 m0, %0" \
                 : "=&s"(keep_)                                                                                         \
                 : "v"(l * 16), "s"(q_), "s"(q_ + 4096), "s"(gV + (size_t)(gg) * (NH * 8 * 1024)), "s"(tq0 + o_),        \
                   "s"(tq0 + o_ + 4096u), "s"(tv0 + o_)                                                                 \
                 : "memory");                                                                                           \
  }
#define T_LAND(jj) { if (l == 0) *(volatile lu32*)(cnt + w) = (jj) + 1; }
#define M_DMA(gg)                                                                                                       \
  {                                                                                                                     \
    unsigned keep_;                                                                                                     \
    asm volatile("s_nop 4\n\ts_mov_b32 %0, m0\n\ts_mov_b32 m0, %3\n\ts_nop 0\n\tglobal_load_lds_dwordx4 %1, %2 sc1\n\t"     \
                 "global_load_lds_dwordx4 %1, %2 offset:1024 sc1\n\ts_mov_b32 m0, %0"                                    \
                 : "=&s"(keep_)                                                                                         \
                 : "v"(l * 16), "s"(gM + (size_t)(gg) * (NH * 2048)), "s"(sb + mo + qt * 2048)                           \
                 : "memory");                                                                                           \
  }
#define M_TEST()                                                                                                        \
  {                                                                                                                     \
    const v4u t_ = *reinterpret_cast<const v4u*>(smem + mo + qt * 2048 + l * 16);                                        \
    const v4u u_ = *reinterpret_cast<const v4u*>(smem + mo + qt * 2048 + 1024 + l * 16);                                 \
    ok = __all(umin3(umin3(t_.x, t_.y, t_.z), umin3(u_.x, u_.y, u_.z), umin3(t_.w, u_.w, 1u)) != 0u);                   \
    if (ok) asm volatile("s_mov_b64 exec, 1\n\tds_add_u32 %0, %1\n\ts_mov_b64 exec, -1" ::"v"(sb + AW_CNT + 8 + 4 * hh), "v"(1u) : "memory"); \
  }
#define F_LOAD(r, gg) \
  asm volatile("s_nop 4\n\tbuffer_load_dword %0, %1, %2, %3 offen sc1" : "=v"(r) : "v"(0), "s"(rsF), "s"((gg) * 4096) : "memory")
#define F_TEST(r) all_set(r)
#define W_FLAG(jj, gg)                                                                                                  \
  {                                                                                                                     \
    unsigned old_;                                                                                                      \
    asm volatile("s_mov_b64 exec, 1\n\tds_add_rtn_u32 %0, %1, %2\n\ts_mov_b64 exec, -1\n\ts_waitcnt lgkmcnt(0)"           \
                 : "=&v"(old_) : "v"(sb + AW_CNT + ((jj) & 1) * 36), "v"(1u) : "memory");                               \
    if ((__builtin_amdgcn_readfirstlane(old_) & 7u) == 7u) {                                                            \
      TSR(AWS(gg), 4, (gg) & 63, threadIdx.x & ~63u);                                                                   \
      if (l < 4) set_flag(FL + FL_FW + (((gg) * 4 + l) * 4 + (a >> 2)) * 32 + (a & 3));                              \
    }                                                                                                                   \
    pend = false;                                                                                                       \
  }
#define LDS_U(p) ((unsigned)__builtin_amdgcn_readfirstlane(*(p)))
#define TOK_MIN() __builtin_elementwise_min(LDS_U((volatile lu32*)(cnt + 4 + 2 * hh)), LDS_U((volatile lu32*)(cnt + 5 + 2 * hh)))
  int gp = 0, gs = job_seg(mode), g1 = job_seg(mode + 3);
  {
    const int q = (l >> 2) & 3, p = l & 3, f = q | ((g & 1) << 2);
    const unsigned ra = sb + (4 * g + q) * 256 + (p & 1) * 16;
    unsigned n0 = 0u;
    bool got = false;
#pragma unroll
    for (int st = 0; st < 4; ++st) {
      if (st == 3 && got) WAIT_VM(12); else if (st == 3) WAIT_VM(0); else WAIT_VM(8);
      RAW_BAR();
#pragma unroll
      for (int k2 = 0; k2 < 2; ++k2) {
        const int ks = 4 * (st >> 1) + 2 * k2 + (st & 1);
#pragma unroll
        for (int ct = 0; ct < 2 * NP; ++ct) {
          const int cp = cp0 + (ct >> 1);
          const unsigned a_ = ra + (st & 1) * 65536 + (cp >> 2) * 16384 + k2 * 8192 + ((2 * (cp & 3) + (p >> 1)) ^ f) * 32 + (ct & 1) * 8;
          const v2u lo = trd(a_), hi = trd(a_ + 4096);
          wo[ct][ks] = v4u{lo.x, lo.y, hi.x, hi.y};
        }
#pragma unroll
        for (int ct = 0; ct < 2 * NP; ++ct) asm volatile("" : "+v"(wo[ct][ks]));
      }
      RAW_BAR();
      if (!AT && st == 1) F_LOAD(n0, gs);
      if (st < 2) wo_issue(A.Wo, a, st + 2, sb);
      if (!AT && st == 2 && F_TEST(n0)) {
        T_DMA(0, gs);
        got = true;
      }
    }
    if (!AT) {
      if (!got) {
        (void)wait_n<SLP_A, 1>(FQ + gs * 1024, 1, TMO);
        T_DMA(0, gs);
      }
      WAIT_VM(0);
      T_LAND(0);
    }
  }
  __builtin_amdgcn_s_waitcnt(0x0F70);
  bool pend = false, asked = false;
  unsigned tb1 = now10() - 130 * GPER, tb2 = tb1;
  FSD;
  for (int s = 0; s < NJ; ++s) {
    char* so = smem + AW_SO + (s & 1) * 16384 + l * 16;
    bool got = g1 == 128, land = got, seen = false;
    if (AT) {
      if (TOK_MIN() < (unsigned)(s + 1)) {
        if (pend) {
          WAIT_VM(0);
          W_FLAG(s - 1, gp);
        }
        for (unsigned spins = 0; TOK_MIN() < (unsigned)(s + 1) && spins < 64u * SPIN_CAP; ++spins) __builtin_amdgcn_s_sleep(1);
      }
      asm volatile("" ::: "memory");
      TSR(AWS(gs), 0, gs & 63, 0);
      FS(0);
      const char* tk = smem + AW_TK + ((s & 1) * 2 + hh) * 24 * 1024 + l * 16;
#define TKF(i) (*reinterpret_cast<const bf16x8*>(tk + (i) * 1024))
      bf16x8 q[4], vt[8];
      float x[12], mx;
      bool ok = false;
      {
        f32x4 s0 = {}, s1 = {};
        bf16x8 kd[4], k0[4];
#pragma unroll
        for (int ks = 0; ks < 4; ++ks) {
          q[ks] = TKF(4 * qt + ks);
          kd[ks] = TKF(8 + 4 * qt + ks);
          k0[ks] = TKF(8 + ks);
        }
#pragma unroll
        for (int i = 0; i < 8; ++i) vt[i] = TKF(16 + i);
        __builtin_amdgcn_sched_barrier(0);
#pragma unroll
        for (int ks = 0; ks < 4; ++ks) {
          s0 = MFMA16(kd[ks], q[ks], s0);
          s1 = MFMA16(k0[ks], q[ks], s1);
        }
        scale_pair(s0[0], s0[1], scale, x[0], x[1]);
        scale_pair(s0[2], s0[3], scale, x[2], x[3]);
        scale_pair(s1[0], s1[1], scale, x[4], x[5]);
        scale_pair(s1[2], s1[3], scale, x[6], x[7]);
#pragma unroll
        for (int i = 0; i < 4; ++i) {
          if (4 * g + i > c) x[i] = -3e38f;
          if (!qt) x[4 + i] = -3e38f;
        }
        mx = fmaxf(fmaxf(fmaxf(x[0], x[1]), fmaxf(x[2], x[3])), fmaxf(fmaxf(x[4], x[5]), fmaxf(x[6], x[7])));
        asm volatile("" : "+v"(mx), "+v"(vt[0]), "+v"(vt[1]), "+v"(vt[2]), "+v"(vt[3]), "+v"(vt[4]), "+v"(vt[5]), "+v"(vt[6]), "+v"(vt[7]));
      }
#undef TKF
      FS(1);
      if (asked) {
        WAIT_VM(1);
        M_TEST();
      }
      FS(2);
      if (!ok) {
        if (pend) {
          WAIT_VM(0);
          W_FLAG(s - 1, gp);
        }
        unsigned tb = gs < 64 ? tb1 : tb2, spins = 0;
        gate(tb + gs * GPER);
        for (; !ok; ++spins) {
          if (spin_timeout(spins, TMO)) break;
          if (spins) __builtin_amdgcn_s_sleep(SLP_M);
          M_DMA(gs);
          WAIT_VM(0);
          M_TEST();
        }
        tb = spins > 1 ? now10() - gs * GPER - GMA : tb - GSTEP;
        if (gs < 64) tb1 = tb; else tb2 = tb;
      }
      FS(3);
      __builtin_amdgcn_s_setprio(PRI_A);
      asked = false;
      for (unsigned spins = 0; LDS_U(cM) < 2u * (s + 1) && spins < 64u * SPIN_CAP; ++spins) __builtin_amdgcn_s_sleep(1);
      v4u km[4];
      v2u vm[8];
#pragma unroll
      for (int ks = 0; ks < 4; ++ks) km[ks] = *reinterpret_cast<const v4u*>(smem + mo + (ks * 32 + g * 8 + (c & 7)) * 16);
#pragma unroll
      for (int i = 0; i < 8; ++i) vm[i] = *reinterpret_cast<const v2u*>(smem + mo + 2048 + ((16 * i + c) * MM + 4 * (g & 1)) * 2);
      TSR(AWS(gs), 1, gs & 63, 0);
      FS(4);
      f32x4 m0 = {};
#pragma unroll
      for (int ks = 0; ks < 4; ++ks) m0 = MFMA16(BF8(km[ks]), q[ks], m0);
      scale_pair(m0[0], m0[1], scale, x[8], x[9]);
      scale_pair(m0[2], m0[3], scale, x[10], x[11]);
#pragma unroll
      for (int i = 0; i < 4; ++i) {
        if (g >= 2) x[8 + i] = -3e38f;
        mx = fmaxf(mx, x[8 + i]);
      }
      mx = xmax32(xmax16(mx));
      float sum = 0.f;
#pragma unroll
      for (int i = 0; i < 12; ++i) {
        x[i] = __expf(x[i] - mx);
        sum += x[i];
      }
      sum = xsum32(xsum16(sum));
      const float inv = 1.0f / sum;
      const v2u pa = {pk2(x[0] * inv, x[1] * inv), pk2(x[2] * inv, x[3] * inv)};
      const v2u pb = {pk2(x[4] * inv, x[5] * inv), pk2(x[6] * inv, x[7] * inv)};
      const v4u p0 = qt ? v4u{pb.x, pb.y, pa.x, pa.y} : v4u{pa.x, pa.y, pb.x, pb.y};
      const v2u p1 = {pk2(x[8] * inv, x[9] * inv), pk2(x[10] * inv, x[11] * inv)};
      FS(5);
#pragma unroll
      for (int k = 0; k < 4; ++k) {
        v4u of;
#pragma unroll
        for (int hf = 0; hf < 2; ++hf) {
          f32x4 ao = {};
          ao = MFMA16(vt[2 * k + hf], BF8(p0), ao);
          ao = MFMA16K(vm[2 * k + hf], p1, ao);
          of[2 * hf] = pk2(ao[0], ao[1]);
          of[2 * hf + 1] = pk2(ao[2], ao[3]);
        }
        *reinterpret_cast<v4u*>(so + ((hh * 2 + qt) * 4 + k) * 1024) = of;
      }
      FS(6);
      seen = g1 >= 64 && g1 < 128 && TOK_MIN() >= (unsigned)(s + 2);
      if (pend) {
        WAIT_VM(0);
        W_FLAG(s - 1, gp);
      }
    } else {
      FS(0);
      unsigned n;
      F_LOAD(n, g1);
      if (pend) {
        WAIT_VM(1);
        W_FLAG(s - 1, gp);
      }
      WAIT_VM(0);
      FS(1);
      if (F_TEST(n)) {
        T_DMA(s + 1, g1);
        got = true;
        if (LDS_U(cM) < 2u * (s + 1)) {
          WAIT_VM(0);
          T_LAND(s + 1);
          land = true;
        }
      }
    }
    FS(7);
    RAW_BAR();
    FS(8);
    __builtin_amdgcn_s_setprio(AT ? PRI_WA : PRI_WB);
    TSR(AWS(gs), 2, gs & 63, 256);
    const int voff = (c >> 1) * 2048 + (c & 1) * 64 + g * 16, soff = (gs * 16 + a) * 32768 + cp0 * 128;
#define OFRAG(i) (*reinterpret_cast<const bf16x8*>(so + (((((i) & 7) >> 2) * 2 + ((i) >> 3)) * 4 + ((i) & 3)) * 1024))
    constexpr int NO = AT ? 16 : 3;
    bf16x8 o[NO];
#pragma unroll
    for (int i = 0; i < (AT ? 16 : 2); ++i) o[i] = OFRAG(i);
    unsigned pr;
    const bool look = seen && g1 >= 128 - AWD;
    if (AT) {
      asm volatile("s_cmp_eq_u32 %3, 0\n\ts_cbranch_scc1 1f\n\ts_nop 4\n\tglobal_load_dword %0, %1, %2 sc1\n1:"
                   : "=v"(pr) : "v"(2044), "s"(gM + (size_t)g1 * (NH * 2048)), "s"(__builtin_amdgcn_readfirstlane((int)look)) : "memory", "scc");
    } else {
      F_LOAD(pr, got ? 128 : g1);
    }
    __builtin_amdgcn_sched_barrier(0);
    FS(9);
#pragma unroll
    for (int rt = 0; rt < 2; ++rt) {
      f32x4 acc[2 * NP] = {};
#pragma unroll
      for (int kk = 0; kk < 8; ++kk) {
        const int i = 8 * rt + kk;
        if (!AT && i + 2 < 16) o[(i + 2) % NO] = OFRAG(i + 2);
#pragma unroll
        for (int ct = 0; ct < 2 * NP; ++ct) acc[ct] = MFMA16(BF8(wo[ct][kk]), o[i % NO], acc[ct]);
        if (!AT) __builtin_amdgcn_sched_barrier(0);
      }
      if (rt == 0) FS(10); else FS(12);
      if (AT && rt == 1 && seen) {
        if (look) {
          WAIT_VM(1);
          seen = F_TEST(pr);
        }
        if (seen) {
          M_DMA(g1);
          asked = true;
        }
      }
      if (!AT && rt == 0) {
        WAIT_VM(0);
        if (got && !land) {
          T_LAND(s + 1);
          land = true;
        }
        if (F_TEST(pr)) {
          T_DMA(s + 1, g1);
          got = true;
        }
      }
      if (!AT && rt == 1 && got && !land) {
        WAIT_VM(3);
        T_LAND(s + 1);
      }
#pragma unroll
      for (int u = 0; u < NP; ++u)
        ST128((v4u{pk2(acc[2 * u][0], acc[2 * u][1]), pk2(acc[2 * u][2], acc[2 * u][3]), pk2(acc[2 * u + 1][0], acc[2 * u + 1][1]),
                   pk2(acc[2 * u + 1][2], acc[2 * u + 1][3])}),
              rsW, voff + u * 128, soff + rt * 16384);
      if (rt == 0) FS(11); else FS(13);
    }
    __builtin_amdgcn_s_setprio(AT ? PRI_0 : 0);
    pend = true;
    FD(s);
    TSR(AWS(gs), 3, gs & 63, 256);
    TSR(AWS(gs), 5, gs & 63, 0);
    if (!AT && !got) {
      WAIT_VM(0);
      W_FLAG(s, gs);
      (void)wait_n<SLP_A, 1>(FQ + g1 * 1024, 1, TMO);
      T_DMA(s + 1, g1);
      WAIT_VM(0);
      T_LAND(s + 1);
    }
    gp = gs;
    gs = g1;
    g1 = job_seg(3 * s + 6 + mode);
  }
  WAIT_VM(0);
  W_FLAG(NJ - 1, gp);
#undef AWS
#undef OFRAG
#undef DMA4
#undef T_DMA
#undef T_LAND
#undef M_DMA
#undef M_TEST
#undef F_LOAD
#undef F_TEST
#undef W_FLAG
#undef LDS_U
#undef TOK_MIN
}
__device__ __forceinline__ void role_aw(const MegaArgs& A, char* smem, const int L, const int a) {
  if (__builtin_amdgcn_readfirstlane(threadIdx.x >> 6) < 4) role_aw_t<true>(A, smem, L, a);
  else role_aw_t<false>(A, smem, L, a);
}

__device__ __forceinline__ void role_red(const MegaArgs& A, char* smem, const int idx) {
  const int w = __builtin_amdgcn_readfirstlane(threadIdx.x >> 6);
  const int l = threadIdx.x & 63;
  const int gr = w >> 1, r = idx * 2 + (w & 1);
  char* ws = A.ws;
  unsigned* FL = (unsigned*)(ws + OFF_FL);
  unsigned* TMO = FL + FL_TMO;
  const __amdgpu_buffer_rsrc_t rsW = MKRS(ws + OFF_PW, SZ_PW);
  const __amdgpu_buffer_rsrc_t rsX = MKRS(ws + OFF_XA, OFF_X3 - OFF_XA + SZ_X);
  volatile unsigned* sw = reinterpret_cast<volatile unsigned*>(smem) + (w >> 1) * 32;
  if (threadIdx.x < 128) reinterpret_cast<volatile unsigned*>(smem)[threadIdx.x] = 0u;
  __syncthreads();
  unsigned tb1 = now10() - 130 * GPER, tb2 = tb1;
  const int rn1 = idx < 8 ? 96 + idx : 224 + idx;
  (void)rn1;
  if (w >= 6) return;
  if (w < 4) {
    const __amdgpu_buffer_rsrc_t rz = MKRS(ws + OFF_X2, SZ_X);
    for (int s = gr; s < NSEG; s += 2) ST128((v4u{0u, 0u, 0u, 0u}), rz, l * 16, (s * SEG + r) * DM * 2);
    DRAIN();
    if (l < 4) set_flag(FL + FL_FZ + (2 * l + (idx >> 3)) * 32 + (idx & 7) * 4 + w);
    if (l == 0) __hip_atomic_fetch_add((lu32*)(unsigned long)(lds_base(smem) + 480), 1u, __ATOMIC_RELAXED, __HIP_MEMORY_SCOPE_WORKGROUP);
  }
  for (unsigned spins = 0; reinterpret_cast<volatile unsigned*>(smem)[120] < 4u && spins < 64u * SPIN_CAP; ++spins) __builtin_amdgcn_s_sleep(2);
  for (int n = gr; n < 128; n += 3) {
    const int gs = job_seg(n), L = gs >> 6, s = gs & 63;
    const unsigned* fw = FL + FL_FW + (gs * 4 + REP) * 4 * 32;
    unsigned tb = L ? tb2 : tb1;
    tb = grp_wait<SLP_R, 4>((w & 1) == 0, sw, n + 1, fw, 16, TMO, tb + gs * GPER) ? now10() - gs * GPER - GMR : tb - GSTEP;
    if (n >= 125 - AWD) tb = now10() + GATE_RT - (n + 3) * GPER;
    if (L || n >= 125 - AWD) tb2 = tb; else tb1 = tb;
#define RTS(k, t) TSR(L ? A.rn : rn1, k, s, (w & 6) * 64 + (t))
    RTS(0, 0);
    RTS(4, 64);
    v4u p[16];
    const v4u xr = LD128(rsX, l * 512, L * (int)(OFF_X3 - OFF_XA) + s * 32768 + r * 16);
#pragma unroll
    for (int k = 0; k < 16; ++k)
      p[k] = LD128(rsW, (l >> 2) * 128 + (l & 3) * 16, (gs * 16 + k) * 32768 + (r >> 1) * 2048 + (r & 1) * 64);
    v4u xo;
    const unsigned nz = L ? 0u : 0x8000u;
#pragma unroll
    for (int e = 0; e < 4; ++e) {
      f32x2_t t[4];
#pragma unroll
      for (int k = 0; k < 4; ++k)
        t[k] = (f32x2_t{lo16(p[4 * k][e]), hi16(p[4 * k][e])} + f32x2_t{lo16(p[4 * k + 1][e]), hi16(p[4 * k + 1][e])}) +
               (f32x2_t{lo16(p[4 * k + 2][e]), hi16(p[4 * k + 2][e])} + f32x2_t{lo16(p[4 * k + 3][e]), hi16(p[4 * k + 3][e])});
      const f32x2_t u = (t[0] + t[1]) + (t[2] + t[3]);
      const unsigned a0 = pk2(u.x, u.y);
      xo[e] = pk2(lo16(xr[e]) + lo16(a0), hi16(xr[e]) + hi16(a0));
      xo[e] = xo[e] ? xo[e] : nz;
    }
    ST128(xo, MKRS(L ? (char*)A.out : ws + OFF_X2, SZ_X), l * 16, (s * SEG + r) * DM * 2);
    RTS(1, 0);
    RTS(5, 64);
    if (L == 0) {
      const unsigned st = row_stats(xo);
      if (l < 4) __hip_atomic_store((gu32*)(FL + FL_FR + ((s * 4 + l) * 4 + (r >> 3)) * 32 + (r & 7)), st, RLX_AGENT);
    }
    RTS(6, 64);
    RTS(3, 0);
#undef RTS
  }
  if (idx == 0 && threadIdx.x == 128) {
    gu64* en = (gu64*)(ws + OFF_TAG + 8192);
    __hip_atomic_store(en, __hip_atomic_load(en, RLX_AGENT) + TAG_STEP, RLX_AGENT);
  }
}

constexpr int CUN = 2144;
__device__ __forceinline__ bf16x8 conv_bfrag(const unsigned* up, const unsigned sh) {
  const unsigned d0 = up[0], d1 = up[1], d2 = up[2], d3 = up[3], d4 = up[4];
  const v4u r = {__builtin_amdgcn_alignbit(d1, d0, sh), __builtin_amdgcn_alignbit(d2, d1, sh),
                 __builtin_amdgcn_alignbit(d3, d2, sh), __builtin_amdgcn_alignbit(d4, d3, sh)};
  return BF8(r);
}
__device__ __forceinline__ void role_conv(const MegaArgs& A, char* smem, const int cv) {
  __builtin_amdgcn_s_waitcnt(0x0F70);
  u16* Kr = reinterpret_cast<u16*>(smem);
  u16* Uc = Kr + 8 * 2 * CKN;
  u16* Xr = Uc + 8 * CUN;
  u16* To = Uc + 8 * 2 * CUN;
  u16* Su = To + 32 * 8;
  u16* Sx = Su + 32 * 8;
  const int tid = threadIdx.x;
  const int w = __builtin_amdgcn_readfirstlane(tid >> 6);
  const int l = tid & 63, c = l & 15, g = l >> 4;
  const int d = 8 * cv + w;
  char* ws = A.ws;
  unsigned* FL = (unsigned*)(ws + OFF_FL);
  unsigned* TMO = FL + FL_TMO;
  u16* kr = Kr + w * 2 * CKN;
  float* prm = reinterpret_cast<float*>(kr);
  v4u tx[4];
  const v2u en = LD64(MKRS(ws + OFF_TAG, 8192 + 8), 0, 8192);
  {
    const unsigned pa = *reinterpret_cast<const unsigned*>(A.logAre + d * NST + 2 * l);
    const unsigned pb = *reinterpret_cast<const unsigned*>(A.Aim + d * NST + 2 * l);
    const unsigned pc = *reinterpret_cast<const unsigned*>(A.Cre + d * NST + 2 * l);
    const unsigned pd = *reinterpret_cast<const unsigned*>(A.Cim + d * NST + 2 * l);
    const float dt = bfr(expf(bf2f(A.logdt[d])));
    if (w < 2) tx[0] = *reinterpret_cast<const v4u*>(A.x0 + (size_t)tid * DM + 8 * cv);
    if (l < 16) reinterpret_cast<unsigned*>(Uc + w * CUN)[l] = 0u;
    if (l < 32) reinterpret_cast<unsigned*>(Uc + w * CUN + 32 + L_SEQ)[l] = 0u;
    if (l < 32) reinterpret_cast<unsigned*>(Uc + w * CUN + 32 + 128)[l] = 0u;
    if (l < 50) {
      reinterpret_cast<v4u*>(kr + 1808)[l] = v4u{0u, 0u, 0u, 0u};
      reinterpret_cast<v4u*>(kr + CKN + 1808)[l] = v4u{0u, 0u, 0u, 0u};
    }
#pragma unroll
    for (int e = 0; e < 2; ++e) {
      const float ar = -bfr(expf(e ? hi16(pa) : lo16(pa))) * dt, ai = (e ? hi16(pb) : lo16(pb)) * dt;
      const float ex = expf(ar);
      float sn, cs;
      sincosf(ai, &sn, &cs);
      prm[2 * l + e] = ar;
      prm[NST + 2 * l + e] = ai;
      prm[2 * NST + 2 * l + e] = (e ? hi16(pc) : lo16(pc)) * dt;
      prm[3 * NST + 2 * l + e] = (e ? hi16(pd) : lo16(pd)) * dt;
      prm[4 * NST + 2 * l + e] = ex * cs;
      prm[5 * NST + 2 * l + e] = ex * sn;
    }
  }
  const float dsk = bf2f(A.Dskip[d]);
  const u16* ka = kr + (c >> 3) * CKN + 2080 - 16 * (c & 7) + 8 * g;
  const unsigned ush = (c & 1) * 16;
#define KFRAG(D0) (*reinterpret_cast<const bf16x8*>(ka - (D0)))
  f32x4 acc[16], far[5];
  v4u xf[2];
  const unsigned* ub1 = reinterpret_cast<const unsigned*>(Uc + w * CUN + (c & ~1) + 8 * g + 32);
  const __amdgpu_buffer_rsrc_t rz1 = MKRS(ws + OFF_XA, SZ_X);
  const int t0 = 16 * (4 * (g & 1) + (g >> 1)) + c;
  {
    const u16* uc = Uc + w * CUN;
    const unsigned* ub = reinterpret_cast<const unsigned*>(uc + (c & ~1) + 8 * g + 32);
    const __amdgpu_buffer_rsrc_t rz = MKRS(ws + OFF_XA, SZ_X);
    const __amdgpu_buffer_rsrc_t rst = MKRS(ws + OFF_GT, L_SEQ * 4);
#define TILE_OUT(T)                                                                            \
  {                                                                                            \
    const float u0 = bf2f(uc[32 + 128 * (T) + t0]), u1 = bf2f(uc[64 + 128 * (T) + t0]);        \
    u16* xp = Xr + (128 * (T) + t0) * 8 + w;                                                   \
    const unsigned yy = pk2(y0, y1), uu = pk2(u0 * dsk, u1 * dsk);                             \
    const unsigned aa = pk2(lo16(yy) + lo16(uu), hi16(yy) + hi16(uu));                         \
    const unsigned oo = pk2(bf2f(xp[0]) + lo16(aa), bf2f(xp[256]) + hi16(aa));                 \
    xp[0] = (u16)oo;                                                                           \
    xp[256] = (u16)(oo >> 16);                                                                 \
  }
#define CHUNK_ST(ck)                                                                           \
  {                                                                                            \
    const v4u o = *reinterpret_cast<const v4u*>(Xr + (64 * (ck) + l) * 8);                     \
    ST128(o, rz, (l >> 5) * 32768 + (l & 31) * 16, (2 * (ck) * 64 + cv) * 512);                \
  }
#define U_ROW(row) U_ROW2(row, __builtin_amdgcn_raw_buffer_load_b32(rst, (row) * 4, 0, AUX_SC1))
#define U_ROW2(row, stv)                                                                       \
  {                                                                                            \
    const unsigned st_ = (stv);                                                                \
    const v4u xr_ = *reinterpret_cast<const v4u*>(Xr + (row) * 8);                             \
    const float mb_ = lo16(st_), ri_ = 1.0f / hi16(st_);                                       \
    _Pragma("unroll") for (int e = 0; e < 4; ++e) {                                            \
      const unsigned t_ = pk2(lo16(xr_[e]) - mb_, hi16(xr_[e]) - mb_);                         \
      const unsigned q_ = pk2(lo16(t_) * ri_, hi16(t_) * ri_);                                 \
      Uc[(2 * e) * CUN + 32 + (row)] = (u16)q_;                                                \
      Uc[(2 * e + 1) * CUN + 32 + (row)] = (u16)(q_ >> 16);                                    \
    }                                                                                          \
  }
    const unsigned* FS = FL + FL_FS + REP * 32 * 32;
    const int gi = 4 * (g * 8 + (l & 7));
    const int tl = 32 * ((l >> 3) & 1) + 8 * g + (l & 7);
    unsigned khl = 0u;
#pragma unroll 1
    for (int ps = 0; ps < 4; ++ps) {
      float v[32];
      {
        const f32x4 ar = *reinterpret_cast<const f32x4*>(prm + gi), ai = *reinterpret_cast<const f32x4*>(prm + NST + gi);
        const f32x4 cr = *reinterpret_cast<const f32x4*>(prm + 2 * NST + gi), ci = *reinterpret_cast<const f32x4*>(prm + 3 * NST + gi);
        const f32x4 wr = *reinterpret_cast<const f32x4*>(prm + 4 * NST + gi), wi = *reinterpret_cast<const f32x4*>(prm + 5 * NST + gi);
        const float p0 = (float)(64 * ps + 32 * ((l >> 3) & 1));
        float yr0[4], yi0[4];
#pragma unroll
        for (int k = 0; k < 4; ++k) {
          const float e = __expf(ar[k] * p0);
          float sn, cs;
          __sincosf(ai[k] * p0, &sn, &cs);
          const float zr = e * cs, zi = e * sn;
          yr0[k] = cr[k] * zr - ci[k] * zi;
          yi0[k] = cr[k] * zi + ci[k] * zr;
        }
        f32x2_t yra = {yr0[0], yr0[1]}, yia = {yi0[0], yi0[1]}, wra = {wr[0], wr[1]}, wia = {wi[0], wi[1]};
        f32x2_t yrb = {yr0[2], yr0[3]}, yib = {yi0[2], yi0[3]}, wrb = {wr[2], wr[3]}, wib = {wi[2], wi[3]};
#pragma unroll
        for (int i = 0; i < 32; ++i) {
          const f32x2_t sm2 = yra + yrb;
          v[i] = sm2.x + sm2.y;
          const f32x2_t ta = yra * wra - yia * wia, tb = yrb * wrb - yib * wib;
          yia = yra * wia + yia * wra;
          yib = yrb * wib + yib * wrb;
          yra = ta;
          yrb = tb;
        }
      }
#pragma unroll
      for (int i = 0; i < 16; ++i) v[i] = swap_add32(v[i], v[i + 16]);
#pragma unroll
      for (int i = 0; i < 8; ++i) {
        v[i] = swap_add16(v[i], v[i + 8]);
        v[i] += dppf<0xB1>(0.f, v[i]);
        v[i] += dppf<0x4E>(0.f, v[i]);
        v[i] += dppf<0x141>(0.f, v[i]);
      }
      float s0 = v[0], s1 = v[1], s2 = v[2], s3 = v[3], s4 = v[4], s5 = v[5], s6 = v[6], s7 = v[7];
      asm volatile("" : "+v"(s0), "+v"(s1), "+v"(s2), "+v"(s3), "+v"(s4), "+v"(s5), "+v"(s6), "+v"(s7));
      s0 = (l & 4) ? s4 : s0; s1 = (l & 4) ? s5 : s1; s2 = (l & 4) ? s6 : s2; s3 = (l & 4) ? s7 : s3;
      s0 = (l & 2) ? s2 : s0; s1 = (l & 2) ? s3 : s1;
      const float kv = 2.0f * ((l & 1) ? s1 : s0);
      khl = pk2(kv, 0.f);
      khl = pk2(kv, kv - lo16(khl));
      kr[2080 - 64 * ps - tl] = (u16)khl;
      kr[CKN + 2080 - 64 * ps - tl] = (u16)(khl >> 16);
      SST(ps);
      if (ps < 2) {
        if (ps == 0) {
          if (w < 2) {
            reinterpret_cast<v4u*>(Xr)[tid] = tx[0];
            const unsigned long long m = (((unsigned long long)en.y << 32) | en.x) + TAG_STEP;
            v4u g0;
            for (unsigned spins = 0; spins < 16u * SPIN_CAP; ++spins) {
              g0 = LD128(MKRS(ws + OFF_G0, 8192), tid * 16, REP * 2048);
              if (__all(((g0.x ^ g0.z) | (g0.y ^ (unsigned)m) | (g0.w ^ (unsigned)(m >> 32))) == 0u)) break;
              asm volatile("s_sleep 2" ::: "memory");
            }
            U_ROW2(tid, g0.x);
          }
          __syncthreads();
        }
        f32x4 a0t = {};
#pragma unroll
        for (int b = -1; b < 4; ++b) a0t = MFMA16(KFRAG(-32 * b), conv_bfrag(ub + 16 * b, ush), a0t);
        const float y0 = swap_add32(a0t[0], a0t[1]), y1 = swap_add32(a0t[2], a0t[3]);
        if ((g & 1) == ps) TILE_OUT(0);
        __syncthreads();
        if (w == ps) {
          CHUNK_ST(w);
          if (ps == 0) zero_wait(ws);
          DRAIN();
          if (l < 8) set_flag(FL + FL_FC + ((2 * w * 4 + l) * 8 + (cv >> 3)) * 32 + (cv & 7));
        }
        SST(4 + ps);
      }
      if (ps == 2) {
        if (w >= 2) tx[0] = *reinterpret_cast<const v4u*>(A.x0 + (size_t)tid * DM + 8 * cv);
#pragma unroll
        for (int i = 1; i < 4; ++i) tx[i] = *reinterpret_cast<const v4u*>(A.x0 + (size_t)(tid + 512 * i) * DM + 8 * cv);
      }
    }
    if (w >= 2) reinterpret_cast<v4u*>(Xr)[tid] = tx[0];
#pragma unroll
    for (int i = 1; i < 4; ++i) reinterpret_cast<v4u*>(Xr)[tid + 512 * i] = tx[i];
    {
      unsigned spins = 0;
      while (!__all(__hip_atomic_load((gu32*)FS + (w + 8 * ((l >> 3) & 3)) * 32 + (l & 7), RLX_AGENT) != 0u)) {
        if (spin_timeout(++spins, TMO)) break;
        __builtin_amdgcn_s_sleep(4);
      }
      asm volatile("" ::: "memory");
    }
    if (w >= 2) U_ROW(tid);
#pragma unroll
    for (int i = 1; i < 4; ++i) U_ROW(tid + 512 * i);
    SST(6);
    __syncthreads();
    {
      f32x4 a1t = {};
#pragma unroll
      for (int b = -1; b < 8; ++b) a1t = MFMA16(KFRAG(128 - 32 * b), conv_bfrag(ub + 16 * b, ush), a1t);
      const float y0 = swap_add32(a1t[0], a1t[1]), y1 = swap_add32(a1t[2], a1t[3]);
      TILE_OUT(1);
    }
    __syncthreads();
    if (w < 2) {
      CHUNK_ST(2 + w);
      DRAIN();
      if (l < 8) set_flag(FL + FL_FC + (((4 + 2 * w) * 4 + l) * 8 + (cv >> 3)) * 32 + (cv & 7));
    }
    SST(8);
    (void)wait_n<8, 1>(FL + FL_KG + cv * 32 + (w >> 2), 1, TMO);
    SST(7);
    {
      const __amdgpu_buffer_rsrc_t rk = MKRS(ws + OFF_KF + (size_t)d * 2 * CKN * 2, 2 * CKN * 2);
      v4u t[8];
#pragma unroll
      for (int i = 0; i < 4; ++i) {
        t[i] = LD128(rk, (64 * i + l) * 16, 0);
        t[4 + i] = LD128(rk, (64 * i + l) * 16, CKN * 2);
      }
#pragma unroll
      for (int i = 0; i < 4; ++i)
        if (64 * i + l < 229) {
          reinterpret_cast<v4u*>(kr)[64 * i + l] = t[i];
          reinterpret_cast<v4u*>(kr + CKN)[64 * i + l] = t[4 + i];
        }
      kr[2080 - 192 - tl] = (u16)khl;
      kr[CKN + 2080 - 192 - tl] = (u16)(khl >> 16);
    }
    asm volatile("" ::: "memory");
#define BULK(TA, TB, HOOK)                                                                      \
  {                                                                                            \
    const bf16x8 bm = conv_bfrag(ub - 16, ush);                                                \
    _Pragma("unroll") for (int T = (TA); T < (TB); ++T) acc[T] = MFMA16(KFRAG(128 * T + 32), bm, (f32x4{})); \
    _Pragma("unroll") for (int T = 11; T < 16; ++T) far[T - 11] = MFMA16(KFRAG(128 * T + 32), bm, (f32x4{})); \
    _Pragma("unroll 1") for (int r = 0; r < 4; ++r) {                                          \
      bf16x8 bf[TB];                                                                           \
      _Pragma("unroll") for (int j = 0; j < (TB); ++j) bf[j] = conv_bfrag(ub + 16 * r + 64 * j, ush); \
      _Pragma("unroll") for (int e = 0; e < (TB); ++e) {                                       \
        const bf16x8 a = KFRAG(128 * e - 32 * r);                                              \
        _Pragma("unroll") for (int T = (e > (TA) ? e : (TA)); T < (TB); ++T) acc[T] = MFMA16(a, bf[T - e], acc[T]); \
      }                                                                                        \
      if (r == 0) { HOOK }                                                                     \
    }                                                                                          \
  }
    BULK(2, 11, if (w == 1) (void)(wait_n<SLP_V, 32>(FL + FL_FZ + REP * 64, 64, TMO)););
    SST(10);
#pragma unroll
    for (int T = 2; T < 11; ++T) {
      const float y0 = swap_add32(acc[T][0], acc[T][1]), y1 = swap_add32(acc[T][2], acc[T][3]);
      TILE_OUT(T);
    }
    __syncthreads();
    if (w >= 2) {
#pragma unroll
      for (int i = 0; i < 3; ++i) CHUNK_ST(3 * w - 2 + i);
#pragma unroll
      for (int i = 0; i < 2; ++i) xf[i] = *reinterpret_cast<const v4u*>(Xr + (64 * ((18 + 2 * w + i) & 31) + l) * 8);
      DRAIN();
      if (l < 24) set_flag(FL + FL_FC + ((6 * w - 4) * 4 + l) * 8 * 32 + (cv >> 3) * 32 + (cv & 7));
    }
#define FAR_PIECE(p)                                                                           \
  {                                                                                            \
    if ((p) < 24) {                                                                            \
      const int o_ = 256 * ((p) >> 2) + 32 * ((p) & 3);                                        \
      const bf16x8 b0 = conv_bfrag(ub1 + (o_ >> 1), ush), b1 = conv_bfrag(ub1 + (o_ >> 1) + 64, ush); \
      _Pragma("unroll") for (int e = 10; e < 16; ++e) {                                        \
        const bf16x8 a = KFRAG(128 * e - o_);                                                  \
        if (e > 10) far[e - 11] = MFMA16(a, b0, far[e - 11]);                                  \
        if (e < 15) far[e - 10] = MFMA16(a, b1, far[e - 10]);                                  \
      }                                                                                        \
    } else if ((p) < 28) {                                                                     \
      bf16x8 bf[4];                                                                            \
      _Pragma("unroll") for (int j = 0; j < 4; ++j) bf[j] = conv_bfrag(ub1 + 16 * ((p) & 3) + 64 * (12 + j), ush); \
      _Pragma("unroll") for (int e = 0; e < 4; ++e) {                                          \
        const bf16x8 a = KFRAG(128 * e - 32 * ((p) & 3));                                      \
        _Pragma("unroll") for (int j = 0; j + e < 4; ++j) far[1 + e + j] = MFMA16(a, bf[j], far[1 + e + j]); \
      }                                                                                        \
    } else if ((p) == 28) {                                                                    \
                     \
      u16* ucw = Uc + w * CUN + 32 + t0;                                                       \
      _Pragma("unroll") for (int T = 11; T < 16; ++T) {                                        \
        const float y0 = swap_add32(far[T - 11][0], far[T - 11][1]), y1 = swap_add32(far[T - 11][2], far[T - 11][3]); \
        const float u0 = bf2f(ucw[128 * T]), u1 = bf2f(ucw[128 * T + 32]);                     \
        const unsigned yy = pk2(y0, y1), uu = pk2(u0 * dsk, u1 * dsk);                         \
        const unsigned aa = pk2(lo16(yy) + lo16(uu), hi16(yy) + hi16(uu));                     \
        ucw[128 * T] = (u16)aa;                                                                \
        ucw[128 * T + 32] = (u16)(aa >> 16);                                                   \
      }                                                                                        \
    } else if (w >= 2 && w < 7) {                                                              \
      int l = tid & 63;                                                                        \
      asm volatile("" : "+v"(l), "+v"(xf[0]), "+v"(xf[1]));                                    \
      if ((p) == 29) {                                                                         \
        _Pragma("unroll") for (int i = 0; i < 2; ++i) {                                        \
          const int ck = 18 + 2 * w + i;                                                       \
          const u16* ur = Uc + 32 + 64 * ck + l;                                               \
          v4u o;                                                                               \
          _Pragma("unroll") for (int e = 0; e < 4; ++e)                                        \
            o[e] = pk2(lo16(xf[i][e]) + bf2f(ur[(2 * e) * CUN]), hi16(xf[i][e]) + bf2f(ur[(2 * e + 1) * CUN])); \
          ST128(o, rz1, (l >> 5) * 32768 + (l & 31) * 16, (2 * ck * 64 + cv) * 512);           \
        }                                                                                      \
      } else {                                                                                 \
        DRAIN();                                                                               \
        if (l < 16) set_flag(FL + FL_FC + (((36 + 4 * w) * 4 + l) * 8 + (cv >> 3)) * 32 + (cv & 7)); \
      }                                                                                        \
    }                                                                                          \
  }
#undef BULK
    SST(9);
#undef TILE_OUT
#undef CHUNK_ST
#undef U_ROW
#undef U_ROW2
  }
  u16* uc = Uc + (8 + w) * CUN;
  const unsigned* ub = reinterpret_cast<const unsigned*>(uc + (c & ~1) + 8 * g + 32);
#pragma unroll
  for (int j = 0; j < 16; ++j) acc[j] = f32x4{};
  const __amdgpu_buffer_rsrc_t rx = MKRS(ws + OFF_X2, SZ_X), rz = MKRS(ws + OFF_X3, SZ_X);
  const __amdgpu_buffer_rsrc_t rf = MKRS(FL + FL_FR, 64 * 16 * 128);
  unsigned st = 0u;
  v2u xx = {0u, 0u};
#define X2_LOAD(ss, far)                                                                                                      \
  {                                                                                                                      \
    xx = LD64(rx, (l >> 1) * (DM * 2) + (l & 1) * 8, (far) ? (int)SZ_X : ((ss) * SEG * DM + 8 * cv) * 2);                 \
    st = __builtin_amdgcn_raw_buffer_load_b32(rf, (l >> 4) * 128 + ((l >> 1) & 7) * 4, ((ss) * 4 + REP) * 512, AUX_SC1); \
  }
  unsigned tl = now10();
  for (int s = 0; s < NSEG; ++s) {
    if (w == 1) {
      for (unsigned spins = 0; !__all(umin3(xx.x, xx.y, st) != 0u); ++spins) {
        if (spin_timeout(spins, TMO)) break;
        if (GATE_V && spins == 0 && !__any(st != 0u)) gate(tl);
        if (spins && !__all(st != 0u)) __builtin_amdgcn_s_sleep(SLP_V);
        X2_LOAD(s, __builtin_popcountll(__ballot(st != 0u)) < CVTHR);
      }
      tl = now10() + GATE_V;
      TS(0, s, 64);
      const float mb = lo16(st), ri = 1.0f / hi16(st);
      const unsigned t0 = pk2(lo16(xx.x) - mb, hi16(xx.x) - mb), t1 = pk2(lo16(xx.y) - mb, hi16(xx.y) - mb);
      *reinterpret_cast<v2u*>(Su + l * 4) = v2u{pk2(lo16(t0) * ri, hi16(t0) * ri), pk2(lo16(t1) * ri, hi16(t1) * ri)};
      *reinterpret_cast<v2u*>(Sx + l * 4) = xx;
    }
    const int q = s & 3;
    const u16* kq = ka + 32 * q;
    const bf16x8 a0 = *reinterpret_cast<const bf16x8*>(kq - 32), a1 = *reinterpret_cast<const bf16x8*>(kq);
    __syncthreads();
    TS(1, s, 0);
    if (s == 0)
      for (int x = l; x < CUN / 2; x += 64) reinterpret_cast<unsigned*>(uc)[x] = 0u;
    if (l < 32) uc[32 + 32 * s + l] = Su[l * 8 + w];
    asm volatile("s_waitcnt lgkmcnt(0)" ::: "memory");
    const bf16x8 bp = conv_bfrag(ub + 16 * (s - 1), ush);
    acc[0] = MFMA16(a0, bp, (q == 0 ? acc[1] : acc[0]));
    const f32x4 tmp = MFMA16(a1, conv_bfrag(ub + 16 * s, ush), acc[0]);
    float y0 = (q & 1) ? tmp[2] : tmp[0], y1 = (q & 1) ? tmp[3] : tmp[1];
    y0 = xsum32(y0);
    y1 = xsum32(y1);
    if (g == (q >> 1)) {
      const float u0 = bf2f(Su[c * 8 + w]), u1 = bf2f(Su[(16 + c) * 8 + w]);
      const float a0 = bfr(bfr(y0) + bfr(u0 * dsk)), a1 = bfr(bfr(y1) + bfr(u1 * dsk));
      To[c * 8 + w] = f2bf(bf2f(Sx[c * 8 + w]) + a0);
      To[(16 + c) * 8 + w] = f2bf(bf2f(Sx[(16 + c) * 8 + w]) + a1);
    }
    __syncthreads();
    if (w == 0 && l < 32) {
      const v4u o = *reinterpret_cast<const v4u*>(To + l * 8);
      ST128(o, rz, l * 16, (s * 64 + cv) * 512);
    }
    if (w == 1) X2_LOAD(s + 1, CVTHR > 0);
    const int left = 15 - (s >> 2);
#define PUSH(J0, SH)                                                                                              \
  if (left >= (J0)) {                                                                                             \
    _Pragma("unroll") for (int j = (J0); j < (J0) + 4 && j < 16; ++j)                                             \
      acc[j] = MFMA16(*reinterpret_cast<const bf16x8*>(kq - 32 - 128 * j), bp, (j + (SH) < 16 ? acc[(j + (SH)) & 15] : f32x4{})); \
  }
    TS(2, s, 0);
    if (w == 0) {
      DRAIN();
      if (l < 4) set_flag(FL + FL_FC + (((64 + s) * 4 + l) * 8 + (cv >> 3)) * 32 + (cv & 7));
    }
    TS(3, s, 0);
    if (q == 0) { PUSH(1, 1) PUSH(5, 1) PUSH(9, 1) PUSH(13, 1) }
    else { PUSH(1, 0) PUSH(5, 0) PUSH(9, 0) PUSH(13, 0) }
#undef PUSH
    MSTAMP();
    if (s >= CVPRE && s < 31 + CVPRE) FAR_PIECE(s - CVPRE);
  }
#undef KFRAG
#undef X2_LOAD
#undef FAR_PIECE
}

__device__ __forceinline__ v4u ln_row_packed(const v4u xr) {
  float v[8];
#pragma unroll
  for (int e = 0; e < 4; ++e) { v[2 * e] = lo16(xr[e]); v[2 * e + 1] = hi16(xr[e]); }
  float s = ((v[0] + v[1]) + (v[2] + v[3])) + ((v[4] + v[5]) + (v[6] + v[7]));
  s = wave_sum(s);
  const float mean = s * (1.0f / DM);
  float q = 0.f;
#pragma unroll
  for (int j = 0; j < 8; ++j) { float t = v[j] - mean; q += t * t; }
  q = wave_sum(q);
  const float var = q * (1.0f / DM);
  const float mb = bfr(mean);
  const float sd = bfr(sqrtf(bfr(bfr(var) + 1e-5f)));
  const float ri = 1.0f / sd;
  v4u o;
#pragma unroll
  for (int e = 0; e < 4; ++e) {
    const unsigned t = pk2(v[2 * e] - mb, v[2 * e + 1] - mb);
    const float x0 = lo16(t), x1 = hi16(t);
    float q0 = x0 * ri, q1 = x1 * ri;
    q0 = __builtin_fmaf(__builtin_fmaf(-q0, sd, x0), ri, q0);
    q1 = __builtin_fmaf(__builtin_fmaf(-q1, sd, x1), ri, q1);
    o[e] = pk2(q0, q1);
  }
  return o;
}

__device__ __forceinline__ float bfr1(float x) { return lo16(pk2(x, x)); }
__device__ __forceinline__ void ln4_packed(v4u (&x)[4]) {
  f32x2_t v[16];
#pragma unroll
  for (int e = 0; e < 16; ++e) v[e] = f32x2_t{lo16(x[e >> 2][e & 3]), hi16(x[e >> 2][e & 3])};
  const f32x2_t t = (((v[0] + v[1]) + (v[2] + v[3])) + ((v[4] + v[5]) + (v[6] + v[7]))) +
                    (((v[8] + v[9]) + (v[10] + v[11])) + ((v[12] + v[13]) + (v[14] + v[15])));
  const float mean = red_mod4(t.x + t.y) * (1.0f / DM);
  const float mb = bfr1(mean);
  f32x2_t q0 = {0.f, 0.f}, q1 = {0.f, 0.f};
#pragma unroll
  for (int e = 0; e < 16; e += 2) {
    v[e] -= mb;
    v[e + 1] -= mb;
    q0 += v[e] * v[e];
    q1 += v[e + 1] * v[e + 1];
  }
  q0 += q1;
  const float dm = mean - mb;
  const float var = fmaxf(red_mod4(q0.x + q0.y) * (1.0f / DM) - dm * dm, 0.f);
  const float sd = bfr1(sqrtf(bfr1(bfr1(var) + 1e-5f)));
  const float ri = 1.0f / sd;
#pragma unroll
  for (int e = 0; e < 16; ++e) {
    const unsigned t2 = pk2(v[e].x, v[e].y);
    const f32x2_t q = f32x2_t{lo16(t2), hi16(t2)} * ri;
    x[e >> 2][e & 3] = pk2(q.x, q.y);
  }
}

extern "C" __device__ float mul1(float, float) __asm("llvm.amdgcn.fmul.legacy");
template <bool G0>
__device__ __forceinline__ void role_qkv_t(const MegaArgs& A, char* smem, const int L, const int h) {
  __builtin_amdgcn_s_waitcnt(0x0F70);
  const int w = __builtin_amdgcn_readfirstlane(threadIdx.x >> 6);
  char* ws = A.ws;
  unsigned* FL = (unsigned*)(ws + OFF_FL);
  unsigned* TMO = FL + FL_TMO;
#define LANE_IDS()                         \
  const int l = lane_id();                 \
  const int c = l & 15, g = l >> 4;        \
  (void)c; (void)g;
  unsigned* FC = FL + FL_FC + (L * 64 * 4 + REP) * 8 * 32;
  const __amdgpu_buffer_rsrc_t rsQ = MKRS(ws + OFF_QK + L * SZ_QK, SZ_QK);
  const __amdgpu_buffer_rsrc_t rsV = MKRS(ws + OFF_VF + L * SZ_O, SZ_O);
  const __amdgpu_buffer_rsrc_t rsM = MKRS(ws + (G0 ? OFF_MEMK : OFF_MEMV) + L * SZ_MEM, SZ_MEM);
  bf16x8 wq[16], wk[16], wv[16];
  {
    LANE_IDS();
    const unsigned sb = lds_base(smem);
    const int q = (l >> 2) & 3, p = l & 3, f = q | ((g & 1) << 2);
    const unsigned ra = sb + (8 * g + q) * 256 + (w ^ f) * 32 + p * 8;
#define W_STAGE(W, hf, VM, NEXT)                                               \
  {                                                                           \
    WAIT_VM(VM);                                                              \
    RAW_BAR();                                                                \
    _Pragma("unroll") for (int t = 0; t < 8; ++t) {                           \
      const unsigned a_ = ra + (hf) * 8192 + t * 16384;                       \
      const v2u r0 = trd(a_), r1 = trd(a_ + 1024);                            \
      W[8 * (hf) + t] = BF8((v4u{r0.x, r0.y, r1.x, r1.y}));                   \
    }                                                                         \
    _Pragma("unroll") for (int t = 0; t < 8; ++t) asm volatile("" : "+v"(W[8 * (hf) + t])); \
    RAW_BAR();                                                                \
    NEXT;                                                                     \
  }
    wqkv_issue<false>(A.Wq, 0, h, w, l, sb);
    wqkv_issue<false>(A.Wq, 1, h, w, l, sb);
    W_STAGE(wq, 0, 8, wqkv_issue<false>(A.Wk, 0, h, w, l, sb));
    SST(4);
    W_STAGE(wq, 1, 8, wqkv_issue<false>(A.Wk, 1, h, w, l, sb));
    SST(5);
    W_STAGE(wk, 0, 8, wqkv_issue<false>(A.Wv, 0, h, w, l, sb));
    W_STAGE(wk, 1, 8, wqkv_issue<false>(A.Wv, 1, h, w, l, sb));
    SST(6);
    W_STAGE(wv, 0, 8, (void)0);
    W_STAGE(wv, 1, 0, (void)0);
#undef W_STAGE
  }
  WSTAMP2();
  constexpr int Q_X = 98304;
  const unsigned sb = lds_base(smem);
  v2u eq, ek, ev;
  {
    LANE_IDS();
    const v4u zt = LD128(MKRS(ws + OFF_TAG, 8192 + 8), l * 16, (blockIdx.x & 7) * 1024);
    const v2u ze = LD64(MKRS(ws + OFF_TAG, 8192 + 8), 0, 8192);
    const v4u one = {0x3f803f80u, 0x3f803f80u, 0x3f803f80u, 0x3f803f80u};
    f32x4 t[3] = {};
#pragma unroll
    for (int ks = 0; ks < 16; ++ks) {
      t[0] = MFMA16(BF8(one), wq[ks], t[0]);
      t[1] = MFMA16(BF8(one), wk[ks], t[1]);
      t[2] = MFMA16(BF8(one), wv[ks], t[2]);
    }
    v2u e[3];
#pragma unroll
    for (int i = 0; i < 3; ++i) {
      const float x = g ? 0.f : t[i][0], x1 = x - bfr1(x);
      e[i] = v2u{pk2(x, x1), pk2(x1 - bfr1(x1), 0.f)};
    }
    eq = e[0]; ek = e[1]; ev = e[2];
    asm volatile("" : "+v"(eq), "+v"(ek), "+v"(ev));
    if (!zero_seen(zt, ze)) zero_wait(ws);
    if (l < 32) *reinterpret_cast<v4u*>(smem + Q_X + w * 2048 + (l >> 4) * 16384 + c * 16) = one;
  }
  constexpr int Q_CNT = Q_X + 11 * 2048 + 256;
  lu32* cnt = (lu32*)(unsigned long)(sb + Q_CNT);
  if (threadIdx.x < 16) cnt[threadIdx.x] = 0u;
#define C_ADD(i) asm volatile("s_mov_b64 exec, 1\n\tds_add_u32 %0, %1\n\ts_mov_b64 exec, -1" ::"v"(sb + Q_CNT + 4 * (i)), "v"(1u) : "memory")
#define C_RD(i) (*(volatile lu32*)(cnt + (i)))
#define C_GET(i) ((unsigned)__builtin_amdgcn_readfirstlane(C_RD(i)))
#define C_WAIT(i, n)                                                                                                    \
  {                                                                                                                     \
    for (unsigned spins = 0; C_GET(i) < (n) && spins < 64u * SPIN_CAP; ++spins) __builtin_amdgcn_s_sleep(1);             \
    asm volatile("" ::: "memory");                                                                                      \
  }
  const char* Xb = ws + (L ? OFF_X3 : OFF_XA) + w * 8192;
  const v4u rsF = mk_srd(FC + w * 64, (64 * 32 - REP * 8 - 2 * w) * 128);
  unsigned l16, vo, fo;
  {
    LANE_IDS();
    l16 = l * 16;
    vo = (c + 16 * (2 * (w & 1) + (g >> 1))) * 16 + 4096 * (g & 1);
    fo = g * 512 + c * 16;
  }
#define DMA4(x) "global_load_lds_dwordx4 %1, " x " sc1\n\tglobal_load_lds_dwordx4 %1, " x " offset:1024 sc1\n\t"       \
                "global_load_lds_dwordx4 %1, " x " offset:2048 sc1\n\tglobal_load_lds_dwordx4 %1, " x " offset:3072 sc1\n\t"
#define X_DMA(ss, bb)                                                                                                   \
  {                                                                                                                     \
    unsigned keep_;                                                                                                     \
    const char* x_ = Xb + (size_t)(ss) * 32768;                                                                         \
    const unsigned d_ = sb + (bb) * 32768 + w * 8192;                                                                   \
    asm volatile("s_nop 4\n\ts_mov_b32 %0, m0\n\ts_mov_b32 m0, %4\n\ts_nop 0\n\t" DMA4("%2")                           \
                 "s_mov_b32 m0, %5\n\ts_nop 0\n\t" DMA4("%3") "s_mov_b32 m0, %0"                                         \
                 : "=&s"(keep_)                                                                                         \
                 : "v"(l16), "s"(x_), "s"(x_ + 4096), "s"(d_), "s"(d_ + 4096u)                                           \
                 : "memory");                                                                                           \
    TS(0, ss, 0);                                                                                                       \
  }
#define X_DMA1(on, x, d, off)                                                                                           \
  {                                                                                                                     \
    unsigned keep_;                                                                                                     \
    asm volatile("s_cmp_eq_u32 %4, 0\n\ts_cbranch_scc1 1f\n\ts_mov_b32 %0, m0\n\ts_mov_b32 m0, %3\n\ts_nop 0\n\t"       \
                 "global_load_lds_dwordx4 %1, %2 offset:%5 sc1\n\ts_mov_b32 m0, %0\n1:"                                  \
                 : "=&s"(keep_)                                                                                         \
                 : "v"(l16), "s"(x), "s"(d), "s"(on), "n"(off)                                                          \
                 : "memory", "scc");                                                                                    \
  }
#define F_LOAD(r, ss)                                                                                                   \
  asm volatile("s_nop 4\n\tbuffer_load_dword %0, %1, %2, %3 offen sc1"                                                  \
               : "=v"(r)                                                                                                \
               : "v"(((l16 >> 2) & 28) + (l16 & 128)), "s"(rsF), "s"((ss) * 4096)                                       \
               : "memory")
  unsigned* FQ = FL + FL_FQ + (L * 64 * 32 + h) * 32;
#define Q_FLAG(ss)                                                                                                      \
  {                                                                                                                     \
    unsigned old_;                                                                                                      \
    asm volatile("s_mov_b64 exec, 1\n\tds_add_rtn_u32 %0, %1, %2\n\ts_mov_b64 exec, -1\n\ts_waitcnt lgkmcnt(0)"           \
                 : "=&v"(old_) : "v"(sb + Q_CNT + ((ss) & 1) * 4), "v"(1u) : "memory");                                 \
    if ((__builtin_amdgcn_readfirstlane(old_) & 7u) == 7u) {                                                            \
      asm volatile("s_mov_b64 exec, 1\n\tglobal_store_dword %0, %1, %2 sc1\n\ts_mov_b64 exec, -1"                        \
                   ::"v"(0), "v"(1u), "s"(FQ + (ss) * 1024) : "memory");                                                \
      TS(3, ss, threadIdx.x & ~63u);                                                                                    \
    }                                                                                                                   \
    pend = false;                                                                                                       \
  }
  __builtin_amdgcn_s_waitcnt(0x0F70);
  RAW_BAR();
  bool have = false, pend = false;
  unsigned tl = now10();
  const unsigned tq = tl - QPL;
  int b = 0;
  unsigned k4 = 4u;
  FSD;
  for (int s = 0; s < NSEG; ++s) {
    FS(0);
    const int b1 = b == 2 ? 0 : b + 1;
    const unsigned u1 = 2u * (b1 ? k4 - 4u : k4);
    unsigned nf;
    if (QPACE && L == 0 && s >= QPS && (int)(tq + s * QPACE - now10()) > 0) {
      if (pend) {
        WAIT_VM(0);
        Q_FLAG(s - 1);
      }
      gate(tq + s * QPACE);
    }
    if (G0) {
      if (!have) {
        for (unsigned spins = 0;; ++spins) {
          unsigned n;
          F_LOAD(n, s);
          WAIT_VM(0);
          if (pend) Q_FLAG(s - 1);
          if (all_set(n) || spin_timeout(spins, TMO)) break;
          if (GATE_Q && spins == 0) gate(tl);
          else if (L) __builtin_amdgcn_s_sleep(SLP_Q);
          else __builtin_amdgcn_s_sleep(SLP_Q1);
        }
        tl = now10() + GATE_Q;
        C_WAIT(8 + b, 2u * k4 - 8u);
        X_DMA(s, b);
        WAIT_VM(0);
        C_ADD(4 + b);
      }
      const unsigned c0 = C_RD(4 + b);
      F_LOAD(nf, s + 1);
      if ((unsigned)__builtin_amdgcn_readfirstlane(c0) < k4) C_WAIT(4 + b, k4);
    } else {
      if (C_GET(4 + b) < k4) {
        if (pend) {
          WAIT_VM(0);
          Q_FLAG(s - 1);
        }
        C_WAIT(4 + b, k4);
      }
      FS(1);
      const char* sp = smem + ((l16 & 128) ? Q_X + ((w + 1) & 1) * 128 : b * 32768 + fo + (w & 3) * 128);
      f32x4 gm = {};
      __builtin_amdgcn_s_setprio(3);
      {
        bf16x8 a[10];
#pragma unroll
        for (int ks = 0; ks < 10; ++ks) a[ks] = *reinterpret_cast<const bf16x8*>(sp + 2048 * ks);
        __builtin_amdgcn_sched_barrier(0);
#pragma unroll
        for (int ks = 0; ks < 16; ++ks) {
          gm = MFMA16(a[ks % 10], a[ks % 10], gm);
          if (ks < 6) a[ks] = *reinterpret_cast<const bf16x8*>(sp + 2048 * (10 + ks));
          __builtin_amdgcn_sched_barrier(0);
        }
      }
      float q2 = (l16 & 16) ? gm[1] : gm[0], q3 = (l16 & 16) ? gm[3] : gm[2];
      asm volatile("" : "+v"(q2), "+v"(q3));
      q2 = (l16 & 32) ? q3 : q2;
      auto sw = __builtin_amdgcn_permlane32_swap(__float_as_uint(q2), __float_as_uint(q2), false, false);
      const float mean = __uint_as_float(sw[1]) * (-1.0f / DM);
      const float var = fmaxf(__builtin_fmaf(-mean, mean, q2 * (1.0f / DM)), 0.f);
      const float sd = bfr1(__builtin_amdgcn_sqrtf(bfr1(bfr1(var) + 1e-5f)));
      asm volatile("s_mov_b64 exec, 0xf0000f\n\tds_write2_b32 %0, %1, %2 offset1:32\n\ts_mov_b64 exec, -1"
                   ::"v"(sb + Q_X + (8 + b) * 2048 + 256 + (w & 3) * 32 + ((l16 >> 2) & 28)), "v"(pk2(mean, mean)),
                   "v"(__builtin_amdgcn_rcpf(sd)) : "memory");
      C_ADD(12 + b);
      FS(2);
      if (pend) {
        WAIT_VM(0);
        Q_FLAG(s - 1);
      }
    }
    TS(G0 ? 1 : 5, s, G0 ? 0 : 256);
    FS(3);
    const char* ap = smem + b * 32768 + fo;
#define AFRAG(i) (*reinterpret_cast<const bf16x8*>(ap + ((i) >> 4) * 256 + 2048 * ((i) & 15)))
    bf16x8 abuf[QPF + 1];
#pragma unroll
    for (int i = 0; i < QPF; ++i) abuf[i] = AFRAG(i);
    f32x4 aq[2] = {}, ak[2] = {}, av[2] = {};
    unsigned cx = 0u;
    int on = 0;
    const char* xn = Xb + (size_t)(s + 1) * 32768;
    const unsigned dn = sb + b1 * 32768 + w * 8192;
    __builtin_amdgcn_s_setprio(G0 ? 1 : 0);
#pragma unroll
    for (int rt = 0; rt < 2; ++rt) {
#pragma unroll
      for (int ks = 0; ks < 16; ++ks) {
        const int i = 16 * rt + ks;
        if (i + QPF < 32) abuf[(i + QPF) % (QPF + 1)] = AFRAG(i + QPF);
        if (G0 && i == 11) cx = C_RD(8 + b1);
        if (i == 28) cx = C_RD(12 + b);
        const bf16x8 a = abuf[i % (QPF + 1)];
        aq[rt] = MFMA16(wq[ks], a, aq[rt]);
        ak[rt] = MFMA16(wk[ks], a, ak[rt]);
        av[rt] = MFMA16(a, wv[ks], av[rt]);
        if (i == QUP) { __builtin_amdgcn_s_setprio(G0 ? 3 : 2); FS(4); }
        __builtin_amdgcn_sched_barrier(0);
        if (G0 && rt == 1 && ks < 8) {
          X_DMA1(on, xn + (ks >> 2) * 4096, dn + (ks >> 2) * 4096, (ks & 3) * 1024);
          __builtin_amdgcn_sched_barrier(0);
        }
      }
      asm volatile("" : "+v"(aq[rt]), "+v"(ak[rt]), "+v"(av[rt]));
      if (rt == 0) FS(5); else FS(7);
      if (G0 && rt == 0) {
        WAIT_VM(0);
        if (pend) Q_FLAG(s - 1);
        have = all_set(nf) && (unsigned)__builtin_amdgcn_readfirstlane(cx) >= u1;
        F_LOAD(nf, have ? NSEG : s + 1);
        if (have) TS(0, s + 1, 0);
        on = __builtin_amdgcn_readfirstlane((int)have);
        FS(6);
      }
    }
    if ((unsigned)__builtin_amdgcn_readfirstlane(cx) < k4) C_WAIT(12 + b, k4);
    FS(8);
    v4u vfrag, qf4, kf4;
    {
      const char* stp = smem + Q_X + (8 + b) * 2048 + 256;
      unsigned nm[2];
      float ri[2];
      f32x4 rv[2];
#pragma unroll
      for (int rt = 0; rt < 2; ++rt) {
        nm[rt] = *reinterpret_cast<const unsigned*>(stp + 64 * rt + ((l16 >> 2) & 60));
        ri[rt] = *reinterpret_cast<const float*>(stp + 128 + 64 * rt + ((l16 >> 2) & 60));
        rv[rt] = *reinterpret_cast<const f32x4*>(stp + 128 + 64 * rt + ((l16 >> 4) & 48));
      }
#pragma unroll
      for (int rt = 0; rt < 2; ++rt) {
        const v2u xm = {nm[rt], nm[rt]};
        aq[rt] = MFMA16K(eq, xm, aq[rt]);
        ak[rt] = MFMA16K(ek, xm, ak[rt]);
        av[rt] = MFMA16K(xm, ev, av[rt]);
      }
      __builtin_amdgcn_s_setprio(QEP);
#pragma unroll
      for (int rt = 0; rt < 2; ++rt) {
        f32x4 tq, tk, tv;
#pragma unroll
        for (int e = 0; e < 4; ++e) {
          tq[e] = mul1(aq[rt][e], ri[rt]);
          tk[e] = mul1(ak[rt][e], ri[rt]);
          tv[e] = mul1(av[rt][e], rv[rt][e]);
        }
        qf4[2 * rt] = pk2(tq[0], tq[1]); qf4[2 * rt + 1] = pk2(tq[2], tq[3]);
        kf4[2 * rt] = pk2(tk[0], tk[1]); kf4[2 * rt + 1] = pk2(tk[2], tk[3]);
        vfrag[2 * rt] = pk2(tv[0], tv[1]); vfrag[2 * rt + 1] = pk2(tv[2], tv[3]);
      }
    }
    {
      auto q0 = __builtin_amdgcn_permlane16_swap(qf4.x, qf4.z, false, false);
      auto q1 = __builtin_amdgcn_permlane16_swap(qf4.y, qf4.w, false, false);
      auto k0 = __builtin_amdgcn_permlane16_swap(kf4.x, kf4.z, false, false);
      auto k1 = __builtin_amdgcn_permlane16_swap(kf4.y, kf4.w, false, false);
      v4u qo = {q0[0], q1[0], q0[1], q1[1]}, ko = {k0[0], k1[0], k0[1], k1[1]};
      const int soff = ((s * NH + h) * 16 + (w >> 1)) * 1024;
      asm volatile("" : "+v"(qo), "+v"(ko), "+v"(vfrag));
      FS(9);
      C_ADD(8 + b);
      if (G0 && have) {
        WAIT_VM(0);
        C_ADD(4 + b1);
      }
      TS(G0 ? 2 : 6, s, G0 ? 0 : 256);
      FS(10);
      ST128(ko, rsQ, vo, soff + 8192);
      ST128(qo, rsQ, vo, soff);
      ST128(vfrag, rsV, l16, ((s * NH + h) * 512 + w * 64) * 16);
      ST64((v2u{0u, 0u}), rsM, l16 >> 1, (s * NH + h) * 2048 + (w & 3) * 512);
      pend = true;
      FS(11);
    }
    if (G0) {
      WAIT_VM(4);
      if (all_set(nf) && C_GET(8 + b1) >= u1) {
        X_DMA(s + 1, b1);
        have = true;
        WAIT_VM(8);
        Q_FLAG(s);
        WAIT_VM(0);
        C_ADD(4 + b1);
      }
    }
    MSTAMP();
    FD(s);
    b = b1;
    if (b == 0) k4 += 4u;
  }
  DRAIN();
  if (pend) Q_FLAG(NSEG - 1);
#undef LANE_IDS
#undef DMA4
#undef X_DMA
#undef X_DMA1
#undef F_LOAD
#undef Q_FLAG
#undef AFRAG
#undef C_ADD
#undef C_RD
#undef C_GET
#undef C_WAIT
}
__device__ __forceinline__ void role_qkv(const MegaArgs& A, char* smem, const int L, const int h) {
  if (__builtin_amdgcn_readfirstlane(threadIdx.x >> 6) < 4) role_qkv_t<true>(A, smem, L, h);
  else role_qkv_t<false>(A, smem, L, h);
}

constexpr int MEGA_WGS = 256;
constexpr size_t MEGA_LDS = (size_t)(32 * 4 * 64 * 8 + 8 * SMS + 15 * QS + 128 + 16 * 64 * 8 + 8 * SOS + 512) * 2;
static_assert(MEGA_LDS <= 163840 && MEGA_LDS >= 162816 + 256, "lds");
static_assert(MEGA_LDS >= (size_t)AW_CNT + 64, "lds");
static_assert(MEGA_LDS >= (size_t)(8 * 2 * CKN + 8 * 2 * CUN + 4 * 32 * 8) * 2, "lds");
static_assert(MEGA_LDS >= (size_t)2 * 32 * SMS * 2 && MEGA_LDS >= 131072, "lds");
static_assert(131072 >= (size_t)(40 * NST + 32 * 400) * 4 && 131072 + 4 * 392 * 4 <= AW_CNT, "lds");
__global__ __launch_bounds__(512, 2) void mega_kernel(const u16* mem0, const u16* Dskip, char* ws, u16* out, const u16* x0,
                                                      const u16* logAre, const u16* Aim, const u16* Cre, const u16* Cim,
                                                      const u16* logdt, const u16* Wq, const u16* Wk, const u16* Wv,
                                                      const u16* Wo) {
  extern __shared__ __attribute__((aligned(16))) char smem[];
  const int b = __builtin_amdgcn_readfirstlane(role_no());
  const MegaArgs A{mem0, Dskip, ws, out, x0, logAre, Aim, Cre, Cim, logdt, Wq, Wk, Wv, Wo, b};
  {
    const int hi = (b >= 32 && b < 96) ? b - 32 : (b >= 200 && b < 248) ? b - 136 : (b >= 96 && b < 104) ? b + 16 : (b >= 248 ? b - 128 : -1);
    if (hi >= 0) start_kgen(A, smem, hi, b - 248, (b >= 64 && b < 104) || (b >= 232 && b < 240) ? b & 15 : -1);
  }
  if ((EXP_OFF & 1) && b >= 64 && b < 80) return;
  if ((EXP_OFF & 8) && b >= 200 && b < 232) return;
  if ((EXP_OFF & 16) && b >= 32 && b < 64) return;
  if ((EXP_OFF & 32) && b >= 80 && b < 96) return;
  if (b < 64) role_chain(A, smem, b >> 5, b & 31, 0);
  else if (b < 104 || (b >= 232 && b < 240)) role_aw(A, smem, b < 80 ? 0 : b < 96 ? 2 : 1, b & 15);
  else if (b < 168) role_conv(A, smem, b - 104);
  else if (b < 232) role_qkv(A, smem, (b - 168) >> 5, (b - 168) & 31);
  else role_red(A, smem, b - 240);
}

extern "C" void kernel_launch(void* const* d_in, const int* in_sizes, int n_in, void* d_out, int out_size, void* d_ws,
                              size_t ws_size, hipStream_t stream) {
  MegaArgs A;
  A.x0 = (const u16*)d_in[0];
  A.logAre = (const u16*)d_in[1];
  A.Aim = (const u16*)d_in[2];
  A.Cre = (const u16*)d_in[3];
  A.Cim = (const u16*)d_in[4];
  A.logdt = (const u16*)d_in[5];
  A.Dskip = (const u16*)d_in[6];
  A.mem0 = (const u16*)d_in[8];
  A.Wq = (const u16*)d_in[9];
  A.Wk = (const u16*)d_in[10];
  A.Wv = (const u16*)d_in[11];
  A.Wo = (const u16*)d_in[12];
  A.ws = (char*)d_ws;
  A.out = (u16*)d_out;

  (void)hipFuncSetAttribute((const void*)mega_kernel, hipFuncAttributeMaxDynamicSharedMemorySize, (int)MEGA_LDS);
  mega_kernel<<<MEGA_WGS, 512, MEGA_LDS, stream>>>(A.mem0, A.Dskip, A.ws, A.out, A.x0, A.logAre, A.Aim, A.Cre, A.Cim, A.logdt,
                                                   A.Wq, A.Wk, A.Wv, A.Wo);
}
```

```cpp
#include <hip/hip_runtime.h>
#include <hip/hip_bf16.h>
#include <stdint.h>

constexpr int L_SEQ = 2048;
constexpr int DM    = 512;
constexpr int NST   = 128;
constexpr int NH    = 32;
constexpr int DH    = 128;
constexpr int HD    = NH * DH;
constexpr int MM    = 8;
constexpr int SEG   = 32;
constexpr int NSEG  = L_SEQ / SEG;
constexpr int LK    = MM + SEG;

typedef unsigned short u16;
using bf16x8 = __attribute__((ext_vector_type(8))) short;
using f32x4  = __attribute__((ext_vector_type(4))) float;
using f32x16 = __attribute__((ext_vector_type(16))) float;

static __device__ __forceinline__ u16 f2bf(float f) {
  unsigned u = __float_as_uint(f);
  u += 0x7fffu + ((u >> 16) & 1u);
  return (u16)(u >> 16);
}
static __device__ __forceinline__ float bf2f(u16 h) { return __uint_as_float(((unsigned)h) << 16); }
static __device__ __forceinline__ float bfr(float f) { return bf2f(f2bf(f)); }

constexpr size_t SZ_X      = (size_t)L_SEQ * DM * 2;
constexpr size_t OFF_KF    = 0;
constexpr size_t OFF_UT    = OFF_KF + (size_t)DM * L_SEQ * 4;
constexpr size_t OFF_YT    = OFF_UT + SZ_X;
constexpr size_t OFF_XA    = OFF_YT + SZ_X;
constexpr size_t OFF_H     = OFF_XA + SZ_X;
constexpr size_t OFF_X2    = OFF_H + SZ_X;
constexpr size_t OFF_U2    = OFF_X2 + SZ_X;
constexpr size_t OFF_X3    = OFF_U2 + SZ_X;
constexpr size_t SZ_QK     = (size_t)L_SEQ * 2 * HD * 2;
constexpr size_t OFF_QK    = OFF_X3 + SZ_X;
constexpr size_t SZ_O      = (size_t)L_SEQ * HD * 2;
constexpr size_t OFF_VF    = OFF_QK + 2 * SZ_QK;
constexpr size_t SZ_PW     = (size_t)2 * NSEG * 16 * SEG * DM * 2;
constexpr size_t OFF_PW    = OFF_VF + 2 * SZ_O;
constexpr size_t SZ_MEM    = (size_t)NSEG * MM * HD * 2;
constexpr size_t OFF_MEMK  = OFF_PW + SZ_PW;
constexpr size_t OFF_MEMV  = OFF_MEMK + 2 * SZ_MEM;
constexpr size_t OFF_GT    = OFF_MEMV + 2 * SZ_MEM;
constexpr size_t SZ_ACC    = (size_t)2 * 32768;
constexpr size_t OFF_Z     = OFF_GT + (size_t)64 * DM * 4;
constexpr size_t OFF_ACC   = OFF_Z;
constexpr unsigned ACC_LO = 0x88000000u, ACC_HI = 0x87fffff7u;
constexpr size_t OFF_FL    = OFF_ACC + 2 * SZ_ACC;
constexpr int FL_FW  = 0;
constexpr int FL_FR  = FL_FW + 2 * 64 * 16 * 32;
constexpr int FL_FC  = FL_FR + 64 * 16 * 32;
constexpr int FL_FQ  = FL_FC + 2 * 64 * 32 * 32;
constexpr int FL_FS  = FL_FQ + 2 * 64 * 32 * 32;
constexpr int FL_FZ  = FL_FS + 4 * 32 * 32;
constexpr int FL_KG  = FL_FZ + 8 * 32;
constexpr int FL_G   = FL_KG + 64 * 32;
constexpr int FL_TK  = FL_G + 4 * 4 * 32;
constexpr int FL_TMO = FL_TK + 2 * 256;
constexpr int FL_END = FL_TMO + 64;
constexpr size_t Z_BYTES   = OFF_FL + (size_t)FL_END * 4 - OFF_Z;
constexpr size_t OFF_TAG   = OFF_Z + Z_BYTES;
constexpr unsigned long long TAG_STEP = 0x9e3779b97f4a7c15ull;
constexpr size_t OFF_G0    = OFF_TAG + 8 * 1024 + 256;
constexpr size_t WS_TOTAL  = OFF_G0 + 8192;
static_assert(Z_BYTES % 256 == 0 && OFF_Z % 256 == 0 && Z_BYTES <= 2 * 128 * 8192, "zero region");
#define REP ((blockIdx.x >> 1) & 3)
static_assert(WS_TOTAL + (4 << 20) <= (size_t)256 * 1024 * 1024, "workspace too large");

constexpr int CKN = 2208;
static_assert((size_t)DM * 2 * CKN * 2 <= (size_t)DM * L_SEQ * 4 + SZ_X, "K buffer");
__device__ __forceinline__ float pos_of_index(int p) {
  if (p < 256) return (float)p;
  if (p < 384) return (float)(256 + 2 * (p - 256));
  if (p < 512) return (float)(512 + 4 * (p - 384));
  if (p < 640) return (float)(1024 + 8 * (p - 512));
  return 2048.0f;
}
__device__ __forceinline__ int index_of_l(int l) {
  float pf = bfr((float)l);
  int p = (int)pf;
  if (p < 256) return p;
  if (p < 512) return 256 + ((p - 256) >> 1);
  if (p < 1024) return 384 + ((p - 512) >> 2);
  if (p < 2048) return 512 + ((p - 1024) >> 3);
  return 640;
}

template <int CTRL> __device__ __forceinline__ float dppr(float x) {
  return __builtin_bit_cast(float, __builtin_amdgcn_update_dpp(0, __builtin_bit_cast(int, x), CTRL, 0xf, 0xf, false));
}
__device__ __forceinline__ float wave_sum(float v) {
  v += dppr<0x121>(v);
  v += dppr<0x122>(v);
  v += dppr<0x124>(v);
  v += dppr<0x128>(v);
  {
    auto r = __builtin_amdgcn_permlane16_swap(__float_as_uint(v), __float_as_uint(v), false, false);
    v = __uint_as_float(r[0]) + __uint_as_float(r[1]);
  }
  {
    auto r = __builtin_amdgcn_permlane32_swap(__float_as_uint(v), __float_as_uint(v), false, false);
    v = __uint_as_float(r[0]) + __uint_as_float(r[1]);
  }
  return v;
}
__device__ __forceinline__ void ln_row8(float (&v)[8]) {
  float s = 0.f;
#pragma unroll
  for (int j = 0; j < 8; ++j) s += v[j];
  s = wave_sum(s);
  const float mean = s * (1.0f / DM);
  float q = 0.f;
#pragma unroll
  for (int j = 0; j < 8; ++j) { float t = v[j] - mean; q += t * t; }
  q = wave_sum(q);
  const float var = q * (1.0f / DM);
  const float mb = bfr(mean);
  const float sd = bfr(sqrtf(bfr(bfr(var) + 1e-5f)));
#pragma unroll
  for (int j = 0; j < 8; ++j) v[j] = bfr(bfr(v[j] - mb) / sd);
}

#define RLX_AGENT __ATOMIC_RELAXED, __HIP_MEMORY_SCOPE_AGENT
typedef __attribute__((address_space(1))) unsigned gu32;
typedef unsigned v4u __attribute__((ext_vector_type(4)));
constexpr int AUX_SC1 = 16;
constexpr unsigned SPIN_CAP = 1u << 18;

__device__ __forceinline__ bool wait_flags(const unsigned* flags, int lane, int nfl, unsigned want, unsigned* tmo) {
  for (unsigned spins = 0;; ++spins) {
    unsigned v = want;
    if (lane < nfl) v = __hip_atomic_load((gu32*)flags + lane * 32, RLX_AGENT);
    if (__all(v >= want)) break;
    if (spins > SPIN_CAP) {
      if (lane == 0) __hip_atomic_store((gu32*)tmo, 1u, RLX_AGENT);
      return false;
    }
  }
  __builtin_amdgcn_fence(__ATOMIC_ACQUIRE, "wavefront");
  return true;
}

#define STAMP(i)
#define STAMP_DUMP()
#define WSTAMP(i)
template <int CTRL> __device__ __forceinline__ float dppf(float old, float x) {
  return __builtin_bit_cast(float, __builtin_amdgcn_update_dpp(__builtin_bit_cast(int, old), __builtin_bit_cast(int, x), CTRL, 0xf, 0xf, false));
}
__device__ __forceinline__ float wave_max_dpp(float x) {
  const float ninf = -3e38f;
  x = fmaxf(x, dppf<0x111>(ninf, x));
  x = fmaxf(x, dppf<0x112>(ninf, x));
  x = fmaxf(x, dppf<0x114>(ninf, x));
  x = fmaxf(x, dppf<0x118>(ninf, x));
  x = fmaxf(x, dppf<0x142>(ninf, x));
  x = fmaxf(x, dppf<0x143>(ninf, x));
  return __builtin_bit_cast(float, __builtin_amdgcn_readlane(__builtin_bit_cast(int, x), 63));
}
__device__ __forceinline__ float wave_sum_dpp(float x) {
  x += dppf<0x111>(0.f, x);
  x += dppf<0x112>(0.f, x);
  x += dppf<0x114>(0.f, x);
  x += dppf<0x118>(0.f, x);
  x += dppf<0x142>(0.f, x);
  x += dppf<0x143>(0.f, x);
  return __builtin_bit_cast(float, __builtin_amdgcn_readlane(__builtin_bit_cast(int, x), 63));
}
constexpr int SMS = 528;
constexpr int QS = 144;
constexpr int VS = 80;
constexpr int SOS = 144;
#define VAR_NREP 4
constexpr int NREP = VAR_NREP;
__device__ __forceinline__ float xsum16(float x) {
  auto r = __builtin_amdgcn_permlane16_swap(__float_as_uint(x), __float_as_uint(x), false, false);
  return __uint_as_float(r[0]) + __uint_as_float(r[1]);
}
__device__ __forceinline__ float xsum32(float x) {
  auto r = __builtin_amdgcn_permlane32_swap(__float_as_uint(x), __float_as_uint(x), false, false);
  return __uint_as_float(r[0]) + __uint_as_float(r[1]);
}
__device__ __forceinline__ float xmax16(float x) {
  auto r = __builtin_amdgcn_permlane16_swap(__float_as_uint(x), __float_as_uint(x), false, false);
  return fmaxf(__uint_as_float(r[0]), __uint_as_float(r[1]));
}
__device__ __forceinline__ float xmax32(float x) {
  auto r = __builtin_amdgcn_permlane32_swap(__float_as_uint(x), __float_as_uint(x), false, false);
  return fmaxf(__uint_as_float(r[0]), __uint_as_float(r[1]));
}
__device__ __forceinline__ float red_mod4(float x) {
  x += dppf<0x124>(0.f, x);
  x += dppf<0x128>(0.f, x);
  return xsum32(xsum16(x));
}
typedef __bf16 bf16x2_t __attribute__((ext_vector_type(2)));
typedef float f32x2_t __attribute__((ext_vector_type(2)));
__device__ __forceinline__ unsigned pk2(float a, float b) {
  f32x2_t v = {a, b};
  bf16x2_t r = __builtin_convertvector(v, bf16x2_t);
  return __builtin_bit_cast(unsigned, r);
}
__device__ __forceinline__ float dot2sq(const unsigned x, const float acc) {
  return __builtin_amdgcn_fdot2_f32_bf16(__builtin_bit_cast(bf16x2_t, x), __builtin_bit_cast(bf16x2_t, x), acc, false);
}
__device__ __forceinline__ float lo16(unsigned u) { return __uint_as_float(u << 16); }
__device__ __forceinline__ float hi16(unsigned u) { return __uint_as_float(u & 0xffff0000u); }
__device__ __forceinline__ void scale_pair(float a, float b, float scale, float& oa, float& ob) {
  unsigned u = pk2(a, b);
  unsigned v = pk2(lo16(u) * scale, hi16(u) * scale);
  oa = lo16(v);
  ob = hi16(v);
}

#define XSPLIT 3
__device__ __forceinline__ int role_no() {
  const int b = blockIdx.x, x = b & 7, j = b >> 3;
  if (XSPLIT >= 3) {
    const int y = XSPLIT == 5 ? x & 3 : x >> 1;
    if (XSPLIT == 5 ? x < 4 : !(x & 1)) {
      if (j >= 16) return 104 + 16 * y + j - 16;
      return XSPLIT == 4 ? 16 * y + j : (j & 8) * 4 + 8 * y + (j & 7);
    }
    if (j < 16) return 168 + (j & 8) * 4 + 8 * y + (j & 7);
    const int i = 4 * y + (j & 3);
    return j < 20 ? 64 + i : j < 24 ? (i < 8 ? 96 + i : 224 + i) : j < 28 ? 80 + i : 240 + i;
  }
  const int L = x >> 2, xx = x & 3;
  if (j < 8) return 32 * L + 8 * xx + j;
  if (j < 12) return 64 + 16 * L + 4 * xx + j - 8;
  if (j < 16) { const int i = 4 * xx + j - 12; return L ? 240 + i : i < 8 ? 96 + i : 224 + i; }
  if (j < 24) return 104 + 8 * x + j - 16;
  return 168 + 32 * L + 8 * xx + j - 24;
}
#define MSTAMP()
#define WSTAMP2()
#define SST(k)
#define TSR(rn, k, ss, t)
#define TS(k, ss, t)
#define FSD
#define FS(k)
#define FD(ss)
struct MegaArgs {
  const u16* mem0;
  const u16* Dskip;
  char* ws;
  u16* out;
  const u16* x0;
  const u16 *logAre, *Aim, *Cre, *Cim, *logdt;
  const u16 *Wq, *Wk, *Wv, *Wo;
  int rn;
};

__device__ __forceinline__ int lane_id() {
  int l;
  asm volatile("v_mbcnt_lo_u32_b32 %0, -1, 0\n\tv_mbcnt_hi_u32_b32 %0, -1, %0" : "=v"(l));
  return l;
}
__device__ __forceinline__ bool spin_timeout(unsigned spins, unsigned* tmo) {
  if (spins > SPIN_CAP) {
    if (lane_id() == 0) __hip_atomic_store((gu32*)tmo, 1u, RLX_AGENT);
    return true;
  }
  return false;
}
#define PSWH (h & 2)
#define RR 0
#define RQM 63
#define RPM 63
#define CD0 144
#define APN 8
#define APL 300
#define APA 130
#define APB 90
#define APP 224
#define CD 20
#define GP 230
#define GR 50
#define GM 0
#define ROT (4 * (h & 1))
#define CPRIO 1
#define P1PRIO 3
#define QLEAD 2
#define POLL_SLEEP 12
#define SLP_C POLL_SLEEP
#define SLP_A POLL_SLEEP
#define SLP_W POLL_SLEEP
#define SLP_R 4
#define SLP_V 4
#define SLP_Q 4
#define SLP_M 4
#define AWPRIO 2
#define SLP_S 8
#define SPOS 4
#define CVPRE 0
#define QHV 0
#define QCHK 0
#define LEADN 99
#define GATE_V 160
#define GATE_R 380
#define GATE_N 440
#define AWAHEAD 1
#define GATE_M 150
#define GATE_M2 380
#define QPACE 0
#define QPS 6
#define QPL 100
#define GATE_Q 170
#define SLP_Q1 24
#define QPF 3
#define QUP 7
#define QEP 3
#define AWD 11
#define GPER 218
#define GMA 60
#define GMR 60
#define GSTEP 30
#define GATE_RT 200
#define GATE_1 160
#define N1S 20
#define N1E 36
#define PRI_A 3
#define PRI_WA 2
#define PRI_WB 1
#define AWSKIP 3
#define PRI_0 3
#define AWDEF 1
#define SLP_F 2
#define EXP_OFF 0
#define AWSPEC 1
#define CVTHR 48
#define NR_C 8
#define NR_R 8
#define NR_W 8
#define NR_N 2
#define QAHEAD 0
#define NS0 4
#define SLP_N POLL_SLEEP
template <int SLP, int WPL>
__device__ __forceinline__ bool wait_n(const unsigned* f, int n, unsigned* tmo) {
  const int l = lane_id();
  for (unsigned spins = 0;; ++spins) {
    unsigned v = 1u;
    if (l < n) v = __hip_atomic_load((gu32*)f + (l / WPL) * 32 + l % WPL, RLX_AGENT);
    if (__all(v != 0u)) break;
    if (spin_timeout(spins, tmo)) return false;
    __builtin_amdgcn_s_sleep(SLP);
  }
  asm volatile("" ::: "memory");
  return true;
}
__device__ __forceinline__ unsigned now10() { return (unsigned)__builtin_amdgcn_s_memrealtime(); }
__device__ __forceinline__ void gate(const unsigned t) {
  for (int i = 0; i < 64; ++i) {
    const int d = (int)(t - now10());
    if (d <= 0) break;
    if (d > 100) __builtin_amdgcn_s_sleep(32);
    else if (d > 25) __builtin_amdgcn_s_sleep(8);
    else __builtin_amdgcn_s_sleep(2);
  }
}
template <int SLP, int WPL>
__device__ __forceinline__ bool grp_wait(const bool leader, volatile unsigned* sw, const unsigned val, const unsigned* f, int n,
                                         unsigned* tmo, const unsigned t) {
  unsigned spins = 0;
  gate(t);
  if (leader) {
    const int l = lane_id();
    for (;; ++spins) {
      unsigned v = 1u;
      if (l < n) v = __hip_atomic_load((gu32*)f + (l / WPL) * 32 + l % WPL, RLX_AGENT);
      if (__all(v != 0u) || spin_timeout(spins, tmo)) break;
      __builtin_amdgcn_s_sleep(SLP);
    }
    if ((threadIdx.x & 63) == 0) *sw = 2u * val + (spins ? 1u : 0u);
  } else {
    unsigned v;
    for (; (v = __builtin_amdgcn_readfirstlane(*sw)) < 2u * val; ++spins) {
      if (spins > 64u * SPIN_CAP) break;
      __builtin_amdgcn_s_sleep(SLP_F);
    }
    spins = v & 1u;
  }
  asm volatile("" ::: "memory");
  return spins != 0u;
}
template <int SLP, int WPL>
__device__ __forceinline__ bool wg_wait(const unsigned* f, int n, unsigned* tmo) {
  if (threadIdx.x < 64) (void)wait_n<SLP, WPL>(f, n, tmo);
  __syncthreads();
  asm volatile("" ::: "memory");
  return true;
}
__device__ __forceinline__ void set_flag(unsigned* f) { __hip_atomic_store((gu32*)f, 1u, RLX_AGENT); }
#define DRAIN() asm volatile("s_waitcnt vmcnt(0)" ::: "memory")
#define MKRS(ptr, bytes) __builtin_amdgcn_make_buffer_rsrc((void*)(ptr), 0, (int)(bytes), 0x00020000)
typedef unsigned v2u __attribute__((ext_vector_type(2)));
#define LD128(rs, voff, soff) __builtin_amdgcn_raw_buffer_load_b128(rs, voff, soff, AUX_SC1)
#define LD64(rs, voff, soff) __builtin_amdgcn_raw_buffer_load_b64(rs, voff, soff, AUX_SC1)
#define ST128(v, rs, voff, soff)                                              \
  do {                                                                        \
    const v4u st_d_ = (v);                                                    \
    __builtin_amdgcn_raw_buffer_store_b128(st_d_, rs, voff, soff, AUX_SC1);   \
    asm volatile("s_nop 1" ::"v"(st_d_) : "memory");                          \
  } while (0)
#define ST64(v, rs, voff, soff) __builtin_amdgcn_raw_buffer_store_b64(v, rs, voff, soff, AUX_SC1)
#define BF8(x) __builtin_bit_cast(bf16x8, x)
#define MFMA16(a, b, c) __builtin_amdgcn_mfma_f32_16x16x32_bf16(a, b, c, 0, 0, 0)
typedef short bf16x4 __attribute__((ext_vector_type(4)));
#define MFMA16K(a, b, c) \
  __builtin_amdgcn_mfma_f32_16x16x16bf16_1k(__builtin_bit_cast(bf16x4, a), __builtin_bit_cast(bf16x4, b), c, 0, 0, 0)
__device__ __forceinline__ float fmax_nn(float a, float b) { return __builtin_elementwise_maximum(a, b); }
typedef __bf16 bfv8 __attribute__((ext_vector_type(8)));
typedef __bf16 bfv16 __attribute__((ext_vector_type(16)));
typedef unsigned v8u __attribute__((ext_vector_type(8)));
#define SMF(a, b, c, idx) \
  __builtin_amdgcn_smfmac_f32_16x16x64_bf16(__builtin_bit_cast(bfv8, a), __builtin_bit_cast(bfv16, b), c, idx, 0, 0)
__device__ __forceinline__ float swap_add32(float a, float b) {
  auto r = __builtin_amdgcn_permlane32_swap(__float_as_uint(a), __float_as_uint(b), false, false);
  return __uint_as_float(r[0]) + __uint_as_float(r[1]);
}
__device__ __forceinline__ float swap_add16(float a, float b) {
  auto r = __builtin_amdgcn_permlane16_swap(__float_as_uint(a), __float_as_uint(b), false, false);
  return __uint_as_float(r[0]) + __uint_as_float(r[1]);
}
__device__ __forceinline__ v4u smf_fix(const v4u d) {
  auto x = __builtin_amdgcn_permlane16_swap(d.x, d.z, false, false);
  auto y = __builtin_amdgcn_permlane16_swap(d.y, d.w, false, false);
  return v4u{x[0], x[1], y[0], y[1]};
}
__device__ __forceinline__ v8u smf_bfrag(const u16* p) {
  const v2u a = *reinterpret_cast<const v2u*>(p), b = *reinterpret_cast<const v2u*>(p + 8);
  const v2u c = *reinterpret_cast<const v2u*>(p + 32), d = *reinterpret_cast<const v2u*>(p + 40);
  return v8u{a.x, b.x, a.y, b.y, c.x, d.x, c.y, d.y};
}

#define VAR_PPA 0
#define VAR_PPB 0
#define VAR_THR 0
#define VAR_THR2 0
#define CV_LEAD 100
#define VAR_EARLY 1
#define VAR_DRAIN 1
#define VAR_D1 0
#define VAR_D2 0
#define VAR_DR2 1
#define VAR_S1 0
#define VAR_S2 0
typedef __attribute__((address_space(1))) unsigned long long gu64;
typedef __attribute__((address_space(1))) char gch;
__device__ __forceinline__ int cvt_rpi(float x) {
  int r;
  asm("v_cvt_rpi_i32_f32 %0, %1" : "=v"(r) : "v"(x));
  return r;
}
typedef short v4s __attribute__((ext_vector_type(4)));
typedef __attribute__((address_space(3))) v4s* lp4s;
__device__ __forceinline__ void glds16(const void* sbase, const unsigned voff, const unsigned lds) {
  unsigned keep;
  asm volatile("s_nop 4\n\ts_mov_b32 %0, m0\n\ts_mov_b32 m0, %3\n\ts_nop 0\n\tglobal_load_lds_dwordx4 %1, %2\n\ts_mov_b32 m0, %0"
               : "=&s"(keep)
               : "v"(voff), "s"(sbase), "s"(lds)
               : "memory");
}
__device__ __forceinline__ void glds16c(const void* sbase, const unsigned voff, const unsigned lds) {
  unsigned keep;
  asm volatile("s_nop 4\n\ts_mov_b32 %0, m0\n\ts_mov_b32 m0, %3\n\ts_nop 0\n\tglobal_load_lds_dwordx4 %1, %2 sc1\n\ts_mov_b32 m0, %0"
               : "=&s"(keep)
               : "v"(voff), "s"(sbase), "s"(lds)
               : "memory");
}
__device__ __forceinline__ v2u trd(const unsigned a) {
  return __builtin_bit_cast(v2u, __builtin_amdgcn_ds_read_tr16_b64_v4i16((lp4s)(unsigned long)a));
}
__device__ __forceinline__ unsigned lds_base(char* smem) { return (unsigned)(unsigned long)(__attribute__((address_space(3))) char*)smem; }
#define RAW_BAR()                                           \
  do {                                                      \
    asm volatile("s_waitcnt lgkmcnt(0)" ::: "memory");      \
    __builtin_amdgcn_s_barrier();                           \
    asm volatile("" ::: "memory");                          \
  } while (0)
#define WAIT_VM(n) asm volatile("s_waitcnt vmcnt(" #n ")" ::: "memory")
template <bool SP>
__device__ __forceinline__ void wqkv_issue(const u16* Wm, const int hf, const int h, const int w, const int l, const unsigned sb) {
  const char* base = (const char*)Wm + (size_t)(256 * hf + 32 * w) * (HD * 2) + h * (DH * 2);
#pragma unroll
  for (int i = 0; i < 8; ++i) {
    const int r = l >> 4;
    const int f = SP ? ((r & 1) | ((i & 1) << 1) | (((i >> 1) & 1) << 2)) : (r | (((i >> 1) & 1) << 2));
    glds16(base, (unsigned)((4 * i + r) * (HD * 2) + (((l & 15) ^ (f << 1)) << 4)), sb + w * 16384 + hf * 8192 + i * 1024);
  }
}

#define PFW 1
__device__ __forceinline__ void pull_weights(const MegaArgs& A, char* smem, const int slot, const int slots) {
  if (!PFW) return;
  const int x = blockIdx.x & 7, j = blockIdx.x >> 3, h = 8 * (x >> 1) + (j & 7), k = (j >> 3) - 1, l = lane_id();
  const bool odd = x & 1, wo = !odd && k == 1;
  const unsigned voff = wo ? l * 64 : (l >> 2) * (HD * 2) + (l & 3) * 64;
  const unsigned dst = lds_base(smem) + 162816;
  unsigned long long pk = (unsigned long long)A.Wk, pq = (unsigned long long)A.Wq, pv = (unsigned long long)A.Wv, po = (unsigned long long)A.Wo;
  asm volatile("" : "+s"(pk), "+s"(pq), "+s"(pv), "+s"(po));
  for (int i = odd ? slot * 3 + k : slot; (PFW & 1) && i < (odd ? 64 : 32); i += odd ? 3 * slots : slots) {
    const unsigned long long m = odd ? (i < 32 ? pk : pv) : (k == 0 ? pq : pk);
    const unsigned long long b = wo ? po + h * (DH * DM * 2) + i * 4096 : m + (i & 31) * (16 * HD * 2) + h * (DH * 2);
    unsigned keep;
    asm volatile("s_nop 4\n\ts_mov_b32 %0, m0\n\ts_mov_b32 m0, %3\n\ts_nop 0\n\tglobal_load_lds_dword %1, %2\n\ts_mov_b32 m0, %0"
                 : "=&s"(keep)
                 : "v"(voff), "s"(b), "s"(dst)
                 : "memory");
  }
  if (PFW & 2) {
    const unsigned long long b = po + (size_t)h * (DH * DM * 2);
    for (int i = slot * 3 + k; i < 32; i += 3 * slots) {
      unsigned keep;
      asm volatile("s_nop 4\n\ts_mov_b32 %0, m0\n\ts_mov_b32 m0, %3\n\ts_nop 0\n\tglobal_load_lds_dword %1, %2\n\ts_mov_b32 m0, %0"
                   : "=&s"(keep)
                   : "v"(l * 64), "s"(b + i * 4096), "s"(dst)
                   : "memory");
    }
  }
}
__device__ __forceinline__ void wo_issue(const u16* Wo, const int a, const int st, const unsigned sb) {
  const int w = __builtin_amdgcn_readfirstlane(threadIdx.x >> 6), l = lane_id(), c = l & 15, g = l >> 4;
  const char* base = (const char*)Wo + ((size_t)((2 * a + (st >> 1)) * DH + 64 * (w & 1) + 32 * (st & 1)) * DM + 128 * (w >> 1)) * 2;
#pragma unroll
  for (int i = 0; i < 8; ++i) {
    const int fd = g | ((i & 1) << 2);
    glds16(base, (unsigned)((4 * i + g) * (DM * 2) + ((c ^ (fd << 1)) << 4)), sb + (st & 1) * 65536 + w * 8192 + i * 1024);
  }
}
__device__ __forceinline__ bool zero_seen(const v4u t, const v2u e) {
  const unsigned long long m = (((unsigned long long)e.y << 32) | e.x) + TAG_STEP;
  const unsigned lo = (unsigned)m, hi = (unsigned)(m >> 32);
  return __all(((t.x ^ lo) | (t.y ^ hi) | (t.z ^ lo) | (t.w ^ hi)) == 0u);
}
__device__ __forceinline__ void zero_wait(char* ws) {
  const __amdgpu_buffer_rsrc_t rs = MKRS(ws + OFF_TAG, 8192 + 8);
  for (unsigned spins = 0; spins < 16u * SPIN_CAP; ++spins) {
    const v4u t = LD128(rs, lane_id() * 16, (blockIdx.x & 7) * 1024);
    const v2u e = LD64(rs, 0, 8192);
    if (zero_seen(t, e)) break;
    asm volatile("s_sleep 4" ::: "memory");
  }
  asm volatile("" ::: "memory");
}
__device__ __forceinline__ unsigned row_stats(const v4u raw) {
  float v[8];
#pragma unroll
  for (int e = 0; e < 4; ++e) { v[2 * e] = lo16(raw[e]); v[2 * e + 1] = hi16(raw[e]); }
  float s = 0.f;
#pragma unroll
  for (int e = 0; e < 8; ++e) s += v[e];
  const float mean = wave_sum(s) * (1.0f / DM);
  float q = 0.f;
#pragma unroll
  for (int e = 0; e < 8; ++e) { const float t = v[e] - mean; q += t * t; }
  const float var = wave_sum(q) * (1.0f / DM);
  const float sd = bfr(sqrtf(bfr(bfr(var) + 1e-5f)));
  return (unsigned)f2bf(mean) | ((unsigned)f2bf(sd) << 16);
}
__device__ __forceinline__ void start_kgen(const MegaArgs& A, char* smem, const int hi, const int xi, const int aw) {
  const int tid = threadIdx.x, j = tid >> 7, u = tid & 127, d = 4 * hi + j;
  const int w = __builtin_amdgcn_readfirstlane(tid >> 6), l = tid & 63;
  float* prm = reinterpret_cast<float*>(smem) + j * 10 * NST;
  float* part = reinterpret_cast<float*>(smem) + 40 * NST + j * 8 * 400;
  unsigned* kd = reinterpret_cast<unsigned*>(smem + 131072) + j * 392;
  v4u raw[8], ra, rb;
  const int r0 = 16 * xi + 2 * w;
  if (xi >= 0) {
    ra = *reinterpret_cast<const v4u*>(A.x0 + (size_t)r0 * DM + l * 8);
    rb = *reinterpret_cast<const v4u*>(A.x0 + (size_t)(r0 + 1) * DM + l * 8);
  }
  const u16 p0 = A.logdt[d], p1 = A.logAre[d * NST + u], p2 = A.Aim[d * NST + u], p3 = A.Cre[d * NST + u], p4 = A.Cim[d * NST + u];
  if (w >= 6) {
#pragma unroll
    for (int i = 0; i < 8; ++i) raw[i] = *reinterpret_cast<const v4u*>(A.x0 + (size_t)(16 * hi + 8 * (w - 6) + i) * DM + l * 8);
  }
  const v2u en = LD64(MKRS(A.ws + OFF_TAG, 8192 + 8), 0, 8192);
  {
    const __amdgpu_buffer_rsrc_t rz = MKRS(A.ws + OFF_Z, Z_BYTES);
    const unsigned i0 = hi < 16 ? ACC_LO : 0u, i1 = hi < 16 ? ACC_HI : 0u;
    ST128((v4u{i0, i1, i0, i1}), rz, tid * 16, hi * 8192);
    ST128((v4u{0u, 0u, 0u, 0u}), rz, tid * 16, (128 + hi) * 8192);
  }
  if (xi >= 0) {
    const unsigned sa = row_stats(ra), sb = row_stats(rb), st = (l & 1) ? sb : sa;
    const unsigned long long m = (((unsigned long long)en.y << 32) | en.x) + TAG_STEP;
    ST128((v4u{st, (unsigned)m, st, (unsigned)(m >> 32)}), MKRS(A.ws + OFF_G0, 8192), (l >> 1) * 2048 + (l & 1) * 16, r0 * 16);
    SST(11);
  }
  {
    const float dt = bfr(expf(bf2f(p0)));
    const float ar = -bfr(expf(bf2f(p1))) * dt, ai = bf2f(p2) * dt;
    prm[u] = ar;
    prm[NST + u] = ai;
    prm[2 * NST + u] = bf2f(p3) * dt;
    prm[3 * NST + u] = bf2f(p4) * dt;
#pragma unroll
    for (int cls = 1; cls < 4; ++cls) {
      const float st = (float)(1 << cls);
      const float e = expf(ar * st);
      float sn, cs;
      sincosf(ai * st, &sn, &cs);
      prm[(2 + 2 * cls) * NST + u] = e * cs;
      prm[(3 + 2 * cls) * NST + u] = e * sn;
    }
  }
  __builtin_amdgcn_s_waitcnt(0x0F70);
  __syncthreads();
  SST(1);
  if (w >= 6) {
    if (w == 7 && l < 8)
      __hip_atomic_store((gu64*)(A.ws + OFF_TAG) + l * 128 + hi, (((unsigned long long)en.y << 32) | en.x) + TAG_STEP, RLX_AGENT);
    __builtin_amdgcn_s_setprio(3);
    if (hi >= 32) pull_weights(A, smem, w - 6, 2);
    v4u wr[32];
    const u16* wop = A.Wo + (size_t)(hi * DH + 64 * (w - 6)) * DM + l * 8;
    if (hi < 32) {
#pragma unroll
      for (int i = 0; i < 32; ++i) wr[i] = *reinterpret_cast<const v4u*>(wop + i * DM);
    }
    unsigned stw = 0u;
#pragma unroll
    for (int i = 0; i < 8; ++i) {
      const unsigned t = row_stats(raw[i]);
      if (l == i) stw = t;
    }
    const int sx = 2 * hi + w - 6;
    if (l < 8) __hip_atomic_store((gu32*)(A.ws + OFF_GT) + 8 * sx + l, stw, RLX_AGENT);
    zero_wait(A.ws);
    DRAIN();
    if (l < 4) set_flag((unsigned*)(A.ws + OFF_FL) + FL_FS + (l * 32 + (sx >> 3)) * 32 + (sx & 7));
    if (hi < 32) {
      float c0 = 0.f, c1 = 0.f, c2 = 0.f, c3 = 0.f;
#pragma unroll
      for (int i = 0; i < 64; ++i) {
        const v4u x = wr[i & 31];
        if (i < 32) wr[i] = *reinterpret_cast<const v4u*>(wop + (32 + i) * DM);
        c0 = dot2sq(x.x, c0); c1 = dot2sq(x.y, c1); c2 = dot2sq(x.z, c2); c3 = dot2sq(x.w, c3);
      }
      const float m = wave_max_dpp(fmaxf(fmaxf(c0, c1), fmaxf(c2, c3)));
      if (l < 4) __hip_atomic_store((gu32*)(A.ws + OFF_FL) + FL_G + (l * 4 + w - 5) * 32 + hi, __float_as_uint(m) | 1u, RLX_AGENT);
    }
    __builtin_amdgcn_s_setprio(0);
  } else {
    const int jm = tid / 96, um = tid % 96;
    const int r = um % 12, ng = um / 12;
    const float* prm = reinterpret_cast<float*>(smem) + jm * 10 * NST;
    float* part = reinterpret_cast<float*>(smem) + 40 * NST + jm * 8 * 400;
    const float* pw = prm + (4 + 2 * (r >> 2)) * NST;
    const float p0 = pos_of_index(256 + 32 * r);
    f32x2_t acc[33];
#pragma unroll
    for (int i = 0; i < 33; ++i) acc[i] = f32x2_t{0.f, 0.f};
#pragma unroll 1
    for (int bt = 0; bt < 4; ++bt) {
      const int n = 16 * ng + 4 * bt;
      const f32x4 ar = *reinterpret_cast<const f32x4*>(prm + n), ai = *reinterpret_cast<const f32x4*>(prm + NST + n);
      const f32x4 cr = *reinterpret_cast<const f32x4*>(prm + 2 * NST + n), ci = *reinterpret_cast<const f32x4*>(prm + 3 * NST + n);
      const f32x4 wr = *reinterpret_cast<const f32x4*>(pw + n), wi = *reinterpret_cast<const f32x4*>(pw + NST + n);
      float yr0[4], yi0[4];
#pragma unroll
      for (int k = 0; k < 4; ++k) {
        const float e = __expf(ar[k] * p0);
        float sn, cs;
        __sincosf(ai[k] * p0, &sn, &cs);
        const float zr = e * cs, zi = e * sn;
        yr0[k] = cr[k] * zr - ci[k] * zi;
        yi0[k] = cr[k] * zi + ci[k] * zr;
      }
      f32x2_t yra = {yr0[0], yr0[1]}, yia = {yi0[0], yi0[1]}, wra = {wr[0], wr[1]}, wia = {wi[0], wi[1]};
      f32x2_t yrb = {yr0[2], yr0[3]}, yib = {yi0[2], yi0[3]}, wrb = {wr[2], wr[3]}, wib = {wi[2], wi[3]};
#pragma unroll
      for (int i = 0; i < 33; ++i) {
        acc[i] += yra + yrb;
        const f32x2_t ta = yra * wra - yia * wia, tb = yrb * wrb - yib * wib;
        yia = yra * wia + yia * wra;
        yib = yrb * wib + yib * wrb;
        yra = ta;
        yrb = tb;
      }
    }
#pragma unroll
    for (int i = 0; i < 33; ++i) part[ng * 400 + 33 * r + i] = acc[i].x + acc[i].y;
  }
  __syncthreads();
  SST(2);
  for (int i = u; i < 385; i += 128) {
    const int o = i < 384 ? 33 * (i >> 5) + (i & 31) : 33 * 11 + 32;
    float v = 0.f;
#pragma unroll
    for (int g = 0; g < 8; ++g) v += part[g * 400 + o];
    v *= 2.0f;
    kd[i] = pk2(v, v - lo16(pk2(v, v)));
  }
  if (u == 1) kd[385] = 0u;
  __syncthreads();
  if (aw >= 0) {
    wo_issue(A.Wo, aw, 0, lds_base(smem));
    wo_issue(A.Wo, aw, 1, lds_base(smem));
  }
  const __amdgpu_buffer_rsrc_t rs = MKRS(A.ws + OFF_KF, DM * 2 * CKN * 2);
  for (int un = u; un < 229; un += 128) {
    v4u oh, ol;
#pragma unroll
    for (int e = 0; e < 4; ++e) {
      const int lag = 2080 - (8 * un + 2 * e);
      const unsigned pb = pk2((float)lag, (float)(lag - 1));
      const unsigned k0 = kd[(unsigned)(lag - 256) < L_SEQ - 256u ? (pb & 0xffffu) - 0x4380u : 385u];
      const unsigned k1 = kd[(unsigned)(lag - 257) < L_SEQ - 256u ? (pb >> 16) - 0x4380u : 385u];
      oh[e] = __builtin_amdgcn_perm(k1, k0, 0x05040100u);
      ol[e] = __builtin_amdgcn_perm(k1, k0, 0x07060302u);
    }
    ST128(oh, rs, (d * 2 * CKN + 8 * un) * 2, 0);
    ST128(ol, rs, (d * 2 * CKN + CKN + 8 * un) * 2, 0);
  }
  DRAIN();
  __syncthreads();
  if (tid == 0) set_flag((unsigned*)(A.ws + OFF_FL) + FL_KG + (hi >> 1) * 32 + (hi & 1));
  SST(3);
}

constexpr int C_SM = 131072, C_SO = C_SM + 8192, C_KT = C_SO + 2048, C_X = C_KT + 16384, C_SQ = C_X + 1024;
constexpr int C_SK = C_SQ + (7 * QS + 128) * 2;
static_assert(C_SK + 8 * QS * 2 <= 163840, "lds");
#define CHAIN_ASM_ATT \
  "s_mov_b32 s66, %[d0]\n" \
  "s_mov_b32 s94, 576\n" \
  "s_mov_b32 s95, 288\n" \
  "s_mov_b32 s84, 0x43400000\n" \
  "s_mov_b32 s85, 0x43400000\n" \
  "s_mov_b32 s91, 0x4ca00000\n" \
  "s_mov_b32 s92, 0x88000000\n" \
  "s_mov_b32 s93, 0x87fffff7\n" \
  "s_mov_b32 s78, 0x3e0293ee\n" \
  "s_mov_b32 s76, 0xff00ff00\n" \
  "s_mov_b32 s77, 0xff00ff00\n" \
  "s_add_u32 s58, s56, s60\n" \
  "s_addc_u32 s59, s57, 0\n" \
  "s_xor_b32 s38, s35, 0\n" \
  "s_add_u32 s26, s58, s38\n" \
  "s_addc_u32 s27, s59, 0\n" \
  "s_xor_b32 s38, s35, 1024\n" \
  "s_add_u32 s28, s58, s38\n" \
  "s_addc_u32 s29, s59, 0\n" \
  "s_xor_b32 s38, s35, 2048\n" \
  "s_add_u32 s30, s58, s38\n" \
  "s_addc_u32 s31, s59, 0\n" \
  "s_xor_b32 s38, s35, 3072\n" \
  "s_add_u32 s32, s58, s38\n" \
  "s_addc_u32 s33, s59, 0\n" \
  "s_lshr_b32 s98, s60, 2\n" \
  "s_add_u32 s98, s98, s74\n" \
  "s_add_u32 s98, s98, 131072\n" \
  "s_lshl_b32 s97, s73, 5\n" \
  "s_add_u32 s97, s97, s74\n" \
  "s_add_u32 s97, s97, 158720\n" \
  "s_lshl_b32 s99, s73, 10\n" \
  "v_mbcnt_lo_u32_b32 v48, -1, 0\n" \
  "v_mbcnt_hi_u32_b32 v48, -1, v48\n" \
  "v_and_b32 v49, 15, v48\n" \
  "v_lshl_add_u32 v9, v49, 1, s97\n" \
  "v_lshrrev_b32 v49, 4, v48\n" \
  "v_mad_u32_u24 v9, v49, s94, v9\n" \
  "v_lshrrev_b32 v49, 5, v48\n" \
  "v_add_u32 v10, s99, v1\n" \
  "s_lshr_b32 s88, s99, 2\n" \
  "s_add_u32 s88, s88, s74\n" \
  "s_add_u32 s88, s88, 139264\n" \
  "v_lshl_add_u32 v7, v49, 10, s88\n" \
  "v_and_b32 v49, 31, v48\n" \
  "v_lshl_add_u32 v7, v49, 3, v7\n" \
  "v_mov_b64 v[12:13], 0\n" \
  "v_mov_b64 v[14:15], 0\n" \
  "v_mov_b64 v[16:17], 0\n" \
  "v_mov_b64 v[18:19], 0\n" \
  "v_mov_b64 v[20:21], 0\n" \
  "v_mov_b64 v[22:23], 0\n" \
  "s_add_u32 s88, s74, 157760\n" \
  "v_mov_b32 v59, s88\n" \
  "v_mov_b32 v24, 0x44870000\n" \
  "v_mul_f32 v24, s72, v24\n" \
  "s_nop 0\n" \
  "v_readfirstlane_b32 s86, v24\n" \
  "s_lshr_b32 s86, s86, 23\n" \
  "s_addk_i32 s86, 0xff83\n" \
  "s_ashr_i32 s86, s86, 1\n" \
  "s_max_i32 s86, s86, -20\n" \
  "s_min_i32 s86, s86, 100\n" \
  "s_mov_b32 s82, s86\n" \
  "s_lshr_b32 s83, s72, 23\n" \
  "s_sub_i32 s83, s83, 242\n" \
  "s_lshl_b32 s88, s73, 10\n" \
  "v_add_u32 v24, s88, v1\n" \
  "ds_read_b128 v[32:35], v24 offset:0\n" \
  "s_waitcnt lgkmcnt(0)\n" \
  "v_mov_b32 v11, 0\n" \
  "v_dot2c_f32_bf16 v11, v32, v32\n" \
  "v_dot2c_f32_bf16 v11, v33, v33\n" \
  "v_dot2c_f32_bf16 v11, v34, v34\n" \
  "v_dot2c_f32_bf16 v11, v35, v35\n" \
  "s_nop 2\n" \
  "v_cmpx_lt_f32 vcc, 2.0, v11\n" \
  "ds_max_u32 v59, v11\n" \
  "s_mov_b64 exec, -1\n" \
  ".p2align 6\n" \
  ".LCA_AL_%=:\n" \
  ".LCA_TOP_%=:\n" \
  "s_waitcnt lgkmcnt(0)\n" \
  "s_barrier\n" \
  "ds_read_b128 v[40:43], v1 offset:0\n" \
  "ds_read_b128 v[44:47], v1 offset:1024\n" \
  "ds_read_b128 v[48:51], v1 offset:2048\n" \
  "ds_read_b128 v[52:55], v1 offset:3072\n" \
  "ds_read_b128 v[56:59], v1 offset:4096\n" \
  "ds_read_b128 v[60:63], v1 offset:5120\n" \
  "s_cmp_lg_u32 s73, 0\n" \
  "s_cbranch_scc1 .LCA_NOZ_%=\n" \
  "v_mov_b64 v[36:37], s[92:93]\n" \
  "s_xor_b32 s88, s60, 0x8000\n" \
  "s_add_u32 s88, s88, s61\n" \
  "buffer_store_dwordx2 v[36:37], v5, s[40:43], s88 offen sc1\n" \
  ".LCA_NOZ_%=:\n" \
  "s_waitcnt lgkmcnt(5)\n" \
  "v_smfmac_f32_16x16x64_bf16 v[20:23], v[40:43], v[192:199], v0\n" \
  "v_smfmac_f32_16x16x64_bf16 v[12:15], v[40:43], v[64:71], v0\n" \
  "v_smfmac_f32_16x16x64_bf16 v[16:19], v[40:43], v[128:135], v0\n" \
  "ds_read_b128 v[40:43], v1 offset:6144\n" \
  "s_waitcnt lgkmcnt(5)\n" \
  "v_smfmac_f32_16x16x64_bf16 v[20:23], v[44:47], v[200:207], v0\n" \
  "v_smfmac_f32_16x16x64_bf16 v[12:15], v[44:47], v[72:79], v0\n" \
  "v_smfmac_f32_16x16x64_bf16 v[16:19], v[44:47], v[136:143], v0\n" \
  "ds_read_b128 v[44:47], v1 offset:7168\n" \
  "s_waitcnt lgkmcnt(5)\n" \
  "v_smfmac_f32_16x16x64_bf16 v[20:23], v[48:51], v[208:215], v0\n" \
  "v_smfmac_f32_16x16x64_bf16 v[12:15], v[48:51], v[80:87], v0\n" \
  "v_smfmac_f32_16x16x64_bf16 v[16:19], v[48:51], v[144:151], v0\n" \
  "ds_read_b128 v[24:27], v1 offset:10240\n" \
  "ds_read_b128 v[28:31], v1 offset:14336\n" \
  "s_waitcnt lgkmcnt(6)\n" \
  "v_smfmac_f32_16x16x64_bf16 v[20:23], v[52:55], v[216:223], v0\n" \
  "v_smfmac_f32_16x16x64_bf16 v[12:15], v[52:55], v[88:95], v0\n" \
  "v_smfmac_f32_16x16x64_bf16 v[16:19], v[52:55], v[152:159], v0\n" \
  "ds_read_b128 v[32:35], v1 offset:11264\n" \
  "ds_read_b128 v[36:39], v1 offset:15360\n" \
  "s_waitcnt lgkmcnt(7)\n" \
  "v_smfmac_f32_16x16x64_bf16 v[20:23], v[56:59], v[224:231], v0\n" \
  "v_smfmac_f32_16x16x64_bf16 v[12:15], v[56:59], v[96:103], v0\n" \
  "v_smfmac_f32_16x16x64_bf16 v[16:19], v[56:59], v[160:167], v0\n" \
  "s_waitcnt lgkmcnt(6)\n" \
  "v_smfmac_f32_16x16x64_bf16 v[20:23], v[60:63], v[232:239], v0\n" \
  "v_smfmac_f32_16x16x64_bf16 v[12:15], v[60:63], v[104:111], v0\n" \
  "v_smfmac_f32_16x16x64_bf16 v[16:19], v[60:63], v[168:175], v0\n" \
  "s_waitcnt lgkmcnt(5)\n" \
  "v_smfmac_f32_16x16x64_bf16 v[20:23], v[40:43], v[240:247], v0\n" \
  "v_smfmac_f32_16x16x64_bf16 v[12:15], v[40:43], v[112:119], v0\n" \
  "v_smfmac_f32_16x16x64_bf16 v[16:19], v[40:43], v[176:183], v0\n" \
  "s_waitcnt lgkmcnt(4)\n" \
  "v_smfmac_f32_16x16x64_bf16 v[20:23], v[44:47], v[248:255], v0\n" \
  "v_smfmac_f32_16x16x64_bf16 v[12:15], v[44:47], v[120:127], v0\n" \
  "v_smfmac_f32_16x16x64_bf16 v[16:19], v[44:47], v[184:191], v0\n" \
  "s_cmp_eq_u32 s71, 0\n" \
  "s_nop 4\n" \
  "v_pk_add_f32 v[20:21], v[20:21], v[22:23]\n" \
  "v_pk_add_f32 v[12:13], v[12:13], v[14:15]\n" \
  "v_pk_add_f32 v[16:17], v[16:17], v[18:19]\n" \
  "v_cvt_pk_bf16_f32 v20, v20, v21\n" \
  "v_cvt_pk_bf16_f32 v12, v12, v13\n" \
  "ds_write_b16 v9, v20 offset:128\n" \
  "ds_write_b16_d16_hi v9, v20 offset:416\n" \
  "v_cvt_pk_bf16_f32 v16, v16, v17\n" \
  "ds_write_b16 v9, v12 offset:0\n" \
  "ds_write_b16_d16_hi v9, v12 offset:288\n" \
  "ds_write_b16 v9, v16 offset:2272\n" \
  "ds_write_b16_d16_hi v9, v16 offset:2560\n" \
  "s_setprio 3\n" \
  ".fill ((16 - (. - .LCA_AL_%=)) & 31) >> 2, 4, 0xbf800000\n" \
  "s_waitcnt lgkmcnt(0)\n" \
  "s_barrier\n" \
  "s_cbranch_scc1 .LCA_LAST_%=\n" \
  "ds_read_b32 v11, v1 offset:26688\n" \
  "ds_read_b128 v[40:43], v2 offset:0\n" \
  "ds_read_b128 v[44:47], v3 offset:0\n" \
  "ds_read_b128 v[48:51], v2 offset:64\n" \
  "ds_read_b128 v[52:55], v3 offset:64\n" \
  "ds_read_b128 v[56:59], v2 offset:128\n" \
  "ds_read_b128 v[60:63], v3 offset:128\n" \
  "s_waitcnt lgkmcnt(5)\n" \
  "v_mfma_f32_16x16x32_bf16 v[12:15], v[24:27], v[40:43], 0\n" \
  "v_mfma_f32_16x16x32_bf16 v[16:19], v[28:31], v[40:43], 0\n" \
  "v_readfirstlane_b32 s86, v11\n" \
  "ds_read_b128 v[24:27], v1 offset:12288\n" \
  "ds_read_b128 v[28:31], v1 offset:16384\n" \
  "s_waitcnt lgkmcnt(6)\n" \
  "v_mfma_f32_16x16x32_bf16 v[20:23], v[44:47], v[40:43], 0\n" \
  "s_lshr_b32 s86, s86, 23\n" \
  "s_add_i32 s86, s86, s83\n" \
  "ds_read_b128 v[40:43], v2 offset:192\n" \
  "ds_read_b128 v[44:47], v3 offset:192\n" \
  "s_waitcnt lgkmcnt(7)\n" \
  "v_mfma_f32_16x16x32_bf16 v[12:15], v[32:35], v[48:51], v[12:15]\n" \
  "v_mfma_f32_16x16x32_bf16 v[16:19], v[36:39], v[48:51], v[16:19]\n" \
  "s_ashr_i32 s86, s86, 1\n" \
  "s_max_i32 s86, s86, s82\n" \
  "ds_read_b128 v[32:35], v1 offset:13312\n" \
  "ds_read_b128 v[36:39], v1 offset:17408\n" \
  "s_waitcnt lgkmcnt(8)\n" \
  "v_mfma_f32_16x16x32_bf16 v[20:23], v[52:55], v[48:51], v[20:23]\n" \
  "s_min_i32 s86, s86, 100\n" \
  "s_lshl_b32 s86, s86, 23\n" \
  "s_add_i32 s75, s86, 0x3c800000\n" \
  "ds_read_b128 v[48:51], v10 offset:18432\n" \
  "ds_read_b128 v[52:55], v10 offset:22528\n" \
  "s_waitcnt lgkmcnt(8)\n" \
  "v_mfma_f32_16x16x32_bf16 v[20:23], v[60:63], v[56:59], v[20:23]\n" \
  "s_waitcnt lgkmcnt(6)\n" \
  "v_mfma_f32_16x16x32_bf16 v[12:15], v[24:27], v[56:59], v[12:15]\n" \
  "v_mfma_f32_16x16x32_bf16 v[16:19], v[28:31], v[56:59], v[16:19]\n" \
  "ds_read_b128 v[56:59], v6 offset:0\n" \
  "ds_read_b128 v[60:63], v6 offset:1024\n" \
  "s_waitcnt lgkmcnt(6)\n" \
  "v_mfma_f32_16x16x32_bf16 v[20:23], v[44:47], v[40:43], v[20:23]\n" \
  "ds_read_b128 v[24:27], v6 offset:2048\n" \
  "ds_read_b128 v[28:31], v6 offset:3072\n" \
  "s_waitcnt lgkmcnt(6)\n" \
  "v_mfma_f32_16x16x32_bf16 v[12:15], v[32:35], v[40:43], v[12:15]\n" \
  "v_mfma_f32_16x16x32_bf16 v[16:19], v[36:39], v[40:43], v[16:19]\n" \
  "ds_read_b128 v[32:35], v6 offset:8192\n" \
  "ds_read_b128 v[36:39], v6 offset:9216\n" \
  "s_waitcnt lgkmcnt(6)\n" \
  "v_cndmask_b32 v20, v20, v21, s[76:77]\n" \
  "v_mov_b32 v41, 0\n" \
  "v_mov_b32 v42, 0\n" \
  "s_nop 1\n" \
  "v_cndmask_b32 v12, v12, v16, s[76:77]\n" \
  "v_cndmask_b32 v13, v13, v17, s[76:77]\n" \
  "v_cndmask_b32 v14, v14, v18, s[76:77]\n" \
  "v_cndmask_b32 v15, v15, v19, s[76:77]\n" \
  "v_maximum3_f32 v16, v20, v12, v13\n" \
  "v_maximum3_f32 v16, v16, v14, v15\n" \
  "s_nop 1\n" \
  "v_max_f32_dpp v16, v16, v16 row_ror:8 row_mask:0xf bank_mask:0xf\n" \
  "v_mov_b32 v17, v16\n" \
  "s_nop 1\n" \
  "v_permlane16_swap_b32 v16, v17\n" \
  "v_max_f32 v16, v16, v17\n" \
  "v_mov_b32 v17, v16\n" \
  "s_nop 1\n" \
  "v_permlane32_swap_b32 v16, v17\n" \
  "v_max_f32 v16, v16, v17\n" \
  "v_mul_f32 v16, s78, v16\n" \
  "v_pk_fma_f32 v[12:13], v[12:13], s[78:79], v[16:17] op_sel_hi:[1,0,0] neg_lo:[0,0,1] neg_hi:[0,0,1]\n" \
  "v_pk_fma_f32 v[14:15], v[14:15], s[78:79], v[16:17] op_sel_hi:[1,0,0] neg_lo:[0,0,1] neg_hi:[0,0,1]\n" \
  "v_fma_f32 v20, v20, s78, -v16\n" \
  "v_exp_f32 v12, v12\n" \
  "v_exp_f32 v13, v13\n" \
  "v_exp_f32 v14, v14\n" \
  "v_exp_f32 v15, v15\n" \
  "v_exp_f32 v20, v20\n" \
  "s_waitcnt lgkmcnt(6)\n" \
  "v_cvt_pk_bf16_f32 v44, v12, v13\n" \
  "v_cvt_pk_bf16_f32 v45, v14, v15\n" \
  "v_pk_add_f32 v[8:9], v[12:13], v[14:15]\n" \
  "v_mov_b32_dpp v46, v44 row_ror:8 row_mask:0xf bank_mask:0xf\n" \
  "v_mov_b32_dpp v17, v20 row_ror:8 row_mask:0xf bank_mask:0xf\n" \
  "v_mov_b32_dpp v47, v45 row_ror:8 row_mask:0xf bank_mask:0xf\n" \
  "v_add_f32 v10, v8, v20\n" \
  "v_cvt_pk_bf16_f32 v40, v20, v17\n" \
  "v_add_f32 v10, v10, v9\n" \
  "v_mfma_f32_16x16x32_bf16 v[12:15], v[48:51], v[44:47], 0\n" \
  "v_cvt_pk_bf16_f32 v43, v20, v17\n" \
  "v_add_f32_dpp v10, v10, v10 row_ror:8 row_mask:0xf bank_mask:0xf\n" \
  "v_mfma_f32_16x16x32_bf16 v[16:19], v[52:55], v[44:47], 0\n" \
  "s_barrier\n" \
  "ds_read_b64 v[22:23], v7 offset:0\n" \
  "buffer_load_dword v7, v5, s[16:19], s24 offen sc1\n" \
  "v_mul_f32 v11, s75, v10\n" \
  "v_mul_f32 v10, s75, v10\n" \
  "s_nop 1\n" \
  "v_permlane16_swap_b32 v10, v11\n" \
  "v_add_f32 v10, v10, v11\n" \
  "ds_read_b128 v[48:51], v6 offset:10240\n" \
  "v_mov_b32 v11, v10\n" \
  "ds_read_b128 v[52:55], v6 offset:11264\n" \
  "v_mov_b64 v[20:21], s[84:85]\n" \
  "v_permlane32_swap_b32 v10, v11\n" \
  "v_add_f32 v10, v10, v11\n" \
  "v_rcp_f32 v10, v10\n" \
  "s_waitcnt lgkmcnt(2)\n" \
  "v_mfma_f32_16x16x16_bf16 v[12:15], v[22:23], v[40:41], v[12:15]\n" \
  "v_mfma_f32_16x16x16_bf16 v[16:19], v[22:23], v[42:43], v[16:19]\n" \
  "s_nop 4\n" \
  "v_mov_b64 v[22:23], s[84:85]\n" \
  "s_nop 0\n" \
  "v_pk_mul_f32 v[12:13], v[12:13], v[10:11] op_sel_hi:[1,0]\n" \
  "v_pk_mul_f32 v[14:15], v[14:15], v[10:11] op_sel_hi:[1,0]\n" \
  "v_pk_mul_f32 v[16:17], v[16:17], v[10:11] op_sel_hi:[1,0]\n" \
  "v_pk_mul_f32 v[18:19], v[18:19], v[10:11] op_sel_hi:[1,0]\n" \
  "v_cvt_pk_bf16_f32 v40, v12, v13\n" \
  "v_cvt_pk_bf16_f32 v41, v14, v15\n" \
  "v_cvt_pk_bf16_f32 v42, v16, v17\n" \
  "ds_write_b64 v4, v[40:41]\n" \
  "v_cvt_pk_bf16_f32 v43, v18, v19\n" \
  "ds_write_b64 v4, v[42:43] offset:1024\n" \
  "v_mov_b64 v[12:13], s[84:85]\n" \
  "v_mov_b64 v[14:15], s[84:85]\n" \
  "v_mov_b64 v[16:17], s[84:85]\n" \
  "v_mov_b64 v[18:19], s[84:85]\n" \
  "v_mov_b64 v[8:9], s[84:85]\n" \
  "v_mov_b64 v[10:11], s[84:85]\n" \
  "s_setprio 0\n" \
  "s_waitcnt lgkmcnt(0)\n" \
  "s_barrier\n" \
  "ds_read_b128 v[40:43], v1 offset:8192\n" \
  "ds_read_b128 v[44:47], v1 offset:9216\n" \
  "s_waitcnt lgkmcnt(1)\n" \
  "v_smfmac_f32_16x16x64_bf16 v[12:15], v[40:43], v[56:63], v0\n" \
  "ds_read_b128 v[56:59], v6 offset:4096\n" \
  "ds_read_b128 v[60:63], v6 offset:5120\n" \
  "v_smfmac_f32_16x16x64_bf16 v[16:19], v[40:43], v[24:31], v0\n" \
  "ds_read_b128 v[24:27], v6 offset:6144\n" \
  "ds_read_b128 v[28:31], v6 offset:7168\n" \
  "s_waitcnt lgkmcnt(4)\n" \
  "v_smfmac_f32_16x16x64_bf16 v[12:15], v[44:47], v[32:39], v0\n" \
  "ds_read_b128 v[32:35], v6 offset:12288\n" \
  "ds_read_b128 v[36:39], v6 offset:13312\n" \
  "v_smfmac_f32_16x16x64_bf16 v[16:19], v[44:47], v[48:55], v0\n" \
  "ds_read_b128 v[48:51], v6 offset:14336\n" \
  "ds_read_b128 v[52:55], v6 offset:15360\n" \
  "s_nop 0\n" \
  "s_waitcnt lgkmcnt(6)\n" \
  "v_smfmac_f32_16x16x64_bf16 v[20:23], v[40:43], v[56:63], v0\n" \
  "v_add_f32 v12, v12, v14\n" \
  "v_add_f32 v13, v13, v15\n" \
  "global_atomic_add_x2 v5, v[12:13], s[26:27]\n" \
  "s_waitcnt lgkmcnt(4)\n" \
  "v_smfmac_f32_16x16x64_bf16 v[8:11], v[40:43], v[24:31], v0\n" \
  "v_add_f32 v16, v16, v18\n" \
  "v_add_f32 v17, v17, v19\n" \
  "global_atomic_add_x2 v5, v[16:17], s[28:29]\n" \
  "s_waitcnt lgkmcnt(2)\n" \
  "v_smfmac_f32_16x16x64_bf16 v[20:23], v[44:47], v[32:39], v0\n" \
  "s_waitcnt lgkmcnt(0)\n" \
  "v_smfmac_f32_16x16x64_bf16 v[8:11], v[44:47], v[48:55], v0\n" \
  "s_nop 5\n" \
  "v_add_f32 v20, v20, v22\n" \
  "v_add_f32 v21, v21, v23\n" \
  "global_atomic_add_x2 v5, v[20:21], s[30:31]\n" \
  "v_add_f32 v8, v8, v10\n" \
  "v_add_f32 v9, v9, v11\n" \
  "global_atomic_add_x2 v5, v[8:9], s[32:33]\n" \
  "v_mbcnt_lo_u32_b32 v48, -1, 0\n" \
  "v_mbcnt_hi_u32_b32 v48, -1, v48\n" \
  "v_lshrrev_b32 v49, 3, v48\n" \
  "v_and_b32 v50, 7, v48\n" \
  "v_lshlrev_b32 v50, 4, v50\n" \
  "v_lshl_or_b32 v44, v49, 8, v50\n" \
  "s_add_u32 s20, s20, 32\n" \
  "s_mov_b32 s21, 0x8000\n" \
  "v_mov_b32 v52, s22\n" \
  "v_mov_b32 v54, s20\n" \
  "s_mul_i32 s88, s71, %[pp]\n" \
  "s_lshl_b32 s89, s71, 10\n" \
  "s_sub_u32 s88, s88, s25\n" \
  "s_mul_i32 s38, s24, %[an]\n" \
  "s_cmp_gt_u32 s89, s38\n" \
  "s_cselect_b32 s89, 0x1000, 0\n" \
  "s_waitcnt vmcnt(4)\n" \
  ".LCA_TKE_%=:\n" \
  "v_readlane_b32 s34, v7, 0\n" \
  "v_readlane_b32 s38, v7, 16\n" \
  "v_readlane_b32 s39, v7, 32\n" \
  "v_readlane_b32 s23, v7, 48\n" \
  "s_max_u32 s34, s34, s38\n" \
  "s_max_u32 s39, s39, s23\n" \
  "v_readlane_b32 s38, v7, 8\n" \
  "s_max_u32 s34, s34, s39\n" \
  "v_readlane_b32 s39, v7, 24\n" \
  "v_readlane_b32 s23, v7, 1\n" \
  "s_sub_u32 s23, s23, s34\n" \
  "s_sub_u32 s23, s23, s88\n" \
  "s_cmp_lt_u32 s23, s89\n" \
  "s_cselect_b32 s23, s23, 0\n" \
  "s_min_u32 s23, s23, %[ka]\n" \
  "s_add_u32 s34, s34, s23\n" \
  "s_add_u32 s34, s34, %[cd]\n" \
  "v_readlane_b32 s23, v7, 40\n" \
  "s_add_u32 s38, s38, s39\n" \
  "v_readlane_b32 s39, v7, 56\n" \
  "s_add_u32 s38, s38, s23\n" \
  "v_mov_b32 v55, s34\n" \
  "s_add_u32 s38, s38, s39\n" \
  "s_cmp_ge_u32 s38, s20\n" \
  "s_cbranch_scc1 .LCA_TKO_%=\n" \
  "s_sub_u32 s21, s21, 1\n" \
  "s_cbranch_scc1 .LCA_TKO_%=\n" \
  "s_sleep 2\n" \
  "buffer_load_dword v7, v5, s[16:19], s24 offen sc1\n" \
  "s_waitcnt vmcnt(0)\n" \
  "s_branch .LCA_TKE_%=\n" \
  ".LCA_TKO_%=:\n" \
  "ds_write_b64 v52, v[54:55]\n" \
  "s_branch .LCA_WAIU_%=\n" \
  ".LCA_WAIS_%=:\n" \
  "s_cmp_lt_i32 s39, -1000\n" \
  "s_cbranch_scc1 .LCA_WAIE_%=\n" \
  "s_sleep 1\n" \
  ".LCA_WAIU_%=:\n" \
  "s_memrealtime s[38:39]\n" \
  "s_waitcnt lgkmcnt(0)\n" \
  "s_sub_i32 s39, s38, s34\n" \
  "s_cmp_lt_i32 s39, -7\n" \
  "s_cbranch_scc1 .LCA_WAIS_%=\n" \
  "s_cmp_lt_i32 s39, 0\n" \
  "s_cbranch_scc1 .LCA_WAIU_%=\n" \
  ".LCA_WAIE_%=:\n" \
  "buffer_load_dwordx4 v[24:27], v44, s[40:43], s60 offen sc1\n" \
  "buffer_load_dwordx4 v[28:31], v44, s[40:43], s60 offen offset:2048 sc1\n" \
  "v_mbcnt_lo_u32_b32 v48, -1, 0\n" \
  "v_mbcnt_hi_u32_b32 v48, -1, v48\n" \
  "v_and_b32 v49, 15, v48\n" \
  "v_lshl_add_u32 v9, v49, 1, s97\n" \
  "v_lshrrev_b32 v49, 4, v48\n" \
  "v_mad_u32_u24 v9, v49, s94, v9\n" \
  "v_lshrrev_b32 v49, 5, v48\n" \
  "v_add_u32 v10, s99, v1\n" \
  "s_lshr_b32 s88, s99, 2\n" \
  "s_add_u32 s88, s88, s74\n" \
  "s_add_u32 s88, s88, 139264\n" \
  "v_lshl_add_u32 v7, v49, 10, s88\n" \
  "v_and_b32 v49, 31, v48\n" \
  "v_lshl_add_u32 v7, v49, 3, v7\n" \
  "v_mov_b64 v[12:13], 0\n" \
  "v_mov_b64 v[14:15], 0\n" \
  "v_mov_b64 v[16:17], 0\n" \
  "v_mov_b64 v[18:19], 0\n" \
  "v_mov_b64 v[20:21], 0\n" \
  "v_mov_b64 v[22:23], 0\n" \
  "s_add_u32 s88, s74, 157760\n" \
  "v_mov_b32 v59, s88\n" \
  "v_and_b32 v49, 3, v48\n" \
  "v_lshl_add_u32 v40, v49, 2, s98\n" \
  "v_lshrrev_b32 v49, 2, v48\n" \
  "v_lshl_add_u32 v40, v49, 5, v40\n" \
  "s_add_i32 s80, s86, 0x35000000\n" \
  "v_mov_b32 v11, 0\n" \
  "s_mov_b32 s89, s60\n" \
  "s_xor_b32 s60, s60, 0x80\n" \
  "s_bitcmp0_b32 s60, 7\n" \
  "s_cselect_b32 s88, 0x8000, 0\n" \
  "s_add_u32 s60, s60, s88\n" \
  "s_and_b32 s60, s60, 0xffff\n" \
  "s_add_u32 s58, s56, s60\n" \
  "s_addc_u32 s59, s57, 0\n" \
  "s_xor_b32 s38, s35, 0\n" \
  "s_add_u32 s26, s58, s38\n" \
  "s_addc_u32 s27, s59, 0\n" \
  "s_xor_b32 s38, s35, 1024\n" \
  "s_add_u32 s28, s58, s38\n" \
  "s_addc_u32 s29, s59, 0\n" \
  "s_xor_b32 s38, s35, 2048\n" \
  "s_add_u32 s30, s58, s38\n" \
  "s_addc_u32 s31, s59, 0\n" \
  "s_xor_b32 s38, s35, 3072\n" \
  "s_add_u32 s32, s58, s38\n" \
  "s_addc_u32 s33, s59, 0\n" \
  "s_sub_u32 s71, s71, 1\n" \
  "s_mov_b32 s87, 0x40000\n" \
  "s_branch .LCA_CHK_%=\n" \
  ".LCA_POLL_%=:\n" \
  "buffer_load_dwordx4 v[24:27], v44, s[40:43], s89 offen sc1\n" \
  "buffer_load_dwordx4 v[28:31], v44, s[40:43], s89 offen offset:2048 sc1\n" \
  ".LCA_CHK_%=:\n" \
  "s_sub_u32 s87, s87, 1\n" \
  "s_cbranch_scc1 .LCA_END_%=\n" \
  "s_waitcnt vmcnt(1)\n" \
  "v_cvt_f32_i32 v32, v24\n" \
  "v_cvt_f32_i32 v34, v26\n" \
  "v_cvt_f32_i32 v33, v25\n" \
  "v_cvt_f32_i32 v35, v27\n" \
  "v_max_f32 v45, |v32|, |v34|\n" \
  "v_pk_mul_f32 v[32:33], v[32:33], s[80:81] op_sel_hi:[1,0]\n" \
  "v_pk_mul_f32 v[34:35], v[34:35], s[80:81] op_sel_hi:[1,0]\n" \
  "s_nop 0\n" \
  "v_cvt_pk_bf16_f32 v32, v32, v34\n" \
  "v_cvt_pk_bf16_f32 v33, v33, v35\n" \
  "ds_write2_b32 v40, v32, v33 offset0:0 offset1:4\n" \
  "s_waitcnt vmcnt(0)\n" \
  "v_cvt_f32_i32 v36, v28\n" \
  "v_cvt_f32_i32 v38, v30\n" \
  "v_cvt_f32_i32 v37, v29\n" \
  "v_cvt_f32_i32 v39, v31\n" \
  "v_max3_f32 v45, |v36|, |v38|, v45\n" \
  "v_pk_mul_f32 v[36:37], v[36:37], s[80:81] op_sel_hi:[1,0]\n" \
  "v_pk_mul_f32 v[38:39], v[38:39], s[80:81] op_sel_hi:[1,0]\n" \
  "v_cmp_lt_f32 vcc, s91, v45\n" \
  "v_cvt_pk_bf16_f32 v36, v36, v38\n" \
  "v_cvt_pk_bf16_f32 v37, v37, v39\n" \
  "ds_write2_b32 v40, v36, v37 offset0:128 offset1:132\n" \
  "s_cmp_eq_u64 vcc, 0\n" \
  "s_cbranch_scc0 .LCA_POLL_%=\n" \
  "v_dot2c_f32_bf16 v11, v32, v32\n" \
  "v_dot2c_f32_bf16 v11, v33, v33\n" \
  "v_dot2c_f32_bf16 v11, v36, v36\n" \
  "v_dot2c_f32_bf16 v11, v37, v37\n" \
  "s_nop 2\n" \
  "v_cmpx_lt_f32 vcc, 2.0, v11\n" \
  "ds_max_u32 v59, v11\n" \
  "s_mov_b64 exec, -1\n" \
  "s_branch .LCA_TOP_%=\n" \
  ".LCA_LAST_%=:\n" \
  ".LCA_END_%=:\n" \
  "s_endpgm\n"
#define CHAIN_ASM_VO \
  "s_mov_b32 s66, %[d0]\n" \
  "s_mov_b32 s94, 576\n" \
  "s_mov_b32 s95, 288\n" \
  "s_mov_b32 s84, 0x43400000\n" \
  "s_mov_b32 s85, 0x43400000\n" \
  "s_mov_b32 s91, 0x4ca00000\n" \
  "s_mov_b32 s92, 0x88000000\n" \
  "s_mov_b32 s93, 0x87fffff7\n" \
  "s_mov_b32 s78, 0x3e0293ee\n" \
  "s_mov_b32 s76, 0xff00ff00\n" \
  "s_mov_b32 s77, 0xff00ff00\n" \
  "s_add_u32 s58, s56, s60\n" \
  "s_addc_u32 s59, s57, 0\n" \
  "s_xor_b32 s38, s35, 0\n" \
  "s_add_u32 s26, s58, s38\n" \
  "s_addc_u32 s27, s59, 0\n" \
  "s_xor_b32 s38, s35, 1024\n" \
  "s_add_u32 s28, s58, s38\n" \
  "s_addc_u32 s29, s59, 0\n" \
  "s_xor_b32 s38, s35, 2048\n" \
  "s_add_u32 s30, s58, s38\n" \
  "s_addc_u32 s31, s59, 0\n" \
  "s_xor_b32 s38, s35, 3072\n" \
  "s_add_u32 s32, s58, s38\n" \
  "s_addc_u32 s33, s59, 0\n" \
  "s_lshr_b32 s98, s60, 2\n" \
  "s_add_u32 s98, s98, s74\n" \
  "s_add_u32 s98, s98, 131072\n" \
  "s_lshl_b32 s97, s73, 5\n" \
  "s_add_u32 s97, s97, s74\n" \
  "s_add_u32 s97, s97, 160992\n" \
  "s_lshl_b32 s99, s73, 11\n" \
  "s_add_u32 s99, s99, s74\n" \
  "s_add_u32 s99, s99, 133120\n" \
  "v_mbcnt_lo_u32_b32 v48, -1, 0\n" \
  "v_mbcnt_hi_u32_b32 v48, -1, v48\n" \
  "s_sub_u32 s88, s73, 4\n" \
  "v_and_b32 v51, 15, v48\n" \
  "v_lshl_add_u32 v4, v51, 1, s97\n" \
  "v_lshrrev_b32 v49, 4, v48\n" \
  "v_mad_u32_u24 v4, v49, s94, v4\n" \
  "v_and_b32 v49, 1, v49\n" \
  "v_lshrrev_b32 v50, 5, v48\n" \
  "v_lshlrev_b32 v8, 3, v50\n" \
  "v_lshl_add_u32 v8, v51, 4, v8\n" \
  "v_lshl_add_u32 v8, v49, 10, v8\n" \
  "s_lshl_b32 s89, s88, 8\n" \
  "s_add_u32 s89, s89, s74\n" \
  "s_add_u32 s89, s89, 139264\n" \
  "v_and_b32 v49, 31, v48\n" \
  "v_lshl_add_u32 v5, v49, 3, s89\n" \
  "v_lshl_add_u32 v5, v50, 10, v5\n" \
  "v_lshrrev_b32 v49, 2, v51\n" \
  "v_and_b32 v50, 5, v51\n" \
  "v_and_or_b32 v49, v49, 2, v50\n" \
  "s_lshl_b32 s89, s88, 6\n" \
  "s_add_u32 s89, s89, s74\n" \
  "s_add_u32 s89, s89, 160992\n" \
  "v_and_b32 v50, 48, v48\n" \
  "v_add_u32 v6, s89, v50\n" \
  "v_mad_u32_u24 v6, v49, s95, v6\n" \
  "v_lshlrev_b32 v7, 3, v50\n" \
  "v_lshl_add_u32 v7, v49, 4, v7\n" \
  "v_and_b32 v49, 2, v51\n" \
  "v_lshl_add_u32 v7, v49, 29, v7\n" \
  "v_lshlrev_b32 v10, 4, v48\n" \
  "v_mov_b64 v[12:13], 0\n" \
  "v_mov_b64 v[14:15], 0\n" \
  "v_mov_b64 v[16:17], 0\n" \
  "v_mov_b64 v[18:19], 0\n" \
  "v_mov_b64 v[20:21], 0\n" \
  "v_mov_b64 v[22:23], 0\n" \
  "s_add_u32 s88, s74, 157760\n" \
  "v_mov_b32 v59, s88\n" \
  "v_mov_b32 v24, 0x44870000\n" \
  "v_mul_f32 v24, s72, v24\n" \
  "s_nop 0\n" \
  "v_readfirstlane_b32 s86, v24\n" \
  "s_lshr_b32 s86, s86, 23\n" \
  "s_addk_i32 s86, 0xff83\n" \
  "s_ashr_i32 s86, s86, 1\n" \
  "s_max_i32 s86, s86, -20\n" \
  "s_min_i32 s86, s86, 100\n" \
  "s_mov_b32 s82, s86\n" \
  "s_lshr_b32 s83, s72, 23\n" \
  "s_sub_i32 s83, s83, 242\n" \
  "s_lshl_b32 s88, s73, 10\n" \
  "v_add_u32 v24, s88, v1\n" \
  "ds_read_b128 v[32:35], v24 offset:0\n" \
  "s_waitcnt lgkmcnt(0)\n" \
  "v_mov_b32 v11, 0\n" \
  "v_dot2c_f32_bf16 v11, v32, v32\n" \
  "v_dot2c_f32_bf16 v11, v33, v33\n" \
  "v_dot2c_f32_bf16 v11, v34, v34\n" \
  "v_dot2c_f32_bf16 v11, v35, v35\n" \
  "s_nop 2\n" \
  "v_cmpx_lt_f32 vcc, 2.0, v11\n" \
  "ds_max_u32 v59, v11\n" \
  "s_mov_b64 exec, -1\n" \
  ".p2align 6\n" \
  ".LCV_AL_%=:\n" \
  ".LCV_TOP_%=:\n" \
  "s_waitcnt lgkmcnt(0)\n" \
  "s_barrier\n" \
  "s_setprio 3\n" \
  "buffer_load_dword v58, off, s[44:47], s68 sc1\n" \
  "s_cmp_lg_u32 s73, 7\n" \
  "s_cbranch_scc1 .LCV_NOTK_%=\n" \
  "s_memrealtime s[38:39]\n" \
  "v_mov_b32 v57, 1\n" \
  "s_mul_i32 s23, s71, %[pp]\n" \
  "s_lshr_b32 s76, s66, 4\n" \
  "s_mov_b64 exec, 1\n" \
  "s_waitcnt lgkmcnt(0)\n" \
  "s_mov_b32 s67, s38\n" \
  "s_add_u32 s38, s38, s76\n" \
  "v_mov_b32 v56, s38\n" \
  "buffer_atomic_umax v56, off, s[16:19], s24\n" \
  "buffer_atomic_add v57, off, s[16:19], s24 offset:64\n" \
  "v_add_u32 v60, s23, v56\n" \
  "buffer_store_dword v60, off, s[16:19], s25 sc1\n" \
  "s_mov_b64 exec, -1\n" \
  ".LCV_NOTK_%=:\n" \
  "ds_read_b128 v[24:27], v1 offset:0\n" \
  "ds_read_b128 v[28:31], v1 offset:1024\n" \
  "ds_read_b128 v[32:35], v1 offset:2048\n" \
  "ds_read_b128 v[36:39], v1 offset:3072\n" \
  "ds_read_b128 v[40:43], v1 offset:4096\n" \
  "ds_read_b128 v[44:47], v1 offset:5120\n" \
  "ds_read_b128 v[48:51], v1 offset:6144\n" \
  "ds_read_b128 v[52:55], v1 offset:7168\n" \
  "s_waitcnt lgkmcnt(7)\n" \
  "v_smfmac_f32_16x16x64_bf16 v[12:15], v[24:27], v[64:71], v0\n" \
  "s_waitcnt lgkmcnt(6)\n" \
  "v_smfmac_f32_16x16x64_bf16 v[12:15], v[28:31], v[72:79], v0\n" \
  "s_waitcnt lgkmcnt(5)\n" \
  "v_smfmac_f32_16x16x64_bf16 v[12:15], v[32:35], v[80:87], v0\n" \
  "s_waitcnt lgkmcnt(4)\n" \
  "v_smfmac_f32_16x16x64_bf16 v[12:15], v[36:39], v[88:95], v0\n" \
  "s_waitcnt lgkmcnt(3)\n" \
  "v_smfmac_f32_16x16x64_bf16 v[12:15], v[40:43], v[96:103], v0\n" \
  "s_waitcnt lgkmcnt(2)\n" \
  "v_smfmac_f32_16x16x64_bf16 v[12:15], v[44:47], v[104:111], v0\n" \
  "s_waitcnt lgkmcnt(1)\n" \
  "v_smfmac_f32_16x16x64_bf16 v[12:15], v[48:51], v[112:119], v0\n" \
  "s_waitcnt lgkmcnt(0)\n" \
  "v_smfmac_f32_16x16x64_bf16 v[12:15], v[52:55], v[120:127], v0\n" \
  "ds_read_b32 v11, v1 offset:26688\n" \
  "s_nop 6\n" \
  "v_pk_add_f32 v[12:13], v[12:13], v[14:15]\n" \
  "s_nop 0\n" \
  "v_cvt_pk_bf16_f32 v12, v12, v13\n" \
  "ds_write_b16 v4, v12 offset:0\n" \
  "ds_write_b16_d16_hi v4, v12 offset:288\n" \
  "s_setprio 0\n" \
  "v_mov_b32 v9, 0\n" \
  "v_mov_b64 v[12:13], s[84:85]\n" \
  "v_mov_b64 v[14:15], s[84:85]\n" \
  "s_waitcnt lgkmcnt(2)\n" \
  "v_readfirstlane_b32 s86, v11\n" \
  "s_lshr_b32 s86, s86, 23\n" \
  "s_add_i32 s86, s86, s83\n" \
  "s_ashr_i32 s86, s86, 1\n" \
  "s_max_i32 s86, s86, s82\n" \
  "s_min_i32 s86, s86, 100\n" \
  "s_lshl_b32 s86, s86, 23\n" \
  "s_cmp_eq_u32 s71, 0\n" \
  "v_smfmac_f32_16x16x64_bf16 v[16:19], v[24:27], v[128:135], v0\n" \
  "v_smfmac_f32_16x16x64_bf16 v[20:23], v[24:27], v[192:199], v0\n" \
  "v_smfmac_f32_16x16x64_bf16 v[16:19], v[28:31], v[136:143], v0\n" \
  "v_smfmac_f32_16x16x64_bf16 v[20:23], v[28:31], v[200:207], v0\n" \
  "v_smfmac_f32_16x16x64_bf16 v[16:19], v[32:35], v[144:151], v0\n" \
  "v_smfmac_f32_16x16x64_bf16 v[20:23], v[32:35], v[208:215], v0\n" \
  "s_waitcnt lgkmcnt(0)\n" \
  "s_barrier\n" \
  "ds_read_b128 v[60:63], v6 offset:0\n" \
  "v_smfmac_f32_16x16x64_bf16 v[16:19], v[36:39], v[152:159], v0\n" \
  "v_smfmac_f32_16x16x64_bf16 v[20:23], v[36:39], v[216:223], v0\n" \
  "v_smfmac_f32_16x16x64_bf16 v[16:19], v[40:43], v[160:167], v0\n" \
  "v_smfmac_f32_16x16x64_bf16 v[20:23], v[40:43], v[224:231], v0\n" \
  "v_smfmac_f32_16x16x64_bf16 v[16:19], v[44:47], v[168:175], v0\n" \
  "v_smfmac_f32_16x16x64_bf16 v[20:23], v[44:47], v[232:239], v0\n" \
  "v_smfmac_f32_16x16x64_bf16 v[16:19], v[48:51], v[176:183], v0\n" \
  "v_smfmac_f32_16x16x64_bf16 v[20:23], v[48:51], v[240:247], v0\n" \
  "v_smfmac_f32_16x16x64_bf16 v[16:19], v[52:55], v[184:191], v0\n" \
  "v_smfmac_f32_16x16x64_bf16 v[20:23], v[52:55], v[248:255], v0\n" \
  "s_nop 6\n" \
  "v_pk_add_f32 v[16:17], v[16:17], v[18:19]\n" \
  "v_pk_add_f32 v[20:21], v[20:21], v[22:23]\n" \
  "v_cvt_pk_bf16_f32 v16, v16, v17\n" \
  "v_cvt_pk_bf16_f32 v17, v20, v21\n" \
  "ds_write_b64 v5, v[16:17]\n" \
  "s_cbranch_scc1 .LCV_LAST_%=\n" \
  "ds_read_b128 v[24:27], v3 offset:0\n" \
  "ds_read_b128 v[28:31], v3 offset:1024\n" \
  "ds_read_b128 v[32:35], v3 offset:2048\n" \
  "ds_read_b128 v[36:39], v3 offset:3072\n" \
  "ds_read_b128 v[40:43], v3 offset:8192\n" \
  "ds_read_b128 v[44:47], v3 offset:9216\n" \
  "s_cmp_eq_u32 s73, 7\n" \
  "s_waitcnt vmcnt(3)\n" \
  "s_cbranch_scc1 .LCV_TKW_%=\n" \
  "s_waitcnt vmcnt(0)\n" \
  ".LCV_TKW_%=:\n" \
  "v_cmp_eq_u32 vcc, 0, v58\n" \
  "s_cmp_eq_u64 vcc, 0\n" \
  "s_cselect_b32 s90, 1, 0\n" \
  "s_waitcnt lgkmcnt(6)\n" \
  "s_barrier\n" \
  "s_cbranch_scc0 .LCV_NOTOK_%=\n" \
  "s_mov_b32 m0, s99\n" \
  "s_add_u32 s88, s99, 8192\n" \
  "global_load_lds_dwordx4 v10, s[62:63] sc1\n" \
  "global_load_lds_dwordx4 v10, s[62:63] offset:1024 sc1\n" \
  "s_mov_b32 m0, s88\n" \
  "s_nop 0\n" \
  "global_load_lds_dwordx4 v10, s[64:65] sc1\n" \
  "global_load_lds_dwordx4 v10, s[64:65] offset:1024 sc1\n" \
  ".LCV_NOTOK_%=:\n" \
  "ds_write_b32 v59, v9\n" \
  "ds_read_b128 v[48:51], v3 offset:10240\n" \
  "ds_read_b128 v[52:55], v3 offset:11264\n" \
  "v_permlane16_swap_b32 v16, v17\n" \
  "v_max_u32 v60, 1, v60\n" \
  "v_max_u32 v61, 1, v61\n" \
  "v_max_u32 v16, 1, v16\n" \
  "v_max_u32 v17, 1, v17\n" \
  "buffer_store_dwordx2 v[16:17], v8, s[52:55], s70 offen sc1\n" \
  "v_max_u32 v62, 1, v62\n" \
  "v_max_u32 v63, 1, v63\n" \
  "buffer_store_dwordx4 v[60:63], v7, s[48:51], s69 offen sc1\n" \
  "v_mov_b64 v[18:19], s[84:85]\n" \
  "v_mov_b64 v[20:21], s[84:85]\n" \
  "v_mov_b64 v[22:23], s[84:85]\n" \
  "v_mov_b64 v[16:17], s[84:85]\n" \
  "ds_read_b128 v[56:59], v3 offset:4096\n" \
  "ds_read_b128 v[60:63], v3 offset:5120\n" \
  "s_waitcnt lgkmcnt(0)\n" \
  "s_barrier\n" \
  "ds_read_b128 v[4:7], v1 offset:8192\n" \
  "ds_read_b128 v[8:11], v1 offset:9216\n" \
  "s_waitcnt lgkmcnt(1)\n" \
  "v_smfmac_f32_16x16x64_bf16 v[12:15], v[4:7], v[24:31], v0\n" \
  "ds_read_b128 v[24:27], v3 offset:12288\n" \
  "ds_read_b128 v[28:31], v3 offset:13312\n" \
  "v_smfmac_f32_16x16x64_bf16 v[16:19], v[4:7], v[32:39], v0\n" \
  "ds_read_b128 v[32:35], v3 offset:6144\n" \
  "ds_read_b128 v[36:39], v3 offset:7168\n" \
  "s_waitcnt lgkmcnt(4)\n" \
  "v_smfmac_f32_16x16x64_bf16 v[12:15], v[8:11], v[40:47], v0\n" \
  "v_smfmac_f32_16x16x64_bf16 v[16:19], v[8:11], v[48:55], v0\n" \
  "s_cmp_eq_u32 s90, 1\n" \
  "s_cbranch_scc1 .LCV_NMT_%=\n" \
  "buffer_load_dword v55, off, s[44:47], s68 sc1\n" \
  ".LCV_NMT_%=:\n" \
  "v_smfmac_f32_16x16x64_bf16 v[20:23], v[4:7], v[56:63], v0\n" \
  "ds_read_b128 v[56:59], v3 offset:14336\n" \
  "ds_read_b128 v[60:63], v3 offset:15360\n" \
  "v_mov_b64 v[40:41], s[84:85]\n" \
  "v_mov_b64 v[42:43], s[84:85]\n" \
  "v_add_f32 v12, v12, v14\n" \
  "v_add_f32 v13, v13, v15\n" \
  "global_atomic_add_x2 v2, v[12:13], s[26:27]\n" \
  "s_waitcnt lgkmcnt(4)\n" \
  "v_smfmac_f32_16x16x64_bf16 v[20:23], v[8:11], v[24:31], v0\n" \
  "v_add_f32 v16, v16, v18\n" \
  "v_add_f32 v17, v17, v19\n" \
  "global_atomic_add_x2 v2, v[16:17], s[28:29]\n" \
  "s_waitcnt lgkmcnt(2)\n" \
  "v_smfmac_f32_16x16x64_bf16 v[40:43], v[4:7], v[32:39], v0\n" \
  "s_waitcnt lgkmcnt(0)\n" \
  "v_smfmac_f32_16x16x64_bf16 v[40:43], v[8:11], v[56:63], v0\n" \
  "s_nop 1\n" \
  "v_add_f32 v20, v20, v22\n" \
  "v_add_f32 v21, v21, v23\n" \
  "global_atomic_add_x2 v2, v[20:21], s[30:31]\n" \
  "s_nop 3\n" \
  "v_add_f32 v40, v40, v42\n" \
  "v_add_f32 v41, v41, v43\n" \
  "global_atomic_add_x2 v2, v[40:41], s[32:33]\n" \
  "s_memrealtime s[76:77]\n" \
  "v_mbcnt_lo_u32_b32 v48, -1, 0\n" \
  "v_mbcnt_hi_u32_b32 v48, -1, v48\n" \
  "s_add_u32 s20, s20, 32\n" \
  "s_cmp_eq_u32 s90, 1\n" \
  "s_cbranch_scc1 .LCV_HAVE_%=\n" \
  "s_mov_b32 s87, 0x40000\n" \
  "v_lshlrev_b32 v10, 4, v48\n" \
  "s_waitcnt vmcnt(4)\n" \
  "v_cmp_eq_u32 vcc, 0, v55\n" \
  "s_cmp_eq_u64 vcc, 0\n" \
  "s_cbranch_scc1 .LCV_GOT_%=\n" \
  ".LCV_LATE_%=:\n" \
  "buffer_load_dword v58, off, s[44:47], s68 sc1\n" \
  "s_sub_u32 s87, s87, 1\n" \
  "s_cbranch_scc1 .LCV_END_%=\n" \
  "s_waitcnt vmcnt(0)\n" \
  "v_cmp_eq_u32 vcc, 0, v58\n" \
  "s_cmp_eq_u64 vcc, 0\n" \
  "s_cbranch_scc1 .LCV_GOT_%=\n" \
  "s_sleep 12\n" \
  "s_branch .LCV_LATE_%=\n" \
  ".LCV_GOT_%=:\n" \
  "s_mov_b32 m0, s99\n" \
  "s_add_u32 s88, s99, 8192\n" \
  "global_load_lds_dwordx4 v10, s[62:63] sc1\n" \
  "global_load_lds_dwordx4 v10, s[62:63] offset:1024 sc1\n" \
  "s_mov_b32 m0, s88\n" \
  "s_nop 0\n" \
  "global_load_lds_dwordx4 v10, s[64:65] sc1\n" \
  "global_load_lds_dwordx4 v10, s[64:65] offset:1024 sc1\n" \
  ".LCV_HAVE_%=:\n" \
  "v_lshrrev_b32 v49, 3, v48\n" \
  "v_and_b32 v50, 7, v48\n" \
  "v_lshlrev_b32 v50, 4, v50\n" \
  "v_lshl_or_b32 v44, v49, 8, v50\n" \
  "s_mov_b32 s21, 0x8000\n" \
  "v_mov_b32 v52, s22\n" \
  "v_mov_b32 v54, s20\n" \
  "s_branch .LCV_TKF_%=\n" \
  ".LCV_TKE_%=:\n" \
  "s_sleep 1\n" \
  ".LCV_TKF_%=:\n" \
  "ds_read_b64 v[54:55], v52\n" \
  "s_sub_u32 s21, s21, 1\n" \
  "s_cbranch_scc1 .LCV_TKO_%=\n" \
  "s_waitcnt lgkmcnt(0)\n" \
  "v_readfirstlane_b32 s38, v54\n" \
  "v_readfirstlane_b32 s34, v55\n" \
  "s_cmp_eq_u32 s38, s20\n" \
  "s_cbranch_scc0 .LCV_TKE_%=\n" \
  ".LCV_TKO_%=:\n" \
  "s_branch .LCV_WAIU_%=\n" \
  ".LCV_WAIS_%=:\n" \
  "s_cmp_lt_i32 s39, -1000\n" \
  "s_cbranch_scc1 .LCV_WAIE_%=\n" \
  "s_sleep 1\n" \
  ".LCV_WAIU_%=:\n" \
  "s_memrealtime s[38:39]\n" \
  "s_waitcnt lgkmcnt(0)\n" \
  "s_sub_i32 s39, s38, s34\n" \
  "s_cmp_lt_i32 s39, -7\n" \
  "s_cbranch_scc1 .LCV_WAIS_%=\n" \
  "s_cmp_lt_i32 s39, 0\n" \
  "s_cbranch_scc1 .LCV_WAIU_%=\n" \
  ".LCV_WAIE_%=:\n" \
  "buffer_load_dwordx4 v[24:27], v44, s[40:43], s60 offen sc1\n" \
  "buffer_load_dwordx4 v[28:31], v44, s[40:43], s60 offen offset:2048 sc1\n" \
  "s_sub_u32 s76, s76, s67\n" \
  "s_lshl_b32 s76, s76, 4\n" \
  "s_sub_i32 s76, s76, s66\n" \
  "s_min_i32 s76, s76, 128\n" \
  "s_max_i32 s76, s76, -256\n" \
  "s_ashr_i32 s76, s76, 2\n" \
  "s_add_i32 s66, s66, s76\n" \
  "v_mbcnt_lo_u32_b32 v48, -1, 0\n" \
  "v_mbcnt_hi_u32_b32 v48, -1, v48\n" \
  "s_sub_u32 s88, s73, 4\n" \
  "v_and_b32 v51, 15, v48\n" \
  "v_lshl_add_u32 v4, v51, 1, s97\n" \
  "v_lshrrev_b32 v49, 4, v48\n" \
  "v_mad_u32_u24 v4, v49, s94, v4\n" \
  "v_and_b32 v49, 1, v49\n" \
  "v_lshrrev_b32 v50, 5, v48\n" \
  "v_lshlrev_b32 v8, 3, v50\n" \
  "v_lshl_add_u32 v8, v51, 4, v8\n" \
  "v_lshl_add_u32 v8, v49, 10, v8\n" \
  "s_lshl_b32 s89, s88, 8\n" \
  "s_add_u32 s89, s89, s74\n" \
  "s_add_u32 s89, s89, 139264\n" \
  "v_and_b32 v49, 31, v48\n" \
  "v_lshl_add_u32 v5, v49, 3, s89\n" \
  "v_lshl_add_u32 v5, v50, 10, v5\n" \
  "v_lshrrev_b32 v49, 2, v51\n" \
  "v_and_b32 v50, 5, v51\n" \
  "v_and_or_b32 v49, v49, 2, v50\n" \
  "s_lshl_b32 s89, s88, 6\n" \
  "s_add_u32 s89, s89, s74\n" \
  "s_add_u32 s89, s89, 160992\n" \
  "v_and_b32 v50, 48, v48\n" \
  "v_add_u32 v6, s89, v50\n" \
  "v_mad_u32_u24 v6, v49, s95, v6\n" \
  "v_lshlrev_b32 v7, 3, v50\n" \
  "v_lshl_add_u32 v7, v49, 4, v7\n" \
  "v_and_b32 v49, 2, v51\n" \
  "v_lshl_add_u32 v7, v49, 29, v7\n" \
  "v_lshlrev_b32 v10, 4, v48\n" \
  "v_mov_b64 v[12:13], 0\n" \
  "v_mov_b64 v[14:15], 0\n" \
  "v_mov_b64 v[16:17], 0\n" \
  "v_mov_b64 v[18:19], 0\n" \
  "v_mov_b64 v[20:21], 0\n" \
  "v_mov_b64 v[22:23], 0\n" \
  "s_add_u32 s88, s74, 157760\n" \
  "v_mov_b32 v59, s88\n" \
  "v_and_b32 v49, 3, v48\n" \
  "v_lshl_add_u32 v40, v49, 2, s98\n" \
  "v_lshrrev_b32 v49, 2, v48\n" \
  "v_lshl_add_u32 v40, v49, 5, v40\n" \
  "s_add_u32 s62, s62, 0x80000\n" \
  "s_addc_u32 s63, s63, 0\n" \
  "s_add_u32 s64, s64, 0x40000\n" \
  "s_addc_u32 s65, s65, 0\n" \
  "s_add_u32 s68, s68, 0x1000\n" \
  "s_add_u32 s69, s69, 0x10000\n" \
  "s_add_u32 s70, s70, 0x10000\n" \
  "s_add_i32 s80, s86, 0x35000000\n" \
  "v_mov_b32 v11, 0\n" \
  "s_mov_b32 s89, s60\n" \
  "s_xor_b32 s60, s60, 0x80\n" \
  "s_bitcmp0_b32 s60, 7\n" \
  "s_cselect_b32 s88, 0x8000, 0\n" \
  "s_add_u32 s60, s60, s88\n" \
  "s_and_b32 s60, s60, 0xffff\n" \
  "s_add_u32 s58, s56, s60\n" \
  "s_addc_u32 s59, s57, 0\n" \
  "s_xor_b32 s38, s35, 0\n" \
  "s_add_u32 s26, s58, s38\n" \
  "s_addc_u32 s27, s59, 0\n" \
  "s_xor_b32 s38, s35, 1024\n" \
  "s_add_u32 s28, s58, s38\n" \
  "s_addc_u32 s29, s59, 0\n" \
  "s_xor_b32 s38, s35, 2048\n" \
  "s_add_u32 s30, s58, s38\n" \
  "s_addc_u32 s31, s59, 0\n" \
  "s_xor_b32 s38, s35, 3072\n" \
  "s_add_u32 s32, s58, s38\n" \
  "s_addc_u32 s33, s59, 0\n" \
  "s_sub_u32 s71, s71, 1\n" \
  "s_mov_b32 s87, 0x40000\n" \
  "s_branch .LCV_CHK_%=\n" \
  ".LCV_POLL_%=:\n" \
  "buffer_load_dwordx4 v[24:27], v44, s[40:43], s89 offen sc1\n" \
  "buffer_load_dwordx4 v[28:31], v44, s[40:43], s89 offen offset:2048 sc1\n" \
  ".LCV_CHK_%=:\n" \
  "s_sub_u32 s87, s87, 1\n" \
  "s_cbranch_scc1 .LCV_END_%=\n" \
  "s_waitcnt vmcnt(1)\n" \
  "v_cvt_f32_i32 v32, v24\n" \
  "v_cvt_f32_i32 v34, v26\n" \
  "v_cvt_f32_i32 v33, v25\n" \
  "v_cvt_f32_i32 v35, v27\n" \
  "v_max_f32 v45, |v32|, |v34|\n" \
  "v_pk_mul_f32 v[32:33], v[32:33], s[80:81] op_sel_hi:[1,0]\n" \
  "v_pk_mul_f32 v[34:35], v[34:35], s[80:81] op_sel_hi:[1,0]\n" \
  "s_nop 0\n" \
  "v_cvt_pk_bf16_f32 v32, v32, v34\n" \
  "v_cvt_pk_bf16_f32 v33, v33, v35\n" \
  "ds_write2_b32 v40, v32, v33 offset0:0 offset1:4\n" \
  "s_waitcnt vmcnt(0)\n" \
  "v_cvt_f32_i32 v36, v28\n" \
  "v_cvt_f32_i32 v38, v30\n" \
  "v_cvt_f32_i32 v37, v29\n" \
  "v_cvt_f32_i32 v39, v31\n" \
  "v_max3_f32 v45, |v36|, |v38|, v45\n" \
  "v_pk_mul_f32 v[36:37], v[36:37], s[80:81] op_sel_hi:[1,0]\n" \
  "v_pk_mul_f32 v[38:39], v[38:39], s[80:81] op_sel_hi:[1,0]\n" \
  "v_cmp_lt_f32 vcc, s91, v45\n" \
  "v_cvt_pk_bf16_f32 v36, v36, v38\n" \
  "v_cvt_pk_bf16_f32 v37, v37, v39\n" \
  "ds_write2_b32 v40, v36, v37 offset0:128 offset1:132\n" \
  "s_cmp_eq_u64 vcc, 0\n" \
  "s_cbranch_scc0 .LCV_POLL_%=\n" \
  "v_dot2c_f32_bf16 v11, v32, v32\n" \
  "v_dot2c_f32_bf16 v11, v33, v33\n" \
  "v_dot2c_f32_bf16 v11, v36, v36\n" \
  "v_dot2c_f32_bf16 v11, v37, v37\n" \
  "s_nop 2\n" \
  "v_cmpx_lt_f32 vcc, 2.0, v11\n" \
  "ds_max_u32 v59, v11\n" \
  "s_mov_b64 exec, -1\n" \
  "s_branch .LCV_TOP_%=\n" \
  ".LCV_LAST_%=:\n" \
  "s_waitcnt lgkmcnt(0)\n" \
  "s_nop 0\n" \
  "v_permlane16_swap_b32 v16, v17\n" \
  "v_max_u32 v60, 1, v60\n" \
  "v_max_u32 v61, 1, v61\n" \
  "v_max_u32 v16, 1, v16\n" \
  "v_max_u32 v17, 1, v17\n" \
  "buffer_store_dwordx2 v[16:17], v8, s[52:55], s70 offen sc1\n" \
  "v_max_u32 v62, 1, v62\n" \
  "v_max_u32 v63, 1, v63\n" \
  "buffer_store_dwordx4 v[60:63], v7, s[48:51], s69 offen sc1\n" \
  "s_waitcnt vmcnt(0)\n" \
  ".LCV_END_%=:\n" \
  "s_endpgm\n"
__device__ __forceinline__ v4u mk_srd(const void* p, const unsigned bytes) {
  const unsigned long long a = (unsigned long long)p;
  return v4u{(unsigned)a, (unsigned)(a >> 32) & 0xffffu, bytes, 0x00020000u};
}
template <bool ATT>
__device__ __forceinline__ void role_chain_t(const MegaArgs& A, char* smem, const int L, const int h, const int rep) {
  __builtin_amdgcn_s_waitcnt(0x0F70);
  u16* swo = reinterpret_cast<u16*>(smem);
  const int w = __builtin_amdgcn_readfirstlane(threadIdx.x >> 6);
  const int wc = (w + ROT) & 7, psw = PSWH;
  char* ws = A.ws;
  unsigned* FL = (unsigned*)(ws + OFF_FL);
  unsigned* TMO = FL + FL_TMO;
#define LANE_IDS()                         \
  const int l = lane_id();                 \
  const int c = l & 15, g = l >> 4;        \
  (void)c; (void)g;
  float* sgm = reinterpret_cast<float*>(smem + C_X);
  float* sgf = sgm + 8;
  unsigned* FQ = FL + FL_FQ + L * 64 * 32 * 32;
  v8u w0[8], w1[8], w2[8];
  {
    LANE_IDS();
    v4u m0r;
    const unsigned sb = lds_base(smem);
    const int q = (l >> 2) & 3, p = l & 3;
    const int f = (q & 1) | ((g & 1) << 1) | ((q >> 1) << 2);
    const unsigned ra = sb + (16 * (g >> 1) + 4 * (g & 1) + (q & 1) + 8 * (q >> 1)) * 256 + (w ^ f) * 32 + p * 8;
#define W_READ(W, hf, X)                                                       \
  {                                                                           \
    _Pragma("unroll") for (int t = 0; t < 4; ++t) {                           \
      const unsigned a_ = (ra ^ (X)) + (hf) * 8192 + t * 32768;               \
      const v2u r0 = trd(a_), r1 = trd(a_ + 512), r2 = trd(a_ + 16384), r3 = trd(a_ + 16384 + 512); \
      W[4 * (hf) + t] = v8u{r0.x, r0.y, r1.x, r1.y, r2.x, r2.y, r3.x, r3.y};  \
    }                                                                         \
    _Pragma("unroll") for (int t = 0; t < 4; ++t) asm volatile("" : "+v"(W[4 * (hf) + t])); \
  }
#define W_STAGE(M, hf, VM, NEXT)                                               \
  {                                                                           \
    WAIT_VM(VM);                                                              \
    RAW_BAR();                                                                \
    if ((M) == 0 && ATT) { W_READ(w0, hf, 0); W_READ(w2, hf, 128); }          \
    if ((M) == 1 && ATT) { W_READ(w1, hf, 0); }                               \
    if ((M) == 1 && !ATT) { W_READ(w0, hf, 0); }                              \
    if ((M) == 2 && !ATT) { W_READ(w1, hf, 128); W_READ(w2, hf, 0); }         \
    RAW_BAR();                                                                \
    NEXT;                                                                     \
  }
#define WO_ISSUE(hf)                                                          \
  {                                                                           \
    const char* base_ = (const char*)A.Wo + (size_t)(h * DH + 64 * (hf) + 8 * w) * (DM * 2); \
    _Pragma("unroll") for (int i = 0; i < 8; ++i) {                           \
      const int fd = (i & 1) | (((i >> 2) & 1) << 1) | ((w & 1) << 2);        \
      glds16(base_, (unsigned)(i * 1024 + ((l ^ (fd << 1)) << 4)), sb + w * 16384 + (hf) * 8192 + i * 1024); \
    }                                                                         \
  }
    wqkv_issue<true>(A.Wv, 0, h, w, l, sb);
    wqkv_issue<true>(A.Wv, 1, h, w, l, sb);
    asm volatile("s_nop 4\n\tglobal_load_dwordx4 %0, %1, %2" : "=v"(m0r) : "v"(((2 * (c >> 2) + (c & 1)) * DM + 64 * w + 16 * g + 4 * (c & 2)) * 2), "s"(A.mem0) : "memory");
    W_STAGE(2, 0, 9, wqkv_issue<true>(A.Wk, 0, h, w, l, sb));
    SST(4);
    W_STAGE(2, 1, 8, wqkv_issue<true>(A.Wk, 1, h, w, l, sb));
    SST(5);
    asm volatile("" : "+v"(m0r));
    *reinterpret_cast<v4u*>(smem + C_SM + w * 1024 + l * 16) = m0r;
    {
      float f2[4] = {};
      if (!ATT) {
#pragma unroll
        for (int t = 0; t < 8; ++t)
#pragma unroll
          for (int e = 0; e < 8; ++e) {
            f2[e & 1] = dot2sq(w1[t][e], f2[e & 1]);
            f2[2 + (e & 1)] = dot2sq(w2[t][e], f2[2 + (e & 1)]);
          }
      }
      const float f = wave_sum_dpp((f2[0] + f2[1]) + (f2[2] + f2[3]));
      if (l == 0) { sgf[w] = f; sgm[16] = 0.f; sgm[32 + w] = 0.f; }
    }
    v4u zt;
    v2u ze;
#define Z_LOAD()                                                                                                        \
  asm volatile("global_load_dwordx4 %0, %2, %3 sc1\n\tglobal_load_dwordx2 %1, %4, %5 sc1"                              \
               : "=&v"(zt), "=&v"(ze)                                                                                   \
               : "v"(l * 16), "s"(ws + OFF_TAG + (blockIdx.x & 7) * 1024), "v"(0), "s"(ws + OFF_TAG + 8192)              \
               : "memory")
    W_STAGE(1, 0, 8, Z_LOAD(); wqkv_issue<true>(A.Wq, 0, h, w, l, sb));
#undef Z_LOAD
    W_STAGE(1, 1, 8, wqkv_issue<true>(A.Wq, 1, h, w, l, sb));
    asm volatile("" : "+v"(zt), "+v"(ze));
    if (w == 0) {
      if (!zero_seen(zt, ze)) zero_wait(ws);
      const float f2 = (sgf[4] + sgf[5]) + (sgf[6] + sgf[7]);
      __builtin_amdgcn_raw_buffer_store_b32(__float_as_uint(f2) | 1u, MKRS(FL + FL_G, L ? 0 : 4 * 512), l * 512 + h * 4, 0, AUX_SC1);
    }
    SST(6);
    W_STAGE(0, 0, 8, WO_ISSUE(0));
    W_STAGE(0, 1, 8, WO_ISSUE(1));
    SST(7);
#undef W_STAGE
#undef W_READ
#undef WO_ISSUE
    const unsigned rb = sb + (2 * (g >> 1) + (q >> 1)) * 16384 + (4 * (g & 1) + (q & 1)) * 1024 + p * 8;
#pragma unroll
    for (int hf = 0; hf < 2; ++hf) {
      if (hf == 0) WAIT_VM(8); else WAIT_VM(0);
      RAW_BAR();
      v4u fr[8];
#pragma unroll
      for (int i = 0; i < 8; ++i) {
        const unsigned a_ = rb + hf * 8192 + (i & 1) * 65536 + ((4 * wc + ((i >> 1) ^ psw)) ^ f) * 32;
        const v2u r0 = trd(a_), r1 = trd(a_ + 2048);
        fr[i] = v4u{r0.x, r0.y, r1.x, r1.y};
      }
#pragma unroll
      for (int i = 0; i < 8; ++i) asm volatile("" : "+v"(fr[i]));
      RAW_BAR();
#pragma unroll
      for (int i = 0; i < 8; ++i) *reinterpret_cast<v4u*>(swo + w * 8192 + hf * 4096 + i * 512 + l * 8) = fr[i];
    }
  }
  WSTAMP2();
  const unsigned sb = lds_base(smem);
  const char* gK = ws + OFF_QK + L * SZ_QK + ((size_t)h * 16 + 2 * w) * 1024;
  const char* gV = ws + OFF_VF + L * SZ_O + ((size_t)h * 8 + 2 * w - 8) * 1024;
  float gw2;
  {
    const gu32* gp = (gu32*)(FL + FL_G + REP * 128) + (lane_id() & 31);
    unsigned g0, g1, g2;
    for (unsigned spins = 0;; ++spins) {
      g0 = __hip_atomic_load(gp, RLX_AGENT);
      g1 = __hip_atomic_load(gp + 32, RLX_AGENT);
      g2 = __hip_atomic_load(gp + 64, RLX_AGENT);
      if (__all(g0 != 0u && g1 != 0u && g2 != 0u)) break;
      if (spin_timeout(spins, TMO)) return;
      __builtin_amdgcn_s_sleep(4);
    }
    const float gm = wave_max_dpp(__uint_as_float(g0) * (__uint_as_float(g1) + __uint_as_float(g2)));
    asm volatile("s_nop 0\n\tv_readfirstlane_b32 %0, %1" : "=s"(gw2) : "v"(gm * 1.07f));
  }
  v8u pv;
  {
    LANE_IDS();
    const unsigned voff = g * 256 + c * 8, wp = sb + w * 16384 + l * 16;
    pv[0] = (c & 2) ? 0xEEEE : 0x4444;
    pv[1] = sb + C_SM + l * 16;
    pv[2] = ATT ? sb + C_SQ + ((c & 7) * QS + 8 * g) * 2 : voff;
    pv[3] = ATT ? sb + C_SK + ((2 * (c >> 2) + (c & 1)) * QS + 8 * g) * 2 : wp;
    pv[4] = sb + (c < 8 ? C_SO : C_SQ) + w * 256 + (2 * (c & 6) + (g & 2) + (c & 1)) * 16 + 8 * (g & 1);
    pv[5] = voff;
    pv[6] = wp;
    pv[7] = sb + C_SO + (l >> 5) * 1024 + w * 256 + (l & 31) * 8;
  }
  if (!ATT) {
    if (!wait_n<4, 1>(FQ + h * 32, 1, TMO)) return;
    const unsigned lo = lane_id() * 16, d = sb + C_KT + (2 * w - 8) * 1024;
    glds16c(gK, lo, d);
    glds16c(gK + 1024, lo, d + 1024);
    glds16c(gV, lo, d + 8192);
    glds16c(gV + 1024, lo, d + 9216);
  }
  DRAIN();
  SST(8);
  const v4u rsA = mk_srd(ws + OFF_ACC + L * SZ_ACC, SZ_ACC);
  const v4u rsF = mk_srd(FQ, 64 * 32 * 32 * 4);
  const v4u rsMK = mk_srd(ws + OFF_MEMK + L * SZ_MEM, SZ_MEM);
  const v4u rsMV = mk_srd(ws + OFF_MEMV + L * SZ_MEM, SZ_MEM);
  const int wr = (w + RR) & 7;
  const char* pAB = ws + OFF_ACC + L * SZ_ACC + (wc - wr) * 4096;
  const char* pGK = gK + NH * 16 * 1024;
  const char* pGV = gV + NH * 8 * 1024;
  unsigned sc[8];
  sc[0] = (32 + h) * 128;
  sc[1] = (h * 4 + w - 4) * 512;
  sc[2] = (h * DH + 16 * (w - 4)) * MM * 2;
  sc[3] = NSEG - 1;
  sc[4] = __float_as_uint(gw2);
  sc[5] = w;
  sc[6] = sb;
  const char* dbg = ws;
  const unsigned tg = (now10() / GR) * GR;
  const v4u rsT = mk_srd(FL + FL_TK, 2048);
#define CHAIN_OPS                                                                                                          \
  ::"{v[64:71]}"(w0[0]), "{v[72:79]}"(w0[1]), "{v[80:87]}"(w0[2]), "{v[88:95]}"(w0[3]), "{v[96:103]}"(w0[4]),              \
      "{v[104:111]}"(w0[5]), "{v[112:119]}"(w0[6]), "{v[120:127]}"(w0[7]), "{v[128:135]}"(w1[0]), "{v[136:143]}"(w1[1]),    \
      "{v[144:151]}"(w1[2]), "{v[152:159]}"(w1[3]), "{v[160:167]}"(w1[4]), "{v[168:175]}"(w1[5]), "{v[176:183]}"(w1[6]),    \
      "{v[184:191]}"(w1[7]), "{v[192:199]}"(w2[0]), "{v[200:207]}"(w2[1]), "{v[208:215]}"(w2[2]), "{v[216:223]}"(w2[3]),    \
      "{v[224:231]}"(w2[4]), "{v[232:239]}"(w2[5]), "{v[240:247]}"(w2[6]), "{v[248:255]}"(w2[7]), "{v[0:7]}"(pv),          \
      "{s[40:43]}"(rsA), "{s[44:47]}"(rsF), "{s[48:51]}"(rsMK), "{s[52:55]}"(rsMV), "{s[56:57]}"(pAB), "{s60}"(wr * 4096), "{s61}"(h * 1024 - wr * 4096), \
      "{s[62:63]}"(pGK), "{s[64:65]}"(pGV), "{s68}"(sc[0]), "{s69}"(sc[1]), "{s70}"(sc[2]),    \
      "{s71}"(sc[3]), "{s72}"(sc[4]), "{s73}"(sc[5]), "{s74}"(sc[6]), "{s[36:37]}"(dbg),                   \
      "{s34}"(tg), "{s35}"(psw * 1024), [gp] "n"(GP), [gr] "n"(GR), [gm] "n"(GM), \
      "{s[16:19]}"(rsT), "{s20}"(0), "{s22}"(sb + C_X + 128 + 8 * (w & 3)), "{s24}"(L * 1024 + (ATT ? 0 : (h & 3) * 256)), [cd] "n"(CD), \
      "{s25}"(ATT ? APA - (APN + 1) * APP + L * (APB - APA + (2 * APN + 1) * APP) : 1032 - L * 1024 + h * 4096), [ka] "n"(APL), [an] "n"(APN), [d0] "n"(16 * CD0), [rq] "n"(RQM), [pp] "n"(APP) \
      : "memory", "vcc", "scc", "s99", "v63"
  if (ATT) asm volatile("s_mov_b64 exec, -1\ns_nop 4\n" CHAIN_ASM_ATT CHAIN_OPS);
  else asm volatile("s_mov_b64 exec, -1\ns_nop 4\n" CHAIN_ASM_VO CHAIN_OPS);
#undef CHAIN_OPS
#undef LANE_IDS
}

__device__ __forceinline__ void role_chain(const MegaArgs& A, char* smem, const int L, const int h, const int rep) {
  if (__builtin_amdgcn_readfirstlane(threadIdx.x >> 6) < 4) role_chain_t<true>(A, smem, L, h, rep);
  else role_chain_t<false>(A, smem, L, h, rep);
}

constexpr int AW_TK = 0, AW_SO = 98304, AW_M = AW_SO + 32768, AW_CNT = AW_M + 8192;
typedef __attribute__((address_space(3))) unsigned lu32;
__device__ __forceinline__ unsigned umin3(unsigned a, unsigned b, unsigned c) {
  return __builtin_elementwise_min(__builtin_elementwise_min(a, b), c);
}
__device__ __forceinline__ bool all_set(const unsigned r) {
  unsigned long long m;
  asm volatile("v_cmp_eq_u32 %0, 0, %1" : "=s"(m) : "v"(r) : "memory");
  return m == 0ull;
}
__device__ __forceinline__ unsigned long long zero_mask(const unsigned r) {
  unsigned long long m;
  asm volatile("v_cmp_eq_u32 %0, 0, %1" : "=s"(m) : "v"(r) : "memory");
  return m;
}
__device__ __forceinline__ int job_seg(const int n) {
  const int m = n - AWD;
  const int r = (m & 1) ? 64 + (m >> 1) : AWD + (m >> 1);
  return __builtin_amdgcn_readfirstlane(n > 127 ? 128 : (n < AWD || n >= 128 - AWD) ? n : r);
}
template <bool AT>
__device__ __forceinline__ void role_aw_t(const MegaArgs& A, char* smem, const int mode, const int a) {
  __builtin_amdgcn_s_waitcnt(0x0F70);
  const int w = __builtin_amdgcn_readfirstlane(threadIdx.x >> 6);
  const int l = threadIdx.x & 63, c = l & 15, g = l >> 4;
  const int qt = w & 1, hh = (w >> 1) & 1, h = 2 * a + hh;
  char* ws = A.ws;
  unsigned* FL = (unsigned*)(ws + OFF_FL);
  unsigned* TMO = FL + FL_TMO;
  const unsigned sb = lds_base(smem);
  constexpr int NP = AT ? 1 : 3;
  const int cp0 = AT ? w : 3 * w - 8;
  v4u wo[2 * NP][8];
  lu32* cnt = (lu32*)(unsigned long)(sb + AW_CNT);
  if (threadIdx.x < 16) cnt[threadIdx.x] = 0u;
  const __amdgpu_buffer_rsrc_t rsW = MKRS(ws + OFF_PW, SZ_PW);
  const unsigned* FQ = FL + FL_FQ + h * 32;
  const v4u rsF = mk_srd(FQ, (128 * 32 - h) * 128);
  const char* gQ = ws + OFF_QK + ((size_t)h * 16 + 8 * qt) * 1024;
  const char* gV = ws + OFF_VF + ((size_t)h * 8 + 4 * qt) * 1024;
  const char* gM = ws + (qt ? OFF_MEMV : OFF_MEMK) + h * 2048;
  const int NJ = (130 - mode) / 3;
#define AWS(gs) (64 + a + ((gs) >> 6) * 16)
  const int mo = AW_M + hh * 4096;
  const unsigned tq0 = __builtin_amdgcn_readfirstlane(sb + AW_TK + (hh * 24 + 8 * qt) * 1024);
  const unsigned tv0 = __builtin_amdgcn_readfirstlane(sb + AW_TK + (hh * 24 + 16 + 4 * qt) * 1024);
  const float scale = 0.08838834764831845f;
  volatile lu32* cM = cnt + 2 + hh;
#define DMA4(x) "global_load_lds_dwordx4 %1, " x " sc1\n\tglobal_load_lds_dwordx4 %1, " x " offset:1024 sc1\n\t"       \
                "global_load_lds_dwordx4 %1, " x " offset:2048 sc1\n\tglobal_load_lds_dwordx4 %1, " x " offset:3072 sc1\n\t"
#define T_DMA(jj, gg)                                                                                                   \
  {                                                                                                                     \
    const unsigned o_ = ((jj) & 1) ? 49152u : 0u;                                                                       \
    const char* q_ = gQ + (size_t)(gg) * (NH * 16 * 1024);                                                              \
    unsigned keep_;                                                                                                     \
    asm volatile("s_nop 4\n\ts_mov_b32 %0, m0\n\ts_mov_b32 m0, %5\n\ts_nop 0\n\t" DMA4("%2")                           \
                 "s_mov_b32 m0, %6\n\ts_nop 0\n\t" DMA4("%3") "s_mov_b32 m0, %7\n\ts_nop 0\n\t" DMA4("%4") "s_mov_b32 m0, %0" \
                 : "=&s"(keep_)                                                                                         \
                 : "v"(l * 16), "s"(q_), "s"(q_ + 4096), "s"(gV + (size_t)(gg) * (NH * 8 * 1024)), "s"(tq0 + o_),        \
                   "s"(tq0 + o_ + 4096u), "s"(tv0 + o_)                                                                 \
                 : "memory");                                                                                           \
  }
#define T_LAND(jj) { if (l == 0) *(volatile lu32*)(cnt + w) = (jj) + 1; }
#define M_DMA(gg)                                                                                                       \
  {                                                                                                                     \
    unsigned keep_;                                                                                                     \
    asm volatile("s_nop 4\n\ts_mov_b32 %0, m0\n\ts_mov_b32 m0, %3\n\ts_nop 0\n\tglobal_load_lds_dwordx4 %1, %2 sc1\n\t"     \
                 "global_load_lds_dwordx4 %1, %2 offset:1024 sc1\n\ts_mov_b32 m0, %0"                                    \
                 : "=&s"(keep_)                                                                                         \
                 : "v"(l * 16), "s"(gM + (size_t)(gg) * (NH * 2048)), "s"(sb + mo + qt * 2048)                           \
                 : "memory");                                                                                           \
  }
#define M_TEST()                                                                                                        \
  {                                                                                                                     \
    const v4u t_ = *reinterpret_cast<const v4u*>(smem + mo + qt * 2048 + l * 16);                                        \
    const v4u u_ = *reinterpret_cast<const v4u*>(smem + mo + qt * 2048 + 1024 + l * 16);                                 \
    ok = __all(umin3(umin3(t_.x, t_.y, t_.z), umin3(u_.x, u_.y, u_.z), umin3(t_.w, u_.w, 1u)) != 0u);                   \
    if (ok) asm volatile("s_mov_b64 exec, 1\n\tds_add_u32 %0, %1\n\ts_mov_b64 exec, -1" ::"v"(sb + AW_CNT + 8 + 4 * hh), "v"(1u) : "memory"); \
  }
#define F_LOAD(r, gg) \
  asm volatile("s_nop 4\n\tbuffer_load_dword %0, %1, %2, %3 offen sc1" : "=v"(r) : "v"(0), "s"(rsF), "s"((gg) * 4096) : "memory")
#define F_TEST(r) all_set(r)
#define W_FLAG(jj, gg)                                                                                                  \
  {                                                                                                                     \
    unsigned old_;                                                                                                      \
    asm volatile("s_mov_b64 exec, 1\n\tds_add_rtn_u32 %0, %1, %2\n\ts_mov_b64 exec, -1\n\ts_waitcnt lgkmcnt(0)"           \
                 : "=&v"(old_) : "v"(sb + AW_CNT + ((jj) & 1) * 36), "v"(1u) : "memory");                               \
    if ((__builtin_amdgcn_readfirstlane(old_) & 7u) == 7u) {                                                            \
      TSR(AWS(gg), 4, (gg) & 63, threadIdx.x & ~63u);                                                                   \
      if (l < 4) set_flag(FL + FL_FW + (((gg) * 4 + l) * 4 + (a >> 2)) * 32 + (a & 3));                              \
    }                                                                                                                   \
    pend = false;                                                                                                       \
  }
#define LDS_U(p) ((unsigned)__builtin_amdgcn_readfirstlane(*(p)))
#define TOK_MIN() __builtin_elementwise_min(LDS_U((volatile lu32*)(cnt + 4 + 2 * hh)), LDS_U((volatile lu32*)(cnt + 5 + 2 * hh)))
  int gp = 0, gs = job_seg(mode), g1 = job_seg(mode + 3);
  {
    const int q = (l >> 2) & 3, p = l & 3, f = q | ((g & 1) << 2);
    const unsigned ra = sb + (4 * g + q) * 256 + (p & 1) * 16;
    unsigned n0 = 0u;
    bool got = false;
#pragma unroll
    for (int st = 0; st < 4; ++st) {
      if (st == 3 && got) WAIT_VM(12); else if (st == 3) WAIT_VM(0); else WAIT_VM(8);
      RAW_BAR();
#pragma unroll
      for (int k2 = 0; k2 < 2; ++k2) {
        const int ks = 4 * (st >> 1) + 2 * k2 + (st & 1);
#pragma unroll
        for (int ct = 0; ct < 2 * NP; ++ct) {
          const int cp = cp0 + (ct >> 1);
          const unsigned a_ = ra + (st & 1) * 65536 + (cp >> 2) * 16384 + k2 * 8192 + ((2 * (cp & 3) + (p >> 1)) ^ f) * 32 + (ct & 1) * 8;
          const v2u lo = trd(a_), hi = trd(a_ + 4096);
          wo[ct][ks] = v4u{lo.x, lo.y, hi.x, hi.y};
        }
#pragma unroll
        for (int ct = 0; ct < 2 * NP; ++ct) asm volatile("" : "+v"(wo[ct][ks]));
      }
      RAW_BAR();
      if (!AT && st == 1) F_LOAD(n0, gs);
      if (st < 2) wo_issue(A.Wo, a, st + 2, sb);
      if (!AT && st == 2 && F_TEST(n0)) {
        T_DMA(0, gs);
        got = true;
      }
    }
    if (!AT) {
      if (!got) {
        (void)wait_n<SLP_A, 1>(FQ + gs * 1024, 1, TMO);
        T_DMA(0, gs);
      }
      WAIT_VM(0);
      T_LAND(0);
    }
  }
  __builtin_amdgcn_s_waitcnt(0x0F70);
  bool pend = false, asked = false;
  unsigned tb1 = now10() - 130 * GPER, tb2 = tb1;
  FSD;
  for (int s = 0; s < NJ; ++s) {
    char* so = smem + AW_SO + (s & 1) * 16384 + l * 16;
    bool got = g1 == 128, land = got, seen = false;
    if (AT) {
      if (TOK_MIN() < (unsigned)(s + 1)) {
        if (pend) {
          WAIT_VM(0);
          W_FLAG(s - 1, gp);
        }
        for (unsigned spins = 0; TOK_MIN() < (unsigned)(s + 1) && spins < 64u * SPIN_CAP; ++spins) __builtin_amdgcn_s_sleep(1);
      }
      asm volatile("" ::: "memory");
      TSR(AWS(gs), 0, gs & 63, 0);
      FS(0);
      const char* tk = smem + AW_TK + ((s & 1) * 2 + hh) * 24 * 1024 + l * 16;
#define TKF(i) (*reinterpret_cast<const bf16x8*>(tk + (i) * 1024))
      bf16x8 q[4], vt[8];
      float x[12], mx;
      bool ok = false;
      {
        f32x4 s0 = {}, s1 = {};
        bf16x8 kd[4], k0[4];
#pragma unroll
        for (int ks = 0; ks < 4; ++ks) {
          q[ks] = TKF(4 * qt + ks);
          kd[ks] = TKF(8 + 4 * qt + ks);
          k0[ks] = TKF(8 + ks);
        }
#pragma unroll
        for (int i = 0; i < 8; ++i) vt[i] = TKF(16 + i);
        __builtin_amdgcn_sched_barrier(0);
#pragma unroll
        for (int ks = 0; ks < 4; ++ks) {
          s0 = MFMA16(kd[ks], q[ks], s0);
          s1 = MFMA16(k0[ks], q[ks], s1);
        }
        scale_pair(s0[0], s0[1], scale, x[0], x[1]);
        scale_pair(s0[2], s0[3], scale, x[2], x[3]);
        scale_pair(s1[0], s1[1], scale, x[4], x[5]);
        scale_pair(s1[2], s1[3], scale, x[6], x[7]);
#pragma unroll
        for (int i = 0; i < 4; ++i) {
          if (4 * g + i > c) x[i] = -3e38f;
          if (!qt) x[4 + i] = -3e38f;
        }
        mx = fmaxf(fmaxf(fmaxf(x[0], x[1]), fmaxf(x[2], x[3])), fmaxf(fmaxf(x[4], x[5]), fmaxf(x[6], x[7])));
        asm volatile("" : "+v"(mx), "+v"(vt[0]), "+v"(vt[1]), "+v"(vt[2]), "+v"(vt[3]), "+v"(vt[4]), "+v"(vt[5]), "+v"(vt[6]), "+v"(vt[7]));
      }
#undef TKF
      FS(1);
      if (asked) {
        WAIT_VM(1);
        M_TEST();
      }
      FS(2);
      if (!ok) {
        if (pend) {
          WAIT_VM(0);
          W_FLAG(s - 1, gp);
        }
        unsigned tb = gs < 64 ? tb1 : tb2, spins = 0;
        gate(tb + gs * GPER);
        for (; !ok; ++spins) {
          if (spin_timeout(spins, TMO)) break;
          if (spins) __builtin_amdgcn_s_sleep(SLP_M);
          M_DMA(gs);
          WAIT_VM(0);
          M_TEST();
        }
        tb = spins > 1 ? now10() - gs * GPER - GMA : tb - GSTEP;
        if (gs < 64) tb1 = tb; else tb2 = tb;
      }
      FS(3);
      __builtin_amdgcn_s_setprio(PRI_A);
      asked = false;
      for (unsigned spins = 0; LDS_U(cM) < 2u * (s + 1) && spins < 64u * SPIN_CAP; ++spins) __builtin_amdgcn_s_sleep(1);
      v4u km[4];
      v2u vm[8];
#pragma unroll
      for (int ks = 0; ks < 4; ++ks) km[ks] = *reinterpret_cast<const v4u*>(smem + mo + (ks * 32 + g * 8 + (c & 7)) * 16);
#pragma unroll
      for (int i = 0; i < 8; ++i) vm[i] = *reinterpret_cast<const v2u*>(smem + mo + 2048 + ((16 * i + c) * MM + 4 * (g & 1)) * 2);
      TSR(AWS(gs), 1, gs & 63, 0);
      FS(4);
      f32x4 m0 = {};
#pragma unroll
      for (int ks = 0; ks < 4; ++ks) m0 = MFMA16(BF8(km[ks]), q[ks], m0);
      scale_pair(m0[0], m0[1], scale, x[8], x[9]);
      scale_pair(m0[2], m0[3], scale, x[10], x[11]);
#pragma unroll
      for (int i = 0; i < 4; ++i) {
        if (g >= 2) x[8 + i] = -3e38f;
        mx = fmaxf(mx, x[8 + i]);
      }
      mx = xmax32(xmax16(mx));
      float sum = 0.f;
#pragma unroll
      for (int i = 0; i < 12; ++i) {
        x[i] = __expf(x[i] - mx);
        sum += x[i];
      }
      sum = xsum32(xsum16(sum));
      const float inv = 1.0f / sum;
      const v2u pa = {pk2(x[0] * inv, x[1] * inv), pk2(x[2] * inv, x[3] * inv)};
      const v2u pb = {pk2(x[4] * inv, x[5] * inv), pk2(x[6] * inv, x[7] * inv)};
      const v4u p0 = qt ? v4u{pb.x, pb.y, pa.x, pa.y} : v4u{pa.x, pa.y, pb.x, pb.y};
      const v2u p1 = {pk2(x[8] * inv, x[9] * inv), pk2(x[10] * inv, x[11] * inv)};
      FS(5);
#pragma unroll
      for (int k = 0; k < 4; ++k) {
        v4u of;
#pragma unroll
        for (int hf = 0; hf < 2; ++hf) {
          f32x4 ao = {};
          ao = MFMA16(vt[2 * k + hf], BF8(p0), ao);
          ao = MFMA16K(vm[2 * k + hf], p1, ao);
          of[2 * hf] = pk2(ao[0], ao[1]);
          of[2 * hf + 1] = pk2(ao[2], ao[3]);
        }
        *reinterpret_cast<v4u*>(so + ((hh * 2 + qt) * 4 + k) * 1024) = of;
      }
      FS(6);
      seen = g1 >= 64 && g1 < 128 && TOK_MIN() >= (unsigned)(s + 2);
      if (pend) {
        WAIT_VM(0);
        W_FLAG(s - 1, gp);
      }
    } else {
      FS(0);
      unsigned n;
      F_LOAD(n, g1);
      if (pend) {
        WAIT_VM(1);
        W_FLAG(s - 1, gp);
      }
      WAIT_VM(0);
      FS(1);
      if (F_TEST(n)) {
        T_DMA(s + 1, g1);
        got = true;
        if (LDS_U(cM) < 2u * (s + 1)) {
          WAIT_VM(0);
          T_LAND(s + 1);
          land = true;
        }
      }
    }
    FS(7);
    RAW_BAR();
    FS(8);
    __builtin_amdgcn_s_setprio(AT ? PRI_WA : PRI_WB);
    TSR(AWS(gs), 2, gs & 63, 256);
    const int voff = (c >> 1) * 2048 + (c & 1) * 64 + g * 16, soff = (gs * 16 + a) * 32768 + cp0 * 128;
#define OFRAG(i) (*reinterpret_cast<const bf16x8*>(so + (((((i) & 7) >> 2) * 2 + ((i) >> 3)) * 4 + ((i) & 3)) * 1024))
    constexpr int NO = AT ? 16 : 3;
    bf16x8 o[NO];
#pragma unroll
    for (int i = 0; i < (AT ? 16 : 2); ++i) o[i] = OFRAG(i);
    unsigned pr;
    const bool look = seen && g1 >= 128 - AWD;
    if (AT) {
      asm volatile("s_cmp_eq_u32 %3, 0\n\ts_cbranch_scc1 1f\n\ts_nop 4\n\tglobal_load_dword %0, %1, %2 sc1\n1:"
                   : "=v"(pr) : "v"(2044), "s"(gM + (size_t)g1 * (NH * 2048)), "s"(__builtin_amdgcn_readfirstlane((int)look)) : "memory", "scc");
    } else {
      F_LOAD(pr, got ? 128 : g1);
    }
    __builtin_amdgcn_sched_barrier(0);
    FS(9);
#pragma unroll
    for (int rt = 0; rt < 2; ++rt) {
      f32x4 acc[2 * NP] = {};
#pragma unroll
      for (int kk = 0; kk < 8; ++kk) {
        const int i = 8 * rt + kk;
        if (!AT && i + 2 < 16) o[(i + 2) % NO] = OFRAG(i + 2);
#pragma unroll
        for (int ct = 0; ct < 2 * NP; ++ct) acc[ct] = MFMA16(BF8(wo[ct][kk]), o[i % NO], acc[ct]);
        if (!AT) __builtin_amdgcn_sched_barrier(0);
      }
      if (rt == 0) FS(10); else FS(12);
      if (AT && rt == 1 && seen) {
        if (look) {
          WAIT_VM(1);
          seen = F_TEST(pr);
        }
        if (seen) {
          M_DMA(g1);
          asked = true;
        }
      }
      if (!AT && rt == 0) {
        WAIT_VM(0);
        if (got && !land) {
          T_LAND(s + 1);
          land = true;
        }
        if (F_TEST(pr)) {
          T_DMA(s + 1, g1);
          got = true;
        }
      }
      if (!AT && rt == 1 && got && !land) {
        WAIT_VM(3);
        T_LAND(s + 1);
      }
#pragma unroll
      for (int u = 0; u < NP; ++u)
        ST128((v4u{pk2(acc[2 * u][0], acc[2 * u][1]), pk2(acc[2 * u][2], acc[2 * u][3]), pk2(acc[2 * u + 1][0], acc[2 * u + 1][1]),
                   pk2(acc[2 * u + 1][2], acc[2 * u + 1][3])}),
              rsW, voff + u * 128, soff + rt * 16384);
      if (rt == 0) FS(11); else FS(13);
    }
    __builtin_amdgcn_s_setprio(AT ? PRI_0 : 0);
    pend = true;
    FD(s);
    TSR(AWS(gs), 3, gs & 63, 256);
    TSR(AWS(gs), 5, gs & 63, 0);
    if (!AT && !got) {
      WAIT_VM(0);
      W_FLAG(s, gs);
      (void)wait_n<SLP_A, 1>(FQ + g1 * 1024, 1, TMO);
      T_DMA(s + 1, g1);
      WAIT_VM(0);
      T_LAND(s + 1);
    }
    gp = gs;
    gs = g1;
    g1 = job_seg(3 * s + 6 + mode);
  }
  WAIT_VM(0);
  W_FLAG(NJ - 1, gp);
#undef AWS
#undef OFRAG
#undef DMA4
#undef T_DMA
#undef T_LAND
#undef M_DMA
#undef M_TEST
#undef F_LOAD
#undef F_TEST
#undef W_FLAG
#undef LDS_U
#undef TOK_MIN
}
__device__ __forceinline__ void role_aw(const MegaArgs& A, char* smem, const int L, const int a) {
  if (__builtin_amdgcn_readfirstlane(threadIdx.x >> 6) < 4) role_aw_t<true>(A, smem, L, a);
  else role_aw_t<false>(A, smem, L, a);
}

__device__ __forceinline__ void role_red(const MegaArgs& A, char* smem, const int idx) {
  const int w = __builtin_amdgcn_readfirstlane(threadIdx.x >> 6);
  const int l = threadIdx.x & 63;
  const int gr = w >> 1, r = idx * 2 + (w & 1);
  char* ws = A.ws;
  unsigned* FL = (unsigned*)(ws + OFF_FL);
  unsigned* TMO = FL + FL_TMO;
  const __amdgpu_buffer_rsrc_t rsW = MKRS(ws + OFF_PW, SZ_PW);
  const __amdgpu_buffer_rsrc_t rsX = MKRS(ws + OFF_XA, OFF_X3 - OFF_XA + SZ_X);
  volatile unsigned* sw = reinterpret_cast<volatile unsigned*>(smem) + (w >> 1) * 32;
  if (threadIdx.x < 128) reinterpret_cast<volatile unsigned*>(smem)[threadIdx.x] = 0u;
  __syncthreads();
  unsigned tb1 = now10() - 130 * GPER, tb2 = tb1;
  const int rn1 = idx < 8 ? 96 + idx : 224 + idx;
  (void)rn1;
  if (w >= 6) return;
  if (w < 4) {
    const __amdgpu_buffer_rsrc_t rz = MKRS(ws + OFF_X2, SZ_X);
    for (int s = gr; s < NSEG; s += 2) ST128((v4u{0u, 0u, 0u, 0u}), rz, l * 16, (s * SEG + r) * DM * 2);
    DRAIN();
    if (l < 4) set_flag(FL + FL_FZ + (2 * l + (idx >> 3)) * 32 + (idx & 7) * 4 + w);
    if (l == 0) __hip_atomic_fetch_add((lu32*)(unsigned long)(lds_base(smem) + 480), 1u, __ATOMIC_RELAXED, __HIP_MEMORY_SCOPE_WORKGROUP);
  }
  for (unsigned spins = 0; reinterpret_cast<volatile unsigned*>(smem)[120] < 4u && spins < 64u * SPIN_CAP; ++spins) __builtin_amdgcn_s_sleep(2);
  for (int n = gr; n < 128; n += 3) {
    const int gs = job_seg(n), L = gs >> 6, s = gs & 63;
    const unsigned* fw = FL + FL_FW + (gs * 4 + REP) * 4 * 32;
    unsigned tb = L ? tb2 : tb1;
    tb = grp_wait<SLP_R, 4>((w & 1) == 0, sw, n + 1, fw, 16, TMO, tb + gs * GPER) ? now10() - gs * GPER - GMR : tb - GSTEP;
    if (n >= 125 - AWD) tb = now10() + GATE_RT - (n + 3) * GPER;
    if (L || n >= 125 - AWD) tb2 = tb; else tb1 = tb;
#define RTS(k, t) TSR(L ? A.rn : rn1, k, s, (w & 6) * 64 + (t))
    RTS(0, 0);
    RTS(4, 64);
    v4u p[16];
    const v4u xr = LD128(rsX, l * 512, L * (int)(OFF_X3 - OFF_XA) + s * 32768 + r * 16);
#pragma unroll
    for (int k = 0; k < 16; ++k)
      p[k] = LD128(rsW, (l >> 2) * 128 + (l & 3) * 16, (gs * 16 + k) * 32768 + (r >> 1) * 2048 + (r & 1) * 64);
    v4u xo;
    const unsigned nz = L ? 0u : 0x8000u;
#pragma unroll
    for (int e = 0; e < 4; ++e) {
      f32x2_t t[4];
#pragma unroll
      for (int k = 0; k < 4; ++k)
        t[k] = (f32x2_t{lo16(p[4 * k][e]), hi16(p[4 * k][e])} + f32x2_t{lo16(p[4 * k + 1][e]), hi16(p[4 * k + 1][e])}) +
               (f32x2_t{lo16(p[4 * k + 2][e]), hi16(p[4 * k + 2][e])} + f32x2_t{lo16(p[4 * k + 3][e]), hi16(p[4 * k + 3][e])});
      const f32x2_t u = (t[0] + t[1]) + (t[2] + t[3]);
      const unsigned a0 = pk2(u.x, u.y);
      xo[e] = pk2(lo16(xr[e]) + lo16(a0), hi16(xr[e]) + hi16(a0));
      xo[e] = xo[e] ? xo[e] : nz;
    }
    ST128(xo, MKRS(L ? (char*)A.out : ws + OFF_X2, SZ_X), l * 16, (s * SEG + r) * DM * 2);
    RTS(1, 0);
    RTS(5, 64);
    if (L == 0) {
      const unsigned st = row_stats(xo);
      if (l < 4) __hip_atomic_store((gu32*)(FL + FL_FR + ((s * 4 + l) * 4 + (r >> 3)) * 32 + (r & 7)), st, RLX_AGENT);
    }
    RTS(6, 64);
    RTS(3, 0);
#undef RTS
  }
  if (idx == 0 && threadIdx.x == 128) {
    gu64* en = (gu64*)(ws + OFF_TAG + 8192);
    __hip_atomic_store(en, __hip_atomic_load(en, RLX_AGENT) + TAG_STEP, RLX_AGENT);
  }
}

constexpr int CUN = 2144;
__device__ __forceinline__ bf16x8 conv_bfrag(const unsigned* up, const unsigned sh) {
  const unsigned d0 = up[0], d1 = up[1], d2 = up[2], d3 = up[3], d4 = up[4];
  const v4u r = {__builtin_amdgcn_alignbit(d1, d0, sh), __builtin_amdgcn_alignbit(d2, d1, sh),
                 __builtin_amdgcn_alignbit(d3, d2, sh), __builtin_amdgcn_alignbit(d4, d3, sh)};
  return BF8(r);
}
__device__ __forceinline__ void role_conv(const MegaArgs& A, char* smem, const int cv) {
  __builtin_amdgcn_s_waitcnt(0x0F70);
  u16* Kr = reinterpret_cast<u16*>(smem);
  u16* Uc = Kr + 8 * 2 * CKN;
  u16* Xr = Uc + 8 * CUN;
  u16* To = Uc + 8 * 2 * CUN;
  u16* Su = To + 32 * 8;
  u16* Sx = Su + 32 * 8;
  const int tid = threadIdx.x;
  const int w = __builtin_amdgcn_readfirstlane(tid >> 6);
  const int l = tid & 63, c = l & 15, g = l >> 4;
  const int d = 8 * cv + w;
  char* ws = A.ws;
  unsigned* FL = (unsigned*)(ws + OFF_FL);
  unsigned* TMO = FL + FL_TMO;
  u16* kr = Kr + w * 2 * CKN;
  float* prm = reinterpret_cast<float*>(kr);
  v4u tx[4];
  const v2u en = LD64(MKRS(ws + OFF_TAG, 8192 + 8), 0, 8192);
  {
    const unsigned pa = *reinterpret_cast<const unsigned*>(A.logAre + d * NST + 2 * l);
    const unsigned pb = *reinterpret_cast<const unsigned*>(A.Aim + d * NST + 2 * l);
    const unsigned pc = *reinterpret_cast<const unsigned*>(A.Cre + d * NST + 2 * l);
    const unsigned pd = *reinterpret_cast<const unsigned*>(A.Cim + d * NST + 2 * l);
    const float dt = bfr(expf(bf2f(A.logdt[d])));
    if (w < 2) tx[0] = *reinterpret_cast<const v4u*>(A.x0 + (size_t)tid * DM + 8 * cv);
    if (l < 16) reinterpret_cast<unsigned*>(Uc + w * CUN)[l] = 0u;
    if (l < 32) reinterpret_cast<unsigned*>(Uc + w * CUN + 32 + L_SEQ)[l] = 0u;
    if (l < 32) reinterpret_cast<unsigned*>(Uc + w * CUN + 32 + 128)[l] = 0u;
    if (l < 50) {
      reinterpret_cast<v4u*>(kr + 1808)[l] = v4u{0u, 0u, 0u, 0u};
      reinterpret_cast<v4u*>(kr + CKN + 1808)[l] = v4u{0u, 0u, 0u, 0u};
    }
#pragma unroll
    for (int e = 0; e < 2; ++e) {
      const float ar = -bfr(expf(e ? hi16(pa) : lo16(pa))) * dt, ai = (e ? hi16(pb) : lo16(pb)) * dt;
      const float ex = expf(ar);
      float sn, cs;
      sincosf(ai, &sn, &cs);
      prm[2 * l + e] = ar;
      prm[NST + 2 * l + e] = ai;
      prm[2 * NST + 2 * l + e] = (e ? hi16(pc) : lo16(pc)) * dt;
      prm[3 * NST + 2 * l + e] = (e ? hi16(pd) : lo16(pd)) * dt;
      prm[4 * NST + 2 * l + e] = ex * cs;
      prm[5 * NST + 2 * l + e] = ex * sn;
    }
  }
  const float dsk = bf2f(A.Dskip[d]);
  const u16* ka = kr + (c >> 3) * CKN + 2080 - 16 * (c & 7) + 8 * g;
  const unsigned ush = (c & 1) * 16;
#define KFRAG(D0) (*reinterpret_cast<const bf16x8*>(ka - (D0)))
  f32x4 acc[16], far[5];
  v4u xf[2];
  const unsigned* ub1 = reinterpret_cast<const unsigned*>(Uc + w * CUN + (c & ~1) + 8 * g + 32);
  const __amdgpu_buffer_rsrc_t rz1 = MKRS(ws + OFF_XA, SZ_X);
  const int t0 = 16 * (4 * (g & 1) + (g >> 1)) + c;
  {
    const u16* uc = Uc + w * CUN;
    const unsigned* ub = reinterpret_cast<const unsigned*>(uc + (c & ~1) + 8 * g + 32);
    const __amdgpu_buffer_rsrc_t rz = MKRS(ws + OFF_XA, SZ_X);
    const __amdgpu_buffer_rsrc_t rst = MKRS(ws + OFF_GT, L_SEQ * 4);
#define TILE_OUT(T)                                                                            \
  {                                                                                            \
    const float u0 = bf2f(uc[32 + 128 * (T) + t0]), u1 = bf2f(uc[64 + 128 * (T) + t0]);        \
    u16* xp = Xr + (128 * (T) + t0) * 8 + w;                                                   \
    const unsigned yy = pk2(y0, y1), uu = pk2(u0 * dsk, u1 * dsk);                             \
    const unsigned aa = pk2(lo16(yy) + lo16(uu), hi16(yy) + hi16(uu));                         \
    const unsigned oo = pk2(bf2f(xp[0]) + lo16(aa), bf2f(xp[256]) + hi16(aa));                 \
    xp[0] = (u16)oo;                                                                           \
    xp[256] = (u16)(oo >> 16);                                                                 \
  }
#define CHUNK_ST(ck)                                                                           \
  {                                                                                            \
    const v4u o = *reinterpret_cast<const v4u*>(Xr + (64 * (ck) + l) * 8);                     \
    ST128(o, rz, (l >> 5) * 32768 + (l & 31) * 16, (2 * (ck) * 64 + cv) * 512);                \
  }
#define U_ROW(row) U_ROW2(row, __builtin_amdgcn_raw_buffer_load_b32(rst, (row) * 4, 0, AUX_SC1))
#define U_ROW2(row, stv)                                                                       \
  {                                                                                            \
    const unsigned st_ = (stv);                                                                \
    const v4u xr_ = *reinterpret_cast<const v4u*>(Xr + (row) * 8);                             \
    const float mb_ = lo16(st_), ri_ = 1.0f / hi16(st_);                                       \
    _Pragma("unroll") for (int e = 0; e < 4; ++e) {                                            \
      const unsigned t_ = pk2(lo16(xr_[e]) - mb_, hi16(xr_[e]) - mb_);                         \
      const unsigned q_ = pk2(lo16(t_) * ri_, hi16(t_) * ri_);                                 \
      Uc[(2 * e) * CUN + 32 + (row)] = (u16)q_;                                                \
      Uc[(2 * e + 1) * CUN + 32 + (row)] = (u16)(q_ >> 16);                                    \
    }                                                                                          \
  }
    const unsigned* FS = FL + FL_FS + REP * 32 * 32;
    const int gi = 4 * (g * 8 + (l & 7));
    const int tl = 32 * ((l >> 3) & 1) + 8 * g + (l & 7);
    unsigned khl = 0u;
#pragma unroll 1
    for (int ps = 0; ps < 4; ++ps) {
      float v[32];
      {
        const f32x4 ar = *reinterpret_cast<const f32x4*>(prm + gi), ai = *reinterpret_cast<const f32x4*>(prm + NST + gi);
        const f32x4 cr = *reinterpret_cast<const f32x4*>(prm + 2 * NST + gi), ci = *reinterpret_cast<const f32x4*>(prm + 3 * NST + gi);
        const f32x4 wr = *reinterpret_cast<const f32x4*>(prm + 4 * NST + gi), wi = *reinterpret_cast<const f32x4*>(prm + 5 * NST + gi);
        const float p0 = (float)(64 * ps + 32 * ((l >> 3) & 1));
        float yr0[4], yi0[4];
#pragma unroll
        for (int k = 0; k < 4; ++k) {
          const float e = __expf(ar[k] * p0);
          float sn, cs;
          __sincosf(ai[k] * p0, &sn, &cs);
          const float zr = e * cs, zi = e * sn;
          yr0[k] = cr[k] * zr - ci[k] * zi;
          yi0[k] = cr[k] * zi + ci[k] * zr;
        }
        f32x2_t yra = {yr0[0], yr0[1]}, yia = {yi0[0], yi0[1]}, wra = {wr[0], wr[1]}, wia = {wi[0], wi[1]};
        f32x2_t yrb = {yr0[2], yr0[3]}, yib = {yi0[2], yi0[3]}, wrb = {wr[2], wr[3]}, wib = {wi[2], wi[3]};
#pragma unroll
        for (int i = 0; i < 32; ++i) {
          const f32x2_t sm2 = yra + yrb;
          v[i] = sm2.x + sm2.y;
          const f32x2_t ta = yra * wra - yia * wia, tb = yrb * wrb - yib * wib;
          yia = yra * wia + yia * wra;
          yib = yrb * wib + yib * wrb;
          yra = ta;
          yrb = tb;
        }
      }
#pragma unroll
      for (int i = 0; i < 16; ++i) v[i] = swap_add32(v[i], v[i + 16]);
#pragma unroll
      for (int i = 0; i < 8; ++i) {
        v[i] = swap_add16(v[i], v[i + 8]);
        v[i] += dppf<0xB1>(0.f, v[i]);
        v[i] += dppf<0x4E>(0.f, v[i]);
        v[i] += dppf<0x141>(0.f, v[i]);
      }
      float s0 = v[0], s1 = v[1], s2 = v[2], s3 = v[3], s4 = v[4], s5 = v[5], s6 = v[6], s7 = v[7];
      asm volatile("" : "+v"(s0), "+v"(s1), "+v"(s2), "+v"(s3), "+v"(s4), "+v"(s5), "+v"(s6), "+v"(s7));
      s0 = (l & 4) ? s4 : s0; s1 = (l & 4) ? s5 : s1; s2 = (l & 4) ? s6 : s2; s3 = (l & 4) ? s7 : s3;
      s0 = (l & 2) ? s2 : s0; s1 = (l & 2) ? s3 : s1;
      const float kv = 2.0f * ((l & 1) ? s1 : s0);
      khl = pk2(kv, 0.f);
      khl = pk2(kv, kv - lo16(khl));
      kr[2080 - 64 * ps - tl] = (u16)khl;
      kr[CKN + 2080 - 64 * ps - tl] = (u16)(khl >> 16);
      SST(ps);
      if (ps < 2) {
        if (ps == 0) {
          if (w < 2) {
            reinterpret_cast<v4u*>(Xr)[tid] = tx[0];
            const unsigned long long m = (((unsigned long long)en.y << 32) | en.x) + TAG_STEP;
            v4u g0;
            for (unsigned spins = 0; spins < 16u * SPIN_CAP; ++spins) {
              g0 = LD128(MKRS(ws + OFF_G0, 8192), tid * 16, REP * 2048);
              if (__all(((g0.x ^ g0.z) | (g0.y ^ (unsigned)m) | (g0.w ^ (unsigned)(m >> 32))) == 0u)) break;
              asm volatile("s_sleep 2" ::: "memory");
            }
            U_ROW2(tid, g0.x);
          }
          __syncthreads();
        }
        f32x4 a0t = {};
#pragma unroll
        for (int b = -1; b < 4; ++b) a0t = MFMA16(KFRAG(-32 * b), conv_bfrag(ub + 16 * b, ush), a0t);
        const float y0 = swap_add32(a0t[0], a0t[1]), y1 = swap_add32(a0t[2], a0t[3]);
        if ((g & 1) == ps) TILE_OUT(0);
        __syncthreads();
        if (w == ps) {
          CHUNK_ST(w);
          if (ps == 0) zero_wait(ws);
          DRAIN();
          if (l < 8) set_flag(FL + FL_FC + ((2 * w * 4 + l) * 8 + (cv >> 3)) * 32 + (cv & 7));
        }
        SST(4 + ps);
      }
      if (ps == 2) {
        if (w >= 2) tx[0] = *reinterpret_cast<const v4u*>(A.x0 + (size_t)tid * DM + 8 * cv);
#pragma unroll
        for (int i = 1; i < 4; ++i) tx[i] = *reinterpret_cast<const v4u*>(A.x0 + (size_t)(tid + 512 * i) * DM + 8 * cv);
      }
    }
    if (w >= 2) reinterpret_cast<v4u*>(Xr)[tid] = tx[0];
#pragma unroll
    for (int i = 1; i < 4; ++i) reinterpret_cast<v4u*>(Xr)[tid + 512 * i] = tx[i];
    {
      unsigned spins = 0;
      while (!__all(__hip_atomic_load((gu32*)FS + (w + 8 * ((l >> 3) & 3)) * 32 + (l & 7), RLX_AGENT) != 0u)) {
        if (spin_timeout(++spins, TMO)) break;
        __builtin_amdgcn_s_sleep(4);
      }
      asm volatile("" ::: "memory");
    }
    if (w >= 2) U_ROW(tid);
#pragma unroll
    for (int i = 1; i < 4; ++i) U_ROW(tid + 512 * i);
    SST(6);
    __syncthreads();
    {
      f32x4 a1t = {};
#pragma unroll
      for (int b = -1; b < 8; ++b) a1t = MFMA16(KFRAG(128 - 32 * b), conv_bfrag(ub + 16 * b, ush), a1t);
      const float y0 = swap_add32(a1t[0], a1t[1]), y1 = swap_add32(a1t[2], a1t[3]);
      TILE_OUT(1);
    }
    __syncthreads();
    if (w < 2) {
      CHUNK_ST(2 + w);
      DRAIN();
      if (l < 8) set_flag(FL + FL_FC + (((4 + 2 * w) * 4 + l) * 8 + (cv >> 3)) * 32 + (cv & 7));
    }
    SST(8);
    (void)wait_n<8, 1>(FL + FL_KG + cv * 32 + (w >> 2), 1, TMO);
    SST(7);
    {
      const __amdgpu_buffer_rsrc_t rk = MKRS(ws + OFF_KF + (size_t)d * 2 * CKN * 2, 2 * CKN * 2);
      v4u t[8];
#pragma unroll
      for (int i = 0; i < 4; ++i) {
        t[i] = LD128(rk, (64 * i + l) * 16, 0);
        t[4 + i] = LD128(rk, (64 * i + l) * 16, CKN * 2);
      }
#pragma unroll
      for (int i = 0; i < 4; ++i)
        if (64 * i + l < 229) {
          reinterpret_cast<v4u*>(kr)[64 * i + l] = t[i];
          reinterpret_cast<v4u*>(kr + CKN)[64 * i + l] = t[4 + i];
        }
      kr[2080 - 192 - tl] = (u16)khl;
      kr[CKN + 2080 - 192 - tl] = (u16)(khl >> 16);
    }
    asm volatile("" ::: "memory");
#define BULK(TA, TB, HOOK)                                                                      \
  {                                                                                            \
    const bf16x8 bm = conv_bfrag(ub - 16, ush);                                                \
    _Pragma("unroll") for (int T = (TA); T < (TB); ++T) acc[T] = MFMA16(KFRAG(128 * T + 32), bm, (f32x4{})); \
    _Pragma("unroll") for (int T = 11; T < 16; ++T) far[T - 11] = MFMA16(KFRAG(128 * T + 32), bm, (f32x4{})); \
    _Pragma("unroll 1") for (int r = 0; r < 4; ++r) {                                          \
      bf16x8 bf[TB];                                                                           \
      _Pragma("unroll") for (int j = 0; j < (TB); ++j) bf[j] = conv_bfrag(ub + 16 * r + 64 * j, ush); \
      _Pragma("unroll") for (int e = 0; e < (TB); ++e) {                                       \
        const bf16x8 a = KFRAG(128 * e - 32 * r);                                              \
        _Pragma("unroll") for (int T = (e > (TA) ? e : (TA)); T < (TB); ++T) acc[T] = MFMA16(a, bf[T - e], acc[T]); \
      }                                                                                        \
      if (r == 0) { HOOK }                                                                     \
    }                                                                                          \
  }
    BULK(2, 11, if (w == 1) (void)(wait_n<SLP_V, 32>(FL + FL_FZ + REP * 64, 64, TMO)););
    SST(10);
#pragma unroll
    for (int T = 2; T < 11; ++T) {
      const float y0 = swap_add32(acc[T][0], acc[T][1]), y1 = swap_add32(acc[T][2], acc[T][3]);
      TILE_OUT(T);
    }
    __syncthreads();
    if (w >= 2) {
#pragma unroll
      for (int i = 0; i < 3; ++i) CHUNK_ST(3 * w - 2 + i);
#pragma unroll
      for (int i = 0; i < 2; ++i) xf[i] = *reinterpret_cast<const v4u*>(Xr + (64 * ((18 + 2 * w + i) & 31) + l) * 8);
      DRAIN();
      if (l < 24) set_flag(FL + FL_FC + ((6 * w - 4) * 4 + l) * 8 * 32 + (cv >> 3) * 32 + (cv & 7));
    }
#define FAR_PIECE(p)                                                                           \
  {                                                                                            \
    if ((p) < 24) {                                                                            \
      const int o_ = 256 * ((p) >> 2) + 32 * ((p) & 3);                                        \
      const bf16x8 b0 = conv_bfrag(ub1 + (o_ >> 1), ush), b1 = conv_bfrag(ub1 + (o_ >> 1) + 64, ush); \
      _Pragma("unroll") for (int e = 10; e < 16; ++e) {                                        \
        const bf16x8 a = KFRAG(128 * e - o_);                                                  \
        if (e > 10) far[e - 11] = MFMA16(a, b0, far[e - 11]);                                  \
        if (e < 15) far[e - 10] = MFMA16(a, b1, far[e - 10]);                                  \
      }                                                                                        \
    } else if ((p) < 28) {                                                                     \
      bf16x8 bf[4];                                                                            \
      _Pragma("unroll") for (int j = 0; j < 4; ++j) bf[j] = conv_bfrag(ub1 + 16 * ((p) & 3) + 64 * (12 + j), ush); \
      _Pragma("unroll") for (int e = 0; e < 4; ++e) {                                          \
        const bf16x8 a = KFRAG(128 * e - 32 * ((p) & 3));                                      \
        _Pragma("unroll") for (int j = 0; j + e < 4; ++j) far[1 + e + j] = MFMA16(a, bf[j], far[1 + e + j]); \
      }                                                                                        \
    } else if ((p) == 28) {                                                                    \
                     \
      u16* ucw = Uc + w * CUN + 32 + t0;                                                       \
      _Pragma("unroll") for (int T = 11; T < 16; ++T) {                                        \
        const float y0 = swap_add32(far[T - 11][0], far[T - 11][1]), y1 = swap_add32(far[T - 11][2], far[T - 11][3]); \
        const float u0 = bf2f(ucw[128 * T]), u1 = bf2f(ucw[128 * T + 32]);                     \
        const unsigned yy = pk2(y0, y1), uu = pk2(u0 * dsk, u1 * dsk);                         \
        const unsigned aa = pk2(lo16(yy) + lo16(uu), hi16(yy) + hi16(uu));                     \
        ucw[128 * T] = (u16)aa;                                                                \
        ucw[128 * T + 32] = (u16)(aa >> 16);                                                   \
      }                                                                                        \
    } else if (w >= 2 && w < 7) {                                                              \
      int l = tid & 63;                                                                        \
      asm volatile("" : "+v"(l), "+v"(xf[0]), "+v"(xf[1]));                                    \
      if ((p) == 29) {                                                                         \
        _Pragma("unroll") for (int i = 0; i < 2; ++i) {                                        \
          const int ck = 18 + 2 * w + i;                                                       \
          const u16* ur = Uc + 32 + 64 * ck + l;                                               \
          v4u o;                                                                               \
          _Pragma("unroll") for (int e = 0; e < 4; ++e)                                        \
            o[e] = pk2(lo16(xf[i][e]) + bf2f(ur[(2 * e) * CUN]), hi16(xf[i][e]) + bf2f(ur[(2 * e + 1) * CUN])); \
          ST128(o, rz1, (l >> 5) * 32768 + (l & 31) * 16, (2 * ck * 64 + cv) * 512);           \
        }                                                                                      \
      } else {                                                                                 \
        DRAIN();                                                                               \
        if (l < 16) set_flag(FL + FL_FC + (((36 + 4 * w) * 4 + l) * 8 + (cv >> 3)) * 32 + (cv & 7)); \
      }                                                                                        \
    }                                                                                          \
  }
#undef BULK
    SST(9);
#undef TILE_OUT
#undef CHUNK_ST
#undef U_ROW
#undef U_ROW2
  }
  u16* uc = Uc + (8 + w) * CUN;
  const unsigned* ub = reinterpret_cast<const unsigned*>(uc + (c & ~1) + 8 * g + 32);
#pragma unroll
  for (int j = 0; j < 16; ++j) acc[j] = f32x4{};
  const __amdgpu_buffer_rsrc_t rx = MKRS(ws + OFF_X2, SZ_X), rz = MKRS(ws + OFF_X3, SZ_X);
  const __amdgpu_buffer_rsrc_t rf = MKRS(FL + FL_FR, 64 * 16 * 128);
  unsigned st = 0u;
  v2u xx = {0u, 0u};
#define X2_LOAD(ss, far)                                                                                                      \
  {                                                                                                                      \
    xx = LD64(rx, (l >> 1) * (DM * 2) + (l & 1) * 8, (far) ? (int)SZ_X : ((ss) * SEG * DM + 8 * cv) * 2);                 \
    st = __builtin_amdgcn_raw_buffer_load_b32(rf, (l >> 4) * 128 + ((l >> 1) & 7) * 4, ((ss) * 4 + REP) * 512, AUX_SC1); \
  }
  unsigned tl = now10();
  for (int s = 0; s < NSEG; ++s) {
    if (w == 1) {
      for (unsigned spins = 0; !__all(umin3(xx.x, xx.y, st) != 0u); ++spins) {
        if (spin_timeout(spins, TMO)) break;
        if (GATE_V && spins == 0 && !__any(st != 0u)) gate(tl);
        if (spins && !__all(st != 0u)) __builtin_amdgcn_s_sleep(SLP_V);
        X2_LOAD(s, __builtin_popcountll(__ballot(st != 0u)) < CVTHR);
      }
      tl = now10() + GATE_V;
      TS(0, s, 64);
      const float mb = lo16(st), ri = 1.0f / hi16(st);
      const unsigned t0 = pk2(lo16(xx.x) - mb, hi16(xx.x) - mb), t1 = pk2(lo16(xx.y) - mb, hi16(xx.y) - mb);
      *reinterpret_cast<v2u*>(Su + l * 4) = v2u{pk2(lo16(t0) * ri, hi16(t0) * ri), pk2(lo16(t1) * ri, hi16(t1) * ri)};
      *reinterpret_cast<v2u*>(Sx + l * 4) = xx;
    }
    const int q = s & 3;
    const u16* kq = ka + 32 * q;
    const bf16x8 a0 = *reinterpret_cast<const bf16x8*>(kq - 32), a1 = *reinterpret_cast<const bf16x8*>(kq);
    __syncthreads();
    TS(1, s, 0);
    if (s == 0)
      for (int x = l; x < CUN / 2; x += 64) reinterpret_cast<unsigned*>(uc)[x] = 0u;
    if (l < 32) uc[32 + 32 * s + l] = Su[l * 8 + w];
    asm volatile("s_waitcnt lgkmcnt(0)" ::: "memory");
    const bf16x8 bp = conv_bfrag(ub + 16 * (s - 1), ush);
    acc[0] = MFMA16(a0, bp, (q == 0 ? acc[1] : acc[0]));
    const f32x4 tmp = MFMA16(a1, conv_bfrag(ub + 16 * s, ush), acc[0]);
    float y0 = (q & 1) ? tmp[2] : tmp[0], y1 = (q & 1) ? tmp[3] : tmp[1];
    y0 = xsum32(y0);
    y1 = xsum32(y1);
    if (g == (q >> 1)) {
      const float u0 = bf2f(Su[c * 8 + w]), u1 = bf2f(Su[(16 + c) * 8 + w]);
      const float a0 = bfr(bfr(y0) + bfr(u0 * dsk)), a1 = bfr(bfr(y1) + bfr(u1 * dsk));
      To[c * 8 + w] = f2bf(bf2f(Sx[c * 8 + w]) + a0);
      To[(16 + c) * 8 + w] = f2bf(bf2f(Sx[(16 + c) * 8 + w]) + a1);
    }
    __syncthreads();
    if (w == 0 && l < 32) {
      const v4u o = *reinterpret_cast<const v4u*>(To + l * 8);
      ST128(o, rz, l * 16, (s * 64 + cv) * 512);
    }
    if (w == 1) X2_LOAD(s + 1, CVTHR > 0);
    const int left = 15 - (s >> 2);
#define PUSH(J0, SH)                                                                                              \
  if (left >= (J0)) {                                                                                             \
    _Pragma("unroll") for (int j = (J0); j < (J0) + 4 && j < 16; ++j)                                             \
      acc[j] = MFMA16(*reinterpret_cast<const bf16x8*>(kq - 32 - 128 * j), bp, (j + (SH) < 16 ? acc[(j + (SH)) & 15] : f32x4{})); \
  }
    TS(2, s, 0);
    if (w == 0) {
      DRAIN();
      if (l < 4) set_flag(FL + FL_FC + (((64 + s) * 4 + l) * 8 + (cv >> 3)) * 32 + (cv & 7));
    }
    TS(3, s, 0);
    if (q == 0) { PUSH(1, 1) PUSH(5, 1) PUSH(9, 1) PUSH(13, 1) }
    else { PUSH(1, 0) PUSH(5, 0) PUSH(9, 0) PUSH(13, 0) }
#undef PUSH
    MSTAMP();
    if (s >= CVPRE && s < 31 + CVPRE) FAR_PIECE(s - CVPRE);
  }
#undef KFRAG
#undef X2_LOAD
#undef FAR_PIECE
}

__device__ __forceinline__ v4u ln_row_packed(const v4u xr) {
  float v[8];
#pragma unroll
  for (int e = 0; e < 4; ++e) { v[2 * e] = lo16(xr[e]); v[2 * e + 1] = hi16(xr[e]); }
  float s = ((v[0] + v[1]) + (v[2] + v[3])) + ((v[4] + v[5]) + (v[6] + v[7]));
  s = wave_sum(s);
  const float mean = s * (1.0f / DM);
  float q = 0.f;
#pragma unroll
  for (int j = 0; j < 8; ++j) { float t = v[j] - mean; q += t * t; }
  q = wave_sum(q);
  const float var = q * (1.0f / DM);
  const float mb = bfr(mean);
  const float sd = bfr(sqrtf(bfr(bfr(var) + 1e-5f)));
  const float ri = 1.0f / sd;
  v4u o;
#pragma unroll
  for (int e = 0; e < 4; ++e) {
    const unsigned t = pk2(v[2 * e] - mb, v[2 * e + 1] - mb);
    const float x0 = lo16(t), x1 = hi16(t);
    float q0 = x0 * ri, q1 = x1 * ri;
    q0 = __builtin_fmaf(__builtin_fmaf(-q0, sd, x0), ri, q0);
    q1 = __builtin_fmaf(__builtin_fmaf(-q1, sd, x1), ri, q1);
    o[e] = pk2(q0, q1);
  }
  return o;
}

__device__ __forceinline__ float bfr1(float x) { return lo16(pk2(x, x)); }
__device__ __forceinline__ void ln4_packed(v4u (&x)[4]) {
  f32x2_t v[16];
#pragma unroll
  for (int e = 0; e < 16; ++e) v[e] = f32x2_t{lo16(x[e >> 2][e & 3]), hi16(x[e >> 2][e & 3])};
  const f32x2_t t = (((v[0] + v[1]) + (v[2] + v[3])) + ((v[4] + v[5]) + (v[6] + v[7]))) +
                    (((v[8] + v[9]) + (v[10] + v[11])) + ((v[12] + v[13]) + (v[14] + v[15])));
  const float mean = red_mod4(t.x + t.y) * (1.0f / DM);
  const float mb = bfr1(mean);
  f32x2_t q0 = {0.f, 0.f}, q1 = {0.f, 0.f};
#pragma unroll
  for (int e = 0; e < 16; e += 2) {
    v[e] -= mb;
    v[e + 1] -= mb;
    q0 += v[e] * v[e];
    q1 += v[e + 1] * v[e + 1];
  }
  q0 += q1;
  const float dm = mean - mb;
  const float var = fmaxf(red_mod4(q0.x + q0.y) * (1.0f / DM) - dm * dm, 0.f);
  const float sd = bfr1(sqrtf(bfr1(bfr1(var) + 1e-5f)));
  const float ri = 1.0f / sd;
#pragma unroll
  for (int e = 0; e < 16; ++e) {
    const unsigned t2 = pk2(v[e].x, v[e].y);
    const f32x2_t q = f32x2_t{lo16(t2), hi16(t2)} * ri;
    x[e >> 2][e & 3] = pk2(q.x, q.y);
  }
}

extern "C" __device__ float mul1(float, float) __asm("llvm.amdgcn.fmul.legacy");
template <bool G0>
__device__ __forceinline__ void role_qkv_t(const MegaArgs& A, char* smem, const int L, const int h) {
  __builtin_amdgcn_s_waitcnt(0x0F70);
  const int w = __builtin_amdgcn_readfirstlane(threadIdx.x >> 6);
  char* ws = A.ws;
  unsigned* FL = (unsigned*)(ws + OFF_FL);
  unsigned* TMO = FL + FL_TMO;
#define LANE_IDS()                         \
  const int l = lane_id();                 \
  const int c = l & 15, g = l >> 4;        \
  (void)c; (void)g;
  unsigned* FC = FL + FL_FC + (L * 64 * 4 + REP) * 8 * 32;
  const __amdgpu_buffer_rsrc_t rsQ = MKRS(ws + OFF_QK + L * SZ_QK, SZ_QK);
  const __amdgpu_buffer_rsrc_t rsV = MKRS(ws + OFF_VF + L * SZ_O, SZ_O);
  const __amdgpu_buffer_rsrc_t rsM = MKRS(ws + (G0 ? OFF_MEMK : OFF_MEMV) + L * SZ_MEM, SZ_MEM);
  bf16x8 wq[16], wk[16], wv[16];
  {
    LANE_IDS();
    const unsigned sb = lds_base(smem);
    const int q = (l >> 2) & 3, p = l & 3, f = q | ((g & 1) << 2);
    const unsigned ra = sb + (8 * g + q) * 256 + (w ^ f) * 32 + p * 8;
#define W_STAGE(W, hf, VM, NEXT)                                               \
  {                                                                           \
    WAIT_VM(VM);                                                              \
    RAW_BAR();                                                                \
    _Pragma("unroll") for (int t = 0; t < 8; ++t) {                           \
      const unsigned a_ = ra + (hf) * 8192 + t * 16384;                       \
      const v2u r0 = trd(a_), r1 = trd(a_ + 1024);                            \
      W[8 * (hf) + t] = BF8((v4u{r0.x, r0.y, r1.x, r1.y}));                   \
    }                                                                         \
    _Pragma("unroll") for (int t = 0; t < 8; ++t) asm volatile("" : "+v"(W[8 * (hf) + t])); \
    RAW_BAR();                                                                \
    NEXT;                                                                     \
  }
    wqkv_issue<false>(A.Wq, 0, h, w, l, sb);
    wqkv_issue<false>(A.Wq, 1, h, w, l, sb);
    W_STAGE(wq, 0, 8, wqkv_issue<false>(A.Wk, 0, h, w, l, sb));
    SST(4);
    W_STAGE(wq, 1, 8, wqkv_issue<false>(A.Wk, 1, h, w, l, sb));
    SST(5);
    W_STAGE(wk, 0, 8, wqkv_issue<false>(A.Wv, 0, h, w, l, sb));
    W_STAGE(wk, 1, 8, wqkv_issue<false>(A.Wv, 1, h, w, l, sb));
    SST(6);
    W_STAGE(wv, 0, 8, (void)0);
    W_STAGE(wv, 1, 0, (void)0);
#undef W_STAGE
  }
  WSTAMP2();
  constexpr int Q_X = 98304;
  const unsigned sb = lds_base(smem);
  v2u eq, ek, ev;
  {
    LANE_IDS();
    const v4u zt = LD128(MKRS(ws + OFF_TAG, 8192 + 8), l * 16, (blockIdx.x & 7) * 1024);
    const v2u ze = LD64(MKRS(ws + OFF_TAG, 8192 + 8), 0, 8192);
    const v4u one = {0x3f803f80u, 0x3f803f80u, 0x3f803f80u, 0x3f803f80u};
    f32x4 t[3] = {};
#pragma unroll
    for (int ks = 0; ks < 16; ++ks) {
      t[0] = MFMA16(BF8(one), wq[ks], t[0]);
      t[1] = MFMA16(BF8(one), wk[ks], t[1]);
      t[2] = MFMA16(BF8(one), wv[ks], t[2]);
    }
    v2u e[3];
#pragma unroll
    for (int i = 0; i < 3; ++i) {
      const float x = g ? 0.f : t[i][0], x1 = x - bfr1(x);
      e[i] = v2u{pk2(x, x1), pk2(x1 - bfr1(x1), 0.f)};
    }
    eq = e[0]; ek = e[1]; ev = e[2];
    asm volatile("" : "+v"(eq), "+v"(ek), "+v"(ev));
    if (!zero_seen(zt, ze)) zero_wait(ws);
    if (l < 32) *reinterpret_cast<v4u*>(smem + Q_X + w * 2048 + (l >> 4) * 16384 + c * 16) = one;
  }
  constexpr int Q_CNT = Q_X + 11 * 2048 + 256;
  lu32* cnt = (lu32*)(unsigned long)(sb + Q_CNT);
  if (threadIdx.x < 16) cnt[threadIdx.x] = 0u;
#define C_ADD(i) asm volatile("s_mov_b64 exec, 1\n\tds_add_u32 %0, %1\n\ts_mov_b64 exec, -1" ::"v"(sb + Q_CNT + 4 * (i)), "v"(1u) : "memory")
#define C_RD(i) (*(volatile lu32*)(cnt + (i)))
#define C_GET(i) ((unsigned)__builtin_amdgcn_readfirstlane(C_RD(i)))
#define C_WAIT(i, n)                                                                                                    \
  {                                                                                                                     \
    for (unsigned spins = 0; C_GET(i) < (n) && spins < 64u * SPIN_CAP; ++spins) __builtin_amdgcn_s_sleep(1);             \
    asm volatile("" ::: "memory");                                                                                      \
  }
  const char* Xb = ws + (L ? OFF_X3 : OFF_XA) + w * 8192;
  const v4u rsF = mk_srd(FC + w * 64, (64 * 32 - REP * 8 - 2 * w) * 128);
  unsigned l16, vo, fo;
  {
    LANE_IDS();
    l16 = l * 16;
    vo = (c + 16 * (2 * (w & 1) + (g >> 1))) * 16 + 4096 * (g & 1);
    fo = g * 512 + c * 16;
  }
#define DMA4(x) "global_load_lds_dwordx4 %1, " x " sc1\n\tglobal_load_lds_dwordx4 %1, " x " offset:1024 sc1\n\t"       \
                "global_load_lds_dwordx4 %1, " x " offset:2048 sc1\n\tglobal_load_lds_dwordx4 %1, " x " offset:3072 sc1\n\t"
#define X_DMA(ss, bb)                                                                                                   \
  {                                                                                                                     \
    unsigned keep_;                                                                                                     \
    const char* x_ = Xb + (size_t)(ss) * 32768;                                                                         \
    const unsigned d_ = sb + (bb) * 32768 + w * 8192;                                                                   \
    asm volatile("s_nop 4\n\ts_mov_b32 %0, m0\n\ts_mov_b32 m0, %4\n\ts_nop 0\n\t" DMA4("%2")                           \
                 "s_mov_b32 m0, %5\n\ts_nop 0\n\t" DMA4("%3") "s_mov_b32 m0, %0"                                         \
                 : "=&s"(keep_)                                                                                         \
                 : "v"(l16), "s"(x_), "s"(x_ + 4096), "s"(d_), "s"(d_ + 4096u)                                           \
                 : "memory");                                                                                           \
    TS(0, ss, 0);                                                                                                       \
  }
#define X_DMA1(on, x, d, off)                                                                                           \
  {                                                                                                                     \
    unsigned keep_;                                                                                                     \
    asm volatile("s_cmp_eq_u32 %4, 0\n\ts_cbranch_scc1 1f\n\ts_mov_b32 %0, m0\n\ts_mov_b32 m0, %3\n\ts_nop 0\n\t"       \
                 "global_load_lds_dwordx4 %1, %2 offset:%5 sc1\n\ts_mov_b32 m0, %0\n1:"                                  \
                 : "=&s"(keep_)                                                                                         \
                 : "v"(l16), "s"(x), "s"(d), "s"(on), "n"(off)                                                          \
                 : "memory", "scc");                                                                                    \
  }
#define F_LOAD(r, ss)                                                                                                   \
  asm volatile("s_nop 4\n\tbuffer_load_dword %0, %1, %2, %3 offen sc1"                                                  \
               : "=v"(r)                                                                                                \
               : "v"(((l16 >> 2) & 28) + (l16 & 128)), "s"(rsF), "s"((ss) * 4096)                                       \
               : "memory")
  unsigned* FQ = FL + FL_FQ + (L * 64 * 32 + h) * 32;
#define Q_FLAG(ss)                                                                                                      \
  {                                                                                                                     \
    unsigned old_;                                                                                                      \
    asm volatile("s_mov_b64 exec, 1\n\tds_add_rtn_u32 %0, %1, %2\n\ts_mov_b64 exec, -1\n\ts_waitcnt lgkmcnt(0)"           \
                 : "=&v"(old_) : "v"(sb + Q_CNT + ((ss) & 1) * 4), "v"(1u) : "memory");                                 \
    if ((__builtin_amdgcn_readfirstlane(old_) & 7u) == 7u) {                                                            \
      asm volatile("s_mov_b64 exec, 1\n\tglobal_store_dword %0, %1, %2 sc1\n\ts_mov_b64 exec, -1"                        \
                   ::"v"(0), "v"(1u), "s"(FQ + (ss) * 1024) : "memory");                                                \
      TS(3, ss, threadIdx.x & ~63u);                                                                                    \
    }                                                                                                                   \
    pend = false;                                                                                                       \
  }
  __builtin_amdgcn_s_waitcnt(0x0F70);
  RAW_BAR();
  bool have = false, pend = false;
  unsigned tl = now10();
  const unsigned tq = tl - QPL;
  int b = 0;
  unsigned k4 = 4u;
  FSD;
  for (int s = 0; s < NSEG; ++s) {
    FS(0);
    const int b1 = b == 2 ? 0 : b + 1;
    const unsigned u1 = 2u * (b1 ? k4 - 4u : k4);
    unsigned nf;
    if (QPACE && L == 0 && s >= QPS && (int)(tq + s * QPACE - now10()) > 0) {
      if (pend) {
        WAIT_VM(0);
        Q_FLAG(s - 1);
      }
      gate(tq + s * QPACE);
    }
    if (G0) {
      if (!have) {
        for (unsigned spins = 0;; ++spins) {
          unsigned n;
          F_LOAD(n, s);
          WAIT_VM(0);
          if (pend) Q_FLAG(s - 1);
          if (all_set(n) || spin_timeout(spins, TMO)) break;
          if (GATE_Q && spins == 0) gate(tl);
          else if (L) __builtin_amdgcn_s_sleep(SLP_Q);
          else __builtin_amdgcn_s_sleep(SLP_Q1);
        }
        tl = now10() + GATE_Q;
        C_WAIT(8 + b, 2u * k4 - 8u);
        X_DMA(s, b);
        WAIT_VM(0);
        C_ADD(4 + b);
      }
      const unsigned c0 = C_RD(4 + b);
      F_LOAD(nf, s + 1);
      if ((unsigned)__builtin_amdgcn_readfirstlane(c0) < k4) C_WAIT(4 + b, k4);
    } else {
      if (C_GET(4 + b) < k4) {
        if (pend) {
          WAIT_VM(0);
          Q_FLAG(s - 1);
        }
        C_WAIT(4 + b, k4);
      }
      FS(1);
      const char* sp = smem + ((l16 & 128) ? Q_X + ((w + 1) & 1) * 128 : b * 32768 + fo + (w & 3) * 128);
      f32x4 gm = {};
      __builtin_amdgcn_s_setprio(3);
      {
        bf16x8 a[10];
#pragma unroll
        for (int ks = 0; ks < 10; ++ks) a[ks] = *reinterpret_cast<const bf16x8*>(sp + 2048 * ks);
        __builtin_amdgcn_sched_barrier(0);
#pragma unroll
        for (int ks = 0; ks < 16; ++ks) {
          gm = MFMA16(a[ks % 10], a[ks % 10], gm);
          if (ks < 6) a[ks] = *reinterpret_cast<const bf16x8*>(sp + 2048 * (10 + ks));
          __builtin_amdgcn_sched_barrier(0);
        }
      }
      float q2 = (l16 & 16) ? gm[1] : gm[0], q3 = (l16 & 16) ? gm[3] : gm[2];
      asm volatile("" : "+v"(q2), "+v"(q3));
      q2 = (l16 & 32) ? q3 : q2;
      auto sw = __builtin_amdgcn_permlane32_swap(__float_as_uint(q2), __float_as_uint(q2), false, false);
      const float mean = __uint_as_float(sw[1]) * (-1.0f / DM);
      const float var = fmaxf(__builtin_fmaf(-mean, mean, q2 * (1.0f / DM)), 0.f);
      const float sd = bfr1(__builtin_amdgcn_sqrtf(bfr1(bfr1(var) + 1e-5f)));
      asm volatile("s_mov_b64 exec, 0xf0000f\n\tds_write2_b32 %0, %1, %2 offset1:32\n\ts_mov_b64 exec, -1"
                   ::"v"(sb + Q_X + (8 + b) * 2048 + 256 + (w & 3) * 32 + ((l16 >> 2) & 28)), "v"(pk2(mean, mean)),
                   "v"(__builtin_amdgcn_rcpf(sd)) : "memory");
      C_ADD(12 + b);
      FS(2);
      if (pend) {
        WAIT_VM(0);
        Q_FLAG(s - 1);
      }
    }
    TS(G0 ? 1 : 5, s, G0 ? 0 : 256);
    FS(3);
    const char* ap = smem + b * 32768 + fo;
#define AFRAG(i) (*reinterpret_cast<const bf16x8*>(ap + ((i) >> 4) * 256 + 2048 * ((i) & 15)))
    bf16x8 abuf[QPF + 1];
#pragma unroll
    for (int i = 0; i < QPF; ++i) abuf[i] = AFRAG(i);
    f32x4 aq[2] = {}, ak[2] = {}, av[2] = {};
    unsigned cx = 0u;
    int on = 0;
    const char* xn = Xb + (size_t)(s + 1) * 32768;
    const unsigned dn = sb + b1 * 32768 + w * 8192;
    __builtin_amdgcn_s_setprio(G0 ? 1 : 0);
#pragma unroll
    for (int rt = 0; rt < 2; ++rt) {
#pragma unroll
      for (int ks = 0; ks < 16; ++ks) {
        const int i = 16 * rt + ks;
        if (i + QPF < 32) abuf[(i + QPF) % (QPF + 1)] = AFRAG(i + QPF);
        if (G0 && i == 11) cx = C_RD(8 + b1);
        if (i == 28) cx = C_RD(12 + b);
        const bf16x8 a = abuf[i % (QPF + 1)];
        aq[rt] = MFMA16(wq[ks], a, aq[rt]);
        ak[rt] = MFMA16(wk[ks], a, ak[rt]);
        av[rt] = MFMA16(a, wv[ks], av[rt]);
        if (i == QUP) { __builtin_amdgcn_s_setprio(G0 ? 3 : 2); FS(4); }
        __builtin_amdgcn_sched_barrier(0);
        if (G0 && rt == 1 && ks < 8) {
          X_DMA1(on, xn + (ks >> 2) * 4096, dn + (ks >> 2) * 4096, (ks & 3) * 1024);
          __builtin_amdgcn_sched_barrier(0);
        }
      }
      asm volatile("" : "+v"(aq[rt]), "+v"(ak[rt]), "+v"(av[rt]));
      if (rt == 0) FS(5); else FS(7);
      if (G0 && rt == 0) {
        WAIT_VM(0);
        if (pend) Q_FLAG(s - 1);
        have = all_set(nf) && (unsigned)__builtin_amdgcn_readfirstlane(cx) >= u1;
        F_LOAD(nf, have ? NSEG : s + 1);
        if (have) TS(0, s + 1, 0);
        on = __builtin_amdgcn_readfirstlane((int)have);
        FS(6);
      }
    }
    if ((unsigned)__builtin_amdgcn_readfirstlane(cx) < k4) C_WAIT(12 + b, k4);
    FS(8);
    v4u vfrag, qf4, kf4;
    {
      const char* stp = smem + Q_X + (8 + b) * 2048 + 256;
      unsigned nm[2];
      float ri[2];
      f32x4 rv[2];
#pragma unroll
      for (int rt = 0; rt < 2; ++rt) {
        nm[rt] = *reinterpret_cast<const unsigned*>(stp + 64 * rt + ((l16 >> 2) & 60));
        ri[rt] = *reinterpret_cast<const float*>(stp + 128 + 64 * rt + ((l16 >> 2) & 60));
        rv[rt] = *reinterpret_cast<const f32x4*>(stp + 128 + 64 * rt + ((l16 >> 4) & 48));
      }
#pragma unroll
      for (int rt = 0; rt < 2; ++rt) {
        const v2u xm = {nm[rt], nm[rt]};
        aq[rt] = MFMA16K(eq, xm, aq[rt]);
        ak[rt] = MFMA16K(ek, xm, ak[rt]);
        av[rt] = MFMA16K(xm, ev, av[rt]);
      }
      __builtin_amdgcn_s_setprio(QEP);
#pragma unroll
      for (int rt = 0; rt < 2; ++rt) {
        f32x4 tq, tk, tv;
#pragma unroll
        for (int e = 0; e < 4; ++e) {
          tq[e] = mul1(aq[rt][e], ri[rt]);
          tk[e] = mul1(ak[rt][e], ri[rt]);
          tv[e] = mul1(av[rt][e], rv[rt][e]);
        }
        qf4[2 * rt] = pk2(tq[0], tq[1]); qf4[2 * rt + 1] = pk2(tq[2], tq[3]);
        kf4[2 * rt] = pk2(tk[0], tk[1]); kf4[2 * rt + 1] = pk2(tk[2], tk[3]);
        vfrag[2 * rt] = pk2(tv[0], tv[1]); vfrag[2 * rt + 1] = pk2(tv[2], tv[3]);
      }
    }
    {
      auto q0 = __builtin_amdgcn_permlane16_swap(qf4.x, qf4.z, false, false);
      auto q1 = __builtin_amdgcn_permlane16_swap(qf4.y, qf4.w, false, false);
      auto k0 = __builtin_amdgcn_permlane16_swap(kf4.x, kf4.z, false, false);
      auto k1 = __builtin_amdgcn_permlane16_swap(kf4.y, kf4.w, false, false);
      v4u qo = {q0[0], q1[0], q0[1], q1[1]}, ko = {k0[0], k1[0], k0[1], k1[1]};
      const int soff = ((s * NH + h) * 16 + (w >> 1)) * 1024;
      asm volatile("" : "+v"(qo), "+v"(ko), "+v"(vfrag));
      FS(9);
      C_ADD(8 + b);
      if (G0 && have) {
        WAIT_VM(0);
        C_ADD(4 + b1);
      }
      TS(G0 ? 2 : 6, s, G0 ? 0 : 256);
      FS(10);
      ST128(ko, rsQ, vo, soff + 8192);
      ST128(qo, rsQ, vo, soff);
      ST128(vfrag, rsV, l16, ((s * NH + h) * 512 + w * 64) * 16);
      ST64((v2u{0u, 0u}), rsM, l16 >> 1, (s * NH + h) * 2048 + (w & 3) * 512);
      pend = true;
      FS(11);
    }
    if (G0) {
      WAIT_VM(4);
      if (all_set(nf) && C_GET(8 + b1) >= u1) {
        X_DMA(s + 1, b1);
        have = true;
        WAIT_VM(8);
        Q_FLAG(s);
        WAIT_VM(0);
        C_ADD(4 + b1);
      }
    }
    MSTAMP();
    FD(s);
    b = b1;
    if (b == 0) k4 += 4u;
  }
  DRAIN();
  if (pend) Q_FLAG(NSEG - 1);
#undef LANE_IDS
#undef DMA4
#undef X_DMA
#undef X_DMA1
#undef F_LOAD
#undef Q_FLAG
#undef AFRAG
#undef C_ADD
#undef C_RD
#undef C_GET
#undef C_WAIT
}
__device__ __forceinline__ void role_qkv(const MegaArgs& A, char* smem, const int L, const int h) {
  if (__builtin_amdgcn_readfirstlane(threadIdx.x >> 6) < 4) role_qkv_t<true>(A, smem, L, h);
  else role_qkv_t<false>(A, smem, L, h);
}

constexpr int MEGA_WGS = 256;
constexpr size_t MEGA_LDS = (size_t)(32 * 4 * 64 * 8 + 8 * SMS + 15 * QS + 128 + 16 * 64 * 8 + 8 * SOS + 512) * 2;
static_assert(MEGA_LDS <= 163840 && MEGA_LDS >= 162816 + 256, "lds");
static_assert(MEGA_LDS >= (size_t)AW_CNT + 64, "lds");
static_assert(MEGA_LDS >= (size_t)(8 * 2 * CKN + 8 * 2 * CUN + 4 * 32 * 8) * 2, "lds");
static_assert(MEGA_LDS >= (size_t)2 * 32 * SMS * 2 && MEGA_LDS >= 131072, "lds");
static_assert(131072 >= (size_t)(40 * NST + 32 * 400) * 4 && 131072 + 4 * 392 * 4 <= AW_CNT, "lds");
__global__ __launch_bounds__(512, 2) void mega_kernel(const u16* mem0, const u16* Dskip, char* ws, u16* out, const u16* x0,
                                                      const u16* logAre, const u16* Aim, const u16* Cre, const u16* Cim,
                                                      const u16* logdt, const u16* Wq, const u16* Wk, const u16* Wv,
                                                      const u16* Wo) {
  extern __shared__ __attribute__((aligned(16))) char smem[];
  const int b = __builtin_amdgcn_readfirstlane(role_no());
  const MegaArgs A{mem0, Dskip, ws, out, x0, logAre, Aim, Cre, Cim, logdt, Wq, Wk, Wv, Wo, b};
  {
    const int hi = (b >= 32 && b < 96) ? b - 32 : (b >= 200 && b < 248) ? b - 136 : (b >= 96 && b < 104) ? b + 16 : (b >= 248 ? b - 128 : -1);
    if (hi >= 0) start_kgen(A, smem, hi, b - 248, (b >= 64 && b < 104) || (b >= 232 && b < 240) ? b & 15 : -1);
  }
  if ((EXP_OFF & 1) && b >= 64 && b < 80) return;
  if ((EXP_OFF & 8) && b >= 200 && b < 232) return;
  if ((EXP_OFF & 16) && b >= 32 && b < 64) return;
  if ((EXP_OFF & 32) && b >= 80 && b < 96) return;
  if (b < 64) role_chain(A, smem, b >> 5, b & 31, 0);
  else if (b < 104 || (b >= 232 && b < 240)) role_aw(A, smem, b < 80 ? 0 : b < 96 ? 2 : 1, b & 15);
  else if (b < 168) role_conv(A, smem, b - 104);
  else if (b < 232) role_qkv(A, smem, (b - 168) >> 5, (b - 168) & 31);
  else role_red(A, smem, b - 240);
}

extern "C" void kernel_launch(void* const* d_in, const int* in_sizes, int n_in, void* d_out, int out_size, void* d_ws,
                              size_t ws_size, hipStream_t stream) {
  MegaArgs A;
  A.x0 = (const u16*)d_in[0];
  A.logAre = (const u16*)d_in[1];
  A.Aim = (const u16*)d_in[2];
  A.Cre = (const u16*)d_in[3];
  A.Cim = (const u16*)d_in[4];
  A.logdt = (const u16*)d_in[5];
  A.Dskip = (const u16*)d_in[6];
  A.mem0 = (const u16*)d_in[8];
  A.Wq = (const u16*)d_in[9];
  A.Wk = (const u16*)d_in[10];
  A.Wv = (const u16*)d_in[11];
  A.Wo = (const u16*)d_in[12];
  A.ws = (char*)d_ws;
  A.out = (u16*)d_out;

  (void)hipFuncSetAttribute((const void*)mega_kernel, hipFuncAttributeMaxDynamicSharedMemorySize, (int)MEGA_LDS);
  mega_kernel<<<MEGA_WGS, 512, MEGA_LDS, stream>>>(A.mem0, A.Dskip, A.ws, A.out, A.x0, A.logAre, A.Aim, A.Cre, A.Cim, A.logdt,
                                                   A.Wq, A.Wk, A.Wv, A.Wo);
}
```

```cpp
#include <hip/hip_runtime.h>
#include <hip/hip_bf16.h>
#include <stdint.h>

constexpr int L_SEQ = 2048;
constexpr int DM    = 512;
constexpr int NST   = 128;
constexpr int NH    = 32;
constexpr int DH    = 128;
constexpr int HD    = NH * DH;
constexpr int MM    = 8;
constexpr int SEG   = 32;
constexpr int NSEG  = L_SEQ / SEG;
constexpr int LK    = MM + SEG;

typedef unsigned short u16;
using bf16x8 = __attribute__((ext_vector_type(8))) short;
using f32x4  = __attribute__((ext_vector_type(4))) float;
using f32x16 = __attribute__((ext_vector_type(16))) float;

static __device__ __forceinline__ u16 f2bf(float f) {
  unsigned u = __float_as_uint(f);
  u += 0x7fffu + ((u >> 16) & 1u);
  return (u16)(u >> 16);
}
static __device__ __forceinline__ float bf2f(u16 h) { return __uint_as_float(((unsigned)h) << 16); }
static __device__ __forceinline__ float bfr(float f) { return bf2f(f2bf(f)); }

constexpr size_t SZ_X      = (size_t)L_SEQ * DM * 2;
constexpr size_t OFF_KF    = 0;
constexpr size_t OFF_UT    = OFF_KF + (size_t)DM * L_SEQ * 4;
constexpr size_t OFF_YT    = OFF_UT + SZ_X;
constexpr size_t OFF_XA    = OFF_YT + SZ_X;
constexpr size_t OFF_H     = OFF_XA + SZ_X;
constexpr size_t OFF_X2    = OFF_H + SZ_X;
constexpr size_t OFF_U2    = OFF_X2 + SZ_X;
constexpr size_t OFF_X3    = OFF_U2 + SZ_X;
constexpr size_t SZ_QK     = (size_t)L_SEQ * 2 * HD * 2;
constexpr size_t OFF_QK    = OFF_X3 + SZ_X;
constexpr size_t SZ_O      = (size_t)L_SEQ * HD * 2;
constexpr size_t OFF_VF    = OFF_QK + 2 * SZ_QK;
constexpr size_t SZ_PW     = (size_t)2 * NSEG * 16 * SEG * DM * 2;
constexpr size_t OFF_PW    = OFF_VF + 2 * SZ_O;
constexpr size_t SZ_MEM    = (size_t)NSEG * MM * HD * 2;
constexpr size_t OFF_MEMK  = OFF_PW + SZ_PW;
constexpr size_t OFF_MEMV  = OFF_MEMK + 2 * SZ_MEM;
constexpr size_t OFF_GT    = OFF_MEMV + 2 * SZ_MEM;
constexpr size_t SZ_ACC    = (size_t)2 * 32768;
constexpr size_t OFF_Z     = OFF_GT + (size_t)64 * DM * 4;
constexpr size_t OFF_ACC   = OFF_Z;
constexpr unsigned ACC_LO = 0x88000000u, ACC_HI = 0x87fffff7u;
constexpr size_t OFF_FL    = OFF_ACC + 2 * SZ_ACC;
constexpr int FL_FW  = 0;
constexpr int FL_FR  = FL_FW + 2 * 64 * 16 * 32;
constexpr int FL_FC  = FL_FR + 64 * 16 * 32;
constexpr int FL_FQ  = FL_FC + 2 * 64 * 32 * 32;
constexpr int FL_FS  = FL_FQ + 2 * 64 * 32 * 32;
constexpr int FL_FZ  = FL_FS + 4 * 32 * 32;
constexpr int FL_KG  = FL_FZ + 8 * 32;
constexpr int FL_G   = FL_KG + 64 * 32;
constexpr int FL_TK  = FL_G + 4 * 4 * 32;
constexpr int FL_TMO = FL_TK + 2 * 256;
constexpr int FL_END = FL_TMO + 64;
constexpr size_t Z_BYTES   = OFF_FL + (size_t)FL_END * 4 - OFF_Z;
constexpr size_t OFF_TAG   = OFF_Z + Z_BYTES;
constexpr unsigned long long TAG_STEP = 0x9e3779b97f4a7c15ull;
constexpr size_t OFF_G0    = OFF_TAG + 8 * 1024 + 256;
constexpr size_t WS_TOTAL  = OFF_G0 + 8192;
static_assert(Z_BYTES % 256 == 0 && OFF_Z % 256 == 0 && Z_BYTES <= 2 * 128 * 8192, "zero region");
#define REP ((blockIdx.x >> 1) & 3)
static_assert(WS_TOTAL + (4 << 20) <= (size_t)256 * 1024 * 1024, "workspace too large");

constexpr int CKN = 2208;
static_assert((size_t)DM * 2 * CKN * 2 <= (size_t)DM * L_SEQ * 4 + SZ_X, "K buffer");
__device__ __forceinline__ float pos_of_index(int p) {
  if (p < 256) return (float)p;
  if (p < 384) return (float)(256 + 2 * (p - 256));
  if (p < 512) return (float)(512 + 4 * (p - 384));
  if (p < 640) return (float)(1024 + 8 * (p - 512));
  return 2048.0f;
}
__device__ __forceinline__ int index_of_l(int l) {
  float pf = bfr((float)l);
  int p = (int)pf;
  if (p < 256) return p;
  if (p < 512) return 256 + ((p - 256) >> 1);
  if (p < 1024) return 384 + ((p - 512) >> 2);
  if (p < 2048) return 512 + ((p - 1024) >> 3);
  return 640;
}

template <int CTRL> __device__ __forceinline__ float dppr(float x) {
  return __builtin_bit_cast(float, __builtin_amdgcn_update_dpp(0, __builtin_bit_cast(int, x), CTRL, 0xf, 0xf, false));
}
__device__ __forceinline__ float wave_sum(float v) {
  v += dppr<0x121>(v);
  v += dppr<0x122>(v);
  v += dppr<0x124>(v);
  v += dppr<0x128>(v);
  {
    auto r = __builtin_amdgcn_permlane16_swap(__float_as_uint(v), __float_as_uint(v), false, false);
    v = __uint_as_float(r[0]) + __uint_as_float(r[1]);
  }
  {
    auto r = __builtin_amdgcn_permlane32_swap(__float_as_uint(v), __float_as_uint(v), false, false);
    v = __uint_as_float(r[0]) + __uint_as_float(r[1]);
  }
  return v;
}
__device__ __forceinline__ void ln_row8(float (&v)[8]) {
  float s = 0.f;
#pragma unroll
  for (int j = 0; j < 8; ++j) s += v[j];
  s = wave_sum(s);
  const float mean = s * (1.0f / DM);
  float q = 0.f;
#pragma unroll
  for (int j = 0; j < 8; ++j) { float t = v[j] - mean; q += t * t; }
  q = wave_sum(q);
  const float var = q * (1.0f / DM);
  const float mb = bfr(mean);
  const float sd = bfr(sqrtf(bfr(bfr(var) + 1e-5f)));
#pragma unroll
  for (int j = 0; j < 8; ++j) v[j] = bfr(bfr(v[j] - mb) / sd);
}

#define RLX_AGENT __ATOMIC_RELAXED, __HIP_MEMORY_SCOPE_AGENT
typedef __attribute__((address_space(1))) unsigned gu32;
typedef unsigned v4u __attribute__((ext_vector_type(4)));
constexpr int AUX_SC1 = 16;
constexpr unsigned SPIN_CAP = 1u << 18;

__device__ __forceinline__ bool wait_flags(const unsigned* flags, int lane, int nfl, unsigned want, unsigned* tmo) {
  for (unsigned spins = 0;; ++spins) {
    unsigned v = want;
    if (lane < nfl) v = __hip_atomic_load((gu32*)flags + lane * 32, RLX_AGENT);
    if (__all(v >= want)) break;
    if (spins > SPIN_CAP) {
      if (lane == 0) __hip_atomic_store((gu32*)tmo, 1u, RLX_AGENT);
      return false;
    }
  }
  __builtin_amdgcn_fence(__ATOMIC_ACQUIRE, "wavefront");
  return true;
}

#define STAMP(i)
#define STAMP_DUMP()
#define WSTAMP(i)
template <int CTRL> __device__ __forceinline__ float dppf(float old, float x) {
  return __builtin_bit_cast(float, __builtin_amdgcn_update_dpp(__builtin_bit_cast(int, old), __builtin_bit_cast(int, x), CTRL, 0xf, 0xf, false));
}
__device__ __forceinline__ float wave_max_dpp(float x) {
  const float ninf = -3e38f;
  x = fmaxf(x, dppf<0x111>(ninf, x));
  x = fmaxf(x, dppf<0x112>(ninf, x));
  x = fmaxf(x, dppf<0x114>(ninf, x));
  x = fmaxf(x, dppf<0x118>(ninf, x));
  x = fmaxf(x, dppf<0x142>(ninf, x));
  x = fmaxf(x, dppf<0x143>(ninf, x));
  return __builtin_bit_cast(float, __builtin_amdgcn_readlane(__builtin_bit_cast(int, x), 63));
}
__device__ __forceinline__ float wave_sum_dpp(float x) {
  x += dppf<0x111>(0.f, x);
  x += dppf<0x112>(0.f, x);
  x += dppf<0x114>(0.f, x);
  x += dppf<0x118>(0.f, x);
  x += dppf<0x142>(0.f, x);
  x += dppf<0x143>(0.f, x);
  return __builtin_bit_cast(float, __builtin_amdgcn_readlane(__builtin_bit_cast(int, x), 63));
}
constexpr int SMS = 528;
constexpr int QS = 144;
constexpr int VS = 80;
constexpr int SOS = 144;
#define VAR_NREP 4
constexpr int NREP = VAR_NREP;
__device__ __forceinline__ float xsum16(float x) {
  auto r = __builtin_amdgcn_permlane16_swap(__float_as_uint(x), __float_as_uint(x), false, false);
  return __uint_as_float(r[0]) + __uint_as_float(r[1]);
}
__device__ __forceinline__ float xsum32(float x) {
  auto r = __builtin_amdgcn_permlane32_swap(__float_as_uint(x), __float_as_uint(x), false, false);
  return __uint_as_float(r[0]) + __uint_as_float(r[1]);
}
__device__ __forceinline__ float xmax16(float x) {
  auto r = __builtin_amdgcn_permlane16_swap(__float_as_uint(x), __float_as_uint(x), false, false);
  return fmaxf(__uint_as_float(r[0]), __uint_as_float(r[1]));
}
__device__ __forceinline__ float xmax32(float x) {
  auto r = __builtin_amdgcn_permlane32_swap(__float_as_uint(x), __float_as_uint(x), false, false);
  return fmaxf(__uint_as_float(r[0]), __uint_as_float(r[1]));
}
__device__ __forceinline__ float red_mod4(float x) {
  x += dppf<0x124>(0.f, x);
  x += dppf<0x128>(0.f, x);
  return xsum32(xsum16(x));
}
typedef __bf16 bf16x2_t __attribute__((ext_vector_type(2)));
typedef float f32x2_t __attribute__((ext_vector_type(2)));
__device__ __forceinline__ unsigned pk2(float a, float b) {
  f32x2_t v = {a, b};
  bf16x2_t r = __builtin_convertvector(v, bf16x2_t);
  return __builtin_bit_cast(unsigned, r);
}
__device__ __forceinline__ float dot2sq(const unsigned x, const float acc) {
  return __builtin_amdgcn_fdot2_f32_bf16(__builtin_bit_cast(bf16x2_t, x), __builtin_bit_cast(bf16x2_t, x), acc, false);
}
__device__ __forceinline__ float lo16(unsigned u) { return __uint_as_float(u << 16); }
__device__ __forceinline__ float hi16(unsigned u) { return __uint_as_float(u & 0xffff0000u); }
__device__ __forceinline__ void scale_pair(float a, float b, float scale, float& oa, float& ob) {
  unsigned u = pk2(a, b);
  unsigned v = pk2(lo16(u) * scale, hi16(u) * scale);
  oa = lo16(v);
  ob = hi16(v);
}

#define XSPLIT 3
__device__ __forceinline__ int role_no() {
  const int b = blockIdx.x, x = b & 7, j = b >> 3;
  if (XSPLIT >= 3) {
    const int y = XSPLIT == 5 ? x & 3 : x >> 1;
    if (XSPLIT == 5 ? x < 4 : !(x & 1)) {
      if (j >= 16) return 104 + 16 * y + j - 16;
      return XSPLIT == 4 ? 16 * y + j : (j & 8) * 4 + 8 * y + (j & 7);
    }
    if (j < 16) return 168 + (j & 8) * 4 + 8 * y + (j & 7);
    const int i = 4 * y + (j & 3);
    return j < 20 ? 64 + i : j < 24 ? (i < 8 ? 96 + i : 224 + i) : j < 28 ? 80 + i : 240 + i;
  }
  const int L = x >> 2, xx = x & 3;
  if (j < 8) return 32 * L + 8 * xx + j;
  if (j < 12) return 64 + 16 * L + 4 * xx + j - 8;
  if (j < 16) { const int i = 4 * xx + j - 12; return L ? 240 + i : i < 8 ? 96 + i : 224 + i; }
  if (j < 24) return 104 + 8 * x + j - 16;
  return 168 + 32 * L + 8 * xx + j - 24;
}
#define MSTAMP()
#define WSTAMP2()
#define SST(k)
#define TSR(rn, k, ss, t)
#define TS(k, ss, t)
#define FSD
#define FS(k)
#define FD(ss)
struct MegaArgs {
  const u16* mem0;
  const u16* Dskip;
  char* ws;
  u16* out;
  const u16* x0;
  const u16 *logAre, *Aim, *Cre, *Cim, *logdt;
  const u16 *Wq, *Wk, *Wv, *Wo;
  int rn;
};

__device__ __forceinline__ int lane_id() {
  int l;
  asm volatile("v_mbcnt_lo_u32_b32 %0, -1, 0\n\tv_mbcnt_hi_u32_b32 %0, -1, %0" : "=v"(l));
  return l;
}
__device__ __forceinline__ bool spin_timeout(unsigned spins, unsigned* tmo) {
  if (spins > SPIN_CAP) {
    if (lane_id() == 0) __hip_atomic_store((gu32*)tmo, 1u, RLX_AGENT);
    return true;
  }
  return false;
}
#define PSWH (h & 2)
#define RR 0
#define RQM 63
#define RPM 63
#define CD0 100
#define APN 9
#define APL 300
#define APA 120
#define APB 80
#define APP 208
#define CD 30
#define GP 230
#define GR 50
#define GM 0
#define ROT (4 * (h & 1))
#define CPRIO 1
#define P1PRIO 3
#define QLEAD 2
#define POLL_SLEEP 12
#define SLP_C POLL_SLEEP
#define SLP_A POLL_SLEEP
#define SLP_W POLL_SLEEP
#define SLP_R 4
#define SLP_V 4
#define SLP_Q 4
#define SLP_M 4
#define AWPRIO 2
#define SLP_S 8
#define SPOS 4
#define CVPRE 0
#define QHV 0
#define QCHK 0
#define LEADN 99
#define GATE_V 140
#define GATE_R 380
#define GATE_N 440
#define AWAHEAD 1
#define GATE_M 150
#define GATE_M2 380
#define QPACE 0
#define QPS 6
#define QPL 100
#define GATE_Q 150
#define SLP_Q1 24
#define QPF 3
#define QUP 7
#define QEP 3
#define AWD 11
#define GPER 200
#define GMA 60
#define GMR 60
#define GSTEP 30
#define GATE_RT 200
#define GATE_1 160
#define N1S 20
#define N1E 36
#define PRI_A 3
#define PRI_WA 2
#define PRI_WB 1
#define AWSKIP 3
#define PRI_0 3
#define AWDEF 1
#define SLP_F 2
#define EXP_OFF 0
#define AWSPEC 1
#define CVTHR 48
#define NR_C 8
#define NR_R 8
#define NR_W 8
#define NR_N 2
#define QAHEAD 0
#define NS0 4
#define SLP_N POLL_SLEEP
template <int SLP, int WPL>
__device__ __forceinline__ bool wait_n(const unsigned* f, int n, unsigned* tmo) {
  const int l = lane_id();
  for (unsigned spins = 0;; ++spins) {
    unsigned v = 1u;
    if (l < n) v = __hip_atomic_load((gu32*)f + (l / WPL) * 32 + l % WPL, RLX_AGENT);
    if (__all(v != 0u)) break;
    if (spin_timeout(spins, tmo)) return false;
    __builtin_amdgcn_s_sleep(SLP);
  }
  asm volatile("" ::: "memory");
  return true;
}
__device__ __forceinline__ unsigned now10() { return (unsigned)__builtin_amdgcn_s_memrealtime(); }
__device__ __forceinline__ void gate(const unsigned t) {
  for (int i = 0; i < 64; ++i) {
    const int d = (int)(t - now10());
    if (d <= 0) break;
    if (d > 100) __builtin_amdgcn_s_sleep(32);
    else if (d > 25) __builtin_amdgcn_s_sleep(8);
    else __builtin_amdgcn_s_sleep(2);
  }
}
template <int SLP, int WPL>
__device__ __forceinline__ bool grp_wait(const bool leader, volatile unsigned* sw, const unsigned val, const unsigned* f, int n,
                                         unsigned* tmo, const unsigned t) {
  unsigned spins = 0;
  gate(t);
  if (leader) {
    const int l = lane_id();
    for (;; ++spins) {
      unsigned v = 1u;
      if (l < n) v = __hip_atomic_load((gu32*)f + (l / WPL) * 32 + l % WPL, RLX_AGENT);
      if (__all(v != 0u) || spin_timeout(spins, tmo)) break;
      __builtin_amdgcn_s_sleep(SLP);
    }
    if ((threadIdx.x & 63) == 0) *sw = 2u * val + (spins ? 1u : 0u);
  } else {
    unsigned v;
    for (; (v = __builtin_amdgcn_readfirstlane(*sw)) < 2u * val; ++spins) {
      if (spins > 64u * SPIN_CAP) break;
      __builtin_amdgcn_s_sleep(SLP_F);
    }
    spins = v & 1u;
  }
  asm volatile("" ::: "memory");
  return spins != 0u;
}
template <int SLP, int WPL>
__device__ __forceinline__ bool wg_wait(const unsigned* f, int n, unsigned* tmo) {
  if (threadIdx.x < 64) (void)wait_n<SLP, WPL>(f, n, tmo);
  __syncthreads();
  asm volatile("" ::: "memory");
  return true;
}
__device__ __forceinline__ void set_flag(unsigned* f) { __hip_atomic_store((gu32*)f, 1u, RLX_AGENT); }
#define DRAIN() asm volatile("s_waitcnt vmcnt(0)" ::: "memory")
#define MKRS(ptr, bytes) __builtin_amdgcn_make_buffer_rsrc((void*)(ptr), 0, (int)(bytes), 0x00020000)
typedef unsigned v2u __attribute__((ext_vector_type(2)));
#define LD128(rs, voff, soff) __builtin_amdgcn_raw_buffer_load_b128(rs, voff, soff, AUX_SC1)
#define LD64(rs, voff, soff) __builtin_amdgcn_raw_buffer_load_b64(rs, voff, soff, AUX_SC1)
#define ST128(v, rs, voff, soff)                                              \
  do {                                                                        \
    const v4u st_d_ = (v);                                                    \
    __builtin_amdgcn_raw_buffer_store_b128(st_d_, rs, voff, soff, AUX_SC1);   \
    asm volatile("s_nop 1" ::"v"(st_d_) : "memory");                          \
  } while (0)
#define ST64(v, rs, voff, soff) __builtin_amdgcn_raw_buffer_store_b64(v, rs, voff, soff, AUX_SC1)
#define BF8(x) __builtin_bit_cast(bf16x8, x)
#define MFMA16(a, b, c) __builtin_amdgcn_mfma_f32_16x16x32_bf16(a, b, c, 0, 0, 0)
typedef short bf16x4 __attribute__((ext_vector_type(4)));
#define MFMA16K(a, b, c) \
  __builtin_amdgcn_mfma_f32_16x16x16bf16_1k(__builtin_bit_cast(bf16x4, a), __builtin_bit_cast(bf16x4, b), c, 0, 0, 0)
__device__ __forceinline__ float fmax_nn(float a, float b) { return __builtin_elementwise_maximum(a, b); }
typedef __bf16 bfv8 __attribute__((ext_vector_type(8)));
typedef __bf16 bfv16 __attribute__((ext_vector_type(16)));
typedef unsigned v8u __attribute__((ext_vector_type(8)));
#define SMF(a, b, c, idx) \
  __builtin_amdgcn_smfmac_f32_16x16x64_bf16(__builtin_bit_cast(bfv8, a), __builtin_bit_cast(bfv16, b), c, idx, 0, 0)
__device__ __forceinline__ float swap_add32(float a, float b) {
  auto r = __builtin_amdgcn_permlane32_swap(__float_as_uint(a), __float_as_uint(b), false, false);
  return __uint_as_float(r[0]) + __uint_as_float(r[1]);
}
__device__ __forceinline__ float swap_add16(float a, float b) {
  auto r = __builtin_amdgcn_permlane16_swap(__float_as_uint(a), __float_as_uint(b), false, false);
  return __uint_as_float(r[0]) + __uint_as_float(r[1]);
}
__device__ __forceinline__ v4u smf_fix(const v4u d) {
  auto x = __builtin_amdgcn_permlane16_swap(d.x, d.z, false, false);
  auto y = __builtin_amdgcn_permlane16_swap(d.y, d.w, false, false);
  return v4u{x[0], x[1], y[0], y[1]};
}
__device__ __forceinline__ v8u smf_bfrag(const u16* p) {
  const v2u a = *reinterpret_cast<const v2u*>(p), b = *reinterpret_cast<const v2u*>(p + 8);
  const v2u c = *reinterpret_cast<const v2u*>(p + 32), d = *reinterpret_cast<const v2u*>(p + 40);
  return v8u{a.x, b.x, a.y, b.y, c.x, d.x, c.y, d.y};
}

#define VAR_PPA 0
#define VAR_PPB 0
#define VAR_THR 0
#define VAR_THR2 0
#define CV_LEAD 100
#define VAR_EARLY 1
#define VAR_DRAIN 1
#define VAR_D1 0
#define VAR_D2 0
#define VAR_DR2 1
#define VAR_S1 0
#define VAR_S2 0
typedef __attribute__((address_space(1))) unsigned long long gu64;
typedef __attribute__((address_space(1))) char gch;
__device__ __forceinline__ int cvt_rpi(float x) {
  int r;
  asm("v_cvt_rpi_i32_f32 %0, %1" : "=v"(r) : "v"(x));
  return r;
}
typedef short v4s __attribute__((ext_vector_type(4)));
typedef __attribute__((address_space(3))) v4s* lp4s;
__device__ __forceinline__ void glds16(const void* sbase, const unsigned voff, const unsigned lds) {
  unsigned keep;
  asm volatile("s_nop 4\n\ts_mov_b32 %0, m0\n\ts_mov_b32 m0, %3\n\ts_nop 0\n\tglobal_load_lds_dwordx4 %1, %2\n\ts_mov_b32 m0, %0"
               : "=&s"(keep)
               : "v"(voff), "s"(sbase), "s"(lds)
               : "memory");
}
__device__ __forceinline__ void glds16c(const void* sbase, const unsigned voff, const unsigned lds) {
  unsigned keep;
  asm volatile("s_nop 4\n\ts_mov_b32 %0, m0\n\ts_mov_b32 m0, %3\n\ts_nop 0\n\tglobal_load_lds_dwordx4 %1, %2 sc1\n\ts_mov_b32 m0, %0"
               : "=&s"(keep)
               : "v"(voff), "s"(sbase), "s"(lds)
               : "memory");
}
__device__ __forceinline__ v2u trd(const unsigned a) {
  return __builtin_bit_cast(v2u, __builtin_amdgcn_ds_read_tr16_b64_v4i16((lp4s)(unsigned long)a));
}
__device__ __forceinline__ unsigned lds_base(char* smem) { return (unsigned)(unsigned long)(__attribute__((address_space(3))) char*)smem; }
#define RAW_BAR()                                           \
  do {                                                      \
    asm volatile("s_waitcnt lgkmcnt(0)" ::: "memory");      \
    __builtin_amdgcn_s_barrier();                           \
    asm volatile("" ::: "memory");                          \
  } while (0)
#define WAIT_VM(n) asm volatile("s_waitcnt vmcnt(" #n ")" ::: "memory")
template <bool SP>
__device__ __forceinline__ void wqkv_issue(const u16* Wm, const int hf, const int h, const int w, const int l, const unsigned sb) {
  const char* base = (const char*)Wm + (size_t)(256 * hf + 32 * w) * (HD * 2) + h * (DH * 2);
#pragma unroll
  for (int i = 0; i < 8; ++i) {
    const int r = l >> 4;
    const int f = SP ? ((r & 1) | ((i & 1) << 1) | (((i >> 1) & 1) << 2)) : (r | (((i >> 1) & 1) << 2));
    glds16(base, (unsigned)((4 * i + r) * (HD * 2) + (((l & 15) ^ (f << 1)) << 4)), sb + w * 16384 + hf * 8192 + i * 1024);
  }
}

#define PFW 1
__device__ __forceinline__ void pull_weights(const MegaArgs& A, char* smem, const int slot, const int slots) {
  if (!PFW) return;
  const int x = blockIdx.x & 7, j = blockIdx.x >> 3, h = 8 * (x >> 1) + (j & 7), k = (j >> 3) - 1, l = lane_id();
  const bool odd = x & 1, wo = !odd && k == 1;
  const unsigned voff = wo ? l * 64 : (l >> 2) * (HD * 2) + (l & 3) * 64;
  const unsigned dst = lds_base(smem) + 162816;
  unsigned long long pk = (unsigned long long)A.Wk, pq = (unsigned long long)A.Wq, pv = (unsigned long long)A.Wv, po = (unsigned long long)A.Wo;
  asm volatile("" : "+s"(pk), "+s"(pq), "+s"(pv), "+s"(po));
  for (int i = odd ? slot * 3 + k : slot; (PFW & 1) && i < (odd ? 64 : 32); i += odd ? 3 * slots : slots) {
    const unsigned long long m = odd ? (i < 32 ? pk : pv) : (k == 0 ? pq : pk);
    const unsigned long long b = wo ? po + h * (DH * DM * 2) + i * 4096 : m + (i & 31) * (16 * HD * 2) + h * (DH * 2);
    unsigned keep;
    asm volatile("s_nop 4\n\ts_mov_b32 %0, m0\n\ts_mov_b32 m0, %3\n\ts_nop 0\n\tglobal_load_lds_dword %1, %2\n\ts_mov_b32 m0, %0"
                 : "=&s"(keep)
                 : "v"(voff), "s"(b), "s"(dst)
                 : "memory");
  }
  if (PFW & 2) {
    const unsigned long long b = po + (size_t)h * (DH * DM * 2);
    for (int i = slot * 3 + k; i < 32; i += 3 * slots) {
      unsigned keep;
      asm volatile("s_nop 4\n\ts_mov_b32 %0, m0\n\ts_mov_b32 m0, %3\n\ts_nop 0\n\tglobal_load_lds_dword %1, %2\n\ts_mov_b32 m0, %0"
                   : "=&s"(keep)
                   : "v"(l * 64), "s"(b + i * 4096), "s"(dst)
                   : "memory");
    }
  }
}
__device__ __forceinline__ void wo_issue(const u16* Wo, const int a, const int st, const unsigned sb) {
  const int w = __builtin_amdgcn_readfirstlane(threadIdx.x >> 6), l = lane_id(), c = l & 15, g = l >> 4;
  const char* base = (const char*)Wo + ((size_t)((2 * a + (st >> 1)) * DH + 64 * (w & 1) + 32 * (st & 1)) * DM + 128 * (w >> 1)) * 2;
#pragma unroll
  for (int i = 0; i < 8; ++i) {
    const int fd = g | ((i & 1) << 2);
    glds16(base, (unsigned)((4 * i + g) * (DM * 2) + ((c ^ (fd << 1)) << 4)), sb + (st & 1) * 65536 + w * 8192 + i * 1024);
  }
}
__device__ __forceinline__ bool zero_seen(const v4u t, const v2u e) {
  const unsigned long long m = (((unsigned long long)e.y << 32) | e.x) + TAG_STEP;
  const unsigned lo = (unsigned)m, hi = (unsigned)(m >> 32);
  return __all(((t.x ^ lo) | (t.y ^ hi) | (t.z ^ lo) | (t.w ^ hi)) == 0u);
}
__device__ __forceinline__ void zero_wait(char* ws) {
  const __amdgpu_buffer_rsrc_t rs = MKRS(ws + OFF_TAG, 8192 + 8);
  for (unsigned spins = 0; spins < 16u * SPIN_CAP; ++spins) {
    const v4u t = LD128(rs, lane_id() * 16, (blockIdx.x & 7) * 1024);
    const v2u e = LD64(rs, 0, 8192);
    if (zero_seen(t, e)) break;
    asm volatile("s_sleep 4" ::: "memory");
  }
  asm volatile("" ::: "memory");
}
__device__ __forceinline__ unsigned row_stats(const v4u raw) {
  float v[8];
#pragma unroll
  for (int e = 0; e < 4; ++e) { v[2 * e] = lo16(raw[e]); v[2 * e + 1] = hi16(raw[e]); }
  float s = 0.f;
#pragma unroll
  for (int e = 0; e < 8; ++e) s += v[e];
  const float mean = wave_sum(s) * (1.0f / DM);
  float q = 0.f;
#pragma unroll
  for (int e = 0; e < 8; ++e) { const float t = v[e] - mean; q += t * t; }
  const float var = wave_sum(q) * (1.0f / DM);
  const float sd = bfr(sqrtf(bfr(bfr(var) + 1e-5f)));
  return (unsigned)f2bf(mean) | ((unsigned)f2bf(sd) << 16);
}
__device__ __forceinline__ void start_kgen(const MegaArgs& A, char* smem, const int hi, const int xi, const int aw) {
  const int tid = threadIdx.x, j = tid >> 7, u = tid & 127, d = 4 * hi + j;
  const int w = __builtin_amdgcn_readfirstlane(tid >> 6), l = tid & 63;
  float* prm = reinterpret_cast<float*>(smem) + j * 10 * NST;
  float* part = reinterpret_cast<float*>(smem) + 40 * NST + j * 8 * 400;
  unsigned* kd = reinterpret_cast<unsigned*>(smem + 131072) + j * 392;
  v4u raw[8], ra, rb;
  const int r0 = 16 * xi + 2 * w;
  if (xi >= 0) {
    ra = *reinterpret_cast<const v4u*>(A.x0 + (size_t)r0 * DM + l * 8);
    rb = *reinterpret_cast<const v4u*>(A.x0 + (size_t)(r0 + 1) * DM + l * 8);
  }
  const u16 p0 = A.logdt[d], p1 = A.logAre[d * NST + u], p2 = A.Aim[d * NST + u], p3 = A.Cre[d * NST + u], p4 = A.Cim[d * NST + u];
  if (w >= 6) {
#pragma unroll
    for (int i = 0; i < 8; ++i) raw[i] = *reinterpret_cast<const v4u*>(A.x0 + (size_t)(16 * hi + 8 * (w - 6) + i) * DM + l * 8);
  }
  const v2u en = LD64(MKRS(A.ws + OFF_TAG, 8192 + 8), 0, 8192);
  {
    const __amdgpu_buffer_rsrc_t rz = MKRS(A.ws + OFF_Z, Z_BYTES);
    const unsigned i0 = hi < 16 ? ACC_LO : 0u, i1 = hi < 16 ? ACC_HI : 0u;
    ST128((v4u{i0, i1, i0, i1}), rz, tid * 16, hi * 8192);
    ST128((v4u{0u, 0u, 0u, 0u}), rz, tid * 16, (128 + hi) * 8192);
  }
  if (xi >= 0) {
    const unsigned sa = row_stats(ra), sb = row_stats(rb), st = (l & 1) ? sb : sa;
    const unsigned long long m = (((unsigned long long)en.y << 32) | en.x) + TAG_STEP;
    ST128((v4u{st, (unsigned)m, st, (unsigned)(m >> 32)}), MKRS(A.ws + OFF_G0, 8192), (l >> 1) * 2048 + (l & 1) * 16, r0 * 16);
    SST(11);
  }
  {
    const float dt = bfr(expf(bf2f(p0)));
    const float ar = -bfr(expf(bf2f(p1))) * dt, ai = bf2f(p2) * dt;
    prm[u] = ar;
    prm[NST + u] = ai;
    prm[2 * NST + u] = bf2f(p3) * dt;
    prm[3 * NST + u] = bf2f(p4) * dt;
#pragma unroll
    for (int cls = 1; cls < 4; ++cls) {
      const float st = (float)(1 << cls);
      const float e = expf(ar * st);
      float sn, cs;
      sincosf(ai * st, &sn, &cs);
      prm[(2 + 2 * cls) * NST + u] = e * cs;
      prm[(3 + 2 * cls) * NST + u] = e * sn;
    }
  }
  __builtin_amdgcn_s_waitcnt(0x0F70);
  __syncthreads();
  SST(1);
  if (w >= 6) {
    if (w == 7 && l < 8)
      __hip_atomic_store((gu64*)(A.ws + OFF_TAG) + l * 128 + hi, (((unsigned long long)en.y << 32) | en.x) + TAG_STEP, RLX_AGENT);
    __builtin_amdgcn_s_setprio(3);
    if (hi >= 32) pull_weights(A, smem, w - 6, 2);
    v4u wr[32];
    const u16* wop = A.Wo + (size_t)(hi * DH + 64 * (w - 6)) * DM + l * 8;
    if (hi < 32) {
#pragma unroll
      for (int i = 0; i < 32; ++i) wr[i] = *reinterpret_cast<const v4u*>(wop + i * DM);
    }
    unsigned stw = 0u;
#pragma unroll
    for (int i = 0; i < 8; ++i) {
      const unsigned t = row_stats(raw[i]);
      if (l == i) stw = t;
    }
    const int sx = 2 * hi + w - 6;
    if (l < 8) __hip_atomic_store((gu32*)(A.ws + OFF_GT) + 8 * sx + l, stw, RLX_AGENT);
    zero_wait(A.ws);
    DRAIN();
    if (l < 4) set_flag((unsigned*)(A.ws + OFF_FL) + FL_FS + (l * 32 + (sx >> 3)) * 32 + (sx & 7));
    if (hi < 32) {
      float c0 = 0.f, c1 = 0.f, c2 = 0.f, c3 = 0.f;
#pragma unroll
      for (int i = 0; i < 64; ++i) {
        const v4u x = wr[i & 31];
        if (i < 32) wr[i] = *reinterpret_cast<const v4u*>(wop + (32 + i) * DM);
        c0 = dot2sq(x.x, c0); c1 = dot2sq(x.y, c1); c2 = dot2sq(x.z, c2); c3 = dot2sq(x.w, c3);
      }
      const float m = wave_max_dpp(fmaxf(fmaxf(c0, c1), fmaxf(c2, c3)));
      if (l < 4) __hip_atomic_store((gu32*)(A.ws + OFF_FL) + FL_G + (l * 4 + w - 5) * 32 + hi, __float_as_uint(m) | 1u, RLX_AGENT);
    }
    __builtin_amdgcn_s_setprio(0);
  } else {
    const int jm = tid / 96, um = tid % 96;
    const int r = um % 12, ng = um / 12;
    const float* prm = reinterpret_cast<float*>(smem) + jm * 10 * NST;
    float* part = reinterpret_cast<float*>(smem) + 40 * NST + jm * 8 * 400;
    const float* pw = prm + (4 + 2 * (r >> 2)) * NST;
    const float p0 = pos_of_index(256 + 32 * r);
    f32x2_t acc[33];
#pragma unroll
    for (int i = 0; i < 33; ++i) acc[i] = f32x2_t{0.f, 0.f};
#pragma unroll 1
    for (int bt = 0; bt < 4; ++bt) {
      const int n = 16 * ng + 4 * bt;
      const f32x4 ar = *reinterpret_cast<const f32x4*>(prm + n), ai = *reinterpret_cast<const f32x4*>(prm + NST + n);
      const f32x4 cr = *reinterpret_cast<const f32x4*>(prm + 2 * NST + n), ci = *reinterpret_cast<const f32x4*>(prm + 3 * NST + n);
      const f32x4 wr = *reinterpret_cast<const f32x4*>(pw + n), wi = *reinterpret_cast<const f32x4*>(pw + NST + n);
      float yr0[4], yi0[4];
#pragma unroll
      for (int k = 0; k < 4; ++k) {
        const float e = __expf(ar[k] * p0);
        float sn, cs;
        __sincosf(ai[k] * p0, &sn, &cs);
        const float zr = e * cs, zi = e * sn;
        yr0[k] = cr[k] * zr - ci[k] * zi;
        yi0[k] = cr[k] * zi + ci[k] * zr;
      }
      f32x2_t yra = {yr0[0], yr0[1]}, yia = {yi0[0], yi0[1]}, wra = {wr[0], wr[1]}, wia = {wi[0], wi[1]};
      f32x2_t yrb = {yr0[2], yr0[3]}, yib = {yi0[2], yi0[3]}, wrb = {wr[2], wr[3]}, wib = {wi[2], wi[3]};
#pragma unroll
      for (int i = 0; i < 33; ++i) {
        acc[i] += yra + yrb;
        const f32x2_t ta = yra * wra - yia * wia, tb = yrb * wrb - yib * wib;
        yia = yra * wia + yia * wra;
        yib = yrb * wib + yib * wrb;
        yra = ta;
        yrb = tb;
      }
    }
#pragma unroll
    for (int i = 0; i < 33; ++i) part[ng * 400 + 33 * r + i] = acc[i].x + acc[i].y;
  }
  __syncthreads();
  SST(2);
  for (int i = u; i < 385; i += 128) {
    const int o = i < 384 ? 33 * (i >> 5) + (i & 31) : 33 * 11 + 32;
    float v = 0.f;
#pragma unroll
    for (int g = 0; g < 8; ++g) v += part[g * 400 + o];
    v *= 2.0f;
    kd[i] = pk2(v, v - lo16(pk2(v, v)));
  }
  if (u == 1) kd[385] = 0u;
  __syncthreads();
  if (aw >= 0) {
    wo_issue(A.Wo, aw, 0, lds_base(smem));
    wo_issue(A.Wo, aw, 1, lds_base(smem));
  }
  const __amdgpu_buffer_rsrc_t rs = MKRS(A.ws + OFF_KF, DM * 2 * CKN * 2);
  for (int un = u; un < 229; un += 128) {
    v4u oh, ol;
#pragma unroll
    for (int e = 0; e < 4; ++e) {
      const int lag = 2080 - (8 * un + 2 * e);
      const unsigned pb = pk2((float)lag, (float)(lag - 1));
      const unsigned k0 = kd[(unsigned)(lag - 256) < L_SEQ - 256u ? (pb & 0xffffu) - 0x4380u : 385u];
      const unsigned k1 = kd[(unsigned)(lag - 257) < L_SEQ - 256u ? (pb >> 16) - 0x4380u : 385u];
      oh[e] = __builtin_amdgcn_perm(k1, k0, 0x05040100u);
      ol[e] = __builtin_amdgcn_perm(k1, k0, 0x07060302u);
    }
    ST128(oh, rs, (d * 2 * CKN + 8 * un) * 2, 0);
    ST128(ol, rs, (d * 2 * CKN + CKN + 8 * un) * 2, 0);
  }
  DRAIN();
  __syncthreads();
  if (tid == 0) set_flag((unsigned*)(A.ws + OFF_FL) + FL_KG + (hi >> 1) * 32 + (hi & 1));
  SST(3);
}

constexpr int C_SM = 131072, C_SO = C_SM + 8192, C_KT = C_SO + 2048, C_X = C_KT + 16384, C_SQ = C_X + 1024;
constexpr int C_SK = C_SQ + 2432;
static_assert(C_SK + 8 * QS * 2 <= 163808, "lds");
#define CHAIN_ASM_ATT \
  "s_mov_b32 s66, %[d0]\n" \
  "s_mov_b32 s94, 576\n" \
  "s_mov_b32 s95, 288\n" \
  "s_mov_b32 s84, 0x43400000\n" \
  "s_mov_b32 s85, 0x43400000\n" \
  "s_mov_b32 s92, 0x88000000\n" \
  "s_mov_b32 s93, 0x87fffff7\n" \
  "s_mov_b32 s78, 0x3e0293ee\n" \
  "s_mov_b32 s76, 0x33333333\n" \
  "s_mov_b32 s77, 0x33333333\n" \
  "s_add_u32 s58, s56, s60\n" \
  "s_addc_u32 s59, s57, 0\n" \
  "s_xor_b32 s38, s35, 0\n" \
  "s_add_u32 s26, s58, s38\n" \
  "s_addc_u32 s27, s59, 0\n" \
  "s_xor_b32 s38, s35, 1024\n" \
  "s_add_u32 s28, s58, s38\n" \
  "s_addc_u32 s29, s59, 0\n" \
  "s_xor_b32 s38, s35, 2048\n" \
  "s_add_u32 s30, s58, s38\n" \
  "s_addc_u32 s31, s59, 0\n" \
  "s_xor_b32 s38, s35, 3072\n" \
  "s_add_u32 s32, s58, s38\n" \
  "s_addc_u32 s33, s59, 0\n" \
  "s_lshr_b32 s98, s60, 2\n" \
  "s_add_u32 s98, s98, s74\n" \
  "s_add_u32 s98, s98, 131072\n" \
  "s_lshl_b32 s97, s73, 5\n" \
  "s_add_u32 s97, s97, s74\n" \
  "s_add_u32 s97, s97, 161152\n" \
  "s_lshl_b32 s99, s73, 10\n" \
  "v_mbcnt_lo_u32_b32 v48, -1, 0\n" \
  "v_mbcnt_hi_u32_b32 v48, -1, v48\n" \
  "v_and_b32 v49, 15, v48\n" \
  "v_lshl_add_u32 v9, v49, 1, s97\n" \
  "v_lshrrev_b32 v49, 4, v48\n" \
  "v_mad_u32_u24 v9, v49, s94, v9\n" \
  "v_add_u32 v7, s99, v1\n" \
  "v_mov_b64 v[12:13], 0\n" \
  "v_mov_b64 v[14:15], 0\n" \
  "v_mov_b64 v[16:17], 0\n" \
  "v_mov_b64 v[18:19], 0\n" \
  "v_mov_b64 v[20:21], 0\n" \
  "v_mov_b64 v[22:23], 0\n" \
  "v_mov_b32 v24, 0x44870000\n" \
  "v_mul_f32 v24, s72, v24\n" \
  "s_nop 0\n" \
  "v_readfirstlane_b32 s86, v24\n" \
  "s_lshr_b32 s86, s86, 23\n" \
  "s_addk_i32 s86, 0xff83\n" \
  "s_ashr_i32 s86, s86, 1\n" \
  "s_max_i32 s86, s86, -20\n" \
  "s_min_i32 s86, s86, 100\n" \
  "s_mov_b32 s82, s86\n" \
  "s_lshr_b32 s83, s72, 23\n" \
  "s_sub_i32 s83, s83, 242\n" \
  "s_lshl_b32 s88, s73, 10\n" \
  "v_add_u32 v24, s88, v1\n" \
  "ds_read_b128 v[32:35], v24 offset:0\n" \
  "s_waitcnt lgkmcnt(0)\n" \
  "v_mov_b32 v45, 0\n" \
  "v_dot2c_f32_bf16 v45, v32, v32\n" \
  "v_dot2c_f32_bf16 v45, v33, v33\n" \
  "v_dot2c_f32_bf16 v45, v34, v34\n" \
  "v_dot2c_f32_bf16 v45, v35, v35\n" \
  "s_add_u32 s88, s74, 157760\n" \
  "v_mov_b32 v44, s88\n" \
  "s_nop 0\n" \
  "v_cmpx_lt_f32 vcc, 2.0, v45\n" \
  "ds_max_u32 v44, v45\n" \
  "s_mov_b64 exec, -1\n" \
  ".p2align 6\n" \
  ".LCA_AL_%=:\n" \
  ".LCA_TOP_%=:\n" \
  "s_waitcnt lgkmcnt(0)\n" \
  "s_barrier\n" \
  "ds_read_b32 v8, v1 offset:26688\n" \
  "s_cmp_lg_u32 s73, 3\n" \
  "s_cbranch_scc1 .LCA_NOTK_%=\n" \
  "s_memrealtime s[38:39]\n" \
  "v_mov_b32 v26, 1\n" \
  "s_mul_i32 s23, s71, %[pp]\n" \
  "s_lshr_b32 s62, s66, 4\n" \
  "s_mov_b64 exec, 1\n" \
  "s_waitcnt lgkmcnt(0)\n" \
  "s_mov_b32 s67, s38\n" \
  "s_add_u32 s38, s38, s62\n" \
  "v_mov_b32 v25, s38\n" \
  "buffer_atomic_umax v25, off, s[16:19], s68\n" \
  "buffer_atomic_add v26, off, s[16:19], s68 offset:64\n" \
  "v_add_u32 v27, s23, v25\n" \
  "buffer_store_dword v27, off, s[16:19], s69 sc1\n" \
  "s_mov_b64 exec, -1\n" \
  "s_branch .LCA_SIDE_%=\n" \
  ".LCA_NOTK_%=:\n" \
  "s_cmp_lg_u32 s73, 0\n" \
  "s_cbranch_scc1 .LCA_NOZ_%=\n" \
  "v_mov_b64 v[26:27], s[92:93]\n" \
  "s_xor_b32 s88, s60, 0x8000\n" \
  "s_add_u32 s88, s88, s61\n" \
  "buffer_store_dwordx2 v[26:27], v5, s[40:43], s88 offen sc1\n" \
  ".LCA_NOZ_%=:\n" \
  "s_nop 7\n" \
  ".LCA_SIDE_%=:\n" \
  "s_waitcnt lgkmcnt(0)\n" \
  "v_readfirstlane_b32 s86, v8\n" \
  "s_mov_b64 vcc, s[76:77]\n" \
  "s_lshr_b32 s86, s86, 23\n" \
  "s_add_i32 s86, s86, s83\n" \
  "s_ashr_i32 s86, s86, 1\n" \
  "s_max_i32 s86, s86, s82\n" \
  "s_min_i32 s86, s86, 100\n" \
  "s_lshl_b32 s86, s86, 23\n" \
  "s_add_i32 s75, s86, 0x3c800000\n" \
  "s_cmp_eq_u32 s71, 0\n" \
  "ds_read_b128 v[32:35], v1 offset:0\n" \
  "ds_read_b128 v[36:39], v1 offset:1024\n" \
  "ds_read_b128 v[48:51], v1 offset:2048\n" \
  "ds_read_b128 v[52:55], v1 offset:3072\n" \
  "ds_read_b128 v[40:43], v1 offset:4096\n" \
  "ds_read_b128 v[56:59], v1 offset:5120\n" \
  "ds_read_b128 v[60:63], v1 offset:6144\n" \
  "ds_read_b128 v[44:47], v1 offset:7168\n" \
  "s_setprio 3\n" \
  "s_waitcnt lgkmcnt(7)\n" \
  "v_smfmac_f32_16x16x64_bf16 v[12:15], v[32:35], v[64:71], v0\n" \
  "s_waitcnt lgkmcnt(6)\n" \
  "v_smfmac_f32_16x16x64_bf16 v[12:15], v[36:39], v[72:79], v0\n" \
  "s_waitcnt lgkmcnt(5)\n" \
  "v_smfmac_f32_16x16x64_bf16 v[12:15], v[48:51], v[80:87], v0\n" \
  "s_waitcnt lgkmcnt(4)\n" \
  "v_smfmac_f32_16x16x64_bf16 v[12:15], v[52:55], v[88:95], v0\n" \
  "s_waitcnt lgkmcnt(3)\n" \
  "v_smfmac_f32_16x16x64_bf16 v[12:15], v[40:43], v[96:103], v0\n" \
  "s_waitcnt lgkmcnt(2)\n" \
  "v_smfmac_f32_16x16x64_bf16 v[12:15], v[56:59], v[104:111], v0\n" \
  "s_waitcnt lgkmcnt(1)\n" \
  "v_smfmac_f32_16x16x64_bf16 v[12:15], v[60:63], v[112:119], v0\n" \
  "s_waitcnt lgkmcnt(0)\n" \
  "v_smfmac_f32_16x16x64_bf16 v[12:15], v[44:47], v[120:127], v0\n" \
  "ds_read_b128 v[24:27], v1 offset:10240\n" \
  "ds_read_b128 v[28:31], v1 offset:14336\n" \
  "s_nop 5\n" \
  "v_add_f32 v12, v12, v14\n" \
  "v_add_f32 v13, v13, v15\n" \
  "v_cvt_pk_bf16_f32 v12, v12, v13\n" \
  "ds_write_b16 v9, v12 offset:0\n" \
  "ds_write_b16_d16_hi v9, v12 offset:288\n" \
  "s_setprio 0\n" \
  "s_barrier\n" \
  "v_smfmac_f32_16x16x64_bf16 v[16:19], v[32:35], v[128:135], v0\n" \
  "v_smfmac_f32_16x16x64_bf16 v[20:23], v[32:35], v[192:199], v0\n" \
  "ds_read_b128 v[32:35], v1 offset:11264\n" \
  "v_smfmac_f32_16x16x64_bf16 v[16:19], v[36:39], v[136:143], v0\n" \
  "v_smfmac_f32_16x16x64_bf16 v[20:23], v[36:39], v[200:207], v0\n" \
  "ds_read_b128 v[36:39], v1 offset:15360\n" \
  "v_smfmac_f32_16x16x64_bf16 v[16:19], v[48:51], v[144:151], v0\n" \
  "v_smfmac_f32_16x16x64_bf16 v[20:23], v[48:51], v[208:215], v0\n" \
  "v_smfmac_f32_16x16x64_bf16 v[16:19], v[52:55], v[152:159], v0\n" \
  "v_smfmac_f32_16x16x64_bf16 v[20:23], v[52:55], v[216:223], v0\n" \
  "v_smfmac_f32_16x16x64_bf16 v[16:19], v[40:43], v[160:167], v0\n" \
  "v_smfmac_f32_16x16x64_bf16 v[20:23], v[40:43], v[224:231], v0\n" \
  ".fill ((16 - (. - .LCA_AL_%=)) & 31) >> 2, 4, 0xbf800000\n" \
  "s_waitcnt lgkmcnt(2)\n" \
  "s_barrier\n" \
  "s_setprio 3\n" \
  "ds_read_b128 v[48:51], v2 offset:0\n" \
  "ds_read_b128 v[52:55], v2 offset:64\n" \
  "v_smfmac_f32_16x16x64_bf16 v[16:19], v[56:59], v[168:175], v0\n" \
  "v_smfmac_f32_16x16x64_bf16 v[20:23], v[56:59], v[232:239], v0\n" \
  "ds_read_b128 v[56:59], v2 offset:128\n" \
  "s_cbranch_scc1 .LCA_LAST_%=\n" \
  "s_waitcnt lgkmcnt(2)\n" \
  "v_mfma_f32_16x16x32_bf16 v[12:15], v[24:27], v[48:51], 0\n" \
  "ds_read_b128 v[24:27], v1 offset:12288\n" \
  "v_mfma_f32_16x16x32_bf16 v[8:11], v[28:31], v[48:51], 0\n" \
  "ds_read_b128 v[28:31], v1 offset:16384\n" \
  "v_mfma_f32_16x16x32_bf16 v[40:43], v[48:51], v[48:51], 0\n" \
  "ds_read_b128 v[48:51], v7 offset:18432\n" \
  "v_smfmac_f32_16x16x64_bf16 v[16:19], v[60:63], v[176:183], v0\n" \
  "v_smfmac_f32_16x16x64_bf16 v[20:23], v[60:63], v[240:247], v0\n" \
  "ds_read_b128 v[60:63], v2 offset:192\n" \
  "s_waitcnt lgkmcnt(5)\n" \
  "v_mfma_f32_16x16x32_bf16 v[12:15], v[32:35], v[52:55], v[12:15]\n" \
  "ds_read_b128 v[32:35], v1 offset:13312\n" \
  "v_mfma_f32_16x16x32_bf16 v[8:11], v[36:39], v[52:55], v[8:11]\n" \
  "ds_read_b128 v[36:39], v1 offset:17408\n" \
  "v_mfma_f32_16x16x32_bf16 v[40:43], v[52:55], v[52:55], v[40:43]\n" \
  "ds_read_b128 v[52:55], v7 offset:22528\n" \
  "s_waitcnt lgkmcnt(7)\n" \
  "v_mfma_f32_16x16x32_bf16 v[40:43], v[56:59], v[56:59], v[40:43]\n" \
  "s_waitcnt lgkmcnt(5)\n" \
  "v_mfma_f32_16x16x32_bf16 v[12:15], v[24:27], v[56:59], v[12:15]\n" \
  "ds_read_b128 v[24:27], v6 offset:2048\n" \
  "v_mfma_f32_16x16x32_bf16 v[8:11], v[28:31], v[56:59], v[8:11]\n" \
  "ds_read_b128 v[28:31], v6 offset:3072\n" \
  "s_waitcnt lgkmcnt(5)\n" \
  "v_mfma_f32_16x16x32_bf16 v[40:43], v[60:63], v[60:63], v[40:43]\n" \
  "ds_read_b128 v[56:59], v6 offset:0\n" \
  "s_waitcnt lgkmcnt(4)\n" \
  "v_mfma_f32_16x16x32_bf16 v[12:15], v[32:35], v[60:63], v[12:15]\n" \
  "ds_read_b128 v[32:35], v6 offset:8192\n" \
  "v_mfma_f32_16x16x32_bf16 v[8:11], v[36:39], v[60:63], v[8:11]\n" \
  "ds_read_b128 v[36:39], v6 offset:9216\n" \
  "v_smfmac_f32_16x16x64_bf16 v[16:19], v[44:47], v[184:191], v0\n" \
  "ds_read_b128 v[60:63], v6 offset:1024\n" \
  "s_waitcnt lgkmcnt(6)\n" \
  "v_smfmac_f32_16x16x64_bf16 v[20:23], v[44:47], v[248:255], v0\n" \
  "v_cndmask_b32_dpp v43, v43, v42, vcc quad_perm:[0,1,0,1] row_mask:0xf bank_mask:0xf\n" \
  "v_mov_b32 v41, 0\n" \
  "v_mov_b32 v42, 0\n" \
  "v_cndmask_b32_dpp v12, v8, v12, vcc quad_perm:[0,1,0,1] row_mask:0xf bank_mask:0xf\n" \
  "v_cndmask_b32_dpp v13, v9, v13, vcc quad_perm:[0,1,0,1] row_mask:0xf bank_mask:0xf\n" \
  "v_cndmask_b32_dpp v14, v10, v14, vcc quad_perm:[0,1,0,1] row_mask:0xf bank_mask:0xf\n" \
  "v_cndmask_b32_dpp v15, v11, v15, vcc quad_perm:[0,1,0,1] row_mask:0xf bank_mask:0xf\n" \
  "v_maximum3_f32 v8, v43, v12, v13\n" \
  "v_maximum3_f32 v8, v8, v14, v15\n" \
  "v_add_f32 v16, v16, v18\n" \
  "v_add_f32 v17, v17, v19\n" \
  "v_max_f32_dpp v8, v8, v8 quad_perm:[2,3,0,1] row_mask:0xf bank_mask:0xf\n" \
  "v_mov_b32 v9, v8\n" \
  "v_add_f32 v20, v20, v22\n" \
  "v_add_f32 v21, v21, v23\n" \
  "v_permlane16_swap_b32 v8, v9\n" \
  "v_max_f32 v8, v8, v9\n" \
  "v_mov_b32 v9, v8\n" \
  "v_cvt_pk_bf16_f32 v16, v16, v17\n" \
  "v_cvt_pk_bf16_f32 v17, v20, v21\n" \
  "v_permlane32_swap_b32 v8, v9\n" \
  "v_max_f32 v8, v8, v9\n" \
  "s_barrier\n" \
  "v_mul_f32 v8, s78, v8\n" \
  "v_pk_fma_f32 v[12:13], v[12:13], s[78:79], v[8:9] op_sel_hi:[1,0,0] neg_lo:[0,0,1] neg_hi:[0,0,1]\n" \
  "v_pk_fma_f32 v[14:15], v[14:15], s[78:79], v[8:9] op_sel_hi:[1,0,0] neg_lo:[0,0,1] neg_hi:[0,0,1]\n" \
  "v_fma_f32 v43, v43, s78, -v8\n" \
  "v_exp_f32 v12, v12\n" \
  "v_exp_f32 v13, v13\n" \
  "v_exp_f32 v14, v14\n" \
  "v_exp_f32 v15, v15\n" \
  "v_exp_f32 v43, v43\n" \
  "v_cvt_pk_bf16_f32 v44, v12, v13\n" \
  "v_cvt_pk_bf16_f32 v45, v14, v15\n" \
  "v_pk_add_f32 v[18:19], v[12:13], v[14:15]\n" \
  "v_mov_b32_dpp v46, v44 quad_perm:[2,3,0,1] row_mask:0xf bank_mask:0xf\n" \
  "v_mov_b32_dpp v9, v43 quad_perm:[2,3,0,1] row_mask:0xf bank_mask:0xf\n" \
  "v_mov_b32_dpp v47, v45 quad_perm:[2,3,0,1] row_mask:0xf bank_mask:0xf\n" \
  "v_add_f32 v10, v18, v43\n" \
  "v_cvt_pk_bf16_f32 v40, v43, v9\n" \
  "v_add_f32 v10, v10, v19\n" \
  "v_mfma_f32_16x16x32_bf16 v[12:15], v[48:51], v[44:47], 0\n" \
  "v_cvt_pk_bf16_f32 v43, v43, v9\n" \
  "v_add_f32_dpp v10, v10, v10 quad_perm:[2,3,0,1] row_mask:0xf bank_mask:0xf\n" \
  "v_mfma_f32_16x16x32_bf16 v[20:23], v[52:55], v[44:47], 0\n" \
  "v_mul_f32 v11, s75, v10\n" \
  "v_mul_f32 v10, s75, v10\n" \
  "v_mfma_f32_16x16x16_bf16 v[12:15], v[16:17], v[40:41], v[12:15]\n" \
  "ds_read_b128 v[48:51], v6 offset:10240\n" \
  "v_permlane16_swap_b32 v10, v11\n" \
  "v_add_f32 v10, v10, v11\n" \
  "v_mfma_f32_16x16x16_bf16 v[20:23], v[16:17], v[42:43], v[20:23]\n" \
  "v_mov_b32 v11, v10\n" \
  "ds_read_b128 v[52:55], v6 offset:11264\n" \
  "v_mov_b64 v[18:19], s[84:85]\n" \
  "v_permlane32_swap_b32 v10, v11\n" \
  "v_add_f32 v10, v10, v11\n" \
  "v_rcp_f32 v10, v10\n" \
  "v_permlane16_swap_b32 v16, v17\n" \
  "v_pk_mul_f32 v[12:13], v[12:13], v[10:11] op_sel_hi:[1,0]\n" \
  "v_pk_mul_f32 v[14:15], v[14:15], v[10:11] op_sel_hi:[1,0]\n" \
  "v_pk_mul_f32 v[20:21], v[20:21], v[10:11] op_sel_hi:[1,0]\n" \
  "v_pk_mul_f32 v[22:23], v[22:23], v[10:11] op_sel_hi:[1,0]\n" \
  "v_cvt_pk_bf16_f32 v40, v12, v13\n" \
  "v_cvt_pk_bf16_f32 v41, v14, v15\n" \
  "v_cvt_pk_bf16_f32 v42, v20, v21\n" \
  "ds_write_b64 v4, v[40:41]\n" \
  "v_cvt_pk_bf16_f32 v43, v22, v23\n" \
  "ds_write_b64 v4, v[42:43] offset:1024\n" \
  "v_max_u32 v16, 1, v16\n" \
  "v_max_u32 v17, 1, v17\n" \
  "buffer_store_dwordx2 v[16:17], v3, s[52:55], s70 offen sc1\n" \
  "v_mov_b64 v[12:13], s[84:85]\n" \
  "v_mov_b64 v[14:15], s[84:85]\n" \
  "v_mov_b64 v[16:17], s[84:85]\n" \
  "v_mov_b64 v[20:21], s[84:85]\n" \
  "v_mov_b64 v[22:23], s[84:85]\n" \
  "v_mov_b64 v[8:9], s[84:85]\n" \
  "v_mov_b64 v[10:11], s[84:85]\n" \
  "s_setprio 0\n" \
  "s_waitcnt lgkmcnt(0)\n" \
  "s_barrier\n" \
  "ds_read_b128 v[40:43], v1 offset:8192\n" \
  "ds_read_b128 v[44:47], v1 offset:9216\n" \
  "buffer_load_dword v7, v5, s[16:19], s24 offen sc1\n" \
  "s_waitcnt lgkmcnt(1)\n" \
  "v_smfmac_f32_16x16x64_bf16 v[12:15], v[40:43], v[56:63], v0\n" \
  "ds_read_b128 v[56:59], v6 offset:4096\n" \
  "ds_read_b128 v[60:63], v6 offset:5120\n" \
  "v_smfmac_f32_16x16x64_bf16 v[16:19], v[40:43], v[24:31], v0\n" \
  "ds_read_b128 v[24:27], v6 offset:6144\n" \
  "ds_read_b128 v[28:31], v6 offset:7168\n" \
  "s_waitcnt lgkmcnt(4)\n" \
  "v_smfmac_f32_16x16x64_bf16 v[12:15], v[44:47], v[32:39], v0\n" \
  "ds_read_b128 v[32:35], v6 offset:12288\n" \
  "ds_read_b128 v[36:39], v6 offset:13312\n" \
  "v_smfmac_f32_16x16x64_bf16 v[16:19], v[44:47], v[48:55], v0\n" \
  "ds_read_b128 v[48:51], v6 offset:14336\n" \
  "ds_read_b128 v[52:55], v6 offset:15360\n" \
  "s_nop 0\n" \
  "s_waitcnt lgkmcnt(6)\n" \
  "v_smfmac_f32_16x16x64_bf16 v[20:23], v[40:43], v[56:63], v0\n" \
  "v_add_f32 v12, v12, v14\n" \
  "v_add_f32 v13, v13, v15\n" \
  "global_atomic_add_x2 v5, v[12:13], s[26:27]\n" \
  "s_waitcnt lgkmcnt(4)\n" \
  "v_smfmac_f32_16x16x64_bf16 v[8:11], v[40:43], v[24:31], v0\n" \
  "v_add_f32 v16, v16, v18\n" \
  "v_add_f32 v17, v17, v19\n" \
  "global_atomic_add_x2 v5, v[16:17], s[28:29]\n" \
  "s_waitcnt lgkmcnt(2)\n" \
  "v_smfmac_f32_16x16x64_bf16 v[20:23], v[44:47], v[32:39], v0\n" \
  "s_waitcnt lgkmcnt(0)\n" \
  "v_smfmac_f32_16x16x64_bf16 v[8:11], v[44:47], v[48:55], v0\n" \
  "s_nop 5\n" \
  "v_add_f32 v20, v20, v22\n" \
  "v_add_f32 v21, v21, v23\n" \
  "global_atomic_add_x2 v5, v[20:21], s[30:31]\n" \
  "v_add_f32 v8, v8, v10\n" \
  "v_add_f32 v9, v9, v11\n" \
  "global_atomic_add_x2 v5, v[8:9], s[32:33]\n" \
  "s_memrealtime s[62:63]\n" \
  "v_mbcnt_lo_u32_b32 v48, -1, 0\n" \
  "v_mbcnt_hi_u32_b32 v48, -1, v48\n" \
  "v_lshrrev_b32 v49, 3, v48\n" \
  "v_and_b32 v50, 7, v48\n" \
  "v_lshlrev_b32 v50, 4, v50\n" \
  "v_lshl_or_b32 v44, v49, 8, v50\n" \
  "s_add_u32 s20, s20, 32\n" \
  "s_mov_b32 s21, 0x8000\n" \
  "v_mov_b32 v52, s22\n" \
  "v_mov_b32 v54, s20\n" \
  "s_mul_i32 s88, s71, %[pp]\n" \
  "s_lshl_b32 s89, s71, 10\n" \
  "s_sub_u32 s88, s88, s25\n" \
  "s_mul_i32 s38, s24, %[an]\n" \
  "s_cmp_gt_u32 s89, s38\n" \
  "s_cselect_b32 s89, 0x1000, 0\n" \
  "s_waitcnt vmcnt(4)\n" \
  ".LCA_TKE_%=:\n" \
  "v_readlane_b32 s34, v7, 0\n" \
  "v_readlane_b32 s38, v7, 16\n" \
  "v_readlane_b32 s39, v7, 32\n" \
  "v_readlane_b32 s23, v7, 48\n" \
  "s_max_u32 s34, s34, s38\n" \
  "s_max_u32 s39, s39, s23\n" \
  "v_readlane_b32 s38, v7, 8\n" \
  "s_max_u32 s34, s34, s39\n" \
  "v_readlane_b32 s39, v7, 24\n" \
  "v_readlane_b32 s23, v7, 1\n" \
  "s_sub_u32 s23, s23, s34\n" \
  "s_sub_u32 s23, s23, s88\n" \
  "s_cmp_lt_u32 s23, s89\n" \
  "s_cselect_b32 s23, s23, 0\n" \
  "s_min_u32 s23, s23, %[ka]\n" \
  "s_add_u32 s34, s34, s23\n" \
  "s_add_u32 s34, s34, %[cd]\n" \
  "v_readlane_b32 s23, v7, 40\n" \
  "s_add_u32 s38, s38, s39\n" \
  "v_readlane_b32 s39, v7, 56\n" \
  "s_add_u32 s38, s38, s23\n" \
  "v_mov_b32 v55, s34\n" \
  "s_add_u32 s38, s38, s39\n" \
  "s_cmp_ge_u32 s38, s20\n" \
  "s_cbranch_scc1 .LCA_TKO_%=\n" \
  "s_sub_u32 s21, s21, 1\n" \
  "s_cbranch_scc1 .LCA_TKO_%=\n" \
  "s_sleep 2\n" \
  "buffer_load_dword v7, v5, s[16:19], s24 offen sc1\n" \
  "s_waitcnt vmcnt(0)\n" \
  "s_branch .LCA_TKE_%=\n" \
  ".LCA_TKO_%=:\n" \
  "ds_write_b64 v52, v[54:55]\n" \
  "s_branch .LCA_WAIU_%=\n" \
  ".LCA_WAIS_%=:\n" \
  "s_cmp_lt_i32 s39, -1000\n" \
  "s_cbranch_scc1 .LCA_WAIE_%=\n" \
  "s_sleep 1\n" \
  ".LCA_WAIU_%=:\n" \
  "s_memrealtime s[38:39]\n" \
  "s_waitcnt lgkmcnt(0)\n" \
  "s_sub_i32 s39, s38, s34\n" \
  "s_cmp_lt_i32 s39, -7\n" \
  "s_cbranch_scc1 .LCA_WAIS_%=\n" \
  "s_cmp_lt_i32 s39, 0\n" \
  "s_cbranch_scc1 .LCA_WAIU_%=\n" \
  ".LCA_WAIE_%=:\n" \
  "buffer_load_dwordx4 v[24:27], v44, s[40:43], s60 offen sc1\n" \
  "buffer_load_dwordx4 v[28:31], v44, s[40:43], s60 offen offset:2048 sc1\n" \
  "s_sub_u32 s62, s62, s67\n" \
  "s_lshl_b32 s62, s62, 4\n" \
  "s_sub_i32 s62, s62, s66\n" \
  "s_min_i32 s62, s62, 128\n" \
  "s_max_i32 s62, s62, -256\n" \
  "s_ashr_i32 s62, s62, 2\n" \
  "s_add_i32 s66, s66, s62\n" \
  "s_add_u32 s70, s70, 0x10000\n" \
  "v_mbcnt_lo_u32_b32 v48, -1, 0\n" \
  "v_mbcnt_hi_u32_b32 v48, -1, v48\n" \
  "v_and_b32 v49, 15, v48\n" \
  "v_lshl_add_u32 v9, v49, 1, s97\n" \
  "v_lshrrev_b32 v49, 4, v48\n" \
  "v_mad_u32_u24 v9, v49, s94, v9\n" \
  "v_add_u32 v7, s99, v1\n" \
  "v_mov_b64 v[12:13], 0\n" \
  "v_mov_b64 v[14:15], 0\n" \
  "v_mov_b64 v[16:17], 0\n" \
  "v_mov_b64 v[18:19], 0\n" \
  "v_mov_b64 v[20:21], 0\n" \
  "v_mov_b64 v[22:23], 0\n" \
  "v_and_b32 v49, 3, v48\n" \
  "v_lshl_add_u32 v40, v49, 2, s98\n" \
  "v_lshrrev_b32 v49, 2, v48\n" \
  "v_bfe_u32 v48, v48, 4, 1\n" \
  "v_xor_b32 v49, v49, v48\n" \
  "v_lshl_add_u32 v40, v49, 5, v40\n" \
  "s_add_i32 s80, s86, 0x35000000\n" \
  "s_sub_u32 s91, 0x4a350000, s86\n" \
  "s_min_u32 s91, s91, 0x4ca00000\n" \
  "s_mov_b32 s89, s60\n" \
  "s_xor_b32 s60, s60, 0x80\n" \
  "s_bitcmp0_b32 s60, 7\n" \
  "s_cselect_b32 s88, 0x8000, 0\n" \
  "s_add_u32 s60, s60, s88\n" \
  "s_and_b32 s60, s60, 0xffff\n" \
  "s_add_u32 s58, s56, s60\n" \
  "s_addc_u32 s59, s57, 0\n" \
  "s_xor_b32 s38, s35, 0\n" \
  "s_add_u32 s26, s58, s38\n" \
  "s_addc_u32 s27, s59, 0\n" \
  "s_xor_b32 s38, s35, 1024\n" \
  "s_add_u32 s28, s58, s38\n" \
  "s_addc_u32 s29, s59, 0\n" \
  "s_xor_b32 s38, s35, 2048\n" \
  "s_add_u32 s30, s58, s38\n" \
  "s_addc_u32 s31, s59, 0\n" \
  "s_xor_b32 s38, s35, 3072\n" \
  "s_add_u32 s32, s58, s38\n" \
  "s_addc_u32 s33, s59, 0\n" \
  "s_sub_u32 s71, s71, 1\n" \
  "s_mov_b32 s87, 0x40000\n" \
  "s_branch .LCA_CHK_%=\n" \
  ".LCA_POLL_%=:\n" \
  "buffer_load_dwordx4 v[24:27], v44, s[40:43], s89 offen sc1\n" \
  "buffer_load_dwordx4 v[28:31], v44, s[40:43], s89 offen offset:2048 sc1\n" \
  ".LCA_CHK_%=:\n" \
  "s_sub_u32 s87, s87, 1\n" \
  "s_cbranch_scc1 .LCA_END_%=\n" \
  "s_waitcnt vmcnt(1)\n" \
  "v_cvt_f32_i32 v32, v24\n" \
  "v_cvt_f32_i32 v33, v25\n" \
  "v_cvt_f32_i32 v34, v26\n" \
  "v_cvt_f32_i32 v35, v27\n" \
  "v_pk_mul_f32 v[24:25], v[32:33], s[80:81] op_sel_hi:[1,0]\n" \
  "v_pk_mul_f32 v[26:27], v[34:35], s[80:81] op_sel_hi:[1,0]\n" \
  "v_max3_f32 v45, |v32|, |v33|, |v34|\n" \
  "v_cvt_pk_bf16_f32 v24, v24, v26\n" \
  "v_cvt_pk_bf16_f32 v25, v25, v27\n" \
  "ds_write2_b32 v40, v24, v25 offset0:0 offset1:4\n" \
  "v_max3_f32 v45, |v45|, |v35|, |v35|\n" \
  "s_waitcnt vmcnt(0)\n" \
  "v_cvt_f32_i32 v32, v28\n" \
  "v_cvt_f32_i32 v33, v29\n" \
  "v_cvt_f32_i32 v34, v30\n" \
  "v_cvt_f32_i32 v35, v31\n" \
  "v_pk_mul_f32 v[28:29], v[32:33], s[80:81] op_sel_hi:[1,0]\n" \
  "v_pk_mul_f32 v[30:31], v[34:35], s[80:81] op_sel_hi:[1,0]\n" \
  "v_max3_f32 v45, |v45|, |v32|, |v33|\n" \
  "v_cvt_pk_bf16_f32 v28, v28, v30\n" \
  "v_cvt_pk_bf16_f32 v29, v29, v31\n" \
  "ds_write2_b32 v40, v28, v29 offset0:128 offset1:132\n" \
  "v_max3_f32 v45, |v45|, |v34|, |v35|\n" \
  "v_cmp_lt_f32 vcc, s91, v45\n" \
  "s_cbranch_vccnz .LCA_BIG_%=\n" \
  ".LCA_SML_%=:\n" \
  "s_branch .LCA_TOP_%=\n" \
  ".LCA_BIG_%=:\n" \
  "v_cmp_lt_f32 vcc, 0x4ca00000, v45\n" \
  "s_cbranch_vccnz .LCA_POLL_%=\n" \
  "v_mul_f32 v45, s80, v45\n" \
  "v_mul_f32 v45, v45, v45\n" \
  "s_add_u32 s88, s74, 157760\n" \
  "v_mov_b32 v44, s88\n" \
  "s_nop 0\n" \
  "v_cmpx_lt_f32 vcc, 2.0, v45\n" \
  "ds_max_u32 v44, v45\n" \
  "s_mov_b64 exec, -1\n" \
  "s_branch .LCA_SML_%=\n" \
  ".LCA_LAST_%=:\n" \
  "s_nop 0\n" \
  "v_smfmac_f32_16x16x64_bf16 v[16:19], v[60:63], v[176:183], v0\n" \
  "v_smfmac_f32_16x16x64_bf16 v[20:23], v[60:63], v[240:247], v0\n" \
  "v_smfmac_f32_16x16x64_bf16 v[16:19], v[44:47], v[184:191], v0\n" \
  "v_smfmac_f32_16x16x64_bf16 v[20:23], v[44:47], v[248:255], v0\n" \
  "s_nop 7\n" \
  "v_add_f32 v16, v16, v18\n" \
  "v_add_f32 v17, v17, v19\n" \
  "v_add_f32 v20, v20, v22\n" \
  "v_add_f32 v21, v21, v23\n" \
  "v_cvt_pk_bf16_f32 v16, v16, v17\n" \
  "v_cvt_pk_bf16_f32 v17, v20, v21\n" \
  "s_nop 1\n" \
  "v_permlane16_swap_b32 v16, v17\n" \
  "v_max_u32 v16, 1, v16\n" \
  "v_max_u32 v17, 1, v17\n" \
  "buffer_store_dwordx2 v[16:17], v3, s[52:55], s70 offen sc1\n" \
  "s_waitcnt vmcnt(0)\n" \
  ".LCA_END_%=:\n" \
  "s_endpgm\n"
#define CHAIN_ASM_VO \
  "s_mov_b32 s66, %[d0]\n" \
  "s_mov_b32 s94, 576\n" \
  "s_mov_b32 s95, 288\n" \
  "s_mov_b32 s84, 0x43400000\n" \
  "s_mov_b32 s85, 0x43400000\n" \
  "s_mov_b32 s92, 0x88000000\n" \
  "s_mov_b32 s93, 0x87fffff7\n" \
  "s_mov_b32 s78, 0x3e0293ee\n" \
  "s_mov_b32 s76, 0x33333333\n" \
  "s_mov_b32 s77, 0x33333333\n" \
  "s_add_u32 s58, s56, s60\n" \
  "s_addc_u32 s59, s57, 0\n" \
  "s_xor_b32 s38, s35, 0\n" \
  "s_add_u32 s26, s58, s38\n" \
  "s_addc_u32 s27, s59, 0\n" \
  "s_xor_b32 s38, s35, 1024\n" \
  "s_add_u32 s28, s58, s38\n" \
  "s_addc_u32 s29, s59, 0\n" \
  "s_xor_b32 s38, s35, 2048\n" \
  "s_add_u32 s30, s58, s38\n" \
  "s_addc_u32 s31, s59, 0\n" \
  "s_xor_b32 s38, s35, 3072\n" \
  "s_add_u32 s32, s58, s38\n" \
  "s_addc_u32 s33, s59, 0\n" \
  "s_lshr_b32 s98, s60, 2\n" \
  "s_add_u32 s98, s98, s74\n" \
  "s_add_u32 s98, s98, 131072\n" \
  "s_lshl_b32 s97, s73, 5\n" \
  "s_add_u32 s97, s97, s74\n" \
  "s_add_u32 s97, s97, 158592\n" \
  "s_lshl_b32 s99, s73, 11\n" \
  "s_add_u32 s99, s99, s74\n" \
  "s_add_u32 s99, s99, 133120\n" \
  "v_mbcnt_lo_u32_b32 v48, -1, 0\n" \
  "v_mbcnt_hi_u32_b32 v48, -1, v48\n" \
  "s_sub_u32 s88, s73, 4\n" \
  "v_and_b32 v51, 15, v48\n" \
  "v_lshl_add_u32 v4, v51, 1, s97\n" \
  "v_lshrrev_b32 v49, 4, v48\n" \
  "v_mad_u32_u24 v4, v49, s94, v4\n" \
  "v_lshrrev_b32 v49, 2, v51\n" \
  "v_and_b32 v50, 5, v51\n" \
  "v_and_or_b32 v49, v49, 2, v50\n" \
  "s_lshl_b32 s89, s88, 6\n" \
  "s_add_u32 s89, s89, s74\n" \
  "s_add_u32 s89, s89, 161152\n" \
  "v_and_b32 v50, 48, v48\n" \
  "v_add_u32 v6, s89, v50\n" \
  "v_mad_u32_u24 v6, v49, s95, v6\n" \
  "v_lshlrev_b32 v7, 3, v50\n" \
  "v_lshl_add_u32 v7, v49, 4, v7\n" \
  "v_and_b32 v49, 2, v51\n" \
  "v_lshl_add_u32 v7, v49, 29, v7\n" \
  "v_lshlrev_b32 v10, 4, v48\n" \
  "v_mov_b64 v[12:13], 0\n" \
  "v_mov_b64 v[14:15], 0\n" \
  "v_mov_b64 v[16:17], 0\n" \
  "v_mov_b64 v[18:19], 0\n" \
  "v_mov_b64 v[20:21], 0\n" \
  "v_mov_b64 v[22:23], 0\n" \
  "v_mov_b32 v24, 0x44870000\n" \
  "v_mul_f32 v24, s72, v24\n" \
  "s_nop 0\n" \
  "v_readfirstlane_b32 s86, v24\n" \
  "s_lshr_b32 s86, s86, 23\n" \
  "s_addk_i32 s86, 0xff83\n" \
  "s_ashr_i32 s86, s86, 1\n" \
  "s_max_i32 s86, s86, -20\n" \
  "s_min_i32 s86, s86, 100\n" \
  "s_mov_b32 s82, s86\n" \
  "s_lshr_b32 s83, s72, 23\n" \
  "s_sub_i32 s83, s83, 242\n" \
  "s_lshl_b32 s88, s73, 10\n" \
  "v_add_u32 v24, s88, v1\n" \
  "ds_read_b128 v[32:35], v24 offset:0\n" \
  "s_waitcnt lgkmcnt(0)\n" \
  "v_mov_b32 v45, 0\n" \
  "v_dot2c_f32_bf16 v45, v32, v32\n" \
  "v_dot2c_f32_bf16 v45, v33, v33\n" \
  "v_dot2c_f32_bf16 v45, v34, v34\n" \
  "v_dot2c_f32_bf16 v45, v35, v35\n" \
  "s_add_u32 s88, s74, 157760\n" \
  "v_mov_b32 v44, s88\n" \
  "s_nop 0\n" \
  "v_cmpx_lt_f32 vcc, 2.0, v45\n" \
  "ds_max_u32 v44, v45\n" \
  "s_mov_b64 exec, -1\n" \
  ".p2align 6\n" \
  ".LCV_AL_%=:\n" \
  ".LCV_TOP_%=:\n" \
  "s_waitcnt lgkmcnt(0)\n" \
  "s_barrier\n" \
  "s_setprio 2\n" \
  "ds_read_b128 v[24:27], v1 offset:0\n" \
  "ds_read_b128 v[28:31], v1 offset:1024\n" \
  "ds_read_b128 v[32:35], v1 offset:2048\n" \
  "ds_read_b128 v[36:39], v1 offset:3072\n" \
  "buffer_load_dword v5, off, s[44:47], s68 sc1\n" \
  "s_waitcnt lgkmcnt(3)\n" \
  "v_smfmac_f32_16x16x64_bf16 v[20:23], v[24:27], v[192:199], v0\n" \
  "ds_read_b128 v[40:43], v1 offset:4096\n" \
  "s_waitcnt lgkmcnt(3)\n" \
  "v_smfmac_f32_16x16x64_bf16 v[20:23], v[28:31], v[200:207], v0\n" \
  "ds_read_b128 v[44:47], v1 offset:5120\n" \
  "s_waitcnt lgkmcnt(3)\n" \
  "v_smfmac_f32_16x16x64_bf16 v[20:23], v[32:35], v[208:215], v0\n" \
  "ds_read_b128 v[48:51], v1 offset:6144\n" \
  "s_waitcnt lgkmcnt(3)\n" \
  "v_smfmac_f32_16x16x64_bf16 v[20:23], v[36:39], v[216:223], v0\n" \
  "ds_read_b128 v[52:55], v1 offset:7168\n" \
  "s_waitcnt lgkmcnt(3)\n" \
  "v_smfmac_f32_16x16x64_bf16 v[20:23], v[40:43], v[224:231], v0\n" \
  "s_waitcnt lgkmcnt(2)\n" \
  "v_smfmac_f32_16x16x64_bf16 v[20:23], v[44:47], v[232:239], v0\n" \
  "s_waitcnt lgkmcnt(1)\n" \
  "v_smfmac_f32_16x16x64_bf16 v[20:23], v[48:51], v[240:247], v0\n" \
  "s_waitcnt lgkmcnt(0)\n" \
  "v_smfmac_f32_16x16x64_bf16 v[20:23], v[52:55], v[248:255], v0\n" \
  "s_nop 0\n" \
  "v_smfmac_f32_16x16x64_bf16 v[12:15], v[24:27], v[64:71], v0\n" \
  "s_nop 0\n" \
  "v_smfmac_f32_16x16x64_bf16 v[12:15], v[28:31], v[72:79], v0\n" \
  "s_nop 0\n" \
  "v_smfmac_f32_16x16x64_bf16 v[12:15], v[32:35], v[80:87], v0\n" \
  "ds_read_b32 v11, v1 offset:26688\n" \
  "v_smfmac_f32_16x16x64_bf16 v[12:15], v[36:39], v[88:95], v0\n" \
  "v_add_f32 v20, v20, v22\n" \
  "v_smfmac_f32_16x16x64_bf16 v[12:15], v[40:43], v[96:103], v0\n" \
  "v_add_f32 v21, v21, v23\n" \
  "v_smfmac_f32_16x16x64_bf16 v[12:15], v[44:47], v[104:111], v0\n" \
  "v_cvt_pk_bf16_f32 v20, v20, v21\n" \
  "v_smfmac_f32_16x16x64_bf16 v[12:15], v[48:51], v[112:119], v0\n" \
  "ds_write_b16 v4, v20 offset:0\n" \
  "v_smfmac_f32_16x16x64_bf16 v[12:15], v[52:55], v[120:127], v0\n" \
  "ds_write_b16_d16_hi v4, v20 offset:288\n" \
  "v_smfmac_f32_16x16x64_bf16 v[16:19], v[24:27], v[128:135], v0\n" \
  "s_nop 1\n" \
  "v_smfmac_f32_16x16x64_bf16 v[16:19], v[28:31], v[136:143], v0\n" \
  "s_nop 1\n" \
  "v_smfmac_f32_16x16x64_bf16 v[16:19], v[32:35], v[144:151], v0\n" \
  "v_add_f32 v12, v12, v14\n" \
  "v_smfmac_f32_16x16x64_bf16 v[16:19], v[36:39], v[152:159], v0\n" \
  "v_add_f32 v13, v13, v15\n" \
  "v_smfmac_f32_16x16x64_bf16 v[16:19], v[40:43], v[160:167], v0\n" \
  "v_cvt_pk_bf16_f32 v12, v12, v13\n" \
  "v_smfmac_f32_16x16x64_bf16 v[16:19], v[44:47], v[168:175], v0\n" \
  "ds_write_b16 v4, v12 offset:128\n" \
  "v_smfmac_f32_16x16x64_bf16 v[16:19], v[48:51], v[176:183], v0\n" \
  "ds_write_b16_d16_hi v4, v12 offset:416\n" \
  "v_smfmac_f32_16x16x64_bf16 v[16:19], v[52:55], v[184:191], v0\n" \
  "s_barrier\n" \
  "s_nop 6\n" \
  "v_add_f32 v16, v16, v18\n" \
  "v_add_f32 v17, v17, v19\n" \
  "v_cvt_pk_bf16_f32 v16, v16, v17\n" \
  "ds_write_b16 v4, v16 offset:2560\n" \
  "ds_write_b16_d16_hi v4, v16 offset:2848\n" \
  "s_waitcnt lgkmcnt(0)\n" \
  "s_barrier\n" \
  "s_setprio 0\n" \
  "ds_read_b128 v[60:63], v6 offset:0\n" \
  "v_readfirstlane_b32 s86, v11\n" \
  "s_add_u32 s88, s74, 157760\n" \
  "v_mov_b32 v8, s88\n" \
  "v_mov_b32 v9, 0\n" \
  "ds_write_b32 v8, v9\n" \
  "s_lshr_b32 s86, s86, 23\n" \
  "s_add_i32 s86, s86, s83\n" \
  "s_ashr_i32 s86, s86, 1\n" \
  "s_max_i32 s86, s86, s82\n" \
  "s_min_i32 s86, s86, 100\n" \
  "s_lshl_b32 s86, s86, 23\n" \
  "s_cmp_eq_u32 s71, 0\n" \
  "s_waitcnt lgkmcnt(1)\n" \
  "v_max_u32 v60, 1, v60\n" \
  "v_max_u32 v61, 1, v61\n" \
  "v_max_u32 v62, 1, v62\n" \
  "v_max_u32 v63, 1, v63\n" \
  "buffer_store_dwordx4 v[60:63], v7, s[48:51], s69 offen sc1\n" \
  "s_cbranch_scc1 .LCV_LAST_%=\n" \
  "s_waitcnt vmcnt(1)\n" \
  "v_cmp_eq_u32 vcc, 0, v5\n" \
  "s_cmp_eq_u64 vcc, 0\n" \
  "s_cselect_b32 s90, 1, 0\n" \
  "s_barrier\n" \
  "ds_read_b128 v[24:27], v3 offset:0\n" \
  "ds_read_b128 v[28:31], v3 offset:1024\n" \
  "ds_read_b128 v[32:35], v3 offset:2048\n" \
  "ds_read_b128 v[36:39], v3 offset:3072\n" \
  "ds_read_b128 v[40:43], v3 offset:8192\n" \
  "ds_read_b128 v[44:47], v3 offset:9216\n" \
  "ds_read_b128 v[48:51], v3 offset:10240\n" \
  "ds_read_b128 v[52:55], v3 offset:11264\n" \
  "ds_read_b128 v[56:59], v3 offset:4096\n" \
  "ds_read_b128 v[60:63], v3 offset:5120\n" \
  "s_cbranch_scc0 .LCV_NOTOK_%=\n" \
  "s_mov_b32 m0, s99\n" \
  "s_add_u32 s88, s99, 8192\n" \
  "global_load_lds_dwordx4 v10, s[62:63] sc1\n" \
  "global_load_lds_dwordx4 v10, s[62:63] offset:1024 sc1\n" \
  "s_mov_b32 m0, s88\n" \
  "s_nop 0\n" \
  "global_load_lds_dwordx4 v10, s[64:65] sc1\n" \
  "global_load_lds_dwordx4 v10, s[64:65] offset:1024 sc1\n" \
  ".LCV_NOTOK_%=:\n" \
  "v_mov_b64 v[12:13], s[84:85]\n" \
  "v_mov_b64 v[14:15], s[84:85]\n" \
  "v_mov_b64 v[16:17], s[84:85]\n" \
  "v_mov_b64 v[18:19], s[84:85]\n" \
  "v_mov_b64 v[20:21], s[84:85]\n" \
  "v_mov_b64 v[22:23], s[84:85]\n" \
  "s_waitcnt lgkmcnt(0)\n" \
  "s_barrier\n" \
  "ds_read_b128 v[4:7], v1 offset:8192\n" \
  "ds_read_b128 v[8:11], v1 offset:9216\n" \
  "s_waitcnt lgkmcnt(1)\n" \
  "v_smfmac_f32_16x16x64_bf16 v[12:15], v[4:7], v[24:31], v0\n" \
  "ds_read_b128 v[24:27], v3 offset:12288\n" \
  "ds_read_b128 v[28:31], v3 offset:13312\n" \
  "v_smfmac_f32_16x16x64_bf16 v[16:19], v[4:7], v[32:39], v0\n" \
  "ds_read_b128 v[32:35], v3 offset:6144\n" \
  "ds_read_b128 v[36:39], v3 offset:7168\n" \
  "s_waitcnt lgkmcnt(4)\n" \
  "v_smfmac_f32_16x16x64_bf16 v[12:15], v[8:11], v[40:47], v0\n" \
  "v_smfmac_f32_16x16x64_bf16 v[16:19], v[8:11], v[48:55], v0\n" \
  "s_cmp_eq_u32 s90, 1\n" \
  "s_cbranch_scc1 .LCV_NMT_%=\n" \
  "buffer_load_dword v55, off, s[44:47], s68 sc1\n" \
  ".LCV_NMT_%=:\n" \
  "v_smfmac_f32_16x16x64_bf16 v[20:23], v[4:7], v[56:63], v0\n" \
  "ds_read_b128 v[56:59], v3 offset:14336\n" \
  "ds_read_b128 v[60:63], v3 offset:15360\n" \
  "v_mov_b64 v[40:41], s[84:85]\n" \
  "v_mov_b64 v[42:43], s[84:85]\n" \
  "v_add_f32 v12, v12, v14\n" \
  "v_add_f32 v13, v13, v15\n" \
  "global_atomic_add_x2 v2, v[12:13], s[26:27]\n" \
  "s_waitcnt lgkmcnt(4)\n" \
  "v_smfmac_f32_16x16x64_bf16 v[20:23], v[8:11], v[24:31], v0\n" \
  "v_add_f32 v16, v16, v18\n" \
  "v_add_f32 v17, v17, v19\n" \
  "global_atomic_add_x2 v2, v[16:17], s[28:29]\n" \
  "s_waitcnt lgkmcnt(2)\n" \
  "v_smfmac_f32_16x16x64_bf16 v[40:43], v[4:7], v[32:39], v0\n" \
  "s_waitcnt lgkmcnt(0)\n" \
  "v_smfmac_f32_16x16x64_bf16 v[40:43], v[8:11], v[56:63], v0\n" \
  "s_nop 1\n" \
  "v_add_f32 v20, v20, v22\n" \
  "v_add_f32 v21, v21, v23\n" \
  "global_atomic_add_x2 v2, v[20:21], s[30:31]\n" \
  "s_nop 3\n" \
  "v_add_f32 v40, v40, v42\n" \
  "v_add_f32 v41, v41, v43\n" \
  "global_atomic_add_x2 v2, v[40:41], s[32:33]\n" \
  "v_mbcnt_lo_u32_b32 v48, -1, 0\n" \
  "v_mbcnt_hi_u32_b32 v48, -1, v48\n" \
  "s_add_u32 s20, s20, 32\n" \
  "s_cmp_eq_u32 s90, 1\n" \
  "s_cbranch_scc1 .LCV_HAVE_%=\n" \
  "s_mov_b32 s87, 0x40000\n" \
  "v_lshlrev_b32 v10, 4, v48\n" \
  "s_waitcnt vmcnt(4)\n" \
  "v_cmp_eq_u32 vcc, 0, v55\n" \
  "s_cmp_eq_u64 vcc, 0\n" \
  "s_cbranch_scc1 .LCV_GOT_%=\n" \
  ".LCV_LATE_%=:\n" \
  "buffer_load_dword v58, off, s[44:47], s68 sc1\n" \
  "s_sub_u32 s87, s87, 1\n" \
  "s_cbranch_scc1 .LCV_END_%=\n" \
  "s_waitcnt vmcnt(0)\n" \
  "v_cmp_eq_u32 vcc, 0, v58\n" \
  "s_cmp_eq_u64 vcc, 0\n" \
  "s_cbranch_scc1 .LCV_GOT_%=\n" \
  "s_sleep 12\n" \
  "s_branch .LCV_LATE_%=\n" \
  ".LCV_GOT_%=:\n" \
  "s_mov_b32 m0, s99\n" \
  "s_add_u32 s88, s99, 8192\n" \
  "global_load_lds_dwordx4 v10, s[62:63] sc1\n" \
  "global_load_lds_dwordx4 v10, s[62:63] offset:1024 sc1\n" \
  "s_mov_b32 m0, s88\n" \
  "s_nop 0\n" \
  "global_load_lds_dwordx4 v10, s[64:65] sc1\n" \
  "global_load_lds_dwordx4 v10, s[64:65] offset:1024 sc1\n" \
  ".LCV_HAVE_%=:\n" \
  "v_lshrrev_b32 v49, 3, v48\n" \
  "v_and_b32 v50, 7, v48\n" \
  "v_lshlrev_b32 v50, 4, v50\n" \
  "v_lshl_or_b32 v44, v49, 8, v50\n" \
  "s_mov_b32 s21, 0x8000\n" \
  "v_mov_b32 v52, s22\n" \
  "v_mov_b32 v54, s20\n" \
  "s_branch .LCV_TKF_%=\n" \
  ".LCV_TKE_%=:\n" \
  "s_sleep 1\n" \
  ".LCV_TKF_%=:\n" \
  "ds_read_b64 v[54:55], v52\n" \
  "s_sub_u32 s21, s21, 1\n" \
  "s_cbranch_scc1 .LCV_TKO_%=\n" \
  "s_waitcnt lgkmcnt(0)\n" \
  "v_readfirstlane_b32 s38, v54\n" \
  "v_readfirstlane_b32 s34, v55\n" \
  "s_cmp_eq_u32 s38, s20\n" \
  "s_cbranch_scc0 .LCV_TKE_%=\n" \
  ".LCV_TKO_%=:\n" \
  "s_branch .LCV_WAIU_%=\n" \
  ".LCV_WAIS_%=:\n" \
  "s_cmp_lt_i32 s39, -1000\n" \
  "s_cbranch_scc1 .LCV_WAIE_%=\n" \
  "s_sleep 1\n" \
  ".LCV_WAIU_%=:\n" \
  "s_memrealtime s[38:39]\n" \
  "s_waitcnt lgkmcnt(0)\n" \
  "s_sub_i32 s39, s38, s34\n" \
  "s_cmp_lt_i32 s39, -7\n" \
  "s_cbranch_scc1 .LCV_WAIS_%=\n" \
  "s_cmp_lt_i32 s39, 0\n" \
  "s_cbranch_scc1 .LCV_WAIU_%=\n" \
  ".LCV_WAIE_%=:\n" \
  "buffer_load_dwordx4 v[24:27], v44, s[40:43], s60 offen sc1\n" \
  "buffer_load_dwordx4 v[28:31], v44, s[40:43], s60 offen offset:2048 sc1\n" \
  "v_mbcnt_lo_u32_b32 v48, -1, 0\n" \
  "v_mbcnt_hi_u32_b32 v48, -1, v48\n" \
  "s_sub_u32 s88, s73, 4\n" \
  "v_and_b32 v51, 15, v48\n" \
  "v_lshl_add_u32 v4, v51, 1, s97\n" \
  "v_lshrrev_b32 v49, 4, v48\n" \
  "v_mad_u32_u24 v4, v49, s94, v4\n" \
  "v_lshrrev_b32 v49, 2, v51\n" \
  "v_and_b32 v50, 5, v51\n" \
  "v_and_or_b32 v49, v49, 2, v50\n" \
  "s_lshl_b32 s89, s88, 6\n" \
  "s_add_u32 s89, s89, s74\n" \
  "s_add_u32 s89, s89, 161152\n" \
  "v_and_b32 v50, 48, v48\n" \
  "v_add_u32 v6, s89, v50\n" \
  "v_mad_u32_u24 v6, v49, s95, v6\n" \
  "v_lshlrev_b32 v7, 3, v50\n" \
  "v_lshl_add_u32 v7, v49, 4, v7\n" \
  "v_and_b32 v49, 2, v51\n" \
  "v_lshl_add_u32 v7, v49, 29, v7\n" \
  "v_lshlrev_b32 v10, 4, v48\n" \
  "v_mov_b64 v[12:13], 0\n" \
  "v_mov_b64 v[14:15], 0\n" \
  "v_mov_b64 v[16:17], 0\n" \
  "v_mov_b64 v[18:19], 0\n" \
  "v_mov_b64 v[20:21], 0\n" \
  "v_mov_b64 v[22:23], 0\n" \
  "v_and_b32 v49, 3, v48\n" \
  "v_lshl_add_u32 v40, v49, 2, s98\n" \
  "v_lshrrev_b32 v49, 2, v48\n" \
  "v_bfe_u32 v48, v48, 4, 1\n" \
  "v_xor_b32 v49, v49, v48\n" \
  "v_lshl_add_u32 v40, v49, 5, v40\n" \
  "s_add_u32 s62, s62, 0x80000\n" \
  "s_addc_u32 s63, s63, 0\n" \
  "s_add_u32 s64, s64, 0x40000\n" \
  "s_addc_u32 s65, s65, 0\n" \
  "s_add_u32 s68, s68, 0x1000\n" \
  "s_add_u32 s69, s69, 0x10000\n" \
  "s_add_i32 s80, s86, 0x35000000\n" \
  "s_sub_u32 s91, 0x4a350000, s86\n" \
  "s_min_u32 s91, s91, 0x4ca00000\n" \
  "s_mov_b32 s89, s60\n" \
  "s_xor_b32 s60, s60, 0x80\n" \
  "s_bitcmp0_b32 s60, 7\n" \
  "s_cselect_b32 s88, 0x8000, 0\n" \
  "s_add_u32 s60, s60, s88\n" \
  "s_and_b32 s60, s60, 0xffff\n" \
  "s_add_u32 s58, s56, s60\n" \
  "s_addc_u32 s59, s57, 0\n" \
  "s_xor_b32 s38, s35, 0\n" \
  "s_add_u32 s26, s58, s38\n" \
  "s_addc_u32 s27, s59, 0\n" \
  "s_xor_b32 s38, s35, 1024\n" \
  "s_add_u32 s28, s58, s38\n" \
  "s_addc_u32 s29, s59, 0\n" \
  "s_xor_b32 s38, s35, 2048\n" \
  "s_add_u32 s30, s58, s38\n" \
  "s_addc_u32 s31, s59, 0\n" \
  "s_xor_b32 s38, s35, 3072\n" \
  "s_add_u32 s32, s58, s38\n" \
  "s_addc_u32 s33, s59, 0\n" \
  "s_sub_u32 s71, s71, 1\n" \
  "s_mov_b32 s87, 0x40000\n" \
  "s_branch .LCV_CHK_%=\n" \
  ".LCV_POLL_%=:\n" \
  "buffer_load_dwordx4 v[24:27], v44, s[40:43], s89 offen sc1\n" \
  "buffer_load_dwordx4 v[28:31], v44, s[40:43], s89 offen offset:2048 sc1\n" \
  ".LCV_CHK_%=:\n" \
  "s_sub_u32 s87, s87, 1\n" \
  "s_cbranch_scc1 .LCV_END_%=\n" \
  "s_waitcnt vmcnt(1)\n" \
  "v_cvt_f32_i32 v32, v24\n" \
  "v_cvt_f32_i32 v33, v25\n" \
  "v_cvt_f32_i32 v34, v26\n" \
  "v_cvt_f32_i32 v35, v27\n" \
  "v_pk_mul_f32 v[24:25], v[32:33], s[80:81] op_sel_hi:[1,0]\n" \
  "v_pk_mul_f32 v[26:27], v[34:35], s[80:81] op_sel_hi:[1,0]\n" \
  "v_max3_f32 v45, |v32|, |v33|, |v34|\n" \
  "v_cvt_pk_bf16_f32 v24, v24, v26\n" \
  "v_cvt_pk_bf16_f32 v25, v25, v27\n" \
  "ds_write2_b32 v40, v24, v25 offset0:0 offset1:4\n" \
  "v_max3_f32 v45, |v45|, |v35|, |v35|\n" \
  "s_waitcnt vmcnt(0)\n" \
  "v_cvt_f32_i32 v32, v28\n" \
  "v_cvt_f32_i32 v33, v29\n" \
  "v_cvt_f32_i32 v34, v30\n" \
  "v_cvt_f32_i32 v35, v31\n" \
  "v_pk_mul_f32 v[28:29], v[32:33], s[80:81] op_sel_hi:[1,0]\n" \
  "v_pk_mul_f32 v[30:31], v[34:35], s[80:81] op_sel_hi:[1,0]\n" \
  "v_max3_f32 v45, |v45|, |v32|, |v33|\n" \
  "v_cvt_pk_bf16_f32 v28, v28, v30\n" \
  "v_cvt_pk_bf16_f32 v29, v29, v31\n" \
  "ds_write2_b32 v40, v28, v29 offset0:128 offset1:132\n" \
  "v_max3_f32 v45, |v45|, |v34|, |v35|\n" \
  "v_cmp_lt_f32 vcc, s91, v45\n" \
  "s_cbranch_vccnz .LCV_BIG_%=\n" \
  ".LCV_SML_%=:\n" \
  "s_branch .LCV_TOP_%=\n" \
  ".LCV_BIG_%=:\n" \
  "v_cmp_lt_f32 vcc, 0x4ca00000, v45\n" \
  "s_cbranch_vccnz .LCV_POLL_%=\n" \
  "v_mul_f32 v45, s80, v45\n" \
  "v_mul_f32 v45, v45, v45\n" \
  "s_add_u32 s88, s74, 157760\n" \
  "v_mov_b32 v44, s88\n" \
  "s_nop 0\n" \
  "v_cmpx_lt_f32 vcc, 2.0, v45\n" \
  "ds_max_u32 v44, v45\n" \
  "s_mov_b64 exec, -1\n" \
  "s_branch .LCV_SML_%=\n" \
  ".LCV_LAST_%=:\n" \
  "s_waitcnt vmcnt(0)\n" \
  ".LCV_END_%=:\n" \
  "s_endpgm\n"
__device__ __forceinline__ v4u mk_srd(const void* p, const unsigned bytes) {
  const unsigned long long a = (unsigned long long)p;
  return v4u{(unsigned)a, (unsigned)(a >> 32) & 0xffffu, bytes, 0x00020000u};
}
template <bool ATT>
__device__ __forceinline__ void role_chain_t(const MegaArgs& A, char* smem, const int L, const int h, const int rep) {
  __builtin_amdgcn_s_waitcnt(0x0F70);
  u16* swo = reinterpret_cast<u16*>(smem);
  const int w = __builtin_amdgcn_readfirstlane(threadIdx.x >> 6);
  const int wc = (w + ROT) & 7, psw = PSWH;
  char* ws = A.ws;
  unsigned* FL = (unsigned*)(ws + OFF_FL);
  unsigned* TMO = FL + FL_TMO;
#define LANE_IDS()                         \
  const int l = lane_id();                 \
  const int c = l & 15, g = l >> 4;        \
  (void)c; (void)g;
  float* sgm = reinterpret_cast<float*>(smem + C_X);
  float* sgf = sgm + 8;
  unsigned* FQ = FL + FL_FQ + L * 64 * 32 * 32;
  v8u w0[8], w1[8], w2[8];
  {
    LANE_IDS();
    v4u m0r;
    const unsigned sb = lds_base(smem);
    const int q = (l >> 2) & 3, p = l & 3;
    const int f = (q & 1) | ((g & 1) << 1) | ((q >> 1) << 2);
    const unsigned ra = sb + (16 * (g >> 1) + 4 * (g & 1) + (q & 1) + 8 * (q >> 1)) * 256 + (w ^ f) * 32 + p * 8;
#define W_READ(W, hf, X)                                                       \
  {                                                                           \
    _Pragma("unroll") for (int t = 0; t < 4; ++t) {                           \
      const unsigned a_ = (ra ^ (X)) + (hf) * 8192 + t * 32768;               \
      const v2u r0 = trd(a_), r1 = trd(a_ + 512), r2 = trd(a_ + 16384), r3 = trd(a_ + 16384 + 512); \
      W[4 * (hf) + t] = v8u{r0.x, r0.y, r1.x, r1.y, r2.x, r2.y, r3.x, r3.y};  \
    }                                                                         \
    _Pragma("unroll") for (int t = 0; t < 4; ++t) asm volatile("" : "+v"(W[4 * (hf) + t])); \
  }
#define W_STAGE(M, hf, VM, NEXT)                                               \
  {                                                                           \
    WAIT_VM(VM);                                                              \
    RAW_BAR();                                                                \
    if ((M) == 0 && !ATT) { W_READ(w0, hf, 0); W_READ(w2, hf, 128); }         \
    if ((M) == 1 && !ATT) { W_READ(w1, hf, 0); }                              \
    if ((M) == 1 && ATT) { W_READ(w0, hf, 0); }                               \
    if ((M) == 2 && ATT) { W_READ(w1, hf, 0); W_READ(w2, hf, 128); }          \
    RAW_BAR();                                                                \
    NEXT;                                                                     \
  }
#define WO_ISSUE(hf)                                                          \
  {                                                                           \
    const char* base_ = (const char*)A.Wo + (size_t)(h * DH + 64 * (hf) + 8 * w) * (DM * 2); \
    _Pragma("unroll") for (int i = 0; i < 8; ++i) {                           \
      const int fd = (i & 1) | (((i >> 2) & 1) << 1) | ((w & 1) << 2);        \
      glds16(base_, (unsigned)(i * 1024 + ((l ^ (fd << 1)) << 4)), sb + w * 16384 + (hf) * 8192 + i * 1024); \
    }                                                                         \
  }
    wqkv_issue<true>(A.Wv, 0, h, w, l, sb);
    wqkv_issue<true>(A.Wv, 1, h, w, l, sb);
    asm volatile("s_nop 4\n\tglobal_load_dwordx4 %0, %1, %2" : "=v"(m0r) : "v"(((2 * (c >> 2) + (c & 1)) * DM + 64 * w + 16 * g + 8 * (((c >> 1) ^ (c >> 3)) & 1)) * 2), "s"(A.mem0) : "memory");
    W_STAGE(2, 0, 9, wqkv_issue<true>(A.Wk, 0, h, w, l, sb));
    SST(4);
    W_STAGE(2, 1, 8, wqkv_issue<true>(A.Wk, 1, h, w, l, sb));
    SST(5);
    asm volatile("" : "+v"(m0r));
    *reinterpret_cast<v4u*>(smem + C_SM + w * 1024 + l * 16) = m0r;
    {
      float f2[4] = {};
      if (ATT) {
#pragma unroll
        for (int t = 0; t < 8; ++t)
#pragma unroll
          for (int e = 0; e < 8; ++e) {
            f2[e & 1] = dot2sq(w1[t][e], f2[e & 1]);
            f2[2 + (e & 1)] = dot2sq(w2[t][e], f2[2 + (e & 1)]);
          }
      }
      const float f = wave_sum_dpp((f2[0] + f2[1]) + (f2[2] + f2[3]));
      if (l == 0) { sgf[w] = f; sgm[16] = 0.f; sgm[32 + w] = 0.f; }
    }
    v4u zt;
    v2u ze;
#define Z_LOAD()                                                                                                        \
  asm volatile("global_load_dwordx4 %0, %2, %3 sc1\n\tglobal_load_dwordx2 %1, %4, %5 sc1"                              \
               : "=&v"(zt), "=&v"(ze)                                                                                   \
               : "v"(l * 16), "s"(ws + OFF_TAG + (blockIdx.x & 7) * 1024), "v"(0), "s"(ws + OFF_TAG + 8192)              \
               : "memory")
    W_STAGE(1, 0, 8, Z_LOAD(); wqkv_issue<true>(A.Wq, 0, h, w, l, sb));
#undef Z_LOAD
    W_STAGE(1, 1, 8, wqkv_issue<true>(A.Wq, 1, h, w, l, sb));
    asm volatile("" : "+v"(zt), "+v"(ze));
    if (w == 0) {
      if (!zero_seen(zt, ze)) zero_wait(ws);
      const float f2 = (sgf[0] + sgf[1]) + (sgf[2] + sgf[3]);
      __builtin_amdgcn_raw_buffer_store_b32(__float_as_uint(f2) | 1u, MKRS(FL + FL_G, L ? 0 : 4 * 512), l * 512 + h * 4, 0, AUX_SC1);
    }
    SST(6);
    W_STAGE(0, 0, 8, WO_ISSUE(0));
    W_STAGE(0, 1, 8, WO_ISSUE(1));
    SST(7);
#undef W_STAGE
#undef W_READ
#undef WO_ISSUE
    const unsigned rb = sb + (2 * (g >> 1) + (q >> 1)) * 16384 + (4 * (g & 1) + (q & 1)) * 1024 + p * 8;
#pragma unroll
    for (int hf = 0; hf < 2; ++hf) {
      if (hf == 0) WAIT_VM(8); else WAIT_VM(0);
      RAW_BAR();
      v4u fr[8];
#pragma unroll
      for (int i = 0; i < 8; ++i) {
        const unsigned a_ = rb + hf * 8192 + (i & 1) * 65536 + ((4 * wc + ((i >> 1) ^ psw)) ^ f) * 32;
        const v2u r0 = trd(a_), r1 = trd(a_ + 2048);
        fr[i] = v4u{r0.x, r0.y, r1.x, r1.y};
      }
#pragma unroll
      for (int i = 0; i < 8; ++i) asm volatile("" : "+v"(fr[i]));
      RAW_BAR();
#pragma unroll
      for (int i = 0; i < 8; ++i) *reinterpret_cast<v4u*>(swo + w * 8192 + hf * 4096 + i * 512 + l * 8) = fr[i];
    }
  }
  WSTAMP2();
  const unsigned sb = lds_base(smem);
  const char* gK = ws + OFF_QK + L * SZ_QK + ((size_t)h * 16 + 2 * w) * 1024;
  const char* gV = ws + OFF_VF + L * SZ_O + ((size_t)h * 8 + 2 * w - 8) * 1024;
  float gw2;
  {
    const gu32* gp = (gu32*)(FL + FL_G + REP * 128) + (lane_id() & 31);
    unsigned g0, g1, g2;
    for (unsigned spins = 0;; ++spins) {
      g0 = __hip_atomic_load(gp, RLX_AGENT);
      g1 = __hip_atomic_load(gp + 32, RLX_AGENT);
      g2 = __hip_atomic_load(gp + 64, RLX_AGENT);
      if (__all(g0 != 0u && g1 != 0u && g2 != 0u)) break;
      if (spin_timeout(spins, TMO)) return;
      __builtin_amdgcn_s_sleep(4);
    }
    const float gm = wave_max_dpp(__uint_as_float(g0) * (__uint_as_float(g1) + __uint_as_float(g2)));
    asm volatile("s_nop 0\n\tv_readfirstlane_b32 %0, %1" : "=s"(gw2) : "v"(gm * 1.07f));
  }
  v8u pv;
  {
    LANE_IDS();
    const unsigned voff = g * 256 + c * 8, wp = sb + w * 16384 + l * 16;
    pv[0] = (((c >> 1) ^ (c >> 3)) & 1) ? 0xEEEE : 0x4444;
    pv[1] = sb + C_SM + l * 16;
    pv[2] = ATT ? sb + ((c & 2) ? C_SK : C_SQ) + ((2 * (c >> 2) + (c & 1)) * QS + 8 * g) * 2 : voff;
    pv[3] = ATT ? (g & 1) * 1024 + c * 16 + (g >> 1) * 8 : wp;
    pv[4] = sb + ((c & 2) ? C_SQ : C_SO) + w * 256 + ((c & 13) + ((g ^ (c >> 2)) & 2)) * 16 + 8 * ((g ^ (c >> 1)) & 1);
    pv[5] = voff;
    pv[6] = wp;
    pv[7] = sb + C_SO + (l >> 5) * 1024 + w * 256 + (l & 31) * 8;
  }
  if (!ATT) {
    if (!wait_n<4, 1>(FQ + h * 32, 1, TMO)) return;
    const unsigned lo = lane_id() * 16, d = sb + C_KT + (2 * w - 8) * 1024;
    glds16c(gK, lo, d);
    glds16c(gK + 1024, lo, d + 1024);
    glds16c(gV, lo, d + 8192);
    glds16c(gV + 1024, lo, d + 9216);
  }
  DRAIN();
  SST(8);
  const v4u rsA = mk_srd(ws + OFF_ACC + L * SZ_ACC, SZ_ACC);
  const v4u rsF = mk_srd(FQ, 64 * 32 * 32 * 4);
  const v4u rsMK = mk_srd(ws + OFF_MEMK + L * SZ_MEM, SZ_MEM);
  const v4u rsMV = mk_srd(ws + OFF_MEMV + L * SZ_MEM, SZ_MEM);
  const int wr = (w + RR) & 7;
  const char* pAB = ws + OFF_ACC + L * SZ_ACC + (wc - wr) * 4096;
  const char* pGK = gK + NH * 16 * 1024;
  const char* pGV = gV + NH * 8 * 1024;
  unsigned sc[8];
  sc[0] = ATT ? L * 1024 + (h & 3) * 256 : (32 + h) * 128;
  sc[1] = ATT ? 1032 - L * 1024 + h * 4096 : (h * 4 + w - 4) * 512;
  sc[2] = (h * DH + 16 * w) * MM * 2;
  sc[3] = NSEG - 1;
  sc[4] = __float_as_uint(gw2);
  sc[5] = w;
  sc[6] = sb;
  const char* dbg = ws;
  const unsigned tg = (now10() / GR) * GR;
  const v4u rsT = mk_srd(FL + FL_TK, 2048);
#define CHAIN_OPS                                                                                                          \
  ::"{v[64:71]}"(w0[0]), "{v[72:79]}"(w0[1]), "{v[80:87]}"(w0[2]), "{v[88:95]}"(w0[3]), "{v[96:103]}"(w0[4]),              \
      "{v[104:111]}"(w0[5]), "{v[112:119]}"(w0[6]), "{v[120:127]}"(w0[7]), "{v[128:135]}"(w1[0]), "{v[136:143]}"(w1[1]),    \
      "{v[144:151]}"(w1[2]), "{v[152:159]}"(w1[3]), "{v[160:167]}"(w1[4]), "{v[168:175]}"(w1[5]), "{v[176:183]}"(w1[6]),    \
      "{v[184:191]}"(w1[7]), "{v[192:199]}"(w2[0]), "{v[200:207]}"(w2[1]), "{v[208:215]}"(w2[2]), "{v[216:223]}"(w2[3]),    \
      "{v[224:231]}"(w2[4]), "{v[232:239]}"(w2[5]), "{v[240:247]}"(w2[6]), "{v[248:255]}"(w2[7]), "{v[0:7]}"(pv),          \
      "{s[40:43]}"(rsA), "{s[44:47]}"(rsF), "{s[48:51]}"(rsMK), "{s[52:55]}"(rsMV), "{s[56:57]}"(pAB), "{s60}"(wr * 4096), "{s61}"(h * 1024 - wr * 4096), \
      "{s[62:63]}"(pGK), "{s[64:65]}"(pGV), "{s68}"(sc[0]), "{s69}"(sc[1]), "{s70}"(sc[2]),    \
      "{s71}"(sc[3]), "{s72}"(sc[4]), "{s73}"(sc[5]), "{s74}"(sc[6]), "{s[36:37]}"(dbg),                   \
      "{s34}"(tg), "{s35}"(psw * 1024), [gp] "n"(GP), [gr] "n"(GR), [gm] "n"(GM), \
      "{s[16:19]}"(rsT), "{s20}"(0), "{s22}"(sb + C_X + 128 + 8 * (w & 3)), "{s24}"(L * 1024), [cd] "n"(CD), \
      "{s25}"(APA - (APN + 1) * APP + L * (APB - APA + (2 * APN + 1) * APP)), [ka] "n"(APL), [an] "n"(APN), [d0] "n"(16 * CD0), [rq] "n"(RQM), [pp] "n"(APP) \
      : "memory", "vcc", "scc", "s99", "v63"
  if (ATT) asm volatile("s_mov_b64 exec, -1\ns_nop 4\n" CHAIN_ASM_ATT CHAIN_OPS);
  else asm volatile("s_mov_b64 exec, -1\ns_nop 4\n" CHAIN_ASM_VO CHAIN_OPS);
#undef CHAIN_OPS
#undef LANE_IDS
}

__device__ __forceinline__ void role_chain(const MegaArgs& A, char* smem, const int L, const int h, const int rep) {
  if (__builtin_amdgcn_readfirstlane(threadIdx.x >> 6) < 4) role_chain_t<true>(A, smem, L, h, rep);
  else role_chain_t<false>(A, smem, L, h, rep);
}

constexpr int AW_TK = 0, AW_SO = 98304, AW_M = AW_SO + 32768, AW_CNT = AW_M + 8192;
typedef __attribute__((address_space(3))) unsigned lu32;
__device__ __forceinline__ unsigned umin3(unsigned a, unsigned b, unsigned c) {
  return __builtin_elementwise_min(__builtin_elementwise_min(a, b), c);
}
__device__ __forceinline__ bool all_set(const unsigned r) {
  unsigned long long m;
  asm volatile("v_cmp_eq_u32 %0, 0, %1" : "=s"(m) : "v"(r) : "memory");
  return m == 0ull;
}
__device__ __forceinline__ unsigned long long zero_mask(const unsigned r) {
  unsigned long long m;
  asm volatile("v_cmp_eq_u32 %0, 0, %1" : "=s"(m) : "v"(r) : "memory");
  return m;
}
__device__ __forceinline__ int job_seg(const int n) {
  const int m = n - AWD;
  const int r = (m & 1) ? 64 + (m >> 1) : AWD + (m >> 1);
  return __builtin_amdgcn_readfirstlane(n > 127 ? 128 : (n < AWD || n >= 128 - AWD) ? n : r);
}
template <bool AT>
__device__ __forceinline__ void role_aw_t(const MegaArgs& A, char* smem, const int mode, const int a) {
  __builtin_amdgcn_s_waitcnt(0x0F70);
  const int w = __builtin_amdgcn_readfirstlane(threadIdx.x >> 6);
  const int l = threadIdx.x & 63, c = l & 15, g = l >> 4;
  const int qt = w & 1, hh = (w >> 1) & 1, h = 2 * a + hh;
  char* ws = A.ws;
  unsigned* FL = (unsigned*)(ws + OFF_FL);
  unsigned* TMO = FL + FL_TMO;
  const unsigned sb = lds_base(smem);
  constexpr int NP = AT ? 1 : 3;
  const int cp0 = AT ? w : 3 * w - 8;
  v4u wo[2 * NP][8];
  lu32* cnt = (lu32*)(unsigned long)(sb + AW_CNT);
  if (threadIdx.x < 16) cnt[threadIdx.x] = 0u;
  const __amdgpu_buffer_rsrc_t rsW = MKRS(ws + OFF_PW, SZ_PW);
  const unsigned* FQ = FL + FL_FQ + h * 32;
  const v4u rsF = mk_srd(FQ, (128 * 32 - h) * 128);
  const char* gQ = ws + OFF_QK + ((size_t)h * 16 + 8 * qt) * 1024;
  const char* gV = ws + OFF_VF + ((size_t)h * 8 + 4 * qt) * 1024;
  const char* gM = ws + (qt ? OFF_MEMV : OFF_MEMK) + h * 2048;
  const int NJ = (130 - mode) / 3;
#define AWS(gs) (64 + a + ((gs) >> 6) * 16)
  const int mo = AW_M + hh * 4096;
  const unsigned tq0 = __builtin_amdgcn_readfirstlane(sb + AW_TK + (hh * 24 + 8 * qt) * 1024);
  const unsigned tv0 = __builtin_amdgcn_readfirstlane(sb + AW_TK + (hh * 24 + 16 + 4 * qt) * 1024);
  const float scale = 0.08838834764831845f;
  volatile lu32* cM = cnt + 2 + hh;
#define DMA4(x) "global_load_lds_dwordx4 %1, " x " sc1\n\tglobal_load_lds_dwordx4 %1, " x " offset:1024 sc1\n\t"       \
                "global_load_lds_dwordx4 %1, " x " offset:2048 sc1\n\tglobal_load_lds_dwordx4 %1, " x " offset:3072 sc1\n\t"
#define T_DMA(jj, gg)                                                                                                   \
  {                                                                                                                     \
    const unsigned o_ = ((jj) & 1) ? 49152u : 0u;                                                                       \
    const char* q_ = gQ + (size_t)(gg) * (NH * 16 * 1024);                                                              \
    unsigned keep_;                                                                                                     \
    asm volatile("s_nop 4\n\ts_mov_b32 %0, m0\n\ts_mov_b32 m0, %5\n\ts_nop 0\n\t" DMA4("%2")                           \
                 "s_mov_b32 m0, %6\n\ts_nop 0\n\t" DMA4("%3") "s_mov_b32 m0, %7\n\ts_nop 0\n\t" DMA4("%4") "s_mov_b32 m0, %0" \
                 : "=&s"(keep_)                                                                                         \
                 : "v"(l * 16), "s"(q_), "s"(q_ + 4096), "s"(gV + (size_t)(gg) * (NH * 8 * 1024)), "s"(tq0 + o_),        \
                   "s"(tq0 + o_ + 4096u), "s"(tv0 + o_)                                                                 \
                 : "memory");                                                                                           \
  }
#define T_LAND(jj) { if (l == 0) *(volatile lu32*)(cnt + w) = (jj) + 1; }
#define M_DMA(gg)                                                                                                       \
  {                                                                                                                     \
    unsigned keep_;                                                                                                     \
    asm volatile("s_nop 4\n\ts_mov_b32 %0, m0\n\ts_mov_b32 m0, %3\n\ts_nop 0\n\tglobal_load_lds_dwordx4 %1, %2 sc1\n\t"     \
                 "global_load_lds_dwordx4 %1, %2 offset:1024 sc1\n\ts_mov_b32 m0, %0"                                    \
                 : "=&s"(keep_)                                                                                         \
                 : "v"(l * 16), "s"(gM + (size_t)(gg) * (NH * 2048)), "s"(sb + mo + qt * 2048)                           \
                 : "memory");                                                                                           \
  }
#define M_TEST()                                                                                                        \
  {                                                                                                                     \
    const v4u t_ = *reinterpret_cast<const v4u*>(smem + mo + qt * 2048 + l * 16);                                        \
    const v4u u_ = *reinterpret_cast<const v4u*>(smem + mo + qt * 2048 + 1024 + l * 16);                                 \
    ok = __all(umin3(umin3(t_.x, t_.y, t_.z), umin3(u_.x, u_.y, u_.z), umin3(t_.w, u_.w, 1u)) != 0u);                   \
    if (ok) asm volatile("s_mov_b64 exec, 1\n\tds_add_u32 %0, %1\n\ts_mov_b64 exec, -1" ::"v"(sb + AW_CNT + 8 + 4 * hh), "v"(1u) : "memory"); \
  }
#define F_LOAD(r, gg) \
  asm volatile("s_nop 4\n\tbuffer_load_dword %0, %1, %2, %3 offen sc1" : "=v"(r) : "v"(0), "s"(rsF), "s"((gg) * 4096) : "memory")
#define F_TEST(r) all_set(r)
#define W_FLAG(jj, gg)                                                                                                  \
  {                                                                                                                     \
    unsigned old_;                                                                                                      \
    asm volatile("s_mov_b64 exec, 1\n\tds_add_rtn_u32 %0, %1, %2\n\ts_mov_b64 exec, -1\n\ts_waitcnt lgkmcnt(0)"           \
                 : "=&v"(old_) : "v"(sb + AW_CNT + ((jj) & 1) * 36), "v"(1u) : "memory");                               \
    if ((__builtin_amdgcn_readfirstlane(old_) & 7u) == 7u) {                                                            \
      TSR(AWS(gg), 4, (gg) & 63, threadIdx.x & ~63u);                                                                   \
      if (l < 4) set_flag(FL + FL_FW + (((gg) * 4 + l) * 4 + (a >> 2)) * 32 + (a & 3));                              \
    }                                                                                                                   \
    pend = false;                                                                                                       \
  }
#define LDS_U(p) ((unsigned)__builtin_amdgcn_readfirstlane(*(p)))
#define TOK_MIN() __builtin_elementwise_min(LDS_U((volatile lu32*)(cnt + 4 + 2 * hh)), LDS_U((volatile lu32*)(cnt + 5 + 2 * hh)))
  int gp = 0, gs = job_seg(mode), g1 = job_seg(mode + 3);
  {
    const int q = (l >> 2) & 3, p = l & 3, f = q | ((g & 1) << 2);
    const unsigned ra = sb + (4 * g + q) * 256 + (p & 1) * 16;
    unsigned n0 = 0u;
    bool got = false;
#pragma unroll
    for (int st = 0; st < 4; ++st) {
      if (st == 3 && got) WAIT_VM(12); else if (st == 3) WAIT_VM(0); else WAIT_VM(8);
      RAW_BAR();
#pragma unroll
      for (int k2 = 0; k2 < 2; ++k2) {
        const int ks = 4 * (st >> 1) + 2 * k2 + (st & 1);
#pragma unroll
        for (int ct = 0; ct < 2 * NP; ++ct) {
          const int cp = cp0 + (ct >> 1);
          const unsigned a_ = ra + (st & 1) * 65536 + (cp >> 2) * 16384 + k2 * 8192 + ((2 * (cp & 3) + (p >> 1)) ^ f) * 32 + (ct & 1) * 8;
          const v2u lo = trd(a_), hi = trd(a_ + 4096);
          wo[ct][ks] = v4u{lo.x, lo.y, hi.x, hi.y};
        }
#pragma unroll
        for (int ct = 0; ct < 2 * NP; ++ct) asm volatile("" : "+v"(wo[ct][ks]));
      }
      RAW_BAR();
      if (!AT && st == 1) F_LOAD(n0, gs);
      if (st < 2) wo_issue(A.Wo, a, st + 2, sb);
      if (!AT && st == 2 && F_TEST(n0)) {
        T_DMA(0, gs);
        got = true;
      }
    }
    if (!AT) {
      if (!got) {
        (void)wait_n<SLP_A, 1>(FQ + gs * 1024, 1, TMO);
        T_DMA(0, gs);
      }
      WAIT_VM(0);
      T_LAND(0);
    }
  }
  __builtin_amdgcn_s_waitcnt(0x0F70);
  bool pend = false, asked = false;
  unsigned tb1 = now10() - 130 * GPER, tb2 = tb1;
  FSD;
  for (int s = 0; s < NJ; ++s) {
    char* so = smem + AW_SO + (s & 1) * 16384 + l * 16;
    bool got = g1 == 128, land = got, seen = false;
    if (AT) {
      if (TOK_MIN() < (unsigned)(s + 1)) {
        if (pend) {
          WAIT_VM(0);
          W_FLAG(s - 1, gp);
        }
        for (unsigned spins = 0; TOK_MIN() < (unsigned)(s + 1) && spins < 64u * SPIN_CAP; ++spins) __builtin_amdgcn_s_sleep(1);
      }
      asm volatile("" ::: "memory");
      TSR(AWS(gs), 0, gs & 63, 0);
      FS(0);
      const char* tk = smem + AW_TK + ((s & 1) * 2 + hh) * 24 * 1024 + l * 16;
#define TKF(i) (*reinterpret_cast<const bf16x8*>(tk + (i) * 1024))
      bf16x8 q[4], vt[8];
      float x[12], mx;
      bool ok = false;
      {
        f32x4 s0 = {}, s1 = {};
        bf16x8 kd[4], k0[4];
#pragma unroll
        for (int ks = 0; ks < 4; ++ks) {
          q[ks] = TKF(4 * qt + ks);
          kd[ks] = TKF(8 + 4 * qt + ks);
          k0[ks] = TKF(8 + ks);
        }
#pragma unroll
        for (int i = 0; i < 8; ++i) vt[i] = TKF(16 + i);
        __builtin_amdgcn_sched_barrier(0);
#pragma unroll
        for (int ks = 0; ks < 4; ++ks) {
          s0 = MFMA16(kd[ks], q[ks], s0);
          s1 = MFMA16(k0[ks], q[ks], s1);
        }
        scale_pair(s0[0], s0[1], scale, x[0], x[1]);
        scale_pair(s0[2], s0[3], scale, x[2], x[3]);
        scale_pair(s1[0], s1[1], scale, x[4], x[5]);
        scale_pair(s1[2], s1[3], scale, x[6], x[7]);
#pragma unroll
        for (int i = 0; i < 4; ++i) {
          if (4 * g + i > c) x[i] = -3e38f;
          if (!qt) x[4 + i] = -3e38f;
        }
        mx = fmaxf(fmaxf(fmaxf(x[0], x[1]), fmaxf(x[2], x[3])), fmaxf(fmaxf(x[4], x[5]), fmaxf(x[6], x[7])));
        asm volatile("" : "+v"(mx), "+v"(vt[0]), "+v"(vt[1]), "+v"(vt[2]), "+v"(vt[3]), "+v"(vt[4]), "+v"(vt[5]), "+v"(vt[6]), "+v"(vt[7]));
      }
#undef TKF
      FS(1);
      if (asked) {
        WAIT_VM(1);
        M_TEST();
      }
      FS(2);
      if (!ok) {
        if (pend) {
          WAIT_VM(0);
          W_FLAG(s - 1, gp);
        }
        unsigned tb = gs < 64 ? tb1 : tb2, spins = 0;
        gate(tb + gs * GPER);
        for (; !ok; ++spins) {
          if (spin_timeout(spins, TMO)) break;
          if (spins) __builtin_amdgcn_s_sleep(SLP_M);
          M_DMA(gs);
          WAIT_VM(0);
          M_TEST();
        }
        tb = spins > 1 ? now10() - gs * GPER - GMA : tb - GSTEP;
        if (gs < 64) tb1 = tb; else tb2 = tb;
      }
      FS(3);
      __builtin_amdgcn_s_setprio(PRI_A);
      asked = false;
      for (unsigned spins = 0; LDS_U(cM) < 2u * (s + 1) && spins < 64u * SPIN_CAP; ++spins) __builtin_amdgcn_s_sleep(1);
      v4u km[4];
      v2u vm[8];
#pragma unroll
      for (int ks = 0; ks < 4; ++ks) km[ks] = *reinterpret_cast<const v4u*>(smem + mo + (ks * 32 + g * 8 + (c & 7)) * 16);
#pragma unroll
      for (int i = 0; i < 8; ++i) vm[i] = *reinterpret_cast<const v2u*>(smem + mo + 2048 + ((16 * i + c) * MM + 4 * (g & 1)) * 2);
      TSR(AWS(gs), 1, gs & 63, 0);
      FS(4);
      f32x4 m0 = {};
#pragma unroll
      for (int ks = 0; ks < 4; ++ks) m0 = MFMA16(BF8(km[ks]), q[ks], m0);
      scale_pair(m0[0], m0[1], scale, x[8], x[9]);
      scale_pair(m0[2], m0[3], scale, x[10], x[11]);
#pragma unroll
      for (int i = 0; i < 4; ++i) {
        if (g >= 2) x[8 + i] = -3e38f;
        mx = fmaxf(mx, x[8 + i]);
      }
      mx = xmax32(xmax16(mx));
      float sum = 0.f;
#pragma unroll
      for (int i = 0; i < 12; ++i) {
        x[i] = __expf(x[i] - mx);
        sum += x[i];
      }
      sum = xsum32(xsum16(sum));
      const float inv = 1.0f / sum;
      const v2u pa = {pk2(x[0] * inv, x[1] * inv), pk2(x[2] * inv, x[3] * inv)};
      const v2u pb = {pk2(x[4] * inv, x[5] * inv), pk2(x[6] * inv, x[7] * inv)};
      const v4u p0 = qt ? v4u{pb.x, pb.y, pa.x, pa.y} : v4u{pa.x, pa.y, pb.x, pb.y};
      const v2u p1 = {pk2(x[8] * inv, x[9] * inv), pk2(x[10] * inv, x[11] * inv)};
      FS(5);
#pragma unroll
      for (int k = 0; k < 4; ++k) {
        v4u of;
#pragma unroll
        for (int hf = 0; hf < 2; ++hf) {
          f32x4 ao = {};
          ao = MFMA16(vt[2 * k + hf], BF8(p0), ao);
          ao = MFMA16K(vm[2 * k + hf], p1, ao);
          of[2 * hf] = pk2(ao[0], ao[1]);
          of[2 * hf + 1] = pk2(ao[2], ao[3]);
        }
        *reinterpret_cast<v4u*>(so + ((hh * 2 + qt) * 4 + k) * 1024) = of;
      }
      FS(6);
      seen = g1 >= 64 && g1 < 128 && TOK_MIN() >= (unsigned)(s + 2);
      if (pend) {
        WAIT_VM(0);
        W_FLAG(s - 1, gp);
      }
    } else {
      FS(0);
      unsigned n;
      F_LOAD(n, g1);
      if (pend) {
        WAIT_VM(1);
        W_FLAG(s - 1, gp);
      }
      WAIT_VM(0);
      FS(1);
      if (F_TEST(n)) {
        T_DMA(s + 1, g1);
        got = true;
        if (LDS_U(cM) < 2u * (s + 1)) {
          WAIT_VM(0);
          T_LAND(s + 1);
          land = true;
        }
      }
    }
    FS(7);
    RAW_BAR();
    FS(8);
    __builtin_amdgcn_s_setprio(AT ? PRI_WA : PRI_WB);
    TSR(AWS(gs), 2, gs & 63, 256);
    const int voff = (c >> 1) * 2048 + (c & 1) * 64 + g * 16, soff = (gs * 16 + a) * 32768 + cp0 * 128;
#define OFRAG(i) (*reinterpret_cast<const bf16x8*>(so + (((((i) & 7) >> 2) * 2 + ((i) >> 3)) * 4 + ((i) & 3)) * 1024))
    constexpr int NO = AT ? 16 : 3;
    bf16x8 o[NO];
#pragma unroll
    for (int i = 0; i < (AT ? 16 : 2); ++i) o[i] = OFRAG(i);
    unsigned pr;
    const bool look = seen && g1 >= 128 - AWD;
    if (AT) {
      asm volatile("s_cmp_eq_u32 %3, 0\n\ts_cbranch_scc1 1f\n\ts_nop 4\n\tglobal_load_dword %0, %1, %2 sc1\n1:"
                   : "=v"(pr) : "v"(2044), "s"(gM + (size_t)g1 * (NH * 2048)), "s"(__builtin_amdgcn_readfirstlane((int)look)) : "memory", "scc");
    } else {
      F_LOAD(pr, got ? 128 : g1);
    }
    __builtin_amdgcn_sched_barrier(0);
    FS(9);
#pragma unroll
    for (int rt = 0; rt < 2; ++rt) {
      f32x4 acc[2 * NP] = {};
#pragma unroll
      for (int kk = 0; kk < 8; ++kk) {
        const int i = 8 * rt + kk;
        if (!AT && i + 2 < 16) o[(i + 2) % NO] = OFRAG(i + 2);
#pragma unroll
        for (int ct = 0; ct < 2 * NP; ++ct) acc[ct] = MFMA16(BF8(wo[ct][kk]), o[i % NO], acc[ct]);
        if (!AT) __builtin_amdgcn_sched_barrier(0);
      }
      if (rt == 0) FS(10); else FS(12);
      if (AT && rt == 1 && seen) {
        if (look) {
          WAIT_VM(1);
          seen = F_TEST(pr);
        }
        if (seen) {
          M_DMA(g1);
          asked = true;
        }
      }
      if (!AT && rt == 0) {
        WAIT_VM(0);
        if (got && !land) {
          T_LAND(s + 1);
          land = true;
        }
        if (F_TEST(pr)) {
          T_DMA(s + 1, g1);
          got = true;
        }
      }
      if (!AT && rt == 1 && got && !land) {
        WAIT_VM(3);
        T_LAND(s + 1);
      }
#pragma unroll
      for (int u = 0; u < NP; ++u)
        ST128((v4u{pk2(acc[2 * u][0], acc[2 * u][1]), pk2(acc[2 * u][2], acc[2 * u][3]), pk2(acc[2 * u + 1][0], acc[2 * u + 1][1]),
                   pk2(acc[2 * u + 1][2], acc[2 * u + 1][3])}),
              rsW, voff + u * 128, soff + rt * 16384);
      if (rt == 0) FS(11); else FS(13);
    }
    __builtin_amdgcn_s_setprio(AT ? PRI_0 : 0);
    pend = true;
    FD(s);
    TSR(AWS(gs), 3, gs & 63, 256);
    TSR(AWS(gs), 5, gs & 63, 0);
    if (!AT && !got) {
      WAIT_VM(0);
      W_FLAG(s, gs);
      (void)wait_n<SLP_A, 1>(FQ + g1 * 1024, 1, TMO);
      T_DMA(s + 1, g1);
      WAIT_VM(0);
      T_LAND(s + 1);
    }
    gp = gs;
    gs = g1;
    g1 = job_seg(3 * s + 6 + mode);
  }
  WAIT_VM(0);
  W_FLAG(NJ - 1, gp);
#undef AWS
#undef OFRAG
#undef DMA4
#undef T_DMA
#undef T_LAND
#undef M_DMA
#undef M_TEST
#undef F_LOAD
#undef F_TEST
#undef W_FLAG
#undef LDS_U
#undef TOK_MIN
}
__device__ __forceinline__ void role_aw(const MegaArgs& A, char* smem, const int L, const int a) {
  if (__builtin_amdgcn_readfirstlane(threadIdx.x >> 6) < 4) role_aw_t<true>(A, smem, L, a);
  else role_aw_t<false>(A, smem, L, a);
}

__device__ __forceinline__ void role_red(const MegaArgs& A, char* smem, const int idx) {
  const int w = __builtin_amdgcn_readfirstlane(threadIdx.x >> 6);
  const int l = threadIdx.x & 63;
  const int gr = w >> 1, r = idx * 2 + (w & 1);
  char* ws = A.ws;
  unsigned* FL = (unsigned*)(ws + OFF_FL);
  unsigned* TMO = FL + FL_TMO;
  const __amdgpu_buffer_rsrc_t rsW = MKRS(ws + OFF_PW, SZ_PW);
  const __amdgpu_buffer_rsrc_t rsX = MKRS(ws + OFF_XA, OFF_X3 - OFF_XA + SZ_X);
  volatile unsigned* sw = reinterpret_cast<volatile unsigned*>(smem) + (w >> 1) * 32;
  if (threadIdx.x < 128) reinterpret_cast<volatile unsigned*>(smem)[threadIdx.x] = 0u;
  __syncthreads();
  unsigned tb1 = now10() - 130 * GPER, tb2 = tb1;
  const int rn1 = idx < 8 ? 96 + idx : 224 + idx;
  (void)rn1;
  if (w >= 6) return;
  if (w < 4) {
    const __amdgpu_buffer_rsrc_t rz = MKRS(ws + OFF_X2, SZ_X);
    for (int s = gr; s < NSEG; s += 2) ST128((v4u{0u, 0u, 0u, 0u}), rz, l * 16, (s * SEG + r) * DM * 2);
    DRAIN();
    if (l < 4) set_flag(FL + FL_FZ + (2 * l + (idx >> 3)) * 32 + (idx & 7) * 4 + w);
    if (l == 0) __hip_atomic_fetch_add((lu32*)(unsigned long)(lds_base(smem) + 480), 1u, __ATOMIC_RELAXED, __HIP_MEMORY_SCOPE_WORKGROUP);
  }
  for (unsigned spins = 0; reinterpret_cast<volatile unsigned*>(smem)[120] < 4u && spins < 64u * SPIN_CAP; ++spins) __builtin_amdgcn_s_sleep(2);
  for (int n = gr; n < 128; n += 3) {
    const int gs = job_seg(n), L = gs >> 6, s = gs & 63;
    const unsigned* fw = FL + FL_FW + (gs * 4 + REP) * 4 * 32;
    unsigned tb = L ? tb2 : tb1;
    tb = grp_wait<SLP_R, 4>((w & 1) == 0, sw, n + 1, fw, 16, TMO, tb + gs * GPER) ? now10() - gs * GPER - GMR : tb - GSTEP;
    if (n >= 125 - AWD) tb = now10() + GATE_RT - (n + 3) * GPER;
    if (L || n >= 125 - AWD) tb2 = tb; else tb1 = tb;
#define RTS(k, t) TSR(L ? A.rn : rn1, k, s, (w & 6) * 64 + (t))
    RTS(0, 0);
    RTS(4, 64);
    v4u p[16];
    const v4u xr = LD128(rsX, l * 512, L * (int)(OFF_X3 - OFF_XA) + s * 32768 + r * 16);
#pragma unroll
    for (int k = 0; k < 16; ++k)
      p[k] = LD128(rsW, (l >> 2) * 128 + (l & 3) * 16, (gs * 16 + k) * 32768 + (r >> 1) * 2048 + (r & 1) * 64);
    v4u xo;
    const unsigned nz = L ? 0u : 0x8000u;
#pragma unroll
    for (int e = 0; e < 4; ++e) {
      f32x2_t t[4];
#pragma unroll
      for (int k = 0; k < 4; ++k)
        t[k] = (f32x2_t{lo16(p[4 * k][e]), hi16(p[4 * k][e])} + f32x2_t{lo16(p[4 * k + 1][e]), hi16(p[4 * k + 1][e])}) +
               (f32x2_t{lo16(p[4 * k + 2][e]), hi16(p[4 * k + 2][e])} + f32x2_t{lo16(p[4 * k + 3][e]), hi16(p[4 * k + 3][e])});
      const f32x2_t u = (t[0] + t[1]) + (t[2] + t[3]);
      const unsigned a0 = pk2(u.x, u.y);
      xo[e] = pk2(lo16(xr[e]) + lo16(a0), hi16(xr[e]) + hi16(a0));
      xo[e] = xo[e] ? xo[e] : nz;
    }
    ST128(xo, MKRS(L ? (char*)A.out : ws + OFF_X2, SZ_X), l * 16, (s * SEG + r) * DM * 2);
    RTS(1, 0);
    RTS(5, 64);
    if (L == 0) {
      const unsigned st = row_stats(xo);
      if (l < 4) __hip_atomic_store((gu32*)(FL + FL_FR + ((s * 4 + l) * 4 + (r >> 3)) * 32 + (r & 7)), st, RLX_AGENT);
    }
    RTS(6, 64);
    RTS(3, 0);
#undef RTS
  }
  if (idx == 0 && threadIdx.x == 128) {
    gu64* en = (gu64*)(ws + OFF_TAG + 8192);
    __hip_atomic_store(en, __hip_atomic_load(en, RLX_AGENT) + TAG_STEP, RLX_AGENT);
  }
}

constexpr int CUN = 2144;
__device__ __forceinline__ bf16x8 conv_bfrag(const unsigned* up, const unsigned sh) {
  const unsigned d0 = up[0], d1 = up[1], d2 = up[2], d3 = up[3], d4 = up[4];
  const v4u r = {__builtin_amdgcn_alignbit(d1, d0, sh), __builtin_amdgcn_alignbit(d2, d1, sh),
                 __builtin_amdgcn_alignbit(d3, d2, sh), __builtin_amdgcn_alignbit(d4, d3, sh)};
  return BF8(r);
}
__device__ __forceinline__ void role_conv(const MegaArgs& A, char* smem, const int cv) {
  __builtin_amdgcn_s_waitcnt(0x0F70);
  u16* Kr = reinterpret_cast<u16*>(smem);
  u16* Uc = Kr + 8 * 2 * CKN;
  u16* Xr = Uc + 8 * CUN;
  u16* To = Uc + 8 * 2 * CUN;
  u16* Su = To + 32 * 8;
  u16* Sx = Su + 32 * 8;
  const int tid = threadIdx.x;
  const int w = __builtin_amdgcn_readfirstlane(tid >> 6);
  const int l = tid & 63, c = l & 15, g = l >> 4;
  const int d = 8 * cv + w;
  char* ws = A.ws;
  unsigned* FL = (unsigned*)(ws + OFF_FL);
  unsigned* TMO = FL + FL_TMO;
  u16* kr = Kr + w * 2 * CKN;
  float* prm = reinterpret_cast<float*>(kr);
  v4u tx[4];
  const v2u en = LD64(MKRS(ws + OFF_TAG, 8192 + 8), 0, 8192);
  {
    const unsigned pa = *reinterpret_cast<const unsigned*>(A.logAre + d * NST + 2 * l);
    const unsigned pb = *reinterpret_cast<const unsigned*>(A.Aim + d * NST + 2 * l);
    const unsigned pc = *reinterpret_cast<const unsigned*>(A.Cre + d * NST + 2 * l);
    const unsigned pd = *reinterpret_cast<const unsigned*>(A.Cim + d * NST + 2 * l);
    const float dt = bfr(expf(bf2f(A.logdt[d])));
    if (w < 2) tx[0] = *reinterpret_cast<const v4u*>(A.x0 + (size_t)tid * DM + 8 * cv);
    if (l < 16) reinterpret_cast<unsigned*>(Uc + w * CUN)[l] = 0u;
    if (l < 32) reinterpret_cast<unsigned*>(Uc + w * CUN + 32 + L_SEQ)[l] = 0u;
    if (l < 32) reinterpret_cast<unsigned*>(Uc + w * CUN + 32 + 128)[l] = 0u;
    if (l < 50) {
      reinterpret_cast<v4u*>(kr + 1808)[l] = v4u{0u, 0u, 0u, 0u};
      reinterpret_cast<v4u*>(kr + CKN + 1808)[l] = v4u{0u, 0u, 0u, 0u};
    }
#pragma unroll
    for (int e = 0; e < 2; ++e) {
      const float ar = -bfr(expf(e ? hi16(pa) : lo16(pa))) * dt, ai = (e ? hi16(pb) : lo16(pb)) * dt;
      const float ex = expf(ar);
      float sn, cs;
      sincosf(ai, &sn, &cs);
      prm[2 * l + e] = ar;
      prm[NST + 2 * l + e] = ai;
      prm[2 * NST + 2 * l + e] = (e ? hi16(pc) : lo16(pc)) * dt;
      prm[3 * NST + 2 * l + e] = (e ? hi16(pd) : lo16(pd)) * dt;
      prm[4 * NST + 2 * l + e] = ex * cs;
      prm[5 * NST + 2 * l + e] = ex * sn;
    }
  }
  const float dsk = bf2f(A.Dskip[d]);
  const u16* ka = kr + (c >> 3) * CKN + 2080 - 16 * (c & 7) + 8 * g;
  const unsigned ush = (c & 1) * 16;
#define KFRAG(D0) (*reinterpret_cast<const bf16x8*>(ka - (D0)))
  f32x4 acc[16], far[5];
  v4u xf[2];
  const unsigned* ub1 = reinterpret_cast<const unsigned*>(Uc + w * CUN + (c & ~1) + 8 * g + 32);
  const __amdgpu_buffer_rsrc_t rz1 = MKRS(ws + OFF_XA, SZ_X);
  const int t0 = 16 * (4 * (g & 1) + (g >> 1)) + c;
  {
    const u16* uc = Uc + w * CUN;
    const unsigned* ub = reinterpret_cast<const unsigned*>(uc + (c & ~1) + 8 * g + 32);
    const __amdgpu_buffer_rsrc_t rz = MKRS(ws + OFF_XA, SZ_X);
    const __amdgpu_buffer_rsrc_t rst = MKRS(ws + OFF_GT, L_SEQ * 4);
#define TILE_OUT(T)                                                                            \
  {                                                                                            \
    const float u0 = bf2f(uc[32 + 128 * (T) + t0]), u1 = bf2f(uc[64 + 128 * (T) + t0]);        \
    u16* xp = Xr + (128 * (T) + t0) * 8 + w;                                                   \
    const unsigned yy = pk2(y0, y1), uu = pk2(u0 * dsk, u1 * dsk);                             \
    const unsigned aa = pk2(lo16(yy) + lo16(uu), hi16(yy) + hi16(uu));                         \
    const unsigned oo = pk2(bf2f(xp[0]) + lo16(aa), bf2f(xp[256]) + hi16(aa));                 \
    xp[0] = (u16)oo;                                                                           \
    xp[256] = (u16)(oo >> 16);                                                                 \
  }
#define CHUNK_ST(ck)                                                                           \
  {                                                                                            \
    const v4u o = *reinterpret_cast<const v4u*>(Xr + (64 * (ck) + l) * 8);                     \
    ST128(o, rz, (l >> 5) * 32768 + (l & 31) * 16, (2 * (ck) * 64 + cv) * 512);                \
  }
#define U_ROW(row) U_ROW2(row, __builtin_amdgcn_raw_buffer_load_b32(rst, (row) * 4, 0, AUX_SC1))
#define U_ROW2(row, stv)                                                                       \
  {                                                                                            \
    const unsigned st_ = (stv);                                                                \
    const v4u xr_ = *reinterpret_cast<const v4u*>(Xr + (row) * 8);                             \
    const float mb_ = lo16(st_), ri_ = 1.0f / hi16(st_);                                       \
    _Pragma("unroll") for (int e = 0; e < 4; ++e) {                                            \
      const unsigned t_ = pk2(lo16(xr_[e]) - mb_, hi16(xr_[e]) - mb_);                         \
      const unsigned q_ = pk2(lo16(t_) * ri_, hi16(t_) * ri_);                                 \
      Uc[(2 * e) * CUN + 32 + (row)] = (u16)q_;                                                \
      Uc[(2 * e + 1) * CUN + 32 + (row)] = (u16)(q_ >> 16);                                    \
    }                                                                                          \
  }
    const unsigned* FS = FL + FL_FS + REP * 32 * 32;
    const int gi = 4 * (g * 8 + (l & 7));
    const int tl = 32 * ((l >> 3) & 1) + 8 * g + (l & 7);
    unsigned khl = 0u;
#pragma unroll 1
    for (int ps = 0; ps < 4; ++ps) {
      float v[32];
      {
        const f32x4 ar = *reinterpret_cast<const f32x4*>(prm + gi), ai = *reinterpret_cast<const f32x4*>(prm + NST + gi);
        const f32x4 cr = *reinterpret_cast<const f32x4*>(prm + 2 * NST + gi), ci = *reinterpret_cast<const f32x4*>(prm + 3 * NST + gi);
        const f32x4 wr = *reinterpret_cast<const f32x4*>(prm + 4 * NST + gi), wi = *reinterpret_cast<const f32x4*>(prm + 5 * NST + gi);
        const float p0 = (float)(64 * ps + 32 * ((l >> 3) & 1));
        float yr0[4], yi0[4];
#pragma unroll
        for (int k = 0; k < 4; ++k) {
          const float e = __expf(ar[k] * p0);
          float sn, cs;
          __sincosf(ai[k] * p0, &sn, &cs);
          const float zr = e * cs, zi = e * sn;
          yr0[k] = cr[k] * zr - ci[k] * zi;
          yi0[k] = cr[k] * zi + ci[k] * zr;
        }
        f32x2_t yra = {yr0[0], yr0[1]}, yia = {yi0[0], yi0[1]}, wra = {wr[0], wr[1]}, wia = {wi[0], wi[1]};
        f32x2_t yrb = {yr0[2], yr0[3]}, yib = {yi0[2], yi0[3]}, wrb = {wr[2], wr[3]}, wib = {wi[2], wi[3]};
#pragma unroll
        for (int i = 0; i < 32; ++i) {
          const f32x2_t sm2 = yra + yrb;
          v[i] = sm2.x + sm2.y;
          const f32x2_t ta = yra * wra - yia * wia, tb = yrb * wrb - yib * wib;
          yia = yra * wia + yia * wra;
          yib = yrb * wib + yib * wrb;
          yra = ta;
          yrb = tb;
        }
      }
#pragma unroll
      for (int i = 0; i < 16; ++i) v[i] = swap_add32(v[i], v[i + 16]);
#pragma unroll
      for (int i = 0; i < 8; ++i) {
        v[i] = swap_add16(v[i], v[i + 8]);
        v[i] += dppf<0xB1>(0.f, v[i]);
        v[i] += dppf<0x4E>(0.f, v[i]);
        v[i] += dppf<0x141>(0.f, v[i]);
      }
      float s0 = v[0], s1 = v[1], s2 = v[2], s3 = v[3], s4 = v[4], s5 = v[5], s6 = v[6], s7 = v[7];
      asm volatile("" : "+v"(s0), "+v"(s1), "+v"(s2), "+v"(s3), "+v"(s4), "+v"(s5), "+v"(s6), "+v"(s7));
      s0 = (l & 4) ? s4 : s0; s1 = (l & 4) ? s5 : s1; s2 = (l & 4) ? s6 : s2; s3 = (l & 4) ? s7 : s3;
      s0 = (l & 2) ? s2 : s0; s1 = (l & 2) ? s3 : s1;
      const float kv = 2.0f * ((l & 1) ? s1 : s0);
      khl = pk2(kv, 0.f);
      khl = pk2(kv, kv - lo16(khl));
      kr[2080 - 64 * ps - tl] = (u16)khl;
      kr[CKN + 2080 - 64 * ps - tl] = (u16)(khl >> 16);
      SST(ps);
      if (ps < 2) {
        if (ps == 0) {
          if (w < 2) {
            reinterpret_cast<v4u*>(Xr)[tid] = tx[0];
            const unsigned long long m = (((unsigned long long)en.y << 32) | en.x) + TAG_STEP;
            v4u g0;
            for (unsigned spins = 0; spins < 16u * SPIN_CAP; ++spins) {
              g0 = LD128(MKRS(ws + OFF_G0, 8192), tid * 16, REP * 2048);
              if (__all(((g0.x ^ g0.z) | (g0.y ^ (unsigned)m) | (g0.w ^ (unsigned)(m >> 32))) == 0u)) break;
              asm volatile("s_sleep 2" ::: "memory");
            }
            U_ROW2(tid, g0.x);
          }
          __syncthreads();
        }
        f32x4 a0t = {};
#pragma unroll
        for (int b = -1; b < 4; ++b) a0t = MFMA16(KFRAG(-32 * b), conv_bfrag(ub + 16 * b, ush), a0t);
        const float y0 = swap_add32(a0t[0], a0t[1]), y1 = swap_add32(a0t[2], a0t[3]);
        if ((g & 1) == ps) TILE_OUT(0);
        __syncthreads();
        if (w == ps) {
          CHUNK_ST(w);
          if (ps == 0) zero_wait(ws);
          DRAIN();
          if (l < 8) set_flag(FL + FL_FC + ((2 * w * 4 + l) * 8 + (cv >> 3)) * 32 + (cv & 7));
        }
        SST(4 + ps);
      }
      if (ps == 2) {
        if (w >= 2) tx[0] = *reinterpret_cast<const v4u*>(A.x0 + (size_t)tid * DM + 8 * cv);
#pragma unroll
        for (int i = 1; i < 4; ++i) tx[i] = *reinterpret_cast<const v4u*>(A.x0 + (size_t)(tid + 512 * i) * DM + 8 * cv);
      }
    }
    if (w >= 2) reinterpret_cast<v4u*>(Xr)[tid] = tx[0];
#pragma unroll
    for (int i = 1; i < 4; ++i) reinterpret_cast<v4u*>(Xr)[tid + 512 * i] = tx[i];
    {
      unsigned spins = 0;
      while (!__all(__hip_atomic_load((gu32*)FS + (w + 8 * ((l >> 3) & 3)) * 32 + (l & 7), RLX_AGENT) != 0u)) {
        if (spin_timeout(++spins, TMO)) break;
        __builtin_amdgcn_s_sleep(4);
      }
      asm volatile("" ::: "memory");
    }
    if (w >= 2) U_ROW(tid);
#pragma unroll
    for (int i = 1; i < 4; ++i) U_ROW(tid + 512 * i);
    SST(6);
    __syncthreads();
    {
      f32x4 a1t = {};
#pragma unroll
      for (int b = -1; b < 8; ++b) a1t = MFMA16(KFRAG(128 - 32 * b), conv_bfrag(ub + 16 * b, ush), a1t);
      const float y0 = swap_add32(a1t[0], a1t[1]), y1 = swap_add32(a1t[2], a1t[3]);
      TILE_OUT(1);
    }
    __syncthreads();
    if (w < 2) {
      CHUNK_ST(2 + w);
      DRAIN();
      if (l < 8) set_flag(FL + FL_FC + (((4 + 2 * w) * 4 + l) * 8 + (cv >> 3)) * 32 + (cv & 7));
    }
    SST(8);
    (void)wait_n<8, 1>(FL + FL_KG + cv * 32 + (w >> 2), 1, TMO);
    SST(7);
    {
      const __amdgpu_buffer_rsrc_t rk = MKRS(ws + OFF_KF + (size_t)d * 2 * CKN * 2, 2 * CKN * 2);
      v4u t[8];
#pragma unroll
      for (int i = 0; i < 4; ++i) {
        t[i] = LD128(rk, (64 * i + l) * 16, 0);
        t[4 + i] = LD128(rk, (64 * i + l) * 16, CKN * 2);
      }
#pragma unroll
      for (int i = 0; i < 4; ++i)
        if (64 * i + l < 229) {
          reinterpret_cast<v4u*>(kr)[64 * i + l] = t[i];
          reinterpret_cast<v4u*>(kr + CKN)[64 * i + l] = t[4 + i];
        }
      kr[2080 - 192 - tl] = (u16)khl;
      kr[CKN + 2080 - 192 - tl] = (u16)(khl >> 16);
    }
    asm volatile("" ::: "memory");
#define BULK(TA, TB, HOOK)                                                                      \
  {                                                                                            \
    const bf16x8 bm = conv_bfrag(ub - 16, ush);                                                \
    _Pragma("unroll") for (int T = (TA); T < (TB); ++T) acc[T] = MFMA16(KFRAG(128 * T + 32), bm, (f32x4{})); \
    _Pragma("unroll") for (int T = 11; T < 16; ++T) far[T - 11] = MFMA16(KFRAG(128 * T + 32), bm, (f32x4{})); \
    _Pragma("unroll 1") for (int r = 0; r < 4; ++r) {                                          \
      bf16x8 bf[TB];                                                                           \
      _Pragma("unroll") for (int j = 0; j < (TB); ++j) bf[j] = conv_bfrag(ub + 16 * r + 64 * j, ush); \
      _Pragma("unroll") for (int e = 0; e < (TB); ++e) {                                       \
        const bf16x8 a = KFRAG(128 * e - 32 * r);                                              \
        _Pragma("unroll") for (int T = (e > (TA) ? e : (TA)); T < (TB); ++T) acc[T] = MFMA16(a, bf[T - e], acc[T]); \
      }                                                                                        \
      if (r == 0) { HOOK }                                                                     \
    }                                                                                          \
  }
    BULK(2, 11, if (w == 1) (void)(wait_n<SLP_V, 32>(FL + FL_FZ + REP * 64, 64, TMO)););
    SST(10);
#pragma unroll
    for (int T = 2; T < 11; ++T) {
      const float y0 = swap_add32(acc[T][0], acc[T][1]), y1 = swap_add32(acc[T][2], acc[T][3]);
      TILE_OUT(T);
    }
    __syncthreads();
    if (w >= 2) {
#pragma unroll
      for (int i = 0; i < 3; ++i) CHUNK_ST(3 * w - 2 + i);
#pragma unroll
      for (int i = 0; i < 2; ++i) xf[i] = *reinterpret_cast<const v4u*>(Xr + (64 * ((18 + 2 * w + i) & 31) + l) * 8);
      DRAIN();
      if (l < 24) set_flag(FL + FL_FC + ((6 * w - 4) * 4 + l) * 8 * 32 + (cv >> 3) * 32 + (cv & 7));
    }
#define FAR_PIECE(p)                                                                           \
  {                                                                                            \
    if ((p) < 24) {                                                                            \
      const int o_ = 256 * ((p) >> 2) + 32 * ((p) & 3);                                        \
      const bf16x8 b0 = conv_bfrag(ub1 + (o_ >> 1), ush), b1 = conv_bfrag(ub1 + (o_ >> 1) + 64, ush); \
      _Pragma("unroll") for (int e = 10; e < 16; ++e) {                                        \
        const bf16x8 a = KFRAG(128 * e - o_);                                                  \
        if (e > 10) far[e - 11] = MFMA16(a, b0, far[e - 11]);                                  \
        if (e < 15) far[e - 10] = MFMA16(a, b1, far[e - 10]);                                  \
      }                                                                                        \
    } else if ((p) < 28) {                                                                     \
      bf16x8 bf[4];                                                                            \
      _Pragma("unroll") for (int j = 0; j < 4; ++j) bf[j] = conv_bfrag(ub1 + 16 * ((p) & 3) + 64 * (12 + j), ush); \
      _Pragma("unroll") for (int e = 0; e < 4; ++e) {                                          \
        const bf16x8 a = KFRAG(128 * e - 32 * ((p) & 3));                                      \
        _Pragma("unroll") for (int j = 0; j + e < 4; ++j) far[1 + e + j] = MFMA16(a, bf[j], far[1 + e + j]); \
      }                                                                                        \
    } else if ((p) == 28) {                                                                    \
                     \
      u16* ucw = Uc + w * CUN + 32 + t0;                                                       \
      _Pragma("unroll") for (int T = 11; T < 16; ++T) {                                        \
        const float y0 = swap_add32(far[T - 11][0], far[T - 11][1]), y1 = swap_add32(far[T - 11][2], far[T - 11][3]); \
        const float u0 = bf2f(ucw[128 * T]), u1 = bf2f(ucw[128 * T + 32]);                     \
        const unsigned yy = pk2(y0, y1), uu = pk2(u0 * dsk, u1 * dsk);                         \
        const unsigned aa = pk2(lo16(yy) + lo16(uu), hi16(yy) + hi16(uu));                     \
        ucw[128 * T] = (u16)aa;                                                                \
        ucw[128 * T + 32] = (u16)(aa >> 16);                                                   \
      }                                                                                        \
    } else if (w >= 2 && w < 7) {                                                              \
      int l = tid & 63;                                                                        \
      asm volatile("" : "+v"(l), "+v"(xf[0]), "+v"(xf[1]));                                    \
      if ((p) == 29) {                                                                         \
        _Pragma("unroll") for (int i = 0; i < 2; ++i) {                                        \
          const int ck = 18 + 2 * w + i;                                                       \
          const u16* ur = Uc + 32 + 64 * ck + l;                                               \
          v4u o;                                                                               \
          _Pragma("unroll") for (int e = 0; e < 4; ++e)                                        \
            o[e] = pk2(lo16(xf[i][e]) + bf2f(ur[(2 * e) * CUN]), hi16(xf[i][e]) + bf2f(ur[(2 * e + 1) * CUN])); \
          ST128(o, rz1, (l >> 5) * 32768 + (l & 31) * 16, (2 * ck * 64 + cv) * 512);           \
        }                                                                                      \
      } else {                                                                                 \
        DRAIN();                                                                               \
        if (l < 16) set_flag(FL + FL_FC + (((36 + 4 * w) * 4 + l) * 8 + (cv >> 3)) * 32 + (cv & 7)); \
      }                                                                                        \
    }                                                                                          \
  }
#undef BULK
    SST(9);
#undef TILE_OUT
#undef CHUNK_ST
#undef U_ROW
#undef U_ROW2
  }
  u16* uc = Uc + (8 + w) * CUN;
  const unsigned* ub = reinterpret_cast<const unsigned*>(uc + (c & ~1) + 8 * g + 32);
#pragma unroll
  for (int j = 0; j < 16; ++j) acc[j] = f32x4{};
  const __amdgpu_buffer_rsrc_t rx = MKRS(ws + OFF_X2, SZ_X), rz = MKRS(ws + OFF_X3, SZ_X);
  const __amdgpu_buffer_rsrc_t rf = MKRS(FL + FL_FR, 64 * 16 * 128);
  unsigned st = 0u;
  v2u xx = {0u, 0u};
#define X2_LOAD(ss, far)                                                                                                      \
  {                                                                                                                      \
    xx = LD64(rx, (l >> 1) * (DM * 2) + (l & 1) * 8, (far) ? (int)SZ_X : ((ss) * SEG * DM + 8 * cv) * 2);                 \
    st = __builtin_amdgcn_raw_buffer_load_b32(rf, (l >> 4) * 128 + ((l >> 1) & 7) * 4, ((ss) * 4 + REP) * 512, AUX_SC1); \
  }
  unsigned tl = now10();
  for (int s = 0; s < NSEG; ++s) {
    if (w == 1) {
      for (unsigned spins = 0; !__all(umin3(xx.x, xx.y, st) != 0u); ++spins) {
        if (spin_timeout(spins, TMO)) break;
        if (GATE_V && spins == 0 && !__any(st != 0u)) gate(tl);
        if (spins && !__all(st != 0u)) __builtin_amdgcn_s_sleep(SLP_V);
        X2_LOAD(s, __builtin_popcountll(__ballot(st != 0u)) < CVTHR);
      }
      tl = now10() + GATE_V;
      TS(0, s, 64);
      const float mb = lo16(st), ri = 1.0f / hi16(st);
      const unsigned t0 = pk2(lo16(xx.x) - mb, hi16(xx.x) - mb), t1 = pk2(lo16(xx.y) - mb, hi16(xx.y) - mb);
      *reinterpret_cast<v2u*>(Su + l * 4) = v2u{pk2(lo16(t0) * ri, hi16(t0) * ri), pk2(lo16(t1) * ri, hi16(t1) * ri)};
      *reinterpret_cast<v2u*>(Sx + l * 4) = xx;
    }
    const int q = s & 3;
    const u16* kq = ka + 32 * q;
    const bf16x8 a0 = *reinterpret_cast<const bf16x8*>(kq - 32), a1 = *reinterpret_cast<const bf16x8*>(kq);
    __syncthreads();
    TS(1, s, 0);
    if (s == 0)
      for (int x = l; x < CUN / 2; x += 64) reinterpret_cast<unsigned*>(uc)[x] = 0u;
    if (l < 32) uc[32 + 32 * s + l] = Su[l * 8 + w];
    asm volatile("s_waitcnt lgkmcnt(0)" ::: "memory");
    const bf16x8 bp = conv_bfrag(ub + 16 * (s - 1), ush);
    acc[0] = MFMA16(a0, bp, (q == 0 ? acc[1] : acc[0]));
    const f32x4 tmp = MFMA16(a1, conv_bfrag(ub + 16 * s, ush), acc[0]);
    float y0 = (q & 1) ? tmp[2] : tmp[0], y1 = (q & 1) ? tmp[3] : tmp[1];
    y0 = xsum32(y0);
    y1 = xsum32(y1);
    if (g == (q >> 1)) {
      const float u0 = bf2f(Su[c * 8 + w]), u1 = bf2f(Su[(16 + c) * 8 + w]);
      const float a0 = bfr(bfr(y0) + bfr(u0 * dsk)), a1 = bfr(bfr(y1) + bfr(u1 * dsk));
      To[c * 8 + w] = f2bf(bf2f(Sx[c * 8 + w]) + a0);
      To[(16 + c) * 8 + w] = f2bf(bf2f(Sx[(16 + c) * 8 + w]) + a1);
    }
    __syncthreads();
    if (w == 0 && l < 32) {
      const v4u o = *reinterpret_cast<const v4u*>(To + l * 8);
      ST128(o, rz, l * 16, (s * 64 + cv) * 512);
    }
    if (w == 1) X2_LOAD(s + 1, CVTHR > 0);
    const int left = 15 - (s >> 2);
#define PUSH(J0, SH)                                                                                              \
  if (left >= (J0)) {                                                                                             \
    _Pragma("unroll") for (int j = (J0); j < (J0) + 4 && j < 16; ++j)                                             \
      acc[j] = MFMA16(*reinterpret_cast<const bf16x8*>(kq - 32 - 128 * j), bp, (j + (SH) < 16 ? acc[(j + (SH)) & 15] : f32x4{})); \
  }
    TS(2, s, 0);
    if (w == 0) {
      DRAIN();
      if (l < 4) set_flag(FL + FL_FC + (((64 + s) * 4 + l) * 8 + (cv >> 3)) * 32 + (cv & 7));
    }
    TS(3, s, 0);
    if (q == 0) { PUSH(1, 1) PUSH(5, 1) PUSH(9, 1) PUSH(13, 1) }
    else { PUSH(1, 0) PUSH(5, 0) PUSH(9, 0) PUSH(13, 0) }
#undef PUSH
    MSTAMP();
    if (s >= CVPRE && s < 31 + CVPRE) FAR_PIECE(s - CVPRE);
  }
#undef KFRAG
#undef X2_LOAD
#undef FAR_PIECE
}

__device__ __forceinline__ v4u ln_row_packed(const v4u xr) {
  float v[8];
#pragma unroll
  for (int e = 0; e < 4; ++e) { v[2 * e] = lo16(xr[e]); v[2 * e + 1] = hi16(xr[e]); }
  float s = ((v[0] + v[1]) + (v[2] + v[3])) + ((v[4] + v[5]) + (v[6] + v[7]));
  s = wave_sum(s);
  const float mean = s * (1.0f / DM);
  float q = 0.f;
#pragma unroll
  for (int j = 0; j < 8; ++j) { float t = v[j] - mean; q += t * t; }
  q = wave_sum(q);
  const float var = q * (1.0f / DM);
  const float mb = bfr(mean);
  const float sd = bfr(sqrtf(bfr(bfr(var) + 1e-5f)));
  const float ri = 1.0f / sd;
  v4u o;
#pragma unroll
  for (int e = 0; e < 4; ++e) {
    const unsigned t = pk2(v[2 * e] - mb, v[2 * e + 1] - mb);
    const float x0 = lo16(t), x1 = hi16(t);
    float q0 = x0 * ri, q1 = x1 * ri;
    q0 = __builtin_fmaf(__builtin_fmaf(-q0, sd, x0), ri, q0);
    q1 = __builtin_fmaf(__builtin_fmaf(-q1, sd, x1), ri, q1);
    o[e] = pk2(q0, q1);
  }
  return o;
}

__device__ __forceinline__ float bfr1(float x) { return lo16(pk2(x, x)); }
__device__ __forceinline__ void ln4_packed(v4u (&x)[4]) {
  f32x2_t v[16];
#pragma unroll
  for (int e = 0; e < 16; ++e) v[e] = f32x2_t{lo16(x[e >> 2][e & 3]), hi16(x[e >> 2][e & 3])};
  const f32x2_t t = (((v[0] + v[1]) + (v[2] + v[3])) + ((v[4] + v[5]) + (v[6] + v[7]))) +
                    (((v[8] + v[9]) + (v[10] + v[11])) + ((v[12] + v[13]) + (v[14] + v[15])));
  const float mean = red_mod4(t.x + t.y) * (1.0f / DM);
  const float mb = bfr1(mean);
  f32x2_t q0 = {0.f, 0.f}, q1 = {0.f, 0.f};
#pragma unroll
  for (int e = 0; e < 16; e += 2) {
    v[e] -= mb;
    v[e + 1] -= mb;
    q0 += v[e] * v[e];
    q1 += v[e + 1] * v[e + 1];
  }
  q0 += q1;
  const float dm = mean - mb;
  const float var = fmaxf(red_mod4(q0.x + q0.y) * (1.0f / DM) - dm * dm, 0.f);
  const float sd = bfr1(sqrtf(bfr1(bfr1(var) + 1e-5f)));
  const float ri = 1.0f / sd;
#pragma unroll
  for (int e = 0; e < 16; ++e) {
    const unsigned t2 = pk2(v[e].x, v[e].y);
    const f32x2_t q = f32x2_t{lo16(t2), hi16(t2)} * ri;
    x[e >> 2][e & 3] = pk2(q.x, q.y);
  }
}

extern "C" __device__ float mul1(float, float) __asm("llvm.amdgcn.fmul.legacy");
template <bool G0>
__device__ __forceinline__ void role_qkv_t(const MegaArgs& A, char* smem, const int L, const int h) {
  __builtin_amdgcn_s_waitcnt(0x0F70);
  const int w = __builtin_amdgcn_readfirstlane(threadIdx.x >> 6);
  char* ws = A.ws;
  unsigned* FL = (unsigned*)(ws + OFF_FL);
  unsigned* TMO = FL + FL_TMO;
#define LANE_IDS()                         \
  const int l = lane_id();                 \
  const int c = l & 15, g = l >> 4;        \
  (void)c; (void)g;
  unsigned* FC = FL + FL_FC + (L * 64 * 4 + REP) * 8 * 32;
  const __amdgpu_buffer_rsrc_t rsQ = MKRS(ws + OFF_QK + L * SZ_QK, SZ_QK);
  const __amdgpu_buffer_rsrc_t rsV = MKRS(ws + OFF_VF + L * SZ_O, SZ_O);
  const __amdgpu_buffer_rsrc_t rsM = MKRS(ws + (G0 ? OFF_MEMK : OFF_MEMV) + L * SZ_MEM, SZ_MEM);
  bf16x8 wq[16], wk[16], wv[16];
  {
    LANE_IDS();
    const unsigned sb = lds_base(smem);
    const int q = (l >> 2) & 3, p = l & 3, f = q | ((g & 1) << 2);
    const unsigned ra = sb + (8 * g + q) * 256 + (w ^ f) * 32 + p * 8;
#define W_STAGE(W, hf, VM, NEXT)                                               \
  {                                                                           \
    WAIT_VM(VM);                                                              \
    RAW_BAR();                                                                \
    _Pragma("unroll") for (int t = 0; t < 8; ++t) {                           \
      const unsigned a_ = ra + (hf) * 8192 + t * 16384;                       \
      const v2u r0 = trd(a_), r1 = trd(a_ + 1024);                            \
      W[8 * (hf) + t] = BF8((v4u{r0.x, r0.y, r1.x, r1.y}));                   \
    }                                                                         \
    _Pragma("unroll") for (int t = 0; t < 8; ++t) asm volatile("" : "+v"(W[8 * (hf) + t])); \
    RAW_BAR();                                                                \
    NEXT;                                                                     \
  }
    wqkv_issue<false>(A.Wq, 0, h, w, l, sb);
    wqkv_issue<false>(A.Wq, 1, h, w, l, sb);
    W_STAGE(wq, 0, 8, wqkv_issue<false>(A.Wk, 0, h, w, l, sb));
    SST(4);
    W_STAGE(wq, 1, 8, wqkv_issue<false>(A.Wk, 1, h, w, l, sb));
    SST(5);
    W_STAGE(wk, 0, 8, wqkv_issue<false>(A.Wv, 0, h, w, l, sb));
    W_STAGE(wk, 1, 8, wqkv_issue<false>(A.Wv, 1, h, w, l, sb));
    SST(6);
    W_STAGE(wv, 0, 8, (void)0);
    W_STAGE(wv, 1, 0, (void)0);
#undef W_STAGE
  }
  WSTAMP2();
  constexpr int Q_X = 98304;
  const unsigned sb = lds_base(smem);
  v2u eq, ek, ev;
  {
    LANE_IDS();
    const v4u zt = LD128(MKRS(ws + OFF_TAG, 8192 + 8), l * 16, (blockIdx.x & 7) * 1024);
    const v2u ze = LD64(MKRS(ws + OFF_TAG, 8192 + 8), 0, 8192);
    const v4u one = {0x3f803f80u, 0x3f803f80u, 0x3f803f80u, 0x3f803f80u};
    f32x4 t[3] = {};
#pragma unroll
    for (int ks = 0; ks < 16; ++ks) {
      t[0] = MFMA16(BF8(one), wq[ks], t[0]);
      t[1] = MFMA16(BF8(one), wk[ks], t[1]);
      t[2] = MFMA16(BF8(one), wv[ks], t[2]);
    }
    v2u e[3];
#pragma unroll
    for (int i = 0; i < 3; ++i) {
      const float x = g ? 0.f : t[i][0], x1 = x - bfr1(x);
      e[i] = v2u{pk2(x, x1), pk2(x1 - bfr1(x1), 0.f)};
    }
    eq = e[0]; ek = e[1]; ev = e[2];
    asm volatile("" : "+v"(eq), "+v"(ek), "+v"(ev));
    if (!zero_seen(zt, ze)) zero_wait(ws);
    if (l < 32) *reinterpret_cast<v4u*>(smem + Q_X + w * 2048 + (l >> 4) * 16384 + c * 16) = one;
  }
  constexpr int Q_CNT = Q_X + 11 * 2048 + 256;
  lu32* cnt = (lu32*)(unsigned long)(sb + Q_CNT);
  if (threadIdx.x < 16) cnt[threadIdx.x] = 0u;
#define C_ADD(i) asm volatile("s_mov_b64 exec, 1\n\tds_add_u32 %0, %1\n\ts_mov_b64 exec, -1" ::"v"(sb + Q_CNT + 4 * (i)), "v"(1u) : "memory")
#define C_RD(i) (*(volatile lu32*)(cnt + (i)))
#define C_GET(i) ((unsigned)__builtin_amdgcn_readfirstlane(C_RD(i)))
#define C_WAIT(i, n)                                                                                                    \
  {                                                                                                                     \
    for (unsigned spins = 0; C_GET(i) < (n) && spins < 64u * SPIN_CAP; ++spins) __builtin_amdgcn_s_sleep(1);             \
    asm volatile("" ::: "memory");                                                                                      \
  }
  const char* Xb = ws + (L ? OFF_X3 : OFF_XA) + w * 8192;
  const v4u rsF = mk_srd(FC + w * 64, (64 * 32 - REP * 8 - 2 * w) * 128);
  unsigned l16, vo, fo;
  {
    LANE_IDS();
    l16 = l * 16;
    vo = (c + 16 * (2 * (w & 1) + (g >> 1))) * 16 + 4096 * (g & 1);
    fo = g * 512 + c * 16;
  }
#define DMA4(x) "global_load_lds_dwordx4 %1, " x " sc1\n\tglobal_load_lds_dwordx4 %1, " x " offset:1024 sc1\n\t"       \
                "global_load_lds_dwordx4 %1, " x " offset:2048 sc1\n\tglobal_load_lds_dwordx4 %1, " x " offset:3072 sc1\n\t"
#define X_DMA(ss, bb)                                                                                                   \
  {                                                                                                                     \
    unsigned keep_;                                                                                                     \
    const char* x_ = Xb + (size_t)(ss) * 32768;                                                                         \
    const unsigned d_ = sb + (bb) * 32768 + w * 8192;                                                                   \
    asm volatile("s_nop 4\n\ts_mov_b32 %0, m0\n\ts_mov_b32 m0, %4\n\ts_nop 0\n\t" DMA4("%2")                           \
                 "s_mov_b32 m0, %5\n\ts_nop 0\n\t" DMA4("%3") "s_mov_b32 m0, %0"                                         \
                 : "=&s"(keep_)                                                                                         \
                 : "v"(l16), "s"(x_), "s"(x_ + 4096), "s"(d_), "s"(d_ + 4096u)                                           \
                 : "memory");                                                                                           \
    TS(0, ss, 0);                                                                                                       \
  }
#define X_DMA1(on, x, d, off)                                                                                           \
  {                                                                                                                     \
    unsigned keep_;                                                                                                     \
    asm volatile("s_cmp_eq_u32 %4, 0\n\ts_cbranch_scc1 1f\n\ts_mov_b32 %0, m0\n\ts_mov_b32 m0, %3\n\ts_nop 0\n\t"       \
                 "global_load_lds_dwordx4 %1, %2 offset:%5 sc1\n\ts_mov_b32 m0, %0\n1:"                                  \
                 : "=&s"(keep_)                                                                                         \
                 : "v"(l16), "s"(x), "s"(d), "s"(on), "n"(off)                                                          \
                 : "memory", "scc");                                                                                    \
  }
#define F_LOAD(r, ss)                                                                                                   \
  asm volatile("s_nop 4\n\tbuffer_load_dword %0, %1, %2, %3 offen sc1"                                                  \
               : "=v"(r)                                                                                                \
               : "v"(((l16 >> 2) & 28) + (l16 & 128)), "s"(rsF), "s"((ss) * 4096)                                       \
               : "memory")
  unsigned* FQ = FL + FL_FQ + (L * 64 * 32 + h) * 32;
#define Q_FLAG(ss)                                                                                                      \
  {                                                                                                                     \
    unsigned old_;                                                                                                      \
    asm volatile("s_mov_b64 exec, 1\n\tds_add_rtn_u32 %0, %1, %2\n\ts_mov_b64 exec, -1\n\ts_waitcnt lgkmcnt(0)"           \
                 : "=&v"(old_) : "v"(sb + Q_CNT + ((ss) & 1) * 4), "v"(1u) : "memory");                                 \
    if ((__builtin_amdgcn_readfirstlane(old_) & 7u) == 7u) {                                                            \
      asm volatile("s_mov_b64 exec, 1\n\tglobal_store_dword %0, %1, %2 sc1\n\ts_mov_b64 exec, -1"                        \
                   ::"v"(0), "v"(1u), "s"(FQ + (ss) * 1024) : "memory");                                                \
      TS(3, ss, threadIdx.x & ~63u);                                                                                    \
    }                                                                                                                   \
    pend = false;                                                                                                       \
  }
  __builtin_amdgcn_s_waitcnt(0x0F70);
  RAW_BAR();
  bool have = false, pend = false;
  unsigned tl = now10();
  const unsigned tq = tl - QPL;
  int b = 0;
  unsigned k4 = 4u;
  FSD;
  for (int s = 0; s < NSEG; ++s) {
    FS(0);
    const int b1 = b == 2 ? 0 : b + 1;
    const unsigned u1 = 2u * (b1 ? k4 - 4u : k4);
    unsigned nf;
    if (QPACE && L == 0 && s >= QPS && (int)(tq + s * QPACE - now10()) > 0) {
      if (pend) {
        WAIT_VM(0);
        Q_FLAG(s - 1);
      }
      gate(tq + s * QPACE);
    }
    if (G0) {
      if (!have) {
        for (unsigned spins = 0;; ++spins) {
          unsigned n;
          F_LOAD(n, s);
          WAIT_VM(0);
          if (pend) Q_FLAG(s - 1);
          if (all_set(n) || spin_timeout(spins, TMO)) break;
          if (GATE_Q && spins == 0) gate(tl);
          else if (L) __builtin_amdgcn_s_sleep(SLP_Q);
          else __builtin_amdgcn_s_sleep(SLP_Q1);
        }
        tl = now10() + GATE_Q;
        C_WAIT(8 + b, 2u * k4 - 8u);
        X_DMA(s, b);
        WAIT_VM(0);
        C_ADD(4 + b);
      }
      const unsigned c0 = C_RD(4 + b);
      F_LOAD(nf, s + 1);
      if ((unsigned)__builtin_amdgcn_readfirstlane(c0) < k4) C_WAIT(4 + b, k4);
    } else {
      if (C_GET(4 + b) < k4) {
        if (pend) {
          WAIT_VM(0);
          Q_FLAG(s - 1);
        }
        C_WAIT(4 + b, k4);
      }
      FS(1);
      const char* sp = smem + ((l16 & 128) ? Q_X + ((w + 1) & 1) * 128 : b * 32768 + fo + (w & 3) * 128);
      f32x4 gm = {};
      __builtin_amdgcn_s_setprio(3);
      {
        bf16x8 a[10];
#pragma unroll
        for (int ks = 0; ks < 10; ++ks) a[ks] = *reinterpret_cast<const bf16x8*>(sp + 2048 * ks);
        __builtin_amdgcn_sched_barrier(0);
#pragma unroll
        for (int ks = 0; ks < 16; ++ks) {
          gm = MFMA16(a[ks % 10], a[ks % 10], gm);
          if (ks < 6) a[ks] = *reinterpret_cast<const bf16x8*>(sp + 2048 * (10 + ks));
          __builtin_amdgcn_sched_barrier(0);
        }
      }
      float q2 = (l16 & 16) ? gm[1] : gm[0], q3 = (l16 & 16) ? gm[3] : gm[2];
      asm volatile("" : "+v"(q2), "+v"(q3));
      q2 = (l16 & 32) ? q3 : q2;
      auto sw = __builtin_amdgcn_permlane32_swap(__float_as_uint(q2), __float_as_uint(q2), false, false);
      const float mean = __uint_as_float(sw[1]) * (-1.0f / DM);
      const float var = fmaxf(__builtin_fmaf(-mean, mean, q2 * (1.0f / DM)), 0.f);
      const float sd = bfr1(__builtin_amdgcn_sqrtf(bfr1(bfr1(var) + 1e-5f)));
      asm volatile("s_mov_b64 exec, 0xf0000f\n\tds_write2_b32 %0, %1, %2 offset1:32\n\ts_mov_b64 exec, -1"
                   ::"v"(sb + Q_X + (8 + b) * 2048 + 256 + (w & 3) * 32 + ((l16 >> 2) & 28)), "v"(pk2(mean, mean)),
                   "v"(__builtin_amdgcn_rcpf(sd)) : "memory");
      C_ADD(12 + b);
      FS(2);
      if (pend) {
        WAIT_VM(0);
        Q_FLAG(s - 1);
      }
    }
    TS(G0 ? 1 : 5, s, G0 ? 0 : 256);
    FS(3);
    const char* ap = smem + b * 32768 + fo;
#define AFRAG(i) (*reinterpret_cast<const bf16x8*>(ap + ((i) >> 4) * 256 + 2048 * ((i) & 15)))
    bf16x8 abuf[QPF + 1];
#pragma unroll
    for (int i = 0; i < QPF; ++i) abuf[i] = AFRAG(i);
    f32x4 aq[2] = {}, ak[2] = {}, av[2] = {};
    unsigned cx = 0u;
    int on = 0;
    const char* xn = Xb + (size_t)(s + 1) * 32768;
    const unsigned dn = sb + b1 * 32768 + w * 8192;
    __builtin_amdgcn_s_setprio(G0 ? 1 : 0);
#pragma unroll
    for (int rt = 0; rt < 2; ++rt) {
#pragma unroll
      for (int ks = 0; ks < 16; ++ks) {
        const int i = 16 * rt + ks;
        if (i + QPF < 32) abuf[(i + QPF) % (QPF + 1)] = AFRAG(i + QPF);
        if (G0 && i == 11) cx = C_RD(8 + b1);
        if (i == 28) cx = C_RD(12 + b);
        const bf16x8 a = abuf[i % (QPF + 1)];
        aq[rt] = MFMA16(wq[ks], a, aq[rt]);
        ak[rt] = MFMA16(wk[ks], a, ak[rt]);
        av[rt] = MFMA16(a, wv[ks], av[rt]);
        if (i == QUP) { __builtin_amdgcn_s_setprio(G0 ? 3 : 2); FS(4); }
        __builtin_amdgcn_sched_barrier(0);
        if (G0 && rt == 1 && ks < 8) {
          X_DMA1(on, xn + (ks >> 2) * 4096, dn + (ks >> 2) * 4096, (ks & 3) * 1024);
          __builtin_amdgcn_sched_barrier(0);
        }
      }
      asm volatile("" : "+v"(aq[rt]), "+v"(ak[rt]), "+v"(av[rt]));
      if (rt == 0) FS(5); else FS(7);
      if (G0 && rt == 0) {
        WAIT_VM(0);
        if (pend) Q_FLAG(s - 1);
        have = all_set(nf) && (unsigned)__builtin_amdgcn_readfirstlane(cx) >= u1;
        F_LOAD(nf, have ? NSEG : s + 1);
        if (have) TS(0, s + 1, 0);
        on = __builtin_amdgcn_readfirstlane((int)have);
        FS(6);
      }
    }
    if ((unsigned)__builtin_amdgcn_readfirstlane(cx) < k4) C_WAIT(12 + b, k4);
    FS(8);
    v4u vfrag, qf4, kf4;
    {
      const char* stp = smem + Q_X + (8 + b) * 2048 + 256;
      unsigned nm[2];
      float ri[2];
      f32x4 rv[2];
#pragma unroll
      for (int rt = 0; rt < 2; ++rt) {
        nm[rt] = *reinterpret_cast<const unsigned*>(stp + 64 * rt + ((l16 >> 2) & 60));
        ri[rt] = *reinterpret_cast<const float*>(stp + 128 + 64 * rt + ((l16 >> 2) & 60));
        rv[rt] = *reinterpret_cast<const f32x4*>(stp + 128 + 64 * rt + ((l16 >> 4) & 48));
      }
#pragma unroll
      for (int rt = 0; rt < 2; ++rt) {
        const v2u xm = {nm[rt], nm[rt]};
        aq[rt] = MFMA16K(eq, xm, aq[rt]);
        ak[rt] = MFMA16K(ek, xm, ak[rt]);
        av[rt] = MFMA16K(xm, ev, av[rt]);
      }
      __builtin_amdgcn_s_setprio(QEP);
#pragma unroll
      for (int rt = 0; rt < 2; ++rt) {
        f32x4 tq, tk, tv;
#pragma unroll
        for (int e = 0; e < 4; ++e) {
          tq[e] = mul1(aq[rt][e], ri[rt]);
          tk[e] = mul1(ak[rt][e], ri[rt]);
          tv[e] = mul1(av[rt][e], rv[rt][e]);
        }
        qf4[2 * rt] = pk2(tq[0], tq[1]); qf4[2 * rt + 1] = pk2(tq[2], tq[3]);
        kf4[2 * rt] = pk2(tk[0], tk[1]); kf4[2 * rt + 1] = pk2(tk[2], tk[3]);
        vfrag[2 * rt] = pk2(tv[0], tv[1]); vfrag[2 * rt + 1] = pk2(tv[2], tv[3]);
      }
    }
    {
      auto q0 = __builtin_amdgcn_permlane16_swap(qf4.x, qf4.z, false, false);
      auto q1 = __builtin_amdgcn_permlane16_swap(qf4.y, qf4.w, false, false);
      auto k0 = __builtin_amdgcn_permlane16_swap(kf4.x, kf4.z, false, false);
      auto k1 = __builtin_amdgcn_permlane16_swap(kf4.y, kf4.w, false, false);
      v4u qo = {q0[0], q1[0], q0[1], q1[1]}, ko = {k0[0], k1[0], k0[1], k1[1]};
      const int soff = ((s * NH + h) * 16 + (w >> 1)) * 1024;
      asm volatile("" : "+v"(qo), "+v"(ko), "+v"(vfrag));
      FS(9);
      C_ADD(8 + b);
      if (G0 && have) {
        WAIT_VM(0);
        C_ADD(4 + b1);
      }
      TS(G0 ? 2 : 6, s, G0 ? 0 : 256);
      FS(10);
      ST128(ko, rsQ, vo, soff + 8192);
      ST128(qo, rsQ, vo, soff);
      ST128(vfrag, rsV, l16, ((s * NH + h) * 512 + w * 64) * 16);
      ST64((v2u{0u, 0u}), rsM, l16 >> 1, (s * NH + h) * 2048 + (w & 3) * 512);
      pend = true;
      FS(11);
    }
    if (G0) {
      WAIT_VM(4);
      if (all_set(nf) && C_GET(8 + b1) >= u1) {
        X_DMA(s + 1, b1);
        have = true;
        WAIT_VM(8);
        Q_FLAG(s);
        WAIT_VM(0);
        C_ADD(4 + b1);
      }
    }
    MSTAMP();
    FD(s);
    b = b1;
    if (b == 0) k4 += 4u;
  }
  DRAIN();
  if (pend) Q_FLAG(NSEG - 1);
#undef LANE_IDS
#undef DMA4
#undef X_DMA
#undef X_DMA1
#undef F_LOAD
#undef Q_FLAG
#undef AFRAG
#undef C_ADD
#undef C_RD
#undef C_GET
#undef C_WAIT
}
__device__ __forceinline__ void role_qkv(const MegaArgs& A, char* smem, const int L, const int h) {
  if (__builtin_amdgcn_readfirstlane(threadIdx.x >> 6) < 4) role_qkv_t<true>(A, smem, L, h);
  else role_qkv_t<false>(A, smem, L, h);
}

constexpr int MEGA_WGS = 256;
constexpr size_t MEGA_LDS = (size_t)(32 * 4 * 64 * 8 + 8 * SMS + 15 * QS + 128 + 16 * 64 * 8 + 8 * SOS + 512) * 2;
static_assert(MEGA_LDS <= 163840 && MEGA_LDS >= 162816 + 256, "lds");
static_assert(MEGA_LDS >= (size_t)AW_CNT + 64, "lds");
static_assert(MEGA_LDS >= (size_t)(8 * 2 * CKN + 8 * 2 * CUN + 4 * 32 * 8) * 2, "lds");
static_assert(MEGA_LDS >= (size_t)2 * 32 * SMS * 2 && MEGA_LDS >= 131072, "lds");
static_assert(131072 >= (size_t)(40 * NST + 32 * 400) * 4 && 131072 + 4 * 392 * 4 <= AW_CNT, "lds");
__global__ __launch_bounds__(512, 2) void mega_kernel(const u16* mem0, const u16* Dskip, char* ws, u16* out, const u16* x0,
                                                      const u16* logAre, const u16* Aim, const u16* Cre, const u16* Cim,
                                                      const u16* logdt, const u16* Wq, const u16* Wk, const u16* Wv,
                                                      const u16* Wo) {
  extern __shared__ __attribute__((aligned(16))) char smem[];
  const int b = __builtin_amdgcn_readfirstlane(role_no());
  const MegaArgs A{mem0, Dskip, ws, out, x0, logAre, Aim, Cre, Cim, logdt, Wq, Wk, Wv, Wo, b};
  {
    const int hi = (b >= 32 && b < 96) ? b - 32 : (b >= 200 && b < 248) ? b - 136 : (b >= 96 && b < 104) ? b + 16 : (b >= 248 ? b - 128 : -1);
    if (hi >= 0) start_kgen(A, smem, hi, b - 248, (b >= 64 && b < 104) || (b >= 232 && b < 240) ? b & 15 : -1);
  }
  if ((EXP_OFF & 1) && b >= 64 && b < 80) return;
  if ((EXP_OFF & 8) && b >= 200 && b < 232) return;
  if ((EXP_OFF & 16) && b >= 32 && b < 64) return;
  if ((EXP_OFF & 32) && b >= 80 && b < 96) return;
  if (b < 64) role_chain(A, smem, b >> 5, b & 31, 0);
  else if (b < 104 || (b >= 232 && b < 240)) role_aw(A, smem, b < 80 ? 0 : b < 96 ? 2 : 1, b & 15);
  else if (b < 168) role_conv(A, smem, b - 104);
  else if (b < 232) role_qkv(A, smem, (b - 168) >> 5, (b - 168) & 31);
  else role_red(A, smem, b - 240);
}

extern "C" void kernel_launch(void* const* d_in, const int* in_sizes, int n_in, void* d_out, int out_size, void* d_ws,
                              size_t ws_size, hipStream_t stream) {
  MegaArgs A;
  A.x0 = (const u16*)d_in[0];
  A.logAre = (const u16*)d_in[1];
  A.Aim = (const u16*)d_in[2];
  A.Cre = (const u16*)d_in[3];
  A.Cim = (const u16*)d_in[4];
  A.logdt = (const u16*)d_in[5];
  A.Dskip = (const u16*)d_in[6];
  A.mem0 = (const u16*)d_in[8];
  A.Wq = (const u16*)d_in[9];
  A.Wk = (const u16*)d_in[10];
  A.Wv = (const u16*)d_in[11];
  A.Wo = (const u16*)d_in[12];
  A.ws = (char*)d_ws;
  A.out = (u16*)d_out;

  (void)hipFuncSetAttribute((const void*)mega_kernel, hipFuncAttributeMaxDynamicSharedMemorySize, (int)MEGA_LDS);
  mega_kernel<<<MEGA_WGS, 512, MEGA_LDS, stream>>>(A.mem0, A.Dskip, A.ws, A.out, A.x0, A.logAre, A.Aim, A.Cre, A.Cim, A.logdt,
                                                   A.Wq, A.Wk, A.Wv, A.Wo);
}
```

```cpp
#include <hip/hip_runtime.h>
#include <hip/hip_bf16.h>
#include <stdint.h>

constexpr int L_SEQ = 2048;
constexpr int DM    = 512;
constexpr int NST   = 128;
constexpr int NH    = 32;
constexpr int DH    = 128;
constexpr int HD    = NH * DH;
constexpr int MM    = 8;
constexpr int SEG   = 32;
constexpr int NSEG  = L_SEQ / SEG;
constexpr int LK    = MM + SEG;

typedef unsigned short u16;
using bf16x8 = __attribute__((ext_vector_type(8))) short;
using f32x4  = __attribute__((ext_vector_type(4))) float;
using f32x16 = __attribute__((ext_vector_type(16))) float;

static __device__ __forceinline__ u16 f2bf(float f) {
  unsigned u = __float_as_uint(f);
  u += 0x7fffu + ((u >> 16) & 1u);
  return (u16)(u >> 16);
}
static __device__ __forceinline__ float bf2f(u16 h) { return __uint_as_float(((unsigned)h) << 16); }
static __device__ __forceinline__ float bfr(float f) { return bf2f(f2bf(f)); }

constexpr size_t SZ_X      = (size_t)L_SEQ * DM * 2;
constexpr size_t OFF_KF    = 0;
constexpr size_t OFF_UT    = OFF_KF + (size_t)DM * L_SEQ * 4;
constexpr size_t OFF_YT    = OFF_UT + SZ_X;
constexpr size_t OFF_XA    = OFF_YT + SZ_X;
constexpr size_t OFF_H     = OFF_XA + SZ_X;
constexpr size_t OFF_X2    = OFF_H + SZ_X;
constexpr size_t OFF_U2    = OFF_X2 + SZ_X;
constexpr size_t OFF_X3    = OFF_U2 + SZ_X;
constexpr size_t SZ_QK     = (size_t)L_SEQ * 2 * HD * 2;
constexpr size_t OFF_QK    = OFF_X3 + SZ_X;
constexpr size_t SZ_O      = (size_t)L_SEQ * HD * 2;
constexpr size_t OFF_VF    = OFF_QK + 2 * SZ_QK;
constexpr size_t SZ_PW     = (size_t)2 * NSEG * 16 * SEG * DM * 2;
constexpr size_t OFF_PW    = OFF_VF + 2 * SZ_O;
constexpr size_t SZ_MEM    = (size_t)NSEG * MM * HD * 2;
constexpr size_t OFF_MEMK  = OFF_PW + SZ_PW;
constexpr size_t OFF_MEMV  = OFF_MEMK + 2 * SZ_MEM;
constexpr size_t OFF_GT    = OFF_MEMV + 2 * SZ_MEM;
constexpr size_t SZ_ACC    = (size_t)2 * 32768;
constexpr size_t OFF_Z     = OFF_GT + (size_t)64 * DM * 4;
constexpr size_t OFF_ACC   = OFF_Z;
constexpr unsigned ACC_LO = 0x88000000u, ACC_HI = 0x87fffff7u;
constexpr size_t OFF_FL    = OFF_ACC + 2 * SZ_ACC;
constexpr int FL_FW  = 0;
constexpr int FL_FR  = FL_FW + 2 * 64 * 16 * 32;
constexpr int FL_FC  = FL_FR + 64 * 16 * 32;
constexpr int FL_FQ  = FL_FC + 2 * 64 * 32 * 32;
constexpr int FL_FS  = FL_FQ + 2 * 65 * 32 * 32;
constexpr int FL_FZ  = FL_FS + 4 * 32 * 32;
constexpr int FL_KG  = FL_FZ + 8 * 32;
constexpr int FL_G   = FL_KG + 64 * 32;
constexpr int FL_TK  = FL_G + 4 * 4 * 32;
constexpr int FL_TMO = FL_TK + 2 * 256;
constexpr int FL_END = FL_TMO + 64;
constexpr size_t Z_BYTES   = OFF_FL + (size_t)FL_END * 4 - OFF_Z;
constexpr size_t OFF_TAG   = OFF_Z + Z_BYTES;
constexpr unsigned long long TAG_STEP = 0x9e3779b97f4a7c15ull;
constexpr size_t OFF_G0    = OFF_TAG + 8 * 1024 + 256;
constexpr size_t WS_TOTAL  = OFF_G0 + 8192;
static_assert(Z_BYTES % 256 == 0 && OFF_Z % 256 == 0 && Z_BYTES <= 2 * 128 * 8192, "zero region");
#define REP ((blockIdx.x >> 1) & 3)
static_assert(WS_TOTAL + (4 << 20) <= (size_t)256 * 1024 * 1024, "workspace too large");

constexpr int CKN = 2208;
static_assert((size_t)DM * 2 * CKN * 2 <= (size_t)DM * L_SEQ * 4 + SZ_X, "K buffer");
__device__ __forceinline__ float pos_of_index(int p) {
  if (p < 256) return (float)p;
  if (p < 384) return (float)(256 + 2 * (p - 256));
  if (p < 512) return (float)(512 + 4 * (p - 384));
  if (p < 640) return (float)(1024 + 8 * (p - 512));
  return 2048.0f;
}
__device__ __forceinline__ int index_of_l(int l) {
  float pf = bfr((float)l);
  int p = (int)pf;
  if (p < 256) return p;
  if (p < 512) return 256 + ((p - 256) >> 1);
  if (p < 1024) return 384 + ((p - 512) >> 2);
  if (p < 2048) return 512 + ((p - 1024) >> 3);
  return 640;
}

template <int CTRL> __device__ __forceinline__ float dppr(float x) {
  return __builtin_bit_cast(float, __builtin_amdgcn_update_dpp(0, __builtin_bit_cast(int, x), CTRL, 0xf, 0xf, false));
}
__device__ __forceinline__ float wave_sum(float v) {
  v += dppr<0x121>(v);
  v += dppr<0x122>(v);
  v += dppr<0x124>(v);
  v += dppr<0x128>(v);
  {
    auto r = __builtin_amdgcn_permlane16_swap(__float_as_uint(v), __float_as_uint(v), false, false);
    v = __uint_as_float(r[0]) + __uint_as_float(r[1]);
  }
  {
    auto r = __builtin_amdgcn_permlane32_swap(__float_as_uint(v), __float_as_uint(v), false, false);
    v = __uint_as_float(r[0]) + __uint_as_float(r[1]);
  }
  return v;
}
__device__ __forceinline__ void ln_row8(float (&v)[8]) {
  float s = 0.f;
#pragma unroll
  for (int j = 0; j < 8; ++j) s += v[j];
  s = wave_sum(s);
  const float mean = s * (1.0f / DM);
  float q = 0.f;
#pragma unroll
  for (int j = 0; j < 8; ++j) { float t = v[j] - mean; q += t * t; }
  q = wave_sum(q);
  const float var = q * (1.0f / DM);
  const float mb = bfr(mean);
  const float sd = bfr(sqrtf(bfr(bfr(var) + 1e-5f)));
#pragma unroll
  for (int j = 0; j < 8; ++j) v[j] = bfr(bfr(v[j] - mb) / sd);
}

#define RLX_AGENT __ATOMIC_RELAXED, __HIP_MEMORY_SCOPE_AGENT
typedef __attribute__((address_space(1))) unsigned gu32;
typedef unsigned v4u __attribute__((ext_vector_type(4)));
constexpr int AUX_SC1 = 16;
constexpr unsigned SPIN_CAP = 1u << 18;

__device__ __forceinline__ bool wait_flags(const unsigned* flags, int lane, int nfl, unsigned want, unsigned* tmo) {
  for (unsigned spins = 0;; ++spins) {
    unsigned v = want;
    if (lane < nfl) v = __hip_atomic_load((gu32*)flags + lane * 32, RLX_AGENT);
    if (__all(v >= want)) break;
    if (spins > SPIN_CAP) {
      if (lane == 0) __hip_atomic_store((gu32*)tmo, 1u, RLX_AGENT);
      return false;
    }
  }
  __builtin_amdgcn_fence(__ATOMIC_ACQUIRE, "wavefront");
  return true;
}

#define STAMP(i)
#define STAMP_DUMP()
#define WSTAMP(i)
template <int CTRL> __device__ __forceinline__ float dppf(float old, float x) {
  return __builtin_bit_cast(float, __builtin_amdgcn_update_dpp(__builtin_bit_cast(int, old), __builtin_bit_cast(int, x), CTRL, 0xf, 0xf, false));
}
__device__ __forceinline__ float wave_max_dpp(float x) {
  const float ninf = -3e38f;
  x = fmaxf(x, dppf<0x111>(ninf, x));
  x = fmaxf(x, dppf<0x112>(ninf, x));
  x = fmaxf(x, dppf<0x114>(ninf, x));
  x = fmaxf(x, dppf<0x118>(ninf, x));
  x = fmaxf(x, dppf<0x142>(ninf, x));
  x = fmaxf(x, dppf<0x143>(ninf, x));
  return __builtin_bit_cast(float, __builtin_amdgcn_readlane(__builtin_bit_cast(int, x), 63));
}
__device__ __forceinline__ float wave_sum_dpp(float x) {
  x += dppf<0x111>(0.f, x);
  x += dppf<0x112>(0.f, x);
  x += dppf<0x114>(0.f, x);
  x += dppf<0x118>(0.f, x);
  x += dppf<0x142>(0.f, x);
  x += dppf<0x143>(0.f, x);
  return __builtin_bit_cast(float, __builtin_amdgcn_readlane(__builtin_bit_cast(int, x), 63));
}
constexpr int SMS = 528;
constexpr int QS = 144;
constexpr int VS = 80;
constexpr int SOS = 144;
#define VAR_NREP 4
constexpr int NREP = VAR_NREP;
__device__ __forceinline__ float xsum16(float x) {
  auto r = __builtin_amdgcn_permlane16_swap(__float_as_uint(x), __float_as_uint(x), false, false);
  return __uint_as_float(r[0]) + __uint_as_float(r[1]);
}
__device__ __forceinline__ float xsum32(float x) {
  auto r = __builtin_amdgcn_permlane32_swap(__float_as_uint(x), __float_as_uint(x), false, false);
  return __uint_as_float(r[0]) + __uint_as_float(r[1]);
}
__device__ __forceinline__ float xmax16(float x) {
  auto r = __builtin_amdgcn_permlane16_swap(__float_as_uint(x), __float_as_uint(x), false, false);
  return fmaxf(__uint_as_float(r[0]), __uint_as_float(r[1]));
}
__device__ __forceinline__ float xmax32(float x) {
  auto r = __builtin_amdgcn_permlane32_swap(__float_as_uint(x), __float_as_uint(x), false, false);
  return fmaxf(__uint_as_float(r[0]), __uint_as_float(r[1]));
}
__device__ __forceinline__ float red_mod4(float x) {
  x += dppf<0x124>(0.f, x);
  x += dppf<0x128>(0.f, x);
  return xsum32(xsum16(x));
}
typedef __bf16 bf16x2_t __attribute__((ext_vector_type(2)));
typedef float f32x2_t __attribute__((ext_vector_type(2)));
__device__ __forceinline__ unsigned pk2(float a, float b) {
  f32x2_t v = {a, b};
  bf16x2_t r = __builtin_convertvector(v, bf16x2_t);
  return __builtin_bit_cast(unsigned, r);
}
__device__ __forceinline__ float dot2sq(const unsigned x, const float acc) {
  return __builtin_amdgcn_fdot2_f32_bf16(__builtin_bit_cast(bf16x2_t, x), __builtin_bit_cast(bf16x2_t, x), acc, false);
}
__device__ __forceinline__ float lo16(unsigned u) { return __uint_as_float(u << 16); }
__device__ __forceinline__ float hi16(unsigned u) { return __uint_as_float(u & 0xffff0000u); }
__device__ __forceinline__ void scale_pair(float a, float b, float scale, float& oa, float& ob) {
  unsigned u = pk2(a, b);
  unsigned v = pk2(lo16(u) * scale, hi16(u) * scale);
  oa = lo16(v);
  ob = hi16(v);
}

#define XSPLIT 3
__device__ __forceinline__ int role_no() {
  const int b = blockIdx.x, x = b & 7, j = b >> 3;
  if (XSPLIT >= 3) {
    const int y = XSPLIT == 5 ? x & 3 : x >> 1;
    if (XSPLIT == 5 ? x < 4 : !(x & 1)) {
      if (j >= 16) return 104 + 16 * y + j - 16;
      return XSPLIT == 4 ? 16 * y + j : (j & 8) * 4 + 8 * y + (j & 7);
    }
    if (j < 16) return 168 + (j & 8) * 4 + 8 * y + (j & 7);
    const int i = 4 * y + (j & 3);
    return j < 20 ? 64 + i : j < 24 ? (i < 8 ? 96 + i : 224 + i) : j < 28 ? 80 + i : 240 + i;
  }
  const int L = x >> 2, xx = x & 3;
  if (j < 8) return 32 * L + 8 * xx + j;
  if (j < 12) return 64 + 16 * L + 4 * xx + j - 8;
  if (j < 16) { const int i = 4 * xx + j - 12; return L ? 240 + i : i < 8 ? 96 + i : 224 + i; }
  if (j < 24) return 104 + 8 * x + j - 16;
  return 168 + 32 * L + 8 * xx + j - 24;
}
#define MSTAMP()
#define WSTAMP2()
#define SST(k)
#define TSR(rn, k, ss, t)
#define TS(k, ss, t)
#define FSD
#define FS(k)
#define FD(ss)
struct MegaArgs {
  const u16* mem0;
  const u16* Dskip;
  char* ws;
  u16* out;
  const u16* x0;
  const u16 *logAre, *Aim, *Cre, *Cim, *logdt;
  const u16 *Wq, *Wk, *Wv, *Wo;
  int rn;
};

__device__ __forceinline__ int lane_id() {
  int l;
  asm volatile("v_mbcnt_lo_u32_b32 %0, -1, 0\n\tv_mbcnt_hi_u32_b32 %0, -1, %0" : "=v"(l));
  return l;
}
__device__ __forceinline__ bool spin_timeout(unsigned spins, unsigned* tmo) {
  if (spins > SPIN_CAP) {
    if (lane_id() == 0) __hip_atomic_store((gu32*)tmo, 1u, RLX_AGENT);
    return true;
  }
  return false;
}
#define PSWH (h & 2)
#define RR 0
#define RQM 63
#define RPM 63
#define CD0 100
#define APN 9
#define APL 300
#define APA 120
#define APB 80
#define APP 208
#define CD 30
#define CDS 30
#define CDL 0
#define XG(h) ((h) >> 3)
#define GP 230
#define GR 50
#define GM 0
#define ROT (4 * (h & 1))
#define CPRIO 1
#define P1PRIO 3
#define QLEAD 2
#define POLL_SLEEP 12
#define SLP_C POLL_SLEEP
#define SLP_A POLL_SLEEP
#define SLP_W POLL_SLEEP
#define SLP_R 4
#define SLP_V 4
#define SLP_Q 4
#define SLP_M 4
#define AWPRIO 2
#define SLP_S 8
#define SPOS 4
#define CVPRE 0
#define QHV 0
#define QCHK 0
#define LEADN 99
#define GATE_V 140
#define GATE_R 380
#define GATE_N 440
#define AWAHEAD 1
#define GATE_M 150
#define GATE_M2 380
#define QPACE 0
#define QPS 6
#define QPL 100
#define GATE_Q 150
#define SLP_Q1 24
#define QPF 3
#define QPV 6
#define QVP 3
#define QP0 2
#define QP1 0
#define QFP 15
#define QUP 7
#define QEP 3
#define AWD 11
#define GPER 200
#define GMA 60
#define GMR 60
#define GSTEP 30
#define GATE_RT 200
#define GATE_1 160
#define N1S 20
#define N1E 36
#define PRI_A 3
#define PRI_WA 2
#define PRI_WB 1
#define AWSKIP 3
#define PRI_0 3
#define AWDEF 1
#define SLP_F 2
#define EXP_OFF 0
#define AWSPEC 1
#define CVTHR 48
#define NR_C 8
#define NR_R 8
#define NR_W 8
#define NR_N 2
#define QAHEAD 0
#define NS0 4
#define SLP_N POLL_SLEEP
template <int SLP, int WPL>
__device__ __forceinline__ bool wait_n(const unsigned* f, int n, unsigned* tmo) {
  const int l = lane_id();
  for (unsigned spins = 0;; ++spins) {
    unsigned v = 1u;
    if (l < n) v = __hip_atomic_load((gu32*)f + (l / WPL) * 32 + l % WPL, RLX_AGENT);
    if (__all(v != 0u)) break;
    if (spin_timeout(spins, tmo)) return false;
    __builtin_amdgcn_s_sleep(SLP);
  }
  asm volatile("" ::: "memory");
  return true;
}
__device__ __forceinline__ unsigned now10() { return (unsigned)__builtin_amdgcn_s_memrealtime(); }
__device__ __forceinline__ void gate(const unsigned t) {
  for (int i = 0; i < 64; ++i) {
    const int d = (int)(t - now10());
    if (d <= 0) break;
    if (d > 100) __builtin_amdgcn_s_sleep(32);
    else if (d > 25) __builtin_amdgcn_s_sleep(8);
    else __builtin_amdgcn_s_sleep(2);
  }
}
template <int SLP, int WPL>
__device__ __forceinline__ bool grp_wait(const bool leader, volatile unsigned* sw, const unsigned val, const unsigned* f, int n,
                                         unsigned* tmo, const unsigned t) {
  unsigned spins = 0;
  gate(t);
  if (leader) {
    const int l = lane_id();
    for (;; ++spins) {
      unsigned v = 1u;
      if (l < n) v = __hip_atomic_load((gu32*)f + (l / WPL) * 32 + l % WPL, RLX_AGENT);
      if (__all(v != 0u) || spin_timeout(spins, tmo)) break;
      __builtin_amdgcn_s_sleep(SLP);
    }
    if ((threadIdx.x & 63) == 0) *sw = 2u * val + (spins ? 1u : 0u);
  } else {
    unsigned v;
    for (; (v = __builtin_amdgcn_readfirstlane(*sw)) < 2u * val; ++spins) {
      if (spins > 64u * SPIN_CAP) break;
      __builtin_amdgcn_s_sleep(SLP_F);
    }
    spins = v & 1u;
  }
  asm volatile("" ::: "memory");
  return spins != 0u;
}
template <int SLP, int WPL>
__device__ __forceinline__ bool wg_wait(const unsigned* f, int n, unsigned* tmo) {
  if (threadIdx.x < 64) (void)wait_n<SLP, WPL>(f, n, tmo);
  __syncthreads();
  asm volatile("" ::: "memory");
  return true;
}
__device__ __forceinline__ void set_flag(unsigned* f) { __hip_atomic_store((gu32*)f, 1u, RLX_AGENT); }
#define DRAIN() asm volatile("s_waitcnt vmcnt(0)" ::: "memory")
#define MKRS(ptr, bytes) __builtin_amdgcn_make_buffer_rsrc((void*)(ptr), 0, (int)(bytes), 0x00020000)
typedef unsigned v2u __attribute__((ext_vector_type(2)));
#define LD128(rs, voff, soff) __builtin_amdgcn_raw_buffer_load_b128(rs, voff, soff, AUX_SC1)
#define LD64(rs, voff, soff) __builtin_amdgcn_raw_buffer_load_b64(rs, voff, soff, AUX_SC1)
#define ST128(v, rs, voff, soff)                                              \
  do {                                                                        \
    const v4u st_d_ = (v);                                                    \
    __builtin_amdgcn_raw_buffer_store_b128(st_d_, rs, voff, soff, AUX_SC1);   \
    asm volatile("s_nop 1" ::"v"(st_d_) : "memory");                          \
  } while (0)
#define ST64(v, rs, voff, soff) __builtin_amdgcn_raw_buffer_store_b64(v, rs, voff, soff, AUX_SC1)
#define BF8(x) __builtin_bit_cast(bf16x8, x)
#define MFMA16(a, b, c) __builtin_amdgcn_mfma_f32_16x16x32_bf16(a, b, c, 0, 0, 0)
typedef short bf16x4 __attribute__((ext_vector_type(4)));
#define MFMA16K(a, b, c) \
  __builtin_amdgcn_mfma_f32_16x16x16bf16_1k(__builtin_bit_cast(bf16x4, a), __builtin_bit_cast(bf16x4, b), c, 0, 0, 0)
__device__ __forceinline__ float fmax_nn(float a, float b) { return __builtin_elementwise_maximum(a, b); }
typedef __bf16 bfv8 __attribute__((ext_vector_type(8)));
typedef __bf16 bfv16 __attribute__((ext_vector_type(16)));
typedef unsigned v8u __attribute__((ext_vector_type(8)));
#define SMF(a, b, c, idx) \
  __builtin_amdgcn_smfmac_f32_16x16x64_bf16(__builtin_bit_cast(bfv8, a), __builtin_bit_cast(bfv16, b), c, idx, 0, 0)
__device__ __forceinline__ float swap_add32(float a, float b) {
  auto r = __builtin_amdgcn_permlane32_swap(__float_as_uint(a), __float_as_uint(b), false, false);
  return __uint_as_float(r[0]) + __uint_as_float(r[1]);
}
__device__ __forceinline__ float swap_add16(float a, float b) {
  auto r = __builtin_amdgcn_permlane16_swap(__float_as_uint(a), __float_as_uint(b), false, false);
  return __uint_as_float(r[0]) + __uint_as_float(r[1]);
}
__device__ __forceinline__ v4u smf_fix(const v4u d) {
  auto x = __builtin_amdgcn_permlane16_swap(d.x, d.z, false, false);
  auto y = __builtin_amdgcn_permlane16_swap(d.y, d.w, false, false);
  return v4u{x[0], x[1], y[0], y[1]};
}
__device__ __forceinline__ v8u smf_bfrag(const u16* p) {
  const v2u a = *reinterpret_cast<const v2u*>(p), b = *reinterpret_cast<const v2u*>(p + 8);
  const v2u c = *reinterpret_cast<const v2u*>(p + 32), d = *reinterpret_cast<const v2u*>(p + 40);
  return v8u{a.x, b.x, a.y, b.y, c.x, d.x, c.y, d.y};
}

#define VAR_PPA 0
#define VAR_PPB 0
#define VAR_THR 0
#define VAR_THR2 0
#define CV_LEAD 100
#define VAR_EARLY 1
#define VAR_DRAIN 1
#define VAR_D1 0
#define VAR_D2 0
#define VAR_DR2 1
#define VAR_S1 0
#define VAR_S2 0
typedef __attribute__((address_space(1))) unsigned long long gu64;
typedef __attribute__((address_space(1))) char gch;
__device__ __forceinline__ int cvt_rpi(float x) {
  int r;
  asm("v_cvt_rpi_i32_f32 %0, %1" : "=v"(r) : "v"(x));
  return r;
}
typedef short v4s __attribute__((ext_vector_type(4)));
typedef __attribute__((address_space(3))) v4s* lp4s;
__device__ __forceinline__ void glds16(const void* sbase, const unsigned voff, const unsigned lds) {
  unsigned keep;
  asm volatile("s_nop 4\n\ts_mov_b32 %0, m0\n\ts_mov_b32 m0, %3\n\ts_nop 0\n\tglobal_load_lds_dwordx4 %1, %2\n\ts_mov_b32 m0, %0"
               : "=&s"(keep)
               : "v"(voff), "s"(sbase), "s"(lds)
               : "memory");
}
__device__ __forceinline__ void glds16c(const void* sbase, const unsigned voff, const unsigned lds) {
  unsigned keep;
  asm volatile("s_nop 4\n\ts_mov_b32 %0, m0\n\ts_mov_b32 m0, %3\n\ts_nop 0\n\tglobal_load_lds_dwordx4 %1, %2 sc1\n\ts_mov_b32 m0, %0"
               : "=&s"(keep)
               : "v"(voff), "s"(sbase), "s"(lds)
               : "memory");
}
__device__ __forceinline__ v2u trd(const unsigned a) {
  return __builtin_bit_cast(v2u, __builtin_amdgcn_ds_read_tr16_b64_v4i16((lp4s)(unsigned long)a));
}
__device__ __forceinline__ unsigned lds_base(char* smem) { return (unsigned)(unsigned long)(__attribute__((address_space(3))) char*)smem; }
#define RAW_BAR()                                           \
  do {                                                      \
    asm volatile("s_waitcnt lgkmcnt(0)" ::: "memory");      \
    __builtin_amdgcn_s_barrier();                           \
    asm volatile("" ::: "memory");                          \
  } while (0)
#define WAIT_VM(n) asm volatile("s_waitcnt vmcnt(" #n ")" ::: "memory")
template <bool SP>
__device__ __forceinline__ void wqkv_issue(const u16* Wm, const int hf, const int h, const int w, const int l, const unsigned sb) {
  const char* base = (const char*)Wm + (size_t)(256 * hf + 32 * w) * (HD * 2) + h * (DH * 2);
#pragma unroll
  for (int i = 0; i < 8; ++i) {
    const int r = l >> 4;
    const int f = SP ? ((r & 1) | ((i & 1) << 1) | (((i >> 1) & 1) << 2)) : (r | (((i >> 1) & 1) << 2));
    glds16(base, (unsigned)((4 * i + r) * (HD * 2) + (((l & 15) ^ (f << 1)) << 4)), sb + w * 16384 + hf * 8192 + i * 1024);
  }
}

#define PFW 1
__device__ __forceinline__ void pull_weights(const MegaArgs& A, char* smem, const int slot, const int slots) {
  if (!PFW) return;
  const int x = blockIdx.x & 7, j = blockIdx.x >> 3, h = 8 * (x >> 1) + (j & 7), k = (j >> 3) - 1, l = lane_id();
  const bool odd = x & 1, wo = !odd && k == 1;
  const unsigned voff = wo ? l * 64 : (l >> 2) * (HD * 2) + (l & 3) * 64;
  const unsigned dst = lds_base(smem) + 162816;
  unsigned long long pk = (unsigned long long)A.Wk, pq = (unsigned long long)A.Wq, pv = (unsigned long long)A.Wv, po = (unsigned long long)A.Wo;
  asm volatile("" : "+s"(pk), "+s"(pq), "+s"(pv), "+s"(po));
  for (int i = odd ? slot * 3 + k : slot; (PFW & 1) && i < (odd ? 64 : 32); i += odd ? 3 * slots : slots) {
    const unsigned long long m = odd ? (i < 32 ? pk : pv) : (k == 0 ? pq : pk);
    const unsigned long long b = wo ? po + h * (DH * DM * 2) + i * 4096 : m + (i & 31) * (16 * HD * 2) + h * (DH * 2);
    unsigned keep;
    asm volatile("s_nop 4\n\ts_mov_b32 %0, m0\n\ts_mov_b32 m0, %3\n\ts_nop 0\n\tglobal_load_lds_dword %1, %2\n\ts_mov_b32 m0, %0"
                 : "=&s"(keep)
                 : "v"(voff), "s"(b), "s"(dst)
                 : "memory");
  }
  if (PFW & 2) {
    const unsigned long long b = po + (size_t)h * (DH * DM * 2);
    for (int i = slot * 3 + k; i < 32; i += 3 * slots) {
      unsigned keep;
      asm volatile("s_nop 4\n\ts_mov_b32 %0, m0\n\ts_mov_b32 m0, %3\n\ts_nop 0\n\tglobal_load_lds_dword %1, %2\n\ts_mov_b32 m0, %0"
                   : "=&s"(keep)
                   : "v"(l * 64), "s"(b + i * 4096), "s"(dst)
                   : "memory");
    }
  }
}
__device__ __forceinline__ void wo_issue(const u16* Wo, const int a, const int st, const unsigned sb) {
  const int w = __builtin_amdgcn_readfirstlane(threadIdx.x >> 6), l = lane_id(), c = l & 15, g = l >> 4;
  const char* base = (const char*)Wo + ((size_t)((2 * a + (st >> 1)) * DH + 64 * (w & 1) + 32 * (st & 1)) * DM + 128 * (w >> 1)) * 2;
#pragma unroll
  for (int i = 0; i < 8; ++i) {
    const int fd = g | ((i & 1) << 2);
    glds16(base, (unsigned)((4 * i + g) * (DM * 2) + ((c ^ (fd << 1)) << 4)), sb + (st & 1) * 65536 + w * 8192 + i * 1024);
  }
}
__device__ __forceinline__ bool zero_seen(const v4u t, const v2u e) {
  const unsigned long long m = (((unsigned long long)e.y << 32) | e.x) + TAG_STEP;
  const unsigned lo = (unsigned)m, hi = (unsigned)(m >> 32);
  return __all(((t.x ^ lo) | (t.y ^ hi) | (t.z ^ lo) | (t.w ^ hi)) == 0u);
}
__device__ __forceinline__ void zero_wait(char* ws) {
  const __amdgpu_buffer_rsrc_t rs = MKRS(ws + OFF_TAG, 8192 + 8);
  for (unsigned spins = 0; spins < 16u * SPIN_CAP; ++spins) {
    const v4u t = LD128(rs, lane_id() * 16, (blockIdx.x & 7) * 1024);
    const v2u e = LD64(rs, 0, 8192);
    if (zero_seen(t, e)) break;
    asm volatile("s_sleep 4" ::: "memory");
  }
  asm volatile("" ::: "memory");
}
__device__ __forceinline__ unsigned row_stats(const v4u raw) {
  float v[8];
#pragma unroll
  for (int e = 0; e < 4; ++e) { v[2 * e] = lo16(raw[e]); v[2 * e + 1] = hi16(raw[e]); }
  float s = 0.f;
#pragma unroll
  for (int e = 0; e < 8; ++e) s += v[e];
  const float mean = wave_sum(s) * (1.0f / DM);
  float q = 0.f;
#pragma unroll
  for (int e = 0; e < 8; ++e) { const float t = v[e] - mean; q += t * t; }
  const float var = wave_sum(q) * (1.0f / DM);
  const float sd = bfr(sqrtf(bfr(bfr(var) + 1e-5f)));
  return (unsigned)f2bf(mean) | ((unsigned)f2bf(sd) << 16);
}
__device__ __forceinline__ void start_kgen(const MegaArgs& A, char* smem, const int hi, const int xi, const int aw) {
  const int tid = threadIdx.x, j = tid >> 7, u = tid & 127, d = 4 * hi + j;
  const int w = __builtin_amdgcn_readfirstlane(tid >> 6), l = tid & 63;
  float* prm = reinterpret_cast<float*>(smem) + j * 10 * NST;
  float* part = reinterpret_cast<float*>(smem) + 40 * NST + j * 8 * 400;
  unsigned* kd = reinterpret_cast<unsigned*>(smem + 131072) + j * 392;
  v4u raw[8], ra, rb;
  const int r0 = 16 * xi + 2 * w;
  if (xi >= 0) {
    ra = *reinterpret_cast<const v4u*>(A.x0 + (size_t)r0 * DM + l * 8);
    rb = *reinterpret_cast<const v4u*>(A.x0 + (size_t)(r0 + 1) * DM + l * 8);
  }
  const u16 p0 = A.logdt[d], p1 = A.logAre[d * NST + u], p2 = A.Aim[d * NST + u], p3 = A.Cre[d * NST + u], p4 = A.Cim[d * NST + u];
  if (w >= 6) {
#pragma unroll
    for (int i = 0; i < 8; ++i) raw[i] = *reinterpret_cast<const v4u*>(A.x0 + (size_t)(16 * hi + 8 * (w - 6) + i) * DM + l * 8);
  }
  const v2u en = LD64(MKRS(A.ws + OFF_TAG, 8192 + 8), 0, 8192);
  {
    const __amdgpu_buffer_rsrc_t rz = MKRS(A.ws + OFF_Z, Z_BYTES);
    const unsigned i0 = hi < 16 ? ACC_LO : 0u, i1 = hi < 16 ? ACC_HI : 0u;
    ST128((v4u{i0, i1, i0, i1}), rz, tid * 16, hi * 8192);
    ST128((v4u{0u, 0u, 0u, 0u}), rz, tid * 16, (128 + hi) * 8192);
  }
  if (xi >= 0) {
    const unsigned sa = row_stats(ra), sb = row_stats(rb), st = (l & 1) ? sb : sa;
    const unsigned long long m = (((unsigned long long)en.y << 32) | en.x) + TAG_STEP;
    ST128((v4u{st, (unsigned)m, st, (unsigned)(m >> 32)}), MKRS(A.ws + OFF_G0, 8192), (l >> 1) * 2048 + (l & 1) * 16, r0 * 16);
    SST(11);
  }
  {
    const float dt = bfr(expf(bf2f(p0)));
    const float ar = -bfr(expf(bf2f(p1))) * dt, ai = bf2f(p2) * dt;
    prm[u] = ar;
    prm[NST + u] = ai;
    prm[2 * NST + u] = bf2f(p3) * dt;
    prm[3 * NST + u] = bf2f(p4) * dt;
#pragma unroll
    for (int cls = 1; cls < 4; ++cls) {
      const float st = (float)(1 << cls);
      const float e = expf(ar * st);
      float sn, cs;
      sincosf(ai * st, &sn, &cs);
      prm[(2 + 2 * cls) * NST + u] = e * cs;
      prm[(3 + 2 * cls) * NST + u] = e * sn;
    }
  }
  __builtin_amdgcn_s_waitcnt(0x0F70);
  __syncthreads();
  SST(1);
  if (w >= 6) {
    if (w == 7 && l < 8)
      __hip_atomic_store((gu64*)(A.ws + OFF_TAG) + l * 128 + hi, (((unsigned long long)en.y << 32) | en.x) + TAG_STEP, RLX_AGENT);
    __builtin_amdgcn_s_setprio(3);
    if (hi >= 32) pull_weights(A, smem, w - 6, 2);
    v4u wr[32];
    const u16* wop = A.Wo + (size_t)(hi * DH + 64 * (w - 6)) * DM + l * 8;
    if (hi < 32) {
#pragma unroll
      for (int i = 0; i < 32; ++i) wr[i] = *reinterpret_cast<const v4u*>(wop + i * DM);
    }
    unsigned stw = 0u;
#pragma unroll
    for (int i = 0; i < 8; ++i) {
      const unsigned t = row_stats(raw[i]);
      if (l == i) stw = t;
    }
    const int sx = 2 * hi + w - 6;
    if (l < 8) __hip_atomic_store((gu32*)(A.ws + OFF_GT) + 8 * sx + l, stw, RLX_AGENT);
    zero_wait(A.ws);
    DRAIN();
    if (l < 4) set_flag((unsigned*)(A.ws + OFF_FL) + FL_FS + (l * 32 + (sx >> 3)) * 32 + (sx & 7));
    if (hi < 32) {
      float c0 = 0.f, c1 = 0.f, c2 = 0.f, c3 = 0.f;
#pragma unroll
      for (int i = 0; i < 64; ++i) {
        const v4u x = wr[i & 31];
        if (i < 32) wr[i] = *reinterpret_cast<const v4u*>(wop + (32 + i) * DM);
        c0 = dot2sq(x.x, c0); c1 = dot2sq(x.y, c1); c2 = dot2sq(x.z, c2); c3 = dot2sq(x.w, c3);
      }
      const float m = wave_max_dpp(fmaxf(fmaxf(c0, c1), fmaxf(c2, c3)));
      if (l < 4) __hip_atomic_store((gu32*)(A.ws + OFF_FL) + FL_G + (l * 4 + w - 5) * 32 + hi, __float_as_uint(m) | 1u, RLX_AGENT);
    }
    __builtin_amdgcn_s_setprio(0);
  } else {
    const int jm = tid / 96, um = tid % 96;
    const int r = um % 12, ng = um / 12;
    const float* prm = reinterpret_cast<float*>(smem) + jm * 10 * NST;
    float* part = reinterpret_cast<float*>(smem) + 40 * NST + jm * 8 * 400;
    const float* pw = prm + (4 + 2 * (r >> 2)) * NST;
    const float p0 = pos_of_index(256 + 32 * r);
    f32x2_t acc[33];
#pragma unroll
    for (int i = 0; i < 33; ++i) acc[i] = f32x2_t{0.f, 0.f};
#pragma unroll 1
    for (int bt = 0; bt < 4; ++bt) {
      const int n = 16 * ng + 4 * bt;
      const f32x4 ar = *reinterpret_cast<const f32x4*>(prm + n), ai = *reinterpret_cast<const f32x4*>(prm + NST + n);
      const f32x4 cr = *reinterpret_cast<const f32x4*>(prm + 2 * NST + n), ci = *reinterpret_cast<const f32x4*>(prm + 3 * NST + n);
      const f32x4 wr = *reinterpret_cast<const f32x4*>(pw + n), wi = *reinterpret_cast<const f32x4*>(pw + NST + n);
      float yr0[4], yi0[4];
#pragma unroll
      for (int k = 0; k < 4; ++k) {
        const float e = __expf(ar[k] * p0);
        float sn, cs;
        __sincosf(ai[k] * p0, &sn, &cs);
        const float zr = e * cs, zi = e * sn;
        yr0[k] = cr[k] * zr - ci[k] * zi;
        yi0[k] = cr[k] * zi + ci[k] * zr;
      }
      f32x2_t yra = {yr0[0], yr0[1]}, yia = {yi0[0], yi0[1]}, wra = {wr[0], wr[1]}, wia = {wi[0], wi[1]};
      f32x2_t yrb = {yr0[2], yr0[3]}, yib = {yi0[2], yi0[3]}, wrb = {wr[2], wr[3]}, wib = {wi[2], wi[3]};
#pragma unroll
      for (int i = 0; i < 33; ++i) {
        acc[i] += yra + yrb;
        const f32x2_t ta = yra * wra - yia * wia, tb = yrb * wrb - yib * wib;
        yia = yra * wia + yia * wra;
        yib = yrb * wib + yib * wrb;
        yra = ta;
        yrb = tb;
      }
    }
#pragma unroll
    for (int i = 0; i < 33; ++i) part[ng * 400 + 33 * r + i] = acc[i].x + acc[i].y;
  }
  __syncthreads();
  SST(2);
  for (int i = u; i < 385; i += 128) {
    const int o = i < 384 ? 33 * (i >> 5) + (i & 31) : 33 * 11 + 32;
    float v = 0.f;
#pragma unroll
    for (int g = 0; g < 8; ++g) v += part[g * 400 + o];
    v *= 2.0f;
    kd[i] = pk2(v, v - lo16(pk2(v, v)));
  }
  if (u == 1) kd[385] = 0u;
  __syncthreads();
  if (aw >= 0) {
    wo_issue(A.Wo, aw, 0, lds_base(smem));
    wo_issue(A.Wo, aw, 1, lds_base(smem));
  }
  const __amdgpu_buffer_rsrc_t rs = MKRS(A.ws + OFF_KF, DM * 2 * CKN * 2);
  for (int un = u; un < 229; un += 128) {
    v4u oh, ol;
#pragma unroll
    for (int e = 0; e < 4; ++e) {
      const int lag = 2080 - (8 * un + 2 * e);
      const unsigned pb = pk2((float)lag, (float)(lag - 1));
      const unsigned k0 = kd[(unsigned)(lag - 256) < L_SEQ - 256u ? (pb & 0xffffu) - 0x4380u : 385u];
      const unsigned k1 = kd[(unsigned)(lag - 257) < L_SEQ - 256u ? (pb >> 16) - 0x4380u : 385u];
      oh[e] = __builtin_amdgcn_perm(k1, k0, 0x05040100u);
      ol[e] = __builtin_amdgcn_perm(k1, k0, 0x07060302u);
    }
    ST128(oh, rs, (d * 2 * CKN + 8 * un) * 2, 0);
    ST128(ol, rs, (d * 2 * CKN + CKN + 8 * un) * 2, 0);
  }
  DRAIN();
  __syncthreads();
  if (tid == 0) set_flag((unsigned*)(A.ws + OFF_FL) + FL_KG + (hi >> 1) * 32 + (hi & 1));
  SST(3);
}

constexpr int C_SM = 131072, C_SO = C_SM + 8192, C_KT = C_SO + 2048, C_X = C_KT + 16384, C_SQ = C_X + 1024;
constexpr int C_SK = C_SQ + 2432;
static_assert(C_SK + 8 * QS * 2 <= 163808, "lds");
#define CHAIN_ASM_ATT \
  "s_mov_b32 s66, %[d0]\n" \
  "s_mov_b32 s94, 576\n" \
  "s_mov_b32 s95, 288\n" \
  "s_mov_b32 s84, 0x43400000\n" \
  "s_mov_b32 s85, 0x43400000\n" \
  "s_mov_b32 s92, 0x88000000\n" \
  "s_mov_b32 s93, 0x87fffff7\n" \
  "s_mov_b32 s78, 0x3e0293ee\n" \
  "s_mov_b32 s76, 0x33333333\n" \
  "s_mov_b32 s77, 0x33333333\n" \
  "s_add_u32 s58, s56, s60\n" \
  "s_addc_u32 s59, s57, 0\n" \
  "s_xor_b32 s38, s35, 0\n" \
  "s_add_u32 s26, s58, s38\n" \
  "s_addc_u32 s27, s59, 0\n" \
  "s_xor_b32 s38, s35, 1024\n" \
  "s_add_u32 s28, s58, s38\n" \
  "s_addc_u32 s29, s59, 0\n" \
  "s_xor_b32 s38, s35, 2048\n" \
  "s_add_u32 s30, s58, s38\n" \
  "s_addc_u32 s31, s59, 0\n" \
  "s_xor_b32 s38, s35, 3072\n" \
  "s_add_u32 s32, s58, s38\n" \
  "s_addc_u32 s33, s59, 0\n" \
  "s_lshr_b32 s98, s60, 2\n" \
  "s_add_u32 s98, s98, s74\n" \
  "s_add_u32 s98, s98, 131072\n" \
  "s_lshl_b32 s97, s73, 5\n" \
  "s_add_u32 s97, s97, s74\n" \
  "s_add_u32 s97, s97, 161152\n" \
  "s_lshl_b32 s99, s73, 10\n" \
  "v_mbcnt_lo_u32_b32 v48, -1, 0\n" \
  "v_mbcnt_hi_u32_b32 v48, -1, v48\n" \
  "v_and_b32 v49, 15, v48\n" \
  "v_lshl_add_u32 v9, v49, 1, s97\n" \
  "v_lshrrev_b32 v49, 4, v48\n" \
  "v_mad_u32_u24 v9, v49, s94, v9\n" \
  "v_add_u32 v7, s99, v1\n" \
  "v_mov_b64 v[12:13], 0\n" \
  "v_mov_b64 v[14:15], 0\n" \
  "v_mov_b64 v[16:17], 0\n" \
  "v_mov_b64 v[18:19], 0\n" \
  "v_mov_b64 v[20:21], 0\n" \
  "v_mov_b64 v[22:23], 0\n" \
  "v_mov_b32 v24, 0x44870000\n" \
  "v_mul_f32 v24, s72, v24\n" \
  "s_nop 0\n" \
  "v_readfirstlane_b32 s86, v24\n" \
  "s_lshr_b32 s86, s86, 23\n" \
  "s_addk_i32 s86, 0xff83\n" \
  "s_ashr_i32 s86, s86, 1\n" \
  "s_max_i32 s86, s86, -20\n" \
  "s_min_i32 s86, s86, 100\n" \
  "s_mov_b32 s82, s86\n" \
  "s_lshr_b32 s83, s72, 23\n" \
  "s_sub_i32 s83, s83, 242\n" \
  "s_lshl_b32 s88, s73, 10\n" \
  "v_add_u32 v24, s88, v1\n" \
  "ds_read_b128 v[32:35], v24 offset:0\n" \
  "s_waitcnt lgkmcnt(0)\n" \
  "v_mov_b32 v45, 0\n" \
  "v_dot2c_f32_bf16 v45, v32, v32\n" \
  "v_dot2c_f32_bf16 v45, v33, v33\n" \
  "v_dot2c_f32_bf16 v45, v34, v34\n" \
  "v_dot2c_f32_bf16 v45, v35, v35\n" \
  "s_add_u32 s88, s74, 157760\n" \
  "v_mov_b32 v44, s88\n" \
  "s_nop 0\n" \
  "v_cmpx_lt_f32 vcc, 2.0, v45\n" \
  "ds_max_u32 v44, v45\n" \
  "s_mov_b64 exec, -1\n" \
  ".p2align 6\n" \
  ".LCA_AL_%=:\n" \
  ".LCA_TOP_%=:\n" \
  "s_waitcnt lgkmcnt(0)\n" \
  "s_barrier\n" \
  "ds_read_b32 v8, v1 offset:26688\n" \
  "s_cmp_lg_u32 s73, 3\n" \
  "s_cbranch_scc1 .LCA_NOTK_%=\n" \
  "s_memrealtime s[38:39]\n" \
  "v_mov_b32 v26, 1\n" \
  "s_mul_i32 s23, s71, %[pp]\n" \
  "s_lshr_b32 s62, s66, 4\n" \
  "s_mov_b64 exec, 1\n" \
  "s_waitcnt lgkmcnt(0)\n" \
  "s_mov_b32 s67, s38\n" \
  "s_add_u32 s38, s38, s62\n" \
  "v_mov_b32 v25, s38\n" \
  "buffer_atomic_umax v25, off, s[16:19], s68\n" \
  "buffer_atomic_add v26, off, s[16:19], s68 offset:64\n" \
  "v_add_u32 v27, s23, v25\n" \
  "buffer_store_dword v27, off, s[16:19], s69 sc1\n" \
  "s_mov_b64 exec, -1\n" \
  "s_branch .LCA_SIDE_%=\n" \
  ".LCA_NOTK_%=:\n" \
  "s_cmp_lg_u32 s73, 0\n" \
  "s_cbranch_scc1 .LCA_NOZ_%=\n" \
  "v_mov_b64 v[26:27], s[92:93]\n" \
  "s_xor_b32 s88, s60, 0x8000\n" \
  "s_add_u32 s88, s88, s61\n" \
  "buffer_store_dwordx2 v[26:27], v5, s[40:43], s88 offen sc1\n" \
  ".LCA_NOZ_%=:\n" \
  "s_nop 7\n" \
  ".LCA_SIDE_%=:\n" \
  "s_waitcnt lgkmcnt(0)\n" \
  "v_readfirstlane_b32 s86, v8\n" \
  "s_mov_b64 vcc, s[76:77]\n" \
  "s_lshr_b32 s86, s86, 23\n" \
  "s_add_i32 s86, s86, s83\n" \
  "s_ashr_i32 s86, s86, 1\n" \
  "s_max_i32 s86, s86, s82\n" \
  "s_min_i32 s86, s86, 100\n" \
  "s_lshl_b32 s86, s86, 23\n" \
  "s_add_i32 s75, s86, 0x3c800000\n" \
  "s_cmp_eq_u32 s71, 0\n" \
  "ds_read_b128 v[32:35], v1 offset:0\n" \
  "ds_read_b128 v[36:39], v1 offset:1024\n" \
  "ds_read_b128 v[48:51], v1 offset:2048\n" \
  "ds_read_b128 v[52:55], v1 offset:3072\n" \
  "ds_read_b128 v[40:43], v1 offset:4096\n" \
  "ds_read_b128 v[56:59], v1 offset:5120\n" \
  "ds_read_b128 v[60:63], v1 offset:6144\n" \
  "ds_read_b128 v[44:47], v1 offset:7168\n" \
  "s_setprio 3\n" \
  "s_waitcnt lgkmcnt(7)\n" \
  "v_smfmac_f32_16x16x64_bf16 v[12:15], v[32:35], v[64:71], v0\n" \
  "s_waitcnt lgkmcnt(6)\n" \
  "v_smfmac_f32_16x16x64_bf16 v[12:15], v[36:39], v[72:79], v0\n" \
  "s_waitcnt lgkmcnt(5)\n" \
  "v_smfmac_f32_16x16x64_bf16 v[12:15], v[48:51], v[80:87], v0\n" \
  "s_waitcnt lgkmcnt(4)\n" \
  "v_smfmac_f32_16x16x64_bf16 v[12:15], v[52:55], v[88:95], v0\n" \
  "s_waitcnt lgkmcnt(3)\n" \
  "v_smfmac_f32_16x16x64_bf16 v[12:15], v[40:43], v[96:103], v0\n" \
  "s_waitcnt lgkmcnt(2)\n" \
  "v_smfmac_f32_16x16x64_bf16 v[12:15], v[56:59], v[104:111], v0\n" \
  "s_waitcnt lgkmcnt(1)\n" \
  "v_smfmac_f32_16x16x64_bf16 v[12:15], v[60:63], v[112:119], v0\n" \
  "s_waitcnt lgkmcnt(0)\n" \
  "v_smfmac_f32_16x16x64_bf16 v[12:15], v[44:47], v[120:127], v0\n" \
  "ds_read_b128 v[24:27], v1 offset:10240\n" \
  "ds_read_b128 v[28:31], v1 offset:14336\n" \
  "s_nop 5\n" \
  "v_add_f32 v12, v12, v14\n" \
  "v_add_f32 v13, v13, v15\n" \
  "v_cvt_pk_bf16_f32 v12, v12, v13\n" \
  "ds_write_b16 v9, v12 offset:0\n" \
  "ds_write_b16_d16_hi v9, v12 offset:288\n" \
  "s_setprio 0\n" \
  "s_barrier\n" \
  "v_smfmac_f32_16x16x64_bf16 v[16:19], v[32:35], v[128:135], v0\n" \
  "v_smfmac_f32_16x16x64_bf16 v[20:23], v[32:35], v[192:199], v0\n" \
  "ds_read_b128 v[32:35], v1 offset:11264\n" \
  "v_smfmac_f32_16x16x64_bf16 v[16:19], v[36:39], v[136:143], v0\n" \
  "v_smfmac_f32_16x16x64_bf16 v[20:23], v[36:39], v[200:207], v0\n" \
  "ds_read_b128 v[36:39], v1 offset:15360\n" \
  "v_smfmac_f32_16x16x64_bf16 v[16:19], v[48:51], v[144:151], v0\n" \
  "v_smfmac_f32_16x16x64_bf16 v[20:23], v[48:51], v[208:215], v0\n" \
  "v_smfmac_f32_16x16x64_bf16 v[16:19], v[52:55], v[152:159], v0\n" \
  "v_smfmac_f32_16x16x64_bf16 v[20:23], v[52:55], v[216:223], v0\n" \
  "v_smfmac_f32_16x16x64_bf16 v[16:19], v[40:43], v[160:167], v0\n" \
  "v_smfmac_f32_16x16x64_bf16 v[20:23], v[40:43], v[224:231], v0\n" \
  ".fill ((16 - (. - .LCA_AL_%=)) & 31) >> 2, 4, 0xbf800000\n" \
  "s_waitcnt lgkmcnt(2)\n" \
  "s_barrier\n" \
  "s_setprio 3\n" \
  "ds_read_b128 v[48:51], v2 offset:0\n" \
  "ds_read_b128 v[52:55], v2 offset:64\n" \
  "v_smfmac_f32_16x16x64_bf16 v[16:19], v[56:59], v[168:175], v0\n" \
  "v_smfmac_f32_16x16x64_bf16 v[20:23], v[56:59], v[232:239], v0\n" \
  "ds_read_b128 v[56:59], v2 offset:128\n" \
  "s_cbranch_scc1 .LCA_LAST_%=\n" \
  "s_waitcnt lgkmcnt(2)\n" \
  "v_mfma_f32_16x16x32_bf16 v[12:15], v[24:27], v[48:51], 0\n" \
  "ds_read_b128 v[24:27], v1 offset:12288\n" \
  "v_mfma_f32_16x16x32_bf16 v[8:11], v[28:31], v[48:51], 0\n" \
  "ds_read_b128 v[28:31], v1 offset:16384\n" \
  "v_mfma_f32_16x16x32_bf16 v[40:43], v[48:51], v[48:51], 0\n" \
  "ds_read_b128 v[48:51], v7 offset:18432\n" \
  "v_smfmac_f32_16x16x64_bf16 v[16:19], v[60:63], v[176:183], v0\n" \
  "v_smfmac_f32_16x16x64_bf16 v[20:23], v[60:63], v[240:247], v0\n" \
  "ds_read_b128 v[60:63], v2 offset:192\n" \
  "s_waitcnt lgkmcnt(5)\n" \
  "v_mfma_f32_16x16x32_bf16 v[12:15], v[32:35], v[52:55], v[12:15]\n" \
  "ds_read_b128 v[32:35], v1 offset:13312\n" \
  "v_mfma_f32_16x16x32_bf16 v[8:11], v[36:39], v[52:55], v[8:11]\n" \
  "ds_read_b128 v[36:39], v1 offset:17408\n" \
  "v_mfma_f32_16x16x32_bf16 v[40:43], v[52:55], v[52:55], v[40:43]\n" \
  "ds_read_b128 v[52:55], v7 offset:22528\n" \
  "s_waitcnt lgkmcnt(7)\n" \
  "v_mfma_f32_16x16x32_bf16 v[40:43], v[56:59], v[56:59], v[40:43]\n" \
  "s_waitcnt lgkmcnt(5)\n" \
  "v_mfma_f32_16x16x32_bf16 v[12:15], v[24:27], v[56:59], v[12:15]\n" \
  "ds_read_b128 v[24:27], v6 offset:2048\n" \
  "v_mfma_f32_16x16x32_bf16 v[8:11], v[28:31], v[56:59], v[8:11]\n" \
  "ds_read_b128 v[28:31], v6 offset:3072\n" \
  "s_waitcnt lgkmcnt(5)\n" \
  "v_mfma_f32_16x16x32_bf16 v[40:43], v[60:63], v[60:63], v[40:43]\n" \
  "ds_read_b128 v[56:59], v6 offset:0\n" \
  "s_waitcnt lgkmcnt(4)\n" \
  "v_mfma_f32_16x16x32_bf16 v[12:15], v[32:35], v[60:63], v[12:15]\n" \
  "ds_read_b128 v[32:35], v6 offset:8192\n" \
  "v_mfma_f32_16x16x32_bf16 v[8:11], v[36:39], v[60:63], v[8:11]\n" \
  "ds_read_b128 v[36:39], v6 offset:9216\n" \
  "v_smfmac_f32_16x16x64_bf16 v[16:19], v[44:47], v[184:191], v0\n" \
  "ds_read_b128 v[60:63], v6 offset:1024\n" \
  "s_waitcnt lgkmcnt(6)\n" \
  "v_smfmac_f32_16x16x64_bf16 v[20:23], v[44:47], v[248:255], v0\n" \
  "v_cndmask_b32_dpp v43, v43, v42, vcc quad_perm:[0,1,0,1] row_mask:0xf bank_mask:0xf\n" \
  "v_mov_b32 v41, 0\n" \
  "v_mov_b32 v42, 0\n" \
  "v_cndmask_b32_dpp v12, v8, v12, vcc quad_perm:[0,1,0,1] row_mask:0xf bank_mask:0xf\n" \
  "v_cndmask_b32_dpp v13, v9, v13, vcc quad_perm:[0,1,0,1] row_mask:0xf bank_mask:0xf\n" \
  "v_cndmask_b32_dpp v14, v10, v14, vcc quad_perm:[0,1,0,1] row_mask:0xf bank_mask:0xf\n" \
  "v_cndmask_b32_dpp v15, v11, v15, vcc quad_perm:[0,1,0,1] row_mask:0xf bank_mask:0xf\n" \
  "v_maximum3_f32 v8, v43, v12, v13\n" \
  "v_maximum3_f32 v8, v8, v14, v15\n" \
  "v_add_f32 v16, v16, v18\n" \
  "v_add_f32 v17, v17, v19\n" \
  "v_max_f32_dpp v8, v8, v8 quad_perm:[2,3,0,1] row_mask:0xf bank_mask:0xf\n" \
  "v_mov_b32 v9, v8\n" \
  "v_add_f32 v20, v20, v22\n" \
  "v_add_f32 v21, v21, v23\n" \
  "v_permlane16_swap_b32 v8, v9\n" \
  "v_max_f32 v8, v8, v9\n" \
  "v_mov_b32 v9, v8\n" \
  "v_cvt_pk_bf16_f32 v16, v16, v17\n" \
  "v_cvt_pk_bf16_f32 v17, v20, v21\n" \
  "v_permlane32_swap_b32 v8, v9\n" \
  "v_max_f32 v8, v8, v9\n" \
  "s_barrier\n" \
  "buffer_load_dword v7, v5, s[16:19], s24 offen sc1\n" \
  "v_mul_f32 v8, s78, v8\n" \
  "v_pk_fma_f32 v[12:13], v[12:13], s[78:79], v[8:9] op_sel_hi:[1,0,0] neg_lo:[0,0,1] neg_hi:[0,0,1]\n" \
  "v_pk_fma_f32 v[14:15], v[14:15], s[78:79], v[8:9] op_sel_hi:[1,0,0] neg_lo:[0,0,1] neg_hi:[0,0,1]\n" \
  "v_fma_f32 v43, v43, s78, -v8\n" \
  "v_exp_f32 v12, v12\n" \
  "v_exp_f32 v13, v13\n" \
  "v_exp_f32 v14, v14\n" \
  "v_exp_f32 v15, v15\n" \
  "v_exp_f32 v43, v43\n" \
  "v_cvt_pk_bf16_f32 v44, v12, v13\n" \
  "v_cvt_pk_bf16_f32 v45, v14, v15\n" \
  "v_pk_add_f32 v[18:19], v[12:13], v[14:15]\n" \
  "v_mov_b32_dpp v46, v44 quad_perm:[2,3,0,1] row_mask:0xf bank_mask:0xf\n" \
  "v_mov_b32_dpp v9, v43 quad_perm:[2,3,0,1] row_mask:0xf bank_mask:0xf\n" \
  "v_mov_b32_dpp v47, v45 quad_perm:[2,3,0,1] row_mask:0xf bank_mask:0xf\n" \
  "v_add_f32 v10, v18, v43\n" \
  "v_cvt_pk_bf16_f32 v40, v43, v9\n" \
  "v_add_f32 v10, v10, v19\n" \
  "v_mfma_f32_16x16x32_bf16 v[12:15], v[48:51], v[44:47], 0\n" \
  "v_cvt_pk_bf16_f32 v43, v43, v9\n" \
  "v_add_f32_dpp v10, v10, v10 quad_perm:[2,3,0,1] row_mask:0xf bank_mask:0xf\n" \
  "v_mfma_f32_16x16x32_bf16 v[20:23], v[52:55], v[44:47], 0\n" \
  "v_mul_f32 v11, s75, v10\n" \
  "v_mul_f32 v10, s75, v10\n" \
  "v_mfma_f32_16x16x16_bf16 v[12:15], v[16:17], v[40:41], v[12:15]\n" \
  "ds_read_b128 v[48:51], v6 offset:10240\n" \
  "v_permlane16_swap_b32 v10, v11\n" \
  "v_add_f32 v10, v10, v11\n" \
  "v_mfma_f32_16x16x16_bf16 v[20:23], v[16:17], v[42:43], v[20:23]\n" \
  "v_mov_b32 v11, v10\n" \
  "ds_read_b128 v[52:55], v6 offset:11264\n" \
  "v_mov_b64 v[18:19], s[84:85]\n" \
  "v_permlane32_swap_b32 v10, v11\n" \
  "v_add_f32 v10, v10, v11\n" \
  "v_rcp_f32 v10, v10\n" \
  "v_permlane16_swap_b32 v16, v17\n" \
  "v_pk_mul_f32 v[12:13], v[12:13], v[10:11] op_sel_hi:[1,0]\n" \
  "v_pk_mul_f32 v[14:15], v[14:15], v[10:11] op_sel_hi:[1,0]\n" \
  "v_pk_mul_f32 v[20:21], v[20:21], v[10:11] op_sel_hi:[1,0]\n" \
  "v_pk_mul_f32 v[22:23], v[22:23], v[10:11] op_sel_hi:[1,0]\n" \
  "v_cvt_pk_bf16_f32 v40, v12, v13\n" \
  "v_cvt_pk_bf16_f32 v41, v14, v15\n" \
  "v_cvt_pk_bf16_f32 v42, v20, v21\n" \
  "ds_write_b64 v4, v[40:41]\n" \
  "v_cvt_pk_bf16_f32 v43, v22, v23\n" \
  "ds_write_b64 v4, v[42:43] offset:1024\n" \
  "v_max_u32 v16, 1, v16\n" \
  "v_max_u32 v17, 1, v17\n" \
  "buffer_store_dwordx2 v[16:17], v3, s[52:55], s70 offen sc1\n" \
  "v_mov_b64 v[12:13], s[84:85]\n" \
  "v_mov_b64 v[14:15], s[84:85]\n" \
  "v_mov_b64 v[16:17], s[84:85]\n" \
  "v_mov_b64 v[20:21], s[84:85]\n" \
  "v_mov_b64 v[22:23], s[84:85]\n" \
  "v_mov_b64 v[8:9], s[84:85]\n" \
  "v_mov_b64 v[10:11], s[84:85]\n" \
  "s_setprio 0\n" \
  "s_waitcnt lgkmcnt(0)\n" \
  "s_barrier\n" \
  "ds_read_b128 v[40:43], v1 offset:8192\n" \
  "ds_read_b128 v[44:47], v1 offset:9216\n" \
  "s_waitcnt lgkmcnt(1)\n" \
  "v_smfmac_f32_16x16x64_bf16 v[12:15], v[40:43], v[56:63], v0\n" \
  "s_waitcnt lgkmcnt(0)\n" \
  "v_smfmac_f32_16x16x64_bf16 v[12:15], v[44:47], v[32:39], v0\n" \
  "ds_read_b128 v[56:59], v6 offset:4096\n" \
  "ds_read_b128 v[60:63], v6 offset:5120\n" \
  "v_smfmac_f32_16x16x64_bf16 v[16:19], v[40:43], v[24:31], v0\n" \
  "ds_read_b128 v[32:35], v6 offset:12288\n" \
  "ds_read_b128 v[36:39], v6 offset:13312\n" \
  "v_smfmac_f32_16x16x64_bf16 v[16:19], v[44:47], v[48:55], v0\n" \
  "ds_read_b128 v[24:27], v6 offset:6144\n" \
  "ds_read_b128 v[28:31], v6 offset:7168\n" \
  "v_add_f32 v12, v12, v14\n" \
  "v_add_f32 v13, v13, v15\n" \
  "global_atomic_add_x2 v5, v[12:13], s[26:27]\n" \
  "ds_read_b128 v[48:51], v6 offset:14336\n" \
  "ds_read_b128 v[52:55], v6 offset:15360\n" \
  "s_waitcnt lgkmcnt(6)\n" \
  "v_add_f32 v16, v16, v18\n" \
  "v_add_f32 v17, v17, v19\n" \
  "global_atomic_add_x2 v5, v[16:17], s[28:29]\n" \
  "v_smfmac_f32_16x16x64_bf16 v[20:23], v[40:43], v[56:63], v0\n" \
  "s_waitcnt lgkmcnt(4)\n" \
  "v_smfmac_f32_16x16x64_bf16 v[20:23], v[44:47], v[32:39], v0\n" \
  "s_waitcnt lgkmcnt(2)\n" \
  "v_smfmac_f32_16x16x64_bf16 v[8:11], v[40:43], v[24:31], v0\n" \
  "s_waitcnt lgkmcnt(0)\n" \
  "v_smfmac_f32_16x16x64_bf16 v[8:11], v[44:47], v[48:55], v0\n" \
  "s_nop 4\n" \
  "v_add_f32 v20, v20, v22\n" \
  "v_add_f32 v21, v21, v23\n" \
  "global_atomic_add_x2 v5, v[20:21], s[30:31]\n" \
  "v_add_f32 v8, v8, v10\n" \
  "v_add_f32 v9, v9, v11\n" \
  "global_atomic_add_x2 v5, v[8:9], s[32:33]\n" \
  "s_memrealtime s[62:63]\n" \
  "v_mbcnt_lo_u32_b32 v48, -1, 0\n" \
  "v_mbcnt_hi_u32_b32 v48, -1, v48\n" \
  "v_lshrrev_b32 v49, 3, v48\n" \
  "v_and_b32 v50, 7, v48\n" \
  "v_lshlrev_b32 v50, 4, v50\n" \
  "v_lshl_or_b32 v44, v49, 8, v50\n" \
  "s_add_u32 s20, s20, 32\n" \
  "s_mov_b32 s21, 0x8000\n" \
  "v_mov_b32 v52, s22\n" \
  "v_mov_b32 v54, s20\n" \
  "s_mul_i32 s88, s71, %[pp]\n" \
  "s_lshl_b32 s89, s71, 10\n" \
  "s_sub_u32 s88, s88, s25\n" \
  "s_mul_i32 s38, s24, %[an]\n" \
  "s_cmp_gt_u32 s89, s38\n" \
  "s_cselect_b32 s89, 0x1000, 0\n" \
  "s_waitcnt vmcnt(5)\n" \
  "v_mov_b32 v46, v7\n" \
  "s_branch .LCA_TKE_%=\n" \
  ".LCA_TKR_%=:\n" \
  "s_sub_u32 s21, s21, 1\n" \
  "s_cbranch_scc1 .LCA_TKO_%=\n" \
  "s_and_b32 s38, s21, 7\n" \
  "s_cmp_lg_u32 s38, 7\n" \
  "s_cbranch_scc1 .LCA_TKS_%=\n" \
  "buffer_load_dword v46, v5, s[16:19], s24 offen sc1\n" \
  ".LCA_TKS_%=:\n" \
  "s_sleep 1\n" \
  ".LCA_TKE_%=:\n" \
  "v_readlane_b32 s38, v46, 8\n" \
  "v_readlane_b32 s39, v46, 24\n" \
  "v_readlane_b32 s23, v46, 40\n" \
  "v_readlane_b32 s34, v46, 56\n" \
  "s_add_u32 s38, s38, s39\n" \
  "s_add_u32 s23, s23, s34\n" \
  "s_add_u32 s38, s38, s23\n" \
  "s_cmp_lt_u32 s38, s20\n" \
  "s_cbranch_scc1 .LCA_TKR_%=\n" \
  ".LCA_TKO_%=:\n" \
  "v_readlane_b32 s34, v46, 0\n" \
  "v_readlane_b32 s38, v46, 16\n" \
  "v_readlane_b32 s39, v46, 32\n" \
  "v_readlane_b32 s23, v46, 48\n" \
  "s_max_u32 s34, s34, s38\n" \
  "s_max_u32 s39, s39, s23\n" \
  "s_add_u32 s34, s34, %[cd]\n" \
  "v_readlane_b32 s23, v46, 1\n" \
  "s_max_u32 s34, s34, s39\n" \
  "s_sub_u32 s23, s23, s34\n" \
  "s_sub_u32 s23, s23, s88\n" \
  "s_cmp_lt_u32 s23, s89\n" \
  "s_cselect_b32 s23, s23, 0\n" \
  "s_min_u32 s23, s23, %[ka]\n" \
  "s_add_u32 s34, s34, s23\n" \
  "s_add_u32 s34, s34, s64\n" \
  "v_mov_b32 v55, s34\n" \
  "ds_write_b64 v52, v[54:55]\n" \
  "s_branch .LCA_WAIU_%=\n" \
  ".LCA_WAIS_%=:\n" \
  "s_cmp_lt_i32 s39, -1000\n" \
  "s_cbranch_scc1 .LCA_WAIE_%=\n" \
  "s_sleep 1\n" \
  ".LCA_WAIU_%=:\n" \
  "s_memrealtime s[38:39]\n" \
  "s_waitcnt lgkmcnt(0)\n" \
  "s_sub_i32 s39, s38, s34\n" \
  "s_cmp_lt_i32 s39, -7\n" \
  "s_cbranch_scc1 .LCA_WAIS_%=\n" \
  "s_cmp_lt_i32 s39, 0\n" \
  "s_cbranch_scc1 .LCA_WAIU_%=\n" \
  ".LCA_WAIE_%=:\n" \
  "buffer_load_dwordx4 v[24:27], v44, s[40:43], s60 offen sc1\n" \
  "buffer_load_dwordx4 v[28:31], v44, s[40:43], s60 offen offset:2048 sc1\n" \
  "s_sub_u32 s62, s62, s67\n" \
  "s_lshl_b32 s62, s62, 4\n" \
  "s_sub_i32 s62, s62, s66\n" \
  "s_min_i32 s62, s62, 128\n" \
  "s_max_i32 s62, s62, -256\n" \
  "s_ashr_i32 s62, s62, 2\n" \
  "s_add_i32 s66, s66, s62\n" \
  "s_add_u32 s70, s70, 0x10000\n" \
  "v_mbcnt_lo_u32_b32 v48, -1, 0\n" \
  "v_mbcnt_hi_u32_b32 v48, -1, v48\n" \
  "v_and_b32 v49, 15, v48\n" \
  "v_lshl_add_u32 v9, v49, 1, s97\n" \
  "v_lshrrev_b32 v49, 4, v48\n" \
  "v_mad_u32_u24 v9, v49, s94, v9\n" \
  "v_add_u32 v7, s99, v1\n" \
  "v_mov_b64 v[12:13], 0\n" \
  "v_mov_b64 v[14:15], 0\n" \
  "v_mov_b64 v[16:17], 0\n" \
  "v_mov_b64 v[18:19], 0\n" \
  "v_mov_b64 v[20:21], 0\n" \
  "v_mov_b64 v[22:23], 0\n" \
  "v_and_b32 v49, 3, v48\n" \
  "v_lshl_add_u32 v40, v49, 2, s98\n" \
  "v_lshrrev_b32 v49, 2, v48\n" \
  "v_bfe_u32 v48, v48, 4, 1\n" \
  "v_xor_b32 v49, v49, v48\n" \
  "v_lshl_add_u32 v40, v49, 5, v40\n" \
  "s_add_i32 s80, s86, 0x35000000\n" \
  "s_sub_u32 s91, 0x4a350000, s86\n" \
  "s_min_u32 s91, s91, 0x4ca00000\n" \
  "s_mov_b32 s89, s60\n" \
  "s_xor_b32 s60, s60, 0x80\n" \
  "s_bitcmp0_b32 s60, 7\n" \
  "s_cselect_b32 s88, 0x8000, 0\n" \
  "s_add_u32 s60, s60, s88\n" \
  "s_and_b32 s60, s60, 0xffff\n" \
  "s_add_u32 s58, s56, s60\n" \
  "s_addc_u32 s59, s57, 0\n" \
  "s_xor_b32 s38, s35, 0\n" \
  "s_add_u32 s26, s58, s38\n" \
  "s_addc_u32 s27, s59, 0\n" \
  "s_xor_b32 s38, s35, 1024\n" \
  "s_add_u32 s28, s58, s38\n" \
  "s_addc_u32 s29, s59, 0\n" \
  "s_xor_b32 s38, s35, 2048\n" \
  "s_add_u32 s30, s58, s38\n" \
  "s_addc_u32 s31, s59, 0\n" \
  "s_xor_b32 s38, s35, 3072\n" \
  "s_add_u32 s32, s58, s38\n" \
  "s_addc_u32 s33, s59, 0\n" \
  "s_sub_u32 s71, s71, 1\n" \
  "s_mov_b32 s87, 0x40000\n" \
  "s_branch .LCA_CHK_%=\n" \
  ".LCA_POLL_%=:\n" \
  "buffer_load_dwordx4 v[24:27], v44, s[40:43], s89 offen sc1\n" \
  "buffer_load_dwordx4 v[28:31], v44, s[40:43], s89 offen offset:2048 sc1\n" \
  ".LCA_CHK_%=:\n" \
  "s_sub_u32 s87, s87, 1\n" \
  "s_cbranch_scc1 .LCA_END_%=\n" \
  "s_waitcnt vmcnt(1)\n" \
  "v_cvt_f32_i32 v32, v24\n" \
  "v_cvt_f32_i32 v33, v25\n" \
  "v_cvt_f32_i32 v34, v26\n" \
  "v_cvt_f32_i32 v35, v27\n" \
  "v_pk_mul_f32 v[24:25], v[32:33], s[80:81] op_sel_hi:[1,0]\n" \
  "v_pk_mul_f32 v[26:27], v[34:35], s[80:81] op_sel_hi:[1,0]\n" \
  "v_max3_f32 v45, |v32|, |v33|, |v34|\n" \
  "v_cvt_pk_bf16_f32 v24, v24, v26\n" \
  "v_cvt_pk_bf16_f32 v25, v25, v27\n" \
  "ds_write2_b32 v40, v24, v25 offset0:0 offset1:4\n" \
  "v_max3_f32 v45, |v45|, |v35|, |v35|\n" \
  "s_waitcnt vmcnt(0)\n" \
  "v_cvt_f32_i32 v32, v28\n" \
  "v_cvt_f32_i32 v33, v29\n" \
  "v_cvt_f32_i32 v34, v30\n" \
  "v_cvt_f32_i32 v35, v31\n" \
  "v_pk_mul_f32 v[28:29], v[32:33], s[80:81] op_sel_hi:[1,0]\n" \
  "v_pk_mul_f32 v[30:31], v[34:35], s[80:81] op_sel_hi:[1,0]\n" \
  "v_max3_f32 v45, |v45|, |v32|, |v33|\n" \
  "v_cvt_pk_bf16_f32 v28, v28, v30\n" \
  "v_cvt_pk_bf16_f32 v29, v29, v31\n" \
  "ds_write2_b32 v40, v28, v29 offset0:128 offset1:132\n" \
  "v_max3_f32 v45, |v45|, |v34|, |v35|\n" \
  "v_cmp_lt_f32 vcc, s91, v45\n" \
  "s_cbranch_vccnz .LCA_BIG_%=\n" \
  ".LCA_SML_%=:\n" \
  "s_branch .LCA_TOP_%=\n" \
  ".LCA_BIG_%=:\n" \
  "v_cmp_lt_f32 vcc, 0x4ca00000, v45\n" \
  "s_cbranch_vccnz .LCA_POLL_%=\n" \
  "v_mul_f32 v45, s80, v45\n" \
  "v_mul_f32 v45, v45, v45\n" \
  "s_add_u32 s88, s74, 157760\n" \
  "v_mov_b32 v44, s88\n" \
  "s_nop 0\n" \
  "v_cmpx_lt_f32 vcc, 2.0, v45\n" \
  "ds_max_u32 v44, v45\n" \
  "s_mov_b64 exec, -1\n" \
  "s_branch .LCA_SML_%=\n" \
  ".LCA_LAST_%=:\n" \
  "s_nop 0\n" \
  "v_smfmac_f32_16x16x64_bf16 v[16:19], v[60:63], v[176:183], v0\n" \
  "v_smfmac_f32_16x16x64_bf16 v[20:23], v[60:63], v[240:247], v0\n" \
  "v_smfmac_f32_16x16x64_bf16 v[16:19], v[44:47], v[184:191], v0\n" \
  "v_smfmac_f32_16x16x64_bf16 v[20:23], v[44:47], v[248:255], v0\n" \
  "s_nop 7\n" \
  "v_add_f32 v16, v16, v18\n" \
  "v_add_f32 v17, v17, v19\n" \
  "v_add_f32 v20, v20, v22\n" \
  "v_add_f32 v21, v21, v23\n" \
  "v_cvt_pk_bf16_f32 v16, v16, v17\n" \
  "v_cvt_pk_bf16_f32 v17, v20, v21\n" \
  "s_nop 1\n" \
  "v_permlane16_swap_b32 v16, v17\n" \
  "v_max_u32 v16, 1, v16\n" \
  "v_max_u32 v17, 1, v17\n" \
  "buffer_store_dwordx2 v[16:17], v3, s[52:55], s70 offen sc1\n" \
  "s_waitcnt vmcnt(0)\n" \
  ".LCA_END_%=:\n" \
  "s_endpgm\n"
#define CHAIN_ASM_VO \
  "s_mov_b32 s66, %[d0]\n" \
  "s_mov_b32 s94, 576\n" \
  "s_mov_b32 s95, 288\n" \
  "s_mov_b32 s84, 0x43400000\n" \
  "s_mov_b32 s85, 0x43400000\n" \
  "s_mov_b32 s92, 0x88000000\n" \
  "s_mov_b32 s93, 0x87fffff7\n" \
  "s_mov_b32 s78, 0x3e0293ee\n" \
  "s_mov_b32 s76, 0x33333333\n" \
  "s_mov_b32 s77, 0x33333333\n" \
  "s_add_u32 s58, s56, s60\n" \
  "s_addc_u32 s59, s57, 0\n" \
  "s_xor_b32 s38, s35, 0\n" \
  "s_add_u32 s26, s58, s38\n" \
  "s_addc_u32 s27, s59, 0\n" \
  "s_xor_b32 s38, s35, 1024\n" \
  "s_add_u32 s28, s58, s38\n" \
  "s_addc_u32 s29, s59, 0\n" \
  "s_xor_b32 s38, s35, 2048\n" \
  "s_add_u32 s30, s58, s38\n" \
  "s_addc_u32 s31, s59, 0\n" \
  "s_xor_b32 s38, s35, 3072\n" \
  "s_add_u32 s32, s58, s38\n" \
  "s_addc_u32 s33, s59, 0\n" \
  "s_lshr_b32 s98, s60, 2\n" \
  "s_add_u32 s98, s98, s74\n" \
  "s_add_u32 s98, s98, 131072\n" \
  "s_lshl_b32 s97, s73, 5\n" \
  "s_add_u32 s97, s97, s74\n" \
  "s_add_u32 s97, s97, 158592\n" \
  "s_lshl_b32 s99, s73, 11\n" \
  "s_add_u32 s99, s99, s74\n" \
  "s_add_u32 s99, s99, 133120\n" \
  "v_mbcnt_lo_u32_b32 v48, -1, 0\n" \
  "v_mbcnt_hi_u32_b32 v48, -1, v48\n" \
  "s_sub_u32 s88, s73, 4\n" \
  "v_and_b32 v51, 15, v48\n" \
  "v_lshl_add_u32 v4, v51, 1, s97\n" \
  "v_lshrrev_b32 v49, 4, v48\n" \
  "v_mad_u32_u24 v4, v49, s94, v4\n" \
  "v_lshrrev_b32 v49, 2, v51\n" \
  "v_and_b32 v50, 5, v51\n" \
  "v_and_or_b32 v49, v49, 2, v50\n" \
  "s_lshl_b32 s89, s88, 6\n" \
  "s_add_u32 s89, s89, s74\n" \
  "s_add_u32 s89, s89, 161152\n" \
  "v_and_b32 v50, 48, v48\n" \
  "v_add_u32 v6, s89, v50\n" \
  "v_mad_u32_u24 v6, v49, s95, v6\n" \
  "v_lshlrev_b32 v7, 3, v50\n" \
  "v_lshl_add_u32 v7, v49, 4, v7\n" \
  "v_and_b32 v49, 2, v51\n" \
  "v_lshl_add_u32 v7, v49, 29, v7\n" \
  "v_lshlrev_b32 v10, 4, v48\n" \
  "v_mov_b64 v[12:13], 0\n" \
  "v_mov_b64 v[14:15], 0\n" \
  "v_mov_b64 v[16:17], 0\n" \
  "v_mov_b64 v[18:19], 0\n" \
  "v_mov_b64 v[20:21], 0\n" \
  "v_mov_b64 v[22:23], 0\n" \
  "v_mov_b32 v24, 0x44870000\n" \
  "v_mul_f32 v24, s72, v24\n" \
  "s_nop 0\n" \
  "v_readfirstlane_b32 s86, v24\n" \
  "s_lshr_b32 s86, s86, 23\n" \
  "s_addk_i32 s86, 0xff83\n" \
  "s_ashr_i32 s86, s86, 1\n" \
  "s_max_i32 s86, s86, -20\n" \
  "s_min_i32 s86, s86, 100\n" \
  "s_mov_b32 s82, s86\n" \
  "s_lshr_b32 s83, s72, 23\n" \
  "s_sub_i32 s83, s83, 242\n" \
  "s_lshl_b32 s88, s73, 10\n" \
  "v_add_u32 v24, s88, v1\n" \
  "ds_read_b128 v[32:35], v24 offset:0\n" \
  "s_waitcnt lgkmcnt(0)\n" \
  "v_mov_b32 v45, 0\n" \
  "v_dot2c_f32_bf16 v45, v32, v32\n" \
  "v_dot2c_f32_bf16 v45, v33, v33\n" \
  "v_dot2c_f32_bf16 v45, v34, v34\n" \
  "v_dot2c_f32_bf16 v45, v35, v35\n" \
  "s_add_u32 s88, s74, 157760\n" \
  "v_mov_b32 v44, s88\n" \
  "s_nop 0\n" \
  "v_cmpx_lt_f32 vcc, 2.0, v45\n" \
  "ds_max_u32 v44, v45\n" \
  "s_mov_b64 exec, -1\n" \
  ".p2align 6\n" \
  ".LCV_AL_%=:\n" \
  ".LCV_TOP_%=:\n" \
  "s_waitcnt lgkmcnt(0)\n" \
  "s_barrier\n" \
  "s_setprio 2\n" \
  "ds_read_b128 v[24:27], v1 offset:0\n" \
  "ds_read_b128 v[28:31], v1 offset:1024\n" \
  "ds_read_b128 v[32:35], v1 offset:2048\n" \
  "ds_read_b128 v[36:39], v1 offset:3072\n" \
  "buffer_load_dword v5, off, s[44:47], s68 sc1\n" \
  "s_waitcnt lgkmcnt(3)\n" \
  "v_smfmac_f32_16x16x64_bf16 v[20:23], v[24:27], v[192:199], v0\n" \
  "ds_read_b128 v[40:43], v1 offset:4096\n" \
  "s_waitcnt lgkmcnt(3)\n" \
  "v_smfmac_f32_16x16x64_bf16 v[20:23], v[28:31], v[200:207], v0\n" \
  "ds_read_b128 v[44:47], v1 offset:5120\n" \
  "s_waitcnt lgkmcnt(3)\n" \
  "v_smfmac_f32_16x16x64_bf16 v[20:23], v[32:35], v[208:215], v0\n" \
  "ds_read_b128 v[48:51], v1 offset:6144\n" \
  "s_waitcnt lgkmcnt(3)\n" \
  "v_smfmac_f32_16x16x64_bf16 v[20:23], v[36:39], v[216:223], v0\n" \
  "ds_read_b128 v[52:55], v1 offset:7168\n" \
  "s_waitcnt lgkmcnt(3)\n" \
  "v_smfmac_f32_16x16x64_bf16 v[20:23], v[40:43], v[224:231], v0\n" \
  "s_waitcnt lgkmcnt(2)\n" \
  "v_smfmac_f32_16x16x64_bf16 v[20:23], v[44:47], v[232:239], v0\n" \
  "s_waitcnt lgkmcnt(1)\n" \
  "v_smfmac_f32_16x16x64_bf16 v[20:23], v[48:51], v[240:247], v0\n" \
  "s_waitcnt lgkmcnt(0)\n" \
  "v_smfmac_f32_16x16x64_bf16 v[20:23], v[52:55], v[248:255], v0\n" \
  "s_nop 0\n" \
  "v_smfmac_f32_16x16x64_bf16 v[12:15], v[24:27], v[64:71], v0\n" \
  "s_nop 0\n" \
  "v_smfmac_f32_16x16x64_bf16 v[12:15], v[28:31], v[72:79], v0\n" \
  "s_nop 0\n" \
  "v_smfmac_f32_16x16x64_bf16 v[12:15], v[32:35], v[80:87], v0\n" \
  "ds_read_b32 v11, v1 offset:26688\n" \
  "v_smfmac_f32_16x16x64_bf16 v[12:15], v[36:39], v[88:95], v0\n" \
  "v_add_f32 v20, v20, v22\n" \
  "v_smfmac_f32_16x16x64_bf16 v[12:15], v[40:43], v[96:103], v0\n" \
  "v_add_f32 v21, v21, v23\n" \
  "v_smfmac_f32_16x16x64_bf16 v[12:15], v[44:47], v[104:111], v0\n" \
  "v_cvt_pk_bf16_f32 v20, v20, v21\n" \
  "v_smfmac_f32_16x16x64_bf16 v[12:15], v[48:51], v[112:119], v0\n" \
  "ds_write_b16 v4, v20 offset:0\n" \
  "v_smfmac_f32_16x16x64_bf16 v[12:15], v[52:55], v[120:127], v0\n" \
  "ds_write_b16_d16_hi v4, v20 offset:288\n" \
  "v_smfmac_f32_16x16x64_bf16 v[16:19], v[24:27], v[128:135], v0\n" \
  "s_nop 1\n" \
  "v_smfmac_f32_16x16x64_bf16 v[16:19], v[28:31], v[136:143], v0\n" \
  "s_nop 1\n" \
  "v_smfmac_f32_16x16x64_bf16 v[16:19], v[32:35], v[144:151], v0\n" \
  "v_add_f32 v12, v12, v14\n" \
  "v_smfmac_f32_16x16x64_bf16 v[16:19], v[36:39], v[152:159], v0\n" \
  "v_add_f32 v13, v13, v15\n" \
  "v_smfmac_f32_16x16x64_bf16 v[16:19], v[40:43], v[160:167], v0\n" \
  "v_cvt_pk_bf16_f32 v12, v12, v13\n" \
  "v_smfmac_f32_16x16x64_bf16 v[16:19], v[44:47], v[168:175], v0\n" \
  "ds_write_b16 v4, v12 offset:128\n" \
  "v_smfmac_f32_16x16x64_bf16 v[16:19], v[48:51], v[176:183], v0\n" \
  "ds_write_b16_d16_hi v4, v12 offset:416\n" \
  "v_smfmac_f32_16x16x64_bf16 v[16:19], v[52:55], v[184:191], v0\n" \
  "s_barrier\n" \
  "s_nop 6\n" \
  "v_add_f32 v16, v16, v18\n" \
  "v_add_f32 v17, v17, v19\n" \
  "v_cvt_pk_bf16_f32 v16, v16, v17\n" \
  "ds_write_b16 v4, v16 offset:2560\n" \
  "ds_write_b16_d16_hi v4, v16 offset:2848\n" \
  "s_waitcnt lgkmcnt(0)\n" \
  "s_barrier\n" \
  "s_setprio 0\n" \
  "ds_read_b128 v[60:63], v6 offset:0\n" \
  "v_readfirstlane_b32 s86, v11\n" \
  "s_add_u32 s88, s74, 157760\n" \
  "v_mov_b32 v8, s88\n" \
  "v_mov_b32 v9, 0\n" \
  "ds_write_b32 v8, v9\n" \
  "s_lshr_b32 s86, s86, 23\n" \
  "s_add_i32 s86, s86, s83\n" \
  "s_ashr_i32 s86, s86, 1\n" \
  "s_max_i32 s86, s86, s82\n" \
  "s_min_i32 s86, s86, 100\n" \
  "s_lshl_b32 s86, s86, 23\n" \
  "s_cmp_eq_u32 s71, 0\n" \
  "s_waitcnt lgkmcnt(1)\n" \
  "v_max_u32 v60, 1, v60\n" \
  "v_max_u32 v61, 1, v61\n" \
  "v_max_u32 v62, 1, v62\n" \
  "v_max_u32 v63, 1, v63\n" \
  "buffer_store_dwordx4 v[60:63], v7, s[48:51], s69 offen sc1\n" \
  "s_cbranch_scc1 .LCV_LAST_%=\n" \
  "s_waitcnt vmcnt(1)\n" \
  "v_cmp_eq_u32 vcc, 0, v5\n" \
  "s_cmp_eq_u64 vcc, 0\n" \
  "s_cselect_b32 s90, 1, 0\n" \
  "s_barrier\n" \
  "ds_read_b128 v[24:27], v3 offset:0\n" \
  "ds_read_b128 v[28:31], v3 offset:1024\n" \
  "ds_read_b128 v[32:35], v3 offset:2048\n" \
  "ds_read_b128 v[36:39], v3 offset:3072\n" \
  "ds_read_b128 v[40:43], v3 offset:8192\n" \
  "ds_read_b128 v[44:47], v3 offset:9216\n" \
  "ds_read_b128 v[48:51], v3 offset:10240\n" \
  "ds_read_b128 v[52:55], v3 offset:11264\n" \
  "ds_read_b128 v[56:59], v3 offset:4096\n" \
  "ds_read_b128 v[60:63], v3 offset:5120\n" \
  "s_cbranch_scc0 .LCV_NOTOK_%=\n" \
  "s_mov_b32 m0, s99\n" \
  "s_add_u32 s88, s99, 8192\n" \
  "global_load_lds_dwordx4 v10, s[62:63] sc1\n" \
  "global_load_lds_dwordx4 v10, s[62:63] offset:1024 sc1\n" \
  "s_mov_b32 m0, s88\n" \
  "s_nop 0\n" \
  "global_load_lds_dwordx4 v10, s[64:65] sc1\n" \
  "global_load_lds_dwordx4 v10, s[64:65] offset:1024 sc1\n" \
  ".LCV_NOTOK_%=:\n" \
  "v_mov_b64 v[12:13], s[84:85]\n" \
  "v_mov_b64 v[14:15], s[84:85]\n" \
  "v_mov_b64 v[16:17], s[84:85]\n" \
  "v_mov_b64 v[18:19], s[84:85]\n" \
  "v_mov_b64 v[20:21], s[84:85]\n" \
  "v_mov_b64 v[22:23], s[84:85]\n" \
  "s_waitcnt lgkmcnt(0)\n" \
  "s_barrier\n" \
  "ds_read_b128 v[4:7], v1 offset:8192\n" \
  "ds_read_b128 v[8:11], v1 offset:9216\n" \
  "s_waitcnt lgkmcnt(1)\n" \
  "v_smfmac_f32_16x16x64_bf16 v[12:15], v[4:7], v[24:31], v0\n" \
  "s_waitcnt lgkmcnt(0)\n" \
  "v_smfmac_f32_16x16x64_bf16 v[12:15], v[8:11], v[40:47], v0\n" \
  "ds_read_b128 v[24:27], v3 offset:12288\n" \
  "ds_read_b128 v[28:31], v3 offset:13312\n" \
  "v_smfmac_f32_16x16x64_bf16 v[16:19], v[4:7], v[32:39], v0\n" \
  "ds_read_b128 v[32:35], v3 offset:6144\n" \
  "ds_read_b128 v[36:39], v3 offset:7168\n" \
  "v_smfmac_f32_16x16x64_bf16 v[16:19], v[8:11], v[48:55], v0\n" \
  "s_cmp_eq_u32 s90, 1\n" \
  "s_cbranch_scc1 .LCV_NMT_%=\n" \
  "buffer_load_dword v55, off, s[44:47], s68 sc1\n" \
  ".LCV_NMT_%=:\n" \
  "v_add_f32 v12, v12, v14\n" \
  "v_add_f32 v13, v13, v15\n" \
  "global_atomic_add_x2 v2, v[12:13], s[26:27]\n" \
  "v_smfmac_f32_16x16x64_bf16 v[20:23], v[4:7], v[56:63], v0\n" \
  "ds_read_b128 v[56:59], v3 offset:14336\n" \
  "ds_read_b128 v[60:63], v3 offset:15360\n" \
  "v_add_f32 v16, v16, v18\n" \
  "v_add_f32 v17, v17, v19\n" \
  "global_atomic_add_x2 v2, v[16:17], s[28:29]\n" \
  "v_mov_b64 v[40:41], s[84:85]\n" \
  "v_mov_b64 v[42:43], s[84:85]\n" \
  "s_waitcnt lgkmcnt(4)\n" \
  "v_smfmac_f32_16x16x64_bf16 v[20:23], v[8:11], v[24:31], v0\n" \
  "s_waitcnt lgkmcnt(2)\n" \
  "v_smfmac_f32_16x16x64_bf16 v[40:43], v[4:7], v[32:39], v0\n" \
  "s_waitcnt lgkmcnt(0)\n" \
  "v_smfmac_f32_16x16x64_bf16 v[40:43], v[8:11], v[56:63], v0\n" \
  "s_nop 3\n" \
  "v_add_f32 v20, v20, v22\n" \
  "v_add_f32 v21, v21, v23\n" \
  "global_atomic_add_x2 v2, v[20:21], s[30:31]\n" \
  "s_nop 0\n" \
  "v_add_f32 v40, v40, v42\n" \
  "v_add_f32 v41, v41, v43\n" \
  "global_atomic_add_x2 v2, v[40:41], s[32:33]\n" \
  "v_mbcnt_lo_u32_b32 v48, -1, 0\n" \
  "v_mbcnt_hi_u32_b32 v48, -1, v48\n" \
  "s_add_u32 s20, s20, 32\n" \
  "s_cmp_eq_u32 s90, 1\n" \
  "s_cbranch_scc1 .LCV_HAVE_%=\n" \
  "s_mov_b32 s87, 0x40000\n" \
  "v_lshlrev_b32 v10, 4, v48\n" \
  "s_waitcnt vmcnt(4)\n" \
  "v_cmp_eq_u32 vcc, 0, v55\n" \
  "s_cmp_eq_u64 vcc, 0\n" \
  "s_cbranch_scc1 .LCV_GOT_%=\n" \
  ".LCV_LATE_%=:\n" \
  "buffer_load_dword v58, off, s[44:47], s68 sc1\n" \
  "s_sub_u32 s87, s87, 1\n" \
  "s_cbranch_scc1 .LCV_END_%=\n" \
  "s_waitcnt vmcnt(0)\n" \
  "v_cmp_eq_u32 vcc, 0, v58\n" \
  "s_cmp_eq_u64 vcc, 0\n" \
  "s_cbranch_scc1 .LCV_GOT_%=\n" \
  "s_sleep 12\n" \
  "s_branch .LCV_LATE_%=\n" \
  ".LCV_GOT_%=:\n" \
  "s_mov_b32 m0, s99\n" \
  "s_add_u32 s88, s99, 8192\n" \
  "global_load_lds_dwordx4 v10, s[62:63] sc1\n" \
  "global_load_lds_dwordx4 v10, s[62:63] offset:1024 sc1\n" \
  "s_mov_b32 m0, s88\n" \
  "s_nop 0\n" \
  "global_load_lds_dwordx4 v10, s[64:65] sc1\n" \
  "global_load_lds_dwordx4 v10, s[64:65] offset:1024 sc1\n" \
  ".LCV_HAVE_%=:\n" \
  "v_lshrrev_b32 v49, 3, v48\n" \
  "v_and_b32 v50, 7, v48\n" \
  "v_lshlrev_b32 v50, 4, v50\n" \
  "v_lshl_or_b32 v44, v49, 8, v50\n" \
  "s_mov_b32 s21, 0x8000\n" \
  "v_mov_b32 v52, s22\n" \
  "v_mov_b32 v54, s20\n" \
  "s_branch .LCV_TKF_%=\n" \
  ".LCV_TKE_%=:\n" \
  "s_sleep 1\n" \
  ".LCV_TKF_%=:\n" \
  "ds_read_b64 v[54:55], v52\n" \
  "s_sub_u32 s21, s21, 1\n" \
  "s_cbranch_scc1 .LCV_TKO_%=\n" \
  "s_waitcnt lgkmcnt(0)\n" \
  "v_readfirstlane_b32 s38, v54\n" \
  "v_readfirstlane_b32 s34, v55\n" \
  "s_cmp_eq_u32 s38, s20\n" \
  "s_cbranch_scc0 .LCV_TKE_%=\n" \
  ".LCV_TKO_%=:\n" \
  "s_branch .LCV_WAIU_%=\n" \
  ".LCV_WAIS_%=:\n" \
  "s_cmp_lt_i32 s39, -1000\n" \
  "s_cbranch_scc1 .LCV_WAIE_%=\n" \
  "s_sleep 1\n" \
  ".LCV_WAIU_%=:\n" \
  "s_memrealtime s[38:39]\n" \
  "s_waitcnt lgkmcnt(0)\n" \
  "s_sub_i32 s39, s38, s34\n" \
  "s_cmp_lt_i32 s39, -7\n" \
  "s_cbranch_scc1 .LCV_WAIS_%=\n" \
  "s_cmp_lt_i32 s39, 0\n" \
  "s_cbranch_scc1 .LCV_WAIU_%=\n" \
  ".LCV_WAIE_%=:\n" \
  "buffer_load_dwordx4 v[24:27], v44, s[40:43], s60 offen sc1\n" \
  "buffer_load_dwordx4 v[28:31], v44, s[40:43], s60 offen offset:2048 sc1\n" \
  "v_mbcnt_lo_u32_b32 v48, -1, 0\n" \
  "v_mbcnt_hi_u32_b32 v48, -1, v48\n" \
  "s_sub_u32 s88, s73, 4\n" \
  "v_and_b32 v51, 15, v48\n" \
  "v_lshl_add_u32 v4, v51, 1, s97\n" \
  "v_lshrrev_b32 v49, 4, v48\n" \
  "v_mad_u32_u24 v4, v49, s94, v4\n" \
  "v_lshrrev_b32 v49, 2, v51\n" \
  "v_and_b32 v50, 5, v51\n" \
  "v_and_or_b32 v49, v49, 2, v50\n" \
  "s_lshl_b32 s89, s88, 6\n" \
  "s_add_u32 s89, s89, s74\n" \
  "s_add_u32 s89, s89, 161152\n" \
  "v_and_b32 v50, 48, v48\n" \
  "v_add_u32 v6, s89, v50\n" \
  "v_mad_u32_u24 v6, v49, s95, v6\n" \
  "v_lshlrev_b32 v7, 3, v50\n" \
  "v_lshl_add_u32 v7, v49, 4, v7\n" \
  "v_and_b32 v49, 2, v51\n" \
  "v_lshl_add_u32 v7, v49, 29, v7\n" \
  "v_lshlrev_b32 v10, 4, v48\n" \
  "v_mov_b64 v[12:13], 0\n" \
  "v_mov_b64 v[14:15], 0\n" \
  "v_mov_b64 v[16:17], 0\n" \
  "v_mov_b64 v[18:19], 0\n" \
  "v_mov_b64 v[20:21], 0\n" \
  "v_mov_b64 v[22:23], 0\n" \
  "v_and_b32 v49, 3, v48\n" \
  "v_lshl_add_u32 v40, v49, 2, s98\n" \
  "v_lshrrev_b32 v49, 2, v48\n" \
  "v_bfe_u32 v48, v48, 4, 1\n" \
  "v_xor_b32 v49, v49, v48\n" \
  "v_lshl_add_u32 v40, v49, 5, v40\n" \
  "s_add_u32 s62, s62, 0x80000\n" \
  "s_addc_u32 s63, s63, 0\n" \
  "s_add_u32 s64, s64, 0x40000\n" \
  "s_addc_u32 s65, s65, 0\n" \
  "s_add_u32 s68, s68, 0x1000\n" \
  "s_add_u32 s69, s69, 0x10000\n" \
  "s_add_i32 s80, s86, 0x35000000\n" \
  "s_sub_u32 s91, 0x4a350000, s86\n" \
  "s_min_u32 s91, s91, 0x4ca00000\n" \
  "s_mov_b32 s89, s60\n" \
  "s_xor_b32 s60, s60, 0x80\n" \
  "s_bitcmp0_b32 s60, 7\n" \
  "s_cselect_b32 s88, 0x8000, 0\n" \
  "s_add_u32 s60, s60, s88\n" \
  "s_and_b32 s60, s60, 0xffff\n" \
  "s_add_u32 s58, s56, s60\n" \
  "s_addc_u32 s59, s57, 0\n" \
  "s_xor_b32 s38, s35, 0\n" \
  "s_add_u32 s26, s58, s38\n" \
  "s_addc_u32 s27, s59, 0\n" \
  "s_xor_b32 s38, s35, 1024\n" \
  "s_add_u32 s28, s58, s38\n" \
  "s_addc_u32 s29, s59, 0\n" \
  "s_xor_b32 s38, s35, 2048\n" \
  "s_add_u32 s30, s58, s38\n" \
  "s_addc_u32 s31, s59, 0\n" \
  "s_xor_b32 s38, s35, 3072\n" \
  "s_add_u32 s32, s58, s38\n" \
  "s_addc_u32 s33, s59, 0\n" \
  "s_sub_u32 s71, s71, 1\n" \
  "s_mov_b32 s87, 0x40000\n" \
  "s_branch .LCV_CHK_%=\n" \
  ".LCV_POLL_%=:\n" \
  "buffer_load_dwordx4 v[24:27], v44, s[40:43], s89 offen sc1\n" \
  "buffer_load_dwordx4 v[28:31], v44, s[40:43], s89 offen offset:2048 sc1\n" \
  ".LCV_CHK_%=:\n" \
  "s_sub_u32 s87, s87, 1\n" \
  "s_cbranch_scc1 .LCV_END_%=\n" \
  "s_waitcnt vmcnt(1)\n" \
  "v_cvt_f32_i32 v32, v24\n" \
  "v_cvt_f32_i32 v33, v25\n" \
  "v_cvt_f32_i32 v34, v26\n" \
  "v_cvt_f32_i32 v35, v27\n" \
  "v_pk_mul_f32 v[24:25], v[32:33], s[80:81] op_sel_hi:[1,0]\n" \
  "v_pk_mul_f32 v[26:27], v[34:35], s[80:81] op_sel_hi:[1,0]\n" \
  "v_max3_f32 v45, |v32|, |v33|, |v34|\n" \
  "v_cvt_pk_bf16_f32 v24, v24, v26\n" \
  "v_cvt_pk_bf16_f32 v25, v25, v27\n" \
  "ds_write2_b32 v40, v24, v25 offset0:0 offset1:4\n" \
  "v_max3_f32 v45, |v45|, |v35|, |v35|\n" \
  "s_waitcnt vmcnt(0)\n" \
  "v_cvt_f32_i32 v32, v28\n" \
  "v_cvt_f32_i32 v33, v29\n" \
  "v_cvt_f32_i32 v34, v30\n" \
  "v_cvt_f32_i32 v35, v31\n" \
  "v_pk_mul_f32 v[28:29], v[32:33], s[80:81] op_sel_hi:[1,0]\n" \
  "v_pk_mul_f32 v[30:31], v[34:35], s[80:81] op_sel_hi:[1,0]\n" \
  "v_max3_f32 v45, |v45|, |v32|, |v33|\n" \
  "v_cvt_pk_bf16_f32 v28, v28, v30\n" \
  "v_cvt_pk_bf16_f32 v29, v29, v31\n" \
  "ds_write2_b32 v40, v28, v29 offset0:128 offset1:132\n" \
  "v_max3_f32 v45, |v45|, |v34|, |v35|\n" \
  "v_cmp_lt_f32 vcc, s91, v45\n" \
  "s_cbranch_vccnz .LCV_BIG_%=\n" \
  ".LCV_SML_%=:\n" \
  "s_branch .LCV_TOP_%=\n" \
  ".LCV_BIG_%=:\n" \
  "v_cmp_lt_f32 vcc, 0x4ca00000, v45\n" \
  "s_cbranch_vccnz .LCV_POLL_%=\n" \
  "v_mul_f32 v45, s80, v45\n" \
  "v_mul_f32 v45, v45, v45\n" \
  "s_add_u32 s88, s74, 157760\n" \
  "v_mov_b32 v44, s88\n" \
  "s_nop 0\n" \
  "v_cmpx_lt_f32 vcc, 2.0, v45\n" \
  "ds_max_u32 v44, v45\n" \
  "s_mov_b64 exec, -1\n" \
  "s_branch .LCV_SML_%=\n" \
  ".LCV_LAST_%=:\n" \
  "s_waitcnt vmcnt(0)\n" \
  ".LCV_END_%=:\n" \
  "s_endpgm\n"
#define QKV_ASM_0 \
  "s_mov_b32 s69, 0\n" \
  "s_branch .LQA_POLL_%=\n" \
  ".p2align 6\n" \
  ".LQA_AL_%=:\n" \
  ".LQA_TOP_%=:\n" \
  "s_barrier\n" \
  "ds_read_b128 v[36:39], v7 offset:0\n" \
  "ds_read_b128 v[40:43], v7 offset:2048\n" \
  "ds_read_b128 v[44:47], v7 offset:4096\n" \
  "s_setprio 2\n" \
  "s_waitcnt lgkmcnt(2)\n" \
  "v_mfma_f32_16x16x32_bf16 v[12:15], v[64:67], v[36:39], 0\n" \
  "ds_read_b128 v[48:51], v7 offset:6144\n" \
  "v_mfma_f32_16x16x32_bf16 v[16:19], v[128:131], v[36:39], 0\n" \
  "v_mfma_f32_16x16x32_bf16 v[20:23], v[36:39], v[192:195], 0\n" \
  "s_cmp_lg_u32 s69, 1\n" \
  "s_cbranch_scc1 .LQA_FDN_%=\n" \
  "s_waitcnt vmcnt(1)\n" \
  "v_mov_b32 v52, s80\n" \
  "v_mov_b32 v53, 0x10000\n" \
  "s_mov_b64 exec, 1\n" \
  "ds_add_rtn_u32 v54, v52, v53\n" \
  "s_mov_b64 exec, -1\n" \
  "s_add_u32 s67, s67, 4\n" \
  "s_add_u32 s64, s64, 4096\n" \
  "s_addc_u32 s65, s65, 0\n" \
  "v_mov_b32 v55, 0\n" \
  "s_waitcnt lgkmcnt(0)\n" \
  "v_readfirstlane_b32 s74, v54\n" \
  "s_and_b32 s76, s74, 0x30000\n" \
  "s_cmp_lg_u32 s76, 0x30000\n" \
  "s_cbranch_scc1 .LQA_FDN_%=\n" \
  "s_mov_b32 s79, 0x100000\n" \
  "s_branch .LQA_FDT_%=\n" \
  ".LQA_FD_%=:\n" \
  "ds_read_b32 v54, v52\n" \
  "s_sub_u32 s79, s79, 1\n" \
  "s_cbranch_scc1 .LQA_FDE_%=\n" \
  "s_waitcnt lgkmcnt(0)\n" \
  "v_readfirstlane_b32 s74, v54\n" \
  ".LQA_FDT_%=:\n" \
  "s_and_b32 s74, s74, 0xffff\n" \
  "s_cmp_lt_u32 s74, s67\n" \
  "s_cbranch_scc1 .LQA_FD_%=\n" \
  ".LQA_FDE_%=:\n" \
  "s_mov_b64 exec, 1\n" \
  "global_store_dword v55, v53, s[64:65] offset:-4096 sc1\n" \
  "s_mov_b64 exec, -1\n" \
  "s_waitcnt vmcnt(1)\n" \
  "s_branch .LQA_LOOK_%=\n" \
  ".LQA_FDN_%=:\n" \
  "s_waitcnt vmcnt(0)\n" \
  ".LQA_LOOK_%=:\n" \
  "v_cmp_eq_u32 vcc, 0, v11\n" \
  "s_mov_b32 m0, s58\n" \
  "s_add_u32 s74, s56, 4096\n" \
  "s_addc_u32 s75, s57, 0\n" \
  "s_add_u32 s76, s58, 4096\n" \
  "s_cmp_eq_u64 vcc, 0\n" \
  "s_cselect_b32 s69, 1, 0\n" \
  "s_waitcnt lgkmcnt(2)\n" \
  "v_mfma_f32_16x16x32_bf16 v[12:15], v[68:71], v[40:43], v[12:15]\n" \
  "ds_read_b128 v[36:39], v7 offset:8192\n" \
  "s_cbranch_scc0 2\n" \
  "global_load_lds_dwordx4 v6, s[56:57] offset:0 sc1\n" \
  "v_mfma_f32_16x16x32_bf16 v[16:19], v[132:135], v[40:43], v[16:19]\n" \
  "s_cbranch_scc0 2\n" \
  "global_load_lds_dwordx4 v6, s[56:57] offset:1024 sc1\n" \
  "v_mfma_f32_16x16x32_bf16 v[20:23], v[40:43], v[196:199], v[20:23]\n" \
  "s_cbranch_scc0 2\n" \
  "global_load_lds_dwordx4 v6, s[56:57] offset:2048 sc1\n" \
  "s_waitcnt lgkmcnt(2)\n" \
  "v_mfma_f32_16x16x32_bf16 v[12:15], v[72:75], v[44:47], v[12:15]\n" \
  "ds_read_b128 v[40:43], v7 offset:10240\n" \
  "s_cbranch_scc0 2\n" \
  "global_load_lds_dwordx4 v6, s[56:57] offset:3072 sc1\n" \
  "v_mfma_f32_16x16x32_bf16 v[16:19], v[136:139], v[44:47], v[16:19]\n" \
  "s_mov_b32 m0, s76\n" \
  "s_cbranch_scc0 2\n" \
  "global_load_lds_dwordx4 v6, s[74:75] offset:0 sc1\n" \
  "v_mfma_f32_16x16x32_bf16 v[20:23], v[44:47], v[200:203], v[20:23]\n" \
  "s_cbranch_scc0 2\n" \
  "global_load_lds_dwordx4 v6, s[74:75] offset:1024 sc1\n" \
  "s_waitcnt lgkmcnt(2)\n" \
  "v_mfma_f32_16x16x32_bf16 v[12:15], v[76:79], v[48:51], v[12:15]\n" \
  "ds_read_b128 v[44:47], v7 offset:12288\n" \
  "s_cbranch_scc0 2\n" \
  "global_load_lds_dwordx4 v6, s[74:75] offset:2048 sc1\n" \
  "v_mfma_f32_16x16x32_bf16 v[16:19], v[140:143], v[48:51], v[16:19]\n" \
  "s_cbranch_scc0 2\n" \
  "global_load_lds_dwordx4 v6, s[74:75] offset:3072 sc1\n" \
  "v_mfma_f32_16x16x32_bf16 v[20:23], v[48:51], v[204:207], v[20:23]\n" \
  "s_cselect_b32 s76, 0x40000, s62\n" \
  "s_add_u32 s74, s59, 4096\n" \
  "s_waitcnt lgkmcnt(2)\n" \
  "v_mfma_f32_16x16x32_bf16 v[12:15], v[80:83], v[36:39], v[12:15]\n" \
  "ds_read_b128 v[48:51], v7 offset:14336\n" \
  "v_mov_b64 v[24:25], 0\n" \
  "v_lshrrev_b32 v26, 1, v6\n" \
  "buffer_store_dwordx2 v[24:25], v26, s[48:51], s61 offen sc1\n" \
  "v_mfma_f32_16x16x32_bf16 v[16:19], v[144:147], v[36:39], v[16:19]\n" \
  "v_mfma_f32_16x16x32_bf16 v[20:23], v[36:39], v[208:211], v[20:23]\n" \
  "s_waitcnt lgkmcnt(2)\n" \
  "v_mfma_f32_16x16x32_bf16 v[12:15], v[84:87], v[40:43], v[12:15]\n" \
  "ds_read_b128 v[36:39], v7 offset:16384\n" \
  "v_mfma_f32_16x16x32_bf16 v[16:19], v[148:151], v[40:43], v[16:19]\n" \
  "v_mfma_f32_16x16x32_bf16 v[20:23], v[40:43], v[212:215], v[20:23]\n" \
  "s_waitcnt lgkmcnt(2)\n" \
  "v_mfma_f32_16x16x32_bf16 v[12:15], v[88:91], v[44:47], v[12:15]\n" \
  "ds_read_b128 v[40:43], v7 offset:18432\n" \
  "v_mfma_f32_16x16x32_bf16 v[16:19], v[152:155], v[44:47], v[16:19]\n" \
  "v_mfma_f32_16x16x32_bf16 v[20:23], v[44:47], v[216:219], v[20:23]\n" \
  "s_waitcnt lgkmcnt(2)\n" \
  "v_mfma_f32_16x16x32_bf16 v[12:15], v[92:95], v[48:51], v[12:15]\n" \
  "ds_read_b128 v[44:47], v7 offset:20480\n" \
  "v_mfma_f32_16x16x32_bf16 v[16:19], v[156:159], v[48:51], v[16:19]\n" \
  "v_mfma_f32_16x16x32_bf16 v[20:23], v[48:51], v[220:223], v[20:23]\n" \
  "s_waitcnt lgkmcnt(2)\n" \
  "v_mfma_f32_16x16x32_bf16 v[12:15], v[96:99], v[36:39], v[12:15]\n" \
  "ds_read_b128 v[48:51], v7 offset:22528\n" \
  "v_mfma_f32_16x16x32_bf16 v[16:19], v[160:163], v[36:39], v[16:19]\n" \
  "v_mfma_f32_16x16x32_bf16 v[20:23], v[36:39], v[224:227], v[20:23]\n" \
  "s_waitcnt lgkmcnt(2)\n" \
  "v_mfma_f32_16x16x32_bf16 v[12:15], v[100:103], v[40:43], v[12:15]\n" \
  "ds_read_b128 v[36:39], v7 offset:24576\n" \
  "v_mfma_f32_16x16x32_bf16 v[16:19], v[164:167], v[40:43], v[16:19]\n" \
  "v_mfma_f32_16x16x32_bf16 v[20:23], v[40:43], v[228:231], v[20:23]\n" \
  "s_waitcnt lgkmcnt(2)\n" \
  "v_mfma_f32_16x16x32_bf16 v[12:15], v[104:107], v[44:47], v[12:15]\n" \
  "ds_read_b128 v[40:43], v7 offset:26624\n" \
  "v_mfma_f32_16x16x32_bf16 v[16:19], v[168:171], v[44:47], v[16:19]\n" \
  "v_mfma_f32_16x16x32_bf16 v[20:23], v[44:47], v[232:235], v[20:23]\n" \
  "s_waitcnt lgkmcnt(2)\n" \
  "v_mfma_f32_16x16x32_bf16 v[12:15], v[108:111], v[48:51], v[12:15]\n" \
  "ds_read_b128 v[44:47], v7 offset:28672\n" \
  "v_mfma_f32_16x16x32_bf16 v[16:19], v[172:175], v[48:51], v[16:19]\n" \
  "v_mfma_f32_16x16x32_bf16 v[20:23], v[48:51], v[236:239], v[20:23]\n" \
  "s_waitcnt lgkmcnt(2)\n" \
  "v_mfma_f32_16x16x32_bf16 v[12:15], v[112:115], v[36:39], v[12:15]\n" \
  "ds_read_b128 v[48:51], v7 offset:30720\n" \
  "v_mfma_f32_16x16x32_bf16 v[16:19], v[176:179], v[36:39], v[16:19]\n" \
  "v_mfma_f32_16x16x32_bf16 v[20:23], v[36:39], v[240:243], v[20:23]\n" \
  "s_waitcnt lgkmcnt(2)\n" \
  "v_mfma_f32_16x16x32_bf16 v[12:15], v[116:119], v[40:43], v[12:15]\n" \
  "ds_read_b128 v[36:39], v7 offset:256\n" \
  "v_mfma_f32_16x16x32_bf16 v[16:19], v[180:183], v[40:43], v[16:19]\n" \
  "v_mfma_f32_16x16x32_bf16 v[20:23], v[40:43], v[244:247], v[20:23]\n" \
  "s_waitcnt lgkmcnt(2)\n" \
  "v_mfma_f32_16x16x32_bf16 v[12:15], v[120:123], v[44:47], v[12:15]\n" \
  "ds_read_b128 v[40:43], v7 offset:2304\n" \
  "v_mfma_f32_16x16x32_bf16 v[16:19], v[184:187], v[44:47], v[16:19]\n" \
  "v_mfma_f32_16x16x32_bf16 v[20:23], v[44:47], v[248:251], v[20:23]\n" \
  "s_waitcnt lgkmcnt(2)\n" \
  "v_mfma_f32_16x16x32_bf16 v[12:15], v[124:127], v[48:51], v[12:15]\n" \
  "ds_read_b128 v[44:47], v7 offset:4352\n" \
  "v_mfma_f32_16x16x32_bf16 v[16:19], v[188:191], v[48:51], v[16:19]\n" \
  "v_mfma_f32_16x16x32_bf16 v[20:23], v[48:51], v[252:255], v[20:23]\n" \
  "s_barrier\n" \
  "s_waitcnt lgkmcnt(2)\n" \
  "v_mfma_f32_16x16x32_bf16 v[24:27], v[64:67], v[36:39], 0\n" \
  "ds_read_b128 v[48:51], v7 offset:6400\n" \
  "v_mfma_f32_16x16x32_bf16 v[28:31], v[128:131], v[36:39], 0\n" \
  "ds_read2_b32 v[52:53], v8 offset0:0 offset1:0\n" \
  "v_mfma_f32_16x16x32_bf16 v[32:35], v[36:39], v[192:195], 0\n" \
  "s_waitcnt lgkmcnt(3)\n" \
  "v_mfma_f32_16x16x32_bf16 v[24:27], v[68:71], v[40:43], v[24:27]\n" \
  "ds_read_b128 v[36:39], v7 offset:8448\n" \
  "v_mfma_f32_16x16x32_bf16 v[28:31], v[132:135], v[40:43], v[28:31]\n" \
  "ds_read2_b32 v[54:55], v8 offset0:16 offset1:16\n" \
  "v_mfma_f32_16x16x32_bf16 v[32:35], v[40:43], v[196:199], v[32:35]\n" \
  "s_waitcnt lgkmcnt(4)\n" \
  "v_mfma_f32_16x16x32_bf16 v[24:27], v[72:75], v[44:47], v[24:27]\n" \
  "ds_read_b128 v[40:43], v7 offset:10496\n" \
  "v_mfma_f32_16x16x32_bf16 v[28:31], v[136:139], v[44:47], v[28:31]\n" \
  "ds_read2_b32 v[56:57], v8 offset0:32 offset1:48\n" \
  "v_mfma_f32_16x16x32_bf16 v[32:35], v[44:47], v[200:203], v[32:35]\n" \
  "s_waitcnt lgkmcnt(5)\n" \
  "v_mfma_f32_16x16x32_bf16 v[24:27], v[76:79], v[48:51], v[24:27]\n" \
  "ds_read_b128 v[44:47], v7 offset:12544\n" \
  "v_mfma_f32_16x16x32_bf16 v[28:31], v[140:143], v[48:51], v[28:31]\n" \
  "ds_read_b128 v[58:61], v9 offset:128\n" \
  "v_mfma_f32_16x16x32_bf16 v[32:35], v[48:51], v[204:207], v[32:35]\n" \
  "s_waitcnt lgkmcnt(6)\n" \
  "v_mfma_f32_16x16x16_bf16 v[12:15], v[0:1], v[52:53], v[12:15]\n" \
  "v_mfma_f32_16x16x16_bf16 v[16:19], v[2:3], v[52:53], v[16:19]\n" \
  "v_mfma_f32_16x16x16_bf16 v[20:23], v[52:53], v[4:5], v[20:23]\n" \
  "s_waitcnt lgkmcnt(4)\n" \
  "v_mfma_f32_16x16x16_bf16 v[24:27], v[0:1], v[54:55], v[24:27]\n" \
  "v_mfma_f32_16x16x16_bf16 v[28:31], v[2:3], v[54:55], v[28:31]\n" \
  "v_mfma_f32_16x16x16_bf16 v[32:35], v[54:55], v[4:5], v[32:35]\n" \
  "v_mfma_f32_16x16x32_bf16 v[24:27], v[80:83], v[36:39], v[24:27]\n" \
  "ds_read_b128 v[48:51], v7 offset:14592\n" \
  "v_mfma_f32_16x16x32_bf16 v[28:31], v[144:147], v[36:39], v[28:31]\n" \
  "ds_read_b128 v[52:55], v9 offset:192\n" \
  "v_mfma_f32_16x16x32_bf16 v[32:35], v[36:39], v[208:211], v[32:35]\n" \
  "s_waitcnt lgkmcnt(5)\n" \
  "v_mfma_f32_16x16x32_bf16 v[24:27], v[84:87], v[40:43], v[24:27]\n" \
  "ds_read_b128 v[36:39], v7 offset:16640\n" \
  "v_mfma_f32_16x16x32_bf16 v[28:31], v[148:151], v[40:43], v[28:31]\n" \
  "v_mfma_f32_16x16x32_bf16 v[32:35], v[40:43], v[212:215], v[32:35]\n" \
  "s_waitcnt lgkmcnt(4)\n" \
  "v_mfma_f32_16x16x32_bf16 v[24:27], v[88:91], v[44:47], v[24:27]\n" \
  "ds_read_b128 v[40:43], v7 offset:18688\n" \
  "v_mfma_f32_16x16x32_bf16 v[28:31], v[152:155], v[44:47], v[28:31]\n" \
  "v_mfma_f32_16x16x32_bf16 v[32:35], v[44:47], v[216:219], v[32:35]\n" \
  "s_waitcnt lgkmcnt(3)\n" \
  "v_mfma_f32_16x16x32_bf16 v[24:27], v[92:95], v[48:51], v[24:27]\n" \
  "ds_read_b128 v[44:47], v7 offset:20736\n" \
  "v_mfma_f32_16x16x32_bf16 v[28:31], v[156:159], v[48:51], v[28:31]\n" \
  "v_mfma_f32_16x16x32_bf16 v[32:35], v[48:51], v[220:223], v[32:35]\n" \
  "s_waitcnt lgkmcnt(2)\n" \
  "v_mfma_f32_16x16x32_bf16 v[24:27], v[96:99], v[36:39], v[24:27]\n" \
  "ds_read_b128 v[48:51], v7 offset:22784\n" \
  "v_mfma_f32_16x16x32_bf16 v[28:31], v[160:163], v[36:39], v[28:31]\n" \
  "v_mfma_f32_16x16x32_bf16 v[32:35], v[36:39], v[224:227], v[32:35]\n" \
  "s_waitcnt lgkmcnt(2)\n" \
  "v_mfma_f32_16x16x32_bf16 v[24:27], v[100:103], v[40:43], v[24:27]\n" \
  "ds_read_b128 v[36:39], v7 offset:24832\n" \
  "v_mfma_f32_16x16x32_bf16 v[28:31], v[164:167], v[40:43], v[28:31]\n" \
  "buffer_load_dword v11, v10, s[52:55], s76 offen sc1\n" \
  "v_mfma_f32_16x16x32_bf16 v[32:35], v[40:43], v[228:231], v[32:35]\n" \
  "s_waitcnt lgkmcnt(2)\n" \
  "v_mfma_f32_16x16x32_bf16 v[24:27], v[104:107], v[44:47], v[24:27]\n" \
  "ds_read_b128 v[40:43], v7 offset:26880\n" \
  "v_mfma_f32_16x16x32_bf16 v[28:31], v[168:171], v[44:47], v[28:31]\n" \
  "v_mfma_f32_16x16x32_bf16 v[32:35], v[44:47], v[232:235], v[32:35]\n" \
  "s_waitcnt lgkmcnt(2)\n" \
  "v_mfma_f32_16x16x32_bf16 v[24:27], v[108:111], v[48:51], v[24:27]\n" \
  "ds_read_b128 v[44:47], v7 offset:28928\n" \
  "v_mfma_f32_16x16x32_bf16 v[28:31], v[172:175], v[48:51], v[28:31]\n" \
  "v_mfma_f32_16x16x32_bf16 v[32:35], v[48:51], v[236:239], v[32:35]\n" \
  "s_waitcnt lgkmcnt(2)\n" \
  "v_mfma_f32_16x16x32_bf16 v[24:27], v[112:115], v[36:39], v[24:27]\n" \
  "ds_read_b128 v[48:51], v7 offset:30976\n" \
  "v_mfma_f32_16x16x32_bf16 v[28:31], v[176:179], v[36:39], v[28:31]\n" \
  "v_mfma_f32_16x16x32_bf16 v[32:35], v[36:39], v[240:243], v[32:35]\n" \
  "s_waitcnt lgkmcnt(2)\n" \
  "v_mfma_f32_16x16x32_bf16 v[24:27], v[116:119], v[40:43], v[24:27]\n" \
  "v_mfma_f32_16x16x32_bf16 v[28:31], v[180:183], v[40:43], v[28:31]\n" \
  "v_mfma_f32_16x16x32_bf16 v[32:35], v[40:43], v[244:247], v[32:35]\n" \
  "s_waitcnt lgkmcnt(1)\n" \
  "v_mfma_f32_16x16x32_bf16 v[24:27], v[120:123], v[44:47], v[24:27]\n" \
  "v_mfma_f32_16x16x32_bf16 v[28:31], v[184:187], v[44:47], v[28:31]\n" \
  "v_mfma_f32_16x16x32_bf16 v[32:35], v[44:47], v[248:251], v[32:35]\n" \
  "s_waitcnt lgkmcnt(0)\n" \
  "v_mfma_f32_16x16x32_bf16 v[24:27], v[124:127], v[48:51], v[24:27]\n" \
  "v_mfma_f32_16x16x32_bf16 v[28:31], v[188:191], v[48:51], v[28:31]\n" \
  "v_mfma_f32_16x16x32_bf16 v[32:35], v[48:51], v[252:255], v[32:35]\n" \
  "s_setprio 2\n" \
  "v_mul_f32 v12, v12, v56\n" \
  "v_mul_f32 v13, v13, v56\n" \
  "v_mul_f32 v14, v14, v56\n" \
  "v_cvt_pk_bf16_f32 v12, v12, v13\n" \
  "v_mul_f32 v15, v15, v56\n" \
  "v_mul_f32 v16, v16, v56\n" \
  "v_cvt_pk_bf16_f32 v13, v14, v15\n" \
  "v_mul_f32 v17, v17, v56\n" \
  "v_mul_f32 v18, v18, v56\n" \
  "v_cvt_pk_bf16_f32 v14, v16, v17\n" \
  "v_mul_f32 v19, v19, v56\n" \
  "v_cvt_pk_bf16_f32 v15, v18, v19\n" \
  "buffer_store_dwordx4 v[12:15], v6, s[40:43], s59 offen offset:0 sc1\n" \
  "v_mul_f32 v24, v24, v57\n" \
  "v_mul_f32 v25, v25, v57\n" \
  "v_mul_f32 v26, v26, v57\n" \
  "v_cvt_pk_bf16_f32 v24, v24, v25\n" \
  "v_mul_f32 v27, v27, v57\n" \
  "v_mul_f32 v28, v28, v57\n" \
  "v_cvt_pk_bf16_f32 v25, v26, v27\n" \
  "v_mul_f32 v29, v29, v57\n" \
  "v_mul_f32 v30, v30, v57\n" \
  "v_cvt_pk_bf16_f32 v26, v28, v29\n" \
  "v_mul_f32 v31, v31, v57\n" \
  "v_cvt_pk_bf16_f32 v27, v30, v31\n" \
  "buffer_store_dwordx4 v[24:27], v6, s[40:43], s74 offen offset:0 sc1\n" \
  "v_mul_f32 v20, v20, v58\n" \
  "v_mul_f32 v21, v21, v59\n" \
  "v_mul_f32 v22, v22, v60\n" \
  "v_mul_f32 v23, v23, v61\n" \
  "v_cvt_pk_bf16_f32 v20, v20, v21\n" \
  "v_cvt_pk_bf16_f32 v21, v22, v23\n" \
  "v_mul_f32 v32, v32, v52\n" \
  "v_mul_f32 v33, v33, v53\n" \
  "v_mul_f32 v34, v34, v54\n" \
  "v_mul_f32 v35, v35, v55\n" \
  "v_cvt_pk_bf16_f32 v22, v32, v33\n" \
  "v_cvt_pk_bf16_f32 v23, v34, v35\n" \
  "buffer_store_dwordx4 v[20:23], v6, s[44:47], s60 offen offset:0 sc1\n" \
  "s_setprio 0\n" \
  "s_add_u32 s59, s59, 0x80000\n" \
  "s_add_u32 s60, s60, 0x40000\n" \
  "s_add_u32 s61, s61, 0x10000\n" \
  "v_xor_b32 v7, 0x8000, v7\n" \
  "s_cmp_eq_u32 s69, 1\n" \
  "s_waitcnt vmcnt(3)\n" \
  "s_cbranch_scc1 .LQA_GO_%=\n" \
  "v_cmp_eq_u32 vcc, 0, v11\n" \
  "s_cmp_eq_u64 vcc, 0\n" \
  "s_cbranch_scc0 .LQA_NOX_%=\n" \
  "s_mov_b32 m0, s58\n" \
  "s_add_u32 s74, s56, 4096\n" \
  "s_addc_u32 s75, s57, 0\n" \
  "s_add_u32 s76, s58, 4096\n" \
  "global_load_lds_dwordx4 v6, s[56:57] offset:0 sc1\n" \
  "global_load_lds_dwordx4 v6, s[56:57] offset:1024 sc1\n" \
  "global_load_lds_dwordx4 v6, s[56:57] offset:2048 sc1\n" \
  "global_load_lds_dwordx4 v6, s[56:57] offset:3072 sc1\n" \
  "s_mov_b32 m0, s76\n" \
  "s_nop 0\n" \
  "global_load_lds_dwordx4 v6, s[74:75] offset:0 sc1\n" \
  "global_load_lds_dwordx4 v6, s[74:75] offset:1024 sc1\n" \
  "global_load_lds_dwordx4 v6, s[74:75] offset:2048 sc1\n" \
  "global_load_lds_dwordx4 v6, s[74:75] offset:3072 sc1\n" \
  "s_mov_b32 s69, 2\n" \
  "s_waitcnt vmcnt(8)\n" \
  "s_branch .LQA_CNT_%=\n" \
  ".LQA_NOX_%=:\n" \
  "s_waitcnt vmcnt(0)\n" \
  ".LQA_CNT_%=:\n" \
  "v_mov_b32 v52, s80\n" \
  "v_mov_b32 v53, 0x10000\n" \
  "s_mov_b64 exec, 1\n" \
  "ds_add_rtn_u32 v54, v52, v53\n" \
  "s_mov_b64 exec, -1\n" \
  "s_add_u32 s67, s67, 4\n" \
  "s_add_u32 s64, s64, 4096\n" \
  "s_addc_u32 s65, s65, 0\n" \
  "v_mov_b32 v55, 0\n" \
  "s_waitcnt lgkmcnt(0)\n" \
  "v_readfirstlane_b32 s74, v54\n" \
  "s_and_b32 s76, s74, 0x30000\n" \
  "s_cmp_lg_u32 s76, 0x30000\n" \
  "s_cbranch_scc1 .LQA_FWN_%=\n" \
  "s_mov_b32 s79, 0x100000\n" \
  "s_branch .LQA_FWT_%=\n" \
  ".LQA_FW_%=:\n" \
  "ds_read_b32 v54, v52\n" \
  "s_sub_u32 s79, s79, 1\n" \
  "s_cbranch_scc1 .LQA_FWE_%=\n" \
  "s_waitcnt lgkmcnt(0)\n" \
  "v_readfirstlane_b32 s74, v54\n" \
  ".LQA_FWT_%=:\n" \
  "s_and_b32 s74, s74, 0xffff\n" \
  "s_cmp_lt_u32 s74, s67\n" \
  "s_cbranch_scc1 .LQA_FW_%=\n" \
  ".LQA_FWE_%=:\n" \
  "s_mov_b64 exec, 1\n" \
  "global_store_dword v55, v53, s[64:65] offset:-4096 sc1\n" \
  "s_mov_b64 exec, -1\n" \
  "s_waitcnt vmcnt(1)\n" \
  "s_branch .LQA_GO_%=\n" \
  ".LQA_FWN_%=:\n" \
  "s_waitcnt vmcnt(0)\n" \
  ".LQA_GO_%=:\n" \
  "s_sub_u32 s66, s66, 1\n" \
  "s_cbranch_scc1 .LQA_LAST_%=\n" \
  "s_cmp_lg_u32 s69, 0\n" \
  "s_cbranch_scc1 .LQA_NEXT_%=\n" \
  ".LQA_GATE_%=:\n" \
  "s_memrealtime s[74:75]\n" \
  "s_waitcnt lgkmcnt(0)\n" \
  "s_sub_i32 s74, s68, s74\n" \
  "s_cmp_gt_u32 s74, %[gq]\n" \
  "s_cbranch_scc1 .LQA_POLL_%=\n" \
  "s_sleep 3\n" \
  "s_branch .LQA_GATE_%=\n" \
  ".LQA_POLL_%=:\n" \
  "s_mov_b32 s79, 0x40000\n" \
  ".LQA_AGAIN_%=:\n" \
  "buffer_load_dword v11, v10, s[52:55], s62 offen sc1\n" \
  "s_sub_u32 s79, s79, 1\n" \
  "s_cbranch_scc1 .LQA_SEEN_%=\n" \
  "s_waitcnt vmcnt(0)\n" \
  "v_cmp_eq_u32 vcc, 0, v11\n" \
  "s_cmp_eq_u64 vcc, 0\n" \
  "s_cbranch_scc1 .LQA_SEEN_%=\n" \
  "s_sleep %[sq]\n" \
  "s_cmp_lg_u32 s63, 0\n" \
  "s_cbranch_scc1 .LQA_AGAIN_%=\n" \
  "s_sleep %[s1]\n" \
  "s_branch .LQA_AGAIN_%=\n" \
  ".LQA_SEEN_%=:\n" \
  "s_mov_b32 m0, s58\n" \
  "s_add_u32 s74, s56, 4096\n" \
  "s_addc_u32 s75, s57, 0\n" \
  "s_add_u32 s76, s58, 4096\n" \
  "global_load_lds_dwordx4 v6, s[56:57] offset:0 sc1\n" \
  "global_load_lds_dwordx4 v6, s[56:57] offset:1024 sc1\n" \
  "global_load_lds_dwordx4 v6, s[56:57] offset:2048 sc1\n" \
  "global_load_lds_dwordx4 v6, s[56:57] offset:3072 sc1\n" \
  "s_mov_b32 m0, s76\n" \
  "s_nop 0\n" \
  "global_load_lds_dwordx4 v6, s[74:75] offset:0 sc1\n" \
  "global_load_lds_dwordx4 v6, s[74:75] offset:1024 sc1\n" \
  "global_load_lds_dwordx4 v6, s[74:75] offset:2048 sc1\n" \
  "global_load_lds_dwordx4 v6, s[74:75] offset:3072 sc1\n" \
  "s_memrealtime s[76:77]\n" \
  "s_waitcnt lgkmcnt(0)\n" \
  "s_add_u32 s68, s76, %[gq]\n" \
  "s_waitcnt vmcnt(0)\n" \
  ".LQA_NEXT_%=:\n" \
  "s_add_u32 s56, s56, 0x8000\n" \
  "s_addc_u32 s57, s57, 0\n" \
  "s_xor_b32 s58, s58, 0x8000\n" \
  "s_add_u32 s62, s62, 4096\n" \
  "buffer_load_dword v11, v10, s[52:55], s62 offen sc1\n" \
  "s_branch .LQA_TOP_%=\n" \
  ".LQA_LAST_%=:\n" \
  "s_barrier\n" \
  "s_cmp_lg_u32 s78, 0\n" \
  "s_cbranch_scc1 .LQA_END_%=\n" \
  "s_add_u32 s67, s67, 4\n" \
  "s_add_u32 s64, s64, 4096\n" \
  "s_addc_u32 s65, s65, 0\n" \
  "s_mov_b32 s74, 0\n" \
  "s_mov_b32 s79, 0x100000\n" \
  "s_branch .LQA_FLT_%=\n" \
  ".LQA_FL_%=:\n" \
  "ds_read_b32 v54, v52\n" \
  "s_sub_u32 s79, s79, 1\n" \
  "s_cbranch_scc1 .LQA_FLE_%=\n" \
  "s_waitcnt lgkmcnt(0)\n" \
  "v_readfirstlane_b32 s74, v54\n" \
  ".LQA_FLT_%=:\n" \
  "s_and_b32 s74, s74, 0xffff\n" \
  "s_cmp_lt_u32 s74, s67\n" \
  "s_cbranch_scc1 .LQA_FL_%=\n" \
  ".LQA_FLE_%=:\n" \
  "s_mov_b64 exec, 1\n" \
  "global_store_dword v55, v53, s[64:65] offset:-4096 sc1\n" \
  "s_mov_b64 exec, -1\n" \
  ".LQA_END_%=:\n" \
  "s_endpgm\n"
#define QKV_ASM_1 \
  "s_barrier\n" \
  "ds_read_b128 v[36:39], v10 offset:0\n" \
  "ds_read_b128 v[40:43], v7 offset:0\n" \
  "ds_read_b128 v[44:47], v10 offset:2048\n" \
  "ds_read_b128 v[48:51], v7 offset:2048\n" \
  ".p2align 6\n" \
  ".LQB_AL_%=:\n" \
  ".LQB_TOP_%=:\n" \
  "s_setprio 3\n" \
  "s_waitcnt lgkmcnt(3)\n" \
  "v_mfma_f32_16x16x32_bf16 v[56:59], v[36:39], v[36:39], 0\n" \
  "ds_read_b128 v[52:55], v10 offset:4096\n" \
  "s_waitcnt lgkmcnt(3)\n" \
  "v_mfma_f32_16x16x32_bf16 v[12:15], v[40:43], v[192:195], 0\n" \
  "ds_read_b128 v[36:39], v7 offset:4096\n" \
  "s_waitcnt lgkmcnt(3)\n" \
  "v_mfma_f32_16x16x32_bf16 v[56:59], v[44:47], v[44:47], v[56:59]\n" \
  "ds_read_b128 v[40:43], v10 offset:6144\n" \
  "s_waitcnt lgkmcnt(3)\n" \
  "v_mfma_f32_16x16x32_bf16 v[12:15], v[48:51], v[196:199], v[12:15]\n" \
  "ds_read_b128 v[44:47], v7 offset:6144\n" \
  "s_waitcnt lgkmcnt(3)\n" \
  "v_mfma_f32_16x16x32_bf16 v[56:59], v[52:55], v[52:55], v[56:59]\n" \
  "ds_read_b128 v[48:51], v10 offset:8192\n" \
  "s_waitcnt lgkmcnt(3)\n" \
  "v_mfma_f32_16x16x32_bf16 v[12:15], v[36:39], v[200:203], v[12:15]\n" \
  "ds_read_b128 v[52:55], v7 offset:8192\n" \
  "s_waitcnt lgkmcnt(3)\n" \
  "v_mfma_f32_16x16x32_bf16 v[56:59], v[40:43], v[40:43], v[56:59]\n" \
  "ds_read_b128 v[36:39], v10 offset:10240\n" \
  "s_waitcnt lgkmcnt(3)\n" \
  "v_mfma_f32_16x16x32_bf16 v[12:15], v[44:47], v[204:207], v[12:15]\n" \
  "ds_read_b128 v[40:43], v7 offset:10240\n" \
  "s_waitcnt lgkmcnt(3)\n" \
  "v_mfma_f32_16x16x32_bf16 v[56:59], v[48:51], v[48:51], v[56:59]\n" \
  "ds_read_b128 v[44:47], v10 offset:12288\n" \
  "s_waitcnt lgkmcnt(3)\n" \
  "v_mfma_f32_16x16x32_bf16 v[12:15], v[52:55], v[208:211], v[12:15]\n" \
  "ds_read_b128 v[48:51], v7 offset:12288\n" \
  "s_waitcnt lgkmcnt(3)\n" \
  "v_mfma_f32_16x16x32_bf16 v[56:59], v[36:39], v[36:39], v[56:59]\n" \
  "ds_read_b128 v[52:55], v10 offset:14336\n" \
  "s_waitcnt lgkmcnt(3)\n" \
  "v_mfma_f32_16x16x32_bf16 v[12:15], v[40:43], v[212:215], v[12:15]\n" \
  "ds_read_b128 v[36:39], v7 offset:14336\n" \
  "s_waitcnt lgkmcnt(3)\n" \
  "v_mfma_f32_16x16x32_bf16 v[56:59], v[44:47], v[44:47], v[56:59]\n" \
  "ds_read_b128 v[40:43], v10 offset:16384\n" \
  "s_waitcnt lgkmcnt(3)\n" \
  "v_mfma_f32_16x16x32_bf16 v[12:15], v[48:51], v[216:219], v[12:15]\n" \
  "ds_read_b128 v[44:47], v7 offset:16384\n" \
  "s_waitcnt lgkmcnt(3)\n" \
  "v_mfma_f32_16x16x32_bf16 v[56:59], v[52:55], v[52:55], v[56:59]\n" \
  "ds_read_b128 v[48:51], v10 offset:18432\n" \
  "s_waitcnt lgkmcnt(3)\n" \
  "v_mfma_f32_16x16x32_bf16 v[12:15], v[36:39], v[220:223], v[12:15]\n" \
  "ds_read_b128 v[52:55], v7 offset:18432\n" \
  "s_waitcnt lgkmcnt(3)\n" \
  "v_mfma_f32_16x16x32_bf16 v[56:59], v[40:43], v[40:43], v[56:59]\n" \
  "ds_read_b128 v[36:39], v10 offset:20480\n" \
  "s_waitcnt lgkmcnt(3)\n" \
  "v_mfma_f32_16x16x32_bf16 v[12:15], v[44:47], v[224:227], v[12:15]\n" \
  "ds_read_b128 v[40:43], v7 offset:20480\n" \
  "s_waitcnt lgkmcnt(3)\n" \
  "v_mfma_f32_16x16x32_bf16 v[56:59], v[48:51], v[48:51], v[56:59]\n" \
  "ds_read_b128 v[44:47], v10 offset:22528\n" \
  "s_waitcnt lgkmcnt(3)\n" \
  "v_mfma_f32_16x16x32_bf16 v[12:15], v[52:55], v[228:231], v[12:15]\n" \
  "ds_read_b128 v[48:51], v7 offset:22528\n" \
  "s_waitcnt lgkmcnt(3)\n" \
  "v_mfma_f32_16x16x32_bf16 v[56:59], v[36:39], v[36:39], v[56:59]\n" \
  "ds_read_b128 v[52:55], v10 offset:24576\n" \
  "s_waitcnt lgkmcnt(3)\n" \
  "v_mfma_f32_16x16x32_bf16 v[12:15], v[40:43], v[232:235], v[12:15]\n" \
  "ds_read_b128 v[36:39], v7 offset:24576\n" \
  "s_waitcnt lgkmcnt(3)\n" \
  "v_mfma_f32_16x16x32_bf16 v[56:59], v[44:47], v[44:47], v[56:59]\n" \
  "ds_read_b128 v[40:43], v10 offset:26624\n" \
  "s_waitcnt lgkmcnt(3)\n" \
  "v_mfma_f32_16x16x32_bf16 v[12:15], v[48:51], v[236:239], v[12:15]\n" \
  "ds_read_b128 v[44:47], v7 offset:26624\n" \
  "s_waitcnt lgkmcnt(3)\n" \
  "v_mfma_f32_16x16x32_bf16 v[56:59], v[52:55], v[52:55], v[56:59]\n" \
  "ds_read_b128 v[48:51], v10 offset:28672\n" \
  "s_waitcnt lgkmcnt(3)\n" \
  "v_mfma_f32_16x16x32_bf16 v[12:15], v[36:39], v[240:243], v[12:15]\n" \
  "ds_read_b128 v[52:55], v7 offset:28672\n" \
  "s_waitcnt lgkmcnt(3)\n" \
  "v_mfma_f32_16x16x32_bf16 v[56:59], v[40:43], v[40:43], v[56:59]\n" \
  "ds_read_b128 v[36:39], v10 offset:30720\n" \
  "s_waitcnt lgkmcnt(3)\n" \
  "v_mfma_f32_16x16x32_bf16 v[12:15], v[44:47], v[244:247], v[12:15]\n" \
  "ds_read_b128 v[40:43], v7 offset:30720\n" \
  "s_waitcnt lgkmcnt(3)\n" \
  "v_mfma_f32_16x16x32_bf16 v[56:59], v[48:51], v[48:51], v[56:59]\n" \
  "ds_read_b128 v[44:47], v7 offset:256\n" \
  "s_waitcnt lgkmcnt(3)\n" \
  "v_mfma_f32_16x16x32_bf16 v[12:15], v[52:55], v[248:251], v[12:15]\n" \
  "ds_read_b128 v[48:51], v7 offset:2304\n" \
  "s_waitcnt lgkmcnt(3)\n" \
  "v_mfma_f32_16x16x32_bf16 v[56:59], v[36:39], v[36:39], v[56:59]\n" \
  "ds_read_b128 v[52:55], v7 offset:4352\n" \
  "s_waitcnt lgkmcnt(3)\n" \
  "v_mfma_f32_16x16x32_bf16 v[12:15], v[40:43], v[252:255], v[12:15]\n" \
  "ds_read_b128 v[36:39], v7 offset:6400\n" \
  "s_waitcnt lgkmcnt(3)\n" \
  "v_mfma_f32_16x16x32_bf16 v[16:19], v[44:47], v[192:195], 0\n" \
  "ds_read_b128 v[40:43], v7 offset:8448\n" \
  "s_waitcnt lgkmcnt(3)\n" \
  "v_mfma_f32_16x16x32_bf16 v[16:19], v[48:51], v[196:199], v[16:19]\n" \
  "ds_read_b128 v[44:47], v7 offset:10496\n" \
  "s_waitcnt lgkmcnt(3)\n" \
  "v_mfma_f32_16x16x32_bf16 v[16:19], v[52:55], v[200:203], v[16:19]\n" \
  "ds_read_b128 v[48:51], v7 offset:12544\n" \
  "s_waitcnt lgkmcnt(3)\n" \
  "v_mfma_f32_16x16x32_bf16 v[16:19], v[36:39], v[204:207], v[16:19]\n" \
  "ds_read_b128 v[52:55], v7 offset:14592\n" \
  "s_waitcnt lgkmcnt(3)\n" \
  "v_mfma_f32_16x16x32_bf16 v[16:19], v[40:43], v[208:211], v[16:19]\n" \
  "ds_read_b128 v[36:39], v7 offset:16640\n" \
  "s_waitcnt lgkmcnt(3)\n" \
  "v_mfma_f32_16x16x32_bf16 v[16:19], v[44:47], v[212:215], v[16:19]\n" \
  "ds_read_b128 v[40:43], v7 offset:18688\n" \
  "s_waitcnt lgkmcnt(3)\n" \
  "v_mfma_f32_16x16x32_bf16 v[16:19], v[48:51], v[216:219], v[16:19]\n" \
  "ds_read_b128 v[44:47], v7 offset:20736\n" \
  "s_waitcnt lgkmcnt(3)\n" \
  "v_mfma_f32_16x16x32_bf16 v[16:19], v[52:55], v[220:223], v[16:19]\n" \
  "ds_read_b128 v[48:51], v7 offset:22784\n" \
  "s_waitcnt lgkmcnt(3)\n" \
  "v_mfma_f32_16x16x32_bf16 v[16:19], v[36:39], v[224:227], v[16:19]\n" \
  "ds_read_b128 v[52:55], v7 offset:24832\n" \
  "s_waitcnt lgkmcnt(3)\n" \
  "v_mfma_f32_16x16x32_bf16 v[16:19], v[40:43], v[228:231], v[16:19]\n" \
  "ds_read_b128 v[36:39], v7 offset:26880\n" \
  "s_waitcnt lgkmcnt(3)\n" \
  "v_mfma_f32_16x16x32_bf16 v[16:19], v[44:47], v[232:235], v[16:19]\n" \
  "ds_read_b128 v[40:43], v7 offset:28928\n" \
  "s_waitcnt lgkmcnt(3)\n" \
  "v_mfma_f32_16x16x32_bf16 v[16:19], v[48:51], v[236:239], v[16:19]\n" \
  "ds_read_b128 v[44:47], v7 offset:30976\n" \
  "s_waitcnt lgkmcnt(3)\n" \
  "v_mfma_f32_16x16x32_bf16 v[16:19], v[52:55], v[240:243], v[16:19]\n" \
  "ds_read_b128 v[48:51], v7 offset:0\n" \
  "s_waitcnt lgkmcnt(3)\n" \
  "v_mfma_f32_16x16x32_bf16 v[16:19], v[36:39], v[244:247], v[16:19]\n" \
  "ds_read_b128 v[52:55], v7 offset:2048\n" \
  "s_waitcnt lgkmcnt(3)\n" \
  "v_mfma_f32_16x16x32_bf16 v[16:19], v[40:43], v[248:251], v[16:19]\n" \
  "ds_read_b128 v[36:39], v7 offset:4096\n" \
  "s_waitcnt lgkmcnt(3)\n" \
  "v_mfma_f32_16x16x32_bf16 v[16:19], v[44:47], v[252:255], v[16:19]\n" \
  "ds_read_b128 v[40:43], v7 offset:6144\n" \
  "s_setprio 0\n" \
  "v_cndmask_b32 v56, v56, v57, s[70:71]\n" \
  "v_cndmask_b32 v57, v58, v59, s[70:71]\n" \
  "v_cndmask_b32 v56, v56, v57, s[72:73]\n" \
  "v_mov_b32 v57, v56\n" \
  "v_mov_b32 v58, v56\n" \
  "v_mul_f32 v56, 0x3b000000, v56\n" \
  "v_xor_b32 v10, v11, v10\n" \
  "v_permlane32_swap_b32 v57, v58\n" \
  "v_mul_f32 v57, 0xbb000000, v58\n" \
  "v_fma_f32 v56, -v57, v57, v56\n" \
  "v_max_f32 v56, 0, v56\n" \
  "v_cvt_pk_bf16_f32 v56, v56, v56\n" \
  "v_lshlrev_b32 v56, 16, v56\n" \
  "v_add_f32 v56, 0x3727c5ac, v56\n" \
  "v_cvt_pk_bf16_f32 v56, v56, v56\n" \
  "v_lshlrev_b32 v56, 16, v56\n" \
  "v_sqrt_f32 v56, v56\n" \
  "v_cvt_pk_bf16_f32 v57, v57, v57\n" \
  "v_cvt_pk_bf16_f32 v56, v56, v56\n" \
  "v_lshlrev_b32 v56, 16, v56\n" \
  "v_rcp_f32 v56, v56\n" \
  "s_mov_b64 exec, 0xf0000f\n" \
  "ds_write2_b32 v62, v57, v56 offset1:32\n" \
  "s_mov_b64 exec, -1\n" \
  "s_cmp_eq_u32 s66, 63\n" \
  "s_cbranch_scc1 .LQB_NOE_%=\n" \
  "v_mul_f32 v20, v20, v60\n" \
  "v_mul_f32 v21, v21, v60\n" \
  "v_mul_f32 v22, v22, v60\n" \
  "v_cvt_pk_bf16_f32 v20, v20, v21\n" \
  "v_mul_f32 v23, v23, v60\n" \
  "v_mul_f32 v24, v24, v60\n" \
  "v_cvt_pk_bf16_f32 v21, v22, v23\n" \
  "v_mul_f32 v25, v25, v60\n" \
  "v_mul_f32 v26, v26, v60\n" \
  "v_cvt_pk_bf16_f32 v22, v24, v25\n" \
  "v_mul_f32 v27, v27, v60\n" \
  "v_cvt_pk_bf16_f32 v23, v26, v27\n" \
  "buffer_store_dwordx4 v[20:23], v6, s[40:43], s59 offen offset:0 sc1\n" \
  "s_add_u32 s76, s59, 4096\n" \
  "v_mul_f32 v28, v28, v61\n" \
  "v_mul_f32 v29, v29, v61\n" \
  "v_mul_f32 v30, v30, v61\n" \
  "v_cvt_pk_bf16_f32 v28, v28, v29\n" \
  "v_mul_f32 v31, v31, v61\n" \
  "v_mul_f32 v32, v32, v61\n" \
  "v_cvt_pk_bf16_f32 v29, v30, v31\n" \
  "v_mul_f32 v33, v33, v61\n" \
  "v_mul_f32 v34, v34, v61\n" \
  "v_cvt_pk_bf16_f32 v30, v32, v33\n" \
  "v_mul_f32 v35, v35, v61\n" \
  "v_cvt_pk_bf16_f32 v31, v34, v35\n" \
  "buffer_store_dwordx4 v[28:31], v6, s[40:43], s76 offen offset:0 sc1\n" \
  "s_add_u32 s59, s59, 0x80000\n" \
  ".LQB_NOE_%=:\n" \
  "s_waitcnt lgkmcnt(0)\n" \
  "s_barrier\n" \
  "ds_read2_b32 v[56:57], v8 offset0:0 offset1:0\n" \
  "ds_read2_b32 v[58:59], v8 offset0:16 offset1:16\n" \
  "ds_read_b128 v[28:31], v9 offset:128\n" \
  "ds_read_b128 v[32:35], v9 offset:192\n" \
  "ds_read2_b32 v[60:61], v8 offset0:32 offset1:48\n" \
  "s_setprio 3\n" \
  "s_waitcnt lgkmcnt(3)\n" \
  "v_mfma_f32_16x16x16_bf16 v[12:15], v[56:57], v[4:5], v[12:15]\n" \
  "v_mfma_f32_16x16x16_bf16 v[16:19], v[58:59], v[4:5], v[16:19]\n" \
  "s_setprio 0\n" \
  "s_waitcnt lgkmcnt(0)\n" \
  "s_nop 4\n" \
  "v_mul_f32 v12, v12, v28\n" \
  "v_mul_f32 v13, v13, v29\n" \
  "v_mul_f32 v14, v14, v30\n" \
  "v_mul_f32 v15, v15, v31\n" \
  "v_cvt_pk_bf16_f32 v12, v12, v13\n" \
  "v_cvt_pk_bf16_f32 v13, v14, v15\n" \
  "v_mul_f32 v16, v16, v32\n" \
  "v_mul_f32 v17, v17, v33\n" \
  "v_mul_f32 v18, v18, v34\n" \
  "v_mul_f32 v19, v19, v35\n" \
  "v_cvt_pk_bf16_f32 v14, v16, v17\n" \
  "v_cvt_pk_bf16_f32 v15, v18, v19\n" \
  "buffer_store_dwordx4 v[12:15], v6, s[44:47], s60 offen offset:0 sc1\n" \
  "v_mov_b64 v[16:17], 0\n" \
  "v_lshrrev_b32 v18, 1, v6\n" \
  "buffer_store_dwordx2 v[16:17], v18, s[48:51], s61 offen sc1\n" \
  "s_setprio 0\n" \
  "s_add_u32 s60, s60, 0x40000\n" \
  "s_add_u32 s61, s61, 0x10000\n" \
  "s_sub_u32 s66, s66, 1\n" \
  "v_mov_b32 v12, s80\n" \
  "v_mov_b32 v13, 1\n" \
  "v_mfma_f32_16x16x16_bf16 v[20:23], v[0:1], v[56:57], 0\n" \
  "v_mfma_f32_16x16x16_bf16 v[24:27], v[2:3], v[56:57], 0\n" \
  "v_mfma_f32_16x16x16_bf16 v[28:31], v[0:1], v[58:59], 0\n" \
  "v_mfma_f32_16x16x16_bf16 v[32:35], v[2:3], v[58:59], 0\n" \
  "v_mfma_f32_16x16x32_bf16 v[20:23], v[64:67], v[48:51], v[20:23]\n" \
  "ds_read_b128 v[44:47], v7 offset:8192\n" \
  "v_mfma_f32_16x16x32_bf16 v[24:27], v[128:131], v[48:51], v[24:27]\n" \
  "v_mfma_f32_16x16x32_bf16 v[20:23], v[68:71], v[52:55], v[20:23]\n" \
  "ds_read_b128 v[48:51], v7 offset:10240\n" \
  "v_mfma_f32_16x16x32_bf16 v[24:27], v[132:135], v[52:55], v[24:27]\n" \
  "v_mfma_f32_16x16x32_bf16 v[20:23], v[72:75], v[36:39], v[20:23]\n" \
  "ds_read_b128 v[52:55], v7 offset:12288\n" \
  "v_mfma_f32_16x16x32_bf16 v[24:27], v[136:139], v[36:39], v[24:27]\n" \
  "v_mfma_f32_16x16x32_bf16 v[20:23], v[76:79], v[40:43], v[20:23]\n" \
  "ds_read_b128 v[36:39], v7 offset:14336\n" \
  "v_mfma_f32_16x16x32_bf16 v[24:27], v[140:143], v[40:43], v[24:27]\n" \
  "s_waitcnt lgkmcnt(3)\n" \
  "v_mfma_f32_16x16x32_bf16 v[20:23], v[80:83], v[44:47], v[20:23]\n" \
  "ds_read_b128 v[40:43], v7 offset:16384\n" \
  "v_mfma_f32_16x16x32_bf16 v[24:27], v[144:147], v[44:47], v[24:27]\n" \
  "s_waitcnt lgkmcnt(3)\n" \
  "v_mfma_f32_16x16x32_bf16 v[20:23], v[84:87], v[48:51], v[20:23]\n" \
  "ds_read_b128 v[44:47], v7 offset:18432\n" \
  "v_mfma_f32_16x16x32_bf16 v[24:27], v[148:151], v[48:51], v[24:27]\n" \
  "s_waitcnt lgkmcnt(3)\n" \
  "v_mfma_f32_16x16x32_bf16 v[20:23], v[88:91], v[52:55], v[20:23]\n" \
  "ds_read_b128 v[48:51], v7 offset:20480\n" \
  "s_waitcnt vmcnt(0)\n" \
  "s_mov_b64 exec, 1\n" \
  "ds_add_u32 v12, v13\n" \
  "s_mov_b64 exec, -1\n" \
  "v_mfma_f32_16x16x32_bf16 v[24:27], v[152:155], v[52:55], v[24:27]\n" \
  "s_waitcnt lgkmcnt(4)\n" \
  "v_mfma_f32_16x16x32_bf16 v[20:23], v[92:95], v[36:39], v[20:23]\n" \
  "ds_read_b128 v[52:55], v7 offset:22528\n" \
  "v_mfma_f32_16x16x32_bf16 v[24:27], v[156:159], v[36:39], v[24:27]\n" \
  "s_waitcnt lgkmcnt(4)\n" \
  "v_mfma_f32_16x16x32_bf16 v[20:23], v[96:99], v[40:43], v[20:23]\n" \
  "ds_read_b128 v[36:39], v7 offset:24576\n" \
  "v_mfma_f32_16x16x32_bf16 v[24:27], v[160:163], v[40:43], v[24:27]\n" \
  "s_waitcnt lgkmcnt(4)\n" \
  "v_mfma_f32_16x16x32_bf16 v[20:23], v[100:103], v[44:47], v[20:23]\n" \
  "ds_read_b128 v[40:43], v7 offset:26624\n" \
  "v_mfma_f32_16x16x32_bf16 v[24:27], v[164:167], v[44:47], v[24:27]\n" \
  "s_waitcnt lgkmcnt(4)\n" \
  "v_mfma_f32_16x16x32_bf16 v[20:23], v[104:107], v[48:51], v[20:23]\n" \
  "ds_read_b128 v[44:47], v7 offset:28672\n" \
  "v_mfma_f32_16x16x32_bf16 v[24:27], v[168:171], v[48:51], v[24:27]\n" \
  "s_waitcnt lgkmcnt(3)\n" \
  "v_mfma_f32_16x16x32_bf16 v[20:23], v[108:111], v[52:55], v[20:23]\n" \
  "ds_read_b128 v[48:51], v7 offset:30720\n" \
  "v_mfma_f32_16x16x32_bf16 v[24:27], v[172:175], v[52:55], v[24:27]\n" \
  "s_waitcnt lgkmcnt(3)\n" \
  "v_mfma_f32_16x16x32_bf16 v[20:23], v[112:115], v[36:39], v[20:23]\n" \
  "ds_read_b128 v[52:55], v7 offset:256\n" \
  "v_mfma_f32_16x16x32_bf16 v[24:27], v[176:179], v[36:39], v[24:27]\n" \
  "s_waitcnt lgkmcnt(3)\n" \
  "v_mfma_f32_16x16x32_bf16 v[20:23], v[116:119], v[40:43], v[20:23]\n" \
  "ds_read_b128 v[36:39], v7 offset:2304\n" \
  "v_mfma_f32_16x16x32_bf16 v[24:27], v[180:183], v[40:43], v[24:27]\n" \
  "s_waitcnt lgkmcnt(3)\n" \
  "v_mfma_f32_16x16x32_bf16 v[20:23], v[120:123], v[44:47], v[20:23]\n" \
  "ds_read_b128 v[40:43], v7 offset:4352\n" \
  "v_mfma_f32_16x16x32_bf16 v[24:27], v[184:187], v[44:47], v[24:27]\n" \
  "s_waitcnt lgkmcnt(3)\n" \
  "v_mfma_f32_16x16x32_bf16 v[20:23], v[124:127], v[48:51], v[20:23]\n" \
  "ds_read_b128 v[44:47], v7 offset:6400\n" \
  "v_mfma_f32_16x16x32_bf16 v[24:27], v[188:191], v[48:51], v[24:27]\n" \
  "s_waitcnt lgkmcnt(3)\n" \
  "v_mfma_f32_16x16x32_bf16 v[28:31], v[64:67], v[52:55], v[28:31]\n" \
  "ds_read_b128 v[48:51], v7 offset:8448\n" \
  "v_mfma_f32_16x16x32_bf16 v[32:35], v[128:131], v[52:55], v[32:35]\n" \
  "s_waitcnt lgkmcnt(3)\n" \
  "v_mfma_f32_16x16x32_bf16 v[28:31], v[68:71], v[36:39], v[28:31]\n" \
  "ds_read_b128 v[52:55], v7 offset:10496\n" \
  "v_mfma_f32_16x16x32_bf16 v[32:35], v[132:135], v[36:39], v[32:35]\n" \
  "s_waitcnt lgkmcnt(3)\n" \
  "v_mfma_f32_16x16x32_bf16 v[28:31], v[72:75], v[40:43], v[28:31]\n" \
  "ds_read_b128 v[36:39], v7 offset:12544\n" \
  "v_mfma_f32_16x16x32_bf16 v[32:35], v[136:139], v[40:43], v[32:35]\n" \
  "s_waitcnt lgkmcnt(3)\n" \
  "v_mfma_f32_16x16x32_bf16 v[28:31], v[76:79], v[44:47], v[28:31]\n" \
  "ds_read_b128 v[40:43], v7 offset:14592\n" \
  "v_mfma_f32_16x16x32_bf16 v[32:35], v[140:143], v[44:47], v[32:35]\n" \
  "s_waitcnt lgkmcnt(3)\n" \
  "v_mfma_f32_16x16x32_bf16 v[28:31], v[80:83], v[48:51], v[28:31]\n" \
  "ds_read_b128 v[44:47], v7 offset:16640\n" \
  "v_mfma_f32_16x16x32_bf16 v[32:35], v[144:147], v[48:51], v[32:35]\n" \
  "s_waitcnt lgkmcnt(3)\n" \
  "v_mfma_f32_16x16x32_bf16 v[28:31], v[84:87], v[52:55], v[28:31]\n" \
  "ds_read_b128 v[48:51], v7 offset:18688\n" \
  "v_mfma_f32_16x16x32_bf16 v[32:35], v[148:151], v[52:55], v[32:35]\n" \
  "s_waitcnt lgkmcnt(3)\n" \
  "v_mfma_f32_16x16x32_bf16 v[28:31], v[88:91], v[36:39], v[28:31]\n" \
  "ds_read_b128 v[52:55], v7 offset:20736\n" \
  "v_mfma_f32_16x16x32_bf16 v[32:35], v[152:155], v[36:39], v[32:35]\n" \
  "s_waitcnt lgkmcnt(3)\n" \
  "v_mfma_f32_16x16x32_bf16 v[28:31], v[92:95], v[40:43], v[28:31]\n" \
  "ds_read_b128 v[36:39], v7 offset:22784\n" \
  "v_mfma_f32_16x16x32_bf16 v[32:35], v[156:159], v[40:43], v[32:35]\n" \
  "s_waitcnt lgkmcnt(3)\n" \
  "v_mfma_f32_16x16x32_bf16 v[28:31], v[96:99], v[44:47], v[28:31]\n" \
  "ds_read_b128 v[40:43], v7 offset:24832\n" \
  "v_mfma_f32_16x16x32_bf16 v[32:35], v[160:163], v[44:47], v[32:35]\n" \
  "s_waitcnt lgkmcnt(3)\n" \
  "v_mfma_f32_16x16x32_bf16 v[28:31], v[100:103], v[48:51], v[28:31]\n" \
  "ds_read_b128 v[44:47], v7 offset:26880\n" \
  "v_mfma_f32_16x16x32_bf16 v[32:35], v[164:167], v[48:51], v[32:35]\n" \
  "s_waitcnt lgkmcnt(3)\n" \
  "v_mfma_f32_16x16x32_bf16 v[28:31], v[104:107], v[52:55], v[28:31]\n" \
  "ds_read_b128 v[48:51], v7 offset:28928\n" \
  "v_mfma_f32_16x16x32_bf16 v[32:35], v[168:171], v[52:55], v[32:35]\n" \
  "s_waitcnt lgkmcnt(3)\n" \
  "v_mfma_f32_16x16x32_bf16 v[28:31], v[108:111], v[36:39], v[28:31]\n" \
  "ds_read_b128 v[52:55], v7 offset:30976\n" \
  "v_mfma_f32_16x16x32_bf16 v[32:35], v[172:175], v[36:39], v[32:35]\n" \
  "s_waitcnt lgkmcnt(3)\n" \
  "v_mfma_f32_16x16x32_bf16 v[28:31], v[112:115], v[40:43], v[28:31]\n" \
  "v_mfma_f32_16x16x32_bf16 v[32:35], v[176:179], v[40:43], v[32:35]\n" \
  "v_xor_b32 v7, 0x8000, v7\n" \
  "s_waitcnt lgkmcnt(0)\n" \
  "s_barrier\n" \
  "ds_read_b128 v[36:39], v10 offset:0\n" \
  "ds_read_b128 v[40:43], v7 offset:0\n" \
  "v_mfma_f32_16x16x32_bf16 v[28:31], v[116:119], v[44:47], v[28:31]\n" \
  "v_mfma_f32_16x16x32_bf16 v[32:35], v[180:183], v[44:47], v[32:35]\n" \
  "ds_read_b128 v[44:47], v10 offset:2048\n" \
  "v_mfma_f32_16x16x32_bf16 v[28:31], v[120:123], v[48:51], v[28:31]\n" \
  "v_mfma_f32_16x16x32_bf16 v[32:35], v[184:187], v[48:51], v[32:35]\n" \
  "ds_read_b128 v[48:51], v7 offset:2048\n" \
  "v_mfma_f32_16x16x32_bf16 v[28:31], v[124:127], v[52:55], v[28:31]\n" \
  "v_mfma_f32_16x16x32_bf16 v[32:35], v[188:191], v[52:55], v[32:35]\n" \
  "s_cbranch_scc1 .LQB_LAST_%=\n" \
  "s_branch .LQB_TOP_%=\n" \
  ".LQB_LAST_%=:\n" \
  "s_nop 7\n" \
  "v_mul_f32 v20, v20, v60\n" \
  "v_mul_f32 v21, v21, v60\n" \
  "v_mul_f32 v22, v22, v60\n" \
  "v_cvt_pk_bf16_f32 v20, v20, v21\n" \
  "v_mul_f32 v23, v23, v60\n" \
  "v_mul_f32 v24, v24, v60\n" \
  "v_cvt_pk_bf16_f32 v21, v22, v23\n" \
  "v_mul_f32 v25, v25, v60\n" \
  "v_mul_f32 v26, v26, v60\n" \
  "v_cvt_pk_bf16_f32 v22, v24, v25\n" \
  "v_mul_f32 v27, v27, v60\n" \
  "v_cvt_pk_bf16_f32 v23, v26, v27\n" \
  "buffer_store_dwordx4 v[20:23], v6, s[40:43], s59 offen offset:0 sc1\n" \
  "s_add_u32 s76, s59, 4096\n" \
  "v_mul_f32 v28, v28, v61\n" \
  "v_mul_f32 v29, v29, v61\n" \
  "v_mul_f32 v30, v30, v61\n" \
  "v_cvt_pk_bf16_f32 v28, v28, v29\n" \
  "v_mul_f32 v31, v31, v61\n" \
  "v_mul_f32 v32, v32, v61\n" \
  "v_cvt_pk_bf16_f32 v29, v30, v31\n" \
  "v_mul_f32 v33, v33, v61\n" \
  "v_mul_f32 v34, v34, v61\n" \
  "v_cvt_pk_bf16_f32 v30, v32, v33\n" \
  "v_mul_f32 v35, v35, v61\n" \
  "v_cvt_pk_bf16_f32 v31, v34, v35\n" \
  "buffer_store_dwordx4 v[28:31], v6, s[40:43], s76 offen offset:0 sc1\n" \
  "s_add_u32 s59, s59, 0x80000\n" \
  "s_waitcnt vmcnt(0)\n" \
  "v_mov_b32 v12, s80\n" \
  "v_mov_b32 v13, 1\n" \
  "s_mov_b64 exec, 1\n" \
  "ds_add_u32 v12, v13\n" \
  "s_mov_b64 exec, -1\n" \
  "s_waitcnt lgkmcnt(0)\n" \
  "s_endpgm\n"
__device__ __forceinline__ v4u mk_srd(const void* p, const unsigned bytes) {
  const unsigned long long a = (unsigned long long)p;
  return v4u{(unsigned)a, (unsigned)(a >> 32) & 0xffffu, bytes, 0x00020000u};
}
template <bool ATT>
__device__ __forceinline__ void role_chain_t(const MegaArgs& A, char* smem, const int L, const int h, const int rep) {
  __builtin_amdgcn_s_waitcnt(0x0F70);
  u16* swo = reinterpret_cast<u16*>(smem);
  const int w = __builtin_amdgcn_readfirstlane(threadIdx.x >> 6);
  const int wc = (w + ROT) & 7, psw = PSWH;
  char* ws = A.ws;
  unsigned* FL = (unsigned*)(ws + OFF_FL);
  unsigned* TMO = FL + FL_TMO;
#define LANE_IDS()                         \
  const int l = lane_id();                 \
  const int c = l & 15, g = l >> 4;        \
  (void)c; (void)g;
  float* sgm = reinterpret_cast<float*>(smem + C_X);
  float* sgf = sgm + 8;
  unsigned* FQ = FL + FL_FQ + L * 65 * 32 * 32;
  v8u w0[8], w1[8], w2[8];
  {
    LANE_IDS();
    v4u m0r;
    const unsigned sb = lds_base(smem);
    const int q = (l >> 2) & 3, p = l & 3;
    const int f = (q & 1) | ((g & 1) << 1) | ((q >> 1) << 2);
    const unsigned ra = sb + (16 * (g >> 1) + 4 * (g & 1) + (q & 1) + 8 * (q >> 1)) * 256 + (w ^ f) * 32 + p * 8;
#define W_READ(W, hf, X)                                                       \
  {                                                                           \
    _Pragma("unroll") for (int t = 0; t < 4; ++t) {                           \
      const unsigned a_ = (ra ^ (X)) + (hf) * 8192 + t * 32768;               \
      const v2u r0 = trd(a_), r1 = trd(a_ + 512), r2 = trd(a_ + 16384), r3 = trd(a_ + 16384 + 512); \
      W[4 * (hf) + t] = v8u{r0.x, r0.y, r1.x, r1.y, r2.x, r2.y, r3.x, r3.y};  \
    }                                                                         \
    _Pragma("unroll") for (int t = 0; t < 4; ++t) asm volatile("" : "+v"(W[4 * (hf) + t])); \
  }
#define W_STAGE(M, hf, VM, NEXT)                                               \
  {                                                                           \
    WAIT_VM(VM);                                                              \
    RAW_BAR();                                                                \
    if ((M) == 0 && !ATT) { W_READ(w0, hf, 0); W_READ(w2, hf, 128); }         \
    if ((M) == 1 && !ATT) { W_READ(w1, hf, 0); }                              \
    if ((M) == 1 && ATT) { W_READ(w0, hf, 0); }                               \
    if ((M) == 2 && ATT) { W_READ(w1, hf, 0); W_READ(w2, hf, 128); }          \
    RAW_BAR();                                                                \
    NEXT;                                                                     \
  }
#define WO_ISSUE(hf)                                                          \
  {                                                                           \
    const char* base_ = (const char*)A.Wo + (size_t)(h * DH + 64 * (hf) + 8 * w) * (DM * 2); \
    _Pragma("unroll") for (int i = 0; i < 8; ++i) {                           \
      const int fd = (i & 1) | (((i >> 2) & 1) << 1) | ((w & 1) << 2);        \
      glds16(base_, (unsigned)(i * 1024 + ((l ^ (fd << 1)) << 4)), sb + w * 16384 + (hf) * 8192 + i * 1024); \
    }                                                                         \
  }
    wqkv_issue<true>(A.Wv, 0, h, w, l, sb);
    wqkv_issue<true>(A.Wv, 1, h, w, l, sb);
    asm volatile("s_nop 4\n\tglobal_load_dwordx4 %0, %1, %2" : "=v"(m0r) : "v"(((2 * (c >> 2) + (c & 1)) * DM + 64 * w + 16 * g + 8 * (((c >> 1) ^ (c >> 3)) & 1)) * 2), "s"(A.mem0) : "memory");
    W_STAGE(2, 0, 9, wqkv_issue<true>(A.Wk, 0, h, w, l, sb));
    SST(4);
    W_STAGE(2, 1, 8, wqkv_issue<true>(A.Wk, 1, h, w, l, sb));
    SST(5);
    asm volatile("" : "+v"(m0r));
    *reinterpret_cast<v4u*>(smem + C_SM + w * 1024 + l * 16) = m0r;
    {
      float f2[4] = {};
      if (ATT) {
#pragma unroll
        for (int t = 0; t < 8; ++t)
#pragma unroll
          for (int e = 0; e < 8; ++e) {
            f2[e & 1] = dot2sq(w1[t][e], f2[e & 1]);
            f2[2 + (e & 1)] = dot2sq(w2[t][e], f2[2 + (e & 1)]);
          }
      }
      const float f = wave_sum_dpp((f2[0] + f2[1]) + (f2[2] + f2[3]));
      if (l == 0) { sgf[w] = f; sgm[16] = 0.f; sgm[32 + w] = 0.f; }
    }
    v4u zt;
    v2u ze;
#define Z_LOAD()                                                                                                        \
  asm volatile("global_load_dwordx4 %0, %2, %3 sc1\n\tglobal_load_dwordx2 %1, %4, %5 sc1"                              \
               : "=&v"(zt), "=&v"(ze)                                                                                   \
               : "v"(l * 16), "s"(ws + OFF_TAG + (blockIdx.x & 7) * 1024), "v"(0), "s"(ws + OFF_TAG + 8192)              \
               : "memory")
    W_STAGE(1, 0, 8, Z_LOAD(); wqkv_issue<true>(A.Wq, 0, h, w, l, sb));
#undef Z_LOAD
    W_STAGE(1, 1, 8, wqkv_issue<true>(A.Wq, 1, h, w, l, sb));
    asm volatile("" : "+v"(zt), "+v"(ze));
    if (w == 0) {
      if (!zero_seen(zt, ze)) zero_wait(ws);
      const float f2 = (sgf[0] + sgf[1]) + (sgf[2] + sgf[3]);
      __builtin_amdgcn_raw_buffer_store_b32(__float_as_uint(f2) | 1u, MKRS(FL + FL_G, L ? 0 : 4 * 512), l * 512 + h * 4, 0, AUX_SC1);
    }
    SST(6);
    W_STAGE(0, 0, 8, WO_ISSUE(0));
    W_STAGE(0, 1, 8, WO_ISSUE(1));
    SST(7);
#undef W_STAGE
#undef W_READ
#undef WO_ISSUE
    const unsigned rb = sb + (2 * (g >> 1) + (q >> 1)) * 16384 + (4 * (g & 1) + (q & 1)) * 1024 + p * 8;
#pragma unroll
    for (int hf = 0; hf < 2; ++hf) {
      if (hf == 0) WAIT_VM(8); else WAIT_VM(0);
      RAW_BAR();
      v4u fr[8];
#pragma unroll
      for (int i = 0; i < 8; ++i) {
        const unsigned a_ = rb + hf * 8192 + (i & 1) * 65536 + ((4 * wc + ((i >> 1) ^ psw)) ^ f) * 32;
        const v2u r0 = trd(a_), r1 = trd(a_ + 2048);
        fr[i] = v4u{r0.x, r0.y, r1.x, r1.y};
      }
#pragma unroll
      for (int i = 0; i < 8; ++i) asm volatile("" : "+v"(fr[i]));
      RAW_BAR();
#pragma unroll
      for (int i = 0; i < 8; ++i) *reinterpret_cast<v4u*>(swo + w * 8192 + hf * 4096 + i * 512 + l * 8) = fr[i];
    }
  }
  WSTAMP2();
  const unsigned sb = lds_base(smem);
  const char* gK = ws + OFF_QK + L * SZ_QK + ((size_t)h * 16 + 2 * w) * 1024;
  const char* gV = ws + OFF_VF + L * SZ_O + ((size_t)h * 8 + 2 * w - 8) * 1024;
  float gw2;
  {
    const gu32* gp = (gu32*)(FL + FL_G + REP * 128) + (lane_id() & 31);
    unsigned g0, g1, g2;
    for (unsigned spins = 0;; ++spins) {
      g0 = __hip_atomic_load(gp, RLX_AGENT);
      g1 = __hip_atomic_load(gp + 32, RLX_AGENT);
      g2 = __hip_atomic_load(gp + 64, RLX_AGENT);
      if (__all(g0 != 0u && g1 != 0u && g2 != 0u)) break;
      if (spin_timeout(spins, TMO)) return;
      __builtin_amdgcn_s_sleep(4);
    }
    const float gm = wave_max_dpp(__uint_as_float(g0) * (__uint_as_float(g1) + __uint_as_float(g2)));
    asm volatile("s_nop 0\n\tv_readfirstlane_b32 %0, %1" : "=s"(gw2) : "v"(gm * 1.07f));
  }
  v8u pv;
  {
    LANE_IDS();
    const unsigned voff = g * 256 + c * 8, wp = sb + w * 16384 + l * 16;
    pv[0] = (((c >> 1) ^ (c >> 3)) & 1) ? 0xEEEE : 0x4444;
    pv[1] = sb + C_SM + l * 16;
    pv[2] = ATT ? sb + ((c & 2) ? C_SK : C_SQ) + ((2 * (c >> 2) + (c & 1)) * QS + 8 * g) * 2 : voff;
    pv[3] = ATT ? (g & 1) * 1024 + c * 16 + (g >> 1) * 8 : wp;
    pv[4] = sb + ((c & 2) ? C_SQ : C_SO) + w * 256 + ((c & 13) + ((g ^ (c >> 2)) & 2)) * 16 + 8 * ((g ^ (c >> 1)) & 1);
    pv[5] = voff;
    pv[6] = wp;
    pv[7] = sb + C_SO + (l >> 5) * 1024 + w * 256 + (l & 31) * 8;
  }
  if (!ATT) {
    if (!wait_n<4, 1>(FQ + h * 32, 1, TMO)) return;
    const unsigned lo = lane_id() * 16, d = sb + C_KT + (2 * w - 8) * 1024;
    glds16c(gK, lo, d);
    glds16c(gK + 1024, lo, d + 1024);
    glds16c(gV, lo, d + 8192);
    glds16c(gV + 1024, lo, d + 9216);
  }
  DRAIN();
  SST(8);
  const v4u rsA = mk_srd(ws + OFF_ACC + L * SZ_ACC, SZ_ACC);
  const v4u rsF = mk_srd(FQ, 64 * 32 * 32 * 4);
  const v4u rsMK = mk_srd(ws + OFF_MEMK + L * SZ_MEM, SZ_MEM);
  const v4u rsMV = mk_srd(ws + OFF_MEMV + L * SZ_MEM, SZ_MEM);
  const int wr = (w + RR) & 7;
  const char* pAB = ws + OFF_ACC + L * SZ_ACC + (wc - wr) * 4096;
  const char* pGK = gK + NH * 16 * 1024;
  const char* pGV = ATT ? (const char*)(size_t)(CDS + (XG(h) >> 1) * CDL) : gV + NH * 8 * 1024;
  unsigned sc[8];
  sc[0] = ATT ? L * 1024 + XG(h) * 256 : (32 + h) * 128;
  sc[1] = ATT ? 1032 - L * 1024 + h * 4096 : (h * 4 + w - 4) * 512;
  sc[2] = (h * DH + 16 * w) * MM * 2;
  sc[3] = NSEG - 1;
  sc[4] = __float_as_uint(gw2);
  sc[5] = w;
  sc[6] = sb;
  const char* dbg = ws;
  const unsigned tg = (now10() / GR) * GR;
  const v4u rsT = mk_srd(FL + FL_TK, 2048);
#define CHAIN_OPS                                                                                                          \
  ::"{v[64:71]}"(w0[0]), "{v[72:79]}"(w0[1]), "{v[80:87]}"(w0[2]), "{v[88:95]}"(w0[3]), "{v[96:103]}"(w0[4]),              \
      "{v[104:111]}"(w0[5]), "{v[112:119]}"(w0[6]), "{v[120:127]}"(w0[7]), "{v[128:135]}"(w1[0]), "{v[136:143]}"(w1[1]),    \
      "{v[144:151]}"(w1[2]), "{v[152:159]}"(w1[3]), "{v[160:167]}"(w1[4]), "{v[168:175]}"(w1[5]), "{v[176:183]}"(w1[6]),    \
      "{v[184:191]}"(w1[7]), "{v[192:199]}"(w2[0]), "{v[200:207]}"(w2[1]), "{v[208:215]}"(w2[2]), "{v[216:223]}"(w2[3]),    \
      "{v[224:231]}"(w2[4]), "{v[232:239]}"(w2[5]), "{v[240:247]}"(w2[6]), "{v[248:255]}"(w2[7]), "{v[0:7]}"(pv),          \
      "{s[40:43]}"(rsA), "{s[44:47]}"(rsF), "{s[48:51]}"(rsMK), "{s[52:55]}"(rsMV), "{s[56:57]}"(pAB), "{s60}"(wr * 4096), "{s61}"(h * 1024 - wr * 4096), \
      "{s[62:63]}"(pGK), "{s[64:65]}"(pGV), "{s68}"(sc[0]), "{s69}"(sc[1]), "{s70}"(sc[2]),    \
      "{s71}"(sc[3]), "{s72}"(sc[4]), "{s73}"(sc[5]), "{s74}"(sc[6]), "{s[36:37]}"(dbg),                   \
      "{s34}"(tg), "{s35}"(psw * 1024), [gp] "n"(GP), [gr] "n"(GR), [gm] "n"(GM), \
      "{s[16:19]}"(rsT), "{s20}"(0), "{s22}"(sb + C_X + 128 + 8 * (w & 3)), "{s24}"(L * 1024), [cd] "n"(CD - CDS), \
      "{s25}"(APA - (APN + 1) * APP + L * (APB - APA + (2 * APN + 1) * APP)), [ka] "n"(APL), [an] "n"(APN), [d0] "n"(16 * CD0), [rq] "n"(RQM), [pp] "n"(APP) \
      : "memory", "vcc", "scc", "s99", "v63"
  if (ATT) asm volatile("s_mov_b64 exec, -1\ns_nop 4\n" CHAIN_ASM_ATT CHAIN_OPS);
  else asm volatile("s_mov_b64 exec, -1\ns_nop 4\n" CHAIN_ASM_VO CHAIN_OPS);
#undef CHAIN_OPS
#undef LANE_IDS
}

__device__ __forceinline__ void role_chain(const MegaArgs& A, char* smem, const int L, const int h, const int rep) {
  if (__builtin_amdgcn_readfirstlane(threadIdx.x >> 6) < 4) role_chain_t<true>(A, smem, L, h, rep);
  else role_chain_t<false>(A, smem, L, h, rep);
}

constexpr int AW_TK = 0, AW_SO = 98304, AW_M = AW_SO + 32768, AW_CNT = AW_M + 8192;
typedef __attribute__((address_space(3))) unsigned lu32;
__device__ __forceinline__ unsigned umin3(unsigned a, unsigned b, unsigned c) {
  return __builtin_elementwise_min(__builtin_elementwise_min(a, b), c);
}
__device__ __forceinline__ bool all_set(const unsigned r) {
  unsigned long long m;
  asm volatile("v_cmp_eq_u32 %0, 0, %1" : "=s"(m) : "v"(r) : "memory");
  return m == 0ull;
}
__device__ __forceinline__ unsigned long long zero_mask(const unsigned r) {
  unsigned long long m;
  asm volatile("v_cmp_eq_u32 %0, 0, %1" : "=s"(m) : "v"(r) : "memory");
  return m;
}
__device__ __forceinline__ int job_seg(const int n) {
  const int m = n - AWD;
  const int r = (m & 1) ? 64 + (m >> 1) : AWD + (m >> 1);
  return __builtin_amdgcn_readfirstlane(n > 127 ? 128 : (n < AWD || n >= 128 - AWD) ? n : r);
}
template <bool AT>
__device__ __forceinline__ void role_aw_t(const MegaArgs& A, char* smem, const int mode, const int a) {
  __builtin_amdgcn_s_waitcnt(0x0F70);
  const int w = __builtin_amdgcn_readfirstlane(threadIdx.x >> 6);
  const int l = threadIdx.x & 63, c = l & 15, g = l >> 4;
  const int qt = w & 1, hh = (w >> 1) & 1, h = 2 * a + hh;
  char* ws = A.ws;
  unsigned* FL = (unsigned*)(ws + OFF_FL);
  unsigned* TMO = FL + FL_TMO;
  const unsigned sb = lds_base(smem);
  constexpr int NP = AT ? 1 : 3;
  const int cp0 = AT ? w : 3 * w - 8;
  v4u wo[2 * NP][8];
  lu32* cnt = (lu32*)(unsigned long)(sb + AW_CNT);
  if (threadIdx.x < 16) cnt[threadIdx.x] = 0u;
  const __amdgpu_buffer_rsrc_t rsW = MKRS(ws + OFF_PW, SZ_PW);
  const unsigned* FQ = FL + FL_FQ + 1024 + h * 32;
  const v4u rsF = mk_srd(FQ, (129 * 32 - h) * 128);
  const char* gQ = ws + OFF_QK + ((size_t)h * 16 + 8 * qt) * 1024;
  const char* gV = ws + OFF_VF + ((size_t)h * 8 + 4 * qt) * 1024;
  const char* gM = ws + (qt ? OFF_MEMV : OFF_MEMK) + h * 2048;
  const int NJ = (130 - mode) / 3;
#define AWS(gs) (64 + a + ((gs) >> 6) * 16)
  const int mo = AW_M + hh * 4096;
  const unsigned tq0 = __builtin_amdgcn_readfirstlane(sb + AW_TK + (hh * 24 + 8 * qt) * 1024);
  const unsigned tv0 = __builtin_amdgcn_readfirstlane(sb + AW_TK + (hh * 24 + 16 + 4 * qt) * 1024);
  const float scale = 0.08838834764831845f;
  volatile lu32* cM = cnt + 2 + hh;
#define DMA4(x) "global_load_lds_dwordx4 %1, " x " sc1\n\tglobal_load_lds_dwordx4 %1, " x " offset:1024 sc1\n\t"       \
                "global_load_lds_dwordx4 %1, " x " offset:2048 sc1\n\tglobal_load_lds_dwordx4 %1, " x " offset:3072 sc1\n\t"
#define T_DMA(jj, gg)                                                                                                   \
  {                                                                                                                     \
    const unsigned o_ = ((jj) & 1) ? 49152u : 0u;                                                                       \
    const char* q_ = gQ + (size_t)(gg) * (NH * 16 * 1024);                                                              \
    unsigned keep_;                                                                                                     \
    asm volatile("s_nop 4\n\ts_mov_b32 %0, m0\n\ts_mov_b32 m0, %5\n\ts_nop 0\n\t" DMA4("%2")                           \
                 "s_mov_b32 m0, %6\n\ts_nop 0\n\t" DMA4("%3") "s_mov_b32 m0, %7\n\ts_nop 0\n\t" DMA4("%4") "s_mov_b32 m0, %0" \
                 : "=&s"(keep_)                                                                                         \
                 : "v"(l * 16), "s"(q_), "s"(q_ + 4096), "s"(gV + (size_t)(gg) * (NH * 8 * 1024)), "s"(tq0 + o_),        \
                   "s"(tq0 + o_ + 4096u), "s"(tv0 + o_)                                                                 \
                 : "memory");                                                                                           \
  }
#define T_LAND(jj) { if (l == 0) *(volatile lu32*)(cnt + w) = (jj) + 1; }
#define M_DMA(gg)                                                                                                       \
  {                                                                                                                     \
    unsigned keep_;                                                                                                     \
    asm volatile("s_nop 4\n\ts_mov_b32 %0, m0\n\ts_mov_b32 m0, %3\n\ts_nop 0\n\tglobal_load_lds_dwordx4 %1, %2 sc1\n\t"     \
                 "global_load_lds_dwordx4 %1, %2 offset:1024 sc1\n\ts_mov_b32 m0, %0"                                    \
                 : "=&s"(keep_)                                                                                         \
                 : "v"(l * 16), "s"(gM + (size_t)(gg) * (NH * 2048)), "s"(sb + mo + qt * 2048)                           \
                 : "memory");                                                                                           \
  }
#define M_TEST()                                                                                                        \
  {                                                                                                                     \
    const v4u t_ = *reinterpret_cast<const v4u*>(smem + mo + qt * 2048 + l * 16);                                        \
    const v4u u_ = *reinterpret_cast<const v4u*>(smem + mo + qt * 2048 + 1024 + l * 16);                                 \
    ok = __all(umin3(umin3(t_.x, t_.y, t_.z), umin3(u_.x, u_.y, u_.z), umin3(t_.w, u_.w, 1u)) != 0u);                   \
    if (ok) asm volatile("s_mov_b64 exec, 1\n\tds_add_u32 %0, %1\n\ts_mov_b64 exec, -1" ::"v"(sb + AW_CNT + 8 + 4 * hh), "v"(1u) : "memory"); \
  }
#define F_LOAD(r, gg) \
  asm volatile("s_nop 4\n\tbuffer_load_dword %0, %1, %2, %3 offen sc1" : "=v"(r) : "v"(0), "s"(rsF), "s"(((gg) + ((gg) >> 6)) * 4096) : "memory")
#define F_TEST(r) all_set(r)
#define W_FLAG(jj, gg)                                                                                                  \
  {                                                                                                                     \
    unsigned old_;                                                                                                      \
    asm volatile("s_mov_b64 exec, 1\n\tds_add_rtn_u32 %0, %1, %2\n\ts_mov_b64 exec, -1\n\ts_waitcnt lgkmcnt(0)"           \
                 : "=&v"(old_) : "v"(sb + AW_CNT + ((jj) & 1) * 36), "v"(1u) : "memory");                               \
    if ((__builtin_amdgcn_readfirstlane(old_) & 7u) == 7u) {                                                            \
      TSR(AWS(gg), 4, (gg) & 63, threadIdx.x & ~63u);                                                                   \
      if (l < 4) set_flag(FL + FL_FW + (((gg) * 4 + l) * 4 + (a >> 2)) * 32 + (a & 3));                              \
    }                                                                                                                   \
    pend = false;                                                                                                       \
  }
#define LDS_U(p) ((unsigned)__builtin_amdgcn_readfirstlane(*(p)))
#define TOK_MIN() __builtin_elementwise_min(LDS_U((volatile lu32*)(cnt + 4 + 2 * hh)), LDS_U((volatile lu32*)(cnt + 5 + 2 * hh)))
  int gp = 0, gs = job_seg(mode), g1 = job_seg(mode + 3);
  {
    const int q = (l >> 2) & 3, p = l & 3, f = q | ((g & 1) << 2);
    const unsigned ra = sb + (4 * g + q) * 256 + (p & 1) * 16;
    unsigned n0 = 0u;
    bool got = false;
#pragma unroll
    for (int st = 0; st < 4; ++st) {
      if (st == 3 && got) WAIT_VM(12); else if (st == 3) WAIT_VM(0); else WAIT_VM(8);
      RAW_BAR();
#pragma unroll
      for (int k2 = 0; k2 < 2; ++k2) {
        const int ks = 4 * (st >> 1) + 2 * k2 + (st & 1);
#pragma unroll
        for (int ct = 0; ct < 2 * NP; ++ct) {
          const int cp = cp0 + (ct >> 1);
          const unsigned a_ = ra + (st & 1) * 65536 + (cp >> 2) * 16384 + k2 * 8192 + ((2 * (cp & 3) + (p >> 1)) ^ f) * 32 + (ct & 1) * 8;
          const v2u lo = trd(a_), hi = trd(a_ + 4096);
          wo[ct][ks] = v4u{lo.x, lo.y, hi.x, hi.y};
        }
#pragma unroll
        for (int ct = 0; ct < 2 * NP; ++ct) asm volatile("" : "+v"(wo[ct][ks]));
      }
      RAW_BAR();
      if (!AT && st == 1) F_LOAD(n0, gs);
      if (st < 2) wo_issue(A.Wo, a, st + 2, sb);
      if (!AT && st == 2 && F_TEST(n0)) {
        T_DMA(0, gs);
        got = true;
      }
    }
    if (!AT) {
      if (!got) {
        (void)wait_n<SLP_A, 1>(FQ + gs * 1024, 1, TMO);
        T_DMA(0, gs);
      }
      WAIT_VM(0);
      T_LAND(0);
    }
  }
  __builtin_amdgcn_s_waitcnt(0x0F70);
  bool pend = false, asked = false;
  unsigned tb1 = now10() - 130 * GPER, tb2 = tb1;
  FSD;
  for (int s = 0; s < NJ; ++s) {
    char* so = smem + AW_SO + (s & 1) * 16384 + l * 16;
    bool got = g1 == 128, land = got, seen = false;
    if (AT) {
      if (TOK_MIN() < (unsigned)(s + 1)) {
        if (pend) {
          WAIT_VM(0);
          W_FLAG(s - 1, gp);
        }
        for (unsigned spins = 0; TOK_MIN() < (unsigned)(s + 1) && spins < 64u * SPIN_CAP; ++spins) __builtin_amdgcn_s_sleep(1);
      }
      asm volatile("" ::: "memory");
      TSR(AWS(gs), 0, gs & 63, 0);
      FS(0);
      const char* tk = smem + AW_TK + ((s & 1) * 2 + hh) * 24 * 1024 + l * 16;
#define TKF(i) (*reinterpret_cast<const bf16x8*>(tk + (i) * 1024))
      bf16x8 q[4], vt[8];
      float x[12], mx;
      bool ok = false;
      {
        f32x4 s0 = {}, s1 = {};
        bf16x8 kd[4], k0[4];
#pragma unroll
        for (int ks = 0; ks < 4; ++ks) {
          q[ks] = TKF(4 * qt + ks);
          kd[ks] = TKF(8 + 4 * qt + ks);
          k0[ks] = TKF(8 + ks);
        }
#pragma unroll
        for (int i = 0; i < 8; ++i) vt[i] = TKF(16 + i);
        __builtin_amdgcn_sched_barrier(0);
#pragma unroll
        for (int ks = 0; ks < 4; ++ks) {
          s0 = MFMA16(kd[ks], q[ks], s0);
          s1 = MFMA16(k0[ks], q[ks], s1);
        }
        scale_pair(s0[0], s0[1], scale, x[0], x[1]);
        scale_pair(s0[2], s0[3], scale, x[2], x[3]);
        scale_pair(s1[0], s1[1], scale, x[4], x[5]);
        scale_pair(s1[2], s1[3], scale, x[6], x[7]);
#pragma unroll
        for (int i = 0; i < 4; ++i) {
          if (4 * g + i > c) x[i] = -3e38f;
          if (!qt) x[4 + i] = -3e38f;
        }
        mx = fmaxf(fmaxf(fmaxf(x[0], x[1]), fmaxf(x[2], x[3])), fmaxf(fmaxf(x[4], x[5]), fmaxf(x[6], x[7])));
        asm volatile("" : "+v"(mx), "+v"(vt[0]), "+v"(vt[1]), "+v"(vt[2]), "+v"(vt[3]), "+v"(vt[4]), "+v"(vt[5]), "+v"(vt[6]), "+v"(vt[7]));
      }
#undef TKF
      FS(1);
      if (asked) {
        WAIT_VM(1);
        M_TEST();
      }
      FS(2);
      if (!ok) {
        if (pend) {
          WAIT_VM(0);
          W_FLAG(s - 1, gp);
        }
        unsigned tb = gs < 64 ? tb1 : tb2, spins = 0;
        gate(tb + gs * GPER);
        for (; !ok; ++spins) {
          if (spin_timeout(spins, TMO)) break;
          if (spins) __builtin_amdgcn_s_sleep(SLP_M);
          M_DMA(gs);
          WAIT_VM(0);
          M_TEST();
        }
        tb = spins > 1 ? now10() - gs * GPER - GMA : tb - GSTEP;
        if (gs < 64) tb1 = tb; else tb2 = tb;
      }
      FS(3);
      __builtin_amdgcn_s_setprio(PRI_A);
      asked = false;
      for (unsigned spins = 0; LDS_U(cM) < 2u * (s + 1) && spins < 64u * SPIN_CAP; ++spins) __builtin_amdgcn_s_sleep(1);
      v4u km[4];
      v2u vm[8];
#pragma unroll
      for (int ks = 0; ks < 4; ++ks) km[ks] = *reinterpret_cast<const v4u*>(smem + mo + (ks * 32 + g * 8 + (c & 7)) * 16);
#pragma unroll
      for (int i = 0; i < 8; ++i) vm[i] = *reinterpret_cast<const v2u*>(smem + mo + 2048 + ((16 * i + c) * MM + 4 * (g & 1)) * 2);
      TSR(AWS(gs), 1, gs & 63, 0);
      FS(4);
      f32x4 m0 = {};
#pragma unroll
      for (int ks = 0; ks < 4; ++ks) m0 = MFMA16(BF8(km[ks]), q[ks], m0);
      scale_pair(m0[0], m0[1], scale, x[8], x[9]);
      scale_pair(m0[2], m0[3], scale, x[10], x[11]);
#pragma unroll
      for (int i = 0; i < 4; ++i) {
        if (g >= 2) x[8 + i] = -3e38f;
        mx = fmaxf(mx, x[8 + i]);
      }
      mx = xmax32(xmax16(mx));
      float sum = 0.f;
#pragma unroll
      for (int i = 0; i < 12; ++i) {
        x[i] = __expf(x[i] - mx);
        sum += x[i];
      }
      sum = xsum32(xsum16(sum));
      const float inv = 1.0f / sum;
      const v2u pa = {pk2(x[0] * inv, x[1] * inv), pk2(x[2] * inv, x[3] * inv)};
      const v2u pb = {pk2(x[4] * inv, x[5] * inv), pk2(x[6] * inv, x[7] * inv)};
      const v4u p0 = qt ? v4u{pb.x, pb.y, pa.x, pa.y} : v4u{pa.x, pa.y, pb.x, pb.y};
      const v2u p1 = {pk2(x[8] * inv, x[9] * inv), pk2(x[10] * inv, x[11] * inv)};
      FS(5);
#pragma unroll
      for (int k = 0; k < 4; ++k) {
        v4u of;
#pragma unroll
        for (int hf = 0; hf < 2; ++hf) {
          f32x4 ao = {};
          ao = MFMA16(vt[2 * k + hf], BF8(p0), ao);
          ao = MFMA16K(vm[2 * k + hf], p1, ao);
          of[2 * hf] = pk2(ao[0], ao[1]);
          of[2 * hf + 1] = pk2(ao[2], ao[3]);
        }
        *reinterpret_cast<v4u*>(so + ((hh * 2 + qt) * 4 + k) * 1024) = of;
      }
      FS(6);
      seen = g1 >= 64 && g1 < 128 && TOK_MIN() >= (unsigned)(s + 2);
      if (pend) {
        WAIT_VM(0);
        W_FLAG(s - 1, gp);
      }
    } else {
      FS(0);
      unsigned n;
      F_LOAD(n, g1);
      if (pend) {
        WAIT_VM(1);
        W_FLAG(s - 1, gp);
      }
      WAIT_VM(0);
      FS(1);
      if (F_TEST(n)) {
        T_DMA(s + 1, g1);
        got = true;
        if (LDS_U(cM) < 2u * (s + 1)) {
          WAIT_VM(0);
          T_LAND(s + 1);
          land = true;
        }
      }
    }
    FS(7);
    RAW_BAR();
    FS(8);
    __builtin_amdgcn_s_setprio(AT ? PRI_WA : PRI_WB);
    TSR(AWS(gs), 2, gs & 63, 256);
    const int voff = (c >> 1) * 2048 + (c & 1) * 64 + g * 16, soff = (gs * 16 + a) * 32768 + cp0 * 128;
#define OFRAG(i) (*reinterpret_cast<const bf16x8*>(so + (((((i) & 7) >> 2) * 2 + ((i) >> 3)) * 4 + ((i) & 3)) * 1024))
    constexpr int NO = AT ? 16 : 3;
    bf16x8 o[NO];
#pragma unroll
    for (int i = 0; i < (AT ? 16 : 2); ++i) o[i] = OFRAG(i);
    unsigned pr;
    const bool look = seen && g1 >= 128 - AWD;
    if (AT) {
      asm volatile("s_cmp_eq_u32 %3, 0\n\ts_cbranch_scc1 1f\n\ts_nop 4\n\tglobal_load_dword %0, %1, %2 sc1\n1:"
                   : "=v"(pr) : "v"(2044), "s"(gM + (size_t)g1 * (NH * 2048)), "s"(__builtin_amdgcn_readfirstlane((int)look)) : "memory", "scc");
    } else {
      F_LOAD(pr, got ? 128 : g1);
    }
    __builtin_amdgcn_sched_barrier(0);
    FS(9);
#pragma unroll
    for (int rt = 0; rt < 2; ++rt) {
      f32x4 acc[2 * NP] = {};
#pragma unroll
      for (int kk = 0; kk < 8; ++kk) {
        const int i = 8 * rt + kk;
        if (!AT && i + 2 < 16) o[(i + 2) % NO] = OFRAG(i + 2);
#pragma unroll
        for (int ct = 0; ct < 2 * NP; ++ct) acc[ct] = MFMA16(BF8(wo[ct][kk]), o[i % NO], acc[ct]);
        if (!AT) __builtin_amdgcn_sched_barrier(0);
      }
      if (rt == 0) FS(10); else FS(12);
      if (AT && rt == 1 && seen) {
        if (look) {
          WAIT_VM(1);
          seen = F_TEST(pr);
        }
        if (seen) {
          M_DMA(g1);
          asked = true;
        }
      }
      if (!AT && rt == 0) {
        WAIT_VM(0);
        if (got && !land) {
          T_LAND(s + 1);
          land = true;
        }
        if (F_TEST(pr)) {
          T_DMA(s + 1, g1);
          got = true;
        }
      }
      if (!AT && rt == 1 && got && !land) {
        WAIT_VM(3);
        T_LAND(s + 1);
      }
#pragma unroll
      for (int u = 0; u < NP; ++u)
        ST128((v4u{pk2(acc[2 * u][0], acc[2 * u][1]), pk2(acc[2 * u][2], acc[2 * u][3]), pk2(acc[2 * u + 1][0], acc[2 * u + 1][1]),
                   pk2(acc[2 * u + 1][2], acc[2 * u + 1][3])}),
              rsW, voff + u * 128, soff + rt * 16384);
      if (rt == 0) FS(11); else FS(13);
    }
    __builtin_amdgcn_s_setprio(AT ? PRI_0 : 0);
    pend = true;
    FD(s);
    TSR(AWS(gs), 3, gs & 63, 256);
    TSR(AWS(gs), 5, gs & 63, 0);
    if (!AT && !got) {
      WAIT_VM(0);
      W_FLAG(s, gs);
      (void)wait_n<SLP_A, 1>(FQ + (g1 + (g1 >> 6)) * 1024, 1, TMO);
      T_DMA(s + 1, g1);
      WAIT_VM(0);
      T_LAND(s + 1);
    }
    gp = gs;
    gs = g1;
    g1 = job_seg(3 * s + 6 + mode);
  }
  WAIT_VM(0);
  W_FLAG(NJ - 1, gp);
#undef AWS
#undef OFRAG
#undef DMA4
#undef T_DMA
#undef T_LAND
#undef M_DMA
#undef M_TEST
#undef F_LOAD
#undef F_TEST
#undef W_FLAG
#undef LDS_U
#undef TOK_MIN
}
__device__ __forceinline__ void role_aw(const MegaArgs& A, char* smem, const int L, const int a) {
  if (__builtin_amdgcn_readfirstlane(threadIdx.x >> 6) < 4) role_aw_t<true>(A, smem, L, a);
  else role_aw_t<false>(A, smem, L, a);
}

__device__ __forceinline__ void role_red(const MegaArgs& A, char* smem, const int idx) {
  const int w = __builtin_amdgcn_readfirstlane(threadIdx.x >> 6);
  const int l = threadIdx.x & 63;
  const int gr = w >> 1, r = idx * 2 + (w & 1);
  char* ws = A.ws;
  unsigned* FL = (unsigned*)(ws + OFF_FL);
  unsigned* TMO = FL + FL_TMO;
  const __amdgpu_buffer_rsrc_t rsW = MKRS(ws + OFF_PW, SZ_PW);
  const __amdgpu_buffer_rsrc_t rsX = MKRS(ws + OFF_XA, OFF_X3 - OFF_XA + SZ_X);
  volatile unsigned* sw = reinterpret_cast<volatile unsigned*>(smem) + (w >> 1) * 32;
  if (threadIdx.x < 128) reinterpret_cast<volatile unsigned*>(smem)[threadIdx.x] = 0u;
  __syncthreads();
  unsigned tb1 = now10() - 130 * GPER, tb2 = tb1;
  const int rn1 = idx < 8 ? 96 + idx : 224 + idx;
  (void)rn1;
  if (w >= 6) return;
  if (w < 4) {
    const __amdgpu_buffer_rsrc_t rz = MKRS(ws + OFF_X2, SZ_X);
    for (int s = gr; s < NSEG; s += 2) ST128((v4u{0u, 0u, 0u, 0u}), rz, l * 16, (s * SEG + r) * DM * 2);
    DRAIN();
    if (l < 4) set_flag(FL + FL_FZ + (2 * l + (idx >> 3)) * 32 + (idx & 7) * 4 + w);
    if (l == 0) __hip_atomic_fetch_add((lu32*)(unsigned long)(lds_base(smem) + 480), 1u, __ATOMIC_RELAXED, __HIP_MEMORY_SCOPE_WORKGROUP);
  }
  for (unsigned spins = 0; reinterpret_cast<volatile unsigned*>(smem)[120] < 4u && spins < 64u * SPIN_CAP; ++spins) __builtin_amdgcn_s_sleep(2);
  for (int n = gr; n < 128; n += 3) {
    const int gs = job_seg(n), L = gs >> 6, s = gs & 63;
    const unsigned* fw = FL + FL_FW + (gs * 4 + REP) * 4 * 32;
    unsigned tb = L ? tb2 : tb1;
    tb = grp_wait<SLP_R, 4>((w & 1) == 0, sw, n + 1, fw, 16, TMO, tb + gs * GPER) ? now10() - gs * GPER - GMR : tb - GSTEP;
    if (n >= 125 - AWD) tb = now10() + GATE_RT - (n + 3) * GPER;
    if (L || n >= 125 - AWD) tb2 = tb; else tb1 = tb;
#define RTS(k, t) TSR(L ? A.rn : rn1, k, s, (w & 6) * 64 + (t))
    RTS(0, 0);
    RTS(4, 64);
    v4u p[16];
    const v4u xr = LD128(rsX, l * 512, L * (int)(OFF_X3 - OFF_XA) + s * 32768 + r * 16);
#pragma unroll
    for (int k = 0; k < 16; ++k)
      p[k] = LD128(rsW, (l >> 2) * 128 + (l & 3) * 16, (gs * 16 + k) * 32768 + (r >> 1) * 2048 + (r & 1) * 64);
    v4u xo;
    const unsigned nz = L ? 0u : 0x8000u;
#pragma unroll
    for (int e = 0; e < 4; ++e) {
      f32x2_t t[4];
#pragma unroll
      for (int k = 0; k < 4; ++k)
        t[k] = (f32x2_t{lo16(p[4 * k][e]), hi16(p[4 * k][e])} + f32x2_t{lo16(p[4 * k + 1][e]), hi16(p[4 * k + 1][e])}) +
               (f32x2_t{lo16(p[4 * k + 2][e]), hi16(p[4 * k + 2][e])} + f32x2_t{lo16(p[4 * k + 3][e]), hi16(p[4 * k + 3][e])});
      const f32x2_t u = (t[0] + t[1]) + (t[2] + t[3]);
      const unsigned a0 = pk2(u.x, u.y);
      xo[e] = pk2(lo16(xr[e]) + lo16(a0), hi16(xr[e]) + hi16(a0));
      xo[e] = xo[e] ? xo[e] : nz;
    }
    ST128(xo, MKRS(L ? (char*)A.out : ws + OFF_X2, SZ_X), l * 16, (s * SEG + r) * DM * 2);
    RTS(1, 0);
    RTS(5, 64);
    if (L == 0) {
      const unsigned st = row_stats(xo);
      if (l < 4) __hip_atomic_store((gu32*)(FL + FL_FR + ((s * 4 + l) * 4 + (r >> 3)) * 32 + (r & 7)), st, RLX_AGENT);
    }
    RTS(6, 64);
    RTS(3, 0);
#undef RTS
  }
  if (idx == 0 && threadIdx.x == 128) {
    gu64* en = (gu64*)(ws + OFF_TAG + 8192);
    __hip_atomic_store(en, __hip_atomic_load(en, RLX_AGENT) + TAG_STEP, RLX_AGENT);
  }
}

constexpr int CUN = 2144;
__device__ __forceinline__ bf16x8 conv_bfrag(const unsigned* up, const unsigned sh) {
  const unsigned d0 = up[0], d1 = up[1], d2 = up[2], d3 = up[3], d4 = up[4];
  const v4u r = {__builtin_amdgcn_alignbit(d1, d0, sh), __builtin_amdgcn_alignbit(d2, d1, sh),
                 __builtin_amdgcn_alignbit(d3, d2, sh), __builtin_amdgcn_alignbit(d4, d3, sh)};
  return BF8(r);
}
__device__ __forceinline__ void role_conv(const MegaArgs& A, char* smem, const int cv) {
  __builtin_amdgcn_s_waitcnt(0x0F70);
  u16* Kr = reinterpret_cast<u16*>(smem);
  u16* Uc = Kr + 8 * 2 * CKN;
  u16* Xr = Uc + 8 * CUN;
  u16* To = Uc + 8 * 2 * CUN;
  u16* Su = To + 32 * 8;
  u16* Sx = Su + 32 * 8;
  const int tid = threadIdx.x;
  const int w = __builtin_amdgcn_readfirstlane(tid >> 6);
  const int l = tid & 63, c = l & 15, g = l >> 4;
  const int d = 8 * cv + w;
  char* ws = A.ws;
  unsigned* FL = (unsigned*)(ws + OFF_FL);
  unsigned* TMO = FL + FL_TMO;
  u16* kr = Kr + w * 2 * CKN;
  float* prm = reinterpret_cast<float*>(kr);
  v4u tx[4];
  const v2u en = LD64(MKRS(ws + OFF_TAG, 8192 + 8), 0, 8192);
  {
    const unsigned pa = *reinterpret_cast<const unsigned*>(A.logAre + d * NST + 2 * l);
    const unsigned pb = *reinterpret_cast<const unsigned*>(A.Aim + d * NST + 2 * l);
    const unsigned pc = *reinterpret_cast<const unsigned*>(A.Cre + d * NST + 2 * l);
    const unsigned pd = *reinterpret_cast<const unsigned*>(A.Cim + d * NST + 2 * l);
    const float dt = bfr(expf(bf2f(A.logdt[d])));
    if (w < 2) tx[0] = *reinterpret_cast<const v4u*>(A.x0 + (size_t)tid * DM + 8 * cv);
    if (l < 16) reinterpret_cast<unsigned*>(Uc + w * CUN)[l] = 0u;
    if (l < 32) reinterpret_cast<unsigned*>(Uc + w * CUN + 32 + L_SEQ)[l] = 0u;
    if (l < 32) reinterpret_cast<unsigned*>(Uc + w * CUN + 32 + 128)[l] = 0u;
    if (l < 50) {
      reinterpret_cast<v4u*>(kr + 1808)[l] = v4u{0u, 0u, 0u, 0u};
      reinterpret_cast<v4u*>(kr + CKN + 1808)[l] = v4u{0u, 0u, 0u, 0u};
    }
#pragma unroll
    for (int e = 0; e < 2; ++e) {
      const float ar = -bfr(expf(e ? hi16(pa) : lo16(pa))) * dt, ai = (e ? hi16(pb) : lo16(pb)) * dt;
      const float ex = expf(ar);
      float sn, cs;
      sincosf(ai, &sn, &cs);
      prm[2 * l + e] = ar;
      prm[NST + 2 * l + e] = ai;
      prm[2 * NST + 2 * l + e] = (e ? hi16(pc) : lo16(pc)) * dt;
      prm[3 * NST + 2 * l + e] = (e ? hi16(pd) : lo16(pd)) * dt;
      prm[4 * NST + 2 * l + e] = ex * cs;
      prm[5 * NST + 2 * l + e] = ex * sn;
    }
  }
  const float dsk = bf2f(A.Dskip[d]);
  const u16* ka = kr + (c >> 3) * CKN + 2080 - 16 * (c & 7) + 8 * g;
  const unsigned ush = (c & 1) * 16;
#define KFRAG(D0) (*reinterpret_cast<const bf16x8*>(ka - (D0)))
  f32x4 acc[16], far[5];
  v4u xf[2];
  const unsigned* ub1 = reinterpret_cast<const unsigned*>(Uc + w * CUN + (c & ~1) + 8 * g + 32);
  const __amdgpu_buffer_rsrc_t rz1 = MKRS(ws + OFF_XA, SZ_X);
  const int t0 = 16 * (4 * (g & 1) + (g >> 1)) + c;
  {
    const u16* uc = Uc + w * CUN;
    const unsigned* ub = reinterpret_cast<const unsigned*>(uc + (c & ~1) + 8 * g + 32);
    const __amdgpu_buffer_rsrc_t rz = MKRS(ws + OFF_XA, SZ_X);
    const __amdgpu_buffer_rsrc_t rst = MKRS(ws + OFF_GT, L_SEQ * 4);
#define TILE_OUT(T)                                                                            \
  {                                                                                            \
    const float u0 = bf2f(uc[32 + 128 * (T) + t0]), u1 = bf2f(uc[64 + 128 * (T) + t0]);        \
    u16* xp = Xr + (128 * (T) + t0) * 8 + w;                                                   \
    const unsigned yy = pk2(y0, y1), uu = pk2(u0 * dsk, u1 * dsk);                             \
    const unsigned aa = pk2(lo16(yy) + lo16(uu), hi16(yy) + hi16(uu));                         \
    const unsigned oo = pk2(bf2f(xp[0]) + lo16(aa), bf2f(xp[256]) + hi16(aa));                 \
    xp[0] = (u16)oo;                                                                           \
    xp[256] = (u16)(oo >> 16);                                                                 \
  }
#define CHUNK_ST(ck)                                                                           \
  {                                                                                            \
    const v4u o = *reinterpret_cast<const v4u*>(Xr + (64 * (ck) + l) * 8);                     \
    ST128(o, rz, (l >> 5) * 32768 + (l & 31) * 16, (2 * (ck) * 64 + cv) * 512);                \
  }
#define U_ROW(row) U_ROW2(row, __builtin_amdgcn_raw_buffer_load_b32(rst, (row) * 4, 0, AUX_SC1))
#define U_ROW2(row, stv)                                                                       \
  {                                                                                            \
    const unsigned st_ = (stv);                                                                \
    const v4u xr_ = *reinterpret_cast<const v4u*>(Xr + (row) * 8);                             \
    const float mb_ = lo16(st_), ri_ = 1.0f / hi16(st_);                                       \
    _Pragma("unroll") for (int e = 0; e < 4; ++e) {                                            \
      const unsigned t_ = pk2(lo16(xr_[e]) - mb_, hi16(xr_[e]) - mb_);                         \
      const unsigned q_ = pk2(lo16(t_) * ri_, hi16(t_) * ri_);                                 \
      Uc[(2 * e) * CUN + 32 + (row)] = (u16)q_;                                                \
      Uc[(2 * e + 1) * CUN + 32 + (row)] = (u16)(q_ >> 16);                                    \
    }                                                                                          \
  }
    const unsigned* FS = FL + FL_FS + REP * 32 * 32;
    const int gi = 4 * (g * 8 + (l & 7));
    const int tl = 32 * ((l >> 3) & 1) + 8 * g + (l & 7);
    unsigned khl = 0u;
#pragma unroll 1
    for (int ps = 0; ps < 4; ++ps) {
      float v[32];
      {
        const f32x4 ar = *reinterpret_cast<const f32x4*>(prm + gi), ai = *reinterpret_cast<const f32x4*>(prm + NST + gi);
        const f32x4 cr = *reinterpret_cast<const f32x4*>(prm + 2 * NST + gi), ci = *reinterpret_cast<const f32x4*>(prm + 3 * NST + gi);
        const f32x4 wr = *reinterpret_cast<const f32x4*>(prm + 4 * NST + gi), wi = *reinterpret_cast<const f32x4*>(prm + 5 * NST + gi);
        const float p0 = (float)(64 * ps + 32 * ((l >> 3) & 1));
        float yr0[4], yi0[4];
#pragma unroll
        for (int k = 0; k < 4; ++k) {
          const float e = __expf(ar[k] * p0);
          float sn, cs;
          __sincosf(ai[k] * p0, &sn, &cs);
          const float zr = e * cs, zi = e * sn;
          yr0[k] = cr[k] * zr - ci[k] * zi;
          yi0[k] = cr[k] * zi + ci[k] * zr;
        }
        f32x2_t yra = {yr0[0], yr0[1]}, yia = {yi0[0], yi0[1]}, wra = {wr[0], wr[1]}, wia = {wi[0], wi[1]};
        f32x2_t yrb = {yr0[2], yr0[3]}, yib = {yi0[2], yi0[3]}, wrb = {wr[2], wr[3]}, wib = {wi[2], wi[3]};
#pragma unroll
        for (int i = 0; i < 32; ++i) {
          const f32x2_t sm2 = yra + yrb;
          v[i] = sm2.x + sm2.y;
          const f32x2_t ta = yra * wra - yia * wia, tb = yrb * wrb - yib * wib;
          yia = yra * wia + yia * wra;
          yib = yrb * wib + yib * wrb;
          yra = ta;
          yrb = tb;
        }
      }
#pragma unroll
      for (int i = 0; i < 16; ++i) v[i] = swap_add32(v[i], v[i + 16]);
#pragma unroll
      for (int i = 0; i < 8; ++i) {
        v[i] = swap_add16(v[i], v[i + 8]);
        v[i] += dppf<0xB1>(0.f, v[i]);
        v[i] += dppf<0x4E>(0.f, v[i]);
        v[i] += dppf<0x141>(0.f, v[i]);
      }
      float s0 = v[0], s1 = v[1], s2 = v[2], s3 = v[3], s4 = v[4], s5 = v[5], s6 = v[6], s7 = v[7];
      asm volatile("" : "+v"(s0), "+v"(s1), "+v"(s2), "+v"(s3), "+v"(s4), "+v"(s5), "+v"(s6), "+v"(s7));
      s0 = (l & 4) ? s4 : s0; s1 = (l & 4) ? s5 : s1; s2 = (l & 4) ? s6 : s2; s3 = (l & 4) ? s7 : s3;
      s0 = (l & 2) ? s2 : s0; s1 = (l & 2) ? s3 : s1;
      const float kv = 2.0f * ((l & 1) ? s1 : s0);
      khl = pk2(kv, 0.f);
      khl = pk2(kv, kv - lo16(khl));
      kr[2080 - 64 * ps - tl] = (u16)khl;
      kr[CKN + 2080 - 64 * ps - tl] = (u16)(khl >> 16);
      SST(ps);
      if (ps < 2) {
        if (ps == 0) {
          if (w < 2) {
            reinterpret_cast<v4u*>(Xr)[tid] = tx[0];
            const unsigned long long m = (((unsigned long long)en.y << 32) | en.x) + TAG_STEP;
            v4u g0;
            for (unsigned spins = 0; spins < 16u * SPIN_CAP; ++spins) {
              g0 = LD128(MKRS(ws + OFF_G0, 8192), tid * 16, REP * 2048);
              if (__all(((g0.x ^ g0.z) | (g0.y ^ (unsigned)m) | (g0.w ^ (unsigned)(m >> 32))) == 0u)) break;
              asm volatile("s_sleep 2" ::: "memory");
            }
            U_ROW2(tid, g0.x);
          }
          __syncthreads();
        }
        f32x4 a0t = {};
#pragma unroll
        for (int b = -1; b < 4; ++b) a0t = MFMA16(KFRAG(-32 * b), conv_bfrag(ub + 16 * b, ush), a0t);
        const float y0 = swap_add32(a0t[0], a0t[1]), y1 = swap_add32(a0t[2], a0t[3]);
        if ((g & 1) == ps) TILE_OUT(0);
        __syncthreads();
        if (w == ps) {
          CHUNK_ST(w);
          if (ps == 0) zero_wait(ws);
          DRAIN();
          if (l < 8) set_flag(FL + FL_FC + ((2 * w * 4 + l) * 8 + (cv >> 3)) * 32 + (cv & 7));
        }
        SST(4 + ps);
      }
      if (ps == 2) {
        if (w >= 2) tx[0] = *reinterpret_cast<const v4u*>(A.x0 + (size_t)tid * DM + 8 * cv);
#pragma unroll
        for (int i = 1; i < 4; ++i) tx[i] = *reinterpret_cast<const v4u*>(A.x0 + (size_t)(tid + 512 * i) * DM + 8 * cv);
      }
    }
    if (w >= 2) reinterpret_cast<v4u*>(Xr)[tid] = tx[0];
#pragma unroll
    for (int i = 1; i < 4; ++i) reinterpret_cast<v4u*>(Xr)[tid + 512 * i] = tx[i];
    {
      unsigned spins = 0;
      while (!__all(__hip_atomic_load((gu32*)FS + (w + 8 * ((l >> 3) & 3)) * 32 + (l & 7), RLX_AGENT) != 0u)) {
        if (spin_timeout(++spins, TMO)) break;
        __builtin_amdgcn_s_sleep(4);
      }
      asm volatile("" ::: "memory");
    }
    if (w >= 2) U_ROW(tid);
#pragma unroll
    for (int i = 1; i < 4; ++i) U_ROW(tid + 512 * i);
    SST(6);
    __syncthreads();
    {
      f32x4 a1t = {};
#pragma unroll
      for (int b = -1; b < 8; ++b) a1t = MFMA16(KFRAG(128 - 32 * b), conv_bfrag(ub + 16 * b, ush), a1t);
      const float y0 = swap_add32(a1t[0], a1t[1]), y1 = swap_add32(a1t[2], a1t[3]);
      TILE_OUT(1);
    }
    __syncthreads();
    if (w < 2) {
      CHUNK_ST(2 + w);
      DRAIN();
      if (l < 8) set_flag(FL + FL_FC + (((4 + 2 * w) * 4 + l) * 8 + (cv >> 3)) * 32 + (cv & 7));
    }
    SST(8);
    (void)wait_n<8, 1>(FL + FL_KG + cv * 32 + (w >> 2), 1, TMO);
    SST(7);
    {
      const __amdgpu_buffer_rsrc_t rk = MKRS(ws + OFF_KF + (size_t)d * 2 * CKN * 2, 2 * CKN * 2);
      v4u t[8];
#pragma unroll
      for (int i = 0; i < 4; ++i) {
        t[i] = LD128(rk, (64 * i + l) * 16, 0);
        t[4 + i] = LD128(rk, (64 * i + l) * 16, CKN * 2);
      }
#pragma unroll
      for (int i = 0; i < 4; ++i)
        if (64 * i + l < 229) {
          reinterpret_cast<v4u*>(kr)[64 * i + l] = t[i];
          reinterpret_cast<v4u*>(kr + CKN)[64 * i + l] = t[4 + i];
        }
      kr[2080 - 192 - tl] = (u16)khl;
      kr[CKN + 2080 - 192 - tl] = (u16)(khl >> 16);
    }
    asm volatile("" ::: "memory");
#define BULK(TA, TB, HOOK)                                                                      \
  {                                                                                            \
    const bf16x8 bm = conv_bfrag(ub - 16, ush);                                                \
    _Pragma("unroll") for (int T = (TA); T < (TB); ++T) acc[T] = MFMA16(KFRAG(128 * T + 32), bm, (f32x4{})); \
    _Pragma("unroll") for (int T = 11; T < 16; ++T) far[T - 11] = MFMA16(KFRAG(128 * T + 32), bm, (f32x4{})); \
    _Pragma("unroll 1") for (int r = 0; r < 4; ++r) {                                          \
      bf16x8 bf[TB];                                                                           \
      _Pragma("unroll") for (int j = 0; j < (TB); ++j) bf[j] = conv_bfrag(ub + 16 * r + 64 * j, ush); \
      _Pragma("unroll") for (int e = 0; e < (TB); ++e) {                                       \
        const bf16x8 a = KFRAG(128 * e - 32 * r);                                              \
        _Pragma("unroll") for (int T = (e > (TA) ? e : (TA)); T < (TB); ++T) acc[T] = MFMA16(a, bf[T - e], acc[T]); \
      }                                                                                        \
      if (r == 0) { HOOK }                                                                     \
    }                                                                                          \
  }
    BULK(2, 11, if (w == 1) (void)(wait_n<SLP_V, 32>(FL + FL_FZ + REP * 64, 64, TMO)););
    SST(10);
#pragma unroll
    for (int T = 2; T < 11; ++T) {
      const float y0 = swap_add32(acc[T][0], acc[T][1]), y1 = swap_add32(acc[T][2], acc[T][3]);
      TILE_OUT(T);
    }
    __syncthreads();
    if (w >= 2) {
#pragma unroll
      for (int i = 0; i < 3; ++i) CHUNK_ST(3 * w - 2 + i);
#pragma unroll
      for (int i = 0; i < 2; ++i) xf[i] = *reinterpret_cast<const v4u*>(Xr + (64 * ((18 + 2 * w + i) & 31) + l) * 8);
      DRAIN();
      if (l < 24) set_flag(FL + FL_FC + ((6 * w - 4) * 4 + l) * 8 * 32 + (cv >> 3) * 32 + (cv & 7));
    }
#define FAR_PIECE(p)                                                                           \
  {                                                                                            \
    if ((p) < 24) {                                                                            \
      const int o_ = 256 * ((p) >> 2) + 32 * ((p) & 3);                                        \
      const bf16x8 b0 = conv_bfrag(ub1 + (o_ >> 1), ush), b1 = conv_bfrag(ub1 + (o_ >> 1) + 64, ush); \
      _Pragma("unroll") for (int e = 10; e < 16; ++e) {                                        \
        const bf16x8 a = KFRAG(128 * e - o_);                                                  \
        if (e > 10) far[e - 11] = MFMA16(a, b0, far[e - 11]);                                  \
        if (e < 15) far[e - 10] = MFMA16(a, b1, far[e - 10]);                                  \
      }                                                                                        \
    } else if ((p) < 28) {                                                                     \
      bf16x8 bf[4];                                                                            \
      _Pragma("unroll") for (int j = 0; j < 4; ++j) bf[j] = conv_bfrag(ub1 + 16 * ((p) & 3) + 64 * (12 + j), ush); \
      _Pragma("unroll") for (int e = 0; e < 4; ++e) {                                          \
        const bf16x8 a = KFRAG(128 * e - 32 * ((p) & 3));                                      \
        _Pragma("unroll") for (int j = 0; j + e < 4; ++j) far[1 + e + j] = MFMA16(a, bf[j], far[1 + e + j]); \
      }                                                                                        \
    } else if ((p) == 28) {                                                                    \
                     \
      u16* ucw = Uc + w * CUN + 32 + t0;                                                       \
      _Pragma("unroll") for (int T = 11; T < 16; ++T) {                                        \
        const float y0 = swap_add32(far[T - 11][0], far[T - 11][1]), y1 = swap_add32(far[T - 11][2], far[T - 11][3]); \
        const float u0 = bf2f(ucw[128 * T]), u1 = bf2f(ucw[128 * T + 32]);                     \
        const unsigned yy = pk2(y0, y1), uu = pk2(u0 * dsk, u1 * dsk);                         \
        const unsigned aa = pk2(lo16(yy) + lo16(uu), hi16(yy) + hi16(uu));                     \
        ucw[128 * T] = (u16)aa;                                                                \
        ucw[128 * T + 32] = (u16)(aa >> 16);                                                   \
      }                                                                                        \
    } else if (w >= 2 && w < 7) {                                                              \
      int l = tid & 63;                                                                        \
      asm volatile("" : "+v"(l), "+v"(xf[0]), "+v"(xf[1]));                                    \
      if ((p) == 29) {                                                                         \
        _Pragma("unroll") for (int i = 0; i < 2; ++i) {                                        \
          const int ck = 18 + 2 * w + i;                                                       \
          const u16* ur = Uc + 32 + 64 * ck + l;                                               \
          v4u o;                                                                               \
          _Pragma("unroll") for (int e = 0; e < 4; ++e)                                        \
            o[e] = pk2(lo16(xf[i][e]) + bf2f(ur[(2 * e) * CUN]), hi16(xf[i][e]) + bf2f(ur[(2 * e + 1) * CUN])); \
          ST128(o, rz1, (l >> 5) * 32768 + (l & 31) * 16, (2 * ck * 64 + cv) * 512);           \
        }                                                                                      \
      } else {                                                                                 \
        DRAIN();                                                                               \
        if (l < 16) set_flag(FL + FL_FC + (((36 + 4 * w) * 4 + l) * 8 + (cv >> 3)) * 32 + (cv & 7)); \
      }                                                                                        \
    }                                                                                          \
  }
#undef BULK
    SST(9);
#undef TILE_OUT
#undef CHUNK_ST
#undef U_ROW
#undef U_ROW2
  }
  u16* uc = Uc + (8 + w) * CUN;
  const unsigned* ub = reinterpret_cast<const unsigned*>(uc + (c & ~1) + 8 * g + 32);
#pragma unroll
  for (int j = 0; j < 16; ++j) acc[j] = f32x4{};
  const __amdgpu_buffer_rsrc_t rx = MKRS(ws + OFF_X2, SZ_X), rz = MKRS(ws + OFF_X3, SZ_X);
  const __amdgpu_buffer_rsrc_t rf = MKRS(FL + FL_FR, 64 * 16 * 128);
  unsigned st = 0u;
  v2u xx = {0u, 0u};
#define X2_LOAD(ss, far)                                                                                                      \
  {                                                                                                                      \
    xx = LD64(rx, (l >> 1) * (DM * 2) + (l & 1) * 8, (far) ? (int)SZ_X : ((ss) * SEG * DM + 8 * cv) * 2);                 \
    st = __builtin_amdgcn_raw_buffer_load_b32(rf, (l >> 4) * 128 + ((l >> 1) & 7) * 4, ((ss) * 4 + REP) * 512, AUX_SC1); \
  }
  unsigned tl = now10();
  for (int s = 0; s < NSEG; ++s) {
    if (w == 1) {
      for (unsigned spins = 0; !__all(umin3(xx.x, xx.y, st) != 0u); ++spins) {
        if (spin_timeout(spins, TMO)) break;
        if (GATE_V && spins == 0 && !__any(st != 0u)) gate(tl);
        if (spins && !__all(st != 0u)) __builtin_amdgcn_s_sleep(SLP_V);
        X2_LOAD(s, __builtin_popcountll(__ballot(st != 0u)) < CVTHR);
      }
      tl = now10() + GATE_V;
      TS(0, s, 64);
      const float mb = lo16(st), ri = 1.0f / hi16(st);
      const unsigned t0 = pk2(lo16(xx.x) - mb, hi16(xx.x) - mb), t1 = pk2(lo16(xx.y) - mb, hi16(xx.y) - mb);
      *reinterpret_cast<v2u*>(Su + l * 4) = v2u{pk2(lo16(t0) * ri, hi16(t0) * ri), pk2(lo16(t1) * ri, hi16(t1) * ri)};
      *reinterpret_cast<v2u*>(Sx + l * 4) = xx;
    }
    const int q = s & 3;
    const u16* kq = ka + 32 * q;
    const bf16x8 a0 = *reinterpret_cast<const bf16x8*>(kq - 32), a1 = *reinterpret_cast<const bf16x8*>(kq);
    __syncthreads();
    TS(1, s, 0);
    if (s == 0)
      for (int x = l; x < CUN / 2; x += 64) reinterpret_cast<unsigned*>(uc)[x] = 0u;
    if (l < 32) uc[32 + 32 * s + l] = Su[l * 8 + w];
    asm volatile("s_waitcnt lgkmcnt(0)" ::: "memory");
    const bf16x8 bp = conv_bfrag(ub + 16 * (s - 1), ush);
    acc[0] = MFMA16(a0, bp, (q == 0 ? acc[1] : acc[0]));
    const f32x4 tmp = MFMA16(a1, conv_bfrag(ub + 16 * s, ush), acc[0]);
    float y0 = (q & 1) ? tmp[2] : tmp[0], y1 = (q & 1) ? tmp[3] : tmp[1];
    y0 = xsum32(y0);
    y1 = xsum32(y1);
    if (g == (q >> 1)) {
      const float u0 = bf2f(Su[c * 8 + w]), u1 = bf2f(Su[(16 + c) * 8 + w]);
      const float a0 = bfr(bfr(y0) + bfr(u0 * dsk)), a1 = bfr(bfr(y1) + bfr(u1 * dsk));
      To[c * 8 + w] = f2bf(bf2f(Sx[c * 8 + w]) + a0);
      To[(16 + c) * 8 + w] = f2bf(bf2f(Sx[(16 + c) * 8 + w]) + a1);
    }
    __syncthreads();
    if (w == 0 && l < 32) {
      const v4u o = *reinterpret_cast<const v4u*>(To + l * 8);
      ST128(o, rz, l * 16, (s * 64 + cv) * 512);
    }
    if (w == 1) X2_LOAD(s + 1, CVTHR > 0);
    const int left = 15 - (s >> 2);
#define PUSH(J0, SH)                                                                                              \
  if (left >= (J0)) {                                                                                             \
    _Pragma("unroll") for (int j = (J0); j < (J0) + 4 && j < 16; ++j)                                             \
      acc[j] = MFMA16(*reinterpret_cast<const bf16x8*>(kq - 32 - 128 * j), bp, (j + (SH) < 16 ? acc[(j + (SH)) & 15] : f32x4{})); \
  }
    TS(2, s, 0);
    if (w == 0) {
      DRAIN();
      if (l < 4) set_flag(FL + FL_FC + (((64 + s) * 4 + l) * 8 + (cv >> 3)) * 32 + (cv & 7));
    }
    TS(3, s, 0);
    if (q == 0) { PUSH(1, 1) PUSH(5, 1) PUSH(9, 1) PUSH(13, 1) }
    else { PUSH(1, 0) PUSH(5, 0) PUSH(9, 0) PUSH(13, 0) }
#undef PUSH
    MSTAMP();
    if (s >= CVPRE && s < 31 + CVPRE) FAR_PIECE(s - CVPRE);
  }
#undef KFRAG
#undef X2_LOAD
#undef FAR_PIECE
}

__device__ __forceinline__ v4u ln_row_packed(const v4u xr) {
  float v[8];
#pragma unroll
  for (int e = 0; e < 4; ++e) { v[2 * e] = lo16(xr[e]); v[2 * e + 1] = hi16(xr[e]); }
  float s = ((v[0] + v[1]) + (v[2] + v[3])) + ((v[4] + v[5]) + (v[6] + v[7]));
  s = wave_sum(s);
  const float mean = s * (1.0f / DM);
  float q = 0.f;
#pragma unroll
  for (int j = 0; j < 8; ++j) { float t = v[j] - mean; q += t * t; }
  q = wave_sum(q);
  const float var = q * (1.0f / DM);
  const float mb = bfr(mean);
  const float sd = bfr(sqrtf(bfr(bfr(var) + 1e-5f)));
  const float ri = 1.0f / sd;
  v4u o;
#pragma unroll
  for (int e = 0; e < 4; ++e) {
    const unsigned t = pk2(v[2 * e] - mb, v[2 * e + 1] - mb);
    const float x0 = lo16(t), x1 = hi16(t);
    float q0 = x0 * ri, q1 = x1 * ri;
    q0 = __builtin_fmaf(__builtin_fmaf(-q0, sd, x0), ri, q0);
    q1 = __builtin_fmaf(__builtin_fmaf(-q1, sd, x1), ri, q1);
    o[e] = pk2(q0, q1);
  }
  return o;
}

__device__ __forceinline__ float bfr1(float x) { return lo16(pk2(x, x)); }
__device__ __forceinline__ void ln4_packed(v4u (&x)[4]) {
  f32x2_t v[16];
#pragma unroll
  for (int e = 0; e < 16; ++e) v[e] = f32x2_t{lo16(x[e >> 2][e & 3]), hi16(x[e >> 2][e & 3])};
  const f32x2_t t = (((v[0] + v[1]) + (v[2] + v[3])) + ((v[4] + v[5]) + (v[6] + v[7]))) +
                    (((v[8] + v[9]) + (v[10] + v[11])) + ((v[12] + v[13]) + (v[14] + v[15])));
  const float mean = red_mod4(t.x + t.y) * (1.0f / DM);
  const float mb = bfr1(mean);
  f32x2_t q0 = {0.f, 0.f}, q1 = {0.f, 0.f};
#pragma unroll
  for (int e = 0; e < 16; e += 2) {
    v[e] -= mb;
    v[e + 1] -= mb;
    q0 += v[e] * v[e];
    q1 += v[e + 1] * v[e + 1];
  }
  q0 += q1;
  const float dm = mean - mb;
  const float var = fmaxf(red_mod4(q0.x + q0.y) * (1.0f / DM) - dm * dm, 0.f);
  const float sd = bfr1(sqrtf(bfr1(bfr1(var) + 1e-5f)));
  const float ri = 1.0f / sd;
#pragma unroll
  for (int e = 0; e < 16; ++e) {
    const unsigned t2 = pk2(v[e].x, v[e].y);
    const f32x2_t q = f32x2_t{lo16(t2), hi16(t2)} * ri;
    x[e >> 2][e & 3] = pk2(q.x, q.y);
  }
}

extern "C" __device__ float mul1(float, float) __asm("llvm.amdgcn.fmul.legacy");
template <bool G0>
__device__ __forceinline__ void role_qkv_t(const MegaArgs& A, char* smem, const int L, const int h) {
  __builtin_amdgcn_s_waitcnt(0x0F70);
  const int w = __builtin_amdgcn_readfirstlane(threadIdx.x >> 6);
  char* ws = A.ws;
  unsigned* FL = (unsigned*)(ws + OFF_FL);
  unsigned* TMO = FL + FL_TMO;
#define LANE_IDS()                         \
  const int l = lane_id();                 \
  const int c = l & 15, g = l >> 4;        \
  (void)c; (void)g;
  unsigned* FC = FL + FL_FC + (L * 64 * 4 + REP) * 8 * 32;
  bf16x8 wa[16], wb[16], wv[16];
  {
    LANE_IDS();
    const unsigned sb = lds_base(smem);
    const int q = (l >> 2) & 3, p = l & 3, f = q | ((g & 1) << 2);
    const unsigned ra = sb + (8 * g + q) * 256 + (w ^ f) * 32 + p * 8;
    const unsigned rb = sb + (8 * g + q) * 256 + ((2 * (w & 3) + (p >> 1)) ^ f) * 32 + (p & 1) * 16;
#define W_READ(W, hf, A0)                                                      \
  {                                                                           \
    _Pragma("unroll") for (int t = 0; t < 8; ++t) {                           \
      const unsigned a_ = (A0) + (hf) * 8192 + t * 16384;                     \
      const v2u r0 = trd(a_), r1 = trd(a_ + 1024);                            \
      W[8 * (hf) + t] = BF8((v4u{r0.x, r0.y, r1.x, r1.y}));                   \
    }                                                                         \
    _Pragma("unroll") for (int t = 0; t < 8; ++t) asm volatile("" : "+v"(W[8 * (hf) + t])); \
  }
#define W_STAGE(M, hf, VM, NEXT)                                               \
  {                                                                           \
    WAIT_VM(VM);                                                              \
    RAW_BAR();                                                                \
    if ((M) == 2) W_READ(wv, hf, ra);                                         \
    if ((M) == (G0 ? 1 : 0)) { W_READ(wa, hf, rb); W_READ(wb, hf, rb + 8); }  \
    RAW_BAR();                                                                \
    NEXT;                                                                     \
  }
    wqkv_issue<false>(A.Wq, 0, h, w, l, sb);
    wqkv_issue<false>(A.Wq, 1, h, w, l, sb);
    W_STAGE(0, 0, 8, wqkv_issue<false>(A.Wk, 0, h, w, l, sb));
    SST(4);
    W_STAGE(0, 1, 8, wqkv_issue<false>(A.Wk, 1, h, w, l, sb));
    SST(5);
    W_STAGE(1, 0, 8, wqkv_issue<false>(A.Wv, 0, h, w, l, sb));
    W_STAGE(1, 1, 8, wqkv_issue<false>(A.Wv, 1, h, w, l, sb));
    SST(6);
    W_STAGE(2, 0, 8, (void)0);
    W_STAGE(2, 1, 0, (void)0);
#undef W_STAGE
#undef W_READ
  }
  WSTAMP2();
  constexpr int Q_X = 98304;
  const unsigned sb = lds_base(smem);
  v2u ea, eb, ev;
  {
    LANE_IDS();
    const v4u zt = LD128(MKRS(ws + OFF_TAG, 8192 + 8), l * 16, (blockIdx.x & 7) * 1024);
    const v2u ze = LD64(MKRS(ws + OFF_TAG, 8192 + 8), 0, 8192);
    const v4u one = {0x3f803f80u, 0x3f803f80u, 0x3f803f80u, 0x3f803f80u};
    f32x4 t[3] = {};
#pragma unroll
    for (int ks = 0; ks < 16; ++ks) {
      t[0] = MFMA16(BF8(one), wa[ks], t[0]);
      t[1] = MFMA16(BF8(one), wb[ks], t[1]);
      t[2] = MFMA16(BF8(one), wv[ks], t[2]);
    }
    v2u e[3];
#pragma unroll
    for (int i = 0; i < 3; ++i) {
      const float x = g ? 0.f : t[i][0], x1 = x - bfr1(x);
      e[i] = v2u{pk2(x, x1), pk2(x1 - bfr1(x1), 0.f)};
    }
    ea = e[0]; eb = e[1]; ev = e[2];
    asm volatile("" : "+v"(ea), "+v"(eb), "+v"(ev));
    if (!zero_seen(zt, ze)) zero_wait(ws);
    if (l < 32) *reinterpret_cast<v4u*>(smem + Q_X + w * 2048 + (l >> 4) * 16384 + c * 16) = one;
  }
  constexpr int Q_ST = Q_X + 8 * 2048 + 256, Q_CNT = Q_X + 11 * 2048 + 256;
  if (threadIdx.x == 0) *(lu32*)(unsigned long)(sb + Q_CNT) = 0u;
  v8u pv, wr[24];
  v4u pw;
  unsigned sw;
  {
    LANE_IDS();
    const unsigned fa = sb + g * 512 + c * 16;
    pv = v8u{ea.x, ea.y, eb.x, eb.y, ev.x, ev.y, (unsigned)l * 16u, fa};
    pw = v4u{sb + Q_ST + c * 4, sb + Q_ST + g * 16,
             G0 ? (l & 7) * 4 + (l & 8) * 16 : (c & 8) ? sb + Q_X + ((w + 1) & 1) * 128 : fa + (w & 3) * 128, (c & 8) ? 0u : 0x8000u};
    sw = sb + Q_ST + (w & 3) * 32 + (l & 7) * 4;
#pragma unroll
    for (int i = 0; i < 8; ++i) {
      wr[i] = __builtin_shufflevector(__builtin_bit_cast(v4u, wa[2 * i]), __builtin_bit_cast(v4u, wa[2 * i + 1]), 0, 1, 2, 3, 4, 5, 6, 7);
      wr[8 + i] = __builtin_shufflevector(__builtin_bit_cast(v4u, wb[2 * i]), __builtin_bit_cast(v4u, wb[2 * i + 1]), 0, 1, 2, 3, 4, 5, 6, 7);
      wr[16 + i] = __builtin_shufflevector(__builtin_bit_cast(v4u, wv[2 * i]), __builtin_bit_cast(v4u, wv[2 * i + 1]), 0, 1, 2, 3, 4, 5, 6, 7);
    }
  }
  const char* Xb = ws + (L ? OFF_X3 : OFF_XA) + w * 8192;
  const v4u rsQ = mk_srd(ws + OFF_QK + L * SZ_QK, SZ_QK), rsV = mk_srd(ws + OFF_VF + L * SZ_O, SZ_O);
  const v4u rsM = mk_srd(ws + (G0 ? OFF_MEMK : OFF_MEMV) + L * SZ_MEM, SZ_MEM);
  const v4u rsF = mk_srd(FC + w * 64, (64 * 32 - REP * 8 - 2 * w) * 128);
  const unsigned* FQ = FL + FL_FQ + (L * 65 * 32 + h) * 32;
  const char* dbg = ws;
  __builtin_amdgcn_s_waitcnt(0x0F70);
  RAW_BAR();
#define QKV_OPS                                                                                                              \
  ::"{v[64:71]}"(wr[0]), "{v[72:79]}"(wr[1]), "{v[80:87]}"(wr[2]), "{v[88:95]}"(wr[3]), "{v[96:103]}"(wr[4]),                \
      "{v[104:111]}"(wr[5]), "{v[112:119]}"(wr[6]), "{v[120:127]}"(wr[7]), "{v[128:135]}"(wr[8]), "{v[136:143]}"(wr[9]),      \
      "{v[144:151]}"(wr[10]), "{v[152:159]}"(wr[11]), "{v[160:167]}"(wr[12]), "{v[168:175]}"(wr[13]), "{v[176:183]}"(wr[14]), \
      "{v[184:191]}"(wr[15]), "{v[192:199]}"(wr[16]), "{v[200:207]}"(wr[17]), "{v[208:215]}"(wr[18]), "{v[216:223]}"(wr[19]), \
      "{v[224:231]}"(wr[20]), "{v[232:239]}"(wr[21]), "{v[240:247]}"(wr[22]), "{v[248:255]}"(wr[23]), "{v[0:7]}"(pv),        \
      "{v[8:11]}"(pw), "{v62}"(sw), "{s[40:43]}"(rsQ), "{s[44:47]}"(rsV), "{s[48:51]}"(rsM), "{s[52:55]}"(rsF),              \
      "{s[56:57]}"(Xb), "{s58}"(sb + w * 8192), "{s59}"((h * 16 + (G0 ? 8 : 0) + (w & 3)) * 1024), "{s60}"((h * 8 + w) * 1024), \
      "{s61}"(h * 2048 + (w & 3) * 512), "{s62}"(0), "{s63}"(L), "{s[64:65]}"(FQ), "{s66}"(NSEG - 1), "{s67}"(0),            \
      "{s[70:71]}"(0xaaaaaaaaaaaaaaaaull), "{s[72:73]}"(0xccccccccccccccccull), "{s78}"(w), "{s80}"(sb + Q_CNT),             \
      "{s[36:37]}"(dbg), [gq] "n"(GATE_Q), [sq] "n"(SLP_Q), [s1] "n"(SLP_Q1 - SLP_Q)                                         \
      : "memory", "vcc", "scc", "s81", "v63"
  if (G0) asm volatile("s_mov_b64 exec, -1\ns_nop 4\n" QKV_ASM_0 QKV_OPS);
  else asm volatile("s_mov_b64 exec, -1\ns_nop 4\n" QKV_ASM_1 QKV_OPS);
#undef QKV_OPS
#undef LANE_IDS
}
__device__ __forceinline__ void role_qkv(const MegaArgs& A, char* smem, const int L, const int h) {
  if (__builtin_amdgcn_readfirstlane(threadIdx.x >> 6) < 4) role_qkv_t<true>(A, smem, L, h);
  else role_qkv_t<false>(A, smem, L, h);
}

constexpr int MEGA_WGS = 256;
constexpr size_t MEGA_LDS = (size_t)(32 * 4 * 64 * 8 + 8 * SMS + 15 * QS + 128 + 16 * 64 * 8 + 8 * SOS + 512) * 2;
static_assert(MEGA_LDS <= 163840 && MEGA_LDS >= 162816 + 256, "lds");
static_assert(MEGA_LDS >= (size_t)AW_CNT + 64, "lds");
static_assert(MEGA_LDS >= (size_t)(8 * 2 * CKN + 8 * 2 * CUN + 4 * 32 * 8) * 2, "lds");
static_assert(MEGA_LDS >= (size_t)2 * 32 * SMS * 2 && MEGA_LDS >= 131072, "lds");
static_assert(131072 >= (size_t)(40 * NST + 32 * 400) * 4 && 131072 + 4 * 392 * 4 <= AW_CNT, "lds");
__global__ __launch_bounds__(512, 2) void mega_kernel(const u16* mem0, const u16* Dskip, char* ws, u16* out, const u16* x0,
                                                      const u16* logAre, const u16* Aim, const u16* Cre, const u16* Cim,
                                                      const u16* logdt, const u16* Wq, const u16* Wk, const u16* Wv,
                                                      const u16* Wo) {
  extern __shared__ __attribute__((aligned(16))) char smem[];
  const int b = __builtin_amdgcn_readfirstlane(role_no());
  const MegaArgs A{mem0, Dskip, ws, out, x0, logAre, Aim, Cre, Cim, logdt, Wq, Wk, Wv, Wo, b};
  {
    const int hi = (b >= 32 && b < 96) ? b - 32 : (b >= 200 && b < 248) ? b - 136 : (b >= 96 && b < 104) ? b + 16 : (b >= 248 ? b - 128 : -1);
    if (hi >= 0) start_kgen(A, smem, hi, b - 248, (b >= 64 && b < 104) || (b >= 232 && b < 240) ? b & 15 : -1);
  }
  if ((EXP_OFF & 1) && b >= 64 && b < 80) return;
  if ((EXP_OFF & 8) && b >= 200 && b < 232) return;
  if ((EXP_OFF & 16) && b >= 32 && b < 64) return;
  if ((EXP_OFF & 32) && b >= 80 && b < 96) return;
  if (b < 64) role_chain(A, smem, b >> 5, b & 31, 0);
  else if (b < 104 || (b >= 232 && b < 240)) role_aw(A, smem, b < 80 ? 0 : b < 96 ? 2 : 1, b & 15);
  else if (b < 168) role_conv(A, smem, b - 104);
  else if (b < 232) role_qkv(A, smem, (b - 168) >> 5, (b - 168) & 31);
  else role_red(A, smem, b - 240);
}

extern "C" void kernel_launch(void* const* d_in, const int* in_sizes, int n_in, void* d_out, int out_size, void* d_ws,
                              size_t ws_size, hipStream_t stream) {
  MegaArgs A;
  A.x0 = (const u16*)d_in[0];
  A.logAre = (const u16*)d_in[1];
  A.Aim = (const u16*)d_in[2];
  A.Cre = (const u16*)d_in[3];
  A.Cim = (const u16*)d_in[4];
  A.logdt = (const u16*)d_in[5];
  A.Dskip = (const u16*)d_in[6];
  A.mem0 = (const u16*)d_in[8];
  A.Wq = (const u16*)d_in[9];
  A.Wk = (const u16*)d_in[10];
  A.Wv = (const u16*)d_in[11];
  A.Wo = (const u16*)d_in[12];
  A.ws = (char*)d_ws;
  A.out = (u16*)d_out;

  (void)hipFuncSetAttribute((const void*)mega_kernel, hipFuncAttributeMaxDynamicSharedMemorySize, (int)MEGA_LDS);
  mega_kernel<<<MEGA_WGS, 512, MEGA_LDS, stream>>>(A.mem0, A.Dskip, A.ws, A.out, A.x0, A.logAre, A.Aim, A.Cre, A.Cim, A.logdt,
                                                   A.Wq, A.Wk, A.Wv, A.Wo);
}
```

```cpp
#include <hip/hip_runtime.h>
#include <hip/hip_bf16.h>
#include <stdint.h>

constexpr int L_SEQ = 2048;
constexpr int DM    = 512;
constexpr int NST   = 128;
constexpr int NH    = 32;
constexpr int DH    = 128;
constexpr int HD    = NH * DH;
constexpr int MM    = 8;
constexpr int SEG   = 32;
constexpr int NSEG  = L_SEQ / SEG;
constexpr int LK    = MM + SEG;

typedef unsigned short u16;
using bf16x8 = __attribute__((ext_vector_type(8))) short;
using f32x4  = __attribute__((ext_vector_type(4))) float;
using f32x16 = __attribute__((ext_vector_type(16))) float;

static __device__ __forceinline__ u16 f2bf(float f) {
  unsigned u = __float_as_uint(f);
  u += 0x7fffu + ((u >> 16) & 1u);
  return (u16)(u >> 16);
}
static __device__ __forceinline__ float bf2f(u16 h) { return __uint_as_float(((unsigned)h) << 16); }
static __device__ __forceinline__ float bfr(float f) { return bf2f(f2bf(f)); }

constexpr size_t SZ_X      = (size_t)L_SEQ * DM * 2;
constexpr size_t OFF_KF    = 0;
constexpr size_t OFF_UT    = OFF_KF + (size_t)DM * L_SEQ * 4;
constexpr size_t OFF_YT    = OFF_UT + SZ_X;
constexpr size_t OFF_XA    = OFF_YT + SZ_X;
constexpr size_t OFF_H     = OFF_XA + SZ_X;
constexpr size_t OFF_X2    = OFF_H + SZ_X;
constexpr size_t OFF_U2    = OFF_X2 + SZ_X;
constexpr size_t OFF_X3    = OFF_U2 + SZ_X;
constexpr size_t SZ_QK     = (size_t)L_SEQ * 2 * HD * 2;
constexpr size_t OFF_QK    = OFF_X3 + SZ_X;
constexpr size_t SZ_O      = (size_t)L_SEQ * HD * 2;
constexpr size_t OFF_VF    = OFF_QK + 2 * SZ_QK;
constexpr size_t SZ_PW     = (size_t)2 * NSEG * 16 * SEG * DM * 2;
constexpr size_t OFF_PW    = OFF_VF + 2 * SZ_O;
constexpr size_t SZ_MEM    = (size_t)NSEG * MM * HD * 2;
constexpr size_t OFF_MEMK  = OFF_PW + SZ_PW;
constexpr size_t OFF_MEMV  = OFF_MEMK + 2 * SZ_MEM;
constexpr size_t OFF_GT    = OFF_MEMV + 2 * SZ_MEM;
constexpr size_t SZ_ACC    = (size_t)2 * 32768;
constexpr size_t OFF_Z     = OFF_GT + (size_t)64 * DM * 4;
constexpr size_t OFF_ACC   = OFF_Z;
constexpr unsigned ACC_LO = 0x88000000u, ACC_HI = 0x87fffff7u;
constexpr size_t OFF_FL    = OFF_ACC + 2 * SZ_ACC;
constexpr int FL_FW  = 0;
constexpr int FL_FR  = FL_FW + 2 * 64 * 16 * 32;
constexpr int FL_FC  = FL_FR + 64 * 16 * 32;
constexpr int FL_FQ  = FL_FC + 2 * 64 * 32 * 32;
constexpr int FL_FS  = FL_FQ + 2 * 65 * 32 * 32;
constexpr int FL_FZ  = FL_FS + 4 * 32 * 32;
constexpr int FL_KG  = FL_FZ + 8 * 32;
constexpr int FL_G   = FL_KG + 64 * 32;
constexpr int FL_TK  = FL_G + 4 * 4 * 32;
constexpr int FL_TMO = FL_TK + 2 * 256;
constexpr int FL_END = FL_TMO + 64;
constexpr size_t Z_BYTES   = OFF_FL + (size_t)FL_END * 4 - OFF_Z;
constexpr size_t OFF_TAG   = OFF_Z + Z_BYTES;
constexpr unsigned long long TAG_STEP = 0x9e3779b97f4a7c15ull;
constexpr size_t OFF_G0    = OFF_TAG + 8 * 1024 + 256;
constexpr size_t WS_TOTAL  = OFF_G0 + 8192;
static_assert(Z_BYTES % 256 == 0 && OFF_Z % 256 == 0 && Z_BYTES <= 2 * 128 * 8192, "zero region");
#define REP ((blockIdx.x >> 1) & 3)
static_assert(WS_TOTAL + (4 << 20) <= (size_t)256 * 1024 * 1024, "workspace too large");

#define CKNP 0
constexpr int CKN = 2208 + CKNP;
static_assert((size_t)DM * 2 * CKN * 2 <= (size_t)DM * L_SEQ * 4 + SZ_X, "K buffer");
__device__ __forceinline__ float pos_of_index(int p) {
  if (p < 256) return (float)p;
  if (p < 384) return (float)(256 + 2 * (p - 256));
  if (p < 512) return (float)(512 + 4 * (p - 384));
  if (p < 640) return (float)(1024 + 8 * (p - 512));
  return 2048.0f;
}
__device__ __forceinline__ int index_of_l(int l) {
  float pf = bfr((float)l);
  int p = (int)pf;
  if (p < 256) return p;
  if (p < 512) return 256 + ((p - 256) >> 1);
  if (p < 1024) return 384 + ((p - 512) >> 2);
  if (p < 2048) return 512 + ((p - 1024) >> 3);
  return 640;
}

template <int CTRL> __device__ __forceinline__ float dppr(float x) {
  return __builtin_bit_cast(float, __builtin_amdgcn_update_dpp(0, __builtin_bit_cast(int, x), CTRL, 0xf, 0xf, false));
}
__device__ __forceinline__ float wave_sum(float v) {
  v += dppr<0x121>(v);
  v += dppr<0x122>(v);
  v += dppr<0x124>(v);
  v += dppr<0x128>(v);
  {
    auto r = __builtin_amdgcn_permlane16_swap(__float_as_uint(v), __float_as_uint(v), false, false);
    v = __uint_as_float(r[0]) + __uint_as_float(r[1]);
  }
  {
    auto r = __builtin_amdgcn_permlane32_swap(__float_as_uint(v), __float_as_uint(v), false, false);
    v = __uint_as_float(r[0]) + __uint_as_float(r[1]);
  }
  return v;
}
__device__ __forceinline__ void ln_row8(float (&v)[8]) {
  float s = 0.f;
#pragma unroll
  for (int j = 0; j < 8; ++j) s += v[j];
  s = wave_sum(s);
  const float mean = s * (1.0f / DM);
  float q = 0.f;
#pragma unroll
  for (int j = 0; j < 8; ++j) { float t = v[j] - mean; q += t * t; }
  q = wave_sum(q);
  const float var = q * (1.0f / DM);
  const float mb = bfr(mean);
  const float sd = bfr(sqrtf(bfr(bfr(var) + 1e-5f)));
#pragma unroll
  for (int j = 0; j < 8; ++j) v[j] = bfr(bfr(v[j] - mb) / sd);
}

#define RLX_AGENT __ATOMIC_RELAXED, __HIP_MEMORY_SCOPE_AGENT
typedef __attribute__((address_space(1))) unsigned gu32;
typedef __attribute__((address_space(3))) unsigned lu32;
typedef unsigned v4u __attribute__((ext_vector_type(4)));
constexpr int AUX_SC1 = 16;
constexpr unsigned SPIN_CAP = 1u << 18;

__device__ __forceinline__ bool wait_flags(const unsigned* flags, int lane, int nfl, unsigned want, unsigned* tmo) {
  for (unsigned spins = 0;; ++spins) {
    unsigned v = want;
    if (lane < nfl) v = __hip_atomic_load((gu32*)flags + lane * 32, RLX_AGENT);
    if (__all(v >= want)) break;
    if (spins > SPIN_CAP) {
      if (lane == 0) __hip_atomic_store((gu32*)tmo, 1u, RLX_AGENT);
      return false;
    }
  }
  __builtin_amdgcn_fence(__ATOMIC_ACQUIRE, "wavefront");
  return true;
}

#define STAMP(i)
#define STAMP_DUMP()
#define WSTAMP(i)
template <int CTRL> __device__ __forceinline__ float dppf(float old, float x) {
  return __builtin_bit_cast(float, __builtin_amdgcn_update_dpp(__builtin_bit_cast(int, old), __builtin_bit_cast(int, x), CTRL, 0xf, 0xf, false));
}
__device__ __forceinline__ float wave_max_dpp(float x) {
  const float ninf = -3e38f;
  x = fmaxf(x, dppf<0x111>(ninf, x));
  x = fmaxf(x, dppf<0x112>(ninf, x));
  x = fmaxf(x, dppf<0x114>(ninf, x));
  x = fmaxf(x, dppf<0x118>(ninf, x));
  x = fmaxf(x, dppf<0x142>(ninf, x));
  x = fmaxf(x, dppf<0x143>(ninf, x));
  return __builtin_bit_cast(float, __builtin_amdgcn_readlane(__builtin_bit_cast(int, x), 63));
}
__device__ __forceinline__ float wave_sum_dpp(float x) {
  x += dppf<0x111>(0.f, x);
  x += dppf<0x112>(0.f, x);
  x += dppf<0x114>(0.f, x);
  x += dppf<0x118>(0.f, x);
  x += dppf<0x142>(0.f, x);
  x += dppf<0x143>(0.f, x);
  return __builtin_bit_cast(float, __builtin_amdgcn_readlane(__builtin_bit_cast(int, x), 63));
}
constexpr int SMS = 528;
constexpr int QS = 144;
constexpr int VS = 80;
constexpr int SOS = 144;
#define VAR_NREP 4
constexpr int NREP = VAR_NREP;
__device__ __forceinline__ float xsum16(float x) {
  auto r = __builtin_amdgcn_permlane16_swap(__float_as_uint(x), __float_as_uint(x), false, false);
  return __uint_as_float(r[0]) + __uint_as_float(r[1]);
}
__device__ __forceinline__ float xsum32(float x) {
  auto r = __builtin_amdgcn_permlane32_swap(__float_as_uint(x), __float_as_uint(x), false, false);
  return __uint_as_float(r[0]) + __uint_as_float(r[1]);
}
__device__ __forceinline__ float xmax16(float x) {
  auto r = __builtin_amdgcn_permlane16_swap(__float_as_uint(x), __float_as_uint(x), false, false);
  return fmaxf(__uint_as_float(r[0]), __uint_as_float(r[1]));
}
__device__ __forceinline__ float xmax32(float x) {
  auto r = __builtin_amdgcn_permlane32_swap(__float_as_uint(x), __float_as_uint(x), false, false);
  return fmaxf(__uint_as_float(r[0]), __uint_as_float(r[1]));
}
__device__ __forceinline__ float red_mod4(float x) {
  x += dppf<0x124>(0.f, x);
  x += dppf<0x128>(0.f, x);
  return xsum32(xsum16(x));
}
typedef __bf16 bf16x2_t __attribute__((ext_vector_type(2)));
typedef float f32x2_t __attribute__((ext_vector_type(2)));
__device__ __forceinline__ unsigned pk2(float a, float b) {
  f32x2_t v = {a, b};
  bf16x2_t r = __builtin_convertvector(v, bf16x2_t);
  return __builtin_bit_cast(unsigned, r);
}
__device__ __forceinline__ float dot2sq(const unsigned x, const float acc) {
  return __builtin_amdgcn_fdot2_f32_bf16(__builtin_bit_cast(bf16x2_t, x), __builtin_bit_cast(bf16x2_t, x), acc, false);
}
__device__ __forceinline__ float lo16(unsigned u) { return __uint_as_float(u << 16); }
__device__ __forceinline__ float hi16(unsigned u) { return __uint_as_float(u & 0xffff0000u); }
__device__ __forceinline__ void scale_pair(float a, float b, float scale, float& oa, float& ob) {
  unsigned u = pk2(a, b);
  unsigned v = pk2(lo16(u) * scale, hi16(u) * scale);
  oa = lo16(v);
  ob = hi16(v);
}

#define XSPLIT 3
__device__ __forceinline__ int role_no() {
  const int b = blockIdx.x, x = b & 7, j = b >> 3;
  if (XSPLIT >= 3) {
    const int y = XSPLIT == 5 ? x & 3 : x >> 1;
    if (XSPLIT >= 6 && (j >> 2) == 6) return (x & 1) ? 104 + 16 * y + j - 16 : 80 + 4 * y + (j & 3);
    if (XSPLIT >= 7 && (j >> 2) == 4) return (x & 1) ? 104 + 16 * y + j - 16 : 64 + 4 * y + (j & 3);
    if (XSPLIT == 5 ? x < 4 : !(x & 1)) {
      if (j >= 16) return 104 + 16 * y + j - 16;
      return XSPLIT == 4 ? 16 * y + j : (j & 8) * 4 + 8 * y + (j & 7);
    }
    if (j < 16) return 168 + (j & 8) * 4 + 8 * y + (j & 7);
    const int i = 4 * y + (j & 3);
    return j < 20 ? 64 + i : j < 24 ? (i < 8 ? 96 + i : 224 + i) : j < 28 ? 80 + i : 240 + i;
  }
  const int L = x >> 2, xx = x & 3;
  if (j < 8) return 32 * L + 8 * xx + j;
  if (j < 12) return 64 + 16 * L + 4 * xx + j - 8;
  if (j < 16) { const int i = 4 * xx + j - 12; return L ? 240 + i : i < 8 ? 96 + i : 224 + i; }
  if (j < 24) return 104 + 8 * x + j - 16;
  return 168 + 32 * L + 8 * xx + j - 24;
}
#define MSTAMP()
#define WSTAMP2()
#define SST(k)
#define TSR(rn, k, ss, t)
#define TS(k, ss, t)
#define RST(k)
#define RSD()
#define LST(k)
#define LSD()
#define FSD
#define FS(k)
#define FD(ss)
struct MegaArgs {
  const u16* mem0;
  const u16* Dskip;
  char* ws;
  u16* out;
  const u16* x0;
  const u16 *logAre, *Aim, *Cre, *Cim, *logdt;
  const u16 *Wq, *Wk, *Wv, *Wo;
  int rn;
};

__device__ __forceinline__ int lane_id() {
  int l;
  asm volatile("v_mbcnt_lo_u32_b32 %0, -1, 0\n\tv_mbcnt_hi_u32_b32 %0, -1, %0" : "=v"(l));
  return l;
}
__device__ __forceinline__ bool spin_timeout(unsigned spins, unsigned* tmo) {
  if (spins > SPIN_CAP) {
    if (lane_id() == 0) __hip_atomic_store((gu32*)tmo, 1u, RLX_AGENT);
    return true;
  }
  return false;
}
#define PSWH (h & 2)
#define RR 0
#define RQM 63
#define RPM 63
#define CD0 100
#define APN 7
#define APL 300
#define APA 120
#define APB 80
#define APP 208
#define CD 30
#define CDS 30
#define CDL 0
#define XG(h) ((h) >> 3)
#define GP 230
#define GR 50
#define GM 0
#define ROT (4 * (h & 1))
#define CPRIO 1
#define P1PRIO 3
#define QLEAD 2
#define POLL_SLEEP 12
#define SLP_C POLL_SLEEP
#define SLP_A POLL_SLEEP
#define SLP_W POLL_SLEEP
#define SLP_R 4
#define SLP_V 4
#define SLP_Q 4
#define SLP_M 4
#define AWPRIO 2
#define SLP_S 8
#define SPOS 4
#define CVPRE 0
#define QHV 0
#define QCHK 0
#define LEADN 99
#define GATE_V 90
#define GATE_R 380
#define GATE_N 440
#define AWAHEAD 1
#define GATE_M 150
#define GATE_M2 380
#define QPACE 0
#define QPS 6
#define QPL 100
#define GATE_Q 150
#define SLP_Q1 24
#define QPF 3
#define QPV 6
#define QVP 3
#define QP0 2
#define QP1 0
#define QFP 15
#define QUP 7
#define QEP 3
#define AWD 9
#define GPER 200
#define GMA 60
#define GMR 60
#define GMS 0
#define GSTEP 30
#define GATE_RT 130
#define GATE_1 160
#define N1S 20
#define N1E 36
#define PRI_A 3
#define PRI_WA 2
#define PRI_WB 1
#define AWSKIP 3
#define PRI_0 3
#define AWDEF 1
#define SLP_F 2
#define EXP_OFF 0
#define AWSPEC 1
#define CVTHR 48
#define NR_C 8
#define NR_R 8
#define NR_W 8
#define NR_N 2
#define QAHEAD 0
#define NS0 4
#define CVST 16
#define CVNB 9
#define CVFULL 1
#define HK3 0
#define AWDT 120
#define SLP_P 2
#define SLP_N POLL_SLEEP
template <int SLP, int WPL>
__device__ __forceinline__ bool wait_n(const unsigned* f, int n, unsigned* tmo) {
  const int l = lane_id();
  for (unsigned spins = 0;; ++spins) {
    unsigned v = 1u;
    if (l < n) v = __hip_atomic_load((gu32*)f + (l / WPL) * 32 + l % WPL, RLX_AGENT);
    if (__all(v != 0u)) break;
    if (spin_timeout(spins, tmo)) return false;
    __builtin_amdgcn_s_sleep(SLP);
  }
  asm volatile("" ::: "memory");
  return true;
}
__device__ __forceinline__ unsigned now10() { return (unsigned)__builtin_amdgcn_s_memrealtime(); }
__device__ __forceinline__ void gate(const unsigned t) {
  for (int i = 0; i < 64; ++i) {
    const int d = (int)(t - now10());
    if (d <= 0) break;
    if (d > 100) __builtin_amdgcn_s_sleep(32);
    else if (d > 25) __builtin_amdgcn_s_sleep(8);
    else __builtin_amdgcn_s_sleep(2);
  }
}
template <int SLP, int WPL>
__device__ __forceinline__ unsigned grp_wait(const bool leader, volatile lu32* sw, const unsigned val, const unsigned* f, int n,
                                             unsigned* tmo, const unsigned t) {
  unsigned r;
  gate(t);
  if (leader) {
    const int l = lane_id();
    unsigned v;
    for (unsigned spins = 0;; ++spins) {
      v = __hip_atomic_load((gu32*)f + ((l & 15) / WPL) * 32 + l % WPL, RLX_AGENT);
      if (__all(v != 0u) || spin_timeout(spins, tmo)) break;
      __builtin_amdgcn_s_sleep(SLP);
    }
    int d = (int)(v - t);
    d = max(d, __builtin_amdgcn_update_dpp(0, d, 0x121, 0xf, 0xf, false));
    d = max(d, __builtin_amdgcn_update_dpp(0, d, 0x122, 0xf, 0xf, false));
    d = max(d, __builtin_amdgcn_update_dpp(0, d, 0x124, 0xf, 0xf, false));
    d = max(d, __builtin_amdgcn_update_dpp(0, d, 0x128, 0xf, 0xf, false));
    r = t + __builtin_amdgcn_readfirstlane(d);
    sw[1 + (val & 2)] = r;
    *sw = val;
  } else {
    for (unsigned spins = 0; __builtin_amdgcn_readfirstlane(*sw) < val && spins <= 64u * SPIN_CAP; ++spins) __builtin_amdgcn_s_sleep(SLP_F);
    r = __builtin_amdgcn_readfirstlane(sw[1 + (val & 2)]);
  }
  asm volatile("" ::: "memory");
  for (int i = 0; i < 256; ++i) {
    const int d = (int)(r - now10());
    if (d <= 0) break;
    if (d > 8) __builtin_amdgcn_s_sleep(1);
  }
  return r;
}
template <int SLP, int WPL>
__device__ __forceinline__ bool wg_wait(const unsigned* f, int n, unsigned* tmo) {
  if (threadIdx.x < 64) (void)wait_n<SLP, WPL>(f, n, tmo);
  __syncthreads();
  asm volatile("" ::: "memory");
  return true;
}
__device__ __forceinline__ void set_flag(unsigned* f) { __hip_atomic_store((gu32*)f, 1u, RLX_AGENT); }
#define DRAIN() asm volatile("s_waitcnt vmcnt(0)" ::: "memory")
#define MKRS(ptr, bytes) __builtin_amdgcn_make_buffer_rsrc((void*)(ptr), 0, (int)(bytes), 0x00020000)
typedef unsigned v2u __attribute__((ext_vector_type(2)));
#define LD128(rs, voff, soff) __builtin_amdgcn_raw_buffer_load_b128(rs, voff, soff, AUX_SC1)
#define LD64(rs, voff, soff) __builtin_amdgcn_raw_buffer_load_b64(rs, voff, soff, AUX_SC1)
#define ST128(v, rs, voff, soff)                                              \
  do {                                                                        \
    const v4u st_d_ = (v);                                                    \
    __builtin_amdgcn_raw_buffer_store_b128(st_d_, rs, voff, soff, AUX_SC1);   \
    asm volatile("s_nop 1" ::"v"(st_d_) : "memory");                          \
  } while (0)
#define ST64(v, rs, voff, soff) __builtin_amdgcn_raw_buffer_store_b64(v, rs, voff, soff, AUX_SC1)
#define BF8(x) __builtin_bit_cast(bf16x8, x)
#define MFMA16(a, b, c) __builtin_amdgcn_mfma_f32_16x16x32_bf16(a, b, c, 0, 0, 0)
typedef short bf16x4 __attribute__((ext_vector_type(4)));
#define MFMA16K(a, b, c) \
  __builtin_amdgcn_mfma_f32_16x16x16bf16_1k(__builtin_bit_cast(bf16x4, a), __builtin_bit_cast(bf16x4, b), c, 0, 0, 0)
__device__ __forceinline__ float fmax_nn(float a, float b) { return __builtin_elementwise_maximum(a, b); }
typedef __bf16 bfv8 __attribute__((ext_vector_type(8)));
typedef __bf16 bfv16 __attribute__((ext_vector_type(16)));
typedef unsigned v8u __attribute__((ext_vector_type(8)));
#define SMF(a, b, c, idx) \
  __builtin_amdgcn_smfmac_f32_16x16x64_bf16(__builtin_bit_cast(bfv8, a), __builtin_bit_cast(bfv16, b), c, idx, 0, 0)
__device__ __forceinline__ float swap_add32(float a, float b) {
  auto r = __builtin_amdgcn_permlane32_swap(__float_as_uint(a), __float_as_uint(b), false, false);
  return __uint_as_float(r[0]) + __uint_as_float(r[1]);
}
__device__ __forceinline__ float swap_add16(float a, float b) {
  auto r = __builtin_amdgcn_permlane16_swap(__float_as_uint(a), __float_as_uint(b), false, false);
  return __uint_as_float(r[0]) + __uint_as_float(r[1]);
}
__device__ __forceinline__ v4u smf_fix(const v4u d) {
  auto x = __builtin_amdgcn_permlane16_swap(d.x, d.z, false, false);
  auto y = __builtin_amdgcn_permlane16_swap(d.y, d.w, false, false);
  return v4u{x[0], x[1], y[0], y[1]};
}
__device__ __forceinline__ v8u smf_bfrag(const u16* p) {
  const v2u a = *reinterpret_cast<const v2u*>(p), b = *reinterpret_cast<const v2u*>(p + 8);
  const v2u c = *reinterpret_cast<const v2u*>(p + 32), d = *reinterpret_cast<const v2u*>(p + 40);
  return v8u{a.x, b.x, a.y, b.y, c.x, d.x, c.y, d.y};
}

#define VAR_PPA 0
#define VAR_PPB 0
#define VAR_THR 0
#define VAR_THR2 0
#define CV_LEAD 100
#define VAR_EARLY 1
#define VAR_DRAIN 1
#define VAR_D1 0
#define VAR_D2 0
#define VAR_DR2 1
#define VAR_S1 0
#define VAR_S2 0
typedef __attribute__((address_space(1))) unsigned long long gu64;
typedef __attribute__((address_space(1))) char gch;
__device__ __forceinline__ int cvt_rpi(float x) {
  int r;
  asm("v_cvt_rpi_i32_f32 %0, %1" : "=v"(r) : "v"(x));
  return r;
}
typedef short v4s __attribute__((ext_vector_type(4)));
typedef __attribute__((address_space(3))) v4s* lp4s;
__device__ __forceinline__ void glds16(const void* sbase, const unsigned voff, const unsigned lds) {
  unsigned keep;
  asm volatile("s_nop 4\n\ts_mov_b32 %0, m0\n\ts_mov_b32 m0, %3\n\ts_nop 0\n\tglobal_load_lds_dwordx4 %1, %2\n\ts_mov_b32 m0, %0"
               : "=&s"(keep)
               : "v"(voff), "s"(sbase), "s"(lds)
               : "memory");
}
__device__ __forceinline__ void glds16c(const void* sbase, const unsigned voff, const unsigned lds) {
  unsigned keep;
  asm volatile("s_nop 4\n\ts_mov_b32 %0, m0\n\ts_mov_b32 m0, %3\n\ts_nop 0\n\tglobal_load_lds_dwordx4 %1, %2 sc1\n\ts_mov_b32 m0, %0"
               : "=&s"(keep)
               : "v"(voff), "s"(sbase), "s"(lds)
               : "memory");
}
__device__ __forceinline__ v2u trd(const unsigned a) {
  return __builtin_bit_cast(v2u, __builtin_amdgcn_ds_read_tr16_b64_v4i16((lp4s)(unsigned long)a));
}
__device__ __forceinline__ unsigned lds_base(char* smem) { return (unsigned)(unsigned long)(__attribute__((address_space(3))) char*)smem; }
#define RAW_BAR()                                           \
  do {                                                      \
    asm volatile("s_waitcnt lgkmcnt(0)" ::: "memory");      \
    __builtin_amdgcn_s_barrier();                           \
    asm volatile("" ::: "memory");                          \
  } while (0)
#define WAIT_VM(n) asm volatile("s_waitcnt vmcnt(" #n ")" ::: "memory")
template <bool SP>
__device__ __forceinline__ void wqkv_issue(const u16* Wm, const int hf, const int h, const int w, const int l, const unsigned sb) {
  const char* base = (const char*)Wm + (size_t)(256 * hf + 32 * w) * (HD * 2) + h * (DH * 2);
#pragma unroll
  for (int i = 0; i < 8; ++i) {
    const int r = l >> 4;
    const int f = SP ? ((r & 1) | ((i & 1) << 1) | (((i >> 1) & 1) << 2)) : (r | (((i >> 1) & 1) << 2));
    glds16(base, (unsigned)((4 * i + r) * (HD * 2) + (((l & 15) ^ (f << 1)) << 4)), sb + w * 16384 + hf * 8192 + i * 1024);
  }
}

#define PFW 1
__device__ __forceinline__ void pull_weights(const MegaArgs& A, char* smem, const int slot, const int slots) {
  if (!PFW) return;
  const int x = blockIdx.x & 7, j = blockIdx.x >> 3, h = 8 * (x >> 1) + (j & 7), k = (j >> 3) - 1, l = lane_id();
  const bool odd = x & 1, wo = !odd && k == 1;
  const unsigned voff = wo ? l * 64 : (l >> 2) * (HD * 2) + (l & 3) * 64;
  const unsigned dst = lds_base(smem) + 162816;
  unsigned long long pk = (unsigned long long)A.Wk, pq = (unsigned long long)A.Wq, pv = (unsigned long long)A.Wv, po = (unsigned long long)A.Wo;
  asm volatile("" : "+s"(pk), "+s"(pq), "+s"(pv), "+s"(po));
  for (int i = odd ? slot * 3 + k : slot; (PFW & 1) && i < (odd ? 64 : 32); i += odd ? 3 * slots : slots) {
    const unsigned long long m = odd ? (i < 32 ? pk : pv) : (k == 0 ? pq : pk);
    const unsigned long long b = wo ? po + h * (DH * DM * 2) + i * 4096 : m + (i & 31) * (16 * HD * 2) + h * (DH * 2);
    unsigned keep;
    asm volatile("s_nop 4\n\ts_mov_b32 %0, m0\n\ts_mov_b32 m0, %3\n\ts_nop 0\n\tglobal_load_lds_dword %1, %2\n\ts_mov_b32 m0, %0"
                 : "=&s"(keep)
                 : "v"(voff), "s"(b), "s"(dst)
                 : "memory");
  }
  if (PFW & 2) {
    const unsigned long long b = po + (size_t)h * (DH * DM * 2);
    for (int i = slot * 3 + k; i < 32; i += 3 * slots) {
      unsigned keep;
      asm volatile("s_nop 4\n\ts_mov_b32 %0, m0\n\ts_mov_b32 m0, %3\n\ts_nop 0\n\tglobal_load_lds_dword %1, %2\n\ts_mov_b32 m0, %0"
                   : "=&s"(keep)
                   : "v"(l * 64), "s"(b + i * 4096), "s"(dst)
                   : "memory");
    }
  }
}
__device__ __forceinline__ void wo_issue(const u16* Wo, const int a, const int st, const unsigned sb) {
  const int w = __builtin_amdgcn_readfirstlane(threadIdx.x >> 6), l = lane_id(), c = l & 15, g = l >> 4;
  const char* base = (const char*)Wo + ((size_t)((2 * a + (st >> 1)) * DH + 64 * (w & 1) + 32 * (st & 1)) * DM + 128 * (w >> 1)) * 2;
#pragma unroll
  for (int i = 0; i < 8; ++i) {
    const int fd = g | ((i & 1) << 2);
    glds16(base, (unsigned)((4 * i + g) * (DM * 2) + ((c ^ (fd << 1)) << 4)), sb + (st & 1) * 65536 + w * 8192 + i * 1024);
  }
}
__device__ __forceinline__ bool zero_seen(const v4u t, const v2u e) {
  const unsigned long long m = (((unsigned long long)e.y << 32) | e.x) + TAG_STEP;
  const unsigned lo = (unsigned)m, hi = (unsigned)(m >> 32);
  return __all(((t.x ^ lo) | (t.y ^ hi) | (t.z ^ lo) | (t.w ^ hi)) == 0u);
}
__device__ __forceinline__ void zero_wait(char* ws) {
  const __amdgpu_buffer_rsrc_t rs = MKRS(ws + OFF_TAG, 8192 + 8);
  for (unsigned spins = 0; spins < 16u * SPIN_CAP; ++spins) {
    const v4u t = LD128(rs, lane_id() * 16, (blockIdx.x & 7) * 1024);
    const v2u e = LD64(rs, 0, 8192);
    if (zero_seen(t, e)) break;
    asm volatile("s_sleep 4" ::: "memory");
  }
  asm volatile("" ::: "memory");
}
__device__ __forceinline__ unsigned row_stats(const v4u raw) {
  float v[8];
#pragma unroll
  for (int e = 0; e < 4; ++e) { v[2 * e] = lo16(raw[e]); v[2 * e + 1] = hi16(raw[e]); }
  float s = 0.f;
#pragma unroll
  for (int e = 0; e < 8; ++e) s += v[e];
  const float mean = wave_sum(s) * (1.0f / DM);
  float q = 0.f;
#pragma unroll
  for (int e = 0; e < 8; ++e) { const float t = v[e] - mean; q += t * t; }
  const float var = wave_sum(q) * (1.0f / DM);
  const float sd = bfr(sqrtf(bfr(bfr(var) + 1e-5f)));
  return (unsigned)f2bf(mean) | ((unsigned)f2bf(sd) << 16);
}
__device__ __forceinline__ void start_kgen(const MegaArgs& A, char* smem, const int hi, const int xi, const int aw) {
  const int tid = threadIdx.x, j = tid >> 7, u = tid & 127, d = 4 * hi + j;
  const int w = __builtin_amdgcn_readfirstlane(tid >> 6), l = tid & 63;
  float* prm = reinterpret_cast<float*>(smem) + j * 10 * NST;
  float* part = reinterpret_cast<float*>(smem) + 40 * NST + j * 8 * 400;
  unsigned* kd = reinterpret_cast<unsigned*>(smem + 131072) + j * 392;
  v4u raw[8], ra, rb;
  const int r0 = 16 * xi + 2 * w;
  if (xi >= 0) {
    ra = *reinterpret_cast<const v4u*>(A.x0 + (size_t)r0 * DM + l * 8);
    rb = *reinterpret_cast<const v4u*>(A.x0 + (size_t)(r0 + 1) * DM + l * 8);
  }
  const u16 p0 = A.logdt[d], p1 = A.logAre[d * NST + u], p2 = A.Aim[d * NST + u], p3 = A.Cre[d * NST + u], p4 = A.Cim[d * NST + u];
  if (w >= 6) {
#pragma unroll
    for (int i = 0; i < 8; ++i) raw[i] = *reinterpret_cast<const v4u*>(A.x0 + (size_t)(16 * hi + 8 * (w - 6) + i) * DM + l * 8);
  }
  const v2u en = LD64(MKRS(A.ws + OFF_TAG, 8192 + 8), 0, 8192);
  {
    const __amdgpu_buffer_rsrc_t rz = MKRS(A.ws + OFF_Z, Z_BYTES);
    const unsigned i0 = hi < 16 ? ACC_LO : 0u, i1 = hi < 16 ? ACC_HI : 0u;
    ST128((v4u{i0, i1, i0, i1}), rz, tid * 16, hi * 8192);
    ST128((v4u{0u, 0u, 0u, 0u}), rz, tid * 16, (128 + hi) * 8192);
  }
  if (xi >= 0) {
    const unsigned sa = row_stats(ra), sb = row_stats(rb), st = (l & 1) ? sb : sa;
    const unsigned long long m = (((unsigned long long)en.y << 32) | en.x) + TAG_STEP;
    ST128((v4u{st, (unsigned)m, st, (unsigned)(m >> 32)}), MKRS(A.ws + OFF_G0, 8192), (l >> 1) * 2048 + (l & 1) * 16, r0 * 16);
    SST(11);
  }
  {
    const float dt = bfr(expf(bf2f(p0)));
    const float ar = -bfr(expf(bf2f(p1))) * dt, ai = bf2f(p2) * dt;
    prm[u] = ar;
    prm[NST + u] = ai;
    prm[2 * NST + u] = bf2f(p3) * dt;
    prm[3 * NST + u] = bf2f(p4) * dt;
#pragma unroll
    for (int cls = 1; cls < 4; ++cls) {
      const float st = (float)(1 << cls);
      const float e = expf(ar * st);
      float sn, cs;
      sincosf(ai * st, &sn, &cs);
      prm[(2 + 2 * cls) * NST + u] = e * cs;
      prm[(3 + 2 * cls) * NST + u] = e * sn;
    }
  }
  __builtin_amdgcn_s_waitcnt(0x0F70);
  __syncthreads();
  SST(1);
  if (w >= 6) {
    if (w == 7 && l < 8)
      __hip_atomic_store((gu64*)(A.ws + OFF_TAG) + l * 128 + hi, (((unsigned long long)en.y << 32) | en.x) + TAG_STEP, RLX_AGENT);
    __builtin_amdgcn_s_setprio(3);
    if (hi >= 32) pull_weights(A, smem, w - 6, 2);
    v4u wr[32];
    const u16* wop = A.Wo + (size_t)(hi * DH + 64 * (w - 6)) * DM + l * 8;
    if (hi < 32) {
#pragma unroll
      for (int i = 0; i < 32; ++i) wr[i] = *reinterpret_cast<const v4u*>(wop + i * DM);
    }
    unsigned stw = 0u;
#pragma unroll
    for (int i = 0; i < 8; ++i) {
      const unsigned t = row_stats(raw[i]);
      if (l == i) stw = t;
    }
    const int sx = 2 * hi + w - 6;
    if (l < 8) __hip_atomic_store((gu32*)(A.ws + OFF_GT) + 8 * sx + l, stw, RLX_AGENT);
    zero_wait(A.ws);
    DRAIN();
    if (l < 4) set_flag((unsigned*)(A.ws + OFF_FL) + FL_FS + (l * 32 + (sx >> 3)) * 32 + (sx & 7));
    if (hi < 32) {
      float c0 = 0.f, c1 = 0.f, c2 = 0.f, c3 = 0.f;
#pragma unroll
      for (int i = 0; i < 64; ++i) {
        const v4u x = wr[i & 31];
        if (i < 32) wr[i] = *reinterpret_cast<const v4u*>(wop + (32 + i) * DM);
        c0 = dot2sq(x.x, c0); c1 = dot2sq(x.y, c1); c2 = dot2sq(x.z, c2); c3 = dot2sq(x.w, c3);
      }
      const float m = wave_max_dpp(fmaxf(fmaxf(c0, c1), fmaxf(c2, c3)));
      if (l < 4) __hip_atomic_store((gu32*)(A.ws + OFF_FL) + FL_G + (l * 4 + w - 5) * 32 + hi, __float_as_uint(m) | 1u, RLX_AGENT);
    }
    __builtin_amdgcn_s_setprio(0);
  } else {
    const int jm = tid / 96, um = tid % 96;
    const int r = um % 12, ng = um / 12;
    const float* prm = reinterpret_cast<float*>(smem) + jm * 10 * NST;
    float* part = reinterpret_cast<float*>(smem) + 40 * NST + jm * 8 * 400;
    const float* pw = prm + (4 + 2 * (r >> 2)) * NST;
    const float p0 = pos_of_index(256 + 32 * r);
    f32x2_t acc[33];
#pragma unroll
    for (int i = 0; i < 33; ++i) acc[i] = f32x2_t{0.f, 0.f};
#pragma unroll 1
    for (int bt = 0; bt < 4; ++bt) {
      const int n = 16 * ng + 4 * bt;
      const f32x4 ar = *reinterpret_cast<const f32x4*>(prm + n), ai = *reinterpret_cast<const f32x4*>(prm + NST + n);
      const f32x4 cr = *reinterpret_cast<const f32x4*>(prm + 2 * NST + n), ci = *reinterpret_cast<const f32x4*>(prm + 3 * NST + n);
      const f32x4 wr = *reinterpret_cast<const f32x4*>(pw + n), wi = *reinterpret_cast<const f32x4*>(pw + NST + n);
      float yr0[4], yi0[4];
#pragma unroll
      for (int k = 0; k < 4; ++k) {
        const float e = __expf(ar[k] * p0);
        float sn, cs;
        __sincosf(ai[k] * p0, &sn, &cs);
        const float zr = e * cs, zi = e * sn;
        yr0[k] = cr[k] * zr - ci[k] * zi;
        yi0[k] = cr[k] * zi + ci[k] * zr;
      }
      f32x2_t yra = {yr0[0], yr0[1]}, yia = {yi0[0], yi0[1]}, wra = {wr[0], wr[1]}, wia = {wi[0], wi[1]};
      f32x2_t yrb = {yr0[2], yr0[3]}, yib = {yi0[2], yi0[3]}, wrb = {wr[2], wr[3]}, wib = {wi[2], wi[3]};
#pragma unroll
      for (int i = 0; i < 33; ++i) {
        acc[i] += yra + yrb;
        const f32x2_t ta = yra * wra - yia * wia, tb = yrb * wrb - yib * wib;
        yia = yra * wia + yia * wra;
        yib = yrb * wib + yib * wrb;
        yra = ta;
        yrb = tb;
      }
    }
#pragma unroll
    for (int i = 0; i < 33; ++i) part[ng * 400 + 33 * r + i] = acc[i].x + acc[i].y;
  }
  __syncthreads();
  SST(2);
  for (int i = u; i < 385; i += 128) {
    const int o = i < 384 ? 33 * (i >> 5) + (i & 31) : 33 * 11 + 32;
    float v = 0.f;
#pragma unroll
    for (int g = 0; g < 8; ++g) v += part[g * 400 + o];
    v *= 2.0f;
    kd[i] = pk2(v, v - lo16(pk2(v, v)));
  }
  if (u == 1) kd[385] = 0u;
  __syncthreads();
  if (aw >= 0) {
    wo_issue(A.Wo, aw, 0, lds_base(smem));
    wo_issue(A.Wo, aw, 1, lds_base(smem));
  }
  const __amdgpu_buffer_rsrc_t rs = MKRS(A.ws + OFF_KF, DM * 2 * CKN * 2);
  for (int un = u; un < 229; un += 128) {
    v4u oh, ol;
#pragma unroll
    for (int e = 0; e < 4; ++e) {
      const int lag = 2080 - (8 * un + 2 * e);
      const unsigned pb = pk2((float)lag, (float)(lag - 1));
      const unsigned k0 = kd[(unsigned)(lag - 256) < L_SEQ - 256u ? (pb & 0xffffu) - 0x4380u : 385u];
      const unsigned k1 = kd[(unsigned)(lag - 257) < L_SEQ - 256u ? (pb >> 16) - 0x4380u : 385u];
      oh[e] = __builtin_amdgcn_perm(k1, k0, 0x05040100u);
      ol[e] = __builtin_amdgcn_perm(k1, k0, 0x07060302u);
    }
    ST128(oh, rs, (d * 2 * CKN + 8 * un) * 2, 0);
    ST128(ol, rs, (d * 2 * CKN + CKN + 8 * un) * 2, 0);
  }
  DRAIN();
  __syncthreads();
  if (tid == 0) set_flag((unsigned*)(A.ws + OFF_FL) + FL_KG + (hi >> 1) * 32 + (hi & 1));
  SST(3);
}

constexpr int C_SM = 131072, C_SO = C_SM + 8192, C_KT = C_SO + 2048, C_X = C_KT + 16384, C_SQ = C_X + 1024;
constexpr int C_SK = C_SQ + 2432;
static_assert(C_SK + 8 * QS * 2 <= 163808, "lds");
#define CHAIN_ASM_ATT \
  "s_mov_b32 s66, %[d0]\n" \
  "s_mov_b32 s94, 576\n" \
  "s_mov_b32 s95, 288\n" \
  "s_mov_b32 s84, 0x43400000\n" \
  "s_mov_b32 s85, 0x43400000\n" \
  "s_mov_b32 s92, 0x88000000\n" \
  "s_mov_b32 s93, 0x87fffff7\n" \
  "s_mov_b32 s78, 0x3e0293ee\n" \
  "s_mov_b32 s76, 0x33333333\n" \
  "s_mov_b32 s77, 0x33333333\n" \
  "s_add_u32 s58, s56, s60\n" \
  "s_addc_u32 s59, s57, 0\n" \
  "s_xor_b32 s38, s35, 0\n" \
  "s_add_u32 s26, s58, s38\n" \
  "s_addc_u32 s27, s59, 0\n" \
  "s_xor_b32 s38, s35, 1024\n" \
  "s_add_u32 s28, s58, s38\n" \
  "s_addc_u32 s29, s59, 0\n" \
  "s_xor_b32 s38, s35, 2048\n" \
  "s_add_u32 s30, s58, s38\n" \
  "s_addc_u32 s31, s59, 0\n" \
  "s_xor_b32 s38, s35, 3072\n" \
  "s_add_u32 s32, s58, s38\n" \
  "s_addc_u32 s33, s59, 0\n" \
  "s_lshr_b32 s98, s60, 2\n" \
  "s_add_u32 s98, s98, s74\n" \
  "s_add_u32 s98, s98, 131072\n" \
  "s_lshl_b32 s97, s73, 5\n" \
  "s_add_u32 s97, s97, s74\n" \
  "s_add_u32 s97, s97, 161152\n" \
  "s_lshl_b32 s99, s73, 10\n" \
  "v_mbcnt_lo_u32_b32 v48, -1, 0\n" \
  "v_mbcnt_hi_u32_b32 v48, -1, v48\n" \
  "v_and_b32 v49, 15, v48\n" \
  "v_lshl_add_u32 v9, v49, 1, s97\n" \
  "v_lshrrev_b32 v49, 4, v48\n" \
  "v_mad_u32_u24 v9, v49, s94, v9\n" \
  "v_add_u32 v7, s99, v1\n" \
  "v_mov_b64 v[12:13], 0\n" \
  "v_mov_b64 v[14:15], 0\n" \
  "v_mov_b64 v[16:17], 0\n" \
  "v_mov_b64 v[18:19], 0\n" \
  "v_mov_b64 v[20:21], 0\n" \
  "v_mov_b64 v[22:23], 0\n" \
  "v_mov_b32 v24, 0x44870000\n" \
  "v_mul_f32 v24, s72, v24\n" \
  "s_nop 0\n" \
  "v_readfirstlane_b32 s86, v24\n" \
  "s_lshr_b32 s86, s86, 23\n" \
  "s_addk_i32 s86, 0xff83\n" \
  "s_ashr_i32 s86, s86, 1\n" \
  "s_max_i32 s86, s86, -20\n" \
  "s_min_i32 s86, s86, 100\n" \
  "s_mov_b32 s82, s86\n" \
  "s_lshr_b32 s83, s72, 23\n" \
  "s_sub_i32 s83, s83, 242\n" \
  "s_lshl_b32 s88, s73, 10\n" \
  "v_add_u32 v24, s88, v1\n" \
  "ds_read_b128 v[32:35], v24 offset:0\n" \
  "s_waitcnt lgkmcnt(0)\n" \
  "v_mov_b32 v45, 0\n" \
  "v_dot2c_f32_bf16 v45, v32, v32\n" \
  "v_dot2c_f32_bf16 v45, v33, v33\n" \
  "v_dot2c_f32_bf16 v45, v34, v34\n" \
  "v_dot2c_f32_bf16 v45, v35, v35\n" \
  "s_add_u32 s88, s74, 157760\n" \
  "v_mov_b32 v44, s88\n" \
  "s_nop 0\n" \
  "v_cmpx_lt_f32 vcc, 2.0, v45\n" \
  "ds_max_u32 v44, v45\n" \
  "s_mov_b64 exec, -1\n" \
  ".p2align 6\n" \
  ".LCA_AL_%=:\n" \
  ".LCA_TOP_%=:\n" \
  "s_waitcnt lgkmcnt(0)\n" \
  "s_barrier\n" \
  "ds_read_b32 v8, v1 offset:26688\n" \
  "s_cmp_lg_u32 s73, 3\n" \
  "s_cbranch_scc1 .LCA_NOTK_%=\n" \
  "s_memrealtime s[38:39]\n" \
  "v_mov_b32 v26, 1\n" \
  "s_mul_i32 s23, s71, %[pp]\n" \
  "s_lshr_b32 s62, s66, 4\n" \
  "s_mov_b64 exec, 1\n" \
  "s_waitcnt lgkmcnt(0)\n" \
  "s_mov_b32 s67, s38\n" \
  "s_add_u32 s38, s38, s62\n" \
  "v_mov_b32 v25, s38\n" \
  "buffer_atomic_umax v25, off, s[16:19], s68\n" \
  "buffer_atomic_add v26, off, s[16:19], s68 offset:64\n" \
  "v_add_u32 v27, s23, v25\n" \
  "buffer_store_dword v27, off, s[16:19], s69 sc1\n" \
  "s_mov_b64 exec, -1\n" \
  "s_branch .LCA_SIDE_%=\n" \
  ".LCA_NOTK_%=:\n" \
  "s_cmp_lg_u32 s73, 0\n" \
  "s_cbranch_scc1 .LCA_NOZ_%=\n" \
  "v_mov_b64 v[26:27], s[92:93]\n" \
  "s_xor_b32 s88, s60, 0x8000\n" \
  "s_add_u32 s88, s88, s61\n" \
  "buffer_store_dwordx2 v[26:27], v5, s[40:43], s88 offen sc1\n" \
  ".LCA_NOZ_%=:\n" \
  "s_nop 7\n" \
  ".LCA_SIDE_%=:\n" \
  "s_waitcnt lgkmcnt(0)\n" \
  "v_readfirstlane_b32 s86, v8\n" \
  "s_mov_b64 vcc, s[76:77]\n" \
  "s_lshr_b32 s86, s86, 23\n" \
  "s_add_i32 s86, s86, s83\n" \
  "s_ashr_i32 s86, s86, 1\n" \
  "s_max_i32 s86, s86, s82\n" \
  "s_min_i32 s86, s86, 100\n" \
  "s_lshl_b32 s86, s86, 23\n" \
  "s_add_i32 s75, s86, 0x3c800000\n" \
  "s_cmp_eq_u32 s71, 0\n" \
  "ds_read_b128 v[32:35], v1 offset:0\n" \
  "ds_read_b128 v[36:39], v1 offset:1024\n" \
  "ds_read_b128 v[48:51], v1 offset:2048\n" \
  "ds_read_b128 v[52:55], v1 offset:3072\n" \
  "ds_read_b128 v[40:43], v1 offset:4096\n" \
  "ds_read_b128 v[56:59], v1 offset:5120\n" \
  "ds_read_b128 v[60:63], v1 offset:6144\n" \
  "ds_read_b128 v[44:47], v1 offset:7168\n" \
  "s_setprio 3\n" \
  "s_waitcnt lgkmcnt(7)\n" \
  "v_smfmac_f32_16x16x64_bf16 v[12:15], v[32:35], v[64:71], v0\n" \
  "s_waitcnt lgkmcnt(6)\n" \
  "v_smfmac_f32_16x16x64_bf16 v[12:15], v[36:39], v[72:79], v0\n" \
  "s_waitcnt lgkmcnt(5)\n" \
  "v_smfmac_f32_16x16x64_bf16 v[12:15], v[48:51], v[80:87], v0\n" \
  "s_waitcnt lgkmcnt(4)\n" \
  "v_smfmac_f32_16x16x64_bf16 v[12:15], v[52:55], v[88:95], v0\n" \
  "s_waitcnt lgkmcnt(3)\n" \
  "v_smfmac_f32_16x16x64_bf16 v[12:15], v[40:43], v[96:103], v0\n" \
  "s_waitcnt lgkmcnt(2)\n" \
  "v_smfmac_f32_16x16x64_bf16 v[12:15], v[56:59], v[104:111], v0\n" \
  "s_waitcnt lgkmcnt(1)\n" \
  "v_smfmac_f32_16x16x64_bf16 v[12:15], v[60:63], v[112:119], v0\n" \
  "s_waitcnt lgkmcnt(0)\n" \
  "v_smfmac_f32_16x16x64_bf16 v[12:15], v[44:47], v[120:127], v0\n" \
  "ds_read_b128 v[24:27], v1 offset:10240\n" \
  "ds_read_b128 v[28:31], v1 offset:14336\n" \
  "s_nop 5\n" \
  "v_add_f32 v12, v12, v14\n" \
  "v_add_f32 v13, v13, v15\n" \
  "v_cvt_pk_bf16_f32 v12, v12, v13\n" \
  "ds_write_b16 v9, v12 offset:0\n" \
  "ds_write_b16_d16_hi v9, v12 offset:288\n" \
  "s_setprio 0\n" \
  "s_barrier\n" \
  "v_smfmac_f32_16x16x64_bf16 v[16:19], v[32:35], v[128:135], v0\n" \
  "v_smfmac_f32_16x16x64_bf16 v[20:23], v[32:35], v[192:199], v0\n" \
  "ds_read_b128 v[32:35], v1 offset:11264\n" \
  "v_smfmac_f32_16x16x64_bf16 v[16:19], v[36:39], v[136:143], v0\n" \
  "v_smfmac_f32_16x16x64_bf16 v[20:23], v[36:39], v[200:207], v0\n" \
  "ds_read_b128 v[36:39], v1 offset:15360\n" \
  "v_smfmac_f32_16x16x64_bf16 v[16:19], v[48:51], v[144:151], v0\n" \
  "v_smfmac_f32_16x16x64_bf16 v[20:23], v[48:51], v[208:215], v0\n" \
  "v_smfmac_f32_16x16x64_bf16 v[16:19], v[52:55], v[152:159], v0\n" \
  "v_smfmac_f32_16x16x64_bf16 v[20:23], v[52:55], v[216:223], v0\n" \
  "v_smfmac_f32_16x16x64_bf16 v[16:19], v[40:43], v[160:167], v0\n" \
  "v_smfmac_f32_16x16x64_bf16 v[20:23], v[40:43], v[224:231], v0\n" \
  ".fill ((16 - (. - .LCA_AL_%=)) & 31) >> 2, 4, 0xbf800000\n" \
  "s_waitcnt lgkmcnt(2)\n" \
  "s_barrier\n" \
  "s_setprio 3\n" \
  "ds_read_b128 v[48:51], v2 offset:0\n" \
  "ds_read_b128 v[52:55], v2 offset:64\n" \
  "v_smfmac_f32_16x16x64_bf16 v[16:19], v[56:59], v[168:175], v0\n" \
  "v_smfmac_f32_16x16x64_bf16 v[20:23], v[56:59], v[232:239], v0\n" \
  "ds_read_b128 v[56:59], v2 offset:128\n" \
  "s_cbranch_scc1 .LCA_LAST_%=\n" \
  "s_waitcnt lgkmcnt(2)\n" \
  "v_mfma_f32_16x16x32_bf16 v[12:15], v[24:27], v[48:51], 0\n" \
  "ds_read_b128 v[24:27], v1 offset:12288\n" \
  "v_mfma_f32_16x16x32_bf16 v[8:11], v[28:31], v[48:51], 0\n" \
  "ds_read_b128 v[28:31], v1 offset:16384\n" \
  "v_mfma_f32_16x16x32_bf16 v[40:43], v[48:51], v[48:51], 0\n" \
  "ds_read_b128 v[48:51], v7 offset:18432\n" \
  "v_smfmac_f32_16x16x64_bf16 v[16:19], v[60:63], v[176:183], v0\n" \
  "v_smfmac_f32_16x16x64_bf16 v[20:23], v[60:63], v[240:247], v0\n" \
  "ds_read_b128 v[60:63], v2 offset:192\n" \
  "s_waitcnt lgkmcnt(5)\n" \
  "v_mfma_f32_16x16x32_bf16 v[12:15], v[32:35], v[52:55], v[12:15]\n" \
  "ds_read_b128 v[32:35], v1 offset:13312\n" \
  "v_mfma_f32_16x16x32_bf16 v[8:11], v[36:39], v[52:55], v[8:11]\n" \
  "ds_read_b128 v[36:39], v1 offset:17408\n" \
  "v_mfma_f32_16x16x32_bf16 v[40:43], v[52:55], v[52:55], v[40:43]\n" \
  "ds_read_b128 v[52:55], v7 offset:22528\n" \
  "s_waitcnt lgkmcnt(7)\n" \
  "v_mfma_f32_16x16x32_bf16 v[40:43], v[56:59], v[56:59], v[40:43]\n" \
  "s_waitcnt lgkmcnt(5)\n" \
  "v_mfma_f32_16x16x32_bf16 v[12:15], v[24:27], v[56:59], v[12:15]\n" \
  "ds_read_b128 v[24:27], v6 offset:2048\n" \
  "v_mfma_f32_16x16x32_bf16 v[8:11], v[28:31], v[56:59], v[8:11]\n" \
  "ds_read_b128 v[28:31], v6 offset:3072\n" \
  "s_waitcnt lgkmcnt(5)\n" \
  "v_mfma_f32_16x16x32_bf16 v[40:43], v[60:63], v[60:63], v[40:43]\n" \
  "ds_read_b128 v[56:59], v6 offset:0\n" \
  "s_waitcnt lgkmcnt(4)\n" \
  "v_mfma_f32_16x16x32_bf16 v[12:15], v[32:35], v[60:63], v[12:15]\n" \
  "ds_read_b128 v[32:35], v6 offset:8192\n" \
  "v_mfma_f32_16x16x32_bf16 v[8:11], v[36:39], v[60:63], v[8:11]\n" \
  "ds_read_b128 v[36:39], v6 offset:9216\n" \
  "v_smfmac_f32_16x16x64_bf16 v[16:19], v[44:47], v[184:191], v0\n" \
  "ds_read_b128 v[60:63], v6 offset:1024\n" \
  "s_waitcnt lgkmcnt(6)\n" \
  "v_smfmac_f32_16x16x64_bf16 v[20:23], v[44:47], v[248:255], v0\n" \
  "v_cndmask_b32_dpp v43, v43, v42, vcc quad_perm:[0,1,0,1] row_mask:0xf bank_mask:0xf\n" \
  "v_mov_b32 v41, 0\n" \
  "v_mov_b32 v42, 0\n" \
  "v_cndmask_b32_dpp v12, v8, v12, vcc quad_perm:[0,1,0,1] row_mask:0xf bank_mask:0xf\n" \
  "v_cndmask_b32_dpp v13, v9, v13, vcc quad_perm:[0,1,0,1] row_mask:0xf bank_mask:0xf\n" \
  "v_cndmask_b32_dpp v14, v10, v14, vcc quad_perm:[0,1,0,1] row_mask:0xf bank_mask:0xf\n" \
  "v_cndmask_b32_dpp v15, v11, v15, vcc quad_perm:[0,1,0,1] row_mask:0xf bank_mask:0xf\n" \
  "v_maximum3_f32 v8, v43, v12, v13\n" \
  "v_maximum3_f32 v8, v8, v14, v15\n" \
  "v_add_f32 v16, v16, v18\n" \
  "v_add_f32 v17, v17, v19\n" \
  "v_max_f32_dpp v8, v8, v8 quad_perm:[2,3,0,1] row_mask:0xf bank_mask:0xf\n" \
  "v_mov_b32 v9, v8\n" \
  "v_add_f32 v20, v20, v22\n" \
  "v_add_f32 v21, v21, v23\n" \
  "v_permlane16_swap_b32 v8, v9\n" \
  "v_max_f32 v8, v8, v9\n" \
  "v_mov_b32 v9, v8\n" \
  "v_cvt_pk_bf16_f32 v16, v16, v17\n" \
  "v_cvt_pk_bf16_f32 v17, v20, v21\n" \
  "v_permlane32_swap_b32 v8, v9\n" \
  "v_max_f32 v8, v8, v9\n" \
  "s_barrier\n" \
  "buffer_load_dword v7, v5, s[16:19], s24 offen sc1\n" \
  "v_mul_f32 v8, s78, v8\n" \
  "v_pk_fma_f32 v[12:13], v[12:13], s[78:79], v[8:9] op_sel_hi:[1,0,0] neg_lo:[0,0,1] neg_hi:[0,0,1]\n" \
  "v_pk_fma_f32 v[14:15], v[14:15], s[78:79], v[8:9] op_sel_hi:[1,0,0] neg_lo:[0,0,1] neg_hi:[0,0,1]\n" \
  "v_fma_f32 v43, v43, s78, -v8\n" \
  "v_exp_f32 v12, v12\n" \
  "v_exp_f32 v13, v13\n" \
  "v_exp_f32 v14, v14\n" \
  "v_exp_f32 v15, v15\n" \
  "v_exp_f32 v43, v43\n" \
  "v_cvt_pk_bf16_f32 v44, v12, v13\n" \
  "v_cvt_pk_bf16_f32 v45, v14, v15\n" \
  "v_pk_add_f32 v[18:19], v[12:13], v[14:15]\n" \
  "v_mov_b32_dpp v46, v44 quad_perm:[2,3,0,1] row_mask:0xf bank_mask:0xf\n" \
  "v_mov_b32_dpp v9, v43 quad_perm:[2,3,0,1] row_mask:0xf bank_mask:0xf\n" \
  "v_mov_b32_dpp v47, v45 quad_perm:[2,3,0,1] row_mask:0xf bank_mask:0xf\n" \
  "v_add_f32 v10, v18, v43\n" \
  "v_cvt_pk_bf16_f32 v40, v43, v9\n" \
  "v_add_f32 v10, v10, v19\n" \
  "v_mfma_f32_16x16x32_bf16 v[12:15], v[48:51], v[44:47], 0\n" \
  "v_cvt_pk_bf16_f32 v43, v43, v9\n" \
  "v_add_f32_dpp v10, v10, v10 quad_perm:[2,3,0,1] row_mask:0xf bank_mask:0xf\n" \
  "v_mfma_f32_16x16x32_bf16 v[20:23], v[52:55], v[44:47], 0\n" \
  "v_mul_f32 v11, s75, v10\n" \
  "v_mul_f32 v10, s75, v10\n" \
  "v_mfma_f32_16x16x16_bf16 v[12:15], v[16:17], v[40:41], v[12:15]\n" \
  "ds_read_b128 v[48:51], v6 offset:10240\n" \
  "v_permlane16_swap_b32 v10, v11\n" \
  "v_add_f32 v10, v10, v11\n" \
  "v_mfma_f32_16x16x16_bf16 v[20:23], v[16:17], v[42:43], v[20:23]\n" \
  "v_mov_b32 v11, v10\n" \
  "ds_read_b128 v[52:55], v6 offset:11264\n" \
  "v_mov_b64 v[18:19], s[84:85]\n" \
  "v_permlane32_swap_b32 v10, v11\n" \
  "v_add_f32 v10, v10, v11\n" \
  "v_rcp_f32 v10, v10\n" \
  "s_nop 0\n" \
  "v_pk_mul_f32 v[12:13], v[12:13], v[10:11] op_sel_hi:[1,0]\n" \
  "v_pk_mul_f32 v[14:15], v[14:15], v[10:11] op_sel_hi:[1,0]\n" \
  "v_pk_mul_f32 v[20:21], v[20:21], v[10:11] op_sel_hi:[1,0]\n" \
  "v_pk_mul_f32 v[22:23], v[22:23], v[10:11] op_sel_hi:[1,0]\n" \
  "v_cvt_pk_bf16_f32 v40, v12, v13\n" \
  "v_cvt_pk_bf16_f32 v41, v14, v15\n" \
  "v_cvt_pk_bf16_f32 v42, v20, v21\n" \
  "ds_write_b64 v4, v[40:41]\n" \
  "v_cvt_pk_bf16_f32 v43, v22, v23\n" \
  "ds_write_b64 v4, v[42:43] offset:1024\n" \
  "v_max_u32 v16, 1, v16\n" \
  "v_max_u32 v17, 1, v17\n" \
  "buffer_store_dwordx2 v[16:17], v3, s[52:55], s70 offen sc1\n" \
  "v_mov_b64 v[12:13], s[84:85]\n" \
  "v_mov_b64 v[14:15], s[84:85]\n" \
  "v_mov_b64 v[16:17], s[84:85]\n" \
  "v_mov_b64 v[20:21], s[84:85]\n" \
  "v_mov_b64 v[22:23], s[84:85]\n" \
  "v_mov_b64 v[8:9], s[84:85]\n" \
  "v_mov_b64 v[10:11], s[84:85]\n" \
  "s_setprio 0\n" \
  "s_waitcnt lgkmcnt(0)\n" \
  "s_barrier\n" \
  "ds_read_b128 v[40:43], v1 offset:8192\n" \
  "ds_read_b128 v[44:47], v1 offset:9216\n" \
  "s_waitcnt lgkmcnt(1)\n" \
  "v_smfmac_f32_16x16x64_bf16 v[12:15], v[40:43], v[56:63], v0\n" \
  "s_waitcnt lgkmcnt(0)\n" \
  "v_smfmac_f32_16x16x64_bf16 v[12:15], v[44:47], v[32:39], v0\n" \
  "ds_read_b128 v[56:59], v6 offset:4096\n" \
  "ds_read_b128 v[60:63], v6 offset:5120\n" \
  "v_smfmac_f32_16x16x64_bf16 v[16:19], v[40:43], v[24:31], v0\n" \
  "ds_read_b128 v[32:35], v6 offset:12288\n" \
  "ds_read_b128 v[36:39], v6 offset:13312\n" \
  "v_smfmac_f32_16x16x64_bf16 v[16:19], v[44:47], v[48:55], v0\n" \
  "ds_read_b128 v[24:27], v6 offset:6144\n" \
  "ds_read_b128 v[28:31], v6 offset:7168\n" \
  "v_add_f32 v12, v12, v14\n" \
  "v_add_f32 v13, v13, v15\n" \
  "global_atomic_add_x2 v5, v[12:13], s[26:27]\n" \
  "ds_read_b128 v[48:51], v6 offset:14336\n" \
  "ds_read_b128 v[52:55], v6 offset:15360\n" \
  "s_waitcnt lgkmcnt(6)\n" \
  "v_add_f32 v16, v16, v18\n" \
  "v_add_f32 v17, v17, v19\n" \
  "global_atomic_add_x2 v5, v[16:17], s[28:29]\n" \
  "v_smfmac_f32_16x16x64_bf16 v[20:23], v[40:43], v[56:63], v0\n" \
  "s_waitcnt lgkmcnt(4)\n" \
  "v_smfmac_f32_16x16x64_bf16 v[20:23], v[44:47], v[32:39], v0\n" \
  "s_waitcnt lgkmcnt(2)\n" \
  "v_smfmac_f32_16x16x64_bf16 v[8:11], v[40:43], v[24:31], v0\n" \
  "s_waitcnt lgkmcnt(0)\n" \
  "v_smfmac_f32_16x16x64_bf16 v[8:11], v[44:47], v[48:55], v0\n" \
  "s_nop 4\n" \
  "v_add_f32 v20, v20, v22\n" \
  "v_add_f32 v21, v21, v23\n" \
  "global_atomic_add_x2 v5, v[20:21], s[30:31]\n" \
  "v_add_f32 v8, v8, v10\n" \
  "v_add_f32 v9, v9, v11\n" \
  "global_atomic_add_x2 v5, v[8:9], s[32:33]\n" \
  "s_memrealtime s[62:63]\n" \
  "v_mbcnt_lo_u32_b32 v48, -1, 0\n" \
  "v_mbcnt_hi_u32_b32 v48, -1, v48\n" \
  "v_lshrrev_b32 v49, 3, v48\n" \
  "v_and_b32 v50, 7, v48\n" \
  "v_lshlrev_b32 v50, 4, v50\n" \
  "v_lshl_or_b32 v44, v49, 8, v50\n" \
  "s_add_u32 s20, s20, 32\n" \
  "s_mov_b32 s21, 0x8000\n" \
  "v_mov_b32 v52, s22\n" \
  "v_mov_b32 v54, s20\n" \
  "s_mul_i32 s88, s71, %[pp]\n" \
  "s_lshl_b32 s89, s71, 10\n" \
  "s_sub_u32 s88, s88, s25\n" \
  "s_mul_i32 s38, s24, %[an]\n" \
  "s_cmp_gt_u32 s89, s38\n" \
  "s_cselect_b32 s89, 0x1000, 0\n" \
  "s_waitcnt vmcnt(5)\n" \
  "v_mov_b32 v46, v7\n" \
  "s_branch .LCA_TKE_%=\n" \
  ".LCA_TKR_%=:\n" \
  "s_sub_u32 s21, s21, 1\n" \
  "s_cbranch_scc1 .LCA_TKO_%=\n" \
  "s_and_b32 s38, s21, 7\n" \
  "s_cmp_lg_u32 s38, 7\n" \
  "s_cbranch_scc1 .LCA_TKS_%=\n" \
  "buffer_load_dword v46, v5, s[16:19], s24 offen sc1\n" \
  ".LCA_TKS_%=:\n" \
  "s_sleep 1\n" \
  ".LCA_TKE_%=:\n" \
  "v_readlane_b32 s38, v46, 8\n" \
  "v_readlane_b32 s39, v46, 24\n" \
  "v_readlane_b32 s23, v46, 40\n" \
  "v_readlane_b32 s34, v46, 56\n" \
  "s_add_u32 s38, s38, s39\n" \
  "s_add_u32 s23, s23, s34\n" \
  "s_add_u32 s38, s38, s23\n" \
  "s_cmp_lt_u32 s38, s20\n" \
  "s_cbranch_scc1 .LCA_TKR_%=\n" \
  ".LCA_TKO_%=:\n" \
  "v_readlane_b32 s34, v46, 0\n" \
  "v_readlane_b32 s38, v46, 16\n" \
  "v_readlane_b32 s39, v46, 32\n" \
  "v_readlane_b32 s23, v46, 48\n" \
  "s_max_u32 s34, s34, s38\n" \
  "s_max_u32 s39, s39, s23\n" \
  "s_add_u32 s34, s34, %[cd]\n" \
  "v_readlane_b32 s23, v46, 1\n" \
  "s_max_u32 s34, s34, s39\n" \
  "s_sub_u32 s23, s23, s34\n" \
  "s_sub_u32 s23, s23, s88\n" \
  "s_cmp_lt_u32 s23, s89\n" \
  "s_cselect_b32 s23, s23, 0\n" \
  "s_min_u32 s23, s23, %[ka]\n" \
  "s_add_u32 s34, s34, s23\n" \
  "s_add_u32 s34, s34, s64\n" \
  "v_mov_b32 v55, s34\n" \
  "ds_write_b64 v52, v[54:55]\n" \
  "s_branch .LCA_WAIU_%=\n" \
  ".LCA_WAIS_%=:\n" \
  "s_cmp_lt_i32 s39, -1000\n" \
  "s_cbranch_scc1 .LCA_WAIE_%=\n" \
  "s_sleep 1\n" \
  ".LCA_WAIU_%=:\n" \
  "s_memrealtime s[38:39]\n" \
  "s_waitcnt lgkmcnt(0)\n" \
  "s_sub_i32 s39, s38, s34\n" \
  "s_cmp_lt_i32 s39, -7\n" \
  "s_cbranch_scc1 .LCA_WAIS_%=\n" \
  "s_cmp_lt_i32 s39, 0\n" \
  "s_cbranch_scc1 .LCA_WAIU_%=\n" \
  ".LCA_WAIE_%=:\n" \
  "buffer_load_dwordx4 v[24:27], v44, s[40:43], s60 offen sc1\n" \
  "buffer_load_dwordx4 v[28:31], v44, s[40:43], s60 offen offset:2048 sc1\n" \
  "s_sub_u32 s62, s62, s67\n" \
  "s_lshl_b32 s62, s62, 4\n" \
  "s_sub_i32 s62, s62, s66\n" \
  "s_min_i32 s62, s62, 128\n" \
  "s_max_i32 s62, s62, -256\n" \
  "s_ashr_i32 s62, s62, 2\n" \
  "s_add_i32 s66, s66, s62\n" \
  "s_add_u32 s70, s70, 0x10000\n" \
  "v_mbcnt_lo_u32_b32 v48, -1, 0\n" \
  "v_mbcnt_hi_u32_b32 v48, -1, v48\n" \
  "v_and_b32 v49, 15, v48\n" \
  "v_lshl_add_u32 v9, v49, 1, s97\n" \
  "v_lshrrev_b32 v49, 4, v48\n" \
  "v_mad_u32_u24 v9, v49, s94, v9\n" \
  "v_add_u32 v7, s99, v1\n" \
  "v_mov_b64 v[12:13], 0\n" \
  "v_mov_b64 v[14:15], 0\n" \
  "v_mov_b64 v[16:17], 0\n" \
  "v_mov_b64 v[18:19], 0\n" \
  "v_mov_b64 v[20:21], 0\n" \
  "v_mov_b64 v[22:23], 0\n" \
  "v_and_b32 v49, 3, v48\n" \
  "v_lshl_add_u32 v40, v49, 2, s98\n" \
  "v_lshrrev_b32 v49, 2, v48\n" \
  "v_bfe_u32 v48, v48, 4, 1\n" \
  "v_xor_b32 v49, v49, v48\n" \
  "v_lshl_add_u32 v40, v49, 5, v40\n" \
  "s_add_i32 s80, s86, 0x35000000\n" \
  "s_sub_u32 s91, 0x4a350000, s86\n" \
  "s_min_u32 s91, s91, 0x4ca00000\n" \
  "s_mov_b32 s89, s60\n" \
  "s_xor_b32 s60, s60, 0x80\n" \
  "s_bitcmp0_b32 s60, 7\n" \
  "s_cselect_b32 s88, 0x8000, 0\n" \
  "s_add_u32 s60, s60, s88\n" \
  "s_and_b32 s60, s60, 0xffff\n" \
  "s_add_u32 s58, s56, s60\n" \
  "s_addc_u32 s59, s57, 0\n" \
  "s_xor_b32 s38, s35, 0\n" \
  "s_add_u32 s26, s58, s38\n" \
  "s_addc_u32 s27, s59, 0\n" \
  "s_xor_b32 s38, s35, 1024\n" \
  "s_add_u32 s28, s58, s38\n" \
  "s_addc_u32 s29, s59, 0\n" \
  "s_xor_b32 s38, s35, 2048\n" \
  "s_add_u32 s30, s58, s38\n" \
  "s_addc_u32 s31, s59, 0\n" \
  "s_xor_b32 s38, s35, 3072\n" \
  "s_add_u32 s32, s58, s38\n" \
  "s_addc_u32 s33, s59, 0\n" \
  "s_sub_u32 s71, s71, 1\n" \
  "s_mov_b32 s87, 0x40000\n" \
  "s_branch .LCA_CHK_%=\n" \
  ".LCA_POLL_%=:\n" \
  "buffer_load_dwordx4 v[24:27], v44, s[40:43], s89 offen sc1\n" \
  "buffer_load_dwordx4 v[28:31], v44, s[40:43], s89 offen offset:2048 sc1\n" \
  ".LCA_CHK_%=:\n" \
  "s_sub_u32 s87, s87, 1\n" \
  "s_cbranch_scc1 .LCA_END_%=\n" \
  "s_waitcnt vmcnt(1)\n" \
  "v_cvt_f32_i32 v32, v24\n" \
  "v_cvt_f32_i32 v33, v25\n" \
  "v_cvt_f32_i32 v34, v26\n" \
  "v_cvt_f32_i32 v35, v27\n" \
  "v_pk_mul_f32 v[24:25], v[32:33], s[80:81] op_sel_hi:[1,0]\n" \
  "v_pk_mul_f32 v[26:27], v[34:35], s[80:81] op_sel_hi:[1,0]\n" \
  "v_max3_f32 v45, |v32|, |v33|, |v34|\n" \
  "v_cvt_pk_bf16_f32 v24, v24, v26\n" \
  "v_cvt_pk_bf16_f32 v25, v25, v27\n" \
  "ds_write2_b32 v40, v24, v25 offset0:0 offset1:4\n" \
  "v_max3_f32 v45, |v45|, |v35|, |v35|\n" \
  "s_waitcnt vmcnt(0)\n" \
  "v_cvt_f32_i32 v32, v28\n" \
  "v_cvt_f32_i32 v33, v29\n" \
  "v_cvt_f32_i32 v34, v30\n" \
  "v_cvt_f32_i32 v35, v31\n" \
  "v_pk_mul_f32 v[28:29], v[32:33], s[80:81] op_sel_hi:[1,0]\n" \
  "v_pk_mul_f32 v[30:31], v[34:35], s[80:81] op_sel_hi:[1,0]\n" \
  "v_max3_f32 v45, |v45|, |v32|, |v33|\n" \
  "v_cvt_pk_bf16_f32 v28, v28, v30\n" \
  "v_cvt_pk_bf16_f32 v29, v29, v31\n" \
  "ds_write2_b32 v40, v28, v29 offset0:128 offset1:132\n" \
  "v_max3_f32 v45, |v45|, |v34|, |v35|\n" \
  "v_cmp_lt_f32 vcc, s91, v45\n" \
  "s_cbranch_vccnz .LCA_BIG_%=\n" \
  ".LCA_SML_%=:\n" \
  "s_branch .LCA_TOP_%=\n" \
  ".LCA_BIG_%=:\n" \
  "v_cmp_lt_f32 vcc, 0x4ca00000, v45\n" \
  "s_cbranch_vccnz .LCA_POLL_%=\n" \
  "v_mul_f32 v45, s80, v45\n" \
  "v_mul_f32 v45, v45, v45\n" \
  "s_add_u32 s88, s74, 157760\n" \
  "v_mov_b32 v44, s88\n" \
  "s_nop 0\n" \
  "v_cmpx_lt_f32 vcc, 2.0, v45\n" \
  "ds_max_u32 v44, v45\n" \
  "s_mov_b64 exec, -1\n" \
  "s_branch .LCA_SML_%=\n" \
  ".LCA_LAST_%=:\n" \
  "s_nop 0\n" \
  "v_smfmac_f32_16x16x64_bf16 v[16:19], v[60:63], v[176:183], v0\n" \
  "v_smfmac_f32_16x16x64_bf16 v[20:23], v[60:63], v[240:247], v0\n" \
  "v_smfmac_f32_16x16x64_bf16 v[16:19], v[44:47], v[184:191], v0\n" \
  "v_smfmac_f32_16x16x64_bf16 v[20:23], v[44:47], v[248:255], v0\n" \
  "s_nop 7\n" \
  "v_add_f32 v16, v16, v18\n" \
  "v_add_f32 v17, v17, v19\n" \
  "v_add_f32 v20, v20, v22\n" \
  "v_add_f32 v21, v21, v23\n" \
  "v_cvt_pk_bf16_f32 v16, v16, v17\n" \
  "v_cvt_pk_bf16_f32 v17, v20, v21\n" \
  "s_nop 1\n" \
  "s_nop 0\n" \
  "v_max_u32 v16, 1, v16\n" \
  "v_max_u32 v17, 1, v17\n" \
  "buffer_store_dwordx2 v[16:17], v3, s[52:55], s70 offen sc1\n" \
  "s_waitcnt vmcnt(0)\n" \
  ".LCA_END_%=:\n" \
  "s_endpgm\n"
#define CHAIN_ASM_VO \
  "s_mov_b32 s66, %[d0]\n" \
  "s_mov_b32 s94, 576\n" \
  "s_mov_b32 s95, 288\n" \
  "s_mov_b32 s84, 0x43400000\n" \
  "s_mov_b32 s85, 0x43400000\n" \
  "s_mov_b32 s92, 0x88000000\n" \
  "s_mov_b32 s93, 0x87fffff7\n" \
  "s_mov_b32 s78, 0x3e0293ee\n" \
  "s_mov_b32 s76, 0x33333333\n" \
  "s_mov_b32 s77, 0x33333333\n" \
  "s_add_u32 s58, s56, s60\n" \
  "s_addc_u32 s59, s57, 0\n" \
  "s_xor_b32 s38, s35, 0\n" \
  "s_add_u32 s26, s58, s38\n" \
  "s_addc_u32 s27, s59, 0\n" \
  "s_xor_b32 s38, s35, 1024\n" \
  "s_add_u32 s28, s58, s38\n" \
  "s_addc_u32 s29, s59, 0\n" \
  "s_xor_b32 s38, s35, 2048\n" \
  "s_add_u32 s30, s58, s38\n" \
  "s_addc_u32 s31, s59, 0\n" \
  "s_xor_b32 s38, s35, 3072\n" \
  "s_add_u32 s32, s58, s38\n" \
  "s_addc_u32 s33, s59, 0\n" \
  "s_lshr_b32 s98, s60, 2\n" \
  "s_add_u32 s98, s98, s74\n" \
  "s_add_u32 s98, s98, 131072\n" \
  "s_lshl_b32 s97, s73, 5\n" \
  "s_add_u32 s97, s97, s74\n" \
  "s_add_u32 s97, s97, 158592\n" \
  "s_lshl_b32 s99, s73, 11\n" \
  "s_add_u32 s99, s99, s74\n" \
  "s_add_u32 s99, s99, 133120\n" \
  "v_mbcnt_lo_u32_b32 v48, -1, 0\n" \
  "v_mbcnt_hi_u32_b32 v48, -1, v48\n" \
  "s_sub_u32 s88, s73, 4\n" \
  "v_and_b32 v51, 15, v48\n" \
  "v_lshl_add_u32 v4, v51, 1, s97\n" \
  "v_lshrrev_b32 v49, 4, v48\n" \
  "v_mad_u32_u24 v4, v49, s94, v4\n" \
  "v_lshrrev_b32 v49, 2, v51\n" \
  "v_and_b32 v50, 5, v51\n" \
  "v_and_or_b32 v49, v49, 2, v50\n" \
  "s_lshl_b32 s89, s88, 6\n" \
  "s_add_u32 s89, s89, s74\n" \
  "s_add_u32 s89, s89, 161152\n" \
  "v_and_b32 v50, 48, v48\n" \
  "v_add_u32 v6, s89, v50\n" \
  "v_mad_u32_u24 v6, v49, s95, v6\n" \
  "v_lshlrev_b32 v7, 3, v50\n" \
  "v_lshl_add_u32 v7, v49, 4, v7\n" \
  "v_and_b32 v49, 2, v51\n" \
  "v_lshl_add_u32 v7, v49, 29, v7\n" \
  "v_lshlrev_b32 v10, 4, v48\n" \
  "v_mov_b64 v[12:13], 0\n" \
  "v_mov_b64 v[14:15], 0\n" \
  "v_mov_b64 v[16:17], 0\n" \
  "v_mov_b64 v[18:19], 0\n" \
  "v_mov_b64 v[20:21], 0\n" \
  "v_mov_b64 v[22:23], 0\n" \
  "v_mov_b32 v24, 0x44870000\n" \
  "v_mul_f32 v24, s72, v24\n" \
  "s_nop 0\n" \
  "v_readfirstlane_b32 s86, v24\n" \
  "s_lshr_b32 s86, s86, 23\n" \
  "s_addk_i32 s86, 0xff83\n" \
  "s_ashr_i32 s86, s86, 1\n" \
  "s_max_i32 s86, s86, -20\n" \
  "s_min_i32 s86, s86, 100\n" \
  "s_mov_b32 s82, s86\n" \
  "s_lshr_b32 s83, s72, 23\n" \
  "s_sub_i32 s83, s83, 242\n" \
  "s_lshl_b32 s88, s73, 10\n" \
  "v_add_u32 v24, s88, v1\n" \
  "ds_read_b128 v[32:35], v24 offset:0\n" \
  "s_waitcnt lgkmcnt(0)\n" \
  "v_mov_b32 v45, 0\n" \
  "v_dot2c_f32_bf16 v45, v32, v32\n" \
  "v_dot2c_f32_bf16 v45, v33, v33\n" \
  "v_dot2c_f32_bf16 v45, v34, v34\n" \
  "v_dot2c_f32_bf16 v45, v35, v35\n" \
  "s_add_u32 s88, s74, 157760\n" \
  "v_mov_b32 v44, s88\n" \
  "s_nop 0\n" \
  "v_cmpx_lt_f32 vcc, 2.0, v45\n" \
  "ds_max_u32 v44, v45\n" \
  "s_mov_b64 exec, -1\n" \
  ".p2align 6\n" \
  ".LCV_AL_%=:\n" \
  ".LCV_TOP_%=:\n" \
  "s_waitcnt lgkmcnt(0)\n" \
  "s_barrier\n" \
  "s_setprio 2\n" \
  "ds_read_b128 v[24:27], v1 offset:0\n" \
  "ds_read_b128 v[28:31], v1 offset:1024\n" \
  "ds_read_b128 v[32:35], v1 offset:2048\n" \
  "ds_read_b128 v[36:39], v1 offset:3072\n" \
  "buffer_load_dword v5, off, s[44:47], s68 sc1\n" \
  "s_waitcnt lgkmcnt(3)\n" \
  "v_smfmac_f32_16x16x64_bf16 v[20:23], v[24:27], v[192:199], v0\n" \
  "ds_read_b128 v[40:43], v1 offset:4096\n" \
  "s_waitcnt lgkmcnt(3)\n" \
  "v_smfmac_f32_16x16x64_bf16 v[20:23], v[28:31], v[200:207], v0\n" \
  "ds_read_b128 v[44:47], v1 offset:5120\n" \
  "s_waitcnt lgkmcnt(3)\n" \
  "v_smfmac_f32_16x16x64_bf16 v[20:23], v[32:35], v[208:215], v0\n" \
  "ds_read_b128 v[48:51], v1 offset:6144\n" \
  "s_waitcnt lgkmcnt(3)\n" \
  "v_smfmac_f32_16x16x64_bf16 v[20:23], v[36:39], v[216:223], v0\n" \
  "ds_read_b128 v[52:55], v1 offset:7168\n" \
  "s_waitcnt lgkmcnt(3)\n" \
  "v_smfmac_f32_16x16x64_bf16 v[20:23], v[40:43], v[224:231], v0\n" \
  "s_waitcnt lgkmcnt(2)\n" \
  "v_smfmac_f32_16x16x64_bf16 v[20:23], v[44:47], v[232:239], v0\n" \
  "s_waitcnt lgkmcnt(1)\n" \
  "v_smfmac_f32_16x16x64_bf16 v[20:23], v[48:51], v[240:247], v0\n" \
  "s_waitcnt lgkmcnt(0)\n" \
  "v_smfmac_f32_16x16x64_bf16 v[20:23], v[52:55], v[248:255], v0\n" \
  "s_nop 0\n" \
  "v_smfmac_f32_16x16x64_bf16 v[12:15], v[24:27], v[64:71], v0\n" \
  "s_nop 0\n" \
  "v_smfmac_f32_16x16x64_bf16 v[12:15], v[28:31], v[72:79], v0\n" \
  "s_nop 0\n" \
  "v_smfmac_f32_16x16x64_bf16 v[12:15], v[32:35], v[80:87], v0\n" \
  "ds_read_b32 v11, v1 offset:26688\n" \
  "v_smfmac_f32_16x16x64_bf16 v[12:15], v[36:39], v[88:95], v0\n" \
  "v_add_f32 v20, v20, v22\n" \
  "v_smfmac_f32_16x16x64_bf16 v[12:15], v[40:43], v[96:103], v0\n" \
  "v_add_f32 v21, v21, v23\n" \
  "v_smfmac_f32_16x16x64_bf16 v[12:15], v[44:47], v[104:111], v0\n" \
  "v_cvt_pk_bf16_f32 v20, v20, v21\n" \
  "v_smfmac_f32_16x16x64_bf16 v[12:15], v[48:51], v[112:119], v0\n" \
  "ds_write_b16 v4, v20 offset:0\n" \
  "v_smfmac_f32_16x16x64_bf16 v[12:15], v[52:55], v[120:127], v0\n" \
  "ds_write_b16_d16_hi v4, v20 offset:288\n" \
  "v_smfmac_f32_16x16x64_bf16 v[16:19], v[24:27], v[128:135], v0\n" \
  "s_nop 1\n" \
  "v_smfmac_f32_16x16x64_bf16 v[16:19], v[28:31], v[136:143], v0\n" \
  "s_nop 1\n" \
  "v_smfmac_f32_16x16x64_bf16 v[16:19], v[32:35], v[144:151], v0\n" \
  "v_add_f32 v12, v12, v14\n" \
  "v_smfmac_f32_16x16x64_bf16 v[16:19], v[36:39], v[152:159], v0\n" \
  "v_add_f32 v13, v13, v15\n" \
  "v_smfmac_f32_16x16x64_bf16 v[16:19], v[40:43], v[160:167], v0\n" \
  "v_cvt_pk_bf16_f32 v12, v12, v13\n" \
  "v_smfmac_f32_16x16x64_bf16 v[16:19], v[44:47], v[168:175], v0\n" \
  "ds_write_b16 v4, v12 offset:128\n" \
  "v_smfmac_f32_16x16x64_bf16 v[16:19], v[48:51], v[176:183], v0\n" \
  "ds_write_b16_d16_hi v4, v12 offset:416\n" \
  "v_smfmac_f32_16x16x64_bf16 v[16:19], v[52:55], v[184:191], v0\n" \
  "s_barrier\n" \
  "s_nop 6\n" \
  "v_add_f32 v16, v16, v18\n" \
  "v_add_f32 v17, v17, v19\n" \
  "v_cvt_pk_bf16_f32 v16, v16, v17\n" \
  "ds_write_b16 v4, v16 offset:2560\n" \
  "ds_write_b16_d16_hi v4, v16 offset:2848\n" \
  "s_waitcnt lgkmcnt(0)\n" \
  "s_barrier\n" \
  "s_setprio 0\n" \
  "ds_read_b128 v[60:63], v6 offset:0\n" \
  "v_readfirstlane_b32 s86, v11\n" \
  "s_add_u32 s88, s74, 157760\n" \
  "v_mov_b32 v8, s88\n" \
  "v_mov_b32 v9, 0\n" \
  "ds_write_b32 v8, v9\n" \
  "s_lshr_b32 s86, s86, 23\n" \
  "s_add_i32 s86, s86, s83\n" \
  "s_ashr_i32 s86, s86, 1\n" \
  "s_max_i32 s86, s86, s82\n" \
  "s_min_i32 s86, s86, 100\n" \
  "s_lshl_b32 s86, s86, 23\n" \
  "s_cmp_eq_u32 s71, 0\n" \
  "s_waitcnt lgkmcnt(1)\n" \
  "v_max_u32 v60, 1, v60\n" \
  "v_max_u32 v61, 1, v61\n" \
  "v_max_u32 v62, 1, v62\n" \
  "v_max_u32 v63, 1, v63\n" \
  "buffer_store_dwordx4 v[60:63], v7, s[48:51], s69 offen sc1\n" \
  "s_cbranch_scc1 .LCV_LAST_%=\n" \
  "s_waitcnt vmcnt(1)\n" \
  "v_cmp_eq_u32 vcc, 0, v5\n" \
  "s_cmp_eq_u64 vcc, 0\n" \
  "s_cselect_b32 s90, 1, 0\n" \
  "s_barrier\n" \
  "ds_read_b128 v[24:27], v3 offset:0\n" \
  "ds_read_b128 v[28:31], v3 offset:1024\n" \
  "ds_read_b128 v[32:35], v3 offset:2048\n" \
  "ds_read_b128 v[36:39], v3 offset:3072\n" \
  "ds_read_b128 v[40:43], v3 offset:8192\n" \
  "ds_read_b128 v[44:47], v3 offset:9216\n" \
  "ds_read_b128 v[48:51], v3 offset:10240\n" \
  "ds_read_b128 v[52:55], v3 offset:11264\n" \
  "ds_read_b128 v[56:59], v3 offset:4096\n" \
  "ds_read_b128 v[60:63], v3 offset:5120\n" \
  "s_cbranch_scc0 .LCV_NOTOK_%=\n" \
  "s_mov_b32 m0, s99\n" \
  "s_add_u32 s88, s99, 8192\n" \
  "global_load_lds_dwordx4 v10, s[62:63] sc1\n" \
  "global_load_lds_dwordx4 v10, s[62:63] offset:1024 sc1\n" \
  "s_mov_b32 m0, s88\n" \
  "s_nop 0\n" \
  "global_load_lds_dwordx4 v10, s[64:65] sc1\n" \
  "global_load_lds_dwordx4 v10, s[64:65] offset:1024 sc1\n" \
  ".LCV_NOTOK_%=:\n" \
  "v_mov_b64 v[12:13], s[84:85]\n" \
  "v_mov_b64 v[14:15], s[84:85]\n" \
  "v_mov_b64 v[16:17], s[84:85]\n" \
  "v_mov_b64 v[18:19], s[84:85]\n" \
  "v_mov_b64 v[20:21], s[84:85]\n" \
  "v_mov_b64 v[22:23], s[84:85]\n" \
  "s_waitcnt lgkmcnt(0)\n" \
  "s_barrier\n" \
  "ds_read_b128 v[4:7], v1 offset:8192\n" \
  "ds_read_b128 v[8:11], v1 offset:9216\n" \
  "s_waitcnt lgkmcnt(1)\n" \
  "v_smfmac_f32_16x16x64_bf16 v[12:15], v[4:7], v[24:31], v0\n" \
  "s_waitcnt lgkmcnt(0)\n" \
  "v_smfmac_f32_16x16x64_bf16 v[12:15], v[8:11], v[40:47], v0\n" \
  "ds_read_b128 v[24:27], v3 offset:12288\n" \
  "ds_read_b128 v[28:31], v3 offset:13312\n" \
  "v_smfmac_f32_16x16x64_bf16 v[16:19], v[4:7], v[32:39], v0\n" \
  "ds_read_b128 v[32:35], v3 offset:6144\n" \
  "ds_read_b128 v[36:39], v3 offset:7168\n" \
  "v_smfmac_f32_16x16x64_bf16 v[16:19], v[8:11], v[48:55], v0\n" \
  "s_cmp_eq_u32 s90, 1\n" \
  "s_cbranch_scc1 .LCV_NMT_%=\n" \
  "buffer_load_dword v55, off, s[44:47], s68 sc1\n" \
  ".LCV_NMT_%=:\n" \
  "v_add_f32 v12, v12, v14\n" \
  "v_add_f32 v13, v13, v15\n" \
  "global_atomic_add_x2 v2, v[12:13], s[26:27]\n" \
  "v_smfmac_f32_16x16x64_bf16 v[20:23], v[4:7], v[56:63], v0\n" \
  "ds_read_b128 v[56:59], v3 offset:14336\n" \
  "ds_read_b128 v[60:63], v3 offset:15360\n" \
  "v_add_f32 v16, v16, v18\n" \
  "v_add_f32 v17, v17, v19\n" \
  "global_atomic_add_x2 v2, v[16:17], s[28:29]\n" \
  "v_mov_b64 v[40:41], s[84:85]\n" \
  "v_mov_b64 v[42:43], s[84:85]\n" \
  "s_waitcnt lgkmcnt(4)\n" \
  "v_smfmac_f32_16x16x64_bf16 v[20:23], v[8:11], v[24:31], v0\n" \
  "s_waitcnt lgkmcnt(2)\n" \
  "v_smfmac_f32_16x16x64_bf16 v[40:43], v[4:7], v[32:39], v0\n" \
  "s_waitcnt lgkmcnt(0)\n" \
  "v_smfmac_f32_16x16x64_bf16 v[40:43], v[8:11], v[56:63], v0\n" \
  "s_nop 3\n" \
  "v_add_f32 v20, v20, v22\n" \
  "v_add_f32 v21, v21, v23\n" \
  "global_atomic_add_x2 v2, v[20:21], s[30:31]\n" \
  "s_nop 0\n" \
  "v_add_f32 v40, v40, v42\n" \
  "v_add_f32 v41, v41, v43\n" \
  "global_atomic_add_x2 v2, v[40:41], s[32:33]\n" \
  "v_mbcnt_lo_u32_b32 v48, -1, 0\n" \
  "v_mbcnt_hi_u32_b32 v48, -1, v48\n" \
  "s_add_u32 s20, s20, 32\n" \
  "s_cmp_eq_u32 s90, 1\n" \
  "s_cbranch_scc1 .LCV_HAVE_%=\n" \
  "s_mov_b32 s87, 0x40000\n" \
  "v_lshlrev_b32 v10, 4, v48\n" \
  "s_waitcnt vmcnt(4)\n" \
  "v_cmp_eq_u32 vcc, 0, v55\n" \
  "s_cmp_eq_u64 vcc, 0\n" \
  "s_cbranch_scc1 .LCV_GOT_%=\n" \
  ".LCV_LATE_%=:\n" \
  "buffer_load_dword v58, off, s[44:47], s68 sc1\n" \
  "s_sub_u32 s87, s87, 1\n" \
  "s_cbranch_scc1 .LCV_END_%=\n" \
  "s_waitcnt vmcnt(0)\n" \
  "v_cmp_eq_u32 vcc, 0, v58\n" \
  "s_cmp_eq_u64 vcc, 0\n" \
  "s_cbranch_scc1 .LCV_GOT_%=\n" \
  "s_sleep 12\n" \
  "s_branch .LCV_LATE_%=\n" \
  ".LCV_GOT_%=:\n" \
  "s_mov_b32 m0, s99\n" \
  "s_add_u32 s88, s99, 8192\n" \
  "global_load_lds_dwordx4 v10, s[62:63] sc1\n" \
  "global_load_lds_dwordx4 v10, s[62:63] offset:1024 sc1\n" \
  "s_mov_b32 m0, s88\n" \
  "s_nop 0\n" \
  "global_load_lds_dwordx4 v10, s[64:65] sc1\n" \
  "global_load_lds_dwordx4 v10, s[64:65] offset:1024 sc1\n" \
  ".LCV_HAVE_%=:\n" \
  "v_lshrrev_b32 v49, 3, v48\n" \
  "v_and_b32 v50, 7, v48\n" \
  "v_lshlrev_b32 v50, 4, v50\n" \
  "v_lshl_or_b32 v44, v49, 8, v50\n" \
  "s_mov_b32 s21, 0x8000\n" \
  "v_mov_b32 v52, s22\n" \
  "v_mov_b32 v54, s20\n" \
  "s_branch .LCV_TKF_%=\n" \
  ".LCV_TKE_%=:\n" \
  "s_sleep 1\n" \
  ".LCV_TKF_%=:\n" \
  "ds_read_b64 v[54:55], v52\n" \
  "s_sub_u32 s21, s21, 1\n" \
  "s_cbranch_scc1 .LCV_TKO_%=\n" \
  "s_waitcnt lgkmcnt(0)\n" \
  "v_readfirstlane_b32 s38, v54\n" \
  "v_readfirstlane_b32 s34, v55\n" \
  "s_cmp_eq_u32 s38, s20\n" \
  "s_cbranch_scc0 .LCV_TKE_%=\n" \
  ".LCV_TKO_%=:\n" \
  "s_branch .LCV_WAIU_%=\n" \
  ".LCV_WAIS_%=:\n" \
  "s_cmp_lt_i32 s39, -1000\n" \
  "s_cbranch_scc1 .LCV_WAIE_%=\n" \
  "s_sleep 1\n" \
  ".LCV_WAIU_%=:\n" \
  "s_memrealtime s[38:39]\n" \
  "s_waitcnt lgkmcnt(0)\n" \
  "s_sub_i32 s39, s38, s34\n" \
  "s_cmp_lt_i32 s39, -7\n" \
  "s_cbranch_scc1 .LCV_WAIS_%=\n" \
  "s_cmp_lt_i32 s39, 0\n" \
  "s_cbranch_scc1 .LCV_WAIU_%=\n" \
  ".LCV_WAIE_%=:\n" \
  "buffer_load_dwordx4 v[24:27], v44, s[40:43], s60 offen sc1\n" \
  "buffer_load_dwordx4 v[28:31], v44, s[40:43], s60 offen offset:2048 sc1\n" \
  "v_mbcnt_lo_u32_b32 v48, -1, 0\n" \
  "v_mbcnt_hi_u32_b32 v48, -1, v48\n" \
  "s_sub_u32 s88, s73, 4\n" \
  "v_and_b32 v51, 15, v48\n" \
  "v_lshl_add_u32 v4, v51, 1, s97\n" \
  "v_lshrrev_b32 v49, 4, v48\n" \
  "v_mad_u32_u24 v4, v49, s94, v4\n" \
  "v_lshrrev_b32 v49, 2, v51\n" \
  "v_and_b32 v50, 5, v51\n" \
  "v_and_or_b32 v49, v49, 2, v50\n" \
  "s_lshl_b32 s89, s88, 6\n" \
  "s_add_u32 s89, s89, s74\n" \
  "s_add_u32 s89, s89, 161152\n" \
  "v_and_b32 v50, 48, v48\n" \
  "v_add_u32 v6, s89, v50\n" \
  "v_mad_u32_u24 v6, v49, s95, v6\n" \
  "v_lshlrev_b32 v7, 3, v50\n" \
  "v_lshl_add_u32 v7, v49, 4, v7\n" \
  "v_and_b32 v49, 2, v51\n" \
  "v_lshl_add_u32 v7, v49, 29, v7\n" \
  "v_lshlrev_b32 v10, 4, v48\n" \
  "v_mov_b64 v[12:13], 0\n" \
  "v_mov_b64 v[14:15], 0\n" \
  "v_mov_b64 v[16:17], 0\n" \
  "v_mov_b64 v[18:19], 0\n" \
  "v_mov_b64 v[20:21], 0\n" \
  "v_mov_b64 v[22:23], 0\n" \
  "v_and_b32 v49, 3, v48\n" \
  "v_lshl_add_u32 v40, v49, 2, s98\n" \
  "v_lshrrev_b32 v49, 2, v48\n" \
  "v_bfe_u32 v48, v48, 4, 1\n" \
  "v_xor_b32 v49, v49, v48\n" \
  "v_lshl_add_u32 v40, v49, 5, v40\n" \
  "s_add_u32 s62, s62, 0x80000\n" \
  "s_addc_u32 s63, s63, 0\n" \
  "s_add_u32 s64, s64, 0x40000\n" \
  "s_addc_u32 s65, s65, 0\n" \
  "s_add_u32 s68, s68, 0x1000\n" \
  "s_add_u32 s69, s69, 0x10000\n" \
  "s_add_i32 s80, s86, 0x35000000\n" \
  "s_sub_u32 s91, 0x4a350000, s86\n" \
  "s_min_u32 s91, s91, 0x4ca00000\n" \
  "s_mov_b32 s89, s60\n" \
  "s_xor_b32 s60, s60, 0x80\n" \
  "s_bitcmp0_b32 s60, 7\n" \
  "s_cselect_b32 s88, 0x8000, 0\n" \
  "s_add_u32 s60, s60, s88\n" \
  "s_and_b32 s60, s60, 0xffff\n" \
  "s_add_u32 s58, s56, s60\n" \
  "s_addc_u32 s59, s57, 0\n" \
  "s_xor_b32 s38, s35, 0\n" \
  "s_add_u32 s26, s58, s38\n" \
  "s_addc_u32 s27, s59, 0\n" \
  "s_xor_b32 s38, s35, 1024\n" \
  "s_add_u32 s28, s58, s38\n" \
  "s_addc_u32 s29, s59, 0\n" \
  "s_xor_b32 s38, s35, 2048\n" \
  "s_add_u32 s30, s58, s38\n" \
  "s_addc_u32 s31, s59, 0\n" \
  "s_xor_b32 s38, s35, 3072\n" \
  "s_add_u32 s32, s58, s38\n" \
  "s_addc_u32 s33, s59, 0\n" \
  "s_sub_u32 s71, s71, 1\n" \
  "s_mov_b32 s87, 0x40000\n" \
  "s_branch .LCV_CHK_%=\n" \
  ".LCV_POLL_%=:\n" \
  "buffer_load_dwordx4 v[24:27], v44, s[40:43], s89 offen sc1\n" \
  "buffer_load_dwordx4 v[28:31], v44, s[40:43], s89 offen offset:2048 sc1\n" \
  ".LCV_CHK_%=:\n" \
  "s_sub_u32 s87, s87, 1\n" \
  "s_cbranch_scc1 .LCV_END_%=\n" \
  "s_waitcnt vmcnt(1)\n" \
  "v_cvt_f32_i32 v32, v24\n" \
  "v_cvt_f32_i32 v33, v25\n" \
  "v_cvt_f32_i32 v34, v26\n" \
  "v_cvt_f32_i32 v35, v27\n" \
  "v_pk_mul_f32 v[24:25], v[32:33], s[80:81] op_sel_hi:[1,0]\n" \
  "v_pk_mul_f32 v[26:27], v[34:35], s[80:81] op_sel_hi:[1,0]\n" \
  "v_max3_f32 v45, |v32|, |v33|, |v34|\n" \
  "v_cvt_pk_bf16_f32 v24, v24, v26\n" \
  "v_cvt_pk_bf16_f32 v25, v25, v27\n" \
  "ds_write2_b32 v40, v24, v25 offset0:0 offset1:4\n" \
  "v_max3_f32 v45, |v45|, |v35|, |v35|\n" \
  "s_waitcnt vmcnt(0)\n" \
  "v_cvt_f32_i32 v32, v28\n" \
  "v_cvt_f32_i32 v33, v29\n" \
  "v_cvt_f32_i32 v34, v30\n" \
  "v_cvt_f32_i32 v35, v31\n" \
  "v_pk_mul_f32 v[28:29], v[32:33], s[80:81] op_sel_hi:[1,0]\n" \
  "v_pk_mul_f32 v[30:31], v[34:35], s[80:81] op_sel_hi:[1,0]\n" \
  "v_max3_f32 v45, |v45|, |v32|, |v33|\n" \
  "v_cvt_pk_bf16_f32 v28, v28, v30\n" \
  "v_cvt_pk_bf16_f32 v29, v29, v31\n" \
  "ds_write2_b32 v40, v28, v29 offset0:128 offset1:132\n" \
  "v_max3_f32 v45, |v45|, |v34|, |v35|\n" \
  "v_cmp_lt_f32 vcc, s91, v45\n" \
  "s_cbranch_vccnz .LCV_BIG_%=\n" \
  ".LCV_SML_%=:\n" \
  "s_branch .LCV_TOP_%=\n" \
  ".LCV_BIG_%=:\n" \
  "v_cmp_lt_f32 vcc, 0x4ca00000, v45\n" \
  "s_cbranch_vccnz .LCV_POLL_%=\n" \
  "v_mul_f32 v45, s80, v45\n" \
  "v_mul_f32 v45, v45, v45\n" \
  "s_add_u32 s88, s74, 157760\n" \
  "v_mov_b32 v44, s88\n" \
  "s_nop 0\n" \
  "v_cmpx_lt_f32 vcc, 2.0, v45\n" \
  "ds_max_u32 v44, v45\n" \
  "s_mov_b64 exec, -1\n" \
  "s_branch .LCV_SML_%=\n" \
  ".LCV_LAST_%=:\n" \
  "s_waitcnt vmcnt(0)\n" \
  ".LCV_END_%=:\n" \
  "s_endpgm\n"
#define QKV_ASM_0 \
  "s_mov_b32 s69, 0\n" \
  "s_branch .LQA_POLL_%=\n" \
  ".p2align 6\n" \
  ".LQA_AL_%=:\n" \
  ".LQA_TOP_%=:\n" \
  "s_barrier\n" \
  "ds_read_b128 v[36:39], v7 offset:0\n" \
  "ds_read_b128 v[40:43], v7 offset:2048\n" \
  "ds_read_b128 v[44:47], v7 offset:4096\n" \
  "s_setprio 2\n" \
  "s_waitcnt lgkmcnt(2)\n" \
  "v_mfma_f32_16x16x32_bf16 v[12:15], v[64:67], v[36:39], 0\n" \
  "ds_read_b128 v[48:51], v7 offset:6144\n" \
  "v_mfma_f32_16x16x32_bf16 v[16:19], v[128:131], v[36:39], 0\n" \
  "v_mfma_f32_16x16x32_bf16 v[20:23], v[36:39], v[192:195], 0\n" \
  "s_cmp_lg_u32 s69, 1\n" \
  "s_cbranch_scc1 .LQA_FDN_%=\n" \
  "s_waitcnt vmcnt(1)\n" \
  "v_mov_b32 v52, s80\n" \
  "v_mov_b32 v53, 0x10000\n" \
  "s_mov_b64 exec, 1\n" \
  "ds_add_rtn_u32 v54, v52, v53\n" \
  "s_mov_b64 exec, -1\n" \
  "s_add_u32 s67, s67, 4\n" \
  "s_add_u32 s64, s64, 4096\n" \
  "s_addc_u32 s65, s65, 0\n" \
  "v_mov_b32 v55, 0\n" \
  "s_waitcnt lgkmcnt(0)\n" \
  "v_readfirstlane_b32 s74, v54\n" \
  "s_and_b32 s76, s74, 0x30000\n" \
  "s_cmp_lg_u32 s76, 0x30000\n" \
  "s_cbranch_scc1 .LQA_FDN_%=\n" \
  "s_mov_b32 s79, 0x100000\n" \
  "s_branch .LQA_FDT_%=\n" \
  ".LQA_FD_%=:\n" \
  "ds_read_b32 v54, v52\n" \
  "s_sub_u32 s79, s79, 1\n" \
  "s_cbranch_scc1 .LQA_FDE_%=\n" \
  "s_waitcnt lgkmcnt(0)\n" \
  "v_readfirstlane_b32 s74, v54\n" \
  ".LQA_FDT_%=:\n" \
  "s_and_b32 s74, s74, 0xffff\n" \
  "s_cmp_lt_u32 s74, s67\n" \
  "s_cbranch_scc1 .LQA_FD_%=\n" \
  ".LQA_FDE_%=:\n" \
  "s_mov_b64 exec, 1\n" \
  "global_store_dword v55, v53, s[64:65] offset:-4096 sc1\n" \
  "s_mov_b64 exec, -1\n" \
  "s_waitcnt vmcnt(1)\n" \
  "s_branch .LQA_LOOK_%=\n" \
  ".LQA_FDN_%=:\n" \
  "s_waitcnt vmcnt(0)\n" \
  ".LQA_LOOK_%=:\n" \
  "v_cmp_eq_u32 vcc, 0, v11\n" \
  "s_mov_b32 m0, s58\n" \
  "s_add_u32 s74, s56, 4096\n" \
  "s_addc_u32 s75, s57, 0\n" \
  "s_add_u32 s76, s58, 4096\n" \
  "s_cmp_eq_u64 vcc, 0\n" \
  "s_cselect_b32 s69, 1, 0\n" \
  "s_waitcnt lgkmcnt(2)\n" \
  "v_mfma_f32_16x16x32_bf16 v[12:15], v[68:71], v[40:43], v[12:15]\n" \
  "ds_read_b128 v[36:39], v7 offset:8192\n" \
  "s_cbranch_scc0 2\n" \
  "global_load_lds_dwordx4 v6, s[56:57] offset:0 sc1\n" \
  "v_mfma_f32_16x16x32_bf16 v[16:19], v[132:135], v[40:43], v[16:19]\n" \
  "s_cbranch_scc0 2\n" \
  "global_load_lds_dwordx4 v6, s[56:57] offset:1024 sc1\n" \
  "v_mfma_f32_16x16x32_bf16 v[20:23], v[40:43], v[196:199], v[20:23]\n" \
  "s_cbranch_scc0 2\n" \
  "global_load_lds_dwordx4 v6, s[56:57] offset:2048 sc1\n" \
  "s_waitcnt lgkmcnt(2)\n" \
  "v_mfma_f32_16x16x32_bf16 v[12:15], v[72:75], v[44:47], v[12:15]\n" \
  "ds_read_b128 v[40:43], v7 offset:10240\n" \
  "s_cbranch_scc0 2\n" \
  "global_load_lds_dwordx4 v6, s[56:57] offset:3072 sc1\n" \
  "v_mfma_f32_16x16x32_bf16 v[16:19], v[136:139], v[44:47], v[16:19]\n" \
  "s_mov_b32 m0, s76\n" \
  "s_cbranch_scc0 2\n" \
  "global_load_lds_dwordx4 v6, s[74:75] offset:0 sc1\n" \
  "v_mfma_f32_16x16x32_bf16 v[20:23], v[44:47], v[200:203], v[20:23]\n" \
  "s_cbranch_scc0 2\n" \
  "global_load_lds_dwordx4 v6, s[74:75] offset:1024 sc1\n" \
  "s_waitcnt lgkmcnt(2)\n" \
  "v_mfma_f32_16x16x32_bf16 v[12:15], v[76:79], v[48:51], v[12:15]\n" \
  "ds_read_b128 v[44:47], v7 offset:12288\n" \
  "s_cbranch_scc0 2\n" \
  "global_load_lds_dwordx4 v6, s[74:75] offset:2048 sc1\n" \
  "v_mfma_f32_16x16x32_bf16 v[16:19], v[140:143], v[48:51], v[16:19]\n" \
  "s_cbranch_scc0 2\n" \
  "global_load_lds_dwordx4 v6, s[74:75] offset:3072 sc1\n" \
  "v_mfma_f32_16x16x32_bf16 v[20:23], v[48:51], v[204:207], v[20:23]\n" \
  "s_cselect_b32 s76, 0x40000, s62\n" \
  "s_add_u32 s74, s59, 4096\n" \
  "s_waitcnt lgkmcnt(2)\n" \
  "v_mfma_f32_16x16x32_bf16 v[12:15], v[80:83], v[36:39], v[12:15]\n" \
  "ds_read_b128 v[48:51], v7 offset:14336\n" \
  "v_mov_b64 v[24:25], 0\n" \
  "v_lshrrev_b32 v26, 1, v6\n" \
  "buffer_store_dwordx2 v[24:25], v26, s[48:51], s61 offen sc1\n" \
  "v_mfma_f32_16x16x32_bf16 v[16:19], v[144:147], v[36:39], v[16:19]\n" \
  "v_mfma_f32_16x16x32_bf16 v[20:23], v[36:39], v[208:211], v[20:23]\n" \
  "s_waitcnt lgkmcnt(2)\n" \
  "v_mfma_f32_16x16x32_bf16 v[12:15], v[84:87], v[40:43], v[12:15]\n" \
  "ds_read_b128 v[36:39], v7 offset:16384\n" \
  "v_mfma_f32_16x16x32_bf16 v[16:19], v[148:151], v[40:43], v[16:19]\n" \
  "v_mfma_f32_16x16x32_bf16 v[20:23], v[40:43], v[212:215], v[20:23]\n" \
  "s_waitcnt lgkmcnt(2)\n" \
  "v_mfma_f32_16x16x32_bf16 v[12:15], v[88:91], v[44:47], v[12:15]\n" \
  "ds_read_b128 v[40:43], v7 offset:18432\n" \
  "v_mfma_f32_16x16x32_bf16 v[16:19], v[152:155], v[44:47], v[16:19]\n" \
  "v_mfma_f32_16x16x32_bf16 v[20:23], v[44:47], v[216:219], v[20:23]\n" \
  "s_waitcnt lgkmcnt(2)\n" \
  "v_mfma_f32_16x16x32_bf16 v[12:15], v[92:95], v[48:51], v[12:15]\n" \
  "ds_read_b128 v[44:47], v7 offset:20480\n" \
  "v_mfma_f32_16x16x32_bf16 v[16:19], v[156:159], v[48:51], v[16:19]\n" \
  "v_mfma_f32_16x16x32_bf16 v[20:23], v[48:51], v[220:223], v[20:23]\n" \
  "s_waitcnt lgkmcnt(2)\n" \
  "v_mfma_f32_16x16x32_bf16 v[12:15], v[96:99], v[36:39], v[12:15]\n" \
  "ds_read_b128 v[48:51], v7 offset:22528\n" \
  "v_mfma_f32_16x16x32_bf16 v[16:19], v[160:163], v[36:39], v[16:19]\n" \
  "v_mfma_f32_16x16x32_bf16 v[20:23], v[36:39], v[224:227], v[20:23]\n" \
  "s_waitcnt lgkmcnt(2)\n" \
  "v_mfma_f32_16x16x32_bf16 v[12:15], v[100:103], v[40:43], v[12:15]\n" \
  "ds_read_b128 v[36:39], v7 offset:24576\n" \
  "v_mfma_f32_16x16x32_bf16 v[16:19], v[164:167], v[40:43], v[16:19]\n" \
  "v_mfma_f32_16x16x32_bf16 v[20:23], v[40:43], v[228:231], v[20:23]\n" \
  "s_waitcnt lgkmcnt(2)\n" \
  "v_mfma_f32_16x16x32_bf16 v[12:15], v[104:107], v[44:47], v[12:15]\n" \
  "ds_read_b128 v[40:43], v7 offset:26624\n" \
  "v_mfma_f32_16x16x32_bf16 v[16:19], v[168:171], v[44:47], v[16:19]\n" \
  "v_mfma_f32_16x16x32_bf16 v[20:23], v[44:47], v[232:235], v[20:23]\n" \
  "s_waitcnt lgkmcnt(2)\n" \
  "v_mfma_f32_16x16x32_bf16 v[12:15], v[108:111], v[48:51], v[12:15]\n" \
  "ds_read_b128 v[44:47], v7 offset:28672\n" \
  "v_mfma_f32_16x16x32_bf16 v[16:19], v[172:175], v[48:51], v[16:19]\n" \
  "v_mfma_f32_16x16x32_bf16 v[20:23], v[48:51], v[236:239], v[20:23]\n" \
  "s_waitcnt lgkmcnt(2)\n" \
  "v_mfma_f32_16x16x32_bf16 v[12:15], v[112:115], v[36:39], v[12:15]\n" \
  "ds_read_b128 v[48:51], v7 offset:30720\n" \
  "v_mfma_f32_16x16x32_bf16 v[16:19], v[176:179], v[36:39], v[16:19]\n" \
  "v_mfma_f32_16x16x32_bf16 v[20:23], v[36:39], v[240:243], v[20:23]\n" \
  "s_waitcnt lgkmcnt(2)\n" \
  "v_mfma_f32_16x16x32_bf16 v[12:15], v[116:119], v[40:43], v[12:15]\n" \
  "ds_read_b128 v[36:39], v7 offset:256\n" \
  "v_mfma_f32_16x16x32_bf16 v[16:19], v[180:183], v[40:43], v[16:19]\n" \
  "v_mfma_f32_16x16x32_bf16 v[20:23], v[40:43], v[244:247], v[20:23]\n" \
  "s_waitcnt lgkmcnt(2)\n" \
  "v_mfma_f32_16x16x32_bf16 v[12:15], v[120:123], v[44:47], v[12:15]\n" \
  "ds_read_b128 v[40:43], v7 offset:2304\n" \
  "v_mfma_f32_16x16x32_bf16 v[16:19], v[184:187], v[44:47], v[16:19]\n" \
  "v_mfma_f32_16x16x32_bf16 v[20:23], v[44:47], v[248:251], v[20:23]\n" \
  "s_waitcnt lgkmcnt(2)\n" \
  "v_mfma_f32_16x16x32_bf16 v[12:15], v[124:127], v[48:51], v[12:15]\n" \
  "ds_read_b128 v[44:47], v7 offset:4352\n" \
  "v_mfma_f32_16x16x32_bf16 v[16:19], v[188:191], v[48:51], v[16:19]\n" \
  "v_mfma_f32_16x16x32_bf16 v[20:23], v[48:51], v[252:255], v[20:23]\n" \
  "s_barrier\n" \
  "s_waitcnt lgkmcnt(2)\n" \
  "v_mfma_f32_16x16x32_bf16 v[24:27], v[64:67], v[36:39], 0\n" \
  "ds_read_b128 v[48:51], v7 offset:6400\n" \
  "v_mfma_f32_16x16x32_bf16 v[28:31], v[128:131], v[36:39], 0\n" \
  "ds_read2_b32 v[52:53], v8 offset0:0 offset1:0\n" \
  "v_mfma_f32_16x16x32_bf16 v[32:35], v[36:39], v[192:195], 0\n" \
  "s_waitcnt lgkmcnt(3)\n" \
  "v_mfma_f32_16x16x32_bf16 v[24:27], v[68:71], v[40:43], v[24:27]\n" \
  "ds_read_b128 v[36:39], v7 offset:8448\n" \
  "v_mfma_f32_16x16x32_bf16 v[28:31], v[132:135], v[40:43], v[28:31]\n" \
  "ds_read2_b32 v[54:55], v8 offset0:16 offset1:16\n" \
  "v_mfma_f32_16x16x32_bf16 v[32:35], v[40:43], v[196:199], v[32:35]\n" \
  "s_waitcnt lgkmcnt(4)\n" \
  "v_mfma_f32_16x16x32_bf16 v[24:27], v[72:75], v[44:47], v[24:27]\n" \
  "ds_read_b128 v[40:43], v7 offset:10496\n" \
  "v_mfma_f32_16x16x32_bf16 v[28:31], v[136:139], v[44:47], v[28:31]\n" \
  "ds_read2_b32 v[56:57], v8 offset0:32 offset1:48\n" \
  "v_mfma_f32_16x16x32_bf16 v[32:35], v[44:47], v[200:203], v[32:35]\n" \
  "s_waitcnt lgkmcnt(5)\n" \
  "v_mfma_f32_16x16x32_bf16 v[24:27], v[76:79], v[48:51], v[24:27]\n" \
  "ds_read_b128 v[44:47], v7 offset:12544\n" \
  "v_mfma_f32_16x16x32_bf16 v[28:31], v[140:143], v[48:51], v[28:31]\n" \
  "ds_read_b128 v[58:61], v9 offset:128\n" \
  "v_mfma_f32_16x16x32_bf16 v[32:35], v[48:51], v[204:207], v[32:35]\n" \
  "s_waitcnt lgkmcnt(6)\n" \
  "v_mfma_f32_16x16x16_bf16 v[12:15], v[0:1], v[52:53], v[12:15]\n" \
  "v_mfma_f32_16x16x16_bf16 v[16:19], v[2:3], v[52:53], v[16:19]\n" \
  "v_mfma_f32_16x16x16_bf16 v[20:23], v[52:53], v[4:5], v[20:23]\n" \
  "s_waitcnt lgkmcnt(4)\n" \
  "v_mfma_f32_16x16x16_bf16 v[24:27], v[0:1], v[54:55], v[24:27]\n" \
  "v_mfma_f32_16x16x16_bf16 v[28:31], v[2:3], v[54:55], v[28:31]\n" \
  "v_mfma_f32_16x16x16_bf16 v[32:35], v[54:55], v[4:5], v[32:35]\n" \
  "v_mfma_f32_16x16x32_bf16 v[24:27], v[80:83], v[36:39], v[24:27]\n" \
  "ds_read_b128 v[48:51], v7 offset:14592\n" \
  "v_mfma_f32_16x16x32_bf16 v[28:31], v[144:147], v[36:39], v[28:31]\n" \
  "ds_read_b128 v[52:55], v9 offset:192\n" \
  "v_mfma_f32_16x16x32_bf16 v[32:35], v[36:39], v[208:211], v[32:35]\n" \
  "s_waitcnt lgkmcnt(5)\n" \
  "v_mfma_f32_16x16x32_bf16 v[24:27], v[84:87], v[40:43], v[24:27]\n" \
  "ds_read_b128 v[36:39], v7 offset:16640\n" \
  "v_mfma_f32_16x16x32_bf16 v[28:31], v[148:151], v[40:43], v[28:31]\n" \
  "v_mfma_f32_16x16x32_bf16 v[32:35], v[40:43], v[212:215], v[32:35]\n" \
  "s_waitcnt lgkmcnt(4)\n" \
  "v_mfma_f32_16x16x32_bf16 v[24:27], v[88:91], v[44:47], v[24:27]\n" \
  "ds_read_b128 v[40:43], v7 offset:18688\n" \
  "v_mfma_f32_16x16x32_bf16 v[28:31], v[152:155], v[44:47], v[28:31]\n" \
  "v_mfma_f32_16x16x32_bf16 v[32:35], v[44:47], v[216:219], v[32:35]\n" \
  "s_waitcnt lgkmcnt(3)\n" \
  "v_mfma_f32_16x16x32_bf16 v[24:27], v[92:95], v[48:51], v[24:27]\n" \
  "ds_read_b128 v[44:47], v7 offset:20736\n" \
  "v_mfma_f32_16x16x32_bf16 v[28:31], v[156:159], v[48:51], v[28:31]\n" \
  "v_mfma_f32_16x16x32_bf16 v[32:35], v[48:51], v[220:223], v[32:35]\n" \
  "s_waitcnt lgkmcnt(2)\n" \
  "v_mfma_f32_16x16x32_bf16 v[24:27], v[96:99], v[36:39], v[24:27]\n" \
  "ds_read_b128 v[48:51], v7 offset:22784\n" \
  "v_mfma_f32_16x16x32_bf16 v[28:31], v[160:163], v[36:39], v[28:31]\n" \
  "v_mfma_f32_16x16x32_bf16 v[32:35], v[36:39], v[224:227], v[32:35]\n" \
  "s_waitcnt lgkmcnt(2)\n" \
  "v_mfma_f32_16x16x32_bf16 v[24:27], v[100:103], v[40:43], v[24:27]\n" \
  "ds_read_b128 v[36:39], v7 offset:24832\n" \
  "v_mfma_f32_16x16x32_bf16 v[28:31], v[164:167], v[40:43], v[28:31]\n" \
  "buffer_load_dword v11, v10, s[52:55], s76 offen sc1\n" \
  "v_mfma_f32_16x16x32_bf16 v[32:35], v[40:43], v[228:231], v[32:35]\n" \
  "s_waitcnt lgkmcnt(2)\n" \
  "v_mfma_f32_16x16x32_bf16 v[24:27], v[104:107], v[44:47], v[24:27]\n" \
  "ds_read_b128 v[40:43], v7 offset:26880\n" \
  "v_mfma_f32_16x16x32_bf16 v[28:31], v[168:171], v[44:47], v[28:31]\n" \
  "v_mfma_f32_16x16x32_bf16 v[32:35], v[44:47], v[232:235], v[32:35]\n" \
  "s_waitcnt lgkmcnt(2)\n" \
  "v_mfma_f32_16x16x32_bf16 v[24:27], v[108:111], v[48:51], v[24:27]\n" \
  "ds_read_b128 v[44:47], v7 offset:28928\n" \
  "v_mfma_f32_16x16x32_bf16 v[28:31], v[172:175], v[48:51], v[28:31]\n" \
  "v_mfma_f32_16x16x32_bf16 v[32:35], v[48:51], v[236:239], v[32:35]\n" \
  "s_waitcnt lgkmcnt(2)\n" \
  "v_mfma_f32_16x16x32_bf16 v[24:27], v[112:115], v[36:39], v[24:27]\n" \
  "ds_read_b128 v[48:51], v7 offset:30976\n" \
  "v_mfma_f32_16x16x32_bf16 v[28:31], v[176:179], v[36:39], v[28:31]\n" \
  "v_mfma_f32_16x16x32_bf16 v[32:35], v[36:39], v[240:243], v[32:35]\n" \
  "s_waitcnt lgkmcnt(2)\n" \
  "v_mfma_f32_16x16x32_bf16 v[24:27], v[116:119], v[40:43], v[24:27]\n" \
  "v_mfma_f32_16x16x32_bf16 v[28:31], v[180:183], v[40:43], v[28:31]\n" \
  "v_mfma_f32_16x16x32_bf16 v[32:35], v[40:43], v[244:247], v[32:35]\n" \
  "s_waitcnt lgkmcnt(1)\n" \
  "v_mfma_f32_16x16x32_bf16 v[24:27], v[120:123], v[44:47], v[24:27]\n" \
  "v_mfma_f32_16x16x32_bf16 v[28:31], v[184:187], v[44:47], v[28:31]\n" \
  "v_mfma_f32_16x16x32_bf16 v[32:35], v[44:47], v[248:251], v[32:35]\n" \
  "s_waitcnt lgkmcnt(0)\n" \
  "v_mfma_f32_16x16x32_bf16 v[24:27], v[124:127], v[48:51], v[24:27]\n" \
  "v_mfma_f32_16x16x32_bf16 v[28:31], v[188:191], v[48:51], v[28:31]\n" \
  "v_mfma_f32_16x16x32_bf16 v[32:35], v[48:51], v[252:255], v[32:35]\n" \
  "s_setprio 2\n" \
  "v_mul_f32 v12, v12, v56\n" \
  "v_mul_f32 v13, v13, v56\n" \
  "v_mul_f32 v14, v14, v56\n" \
  "v_cvt_pk_bf16_f32 v12, v12, v13\n" \
  "v_mul_f32 v15, v15, v56\n" \
  "v_mul_f32 v16, v16, v56\n" \
  "v_cvt_pk_bf16_f32 v13, v14, v15\n" \
  "v_mul_f32 v17, v17, v56\n" \
  "v_mul_f32 v18, v18, v56\n" \
  "v_cvt_pk_bf16_f32 v14, v16, v17\n" \
  "v_mul_f32 v19, v19, v56\n" \
  "v_cvt_pk_bf16_f32 v15, v18, v19\n" \
  "buffer_store_dwordx4 v[12:15], v6, s[40:43], s59 offen offset:0 sc1\n" \
  "v_mul_f32 v24, v24, v57\n" \
  "v_mul_f32 v25, v25, v57\n" \
  "v_mul_f32 v26, v26, v57\n" \
  "v_cvt_pk_bf16_f32 v24, v24, v25\n" \
  "v_mul_f32 v27, v27, v57\n" \
  "v_mul_f32 v28, v28, v57\n" \
  "v_cvt_pk_bf16_f32 v25, v26, v27\n" \
  "v_mul_f32 v29, v29, v57\n" \
  "v_mul_f32 v30, v30, v57\n" \
  "v_cvt_pk_bf16_f32 v26, v28, v29\n" \
  "v_mul_f32 v31, v31, v57\n" \
  "v_cvt_pk_bf16_f32 v27, v30, v31\n" \
  "buffer_store_dwordx4 v[24:27], v6, s[40:43], s74 offen offset:0 sc1\n" \
  "v_mul_f32 v20, v20, v58\n" \
  "v_mul_f32 v21, v21, v59\n" \
  "v_mul_f32 v22, v22, v60\n" \
  "v_mul_f32 v23, v23, v61\n" \
  "v_cvt_pk_bf16_f32 v20, v20, v21\n" \
  "v_cvt_pk_bf16_f32 v21, v22, v23\n" \
  "v_mul_f32 v32, v32, v52\n" \
  "v_mul_f32 v33, v33, v53\n" \
  "v_mul_f32 v34, v34, v54\n" \
  "v_mul_f32 v35, v35, v55\n" \
  "v_cvt_pk_bf16_f32 v22, v32, v33\n" \
  "v_cvt_pk_bf16_f32 v23, v34, v35\n" \
  "buffer_store_dwordx4 v[20:23], v6, s[44:47], s60 offen offset:0 sc1\n" \
  "s_setprio 0\n" \
  "s_add_u32 s59, s59, 0x80000\n" \
  "s_add_u32 s60, s60, 0x40000\n" \
  "s_add_u32 s61, s61, 0x10000\n" \
  "v_xor_b32 v7, 0x8000, v7\n" \
  "s_cmp_eq_u32 s69, 1\n" \
  "s_waitcnt vmcnt(3)\n" \
  "s_cbranch_scc1 .LQA_GO_%=\n" \
  "v_cmp_eq_u32 vcc, 0, v11\n" \
  "s_cmp_eq_u64 vcc, 0\n" \
  "s_cbranch_scc0 .LQA_NOX_%=\n" \
  "s_mov_b32 m0, s58\n" \
  "s_add_u32 s74, s56, 4096\n" \
  "s_addc_u32 s75, s57, 0\n" \
  "s_add_u32 s76, s58, 4096\n" \
  "global_load_lds_dwordx4 v6, s[56:57] offset:0 sc1\n" \
  "global_load_lds_dwordx4 v6, s[56:57] offset:1024 sc1\n" \
  "global_load_lds_dwordx4 v6, s[56:57] offset:2048 sc1\n" \
  "global_load_lds_dwordx4 v6, s[56:57] offset:3072 sc1\n" \
  "s_mov_b32 m0, s76\n" \
  "s_nop 0\n" \
  "global_load_lds_dwordx4 v6, s[74:75] offset:0 sc1\n" \
  "global_load_lds_dwordx4 v6, s[74:75] offset:1024 sc1\n" \
  "global_load_lds_dwordx4 v6, s[74:75] offset:2048 sc1\n" \
  "global_load_lds_dwordx4 v6, s[74:75] offset:3072 sc1\n" \
  "s_mov_b32 s69, 2\n" \
  "s_waitcnt vmcnt(8)\n" \
  "s_branch .LQA_CNT_%=\n" \
  ".LQA_NOX_%=:\n" \
  "s_waitcnt vmcnt(0)\n" \
  ".LQA_CNT_%=:\n" \
  "v_mov_b32 v52, s80\n" \
  "v_mov_b32 v53, 0x10000\n" \
  "s_mov_b64 exec, 1\n" \
  "ds_add_rtn_u32 v54, v52, v53\n" \
  "s_mov_b64 exec, -1\n" \
  "s_add_u32 s67, s67, 4\n" \
  "s_add_u32 s64, s64, 4096\n" \
  "s_addc_u32 s65, s65, 0\n" \
  "v_mov_b32 v55, 0\n" \
  "s_waitcnt lgkmcnt(0)\n" \
  "v_readfirstlane_b32 s74, v54\n" \
  "s_and_b32 s76, s74, 0x30000\n" \
  "s_cmp_lg_u32 s76, 0x30000\n" \
  "s_cbranch_scc1 .LQA_FWN_%=\n" \
  "s_mov_b32 s79, 0x100000\n" \
  "s_branch .LQA_FWT_%=\n" \
  ".LQA_FW_%=:\n" \
  "ds_read_b32 v54, v52\n" \
  "s_sub_u32 s79, s79, 1\n" \
  "s_cbranch_scc1 .LQA_FWE_%=\n" \
  "s_waitcnt lgkmcnt(0)\n" \
  "v_readfirstlane_b32 s74, v54\n" \
  ".LQA_FWT_%=:\n" \
  "s_and_b32 s74, s74, 0xffff\n" \
  "s_cmp_lt_u32 s74, s67\n" \
  "s_cbranch_scc1 .LQA_FW_%=\n" \
  ".LQA_FWE_%=:\n" \
  "s_mov_b64 exec, 1\n" \
  "global_store_dword v55, v53, s[64:65] offset:-4096 sc1\n" \
  "s_mov_b64 exec, -1\n" \
  "s_waitcnt vmcnt(1)\n" \
  "s_branch .LQA_GO_%=\n" \
  ".LQA_FWN_%=:\n" \
  "s_waitcnt vmcnt(0)\n" \
  ".LQA_GO_%=:\n" \
  "s_sub_u32 s66, s66, 1\n" \
  "s_cbranch_scc1 .LQA_LAST_%=\n" \
  "s_cmp_lg_u32 s69, 0\n" \
  "s_cbranch_scc1 .LQA_NEXT_%=\n" \
  ".LQA_GATE_%=:\n" \
  "s_memrealtime s[74:75]\n" \
  "s_waitcnt lgkmcnt(0)\n" \
  "s_sub_i32 s74, s68, s74\n" \
  "s_cmp_gt_u32 s74, %[gq]\n" \
  "s_cbranch_scc1 .LQA_POLL_%=\n" \
  "s_sleep 3\n" \
  "s_branch .LQA_GATE_%=\n" \
  ".LQA_POLL_%=:\n" \
  "s_mov_b32 s79, 0x40000\n" \
  ".LQA_AGAIN_%=:\n" \
  "buffer_load_dword v11, v10, s[52:55], s62 offen sc1\n" \
  "s_sub_u32 s79, s79, 1\n" \
  "s_cbranch_scc1 .LQA_SEEN_%=\n" \
  "s_waitcnt vmcnt(0)\n" \
  "v_cmp_eq_u32 vcc, 0, v11\n" \
  "s_cmp_eq_u64 vcc, 0\n" \
  "s_cbranch_scc1 .LQA_SEEN_%=\n" \
  "s_sleep %[sq]\n" \
  "s_cmp_lg_u32 s63, 0\n" \
  "s_cbranch_scc1 .LQA_AGAIN_%=\n" \
  "s_sleep %[s1]\n" \
  "s_branch .LQA_AGAIN_%=\n" \
  ".LQA_SEEN_%=:\n" \
  "s_mov_b32 m0, s58\n" \
  "s_add_u32 s74, s56, 4096\n" \
  "s_addc_u32 s75, s57, 0\n" \
  "s_add_u32 s76, s58, 4096\n" \
  "global_load_lds_dwordx4 v6, s[56:57] offset:0 sc1\n" \
  "global_load_lds_dwordx4 v6, s[56:57] offset:1024 sc1\n" \
  "global_load_lds_dwordx4 v6, s[56:57] offset:2048 sc1\n" \
  "global_load_lds_dwordx4 v6, s[56:57] offset:3072 sc1\n" \
  "s_mov_b32 m0, s76\n" \
  "s_nop 0\n" \
  "global_load_lds_dwordx4 v6, s[74:75] offset:0 sc1\n" \
  "global_load_lds_dwordx4 v6, s[74:75] offset:1024 sc1\n" \
  "global_load_lds_dwordx4 v6, s[74:75] offset:2048 sc1\n" \
  "global_load_lds_dwordx4 v6, s[74:75] offset:3072 sc1\n" \
  "s_memrealtime s[76:77]\n" \
  "s_waitcnt lgkmcnt(0)\n" \
  "s_add_u32 s68, s76, %[gq]\n" \
  "s_waitcnt vmcnt(0)\n" \
  ".LQA_NEXT_%=:\n" \
  "s_add_u32 s56, s56, 0x8000\n" \
  "s_addc_u32 s57, s57, 0\n" \
  "s_xor_b32 s58, s58, 0x8000\n" \
  "s_add_u32 s62, s62, 4096\n" \
  "buffer_load_dword v11, v10, s[52:55], s62 offen sc1\n" \
  "s_branch .LQA_TOP_%=\n" \
  ".LQA_LAST_%=:\n" \
  "s_barrier\n" \
  "s_cmp_lg_u32 s78, 0\n" \
  "s_cbranch_scc1 .LQA_END_%=\n" \
  "s_add_u32 s67, s67, 4\n" \
  "s_add_u32 s64, s64, 4096\n" \
  "s_addc_u32 s65, s65, 0\n" \
  "s_mov_b32 s74, 0\n" \
  "s_mov_b32 s79, 0x100000\n" \
  "s_branch .LQA_FLT_%=\n" \
  ".LQA_FL_%=:\n" \
  "ds_read_b32 v54, v52\n" \
  "s_sub_u32 s79, s79, 1\n" \
  "s_cbranch_scc1 .LQA_FLE_%=\n" \
  "s_waitcnt lgkmcnt(0)\n" \
  "v_readfirstlane_b32 s74, v54\n" \
  ".LQA_FLT_%=:\n" \
  "s_and_b32 s74, s74, 0xffff\n" \
  "s_cmp_lt_u32 s74, s67\n" \
  "s_cbranch_scc1 .LQA_FL_%=\n" \
  ".LQA_FLE_%=:\n" \
  "s_mov_b64 exec, 1\n" \
  "global_store_dword v55, v53, s[64:65] offset:-4096 sc1\n" \
  "s_mov_b64 exec, -1\n" \
  ".LQA_END_%=:\n" \
  "s_endpgm\n"
#define QKV_ASM_1 \
  "s_barrier\n" \
  "ds_read_b128 v[36:39], v10 offset:0\n" \
  "ds_read_b128 v[40:43], v7 offset:0\n" \
  "ds_read_b128 v[44:47], v10 offset:2048\n" \
  "ds_read_b128 v[48:51], v7 offset:2048\n" \
  ".p2align 6\n" \
  ".LQB_AL_%=:\n" \
  ".LQB_TOP_%=:\n" \
  "s_setprio 3\n" \
  "s_waitcnt lgkmcnt(3)\n" \
  "v_mfma_f32_16x16x32_bf16 v[56:59], v[36:39], v[36:39], 0\n" \
  "ds_read_b128 v[52:55], v10 offset:4096\n" \
  "s_waitcnt lgkmcnt(3)\n" \
  "v_mfma_f32_16x16x32_bf16 v[12:15], v[40:43], v[192:195], 0\n" \
  "ds_read_b128 v[36:39], v7 offset:4096\n" \
  "s_waitcnt lgkmcnt(3)\n" \
  "v_mfma_f32_16x16x32_bf16 v[56:59], v[44:47], v[44:47], v[56:59]\n" \
  "ds_read_b128 v[40:43], v10 offset:6144\n" \
  "s_waitcnt lgkmcnt(3)\n" \
  "v_mfma_f32_16x16x32_bf16 v[12:15], v[48:51], v[196:199], v[12:15]\n" \
  "ds_read_b128 v[44:47], v7 offset:6144\n" \
  "s_waitcnt lgkmcnt(3)\n" \
  "v_mfma_f32_16x16x32_bf16 v[56:59], v[52:55], v[52:55], v[56:59]\n" \
  "ds_read_b128 v[48:51], v10 offset:8192\n" \
  "s_waitcnt lgkmcnt(3)\n" \
  "v_mfma_f32_16x16x32_bf16 v[12:15], v[36:39], v[200:203], v[12:15]\n" \
  "ds_read_b128 v[52:55], v7 offset:8192\n" \
  "s_waitcnt lgkmcnt(3)\n" \
  "v_mfma_f32_16x16x32_bf16 v[56:59], v[40:43], v[40:43], v[56:59]\n" \
  "ds_read_b128 v[36:39], v10 offset:10240\n" \
  "s_waitcnt lgkmcnt(3)\n" \
  "v_mfma_f32_16x16x32_bf16 v[12:15], v[44:47], v[204:207], v[12:15]\n" \
  "ds_read_b128 v[40:43], v7 offset:10240\n" \
  "s_waitcnt lgkmcnt(3)\n" \
  "v_mfma_f32_16x16x32_bf16 v[56:59], v[48:51], v[48:51], v[56:59]\n" \
  "ds_read_b128 v[44:47], v10 offset:12288\n" \
  "s_waitcnt lgkmcnt(3)\n" \
  "v_mfma_f32_16x16x32_bf16 v[12:15], v[52:55], v[208:211], v[12:15]\n" \
  "ds_read_b128 v[48:51], v7 offset:12288\n" \
  "s_waitcnt lgkmcnt(3)\n" \
  "v_mfma_f32_16x16x32_bf16 v[56:59], v[36:39], v[36:39], v[56:59]\n" \
  "ds_read_b128 v[52:55], v10 offset:14336\n" \
  "s_waitcnt lgkmcnt(3)\n" \
  "v_mfma_f32_16x16x32_bf16 v[12:15], v[40:43], v[212:215], v[12:15]\n" \
  "ds_read_b128 v[36:39], v7 offset:14336\n" \
  "s_waitcnt lgkmcnt(3)\n" \
  "v_mfma_f32_16x16x32_bf16 v[56:59], v[44:47], v[44:47], v[56:59]\n" \
  "ds_read_b128 v[40:43], v10 offset:16384\n" \
  "s_waitcnt lgkmcnt(3)\n" \
  "v_mfma_f32_16x16x32_bf16 v[12:15], v[48:51], v[216:219], v[12:15]\n" \
  "ds_read_b128 v[44:47], v7 offset:16384\n" \
  "s_waitcnt lgkmcnt(3)\n" \
  "v_mfma_f32_16x16x32_bf16 v[56:59], v[52:55], v[52:55], v[56:59]\n" \
  "ds_read_b128 v[48:51], v10 offset:18432\n" \
  "s_waitcnt lgkmcnt(3)\n" \
  "v_mfma_f32_16x16x32_bf16 v[12:15], v[36:39], v[220:223], v[12:15]\n" \
  "ds_read_b128 v[52:55], v7 offset:18432\n" \
  "s_waitcnt lgkmcnt(3)\n" \
  "v_mfma_f32_16x16x32_bf16 v[56:59], v[40:43], v[40:43], v[56:59]\n" \
  "ds_read_b128 v[36:39], v10 offset:20480\n" \
  "s_waitcnt lgkmcnt(3)\n" \
  "v_mfma_f32_16x16x32_bf16 v[12:15], v[44:47], v[224:227], v[12:15]\n" \
  "ds_read_b128 v[40:43], v7 offset:20480\n" \
  "s_waitcnt lgkmcnt(3)\n" \
  "v_mfma_f32_16x16x32_bf16 v[56:59], v[48:51], v[48:51], v[56:59]\n" \
  "ds_read_b128 v[44:47], v10 offset:22528\n" \
  "s_waitcnt lgkmcnt(3)\n" \
  "v_mfma_f32_16x16x32_bf16 v[12:15], v[52:55], v[228:231], v[12:15]\n" \
  "ds_read_b128 v[48:51], v7 offset:22528\n" \
  "s_waitcnt lgkmcnt(3)\n" \
  "v_mfma_f32_16x16x32_bf16 v[56:59], v[36:39], v[36:39], v[56:59]\n" \
  "ds_read_b128 v[52:55], v10 offset:24576\n" \
  "s_waitcnt lgkmcnt(3)\n" \
  "v_mfma_f32_16x16x32_bf16 v[12:15], v[40:43], v[232:235], v[12:15]\n" \
  "ds_read_b128 v[36:39], v7 offset:24576\n" \
  "s_waitcnt lgkmcnt(3)\n" \
  "v_mfma_f32_16x16x32_bf16 v[56:59], v[44:47], v[44:47], v[56:59]\n" \
  "ds_read_b128 v[40:43], v10 offset:26624\n" \
  "s_waitcnt lgkmcnt(3)\n" \
  "v_mfma_f32_16x16x32_bf16 v[12:15], v[48:51], v[236:239], v[12:15]\n" \
  "ds_read_b128 v[44:47], v7 offset:26624\n" \
  "s_waitcnt lgkmcnt(3)\n" \
  "v_mfma_f32_16x16x32_bf16 v[56:59], v[52:55], v[52:55], v[56:59]\n" \
  "ds_read_b128 v[48:51], v10 offset:28672\n" \
  "s_waitcnt lgkmcnt(3)\n" \
  "v_mfma_f32_16x16x32_bf16 v[12:15], v[36:39], v[240:243], v[12:15]\n" \
  "ds_read_b128 v[52:55], v7 offset:28672\n" \
  "s_waitcnt lgkmcnt(3)\n" \
  "v_mfma_f32_16x16x32_bf16 v[56:59], v[40:43], v[40:43], v[56:59]\n" \
  "ds_read_b128 v[36:39], v10 offset:30720\n" \
  "s_waitcnt lgkmcnt(3)\n" \
  "v_mfma_f32_16x16x32_bf16 v[12:15], v[44:47], v[244:247], v[12:15]\n" \
  "ds_read_b128 v[40:43], v7 offset:30720\n" \
  "s_waitcnt lgkmcnt(3)\n" \
  "v_mfma_f32_16x16x32_bf16 v[56:59], v[48:51], v[48:51], v[56:59]\n" \
  "ds_read_b128 v[44:47], v7 offset:256\n" \
  "s_waitcnt lgkmcnt(3)\n" \
  "v_mfma_f32_16x16x32_bf16 v[12:15], v[52:55], v[248:251], v[12:15]\n" \
  "ds_read_b128 v[48:51], v7 offset:2304\n" \
  "s_waitcnt lgkmcnt(3)\n" \
  "v_mfma_f32_16x16x32_bf16 v[56:59], v[36:39], v[36:39], v[56:59]\n" \
  "ds_read_b128 v[52:55], v7 offset:4352\n" \
  "s_waitcnt lgkmcnt(3)\n" \
  "v_mfma_f32_16x16x32_bf16 v[12:15], v[40:43], v[252:255], v[12:15]\n" \
  "ds_read_b128 v[36:39], v7 offset:6400\n" \
  "s_waitcnt lgkmcnt(3)\n" \
  "v_mfma_f32_16x16x32_bf16 v[16:19], v[44:47], v[192:195], 0\n" \
  "ds_read_b128 v[40:43], v7 offset:8448\n" \
  "s_waitcnt lgkmcnt(3)\n" \
  "v_mfma_f32_16x16x32_bf16 v[16:19], v[48:51], v[196:199], v[16:19]\n" \
  "ds_read_b128 v[44:47], v7 offset:10496\n" \
  "s_waitcnt lgkmcnt(3)\n" \
  "v_mfma_f32_16x16x32_bf16 v[16:19], v[52:55], v[200:203], v[16:19]\n" \
  "ds_read_b128 v[48:51], v7 offset:12544\n" \
  "s_waitcnt lgkmcnt(3)\n" \
  "v_mfma_f32_16x16x32_bf16 v[16:19], v[36:39], v[204:207], v[16:19]\n" \
  "ds_read_b128 v[52:55], v7 offset:14592\n" \
  "s_waitcnt lgkmcnt(3)\n" \
  "v_mfma_f32_16x16x32_bf16 v[16:19], v[40:43], v[208:211], v[16:19]\n" \
  "ds_read_b128 v[36:39], v7 offset:16640\n" \
  "s_waitcnt lgkmcnt(3)\n" \
  "v_mfma_f32_16x16x32_bf16 v[16:19], v[44:47], v[212:215], v[16:19]\n" \
  "ds_read_b128 v[40:43], v7 offset:18688\n" \
  "s_waitcnt lgkmcnt(3)\n" \
  "v_mfma_f32_16x16x32_bf16 v[16:19], v[48:51], v[216:219], v[16:19]\n" \
  "ds_read_b128 v[44:47], v7 offset:20736\n" \
  "s_waitcnt lgkmcnt(3)\n" \
  "v_mfma_f32_16x16x32_bf16 v[16:19], v[52:55], v[220:223], v[16:19]\n" \
  "ds_read_b128 v[48:51], v7 offset:22784\n" \
  "s_waitcnt lgkmcnt(3)\n" \
  "v_mfma_f32_16x16x32_bf16 v[16:19], v[36:39], v[224:227], v[16:19]\n" \
  "ds_read_b128 v[52:55], v7 offset:24832\n" \
  "s_waitcnt lgkmcnt(3)\n" \
  "v_mfma_f32_16x16x32_bf16 v[16:19], v[40:43], v[228:231], v[16:19]\n" \
  "ds_read_b128 v[36:39], v7 offset:26880\n" \
  "s_waitcnt lgkmcnt(3)\n" \
  "v_mfma_f32_16x16x32_bf16 v[16:19], v[44:47], v[232:235], v[16:19]\n" \
  "ds_read_b128 v[40:43], v7 offset:28928\n" \
  "s_waitcnt lgkmcnt(3)\n" \
  "v_mfma_f32_16x16x32_bf16 v[16:19], v[48:51], v[236:239], v[16:19]\n" \
  "ds_read_b128 v[44:47], v7 offset:30976\n" \
  "s_waitcnt lgkmcnt(3)\n" \
  "v_mfma_f32_16x16x32_bf16 v[16:19], v[52:55], v[240:243], v[16:19]\n" \
  "ds_read_b128 v[48:51], v7 offset:0\n" \
  "s_waitcnt lgkmcnt(3)\n" \
  "v_mfma_f32_16x16x32_bf16 v[16:19], v[36:39], v[244:247], v[16:19]\n" \
  "ds_read_b128 v[52:55], v7 offset:2048\n" \
  "s_waitcnt lgkmcnt(3)\n" \
  "v_mfma_f32_16x16x32_bf16 v[16:19], v[40:43], v[248:251], v[16:19]\n" \
  "ds_read_b128 v[36:39], v7 offset:4096\n" \
  "s_waitcnt lgkmcnt(3)\n" \
  "v_mfma_f32_16x16x32_bf16 v[16:19], v[44:47], v[252:255], v[16:19]\n" \
  "ds_read_b128 v[40:43], v7 offset:6144\n" \
  "s_setprio 0\n" \
  "v_cndmask_b32 v56, v56, v57, s[70:71]\n" \
  "v_cndmask_b32 v57, v58, v59, s[70:71]\n" \
  "v_cndmask_b32 v56, v56, v57, s[72:73]\n" \
  "v_mov_b32 v57, v56\n" \
  "v_mov_b32 v58, v56\n" \
  "v_mul_f32 v56, 0x3b000000, v56\n" \
  "v_xor_b32 v10, v11, v10\n" \
  "v_permlane32_swap_b32 v57, v58\n" \
  "v_mul_f32 v57, 0xbb000000, v58\n" \
  "v_fma_f32 v56, -v57, v57, v56\n" \
  "v_max_f32 v56, 0, v56\n" \
  "v_cvt_pk_bf16_f32 v56, v56, v56\n" \
  "v_lshlrev_b32 v56, 16, v56\n" \
  "v_add_f32 v56, 0x3727c5ac, v56\n" \
  "v_cvt_pk_bf16_f32 v56, v56, v56\n" \
  "v_lshlrev_b32 v56, 16, v56\n" \
  "v_sqrt_f32 v56, v56\n" \
  "v_cvt_pk_bf16_f32 v57, v57, v57\n" \
  "v_cvt_pk_bf16_f32 v56, v56, v56\n" \
  "v_lshlrev_b32 v56, 16, v56\n" \
  "v_rcp_f32 v56, v56\n" \
  "s_mov_b64 exec, 0xf0000f\n" \
  "ds_write2_b32 v62, v57, v56 offset1:32\n" \
  "s_mov_b64 exec, -1\n" \
  "s_cmp_eq_u32 s66, 63\n" \
  "s_cbranch_scc1 .LQB_NOE_%=\n" \
  "v_mul_f32 v20, v20, v60\n" \
  "v_mul_f32 v21, v21, v60\n" \
  "v_mul_f32 v22, v22, v60\n" \
  "v_cvt_pk_bf16_f32 v20, v20, v21\n" \
  "v_mul_f32 v23, v23, v60\n" \
  "v_mul_f32 v24, v24, v60\n" \
  "v_cvt_pk_bf16_f32 v21, v22, v23\n" \
  "v_mul_f32 v25, v25, v60\n" \
  "v_mul_f32 v26, v26, v60\n" \
  "v_cvt_pk_bf16_f32 v22, v24, v25\n" \
  "v_mul_f32 v27, v27, v60\n" \
  "v_cvt_pk_bf16_f32 v23, v26, v27\n" \
  "buffer_store_dwordx4 v[20:23], v6, s[40:43], s59 offen offset:0 sc1\n" \
  "s_add_u32 s76, s59, 4096\n" \
  "v_mul_f32 v28, v28, v61\n" \
  "v_mul_f32 v29, v29, v61\n" \
  "v_mul_f32 v30, v30, v61\n" \
  "v_cvt_pk_bf16_f32 v28, v28, v29\n" \
  "v_mul_f32 v31, v31, v61\n" \
  "v_mul_f32 v32, v32, v61\n" \
  "v_cvt_pk_bf16_f32 v29, v30, v31\n" \
  "v_mul_f32 v33, v33, v61\n" \
  "v_mul_f32 v34, v34, v61\n" \
  "v_cvt_pk_bf16_f32 v30, v32, v33\n" \
  "v_mul_f32 v35, v35, v61\n" \
  "v_cvt_pk_bf16_f32 v31, v34, v35\n" \
  "buffer_store_dwordx4 v[28:31], v6, s[40:43], s76 offen offset:0 sc1\n" \
  "s_add_u32 s59, s59, 0x80000\n" \
  ".LQB_NOE_%=:\n" \
  "s_waitcnt lgkmcnt(0)\n" \
  "s_barrier\n" \
  "ds_read2_b32 v[56:57], v8 offset0:0 offset1:0\n" \
  "ds_read2_b32 v[58:59], v8 offset0:16 offset1:16\n" \
  "ds_read_b128 v[28:31], v9 offset:128\n" \
  "ds_read_b128 v[32:35], v9 offset:192\n" \
  "ds_read2_b32 v[60:61], v8 offset0:32 offset1:48\n" \
  "s_setprio 3\n" \
  "s_waitcnt lgkmcnt(3)\n" \
  "v_mfma_f32_16x16x16_bf16 v[12:15], v[56:57], v[4:5], v[12:15]\n" \
  "v_mfma_f32_16x16x16_bf16 v[16:19], v[58:59], v[4:5], v[16:19]\n" \
  "s_setprio 0\n" \
  "s_waitcnt lgkmcnt(0)\n" \
  "s_nop 4\n" \
  "v_mul_f32 v12, v12, v28\n" \
  "v_mul_f32 v13, v13, v29\n" \
  "v_mul_f32 v14, v14, v30\n" \
  "v_mul_f32 v15, v15, v31\n" \
  "v_cvt_pk_bf16_f32 v12, v12, v13\n" \
  "v_cvt_pk_bf16_f32 v13, v14, v15\n" \
  "v_mul_f32 v16, v16, v32\n" \
  "v_mul_f32 v17, v17, v33\n" \
  "v_mul_f32 v18, v18, v34\n" \
  "v_mul_f32 v19, v19, v35\n" \
  "v_cvt_pk_bf16_f32 v14, v16, v17\n" \
  "v_cvt_pk_bf16_f32 v15, v18, v19\n" \
  "buffer_store_dwordx4 v[12:15], v6, s[44:47], s60 offen offset:0 sc1\n" \
  "v_mov_b64 v[16:17], 0\n" \
  "v_lshrrev_b32 v18, 1, v6\n" \
  "buffer_store_dwordx2 v[16:17], v18, s[48:51], s61 offen sc1\n" \
  "s_setprio 0\n" \
  "s_add_u32 s60, s60, 0x40000\n" \
  "s_add_u32 s61, s61, 0x10000\n" \
  "s_sub_u32 s66, s66, 1\n" \
  "v_mov_b32 v12, s80\n" \
  "v_mov_b32 v13, 1\n" \
  "v_mfma_f32_16x16x16_bf16 v[20:23], v[0:1], v[56:57], 0\n" \
  "v_mfma_f32_16x16x16_bf16 v[24:27], v[2:3], v[56:57], 0\n" \
  "v_mfma_f32_16x16x16_bf16 v[28:31], v[0:1], v[58:59], 0\n" \
  "v_mfma_f32_16x16x16_bf16 v[32:35], v[2:3], v[58:59], 0\n" \
  "v_mfma_f32_16x16x32_bf16 v[20:23], v[64:67], v[48:51], v[20:23]\n" \
  "ds_read_b128 v[44:47], v7 offset:8192\n" \
  "v_mfma_f32_16x16x32_bf16 v[24:27], v[128:131], v[48:51], v[24:27]\n" \
  "v_mfma_f32_16x16x32_bf16 v[20:23], v[68:71], v[52:55], v[20:23]\n" \
  "ds_read_b128 v[48:51], v7 offset:10240\n" \
  "v_mfma_f32_16x16x32_bf16 v[24:27], v[132:135], v[52:55], v[24:27]\n" \
  "v_mfma_f32_16x16x32_bf16 v[20:23], v[72:75], v[36:39], v[20:23]\n" \
  "ds_read_b128 v[52:55], v7 offset:12288\n" \
  "v_mfma_f32_16x16x32_bf16 v[24:27], v[136:139], v[36:39], v[24:27]\n" \
  "v_mfma_f32_16x16x32_bf16 v[20:23], v[76:79], v[40:43], v[20:23]\n" \
  "ds_read_b128 v[36:39], v7 offset:14336\n" \
  "v_mfma_f32_16x16x32_bf16 v[24:27], v[140:143], v[40:43], v[24:27]\n" \
  "s_waitcnt lgkmcnt(3)\n" \
  "v_mfma_f32_16x16x32_bf16 v[20:23], v[80:83], v[44:47], v[20:23]\n" \
  "ds_read_b128 v[40:43], v7 offset:16384\n" \
  "v_mfma_f32_16x16x32_bf16 v[24:27], v[144:147], v[44:47], v[24:27]\n" \
  "s_waitcnt lgkmcnt(3)\n" \
  "v_mfma_f32_16x16x32_bf16 v[20:23], v[84:87], v[48:51], v[20:23]\n" \
  "ds_read_b128 v[44:47], v7 offset:18432\n" \
  "v_mfma_f32_16x16x32_bf16 v[24:27], v[148:151], v[48:51], v[24:27]\n" \
  "s_waitcnt lgkmcnt(3)\n" \
  "v_mfma_f32_16x16x32_bf16 v[20:23], v[88:91], v[52:55], v[20:23]\n" \
  "ds_read_b128 v[48:51], v7 offset:20480\n" \
  "s_waitcnt vmcnt(0)\n" \
  "s_mov_b64 exec, 1\n" \
  "ds_add_u32 v12, v13\n" \
  "s_mov_b64 exec, -1\n" \
  "v_mfma_f32_16x16x32_bf16 v[24:27], v[152:155], v[52:55], v[24:27]\n" \
  "s_waitcnt lgkmcnt(4)\n" \
  "v_mfma_f32_16x16x32_bf16 v[20:23], v[92:95], v[36:39], v[20:23]\n" \
  "ds_read_b128 v[52:55], v7 offset:22528\n" \
  "v_mfma_f32_16x16x32_bf16 v[24:27], v[156:159], v[36:39], v[24:27]\n" \
  "s_waitcnt lgkmcnt(4)\n" \
  "v_mfma_f32_16x16x32_bf16 v[20:23], v[96:99], v[40:43], v[20:23]\n" \
  "ds_read_b128 v[36:39], v7 offset:24576\n" \
  "v_mfma_f32_16x16x32_bf16 v[24:27], v[160:163], v[40:43], v[24:27]\n" \
  "s_waitcnt lgkmcnt(4)\n" \
  "v_mfma_f32_16x16x32_bf16 v[20:23], v[100:103], v[44:47], v[20:23]\n" \
  "ds_read_b128 v[40:43], v7 offset:26624\n" \
  "v_mfma_f32_16x16x32_bf16 v[24:27], v[164:167], v[44:47], v[24:27]\n" \
  "s_waitcnt lgkmcnt(4)\n" \
  "v_mfma_f32_16x16x32_bf16 v[20:23], v[104:107], v[48:51], v[20:23]\n" \
  "ds_read_b128 v[44:47], v7 offset:28672\n" \
  "v_mfma_f32_16x16x32_bf16 v[24:27], v[168:171], v[48:51], v[24:27]\n" \
  "s_waitcnt lgkmcnt(3)\n" \
  "v_mfma_f32_16x16x32_bf16 v[20:23], v[108:111], v[52:55], v[20:23]\n" \
  "ds_read_b128 v[48:51], v7 offset:30720\n" \
  "v_mfma_f32_16x16x32_bf16 v[24:27], v[172:175], v[52:55], v[24:27]\n" \
  "s_waitcnt lgkmcnt(3)\n" \
  "v_mfma_f32_16x16x32_bf16 v[20:23], v[112:115], v[36:39], v[20:23]\n" \
  "ds_read_b128 v[52:55], v7 offset:256\n" \
  "v_mfma_f32_16x16x32_bf16 v[24:27], v[176:179], v[36:39], v[24:27]\n" \
  "s_waitcnt lgkmcnt(3)\n" \
  "v_mfma_f32_16x16x32_bf16 v[20:23], v[116:119], v[40:43], v[20:23]\n" \
  "ds_read_b128 v[36:39], v7 offset:2304\n" \
  "v_mfma_f32_16x16x32_bf16 v[24:27], v[180:183], v[40:43], v[24:27]\n" \
  "s_waitcnt lgkmcnt(3)\n" \
  "v_mfma_f32_16x16x32_bf16 v[20:23], v[120:123], v[44:47], v[20:23]\n" \
  "ds_read_b128 v[40:43], v7 offset:4352\n" \
  "v_mfma_f32_16x16x32_bf16 v[24:27], v[184:187], v[44:47], v[24:27]\n" \
  "s_waitcnt lgkmcnt(3)\n" \
  "v_mfma_f32_16x16x32_bf16 v[20:23], v[124:127], v[48:51], v[20:23]\n" \
  "ds_read_b128 v[44:47], v7 offset:6400\n" \
  "v_mfma_f32_16x16x32_bf16 v[24:27], v[188:191], v[48:51], v[24:27]\n" \
  "s_waitcnt lgkmcnt(3)\n" \
  "v_mfma_f32_16x16x32_bf16 v[28:31], v[64:67], v[52:55], v[28:31]\n" \
  "ds_read_b128 v[48:51], v7 offset:8448\n" \
  "v_mfma_f32_16x16x32_bf16 v[32:35], v[128:131], v[52:55], v[32:35]\n" \
  "s_waitcnt lgkmcnt(3)\n" \
  "v_mfma_f32_16x16x32_bf16 v[28:31], v[68:71], v[36:39], v[28:31]\n" \
  "ds_read_b128 v[52:55], v7 offset:10496\n" \
  "v_mfma_f32_16x16x32_bf16 v[32:35], v[132:135], v[36:39], v[32:35]\n" \
  "s_waitcnt lgkmcnt(3)\n" \
  "v_mfma_f32_16x16x32_bf16 v[28:31], v[72:75], v[40:43], v[28:31]\n" \
  "ds_read_b128 v[36:39], v7 offset:12544\n" \
  "v_mfma_f32_16x16x32_bf16 v[32:35], v[136:139], v[40:43], v[32:35]\n" \
  "s_waitcnt lgkmcnt(3)\n" \
  "v_mfma_f32_16x16x32_bf16 v[28:31], v[76:79], v[44:47], v[28:31]\n" \
  "ds_read_b128 v[40:43], v7 offset:14592\n" \
  "v_mfma_f32_16x16x32_bf16 v[32:35], v[140:143], v[44:47], v[32:35]\n" \
  "s_waitcnt lgkmcnt(3)\n" \
  "v_mfma_f32_16x16x32_bf16 v[28:31], v[80:83], v[48:51], v[28:31]\n" \
  "ds_read_b128 v[44:47], v7 offset:16640\n" \
  "v_mfma_f32_16x16x32_bf16 v[32:35], v[144:147], v[48:51], v[32:35]\n" \
  "s_waitcnt lgkmcnt(3)\n" \
  "v_mfma_f32_16x16x32_bf16 v[28:31], v[84:87], v[52:55], v[28:31]\n" \
  "ds_read_b128 v[48:51], v7 offset:18688\n" \
  "v_mfma_f32_16x16x32_bf16 v[32:35], v[148:151], v[52:55], v[32:35]\n" \
  "s_waitcnt lgkmcnt(3)\n" \
  "v_mfma_f32_16x16x32_bf16 v[28:31], v[88:91], v[36:39], v[28:31]\n" \
  "ds_read_b128 v[52:55], v7 offset:20736\n" \
  "v_mfma_f32_16x16x32_bf16 v[32:35], v[152:155], v[36:39], v[32:35]\n" \
  "s_waitcnt lgkmcnt(3)\n" \
  "v_mfma_f32_16x16x32_bf16 v[28:31], v[92:95], v[40:43], v[28:31]\n" \
  "ds_read_b128 v[36:39], v7 offset:22784\n" \
  "v_mfma_f32_16x16x32_bf16 v[32:35], v[156:159], v[40:43], v[32:35]\n" \
  "s_waitcnt lgkmcnt(3)\n" \
  "v_mfma_f32_16x16x32_bf16 v[28:31], v[96:99], v[44:47], v[28:31]\n" \
  "ds_read_b128 v[40:43], v7 offset:24832\n" \
  "v_mfma_f32_16x16x32_bf16 v[32:35], v[160:163], v[44:47], v[32:35]\n" \
  "s_waitcnt lgkmcnt(3)\n" \
  "v_mfma_f32_16x16x32_bf16 v[28:31], v[100:103], v[48:51], v[28:31]\n" \
  "ds_read_b128 v[44:47], v7 offset:26880\n" \
  "v_mfma_f32_16x16x32_bf16 v[32:35], v[164:167], v[48:51], v[32:35]\n" \
  "s_waitcnt lgkmcnt(3)\n" \
  "v_mfma_f32_16x16x32_bf16 v[28:31], v[104:107], v[52:55], v[28:31]\n" \
  "ds_read_b128 v[48:51], v7 offset:28928\n" \
  "v_mfma_f32_16x16x32_bf16 v[32:35], v[168:171], v[52:55], v[32:35]\n" \
  "s_waitcnt lgkmcnt(3)\n" \
  "v_mfma_f32_16x16x32_bf16 v[28:31], v[108:111], v[36:39], v[28:31]\n" \
  "ds_read_b128 v[52:55], v7 offset:30976\n" \
  "v_mfma_f32_16x16x32_bf16 v[32:35], v[172:175], v[36:39], v[32:35]\n" \
  "s_waitcnt lgkmcnt(3)\n" \
  "v_mfma_f32_16x16x32_bf16 v[28:31], v[112:115], v[40:43], v[28:31]\n" \
  "v_mfma_f32_16x16x32_bf16 v[32:35], v[176:179], v[40:43], v[32:35]\n" \
  "v_xor_b32 v7, 0x8000, v7\n" \
  "s_waitcnt lgkmcnt(0)\n" \
  "s_barrier\n" \
  "ds_read_b128 v[36:39], v10 offset:0\n" \
  "ds_read_b128 v[40:43], v7 offset:0\n" \
  "v_mfma_f32_16x16x32_bf16 v[28:31], v[116:119], v[44:47], v[28:31]\n" \
  "v_mfma_f32_16x16x32_bf16 v[32:35], v[180:183], v[44:47], v[32:35]\n" \
  "ds_read_b128 v[44:47], v10 offset:2048\n" \
  "v_mfma_f32_16x16x32_bf16 v[28:31], v[120:123], v[48:51], v[28:31]\n" \
  "v_mfma_f32_16x16x32_bf16 v[32:35], v[184:187], v[48:51], v[32:35]\n" \
  "ds_read_b128 v[48:51], v7 offset:2048\n" \
  "v_mfma_f32_16x16x32_bf16 v[28:31], v[124:127], v[52:55], v[28:31]\n" \
  "v_mfma_f32_16x16x32_bf16 v[32:35], v[188:191], v[52:55], v[32:35]\n" \
  "s_cbranch_scc1 .LQB_LAST_%=\n" \
  "s_branch .LQB_TOP_%=\n" \
  ".LQB_LAST_%=:\n" \
  "s_nop 7\n" \
  "v_mul_f32 v20, v20, v60\n" \
  "v_mul_f32 v21, v21, v60\n" \
  "v_mul_f32 v22, v22, v60\n" \
  "v_cvt_pk_bf16_f32 v20, v20, v21\n" \
  "v_mul_f32 v23, v23, v60\n" \
  "v_mul_f32 v24, v24, v60\n" \
  "v_cvt_pk_bf16_f32 v21, v22, v23\n" \
  "v_mul_f32 v25, v25, v60\n" \
  "v_mul_f32 v26, v26, v60\n" \
  "v_cvt_pk_bf16_f32 v22, v24, v25\n" \
  "v_mul_f32 v27, v27, v60\n" \
  "v_cvt_pk_bf16_f32 v23, v26, v27\n" \
  "buffer_store_dwordx4 v[20:23], v6, s[40:43], s59 offen offset:0 sc1\n" \
  "s_add_u32 s76, s59, 4096\n" \
  "v_mul_f32 v28, v28, v61\n" \
  "v_mul_f32 v29, v29, v61\n" \
  "v_mul_f32 v30, v30, v61\n" \
  "v_cvt_pk_bf16_f32 v28, v28, v29\n" \
  "v_mul_f32 v31, v31, v61\n" \
  "v_mul_f32 v32, v32, v61\n" \
  "v_cvt_pk_bf16_f32 v29, v30, v31\n" \
  "v_mul_f32 v33, v33, v61\n" \
  "v_mul_f32 v34, v34, v61\n" \
  "v_cvt_pk_bf16_f32 v30, v32, v33\n" \
  "v_mul_f32 v35, v35, v61\n" \
  "v_cvt_pk_bf16_f32 v31, v34, v35\n" \
  "buffer_store_dwordx4 v[28:31], v6, s[40:43], s76 offen offset:0 sc1\n" \
  "s_add_u32 s59, s59, 0x80000\n" \
  "s_waitcnt vmcnt(0)\n" \
  "v_mov_b32 v12, s80\n" \
  "v_mov_b32 v13, 1\n" \
  "s_mov_b64 exec, 1\n" \
  "ds_add_u32 v12, v13\n" \
  "s_mov_b64 exec, -1\n" \
  "s_waitcnt lgkmcnt(0)\n" \
  "s_endpgm\n"
#define AW_ASM_0 \
  "ds_write_b128 v7, v[8:11] offset:28672\n" \
  "ds_write_b128 v7, v[12:15] offset:29696\n" \
  ".p2align 6\n" \
  ".LWA_AL_%=:\n" \
  ".LWA_TOP_%=:\n" \
  "ds_read_b128 v[64:67], v7 offset:28672\n" \
  "ds_read_b128 v[68:71], v7 offset:29696\n" \
  "s_barrier\n" \
  "s_setprio 3\n" \
  "ds_read_b128 v[16:19], v4 offset:0\n" \
  "ds_read_b128 v[32:35], v3 offset:8192\n" \
  "ds_read_b128 v[48:51], v3 offset:12288\n" \
  "ds_read_b128 v[20:23], v4 offset:1024\n" \
  "ds_read_b128 v[36:39], v3 offset:9216\n" \
  "ds_read_b128 v[52:55], v3 offset:13312\n" \
  "ds_read_b128 v[24:27], v4 offset:2048\n" \
  "ds_read_b128 v[40:43], v3 offset:10240\n" \
  "ds_read_b128 v[56:59], v3 offset:14336\n" \
  "ds_read_b128 v[28:31], v4 offset:3072\n" \
  "ds_read_b128 v[44:47], v3 offset:11264\n" \
  "ds_read_b128 v[60:63], v3 offset:15360\n" \
  "s_waitcnt lgkmcnt(10)\n" \
  "v_mfma_f32_16x16x32_bf16 v[64:67], v[32:35], v[16:19], v[64:67]\n" \
  "ds_read_b128 v[72:75], v3 offset:16384\n" \
  "s_waitcnt lgkmcnt(10)\n" \
  "v_mfma_f32_16x16x32_bf16 v[68:71], v[48:51], v[16:19], v[68:71]\n" \
  "ds_read_b128 v[76:79], v3 offset:17408\n" \
  "s_waitcnt lgkmcnt(9)\n" \
  "v_mfma_f32_16x16x32_bf16 v[64:67], v[36:39], v[20:23], v[64:67]\n" \
  "ds_read_b128 v[80:83], v3 offset:18432\n" \
  "s_waitcnt lgkmcnt(9)\n" \
  "v_mfma_f32_16x16x32_bf16 v[68:71], v[52:55], v[20:23], v[68:71]\n" \
  "ds_read_b128 v[84:87], v3 offset:19456\n" \
  "s_waitcnt lgkmcnt(8)\n" \
  "v_mfma_f32_16x16x32_bf16 v[64:67], v[40:43], v[24:27], v[64:67]\n" \
  "ds_read_b128 v[88:91], v3 offset:20480\n" \
  "s_waitcnt lgkmcnt(8)\n" \
  "v_mfma_f32_16x16x32_bf16 v[68:71], v[56:59], v[24:27], v[68:71]\n" \
  "ds_read_b128 v[92:95], v3 offset:21504\n" \
  "s_waitcnt lgkmcnt(7)\n" \
  "v_mfma_f32_16x16x32_bf16 v[64:67], v[44:47], v[28:31], v[64:67]\n" \
  "ds_read_b128 v[96:99], v3 offset:22528\n" \
  "s_waitcnt lgkmcnt(7)\n" \
  "v_mfma_f32_16x16x32_bf16 v[68:71], v[60:63], v[28:31], v[68:71]\n" \
  "ds_read_b128 v[100:103], v3 offset:23552\n" \
  "s_add_u32 s56, s56, 3\n" \
  "s_sub_u32 s68, s56, %[wd]\n" \
  "s_bitcmp1_b32 s68, 0\n" \
  "s_cselect_b32 s58, 64, %[wd]\n" \
  "v_maximum3_f32 v10, v64, v65, v66\n" \
  "s_lshr_b32 s70, s68, 1\n" \
  "s_add_u32 s58, s58, s70\n" \
  "v_maximum3_f32 v10, v10, v67, v68\n" \
  "v_maximum3_f32 v10, v10, v69, v70\n" \
  "v_max_f32 v8, v10, v71\n" \
  "v_mov_b32 v9, v8\n" \
  "s_cmp_lt_u32 s68, %[w2]\n" \
  "s_cselect_b32 s58, s58, s56\n" \
  "v_permlane16_swap_b32 v8, v9\n" \
  "v_max_f32 v8, v8, v9\n" \
  "v_mov_b32 v9, v8\n" \
  "s_min_u32 s58, s58, 128\n" \
  "s_nop 0\n" \
  "v_permlane32_swap_b32 v8, v9\n" \
  "v_max_f32 v8, v8, v9\n" \
  "v_mul_f32 v10, s80, v8\n" \
  "v_pk_fma_f32 v[64:65], v[64:65], s[80:81], v[10:11] op_sel_hi:[1,0,0] neg_lo:[0,0,1] neg_hi:[0,0,1]\n" \
  "v_pk_fma_f32 v[66:67], v[66:67], s[80:81], v[10:11] op_sel_hi:[1,0,0] neg_lo:[0,0,1] neg_hi:[0,0,1]\n" \
  "v_pk_fma_f32 v[68:69], v[68:69], s[80:81], v[10:11] op_sel_hi:[1,0,0] neg_lo:[0,0,1] neg_hi:[0,0,1]\n" \
  "v_pk_fma_f32 v[70:71], v[70:71], s[80:81], v[10:11] op_sel_hi:[1,0,0] neg_lo:[0,0,1] neg_hi:[0,0,1]\n" \
  "v_exp_f32 v64, v64\n" \
  "v_exp_f32 v65, v65\n" \
  "v_exp_f32 v66, v66\n" \
  "v_exp_f32 v67, v67\n" \
  "v_exp_f32 v68, v68\n" \
  "v_exp_f32 v69, v69\n" \
  "v_exp_f32 v70, v70\n" \
  "v_exp_f32 v71, v71\n" \
  "v_cvt_pk_bf16_f32 v104, v64, v65\n" \
  "v_cvt_pk_bf16_f32 v105, v66, v67\n" \
  "v_cvt_pk_bf16_f32 v106, v68, v69\n" \
  "v_cvt_pk_bf16_f32 v107, v70, v71\n" \
  "v_pk_add_f32 v[64:65], v[64:65], v[66:67]\n" \
  "v_pk_add_f32 v[68:69], v[68:69], v[70:71]\n" \
  "s_waitcnt lgkmcnt(0)\n" \
  "v_pk_add_f32 v[64:65], v[64:65], v[68:69]\n" \
  "v_mfma_f32_16x16x32_bf16 v[32:35], v[72:75], v[104:107], 0\n" \
  "v_mfma_f32_16x16x32_bf16 v[48:51], v[88:91], v[104:107], 0\n" \
  "v_mfma_f32_16x16x32_bf16 v[36:39], v[76:79], v[104:107], 0\n" \
  "v_mfma_f32_16x16x32_bf16 v[52:55], v[92:95], v[104:107], 0\n" \
  "v_mfma_f32_16x16x32_bf16 v[40:43], v[80:83], v[104:107], 0\n" \
  "v_mfma_f32_16x16x32_bf16 v[56:59], v[96:99], v[104:107], 0\n" \
  "v_mfma_f32_16x16x32_bf16 v[44:47], v[84:87], v[104:107], 0\n" \
  "v_mfma_f32_16x16x32_bf16 v[60:63], v[100:103], v[104:107], 0\n" \
  "v_add_f32 v11, v64, v65\n" \
  "s_cmp_lt_u32 s56, %[w3]\n" \
  "s_cselect_b32 s68, s57, 0\n" \
  "s_cmp_ge_u32 s68, 64\n" \
  "s_cbranch_scc1 .LWA_EASY_%=\n" \
  "v_cvt_pk_bf16_f32 v108, v32, v33\n" \
  "v_cvt_pk_bf16_f32 v109, v48, v49\n" \
  "v_cvt_pk_bf16_f32 v110, v34, v35\n" \
  "v_cvt_pk_bf16_f32 v111, v50, v51\n" \
  "ds_write_b128 v7, v[108:111] offset:0\n" \
  "v_cvt_pk_bf16_f32 v112, v36, v37\n" \
  "v_cvt_pk_bf16_f32 v113, v52, v53\n" \
  "v_cvt_pk_bf16_f32 v114, v38, v39\n" \
  "v_cvt_pk_bf16_f32 v115, v54, v55\n" \
  "ds_write_b128 v7, v[112:115] offset:1024\n" \
  "v_cvt_pk_bf16_f32 v116, v40, v41\n" \
  "v_cvt_pk_bf16_f32 v117, v56, v57\n" \
  "v_cvt_pk_bf16_f32 v118, v42, v43\n" \
  "v_cvt_pk_bf16_f32 v119, v58, v59\n" \
  "ds_write_b128 v7, v[116:119] offset:2048\n" \
  "v_cvt_pk_bf16_f32 v120, v44, v45\n" \
  "v_cvt_pk_bf16_f32 v121, v60, v61\n" \
  "v_cvt_pk_bf16_f32 v122, v46, v47\n" \
  "v_cvt_pk_bf16_f32 v123, v62, v63\n" \
  "ds_write_b128 v7, v[120:123] offset:3072\n" \
  "s_waitcnt vmcnt(0)\n" \
  "s_waitcnt lgkmcnt(0)\n" \
  "s_barrier\n" \
  "s_setprio 2\n" \
  "ds_read_b128 v[48:51], v1 offset:0\n" \
  "ds_read_b128 v[52:55], v1 offset:1024\n" \
  "ds_read_b128 v[56:59], v1 offset:2048\n" \
  "s_waitcnt lgkmcnt(2)\n" \
  "v_mfma_f32_16x16x32_bf16 v[64:67], v[128:131], v[48:51], 0\n" \
  "ds_read_b128 v[60:63], v1 offset:3072\n" \
  "s_lshl_b32 s75, s57, 19\n" \
  "v_mfma_f32_16x16x32_bf16 v[68:71], v[160:163], v[48:51], 0\n" \
  "s_add_u32 s75, s75, s60\n" \
  "v_mfma_f32_16x16x32_bf16 v[72:75], v[192:195], v[48:51], 0\n" \
  "s_add_u32 s76, s75, 16384\n" \
  "v_mfma_f32_16x16x32_bf16 v[76:79], v[224:227], v[48:51], 0\n" \
  "s_waitcnt lgkmcnt(2)\n" \
  "v_mfma_f32_16x16x32_bf16 v[64:67], v[132:135], v[52:55], v[64:67]\n" \
  "ds_read_b128 v[48:51], v1 offset:8192\n" \
  "v_mfma_f32_16x16x32_bf16 v[68:71], v[164:167], v[52:55], v[68:71]\n" \
  "v_mfma_f32_16x16x32_bf16 v[72:75], v[196:199], v[52:55], v[72:75]\n" \
  "v_mfma_f32_16x16x32_bf16 v[76:79], v[228:231], v[52:55], v[76:79]\n" \
  "s_waitcnt lgkmcnt(2)\n" \
  "v_mfma_f32_16x16x32_bf16 v[64:67], v[136:139], v[56:59], v[64:67]\n" \
  "ds_read_b128 v[52:55], v1 offset:9216\n" \
  "v_mfma_f32_16x16x32_bf16 v[68:71], v[168:171], v[56:59], v[68:71]\n" \
  "v_mfma_f32_16x16x32_bf16 v[72:75], v[200:203], v[56:59], v[72:75]\n" \
  "v_mfma_f32_16x16x32_bf16 v[76:79], v[232:235], v[56:59], v[76:79]\n" \
  "s_waitcnt lgkmcnt(2)\n" \
  "v_mfma_f32_16x16x32_bf16 v[64:67], v[140:143], v[60:63], v[64:67]\n" \
  "ds_read_b128 v[56:59], v1 offset:10240\n" \
  "v_mfma_f32_16x16x32_bf16 v[68:71], v[172:175], v[60:63], v[68:71]\n" \
  "v_mfma_f32_16x16x32_bf16 v[72:75], v[204:207], v[60:63], v[72:75]\n" \
  "v_mfma_f32_16x16x32_bf16 v[76:79], v[236:239], v[60:63], v[76:79]\n" \
  "s_waitcnt lgkmcnt(2)\n" \
  "v_mfma_f32_16x16x32_bf16 v[96:99], v[144:147], v[48:51], 0\n" \
  "ds_read_b128 v[60:63], v1 offset:11264\n" \
  "v_mfma_f32_16x16x32_bf16 v[100:103], v[176:179], v[48:51], 0\n" \
  "v_mfma_f32_16x16x32_bf16 v[104:107], v[208:211], v[48:51], 0\n" \
  "v_mfma_f32_16x16x32_bf16 v[108:111], v[240:243], v[48:51], 0\n" \
  "s_waitcnt lgkmcnt(2)\n" \
  "v_mfma_f32_16x16x32_bf16 v[96:99], v[148:151], v[52:55], v[96:99]\n" \
  "ds_read_b128 v[48:51], v1 offset:4096\n" \
  "v_mfma_f32_16x16x32_bf16 v[100:103], v[180:183], v[52:55], v[100:103]\n" \
  "v_mfma_f32_16x16x32_bf16 v[104:107], v[212:215], v[52:55], v[104:107]\n" \
  "v_mfma_f32_16x16x32_bf16 v[108:111], v[244:247], v[52:55], v[108:111]\n" \
  "s_waitcnt lgkmcnt(2)\n" \
  "v_mfma_f32_16x16x32_bf16 v[96:99], v[152:155], v[56:59], v[96:99]\n" \
  "ds_read_b128 v[52:55], v1 offset:5120\n" \
  "v_mfma_f32_16x16x32_bf16 v[100:103], v[184:187], v[56:59], v[100:103]\n" \
  "v_mfma_f32_16x16x32_bf16 v[104:107], v[216:219], v[56:59], v[104:107]\n" \
  "v_mfma_f32_16x16x32_bf16 v[108:111], v[248:251], v[56:59], v[108:111]\n" \
  "s_waitcnt lgkmcnt(2)\n" \
  "v_mfma_f32_16x16x32_bf16 v[96:99], v[156:159], v[60:63], v[96:99]\n" \
  "ds_read_b128 v[56:59], v1 offset:6144\n" \
  "v_mfma_f32_16x16x32_bf16 v[100:103], v[188:191], v[60:63], v[100:103]\n" \
  "v_mfma_f32_16x16x32_bf16 v[104:107], v[220:223], v[60:63], v[104:107]\n" \
  "v_mfma_f32_16x16x32_bf16 v[108:111], v[252:255], v[60:63], v[108:111]\n" \
  "s_waitcnt lgkmcnt(2)\n" \
  "v_mfma_f32_16x16x32_bf16 v[80:83], v[128:131], v[48:51], 0\n" \
  "ds_read_b128 v[60:63], v1 offset:7168\n" \
  "v_mfma_f32_16x16x32_bf16 v[84:87], v[160:163], v[48:51], 0\n" \
  "v_mfma_f32_16x16x32_bf16 v[88:91], v[192:195], v[48:51], 0\n" \
  "v_mfma_f32_16x16x32_bf16 v[92:95], v[224:227], v[48:51], 0\n" \
  "s_waitcnt lgkmcnt(2)\n" \
  "v_mfma_f32_16x16x32_bf16 v[80:83], v[132:135], v[52:55], v[80:83]\n" \
  "ds_read_b128 v[48:51], v1 offset:12288\n" \
  "v_mfma_f32_16x16x32_bf16 v[84:87], v[164:167], v[52:55], v[84:87]\n" \
  "v_mfma_f32_16x16x32_bf16 v[88:91], v[196:199], v[52:55], v[88:91]\n" \
  "v_mfma_f32_16x16x32_bf16 v[92:95], v[228:231], v[52:55], v[92:95]\n" \
  "s_waitcnt lgkmcnt(2)\n" \
  "v_mfma_f32_16x16x32_bf16 v[80:83], v[136:139], v[56:59], v[80:83]\n" \
  "ds_read_b128 v[52:55], v1 offset:13312\n" \
  "v_mfma_f32_16x16x32_bf16 v[84:87], v[168:171], v[56:59], v[84:87]\n" \
  "v_mfma_f32_16x16x32_bf16 v[88:91], v[200:203], v[56:59], v[88:91]\n" \
  "v_mfma_f32_16x16x32_bf16 v[92:95], v[232:235], v[56:59], v[92:95]\n" \
  "s_waitcnt lgkmcnt(2)\n" \
  "v_mfma_f32_16x16x32_bf16 v[80:83], v[140:143], v[60:63], v[80:83]\n" \
  "ds_read_b128 v[56:59], v1 offset:14336\n" \
  "v_mfma_f32_16x16x32_bf16 v[84:87], v[172:175], v[60:63], v[84:87]\n" \
  "v_mfma_f32_16x16x32_bf16 v[88:91], v[204:207], v[60:63], v[88:91]\n" \
  "v_mfma_f32_16x16x32_bf16 v[92:95], v[236:239], v[60:63], v[92:95]\n" \
  "s_waitcnt lgkmcnt(2)\n" \
  "v_mfma_f32_16x16x32_bf16 v[112:115], v[144:147], v[48:51], 0\n" \
  "ds_read_b128 v[60:63], v1 offset:15360\n" \
  "v_mfma_f32_16x16x32_bf16 v[116:119], v[176:179], v[48:51], 0\n" \
  "v_mfma_f32_16x16x32_bf16 v[120:123], v[208:211], v[48:51], 0\n" \
  "v_mfma_f32_16x16x32_bf16 v[124:127], v[240:243], v[48:51], 0\n" \
  "s_waitcnt lgkmcnt(2)\n" \
  "v_mfma_f32_16x16x32_bf16 v[112:115], v[148:151], v[52:55], v[112:115]\n" \
  "v_mfma_f32_16x16x32_bf16 v[116:119], v[180:183], v[52:55], v[116:119]\n" \
  "v_mfma_f32_16x16x32_bf16 v[120:123], v[212:215], v[52:55], v[120:123]\n" \
  "v_mfma_f32_16x16x32_bf16 v[124:127], v[244:247], v[52:55], v[124:127]\n" \
  "s_waitcnt lgkmcnt(1)\n" \
  "v_mfma_f32_16x16x32_bf16 v[112:115], v[152:155], v[56:59], v[112:115]\n" \
  "v_mfma_f32_16x16x32_bf16 v[116:119], v[184:187], v[56:59], v[116:119]\n" \
  "v_mfma_f32_16x16x32_bf16 v[120:123], v[216:219], v[56:59], v[120:123]\n" \
  "v_mfma_f32_16x16x32_bf16 v[124:127], v[248:251], v[56:59], v[124:127]\n" \
  "s_waitcnt lgkmcnt(0)\n" \
  "v_mfma_f32_16x16x32_bf16 v[112:115], v[156:159], v[60:63], v[112:115]\n" \
  "v_mfma_f32_16x16x32_bf16 v[116:119], v[188:191], v[60:63], v[116:119]\n" \
  "v_mfma_f32_16x16x32_bf16 v[120:123], v[220:223], v[60:63], v[120:123]\n" \
  "v_mfma_f32_16x16x32_bf16 v[124:127], v[252:255], v[60:63], v[124:127]\n" \
  "s_setprio 3\n" \
  "s_barrier\n" \
  "ds_read_b128 v[32:35], v5 offset:0\n" \
  "ds_read_b128 v[36:39], v5 offset:512\n" \
  "ds_read_b128 v[40:43], v5 offset:1024\n" \
  "ds_read_b128 v[44:47], v5 offset:1536\n" \
  "ds_read_b64_tr_b16 v[48:49], v6 offset:0\n" \
  "ds_read_b64_tr_b16 v[50:51], v6 offset:32\n" \
  "ds_read_b64_tr_b16 v[52:53], v6 offset:1024\n" \
  "ds_read_b64_tr_b16 v[54:55], v6 offset:1056\n" \
  "ds_read_b64_tr_b16 v[56:57], v6 offset:4096\n" \
  "ds_read_b64_tr_b16 v[58:59], v6 offset:4128\n" \
  "ds_read_b64_tr_b16 v[60:61], v6 offset:5120\n" \
  "ds_read_b64_tr_b16 v[62:63], v6 offset:5152\n" \
  "s_waitcnt lgkmcnt(11)\n" \
  "v_mfma_f32_16x16x32_bf16 v[12:15], v[32:35], v[16:19], 0\n" \
  "s_waitcnt lgkmcnt(10)\n" \
  "v_mfma_f32_16x16x32_bf16 v[12:15], v[36:39], v[20:23], v[12:15]\n" \
  "s_waitcnt lgkmcnt(9)\n" \
  "v_mfma_f32_16x16x32_bf16 v[12:15], v[40:43], v[24:27], v[12:15]\n" \
  "s_waitcnt lgkmcnt(8)\n" \
  "v_mfma_f32_16x16x32_bf16 v[12:15], v[44:47], v[28:31], v[12:15]\n" \
  "v_mov_b64 v[16:17], 0\n" \
  "v_mov_b64 v[18:19], 0\n" \
  "v_mov_b64 v[20:21], 0\n" \
  "v_mov_b64 v[22:23], 0\n" \
  "v_mov_b64 v[24:25], 0\n" \
  "v_mov_b64 v[26:27], 0\n" \
  "v_mov_b64 v[28:29], 0\n" \
  "v_mov_b64 v[30:31], 0\n" \
  "v_maximum3_f32 v8, v8, v12, v13\n" \
  "v_mov_b32 v9, v8\n" \
  "v_mov_b64 v[32:33], 0\n" \
  "v_mov_b64 v[34:35], 0\n" \
  "v_permlane16_swap_b32 v8, v9\n" \
  "v_max_f32 v8, v8, v9\n" \
  "v_mov_b32 v9, v8\n" \
  "v_mov_b64 v[36:37], 0\n" \
  "v_mov_b64 v[38:39], 0\n" \
  "v_permlane32_swap_b32 v8, v9\n" \
  "v_max_f32 v8, v8, v9\n" \
  "v_mul_f32 v8, s80, v8\n" \
  "v_sub_f32 v14, v10, v8\n" \
  "v_fma_f32 v12, v12, s80, -v8\n" \
  "v_fma_f32 v13, v13, s80, -v8\n" \
  "v_exp_f32 v14, v14\n" \
  "v_exp_f32 v12, v12\n" \
  "v_exp_f32 v13, v13\n" \
  "v_fma_f32 v11, v11, v14, v12\n" \
  "v_add_f32 v11, v11, v13\n" \
  "v_mov_b32 v9, v11\n" \
  "v_mov_b64 v[40:41], 0\n" \
  "v_mov_b64 v[42:43], 0\n" \
  "v_permlane16_swap_b32 v11, v9\n" \
  "v_add_f32 v11, v11, v9\n" \
  "v_mov_b32 v9, v11\n" \
  "v_mov_b64 v[44:45], 0\n" \
  "v_mov_b64 v[46:47], 0\n" \
  "v_permlane32_swap_b32 v11, v9\n" \
  "v_add_f32 v11, v11, v9\n" \
  "v_rcp_f32 v15, v11\n" \
  "v_add_u32 v9, s66, v1\n" \
  "v_mul_f32 v12, v12, v15\n" \
  "v_mul_f32 v13, v13, v15\n" \
  "v_mul_f32 v14, v14, v15\n" \
  "v_cvt_pk_bf16_f32 v12, v12, v13\n" \
  "ds_write2_b32 v9, v12, v14 offset1:2\n" \
  "s_waitcnt lgkmcnt(0)\n" \
  "s_barrier\n" \
  "ds_read_b128 v[8:11], v1 offset:24576\n" \
  "ds_read_b128 v[12:15], v1 offset:25600\n" \
  "s_setprio 0\n" \
  "v_smfmac_f32_16x16x64_bf16 v[16:19], v[48:51], v[128:135], v0\n" \
  "v_smfmac_f32_16x16x64_bf16 v[20:23], v[48:51], v[160:167], v0\n" \
  "v_smfmac_f32_16x16x64_bf16 v[24:27], v[48:51], v[192:199], v0\n" \
  "v_smfmac_f32_16x16x64_bf16 v[28:31], v[48:51], v[224:231], v0\n" \
  "v_smfmac_f32_16x16x64_bf16 v[16:19], v[52:55], v[136:143], v0\n" \
  "v_smfmac_f32_16x16x64_bf16 v[20:23], v[52:55], v[168:175], v0\n" \
  "v_smfmac_f32_16x16x64_bf16 v[24:27], v[52:55], v[200:207], v0\n" \
  "v_smfmac_f32_16x16x64_bf16 v[28:31], v[52:55], v[232:239], v0\n" \
  "v_smfmac_f32_16x16x64_bf16 v[32:35], v[56:59], v[144:151], v0\n" \
  "s_waitcnt lgkmcnt(0)\n" \
  "v_pk_mul_f32 v[64:65], v[64:65], v[10:11] op_sel_hi:[1,0]\n" \
  "v_pk_mul_f32 v[66:67], v[66:67], v[10:11] op_sel_hi:[1,0]\n" \
  "v_smfmac_f32_16x16x64_bf16 v[36:39], v[56:59], v[176:183], v0\n" \
  "v_pk_mul_f32 v[68:69], v[68:69], v[10:11] op_sel_hi:[1,0]\n" \
  "v_pk_mul_f32 v[70:71], v[70:71], v[10:11] op_sel_hi:[1,0]\n" \
  "v_pk_fma_f32 v[64:65], v[96:97], v[10:11], v[64:65] op_sel:[0,1,0]\n" \
  "v_smfmac_f32_16x16x64_bf16 v[40:43], v[56:59], v[208:215], v0\n" \
  "v_pk_fma_f32 v[66:67], v[98:99], v[10:11], v[66:67] op_sel:[0,1,0]\n" \
  "v_pk_fma_f32 v[68:69], v[100:101], v[10:11], v[68:69] op_sel:[0,1,0]\n" \
  "v_pk_fma_f32 v[70:71], v[102:103], v[10:11], v[70:71] op_sel:[0,1,0]\n" \
  "v_smfmac_f32_16x16x64_bf16 v[44:47], v[56:59], v[240:247], v0\n" \
  "v_pk_mul_f32 v[72:73], v[72:73], v[10:11] op_sel_hi:[1,0]\n" \
  "v_pk_mul_f32 v[74:75], v[74:75], v[10:11] op_sel_hi:[1,0]\n" \
  "v_pk_mul_f32 v[76:77], v[76:77], v[10:11] op_sel_hi:[1,0]\n" \
  "v_smfmac_f32_16x16x64_bf16 v[32:35], v[60:63], v[152:159], v0\n" \
  "v_pk_mul_f32 v[78:79], v[78:79], v[10:11] op_sel_hi:[1,0]\n" \
  "v_pk_fma_f32 v[72:73], v[104:105], v[10:11], v[72:73] op_sel:[0,1,0]\n" \
  "v_pk_fma_f32 v[74:75], v[106:107], v[10:11], v[74:75] op_sel:[0,1,0]\n" \
  "v_smfmac_f32_16x16x64_bf16 v[36:39], v[60:63], v[184:191], v0\n" \
  "v_pk_fma_f32 v[76:77], v[108:109], v[10:11], v[76:77] op_sel:[0,1,0]\n" \
  "v_pk_fma_f32 v[78:79], v[110:111], v[10:11], v[78:79] op_sel:[0,1,0]\n" \
  "v_pk_mul_f32 v[80:81], v[80:81], v[14:15] op_sel_hi:[1,0]\n" \
  "v_smfmac_f32_16x16x64_bf16 v[40:43], v[60:63], v[216:223], v0\n" \
  "v_pk_mul_f32 v[82:83], v[82:83], v[14:15] op_sel_hi:[1,0]\n" \
  "v_pk_mul_f32 v[84:85], v[84:85], v[14:15] op_sel_hi:[1,0]\n" \
  "v_pk_mul_f32 v[86:87], v[86:87], v[14:15] op_sel_hi:[1,0]\n" \
  "v_smfmac_f32_16x16x64_bf16 v[44:47], v[60:63], v[248:255], v0\n" \
  "v_pk_fma_f32 v[80:81], v[112:113], v[14:15], v[80:81] op_sel:[0,1,0]\n" \
  "v_pk_fma_f32 v[82:83], v[114:115], v[14:15], v[82:83] op_sel:[0,1,0]\n" \
  "v_pk_fma_f32 v[84:85], v[116:117], v[14:15], v[84:85] op_sel:[0,1,0]\n" \
  "v_pk_fma_f32 v[86:87], v[118:119], v[14:15], v[86:87] op_sel:[0,1,0]\n" \
  "v_pk_mul_f32 v[88:89], v[88:89], v[14:15] op_sel_hi:[1,0]\n" \
  "v_pk_mul_f32 v[90:91], v[90:91], v[14:15] op_sel_hi:[1,0]\n" \
  "v_pk_mul_f32 v[92:93], v[92:93], v[14:15] op_sel_hi:[1,0]\n" \
  "v_pk_mul_f32 v[94:95], v[94:95], v[14:15] op_sel_hi:[1,0]\n" \
  "v_pk_fma_f32 v[88:89], v[120:121], v[14:15], v[88:89] op_sel:[0,1,0]\n" \
  "v_pk_fma_f32 v[90:91], v[122:123], v[14:15], v[90:91] op_sel:[0,1,0]\n" \
  "v_pk_fma_f32 v[92:93], v[124:125], v[14:15], v[92:93] op_sel:[0,1,0]\n" \
  "v_pk_fma_f32 v[94:95], v[126:127], v[14:15], v[94:95] op_sel:[0,1,0]\n" \
  "v_pk_add_f32 v[16:17], v[16:17], v[18:19]\n" \
  "v_pk_add_f32 v[32:33], v[32:33], v[34:35]\n" \
  "v_cvt_pk_bf16_f32 v16, v16, v17\n" \
  "v_cvt_pk_bf16_f32 v17, v32, v33\n" \
  "v_pk_add_f32 v[20:21], v[20:21], v[22:23]\n" \
  "v_pk_add_f32 v[36:37], v[36:37], v[38:39]\n" \
  "v_cvt_pk_bf16_f32 v20, v20, v21\n" \
  "v_cvt_pk_bf16_f32 v21, v36, v37\n" \
  "v_pk_add_f32 v[24:25], v[24:25], v[26:27]\n" \
  "v_pk_add_f32 v[40:41], v[40:41], v[42:43]\n" \
  "v_mfma_f32_16x16x16_bf16 v[64:67], v[16:17], v[8:9], v[64:67]\n" \
  "v_mfma_f32_16x16x16_bf16 v[68:71], v[20:21], v[8:9], v[68:71]\n" \
  "v_cvt_pk_bf16_f32 v24, v24, v25\n" \
  "v_cvt_pk_bf16_f32 v25, v40, v41\n" \
  "v_mfma_f32_16x16x16_bf16 v[80:83], v[16:17], v[12:13], v[80:83]\n" \
  "v_mfma_f32_16x16x16_bf16 v[84:87], v[20:21], v[12:13], v[84:87]\n" \
  "v_pk_add_f32 v[28:29], v[28:29], v[30:31]\n" \
  "v_pk_add_f32 v[44:45], v[44:45], v[46:47]\n" \
  "v_cvt_pk_bf16_f32 v28, v28, v29\n" \
  "v_cvt_pk_bf16_f32 v29, v44, v45\n" \
  "s_nop 1\n" \
  "v_mfma_f32_16x16x16_bf16 v[72:75], v[24:25], v[8:9], v[72:75]\n" \
  "v_mfma_f32_16x16x16_bf16 v[76:79], v[28:29], v[8:9], v[76:79]\n" \
  "v_mfma_f32_16x16x16_bf16 v[88:91], v[24:25], v[12:13], v[88:91]\n" \
  "v_mfma_f32_16x16x16_bf16 v[92:95], v[28:29], v[12:13], v[92:95]\n" \
  "v_cvt_pk_bf16_f32 v64, v64, v65\n" \
  "v_cvt_pk_bf16_f32 v65, v66, v67\n" \
  "v_cvt_pk_bf16_f32 v66, v68, v69\n" \
  "v_cvt_pk_bf16_f32 v67, v70, v71\n" \
  "buffer_store_dwordx4 v[64:67], v2, s[40:43], s75 offen offset:0 sc1\n" \
  "v_cvt_pk_bf16_f32 v80, v80, v81\n" \
  "v_cvt_pk_bf16_f32 v81, v82, v83\n" \
  "v_cvt_pk_bf16_f32 v82, v84, v85\n" \
  "v_cvt_pk_bf16_f32 v83, v86, v87\n" \
  "buffer_store_dwordx4 v[80:83], v2, s[40:43], s76 offen offset:0 sc1\n" \
  "v_cvt_pk_bf16_f32 v72, v72, v73\n" \
  "v_cvt_pk_bf16_f32 v73, v74, v75\n" \
  "v_cvt_pk_bf16_f32 v74, v76, v77\n" \
  "v_cvt_pk_bf16_f32 v75, v78, v79\n" \
  "buffer_store_dwordx4 v[72:75], v2, s[40:43], s75 offen offset:128 sc1\n" \
  "v_cvt_pk_bf16_f32 v88, v88, v89\n" \
  "v_cvt_pk_bf16_f32 v89, v90, v91\n" \
  "v_cvt_pk_bf16_f32 v90, v92, v93\n" \
  "v_cvt_pk_bf16_f32 v91, v94, v95\n" \
  "buffer_store_dwordx4 v[88:91], v2, s[40:43], s76 offen offset:128 sc1\n" \
  ".LWA_NEXT_%=:\n" \
  "v_xor_b32 v3, 0x10000, v3\n" \
  "v_xor_b32 v4, 0x10000, v4\n" \
  "s_mov_b32 s57, s58\n" \
  "s_cmp_eq_u32 s57, 128\n" \
  "s_cbranch_scc1 .LWA_END_%=\n" \
  "s_branch .LWA_TOP_%=\n" \
  ".LWA_END_%=:\n" \
  "s_endpgm\n" \
  ".LWA_EASY_%=:\n" \
  "v_lshrrev_b32 v9, 1, v1\n" \
  "v_mov_b32 v105, 0\n" \
  "v_add_u32 v64, s63, v9\n" \
  "v_mov_b32 v106, 0\n" \
  "s_barrier\n" \
  "ds_read_b128 v[72:75], v5 offset:0\n" \
  "ds_read_b128 v[76:79], v5 offset:512\n" \
  "ds_read_b128 v[80:83], v5 offset:1024\n" \
  "ds_read_b128 v[84:87], v5 offset:1536\n" \
  "ds_read_b64 v[88:89], v64 offset:0\n" \
  "ds_read_b64 v[90:91], v64 offset:512\n" \
  "ds_read_b64 v[92:93], v64 offset:1024\n" \
  "ds_read_b64 v[94:95], v64 offset:1536\n" \
  "s_waitcnt lgkmcnt(7)\n" \
  "v_mfma_f32_16x16x32_bf16 v[64:67], v[72:75], v[16:19], 0\n" \
  "s_waitcnt lgkmcnt(6)\n" \
  "v_mfma_f32_16x16x32_bf16 v[64:67], v[76:79], v[20:23], v[64:67]\n" \
  "s_waitcnt lgkmcnt(5)\n" \
  "v_mfma_f32_16x16x32_bf16 v[64:67], v[80:83], v[24:27], v[64:67]\n" \
  "s_waitcnt lgkmcnt(4)\n" \
  "v_mfma_f32_16x16x32_bf16 v[64:67], v[84:87], v[28:31], v[64:67]\n" \
  "s_nop 7\n" \
  "v_maximum3_f32 v8, v8, v64, v65\n" \
  "v_mov_b32 v9, v8\n" \
  "s_nop 1\n" \
  "v_permlane16_swap_b32 v8, v9\n" \
  "v_max_f32 v8, v8, v9\n" \
  "v_mov_b32 v9, v8\n" \
  "s_nop 1\n" \
  "v_permlane32_swap_b32 v8, v9\n" \
  "v_max_f32 v8, v8, v9\n" \
  "v_mul_f32 v8, s80, v8\n" \
  "v_sub_f32 v66, v10, v8\n" \
  "v_fma_f32 v64, v64, s80, -v8\n" \
  "v_fma_f32 v65, v65, s80, -v8\n" \
  "v_exp_f32 v66, v66\n" \
  "v_exp_f32 v64, v64\n" \
  "v_exp_f32 v65, v65\n" \
  "v_fma_f32 v11, v11, v66, v64\n" \
  "v_add_f32 v11, v11, v65\n" \
  "v_mov_b32 v9, v11\n" \
  "s_nop 1\n" \
  "v_permlane16_swap_b32 v11, v9\n" \
  "v_add_f32 v11, v11, v9\n" \
  "v_mov_b32 v9, v11\n" \
  "s_nop 1\n" \
  "v_permlane32_swap_b32 v11, v9\n" \
  "v_add_f32 v11, v11, v9\n" \
  "v_rcp_f32 v68, v11\n" \
  "s_waitcnt lgkmcnt(0)\n" \
  "v_mul_f32 v66, v66, v68\n" \
  "v_mul_f32 v64, v64, v68\n" \
  "v_mul_f32 v65, v65, v68\n" \
  "v_pk_mul_f32 v[32:33], v[32:33], v[66:67] op_sel_hi:[1,0]\n" \
  "v_cvt_pk_bf16_f32 v104, v64, v65\n" \
  "v_pk_mul_f32 v[34:35], v[34:35], v[66:67] op_sel_hi:[1,0]\n" \
  "v_cvt_pk_bf16_f32 v107, v64, v65\n" \
  "v_mul_f32 v48, v48, v66\n" \
  "v_mul_f32 v49, v49, v66\n" \
  "v_mul_f32 v50, v50, v66\n" \
  "v_mul_f32 v51, v51, v66\n" \
  "v_mfma_f32_16x16x16_bf16 v[32:35], v[88:89], v[104:105], v[32:35]\n" \
  "v_mul_f32 v36, v36, v66\n" \
  "v_mul_f32 v37, v37, v66\n" \
  "v_mul_f32 v38, v38, v66\n" \
  "v_mul_f32 v39, v39, v66\n" \
  "v_mfma_f32_16x16x16_bf16 v[48:51], v[88:89], v[106:107], v[48:51]\n" \
  "v_mul_f32 v52, v52, v66\n" \
  "v_mul_f32 v53, v53, v66\n" \
  "v_mul_f32 v54, v54, v66\n" \
  "v_mul_f32 v55, v55, v66\n" \
  "v_mfma_f32_16x16x16_bf16 v[36:39], v[90:91], v[104:105], v[36:39]\n" \
  "v_mul_f32 v40, v40, v66\n" \
  "v_mul_f32 v41, v41, v66\n" \
  "v_mul_f32 v42, v42, v66\n" \
  "v_mul_f32 v43, v43, v66\n" \
  "v_mfma_f32_16x16x16_bf16 v[52:55], v[90:91], v[106:107], v[52:55]\n" \
  "v_mul_f32 v56, v56, v66\n" \
  "v_mul_f32 v57, v57, v66\n" \
  "v_mul_f32 v58, v58, v66\n" \
  "v_mul_f32 v59, v59, v66\n" \
  "v_mfma_f32_16x16x16_bf16 v[40:43], v[92:93], v[104:105], v[40:43]\n" \
  "v_mul_f32 v44, v44, v66\n" \
  "v_mul_f32 v45, v45, v66\n" \
  "v_mul_f32 v46, v46, v66\n" \
  "v_mul_f32 v47, v47, v66\n" \
  "v_mfma_f32_16x16x16_bf16 v[56:59], v[92:93], v[106:107], v[56:59]\n" \
  "v_mul_f32 v60, v60, v66\n" \
  "v_mul_f32 v61, v61, v66\n" \
  "v_mul_f32 v62, v62, v66\n" \
  "v_mul_f32 v63, v63, v66\n" \
  "v_mfma_f32_16x16x16_bf16 v[44:47], v[94:95], v[104:105], v[44:47]\n" \
  "v_cvt_pk_bf16_f32 v108, v32, v33\n" \
  "v_cvt_pk_bf16_f32 v109, v48, v49\n" \
  "v_cvt_pk_bf16_f32 v110, v34, v35\n" \
  "v_cvt_pk_bf16_f32 v111, v50, v51\n" \
  "ds_write_b128 v7, v[108:111] offset:0\n" \
  "v_mfma_f32_16x16x16_bf16 v[60:63], v[94:95], v[106:107], v[60:63]\n" \
  "v_cvt_pk_bf16_f32 v112, v36, v37\n" \
  "v_cvt_pk_bf16_f32 v113, v52, v53\n" \
  "v_cvt_pk_bf16_f32 v114, v38, v39\n" \
  "v_cvt_pk_bf16_f32 v115, v54, v55\n" \
  "ds_write_b128 v7, v[112:115] offset:1024\n" \
  "v_cvt_pk_bf16_f32 v116, v40, v41\n" \
  "v_cvt_pk_bf16_f32 v117, v56, v57\n" \
  "v_cvt_pk_bf16_f32 v118, v42, v43\n" \
  "v_cvt_pk_bf16_f32 v119, v58, v59\n" \
  "ds_write_b128 v7, v[116:119] offset:2048\n" \
  "v_cvt_pk_bf16_f32 v120, v44, v45\n" \
  "v_cvt_pk_bf16_f32 v121, v60, v61\n" \
  "v_cvt_pk_bf16_f32 v122, v46, v47\n" \
  "v_cvt_pk_bf16_f32 v123, v62, v63\n" \
  "ds_write_b128 v7, v[120:123] offset:3072\n" \
  "s_waitcnt lgkmcnt(0)\n" \
  "s_barrier\n" \
  "s_setprio 2\n" \
  "ds_read_b128 v[48:51], v1 offset:0\n" \
  "ds_read_b128 v[52:55], v1 offset:1024\n" \
  "ds_read_b128 v[56:59], v1 offset:2048\n" \
  "s_waitcnt lgkmcnt(2)\n" \
  "v_mfma_f32_16x16x32_bf16 v[64:67], v[128:131], v[48:51], 0\n" \
  "ds_read_b128 v[60:63], v1 offset:3072\n" \
  "s_lshl_b32 s75, s57, 19\n" \
  "v_mfma_f32_16x16x32_bf16 v[68:71], v[160:163], v[48:51], 0\n" \
  "s_add_u32 s75, s75, s60\n" \
  "v_mfma_f32_16x16x32_bf16 v[72:75], v[192:195], v[48:51], 0\n" \
  "s_add_u32 s76, s75, 16384\n" \
  "v_mfma_f32_16x16x32_bf16 v[76:79], v[224:227], v[48:51], 0\n" \
  "s_waitcnt lgkmcnt(2)\n" \
  "v_mfma_f32_16x16x32_bf16 v[64:67], v[132:135], v[52:55], v[64:67]\n" \
  "ds_read_b128 v[48:51], v1 offset:8192\n" \
  "v_mfma_f32_16x16x32_bf16 v[68:71], v[164:167], v[52:55], v[68:71]\n" \
  "v_mfma_f32_16x16x32_bf16 v[72:75], v[196:199], v[52:55], v[72:75]\n" \
  "v_mfma_f32_16x16x32_bf16 v[76:79], v[228:231], v[52:55], v[76:79]\n" \
  "s_waitcnt lgkmcnt(2)\n" \
  "v_mfma_f32_16x16x32_bf16 v[64:67], v[136:139], v[56:59], v[64:67]\n" \
  "ds_read_b128 v[52:55], v1 offset:9216\n" \
  "v_mfma_f32_16x16x32_bf16 v[68:71], v[168:171], v[56:59], v[68:71]\n" \
  "v_mfma_f32_16x16x32_bf16 v[72:75], v[200:203], v[56:59], v[72:75]\n" \
  "v_mfma_f32_16x16x32_bf16 v[76:79], v[232:235], v[56:59], v[76:79]\n" \
  "s_waitcnt lgkmcnt(2)\n" \
  "v_mfma_f32_16x16x32_bf16 v[64:67], v[140:143], v[60:63], v[64:67]\n" \
  "ds_read_b128 v[56:59], v1 offset:10240\n" \
  "v_mfma_f32_16x16x32_bf16 v[68:71], v[172:175], v[60:63], v[68:71]\n" \
  "v_mfma_f32_16x16x32_bf16 v[72:75], v[204:207], v[60:63], v[72:75]\n" \
  "v_mfma_f32_16x16x32_bf16 v[76:79], v[236:239], v[60:63], v[76:79]\n" \
  "s_waitcnt lgkmcnt(2)\n" \
  "v_mfma_f32_16x16x32_bf16 v[64:67], v[144:147], v[48:51], v[64:67]\n" \
  "ds_read_b128 v[60:63], v1 offset:11264\n" \
  "v_mfma_f32_16x16x32_bf16 v[68:71], v[176:179], v[48:51], v[68:71]\n" \
  "v_mfma_f32_16x16x32_bf16 v[72:75], v[208:211], v[48:51], v[72:75]\n" \
  "v_mfma_f32_16x16x32_bf16 v[76:79], v[240:243], v[48:51], v[76:79]\n" \
  "s_waitcnt lgkmcnt(2)\n" \
  "v_mfma_f32_16x16x32_bf16 v[64:67], v[148:151], v[52:55], v[64:67]\n" \
  "ds_read_b128 v[48:51], v1 offset:4096\n" \
  "v_mfma_f32_16x16x32_bf16 v[68:71], v[180:183], v[52:55], v[68:71]\n" \
  "v_mfma_f32_16x16x32_bf16 v[72:75], v[212:215], v[52:55], v[72:75]\n" \
  "v_mfma_f32_16x16x32_bf16 v[76:79], v[244:247], v[52:55], v[76:79]\n" \
  "s_waitcnt lgkmcnt(2)\n" \
  "v_mfma_f32_16x16x32_bf16 v[64:67], v[152:155], v[56:59], v[64:67]\n" \
  "ds_read_b128 v[52:55], v1 offset:5120\n" \
  "v_mfma_f32_16x16x32_bf16 v[68:71], v[184:187], v[56:59], v[68:71]\n" \
  "v_mfma_f32_16x16x32_bf16 v[72:75], v[216:219], v[56:59], v[72:75]\n" \
  "v_mfma_f32_16x16x32_bf16 v[76:79], v[248:251], v[56:59], v[76:79]\n" \
  "s_waitcnt lgkmcnt(2)\n" \
  "v_mfma_f32_16x16x32_bf16 v[64:67], v[156:159], v[60:63], v[64:67]\n" \
  "ds_read_b128 v[56:59], v1 offset:6144\n" \
  "v_mfma_f32_16x16x32_bf16 v[68:71], v[188:191], v[60:63], v[68:71]\n" \
  "v_mfma_f32_16x16x32_bf16 v[72:75], v[220:223], v[60:63], v[72:75]\n" \
  "v_mfma_f32_16x16x32_bf16 v[76:79], v[252:255], v[60:63], v[76:79]\n" \
  "s_waitcnt lgkmcnt(2)\n" \
  "v_mfma_f32_16x16x32_bf16 v[80:83], v[128:131], v[48:51], 0\n" \
  "ds_read_b128 v[60:63], v1 offset:7168\n" \
  "v_mfma_f32_16x16x32_bf16 v[84:87], v[160:163], v[48:51], 0\n" \
  "v_mfma_f32_16x16x32_bf16 v[88:91], v[192:195], v[48:51], 0\n" \
  "v_mfma_f32_16x16x32_bf16 v[92:95], v[224:227], v[48:51], 0\n" \
  "s_waitcnt lgkmcnt(2)\n" \
  "v_mfma_f32_16x16x32_bf16 v[80:83], v[132:135], v[52:55], v[80:83]\n" \
  "ds_read_b128 v[48:51], v1 offset:12288\n" \
  "v_mfma_f32_16x16x32_bf16 v[84:87], v[164:167], v[52:55], v[84:87]\n" \
  "v_cvt_pk_bf16_f32 v64, v64, v65\n" \
  "v_mfma_f32_16x16x32_bf16 v[88:91], v[196:199], v[52:55], v[88:91]\n" \
  "v_cvt_pk_bf16_f32 v65, v66, v67\n" \
  "v_mfma_f32_16x16x32_bf16 v[92:95], v[228:231], v[52:55], v[92:95]\n" \
  "s_waitcnt lgkmcnt(2)\n" \
  "v_mfma_f32_16x16x32_bf16 v[80:83], v[136:139], v[56:59], v[80:83]\n" \
  "ds_read_b128 v[52:55], v1 offset:13312\n" \
  "v_mfma_f32_16x16x32_bf16 v[84:87], v[168:171], v[56:59], v[84:87]\n" \
  "v_cvt_pk_bf16_f32 v66, v68, v69\n" \
  "v_mfma_f32_16x16x32_bf16 v[88:91], v[200:203], v[56:59], v[88:91]\n" \
  "v_cvt_pk_bf16_f32 v67, v70, v71\n" \
  "v_mfma_f32_16x16x32_bf16 v[92:95], v[232:235], v[56:59], v[92:95]\n" \
  "s_waitcnt lgkmcnt(2)\n" \
  "v_mfma_f32_16x16x32_bf16 v[80:83], v[140:143], v[60:63], v[80:83]\n" \
  "ds_read_b128 v[56:59], v1 offset:14336\n" \
  "v_mfma_f32_16x16x32_bf16 v[84:87], v[172:175], v[60:63], v[84:87]\n" \
  "buffer_store_dwordx4 v[64:67], v2, s[40:43], s75 offen offset:0 sc1\n" \
  "v_mfma_f32_16x16x32_bf16 v[88:91], v[204:207], v[60:63], v[88:91]\n" \
  "v_cvt_pk_bf16_f32 v72, v72, v73\n" \
  "v_mfma_f32_16x16x32_bf16 v[92:95], v[236:239], v[60:63], v[92:95]\n" \
  "s_waitcnt lgkmcnt(2)\n" \
  "v_mfma_f32_16x16x32_bf16 v[80:83], v[144:147], v[48:51], v[80:83]\n" \
  "ds_read_b128 v[60:63], v1 offset:15360\n" \
  "v_mfma_f32_16x16x32_bf16 v[84:87], v[176:179], v[48:51], v[84:87]\n" \
  "v_cvt_pk_bf16_f32 v73, v74, v75\n" \
  "v_mfma_f32_16x16x32_bf16 v[88:91], v[208:211], v[48:51], v[88:91]\n" \
  "v_cvt_pk_bf16_f32 v74, v76, v77\n" \
  "v_mfma_f32_16x16x32_bf16 v[92:95], v[240:243], v[48:51], v[92:95]\n" \
  "s_waitcnt lgkmcnt(2)\n" \
  "v_mfma_f32_16x16x32_bf16 v[80:83], v[148:151], v[52:55], v[80:83]\n" \
  "v_mfma_f32_16x16x32_bf16 v[84:87], v[180:183], v[52:55], v[84:87]\n" \
  "v_cvt_pk_bf16_f32 v75, v78, v79\n" \
  "v_mfma_f32_16x16x32_bf16 v[88:91], v[212:215], v[52:55], v[88:91]\n" \
  "buffer_store_dwordx4 v[72:75], v2, s[40:43], s75 offen offset:128 sc1\n" \
  "v_mfma_f32_16x16x32_bf16 v[92:95], v[244:247], v[52:55], v[92:95]\n" \
  "s_waitcnt lgkmcnt(1)\n" \
  "v_mfma_f32_16x16x32_bf16 v[80:83], v[152:155], v[56:59], v[80:83]\n" \
  "v_mfma_f32_16x16x32_bf16 v[84:87], v[184:187], v[56:59], v[84:87]\n" \
  "v_mfma_f32_16x16x32_bf16 v[88:91], v[216:219], v[56:59], v[88:91]\n" \
  "v_mfma_f32_16x16x32_bf16 v[92:95], v[248:251], v[56:59], v[92:95]\n" \
  "s_waitcnt lgkmcnt(0)\n" \
  "v_mfma_f32_16x16x32_bf16 v[80:83], v[156:159], v[60:63], v[80:83]\n" \
  "v_mfma_f32_16x16x32_bf16 v[84:87], v[188:191], v[60:63], v[84:87]\n" \
  "v_mfma_f32_16x16x32_bf16 v[88:91], v[220:223], v[60:63], v[88:91]\n" \
  "v_mfma_f32_16x16x32_bf16 v[92:95], v[252:255], v[60:63], v[92:95]\n" \
  "s_nop 4\n" \
  "v_cvt_pk_bf16_f32 v80, v80, v81\n" \
  "v_cvt_pk_bf16_f32 v81, v82, v83\n" \
  "v_cvt_pk_bf16_f32 v82, v84, v85\n" \
  "v_cvt_pk_bf16_f32 v83, v86, v87\n" \
  "buffer_store_dwordx4 v[80:83], v2, s[40:43], s76 offen offset:0 sc1\n" \
  "v_cvt_pk_bf16_f32 v88, v88, v89\n" \
  "v_cvt_pk_bf16_f32 v89, v90, v91\n" \
  "v_cvt_pk_bf16_f32 v90, v92, v93\n" \
  "v_cvt_pk_bf16_f32 v91, v94, v95\n" \
  "buffer_store_dwordx4 v[88:91], v2, s[40:43], s76 offen offset:128 sc1\n" \
  "s_branch .LWA_NEXT_%=\n"
#define AW_ASM_1 \
  "v_lshlrev_b32 v7, 5, v0\n" \
  "s_mov_b32 s79, -1\n" \
  "s_cmp_eq_u32 s67, 0\n" \
  "s_cbranch_scc1 .LWB_LATE_%=\n" \
  "s_waitcnt vmcnt(0)\n" \
  ".p2align 6\n" \
  ".LWB_AL_%=:\n" \
  ".LWB_TOP_%=:\n" \
  "s_barrier\n" \
  "s_add_u32 s56, s56, 3\n" \
  "s_sub_u32 s68, s56, %[wd]\n" \
  "s_bitcmp1_b32 s68, 0\n" \
  "s_cselect_b32 s58, 64, %[wd]\n" \
  "s_lshr_b32 s70, s68, 1\n" \
  "s_add_u32 s58, s58, s70\n" \
  "s_cmp_lt_u32 s68, %[w2]\n" \
  "s_cselect_b32 s58, s58, s56\n" \
  "s_min_u32 s58, s58, 128\n" \
  "s_lshr_b32 s68, s58, 6\n" \
  "s_add_u32 s68, s68, s58\n" \
  "s_lshl_b32 s68, s68, 12\n" \
  "buffer_load_dword v5, off, s[44:47], s68 sc1\n" \
  "s_xor_b32 s61, s61, 0x10000\n" \
  "s_xor_b32 s62, s62, 0x10000\n" \
  "s_cmp_lt_u32 s57, 64\n" \
  "s_cselect_b32 s75, s64, s65\n" \
  "s_mul_i32 s76, s57, %[gp]\n" \
  "s_add_u32 s74, s75, s76\n" \
  "s_mov_b32 s67, 0\n" \
  "s_mov_b32 s77, 0\n" \
  "s_cmp_lt_u32 s56, %[w3]\n" \
  "s_cselect_b32 s68, s57, 0\n" \
  "s_cmp_ge_u32 s68, 64\n" \
  "s_cbranch_scc1 .LWB_GATEE_%=\n" \
  "s_lshl_b32 s59, s57, 19\n" \
  "s_add_u32 s59, s59, s66\n" \
  "s_add_u32 s78, s59, 4096\n" \
  "v_mov_b64 v[8:9], -1\n" \
  "v_mov_b64 v[10:11], -1\n" \
  "buffer_store_dwordx4 v[8:11], v0, s[40:43], s59 offen offset:0 sc1\n" \
  "buffer_store_dwordx4 v[8:11], v0, s[40:43], s59 offen offset:1024 sc1\n" \
  "buffer_store_dwordx4 v[8:11], v0, s[40:43], s59 offen offset:2048 sc1\n" \
  "buffer_store_dwordx4 v[8:11], v0, s[40:43], s59 offen offset:3072 sc1\n" \
  "buffer_store_dwordx4 v[8:11], v0, s[40:43], s78 offen offset:0 sc1\n" \
  "buffer_store_dwordx4 v[8:11], v0, s[40:43], s78 offen offset:1024 sc1\n" \
  "v_mov_b64 v[16:17], 0\n" \
  "v_mov_b64 v[18:19], 0\n" \
  "v_mov_b64 v[20:21], 0\n" \
  "v_mov_b64 v[22:23], 0\n" \
  "v_mov_b64 v[24:25], 0\n" \
  "v_mov_b64 v[26:27], 0\n" \
  "v_mov_b64 v[28:29], 0\n" \
  "v_mov_b64 v[30:31], 0\n" \
  "v_mov_b64 v[32:33], 0\n" \
  "v_mov_b64 v[34:35], 0\n" \
  "v_mov_b64 v[36:37], 0\n" \
  "v_mov_b64 v[38:39], 0\n" \
  "v_mov_b64 v[40:41], 0\n" \
  "v_mov_b64 v[42:43], 0\n" \
  "v_mov_b64 v[44:45], 0\n" \
  "v_mov_b64 v[46:47], 0\n" \
  "s_cmp_lt_i32 s79, 0\n" \
  "s_waitcnt vmcnt(6)\n" \
  "s_barrier\n" \
  "s_cbranch_scc1 .LWB_NOD_%=\n" \
  "s_memrealtime s[68:69]\n" \
  "s_lshl_b32 s72, s79, 11\n" \
  "s_add_u32 s72, s72, s54\n" \
  "s_addc_u32 s73, s55, 0\n" \
  "s_waitcnt lgkmcnt(0)\n" \
  "s_add_u32 s70, s68, 0\n" \
  "s_or_b32 s70, s70, 1\n" \
  "v_mov_b32 v13, s70\n" \
  "s_mov_b64 exec, s[82:83]\n" \
  "global_store_dword v7, v13, s[72:73] sc1\n" \
  "s_mov_b64 exec, -1\n" \
  "s_mov_b32 s79, -1\n" \
  ".LWB_NOD_%=:\n" \
  "s_setprio 0\n" \
  "buffer_store_dwordx4 v[8:11], v0, s[40:43], s78 offen offset:2048 sc1\n" \
  "buffer_store_dwordx4 v[8:11], v0, s[40:43], s78 offen offset:3072 sc1\n" \
  "v_cmp_eq_u32 vcc, 0, v5\n" \
  "s_cmp_eq_u64 vcc, 0\n" \
  "s_cbranch_scc0 .LWB_NOT_%=\n" \
  "s_mov_b32 m0, s61\n" \
  "s_lshl_b32 s68, s58, 19\n" \
  "s_add_u32 s68, s68, s48\n" \
  "s_addc_u32 s69, s49, 0\n" \
  "global_load_lds_dwordx4 v0, s[68:69] offset:0 sc1\n" \
  "global_load_lds_dwordx4 v0, s[68:69] offset:1024 sc1\n" \
  "global_load_lds_dwordx4 v0, s[68:69] offset:2048 sc1\n" \
  "global_load_lds_dwordx4 v0, s[68:69] offset:3072 sc1\n" \
  "s_add_u32 m0, s61, 4096\n" \
  "s_add_u32 s70, s68, 4096\n" \
  "s_addc_u32 s71, s69, 0\n" \
  "global_load_lds_dwordx4 v0, s[70:71] offset:0 sc1\n" \
  "global_load_lds_dwordx4 v0, s[70:71] offset:1024 sc1\n" \
  "global_load_lds_dwordx4 v0, s[70:71] offset:2048 sc1\n" \
  "global_load_lds_dwordx4 v0, s[70:71] offset:3072 sc1\n" \
  "s_mov_b32 m0, s62\n" \
  "s_lshl_b32 s72, s58, 18\n" \
  "s_add_u32 s72, s72, s50\n" \
  "s_addc_u32 s73, s51, 0\n" \
  "global_load_lds_dwordx4 v0, s[72:73] offset:0 sc1\n" \
  "global_load_lds_dwordx4 v0, s[72:73] offset:1024 sc1\n" \
  "global_load_lds_dwordx4 v0, s[72:73] offset:2048 sc1\n" \
  "global_load_lds_dwordx4 v0, s[72:73] offset:3072 sc1\n" \
  "s_mov_b32 s67, 1\n" \
  ".LWB_NOT_%=:\n" \
  "ds_read_b128 v[48:51], v1 offset:0\n" \
  "ds_read_b128 v[52:55], v1 offset:1024\n" \
  "ds_read_b128 v[56:59], v1 offset:2048\n" \
  "s_waitcnt lgkmcnt(2)\n" \
  "v_mfma_f32_16x16x32_bf16 v[64:67], v[128:131], v[48:51], 0\n" \
  "ds_read_b128 v[60:63], v1 offset:3072\n" \
  "v_mfma_f32_16x16x32_bf16 v[68:71], v[160:163], v[48:51], 0\n" \
  "v_mfma_f32_16x16x32_bf16 v[72:75], v[192:195], v[48:51], 0\n" \
  "v_mfma_f32_16x16x32_bf16 v[76:79], v[224:227], v[48:51], 0\n" \
  "s_waitcnt lgkmcnt(2)\n" \
  "v_mfma_f32_16x16x32_bf16 v[64:67], v[132:135], v[52:55], v[64:67]\n" \
  "ds_read_b128 v[48:51], v1 offset:8192\n" \
  "v_mfma_f32_16x16x32_bf16 v[68:71], v[164:167], v[52:55], v[68:71]\n" \
  "v_mfma_f32_16x16x32_bf16 v[72:75], v[196:199], v[52:55], v[72:75]\n" \
  "v_mfma_f32_16x16x32_bf16 v[76:79], v[228:231], v[52:55], v[76:79]\n" \
  "s_waitcnt lgkmcnt(2)\n" \
  "v_mfma_f32_16x16x32_bf16 v[64:67], v[136:139], v[56:59], v[64:67]\n" \
  "ds_read_b128 v[52:55], v1 offset:9216\n" \
  "s_memrealtime s[68:69]\n" \
  "v_mfma_f32_16x16x32_bf16 v[68:71], v[168:171], v[56:59], v[68:71]\n" \
  "v_mfma_f32_16x16x32_bf16 v[72:75], v[200:203], v[56:59], v[72:75]\n" \
  "v_mfma_f32_16x16x32_bf16 v[76:79], v[232:235], v[56:59], v[76:79]\n" \
  "s_waitcnt lgkmcnt(2)\n" \
  "v_mfma_f32_16x16x32_bf16 v[64:67], v[140:143], v[60:63], v[64:67]\n" \
  "ds_read_b128 v[56:59], v1 offset:10240\n" \
  "s_waitcnt lgkmcnt(0)\n" \
  "s_sub_i32 s70, s74, s68\n" \
  "s_cmp_gt_i32 s70, 0\n" \
  "s_cbranch_scc1 .LWB_NOTYET_%=\n" \
  "s_mov_b32 m0, s63\n" \
  "s_lshl_b32 s72, s57, 16\n" \
  "s_add_u32 s72, s72, s52\n" \
  "s_addc_u32 s73, s53, 0\n" \
  "global_load_lds_dwordx4 v0, s[72:73] offset:0 sc1\n" \
  "global_load_lds_dwordx4 v0, s[72:73] offset:1024 sc1\n" \
  "s_mov_b32 s77, 1\n" \
  ".LWB_NOTYET_%=:\n" \
  "s_lshl_b32 s70, s67, 7\n" \
  "s_or_b32 s70, s70, s58\n" \
  "s_lshr_b32 s68, s70, 6\n" \
  "s_add_u32 s68, s68, s70\n" \
  "s_lshl_b32 s68, s68, 12\n" \
  "buffer_load_dword v5, off, s[44:47], s68 sc1\n" \
  "v_mfma_f32_16x16x32_bf16 v[68:71], v[172:175], v[60:63], v[68:71]\n" \
  "v_mfma_f32_16x16x32_bf16 v[72:75], v[204:207], v[60:63], v[72:75]\n" \
  "v_mfma_f32_16x16x32_bf16 v[76:79], v[236:239], v[60:63], v[76:79]\n" \
  "s_waitcnt lgkmcnt(2)\n" \
  "v_mfma_f32_16x16x32_bf16 v[96:99], v[144:147], v[48:51], 0\n" \
  "ds_read_b128 v[60:63], v1 offset:11264\n" \
  "v_mfma_f32_16x16x32_bf16 v[100:103], v[176:179], v[48:51], 0\n" \
  "v_mfma_f32_16x16x32_bf16 v[104:107], v[208:211], v[48:51], 0\n" \
  "v_mfma_f32_16x16x32_bf16 v[108:111], v[240:243], v[48:51], 0\n" \
  "s_waitcnt lgkmcnt(2)\n" \
  "v_mfma_f32_16x16x32_bf16 v[96:99], v[148:151], v[52:55], v[96:99]\n" \
  "ds_read_b128 v[48:51], v1 offset:4096\n" \
  "v_mfma_f32_16x16x32_bf16 v[100:103], v[180:183], v[52:55], v[100:103]\n" \
  "v_mfma_f32_16x16x32_bf16 v[104:107], v[212:215], v[52:55], v[104:107]\n" \
  "v_mfma_f32_16x16x32_bf16 v[108:111], v[244:247], v[52:55], v[108:111]\n" \
  "s_waitcnt lgkmcnt(2)\n" \
  "v_mfma_f32_16x16x32_bf16 v[96:99], v[152:155], v[56:59], v[96:99]\n" \
  "ds_read_b128 v[52:55], v1 offset:5120\n" \
  "v_mfma_f32_16x16x32_bf16 v[100:103], v[184:187], v[56:59], v[100:103]\n" \
  "v_mfma_f32_16x16x32_bf16 v[104:107], v[216:219], v[56:59], v[104:107]\n" \
  "v_mfma_f32_16x16x32_bf16 v[108:111], v[248:251], v[56:59], v[108:111]\n" \
  "s_waitcnt lgkmcnt(2)\n" \
  "v_mfma_f32_16x16x32_bf16 v[96:99], v[156:159], v[60:63], v[96:99]\n" \
  "ds_read_b128 v[56:59], v1 offset:6144\n" \
  "v_mfma_f32_16x16x32_bf16 v[100:103], v[188:191], v[60:63], v[100:103]\n" \
  "v_mfma_f32_16x16x32_bf16 v[104:107], v[220:223], v[60:63], v[104:107]\n" \
  "v_mfma_f32_16x16x32_bf16 v[108:111], v[252:255], v[60:63], v[108:111]\n" \
  "s_waitcnt lgkmcnt(2)\n" \
  "v_mfma_f32_16x16x32_bf16 v[80:83], v[128:131], v[48:51], 0\n" \
  "ds_read_b128 v[60:63], v1 offset:7168\n" \
  "v_mfma_f32_16x16x32_bf16 v[84:87], v[160:163], v[48:51], 0\n" \
  "v_mfma_f32_16x16x32_bf16 v[88:91], v[192:195], v[48:51], 0\n" \
  "v_mfma_f32_16x16x32_bf16 v[92:95], v[224:227], v[48:51], 0\n" \
  "s_waitcnt lgkmcnt(2)\n" \
  "v_mfma_f32_16x16x32_bf16 v[80:83], v[132:135], v[52:55], v[80:83]\n" \
  "ds_read_b128 v[48:51], v1 offset:12288\n" \
  "v_mfma_f32_16x16x32_bf16 v[84:87], v[164:167], v[52:55], v[84:87]\n" \
  "v_mfma_f32_16x16x32_bf16 v[88:91], v[196:199], v[52:55], v[88:91]\n" \
  "v_mfma_f32_16x16x32_bf16 v[92:95], v[228:231], v[52:55], v[92:95]\n" \
  "s_waitcnt lgkmcnt(2)\n" \
  "v_mfma_f32_16x16x32_bf16 v[80:83], v[136:139], v[56:59], v[80:83]\n" \
  "ds_read_b128 v[52:55], v1 offset:13312\n" \
  "v_mfma_f32_16x16x32_bf16 v[84:87], v[168:171], v[56:59], v[84:87]\n" \
  "v_mfma_f32_16x16x32_bf16 v[88:91], v[200:203], v[56:59], v[88:91]\n" \
  "v_mfma_f32_16x16x32_bf16 v[92:95], v[232:235], v[56:59], v[92:95]\n" \
  "s_waitcnt lgkmcnt(2)\n" \
  "v_mfma_f32_16x16x32_bf16 v[80:83], v[140:143], v[60:63], v[80:83]\n" \
  "ds_read_b128 v[56:59], v1 offset:14336\n" \
  "v_mfma_f32_16x16x32_bf16 v[84:87], v[172:175], v[60:63], v[84:87]\n" \
  "v_mfma_f32_16x16x32_bf16 v[88:91], v[204:207], v[60:63], v[88:91]\n" \
  "v_mfma_f32_16x16x32_bf16 v[92:95], v[236:239], v[60:63], v[92:95]\n" \
  "s_waitcnt lgkmcnt(2)\n" \
  "v_mfma_f32_16x16x32_bf16 v[112:115], v[144:147], v[48:51], 0\n" \
  "ds_read_b128 v[60:63], v1 offset:15360\n" \
  "v_mfma_f32_16x16x32_bf16 v[116:119], v[176:179], v[48:51], 0\n" \
  "v_mfma_f32_16x16x32_bf16 v[120:123], v[208:211], v[48:51], 0\n" \
  "v_mfma_f32_16x16x32_bf16 v[124:127], v[240:243], v[48:51], 0\n" \
  "s_waitcnt lgkmcnt(2)\n" \
  "v_mfma_f32_16x16x32_bf16 v[112:115], v[148:151], v[52:55], v[112:115]\n" \
  "v_mfma_f32_16x16x32_bf16 v[116:119], v[180:183], v[52:55], v[116:119]\n" \
  "v_mfma_f32_16x16x32_bf16 v[120:123], v[212:215], v[52:55], v[120:123]\n" \
  "s_waitcnt vmcnt(0)\n" \
  "ds_read_b128 v[8:11], v3 offset:0\n" \
  "ds_read_b128 v[12:15], v3 offset:1024\n" \
  "v_mfma_f32_16x16x32_bf16 v[124:127], v[244:247], v[52:55], v[124:127]\n" \
  "s_waitcnt lgkmcnt(3)\n" \
  "v_mfma_f32_16x16x32_bf16 v[112:115], v[152:155], v[56:59], v[112:115]\n" \
  "v_mfma_f32_16x16x32_bf16 v[116:119], v[184:187], v[56:59], v[116:119]\n" \
  "v_mfma_f32_16x16x32_bf16 v[120:123], v[216:219], v[56:59], v[120:123]\n" \
  "v_mfma_f32_16x16x32_bf16 v[124:127], v[248:251], v[56:59], v[124:127]\n" \
  "s_waitcnt lgkmcnt(2)\n" \
  "v_mfma_f32_16x16x32_bf16 v[112:115], v[156:159], v[60:63], v[112:115]\n" \
  "s_waitcnt lgkmcnt(0)\n" \
  "v_min3_u32 v8, v8, v9, v10\n" \
  "v_mfma_f32_16x16x32_bf16 v[116:119], v[188:191], v[60:63], v[116:119]\n" \
  "v_min3_u32 v12, v12, v13, v14\n" \
  "v_mfma_f32_16x16x32_bf16 v[120:123], v[220:223], v[60:63], v[120:123]\n" \
  "v_min3_u32 v8, v8, v11, v15\n" \
  "v_mfma_f32_16x16x32_bf16 v[124:127], v[252:255], v[60:63], v[124:127]\n" \
  "v_min_u32 v8, v8, v12\n" \
  "v_cmp_eq_u32 vcc, 0, v5\n" \
  "s_cmp_eq_u64 vcc, 0\n" \
  "s_cbranch_scc0 .LWB_NOU_%=\n" \
  "s_mov_b32 m0, s61\n" \
  "s_lshl_b32 s68, s58, 19\n" \
  "s_add_u32 s68, s68, s48\n" \
  "s_addc_u32 s69, s49, 0\n" \
  "global_load_lds_dwordx4 v0, s[68:69] offset:0 sc1\n" \
  "global_load_lds_dwordx4 v0, s[68:69] offset:1024 sc1\n" \
  "global_load_lds_dwordx4 v0, s[68:69] offset:2048 sc1\n" \
  "global_load_lds_dwordx4 v0, s[68:69] offset:3072 sc1\n" \
  "s_add_u32 m0, s61, 4096\n" \
  "s_add_u32 s70, s68, 4096\n" \
  "s_addc_u32 s71, s69, 0\n" \
  "global_load_lds_dwordx4 v0, s[70:71] offset:0 sc1\n" \
  "global_load_lds_dwordx4 v0, s[70:71] offset:1024 sc1\n" \
  "global_load_lds_dwordx4 v0, s[70:71] offset:2048 sc1\n" \
  "global_load_lds_dwordx4 v0, s[70:71] offset:3072 sc1\n" \
  "s_mov_b32 m0, s62\n" \
  "s_lshl_b32 s72, s58, 18\n" \
  "s_add_u32 s72, s72, s50\n" \
  "s_addc_u32 s73, s51, 0\n" \
  "global_load_lds_dwordx4 v0, s[72:73] offset:0 sc1\n" \
  "global_load_lds_dwordx4 v0, s[72:73] offset:1024 sc1\n" \
  "global_load_lds_dwordx4 v0, s[72:73] offset:2048 sc1\n" \
  "global_load_lds_dwordx4 v0, s[72:73] offset:3072 sc1\n" \
  "s_mov_b32 s67, 1\n" \
  ".LWB_NOU_%=:\n" \
  "s_cmp_eq_u32 s77, 1\n" \
  "s_cbranch_scc1 .LWB_TEST_%=\n" \
  ".LWB_GATE_%=:\n" \
  "s_memrealtime s[68:69]\n" \
  "s_waitcnt lgkmcnt(0)\n" \
  "s_sub_i32 s70, s74, s68\n" \
  "s_cmp_gt_u32 s70, 2000\n" \
  "s_cbranch_scc1 .LWB_ASK_%=\n" \
  "s_sleep 2\n" \
  "s_cmp_lt_u32 s70, 40\n" \
  "s_cbranch_scc1 .LWB_GATE_%=\n" \
  "s_sleep 8\n" \
  "s_branch .LWB_GATE_%=\n" \
  ".LWB_AGAIN_%=:\n" \
  "s_sleep %[sm]\n" \
  ".LWB_ASK_%=:\n" \
  "s_mov_b32 m0, s63\n" \
  "s_lshl_b32 s72, s57, 16\n" \
  "s_add_u32 s72, s72, s52\n" \
  "s_addc_u32 s73, s53, 0\n" \
  "global_load_lds_dwordx4 v0, s[72:73] offset:0 sc1\n" \
  "global_load_lds_dwordx4 v0, s[72:73] offset:1024 sc1\n" \
  "s_add_u32 s77, s77, 1\n" \
  "s_waitcnt vmcnt(0)\n" \
  "ds_read_b128 v[8:11], v3 offset:0\n" \
  "ds_read_b128 v[12:15], v3 offset:1024\n" \
  "s_cmp_gt_u32 s77, 0x40000\n" \
  "s_cbranch_scc1 .LWB_OK_%=\n" \
  "s_waitcnt lgkmcnt(1)\n" \
  "v_min3_u32 v8, v8, v9, v10\n" \
  "s_waitcnt lgkmcnt(0)\n" \
  "v_min3_u32 v12, v12, v13, v14\n" \
  "v_min3_u32 v8, v8, v11, v15\n" \
  "v_min_u32 v8, v8, v12\n" \
  ".LWB_TEST_%=:\n" \
  "v_cmp_eq_u32 vcc, 0, v8\n" \
  "s_cmp_eq_u64 vcc, 0\n" \
  "s_cbranch_scc0 .LWB_AGAIN_%=\n" \
  ".LWB_OK_%=:\n" \
  "s_waitcnt lgkmcnt(0)\n" \
  "s_barrier\n" \
  "ds_read_b64_tr_b16 v[48:49], v6 offset:0\n" \
  "ds_read_b64_tr_b16 v[50:51], v6 offset:32\n" \
  "ds_read_b64_tr_b16 v[52:53], v6 offset:1024\n" \
  "ds_read_b64_tr_b16 v[54:55], v6 offset:1056\n" \
  "ds_read_b64_tr_b16 v[56:57], v6 offset:4096\n" \
  "ds_read_b64_tr_b16 v[58:59], v6 offset:4128\n" \
  "ds_read_b64_tr_b16 v[60:61], v6 offset:5120\n" \
  "ds_read_b64_tr_b16 v[62:63], v6 offset:5152\n" \
  "s_memrealtime s[68:69]\n" \
  "s_lshl_b32 s72, s57, 11\n" \
  "s_add_u32 s72, s72, s54\n" \
  "s_addc_u32 s73, s55, 0\n" \
  "s_waitcnt lgkmcnt(0)\n" \
  "s_add_u32 s70, s68, %[dt]\n" \
  "s_or_b32 s70, s70, 1\n" \
  "v_mov_b32 v13, s70\n" \
  "s_mov_b64 exec, s[82:83]\n" \
  "global_store_dword v7, v13, s[72:73] sc1\n" \
  "s_mov_b64 exec, -1\n" \
  "s_waitcnt lgkmcnt(6)\n" \
  "v_smfmac_f32_16x16x64_bf16 v[16:19], v[48:51], v[128:135], v4\n" \
  "v_smfmac_f32_16x16x64_bf16 v[20:23], v[48:51], v[160:167], v4\n" \
  "v_smfmac_f32_16x16x64_bf16 v[24:27], v[48:51], v[192:199], v4\n" \
  "v_smfmac_f32_16x16x64_bf16 v[28:31], v[48:51], v[224:231], v4\n" \
  "s_waitcnt lgkmcnt(4)\n" \
  "v_smfmac_f32_16x16x64_bf16 v[16:19], v[52:55], v[136:143], v4\n" \
  "v_smfmac_f32_16x16x64_bf16 v[20:23], v[52:55], v[168:175], v4\n" \
  "v_smfmac_f32_16x16x64_bf16 v[24:27], v[52:55], v[200:207], v4\n" \
  "v_smfmac_f32_16x16x64_bf16 v[28:31], v[52:55], v[232:239], v4\n" \
  "s_waitcnt lgkmcnt(2)\n" \
  "v_smfmac_f32_16x16x64_bf16 v[32:35], v[56:59], v[144:151], v4\n" \
  "v_smfmac_f32_16x16x64_bf16 v[36:39], v[56:59], v[176:183], v4\n" \
  "v_smfmac_f32_16x16x64_bf16 v[40:43], v[56:59], v[208:215], v4\n" \
  "v_smfmac_f32_16x16x64_bf16 v[44:47], v[56:59], v[240:247], v4\n" \
  "s_waitcnt lgkmcnt(0)\n" \
  "v_smfmac_f32_16x16x64_bf16 v[32:35], v[60:63], v[152:159], v4\n" \
  "v_smfmac_f32_16x16x64_bf16 v[36:39], v[60:63], v[184:191], v4\n" \
  "v_smfmac_f32_16x16x64_bf16 v[40:43], v[60:63], v[216:223], v4\n" \
  "v_smfmac_f32_16x16x64_bf16 v[44:47], v[60:63], v[248:255], v4\n" \
  "s_waitcnt lgkmcnt(0)\n" \
  "s_sub_u32 s68, s68, s76\n" \
  "s_sub_u32 s68, s68, %[ga]\n" \
  "s_sub_u32 s75, s75, %[gs]\n" \
  "s_cmp_gt_u32 s77, 1\n" \
  "s_cselect_b32 s75, s68, s75\n" \
  "s_cmp_lt_u32 s57, 64\n" \
  "s_cselect_b32 s64, s75, s64\n" \
  "s_cselect_b32 s65, s65, s75\n" \
  "s_lshl_b32 s75, s57, 19\n" \
  "s_add_u32 s75, s75, s60\n" \
  "s_add_u32 s76, s75, 16384\n" \
  "v_pk_add_f32 v[16:17], v[16:17], v[18:19]\n" \
  "v_pk_add_f32 v[32:33], v[32:33], v[34:35]\n" \
  "v_cvt_pk_bf16_f32 v16, v16, v17\n" \
  "v_cvt_pk_bf16_f32 v17, v32, v33\n" \
  "v_pk_add_f32 v[20:21], v[20:21], v[22:23]\n" \
  "v_pk_add_f32 v[36:37], v[36:37], v[38:39]\n" \
  "v_cvt_pk_bf16_f32 v20, v20, v21\n" \
  "v_cvt_pk_bf16_f32 v21, v36, v37\n" \
  "v_pk_add_f32 v[24:25], v[24:25], v[26:27]\n" \
  "v_pk_add_f32 v[40:41], v[40:41], v[42:43]\n" \
  "v_cvt_pk_bf16_f32 v24, v24, v25\n" \
  "v_cvt_pk_bf16_f32 v25, v40, v41\n" \
  "v_pk_add_f32 v[28:29], v[28:29], v[30:31]\n" \
  "v_pk_add_f32 v[44:45], v[44:45], v[46:47]\n" \
  "v_cvt_pk_bf16_f32 v28, v28, v29\n" \
  "v_cvt_pk_bf16_f32 v29, v44, v45\n" \
  "s_barrier\n" \
  "ds_read_b128 v[32:35], v1 offset:24576\n" \
  "ds_read_b128 v[36:39], v1 offset:25600\n" \
  "s_setprio 1\n" \
  "s_waitcnt lgkmcnt(0)\n" \
  "v_pk_mul_f32 v[64:65], v[64:65], v[34:35] op_sel_hi:[1,0]\n" \
  "v_pk_mul_f32 v[66:67], v[66:67], v[34:35] op_sel_hi:[1,0]\n" \
  "v_pk_mul_f32 v[68:69], v[68:69], v[34:35] op_sel_hi:[1,0]\n" \
  "v_pk_mul_f32 v[70:71], v[70:71], v[34:35] op_sel_hi:[1,0]\n" \
  "v_pk_fma_f32 v[64:65], v[96:97], v[34:35], v[64:65] op_sel:[0,1,0]\n" \
  "v_pk_fma_f32 v[66:67], v[98:99], v[34:35], v[66:67] op_sel:[0,1,0]\n" \
  "v_pk_fma_f32 v[68:69], v[100:101], v[34:35], v[68:69] op_sel:[0,1,0]\n" \
  "v_pk_fma_f32 v[70:71], v[102:103], v[34:35], v[70:71] op_sel:[0,1,0]\n" \
  "s_nop 1\n" \
  "v_mfma_f32_16x16x16_bf16 v[64:67], v[16:17], v[32:33], v[64:67]\n" \
  "v_mfma_f32_16x16x16_bf16 v[68:71], v[20:21], v[32:33], v[68:71]\n" \
  "v_pk_mul_f32 v[72:73], v[72:73], v[34:35] op_sel_hi:[1,0]\n" \
  "v_pk_mul_f32 v[74:75], v[74:75], v[34:35] op_sel_hi:[1,0]\n" \
  "v_pk_mul_f32 v[76:77], v[76:77], v[34:35] op_sel_hi:[1,0]\n" \
  "v_pk_mul_f32 v[78:79], v[78:79], v[34:35] op_sel_hi:[1,0]\n" \
  "v_pk_fma_f32 v[72:73], v[104:105], v[34:35], v[72:73] op_sel:[0,1,0]\n" \
  "v_pk_fma_f32 v[74:75], v[106:107], v[34:35], v[74:75] op_sel:[0,1,0]\n" \
  "v_pk_fma_f32 v[76:77], v[108:109], v[34:35], v[76:77] op_sel:[0,1,0]\n" \
  "v_pk_fma_f32 v[78:79], v[110:111], v[34:35], v[78:79] op_sel:[0,1,0]\n" \
  "s_nop 1\n" \
  "v_mfma_f32_16x16x16_bf16 v[72:75], v[24:25], v[32:33], v[72:75]\n" \
  "v_mfma_f32_16x16x16_bf16 v[76:79], v[28:29], v[32:33], v[76:79]\n" \
  "v_cvt_pk_bf16_f32 v64, v64, v65\n" \
  "v_cvt_pk_bf16_f32 v65, v66, v67\n" \
  "v_cvt_pk_bf16_f32 v66, v68, v69\n" \
  "v_cvt_pk_bf16_f32 v67, v70, v71\n" \
  "buffer_store_dwordx4 v[64:67], v2, s[40:43], s75 offen offset:0 sc1\n" \
  "v_pk_mul_f32 v[80:81], v[80:81], v[38:39] op_sel_hi:[1,0]\n" \
  "v_pk_mul_f32 v[82:83], v[82:83], v[38:39] op_sel_hi:[1,0]\n" \
  "v_pk_mul_f32 v[84:85], v[84:85], v[38:39] op_sel_hi:[1,0]\n" \
  "v_pk_mul_f32 v[86:87], v[86:87], v[38:39] op_sel_hi:[1,0]\n" \
  "v_pk_fma_f32 v[80:81], v[112:113], v[38:39], v[80:81] op_sel:[0,1,0]\n" \
  "v_pk_fma_f32 v[82:83], v[114:115], v[38:39], v[82:83] op_sel:[0,1,0]\n" \
  "v_pk_fma_f32 v[84:85], v[116:117], v[38:39], v[84:85] op_sel:[0,1,0]\n" \
  "v_pk_fma_f32 v[86:87], v[118:119], v[38:39], v[86:87] op_sel:[0,1,0]\n" \
  "s_nop 1\n" \
  "v_mfma_f32_16x16x16_bf16 v[80:83], v[16:17], v[36:37], v[80:83]\n" \
  "v_mfma_f32_16x16x16_bf16 v[84:87], v[20:21], v[36:37], v[84:87]\n" \
  "v_cvt_pk_bf16_f32 v72, v72, v73\n" \
  "v_cvt_pk_bf16_f32 v73, v74, v75\n" \
  "v_cvt_pk_bf16_f32 v74, v76, v77\n" \
  "v_cvt_pk_bf16_f32 v75, v78, v79\n" \
  "buffer_store_dwordx4 v[72:75], v2, s[40:43], s75 offen offset:128 sc1\n" \
  "v_pk_mul_f32 v[88:89], v[88:89], v[38:39] op_sel_hi:[1,0]\n" \
  "v_pk_mul_f32 v[90:91], v[90:91], v[38:39] op_sel_hi:[1,0]\n" \
  "v_pk_mul_f32 v[92:93], v[92:93], v[38:39] op_sel_hi:[1,0]\n" \
  "v_pk_mul_f32 v[94:95], v[94:95], v[38:39] op_sel_hi:[1,0]\n" \
  "v_pk_fma_f32 v[88:89], v[120:121], v[38:39], v[88:89] op_sel:[0,1,0]\n" \
  "v_pk_fma_f32 v[90:91], v[122:123], v[38:39], v[90:91] op_sel:[0,1,0]\n" \
  "v_pk_fma_f32 v[92:93], v[124:125], v[38:39], v[92:93] op_sel:[0,1,0]\n" \
  "v_pk_fma_f32 v[94:95], v[126:127], v[38:39], v[94:95] op_sel:[0,1,0]\n" \
  "s_nop 1\n" \
  "v_mfma_f32_16x16x16_bf16 v[88:91], v[24:25], v[36:37], v[88:91]\n" \
  "v_mfma_f32_16x16x16_bf16 v[92:95], v[28:29], v[36:37], v[92:95]\n" \
  "v_cvt_pk_bf16_f32 v80, v80, v81\n" \
  "v_cvt_pk_bf16_f32 v81, v82, v83\n" \
  "v_cvt_pk_bf16_f32 v82, v84, v85\n" \
  "v_cvt_pk_bf16_f32 v83, v86, v87\n" \
  "buffer_store_dwordx4 v[80:83], v2, s[40:43], s76 offen offset:0 sc1\n" \
  "s_nop 2\n" \
  "v_cvt_pk_bf16_f32 v88, v88, v89\n" \
  "v_cvt_pk_bf16_f32 v89, v90, v91\n" \
  "v_cvt_pk_bf16_f32 v90, v92, v93\n" \
  "v_cvt_pk_bf16_f32 v91, v94, v95\n" \
  "buffer_store_dwordx4 v[88:91], v2, s[40:43], s76 offen offset:128 sc1\n" \
  "s_waitcnt vmcnt(4)\n" \
  ".LWB_NEXT_%=:\n" \
  "s_setprio 0\n" \
  "s_mov_b32 s57, s58\n" \
  "s_cmp_eq_u32 s67, 0\n" \
  "s_cbranch_scc1 .LWB_MISS_%=\n" \
  "s_branch .LWB_TOP_%=\n" \
  ".LWB_MISS_%=:\n" \
  "s_cmp_eq_u32 s57, 128\n" \
  "s_cbranch_scc1 .LWB_END_%=\n" \
  ".LWB_LATE_%=:\n" \
  "s_mov_b32 s77, 0x40000\n" \
  ".LWB_POLL_%=:\n" \
  "s_lshr_b32 s68, s57, 6\n" \
  "s_add_u32 s68, s68, s57\n" \
  "s_lshl_b32 s68, s68, 12\n" \
  "buffer_load_dword v5, off, s[44:47], s68 sc1\n" \
  "s_sub_u32 s77, s77, 1\n" \
  "s_cbranch_scc1 .LWB_SEEN_%=\n" \
  "s_waitcnt vmcnt(0)\n" \
  "v_cmp_eq_u32 vcc, 0, v5\n" \
  "s_cmp_eq_u64 vcc, 0\n" \
  "s_cbranch_scc1 .LWB_SEEN_%=\n" \
  "s_sleep %[sa]\n" \
  "s_branch .LWB_POLL_%=\n" \
  ".LWB_SEEN_%=:\n" \
  "s_mov_b32 m0, s61\n" \
  "s_lshl_b32 s68, s57, 19\n" \
  "s_add_u32 s68, s68, s48\n" \
  "s_addc_u32 s69, s49, 0\n" \
  "global_load_lds_dwordx4 v0, s[68:69] offset:0 sc1\n" \
  "global_load_lds_dwordx4 v0, s[68:69] offset:1024 sc1\n" \
  "global_load_lds_dwordx4 v0, s[68:69] offset:2048 sc1\n" \
  "global_load_lds_dwordx4 v0, s[68:69] offset:3072 sc1\n" \
  "s_add_u32 m0, s61, 4096\n" \
  "s_add_u32 s70, s68, 4096\n" \
  "s_addc_u32 s71, s69, 0\n" \
  "global_load_lds_dwordx4 v0, s[70:71] offset:0 sc1\n" \
  "global_load_lds_dwordx4 v0, s[70:71] offset:1024 sc1\n" \
  "global_load_lds_dwordx4 v0, s[70:71] offset:2048 sc1\n" \
  "global_load_lds_dwordx4 v0, s[70:71] offset:3072 sc1\n" \
  "s_mov_b32 m0, s62\n" \
  "s_lshl_b32 s72, s57, 18\n" \
  "s_add_u32 s72, s72, s50\n" \
  "s_addc_u32 s73, s51, 0\n" \
  "global_load_lds_dwordx4 v0, s[72:73] offset:0 sc1\n" \
  "global_load_lds_dwordx4 v0, s[72:73] offset:1024 sc1\n" \
  "global_load_lds_dwordx4 v0, s[72:73] offset:2048 sc1\n" \
  "global_load_lds_dwordx4 v0, s[72:73] offset:3072 sc1\n" \
  "s_waitcnt vmcnt(0)\n" \
  "s_branch .LWB_TOP_%=\n" \
  ".LWB_END_%=:\n" \
  "s_endpgm\n" \
  ".LWB_GATEE_%=:\n" \
  "s_memrealtime s[68:69]\n" \
  "s_waitcnt lgkmcnt(0)\n" \
  "s_sub_i32 s70, s74, s68\n" \
  "s_cmp_gt_u32 s70, 2000\n" \
  "s_cbranch_scc1 .LWB_ASKE_%=\n" \
  "s_sleep 2\n" \
  "s_cmp_lt_u32 s70, 40\n" \
  "s_cbranch_scc1 .LWB_GATEE_%=\n" \
  "s_sleep 8\n" \
  "s_branch .LWB_GATEE_%=\n" \
  ".LWB_AGAINE_%=:\n" \
  "s_sleep %[sm]\n" \
  ".LWB_ASKE_%=:\n" \
  "s_mov_b32 m0, s63\n" \
  "s_lshl_b32 s72, s57, 16\n" \
  "s_add_u32 s72, s72, s52\n" \
  "s_addc_u32 s73, s53, 0\n" \
  "global_load_lds_dwordx4 v0, s[72:73] offset:0 sc1\n" \
  "global_load_lds_dwordx4 v0, s[72:73] offset:1024 sc1\n" \
  "s_add_u32 s77, s77, 1\n" \
  "s_waitcnt vmcnt(0)\n" \
  "ds_read_b128 v[8:11], v3 offset:0\n" \
  "ds_read_b128 v[12:15], v3 offset:1024\n" \
  "s_cmp_gt_u32 s77, 0x40000\n" \
  "s_cbranch_scc1 .LWB_OKE_%=\n" \
  "s_waitcnt lgkmcnt(1)\n" \
  "v_min3_u32 v8, v8, v9, v10\n" \
  "s_waitcnt lgkmcnt(0)\n" \
  "v_min3_u32 v12, v12, v13, v14\n" \
  "v_min3_u32 v8, v8, v11, v15\n" \
  "v_min_u32 v8, v8, v12\n" \
  ".LWB_TESTE_%=:\n" \
  "v_cmp_eq_u32 vcc, 0, v8\n" \
  "s_cmp_eq_u64 vcc, 0\n" \
  "s_cbranch_scc0 .LWB_AGAINE_%=\n" \
  ".LWB_OKE_%=:\n" \
  "s_waitcnt lgkmcnt(0)\n" \
  "s_barrier\n" \
  "s_memrealtime s[68:69]\n" \
  "s_waitcnt lgkmcnt(0)\n" \
  "s_sub_u32 s68, s68, s76\n" \
  "s_sub_u32 s68, s68, %[ga]\n" \
  "s_sub_u32 s75, s75, %[gs]\n" \
  "s_cmp_gt_u32 s77, 1\n" \
  "s_cselect_b32 s75, s68, s75\n" \
  "s_cmp_lt_u32 s57, 64\n" \
  "s_cselect_b32 s64, s75, s64\n" \
  "s_cselect_b32 s65, s65, s75\n" \
  "v_cmp_eq_u32 vcc, 0, v5\n" \
  "s_cmp_eq_u64 vcc, 0\n" \
  "s_cbranch_scc0 .LWB_NOE_%=\n" \
  "s_mov_b32 m0, s61\n" \
  "s_lshl_b32 s68, s58, 19\n" \
  "s_add_u32 s68, s68, s48\n" \
  "s_addc_u32 s69, s49, 0\n" \
  "global_load_lds_dwordx4 v0, s[68:69] offset:0 sc1\n" \
  "global_load_lds_dwordx4 v0, s[68:69] offset:1024 sc1\n" \
  "global_load_lds_dwordx4 v0, s[68:69] offset:2048 sc1\n" \
  "global_load_lds_dwordx4 v0, s[68:69] offset:3072 sc1\n" \
  "s_add_u32 m0, s61, 4096\n" \
  "s_add_u32 s70, s68, 4096\n" \
  "s_addc_u32 s71, s69, 0\n" \
  "global_load_lds_dwordx4 v0, s[70:71] offset:0 sc1\n" \
  "global_load_lds_dwordx4 v0, s[70:71] offset:1024 sc1\n" \
  "global_load_lds_dwordx4 v0, s[70:71] offset:2048 sc1\n" \
  "global_load_lds_dwordx4 v0, s[70:71] offset:3072 sc1\n" \
  "s_mov_b32 m0, s62\n" \
  "s_lshl_b32 s72, s58, 18\n" \
  "s_add_u32 s72, s72, s50\n" \
  "s_addc_u32 s73, s51, 0\n" \
  "global_load_lds_dwordx4 v0, s[72:73] offset:0 sc1\n" \
  "global_load_lds_dwordx4 v0, s[72:73] offset:1024 sc1\n" \
  "global_load_lds_dwordx4 v0, s[72:73] offset:2048 sc1\n" \
  "global_load_lds_dwordx4 v0, s[72:73] offset:3072 sc1\n" \
  "s_mov_b32 s67, 1\n" \
  ".LWB_NOE_%=:\n" \
  "s_barrier\n" \
  "s_setprio 0\n" \
  "ds_read_b128 v[48:51], v1 offset:0\n" \
  "ds_read_b128 v[52:55], v1 offset:1024\n" \
  "ds_read_b128 v[56:59], v1 offset:2048\n" \
  "s_waitcnt lgkmcnt(2)\n" \
  "v_mfma_f32_16x16x32_bf16 v[64:67], v[128:131], v[48:51], 0\n" \
  "ds_read_b128 v[60:63], v1 offset:3072\n" \
  "s_lshl_b32 s75, s57, 19\n" \
  "v_mfma_f32_16x16x32_bf16 v[68:71], v[160:163], v[48:51], 0\n" \
  "s_add_u32 s75, s75, s60\n" \
  "v_mfma_f32_16x16x32_bf16 v[72:75], v[192:195], v[48:51], 0\n" \
  "s_add_u32 s76, s75, 16384\n" \
  "v_mfma_f32_16x16x32_bf16 v[76:79], v[224:227], v[48:51], 0\n" \
  "s_waitcnt lgkmcnt(2)\n" \
  "v_mfma_f32_16x16x32_bf16 v[64:67], v[132:135], v[52:55], v[64:67]\n" \
  "ds_read_b128 v[48:51], v1 offset:8192\n" \
  "v_mfma_f32_16x16x32_bf16 v[68:71], v[164:167], v[52:55], v[68:71]\n" \
  "v_mfma_f32_16x16x32_bf16 v[72:75], v[196:199], v[52:55], v[72:75]\n" \
  "v_mfma_f32_16x16x32_bf16 v[76:79], v[228:231], v[52:55], v[76:79]\n" \
  "s_waitcnt lgkmcnt(2)\n" \
  "v_mfma_f32_16x16x32_bf16 v[64:67], v[136:139], v[56:59], v[64:67]\n" \
  "ds_read_b128 v[52:55], v1 offset:9216\n" \
  "v_mfma_f32_16x16x32_bf16 v[68:71], v[168:171], v[56:59], v[68:71]\n" \
  "v_mfma_f32_16x16x32_bf16 v[72:75], v[200:203], v[56:59], v[72:75]\n" \
  "v_mfma_f32_16x16x32_bf16 v[76:79], v[232:235], v[56:59], v[76:79]\n" \
  "s_waitcnt lgkmcnt(2)\n" \
  "v_mfma_f32_16x16x32_bf16 v[64:67], v[140:143], v[60:63], v[64:67]\n" \
  "ds_read_b128 v[56:59], v1 offset:10240\n" \
  "v_mfma_f32_16x16x32_bf16 v[68:71], v[172:175], v[60:63], v[68:71]\n" \
  "v_mfma_f32_16x16x32_bf16 v[72:75], v[204:207], v[60:63], v[72:75]\n" \
  "v_mfma_f32_16x16x32_bf16 v[76:79], v[236:239], v[60:63], v[76:79]\n" \
  "s_waitcnt lgkmcnt(2)\n" \
  "v_mfma_f32_16x16x32_bf16 v[64:67], v[144:147], v[48:51], v[64:67]\n" \
  "ds_read_b128 v[60:63], v1 offset:11264\n" \
  "v_mfma_f32_16x16x32_bf16 v[68:71], v[176:179], v[48:51], v[68:71]\n" \
  "v_mfma_f32_16x16x32_bf16 v[72:75], v[208:211], v[48:51], v[72:75]\n" \
  "v_mfma_f32_16x16x32_bf16 v[76:79], v[240:243], v[48:51], v[76:79]\n" \
  "s_waitcnt lgkmcnt(2)\n" \
  "v_mfma_f32_16x16x32_bf16 v[64:67], v[148:151], v[52:55], v[64:67]\n" \
  "ds_read_b128 v[48:51], v1 offset:4096\n" \
  "v_mfma_f32_16x16x32_bf16 v[68:71], v[180:183], v[52:55], v[68:71]\n" \
  "v_mfma_f32_16x16x32_bf16 v[72:75], v[212:215], v[52:55], v[72:75]\n" \
  "v_mfma_f32_16x16x32_bf16 v[76:79], v[244:247], v[52:55], v[76:79]\n" \
  "s_waitcnt lgkmcnt(2)\n" \
  "v_mfma_f32_16x16x32_bf16 v[64:67], v[152:155], v[56:59], v[64:67]\n" \
  "ds_read_b128 v[52:55], v1 offset:5120\n" \
  "v_mfma_f32_16x16x32_bf16 v[68:71], v[184:187], v[56:59], v[68:71]\n" \
  "v_mfma_f32_16x16x32_bf16 v[72:75], v[216:219], v[56:59], v[72:75]\n" \
  "v_mfma_f32_16x16x32_bf16 v[76:79], v[248:251], v[56:59], v[76:79]\n" \
  "s_waitcnt lgkmcnt(2)\n" \
  "v_mfma_f32_16x16x32_bf16 v[64:67], v[156:159], v[60:63], v[64:67]\n" \
  "ds_read_b128 v[56:59], v1 offset:6144\n" \
  "v_mfma_f32_16x16x32_bf16 v[68:71], v[188:191], v[60:63], v[68:71]\n" \
  "v_mfma_f32_16x16x32_bf16 v[72:75], v[220:223], v[60:63], v[72:75]\n" \
  "v_mfma_f32_16x16x32_bf16 v[76:79], v[252:255], v[60:63], v[76:79]\n" \
  "s_waitcnt lgkmcnt(2)\n" \
  "v_mfma_f32_16x16x32_bf16 v[80:83], v[128:131], v[48:51], 0\n" \
  "ds_read_b128 v[60:63], v1 offset:7168\n" \
  "v_mfma_f32_16x16x32_bf16 v[84:87], v[160:163], v[48:51], 0\n" \
  "v_mfma_f32_16x16x32_bf16 v[88:91], v[192:195], v[48:51], 0\n" \
  "v_mfma_f32_16x16x32_bf16 v[92:95], v[224:227], v[48:51], 0\n" \
  "s_waitcnt lgkmcnt(2)\n" \
  "v_mfma_f32_16x16x32_bf16 v[80:83], v[132:135], v[52:55], v[80:83]\n" \
  "ds_read_b128 v[48:51], v1 offset:12288\n" \
  "v_mfma_f32_16x16x32_bf16 v[84:87], v[164:167], v[52:55], v[84:87]\n" \
  "v_cvt_pk_bf16_f32 v64, v64, v65\n" \
  "v_mfma_f32_16x16x32_bf16 v[88:91], v[196:199], v[52:55], v[88:91]\n" \
  "v_cvt_pk_bf16_f32 v65, v66, v67\n" \
  "v_mfma_f32_16x16x32_bf16 v[92:95], v[228:231], v[52:55], v[92:95]\n" \
  "s_waitcnt lgkmcnt(2)\n" \
  "v_mfma_f32_16x16x32_bf16 v[80:83], v[136:139], v[56:59], v[80:83]\n" \
  "ds_read_b128 v[52:55], v1 offset:13312\n" \
  "v_mfma_f32_16x16x32_bf16 v[84:87], v[168:171], v[56:59], v[84:87]\n" \
  "v_cvt_pk_bf16_f32 v66, v68, v69\n" \
  "v_mfma_f32_16x16x32_bf16 v[88:91], v[200:203], v[56:59], v[88:91]\n" \
  "v_cvt_pk_bf16_f32 v67, v70, v71\n" \
  "v_mfma_f32_16x16x32_bf16 v[92:95], v[232:235], v[56:59], v[92:95]\n" \
  "s_waitcnt lgkmcnt(2)\n" \
  "v_mfma_f32_16x16x32_bf16 v[80:83], v[140:143], v[60:63], v[80:83]\n" \
  "ds_read_b128 v[56:59], v1 offset:14336\n" \
  "v_mfma_f32_16x16x32_bf16 v[84:87], v[172:175], v[60:63], v[84:87]\n" \
  "buffer_store_dwordx4 v[64:67], v2, s[40:43], s75 offen offset:0 sc1\n" \
  "v_mfma_f32_16x16x32_bf16 v[88:91], v[204:207], v[60:63], v[88:91]\n" \
  "v_cvt_pk_bf16_f32 v72, v72, v73\n" \
  "v_mfma_f32_16x16x32_bf16 v[92:95], v[236:239], v[60:63], v[92:95]\n" \
  "s_waitcnt lgkmcnt(2)\n" \
  "v_mfma_f32_16x16x32_bf16 v[80:83], v[144:147], v[48:51], v[80:83]\n" \
  "ds_read_b128 v[60:63], v1 offset:15360\n" \
  "v_mfma_f32_16x16x32_bf16 v[84:87], v[176:179], v[48:51], v[84:87]\n" \
  "v_cvt_pk_bf16_f32 v73, v74, v75\n" \
  "v_mfma_f32_16x16x32_bf16 v[88:91], v[208:211], v[48:51], v[88:91]\n" \
  "v_cvt_pk_bf16_f32 v74, v76, v77\n" \
  "v_mfma_f32_16x16x32_bf16 v[92:95], v[240:243], v[48:51], v[92:95]\n" \
  "s_waitcnt lgkmcnt(2)\n" \
  "v_mfma_f32_16x16x32_bf16 v[80:83], v[148:151], v[52:55], v[80:83]\n" \
  "v_mfma_f32_16x16x32_bf16 v[84:87], v[180:183], v[52:55], v[84:87]\n" \
  "v_cvt_pk_bf16_f32 v75, v78, v79\n" \
  "v_mfma_f32_16x16x32_bf16 v[88:91], v[212:215], v[52:55], v[88:91]\n" \
  "buffer_store_dwordx4 v[72:75], v2, s[40:43], s75 offen offset:128 sc1\n" \
  "v_mfma_f32_16x16x32_bf16 v[92:95], v[244:247], v[52:55], v[92:95]\n" \
  "s_waitcnt lgkmcnt(1)\n" \
  "v_mfma_f32_16x16x32_bf16 v[80:83], v[152:155], v[56:59], v[80:83]\n" \
  "v_mfma_f32_16x16x32_bf16 v[84:87], v[184:187], v[56:59], v[84:87]\n" \
  "v_mfma_f32_16x16x32_bf16 v[88:91], v[216:219], v[56:59], v[88:91]\n" \
  "v_mfma_f32_16x16x32_bf16 v[92:95], v[248:251], v[56:59], v[92:95]\n" \
  "s_waitcnt lgkmcnt(0)\n" \
  "v_mfma_f32_16x16x32_bf16 v[80:83], v[156:159], v[60:63], v[80:83]\n" \
  "v_mfma_f32_16x16x32_bf16 v[84:87], v[188:191], v[60:63], v[84:87]\n" \
  "v_mfma_f32_16x16x32_bf16 v[88:91], v[220:223], v[60:63], v[88:91]\n" \
  "v_mfma_f32_16x16x32_bf16 v[92:95], v[252:255], v[60:63], v[92:95]\n" \
  "s_nop 4\n" \
  "v_cvt_pk_bf16_f32 v80, v80, v81\n" \
  "v_cvt_pk_bf16_f32 v81, v82, v83\n" \
  "v_cvt_pk_bf16_f32 v82, v84, v85\n" \
  "v_cvt_pk_bf16_f32 v83, v86, v87\n" \
  "buffer_store_dwordx4 v[80:83], v2, s[40:43], s76 offen offset:0 sc1\n" \
  "v_cvt_pk_bf16_f32 v88, v88, v89\n" \
  "v_cvt_pk_bf16_f32 v89, v90, v91\n" \
  "v_cvt_pk_bf16_f32 v90, v92, v93\n" \
  "v_cvt_pk_bf16_f32 v91, v94, v95\n" \
  "buffer_store_dwordx4 v[88:91], v2, s[40:43], s76 offen offset:128 sc1\n" \
  "s_mov_b32 s79, s57\n" \
  "s_waitcnt vmcnt(4)\n" \
  "s_branch .LWB_NEXT_%=\n"
#define CONV_ASM \
  "v_mov_b64 v[64:65], 0\n" \
  "v_mov_b64 v[66:67], 0\n" \
  "v_mov_b64 v[68:69], 0\n" \
  "v_mov_b64 v[70:71], 0\n" \
  "v_mov_b64 v[72:73], 0\n" \
  "v_mov_b64 v[74:75], 0\n" \
  "v_mov_b64 v[76:77], 0\n" \
  "v_mov_b64 v[78:79], 0\n" \
  "v_mov_b64 v[80:81], 0\n" \
  "v_mov_b64 v[82:83], 0\n" \
  "v_mov_b64 v[84:85], 0\n" \
  "v_mov_b64 v[86:87], 0\n" \
  "v_mov_b64 v[88:89], 0\n" \
  "v_mov_b64 v[90:91], 0\n" \
  "v_mov_b64 v[92:93], 0\n" \
  "v_mov_b64 v[94:95], 0\n" \
  "v_mov_b64 v[96:97], 0\n" \
  "v_mov_b64 v[98:99], 0\n" \
  "v_mov_b64 v[100:101], 0\n" \
  "v_mov_b64 v[102:103], 0\n" \
  "v_mov_b64 v[104:105], 0\n" \
  "v_mov_b64 v[106:107], 0\n" \
  "v_mov_b64 v[108:109], 0\n" \
  "v_mov_b64 v[110:111], 0\n" \
  "v_mov_b64 v[112:113], 0\n" \
  "v_mov_b64 v[114:115], 0\n" \
  "v_mov_b64 v[116:117], 0\n" \
  "v_mov_b64 v[118:119], 0\n" \
  "v_mov_b64 v[120:121], 0\n" \
  "v_mov_b64 v[122:123], 0\n" \
  "v_mov_b64 v[124:125], 0\n" \
  "v_mov_b64 v[126:127], 0\n" \
  "v_mov_b32 v20, 0\n" \
  "v_mov_b32 v41, 0\n" \
  "s_branch .LVA_TAPS_%=\n" \
  ".p2align 6\n" \
  ".LVA_AL_%=:\n" \
  ".LVA_TOP_%=:\n" \
  "s_cmp_lg_u32 s63, 1\n" \
  "s_cbranch_scc1 .LVA_BAR_%=\n" \
  "s_waitcnt vmcnt(0)\n" \
  "v_min3_u32 v56, v42, v43, v41\n" \
  "v_cmp_ne_u32 s[74:75], 0, v41\n" \
  "v_cmp_eq_u32 vcc, 0, v56\n" \
  "s_bcnt1_i32_b64 s78, s[74:75]\n" \
  "s_cmp_ge_u32 s78, %[th]\n" \
  "s_cselect_b32 s70, 0, 0x200000\n" \
  "s_cbranch_vccz .LVA_SEEN_%=\n" \
  "s_mov_b32 s79, 0\n" \
  "s_cmp_lg_u32 s78, 0\n" \
  "s_cbranch_scc1 .LVA_ASK_%=\n" \
  ".LVA_GATE_%=:\n" \
  "s_memrealtime s[74:75]\n" \
  "s_waitcnt lgkmcnt(0)\n" \
  "s_sub_i32 s74, s69, s74\n" \
  "s_cmp_gt_u32 s74, %[gv]\n" \
  "s_cbranch_scc1 .LVA_ASK_%=\n" \
  "s_sleep 2\n" \
  "s_branch .LVA_GATE_%=\n" \
  ".LVA_AGAIN_%=:\n" \
  "s_cmp_eq_u32 s78, 64\n" \
  "s_cbranch_scc1 .LVA_ASK_%=\n" \
  "s_sleep %[sv]\n" \
  ".LVA_ASK_%=:\n" \
  "s_cmp_ge_u32 s79, %[fu]\n" \
  "s_cselect_b32 s78, 64, s78\n" \
  "s_cmp_ge_u32 s78, %[th]\n" \
  "s_cselect_b32 s76, s65, 0x200000\n" \
  "buffer_load_dwordx2 v[42:43], v37, s[40:43], s76 offen sc1\n" \
  "buffer_load_dword v41, v38, s[44:47], s66 offen sc1\n" \
  "s_add_u32 s79, s79, 1\n" \
  "s_cmp_gt_u32 s79, 0x40000\n" \
  "s_cbranch_scc1 .LVA_SEEN_%=\n" \
  "s_waitcnt vmcnt(0)\n" \
  "v_min3_u32 v56, v42, v43, v41\n" \
  "v_cmp_ne_u32 s[74:75], 0, v41\n" \
  "v_cmp_eq_u32 vcc, 0, v56\n" \
  "s_bcnt1_i32_b64 s78, s[74:75]\n" \
  "s_cbranch_vccnz .LVA_AGAIN_%=\n" \
  ".LVA_SEEN_%=:\n" \
  "v_and_b32 v62, 0xffff0000, v41\n" \
  "v_lshlrev_b32 v60, 16, v41\n" \
  "v_lshlrev_b32 v56, 16, v42\n" \
  "v_and_b32 v57, 0xffff0000, v42\n" \
  "v_lshlrev_b32 v58, 16, v43\n" \
  "v_and_b32 v59, 0xffff0000, v43\n" \
  "v_rcp_f32 v62, v62\n" \
  "v_pk_add_f32 v[56:57], v[56:57], v[60:61] op_sel_hi:[1,0] neg_lo:[0,1] neg_hi:[0,1]\n" \
  "v_pk_add_f32 v[58:59], v[58:59], v[60:61] op_sel_hi:[1,0] neg_lo:[0,1] neg_hi:[0,1]\n" \
  "v_cvt_pk_bf16_f32 v57, v56, v57\n" \
  "v_cvt_pk_bf16_f32 v59, v58, v59\n" \
  "v_lshlrev_b32 v56, 16, v57\n" \
  "v_and_b32 v57, 0xffff0000, v57\n" \
  "v_lshlrev_b32 v58, 16, v59\n" \
  "v_and_b32 v59, 0xffff0000, v59\n" \
  "v_pk_mul_f32 v[56:57], v[56:57], v[62:63] op_sel_hi:[1,0]\n" \
  "v_pk_mul_f32 v[58:59], v[58:59], v[62:63] op_sel_hi:[1,0]\n" \
  "v_cvt_pk_bf16_f32 v56, v56, v57\n" \
  "v_cvt_pk_bf16_f32 v58, v58, v59\n" \
  "ds_write_b16 v39, v56\n" \
  "ds_write_b16_d16_hi v39, v56 offset:4288\n" \
  "ds_write_b16 v39, v58 offset:8576\n" \
  "ds_write_b16_d16_hi v39, v58 offset:12864\n" \
  "ds_write_b64 v40, v[42:43] offset:512\n" \
  ".LVA_BAR_%=:\n" \
  "s_waitcnt lgkmcnt(0)\n" \
  "s_barrier\n" \
  "ds_read_b128 v[56:59], v0 offset:0\n" \
  "ds_read_u16 v60, v2 offset:0\n" \
  "ds_read_u16 v61, v3 offset:512\n" \
  "ds_read2_b32 v[200:201], v4 offset0:0 offset1:16\n" \
  "ds_read2_b32 v[202:203], v4 offset0:1 offset1:17\n" \
  "ds_read2_b32 v[204:205], v4 offset0:2 offset1:18\n" \
  "ds_read2_b32 v[206:207], v4 offset0:3 offset1:19\n" \
  "ds_read2_b32 v[208:209], v4 offset0:4 offset1:20\n" \
  "s_setprio 3\n" \
  "s_waitcnt lgkmcnt(7)\n" \
  "v_mfma_f32_16x16x32_bf16 v[20:23], v[56:59], v[12:15], v[20:23]\n" \
  "v_mfma_f32_16x16x32_bf16 v[20:23], v[56:59], v[16:19], v[20:23]\n" \
  "s_waitcnt lgkmcnt(5)\n" \
  "v_lshlrev_b32 v60, 16, v60\n" \
  "v_lshlrev_b32 v61, 16, v61\n" \
  "v_mul_f32 v60, s64, v60\n" \
  "v_add_u32 v0, v1, v0\n" \
  "v_add_u32 v2, 64, v2\n" \
  "v_add_u32 v4, 64, v4\n" \
  "s_nop 0\n" \
  "v_cvt_pk_bf16_f32 v62, v20, v60\n" \
  "v_lshlrev_b32 v63, 16, v62\n" \
  "v_and_b32 v62, 0xffff0000, v62\n" \
  "v_add_f32 v62, v62, v63\n" \
  "v_cvt_pk_bf16_f32 v62, v62, v62\n" \
  "v_lshlrev_b32 v62, 16, v62\n" \
  "v_add_f32 v62, v62, v61\n" \
  "v_cvt_pk_bf16_f32 v62, v62, v62\n" \
  "s_mov_b64 exec, s[72:73]\n" \
  "ds_write_b16 v3, v62\n" \
  "s_mov_b64 exec, -1\n" \
  "s_cmp_gt_u32 s63, 1\n" \
  "s_waitcnt lgkmcnt(0)\n" \
  "s_barrier\n" \
  "s_cbranch_scc1 .LVA_REST_%=\n" \
  "s_cmp_eq_u32 s63, 1\n" \
  "s_cbranch_scc1 .LVA_ONE_%=\n" \
  "ds_read_b128 v[56:59], v40 offset:0\n" \
  "s_mov_b32 exec_hi, 0\n" \
  "s_waitcnt lgkmcnt(0)\n" \
  "buffer_store_dwordx4 v[56:59], v7, s[48:51], s67 offen sc1\n" \
  "s_mov_b32 exec_hi, -1\n" \
  "s_add_u32 s67, s67, 0x8000\n" \
  "s_branch .LVA_GO_%=\n" \
  ".LVA_ONE_%=:\n" \
  "s_add_u32 s65, s65, 0x8000\n" \
  "s_add_u32 s66, s66, 2048\n" \
  "s_max_u32 s76, s65, s70\n" \
  "buffer_load_dwordx2 v[42:43], v37, s[40:43], s76 offen sc1\n" \
  "buffer_load_dword v41, v38, s[44:47], s66 offen sc1\n" \
  "s_memrealtime s[74:75]\n" \
  "v_add_u32 v39, 64, v39\n" \
  "s_waitcnt lgkmcnt(0)\n" \
  "s_add_u32 s69, s74, %[gv]\n" \
  "s_branch .LVA_GO_%=\n" \
  ".LVA_REST_%=:\n" \
  "s_setprio 0\n" \
  ".LVA_GO_%=:\n" \
  "v_alignbit_b32 v246, v202, v200, v6\n" \
  "v_alignbit_b32 v247, v204, v202, v6\n" \
  "v_alignbit_b32 v248, v206, v204, v6\n" \
  "v_alignbit_b32 v249, v208, v206, v6\n" \
  "v_alignbit_b32 v250, v203, v201, v6\n" \
  "v_alignbit_b32 v251, v205, v203, v6\n" \
  "v_alignbit_b32 v252, v207, v205, v6\n" \
  "v_alignbit_b32 v253, v209, v207, v6\n" \
  "ds_read2st64_b32 v[200:201], v28 offset0:17 offset1:16\n" \
  "ds_read2st64_b32 v[202:203], v29 offset0:17 offset1:16\n" \
  "ds_read2st64_b32 v[204:205], v30 offset0:17 offset1:16\n" \
  "ds_read2st64_b32 v[206:207], v31 offset0:17 offset1:16\n" \
  "ds_read2st64_b32 v[208:209], v32 offset0:17 offset1:16\n" \
  "ds_read2st64_b32 v[210:211], v28 offset0:15 offset1:14\n" \
  "ds_read2st64_b32 v[212:213], v29 offset0:15 offset1:14\n" \
  "ds_read2st64_b32 v[214:215], v30 offset0:15 offset1:14\n" \
  "ds_read2st64_b32 v[216:217], v31 offset0:15 offset1:14\n" \
  "ds_read2st64_b32 v[218:219], v32 offset0:15 offset1:14\n" \
  "v_and_b32 v250, v250, v8\n" \
  "v_and_b32 v251, v251, v9\n" \
  "v_and_b32 v252, v252, v10\n" \
  "v_and_b32 v253, v253, v11\n" \
  "s_bitcmp1_b32 s62, 1\n" \
  "s_cbranch_scc1 .LVA_QH_%=\n" \
  "s_bitcmp1_b32 s62, 0\n" \
  "s_cbranch_scc1 .LVA_QB_%=\n" \
  ".LVA_QA_%=:\n" \
  "v_mfma_f32_16x16x32_bf16 v[64:67], v[128:131], v[246:249], v[68:71]\n" \
  "v_mfma_f32_16x16x32_bf16 v[68:71], v[132:135], v[246:249], v[72:75]\n" \
  "v_mfma_f32_16x16x32_bf16 v[72:75], v[136:139], v[246:249], v[76:79]\n" \
  "v_mfma_f32_16x16x32_bf16 v[76:79], v[140:143], v[246:249], v[80:83]\n" \
  "s_cmp_ge_u32 s62, 48\n" \
  "s_cbranch_scc1 .LVA_EA_%=\n" \
  "v_mfma_f32_16x16x32_bf16 v[80:83], v[144:147], v[246:249], v[84:87]\n" \
  "v_mfma_f32_16x16x32_bf16 v[84:87], v[148:151], v[246:249], v[88:91]\n" \
  "v_mfma_f32_16x16x32_bf16 v[88:91], v[152:155], v[246:249], v[92:95]\n" \
  "v_mfma_f32_16x16x32_bf16 v[92:95], v[156:159], v[246:249], v[96:99]\n" \
  "s_cmp_ge_u32 s62, 32\n" \
  "s_cbranch_scc1 .LVA_EA_%=\n" \
  "v_mfma_f32_16x16x32_bf16 v[96:99], v[160:163], v[246:249], v[100:103]\n" \
  "v_mfma_f32_16x16x32_bf16 v[100:103], v[164:167], v[246:249], v[104:107]\n" \
  "v_mfma_f32_16x16x32_bf16 v[104:107], v[168:171], v[246:249], v[108:111]\n" \
  "v_mfma_f32_16x16x32_bf16 v[108:111], v[172:175], v[246:249], v[112:115]\n" \
  "s_cmp_ge_u32 s62, 16\n" \
  "s_cbranch_scc1 .LVA_EA_%=\n" \
  "v_mfma_f32_16x16x32_bf16 v[112:115], v[176:179], v[246:249], v[116:119]\n" \
  "v_mfma_f32_16x16x32_bf16 v[116:119], v[180:183], v[246:249], v[120:123]\n" \
  "v_mfma_f32_16x16x32_bf16 v[120:123], v[184:187], v[246:249], v[124:127]\n" \
  "v_mfma_f32_16x16x32_bf16 v[124:127], v[188:191], v[246:249], 0\n" \
  ".LVA_EA_%=:\n" \
  "s_nop 1\n" \
  "v_mfma_f32_16x16x32_bf16 v[20:23], v[192:195], v[250:253], v[64:67]\n" \
  "s_nop 7\n" \
  "v_permlane32_swap_b32 v22, v23\n" \
  "v_add_f32 v20, v22, v23\n" \
  "s_branch .LVA_PD_%=\n" \
  ".LVA_QB_%=:\n" \
  "v_mfma_f32_16x16x32_bf16 v[64:67], v[128:131], v[246:249], v[64:67]\n" \
  "v_mfma_f32_16x16x32_bf16 v[68:71], v[132:135], v[246:249], v[68:71]\n" \
  "v_mfma_f32_16x16x32_bf16 v[72:75], v[136:139], v[246:249], v[72:75]\n" \
  "v_mfma_f32_16x16x32_bf16 v[76:79], v[140:143], v[246:249], v[76:79]\n" \
  "s_cmp_ge_u32 s62, 48\n" \
  "s_cbranch_scc1 .LVA_EB_%=\n" \
  "v_mfma_f32_16x16x32_bf16 v[80:83], v[144:147], v[246:249], v[80:83]\n" \
  "v_mfma_f32_16x16x32_bf16 v[84:87], v[148:151], v[246:249], v[84:87]\n" \
  "v_mfma_f32_16x16x32_bf16 v[88:91], v[152:155], v[246:249], v[88:91]\n" \
  "v_mfma_f32_16x16x32_bf16 v[92:95], v[156:159], v[246:249], v[92:95]\n" \
  "s_cmp_ge_u32 s62, 32\n" \
  "s_cbranch_scc1 .LVA_EB_%=\n" \
  "v_mfma_f32_16x16x32_bf16 v[96:99], v[160:163], v[246:249], v[96:99]\n" \
  "v_mfma_f32_16x16x32_bf16 v[100:103], v[164:167], v[246:249], v[100:103]\n" \
  "v_mfma_f32_16x16x32_bf16 v[104:107], v[168:171], v[246:249], v[104:107]\n" \
  "v_mfma_f32_16x16x32_bf16 v[108:111], v[172:175], v[246:249], v[108:111]\n" \
  "s_cmp_ge_u32 s62, 16\n" \
  "s_cbranch_scc1 .LVA_EB_%=\n" \
  "v_mfma_f32_16x16x32_bf16 v[112:115], v[176:179], v[246:249], v[112:115]\n" \
  "v_mfma_f32_16x16x32_bf16 v[116:119], v[180:183], v[246:249], v[116:119]\n" \
  "v_mfma_f32_16x16x32_bf16 v[120:123], v[184:187], v[246:249], v[120:123]\n" \
  "v_mfma_f32_16x16x32_bf16 v[124:127], v[188:191], v[246:249], v[124:127]\n" \
  ".LVA_EB_%=:\n" \
  "s_nop 1\n" \
  "v_mfma_f32_16x16x32_bf16 v[20:23], v[192:195], v[250:253], v[64:67]\n" \
  "s_not_b64 s[72:73], s[72:73]\n" \
  "s_nop 6\n" \
  "v_permlane32_swap_b32 v20, v21\n" \
  "v_add_f32 v20, v20, v21\n" \
  "s_branch .LVA_PD_%=\n" \
  ".LVA_QH_%=:\n" \
  "s_bitcmp1_b32 s62, 0\n" \
  "s_cbranch_scc1 .LVA_QD_%=\n" \
  ".LVA_QC_%=:\n" \
  "v_mfma_f32_16x16x32_bf16 v[64:67], v[128:131], v[246:249], v[64:67]\n" \
  "v_mfma_f32_16x16x32_bf16 v[68:71], v[132:135], v[246:249], v[68:71]\n" \
  "v_mfma_f32_16x16x32_bf16 v[72:75], v[136:139], v[246:249], v[72:75]\n" \
  "v_mfma_f32_16x16x32_bf16 v[76:79], v[140:143], v[246:249], v[76:79]\n" \
  "s_cmp_ge_u32 s62, 48\n" \
  "s_cbranch_scc1 .LVA_EC_%=\n" \
  "v_mfma_f32_16x16x32_bf16 v[80:83], v[144:147], v[246:249], v[80:83]\n" \
  "v_mfma_f32_16x16x32_bf16 v[84:87], v[148:151], v[246:249], v[84:87]\n" \
  "v_mfma_f32_16x16x32_bf16 v[88:91], v[152:155], v[246:249], v[88:91]\n" \
  "v_mfma_f32_16x16x32_bf16 v[92:95], v[156:159], v[246:249], v[92:95]\n" \
  "s_cmp_ge_u32 s62, 32\n" \
  "s_cbranch_scc1 .LVA_EC_%=\n" \
  "v_mfma_f32_16x16x32_bf16 v[96:99], v[160:163], v[246:249], v[96:99]\n" \
  "v_mfma_f32_16x16x32_bf16 v[100:103], v[164:167], v[246:249], v[100:103]\n" \
  "v_mfma_f32_16x16x32_bf16 v[104:107], v[168:171], v[246:249], v[104:107]\n" \
  "v_mfma_f32_16x16x32_bf16 v[108:111], v[172:175], v[246:249], v[108:111]\n" \
  "s_cmp_ge_u32 s62, 16\n" \
  "s_cbranch_scc1 .LVA_EC_%=\n" \
  "v_mfma_f32_16x16x32_bf16 v[112:115], v[176:179], v[246:249], v[112:115]\n" \
  "v_mfma_f32_16x16x32_bf16 v[116:119], v[180:183], v[246:249], v[116:119]\n" \
  "v_mfma_f32_16x16x32_bf16 v[120:123], v[184:187], v[246:249], v[120:123]\n" \
  "v_mfma_f32_16x16x32_bf16 v[124:127], v[188:191], v[246:249], v[124:127]\n" \
  ".LVA_EC_%=:\n" \
  "s_nop 1\n" \
  "v_mfma_f32_16x16x32_bf16 v[20:23], v[192:195], v[250:253], v[64:67]\n" \
  "s_nop 7\n" \
  "v_permlane32_swap_b32 v22, v23\n" \
  "v_add_f32 v20, v22, v23\n" \
  "s_branch .LVA_PD_%=\n" \
  ".LVA_QD_%=:\n" \
  "v_mfma_f32_16x16x32_bf16 v[64:67], v[128:131], v[246:249], v[64:67]\n" \
  "v_mfma_f32_16x16x32_bf16 v[68:71], v[132:135], v[246:249], v[68:71]\n" \
  "v_mfma_f32_16x16x32_bf16 v[72:75], v[136:139], v[246:249], v[72:75]\n" \
  "v_mfma_f32_16x16x32_bf16 v[76:79], v[140:143], v[246:249], v[76:79]\n" \
  "s_cmp_ge_u32 s62, 48\n" \
  "s_cbranch_scc1 .LVA_ED_%=\n" \
  "v_mfma_f32_16x16x32_bf16 v[80:83], v[144:147], v[246:249], v[80:83]\n" \
  "v_mfma_f32_16x16x32_bf16 v[84:87], v[148:151], v[246:249], v[84:87]\n" \
  "v_mfma_f32_16x16x32_bf16 v[88:91], v[152:155], v[246:249], v[88:91]\n" \
  "v_mfma_f32_16x16x32_bf16 v[92:95], v[156:159], v[246:249], v[92:95]\n" \
  "s_cmp_ge_u32 s62, 32\n" \
  "s_cbranch_scc1 .LVA_ED_%=\n" \
  "v_mfma_f32_16x16x32_bf16 v[96:99], v[160:163], v[246:249], v[96:99]\n" \
  "v_mfma_f32_16x16x32_bf16 v[100:103], v[164:167], v[246:249], v[100:103]\n" \
  "v_mfma_f32_16x16x32_bf16 v[104:107], v[168:171], v[246:249], v[104:107]\n" \
  "v_mfma_f32_16x16x32_bf16 v[108:111], v[172:175], v[246:249], v[108:111]\n" \
  "s_cmp_ge_u32 s62, 16\n" \
  "s_cbranch_scc1 .LVA_ED_%=\n" \
  "v_mfma_f32_16x16x32_bf16 v[112:115], v[176:179], v[246:249], v[112:115]\n" \
  "v_mfma_f32_16x16x32_bf16 v[116:119], v[180:183], v[246:249], v[116:119]\n" \
  "v_mfma_f32_16x16x32_bf16 v[120:123], v[184:187], v[246:249], v[120:123]\n" \
  "v_mfma_f32_16x16x32_bf16 v[124:127], v[188:191], v[246:249], v[124:127]\n" \
  ".LVA_ED_%=:\n" \
  "s_nop 1\n" \
  "v_mfma_f32_16x16x32_bf16 v[20:23], v[192:195], v[250:253], v[68:71]\n" \
  "s_not_b64 s[72:73], s[72:73]\n" \
  "s_nop 6\n" \
  "v_permlane32_swap_b32 v20, v21\n" \
  "v_add_f32 v20, v20, v21\n" \
  ".LVA_PD_%=:\n" \
  "s_cmp_lg_u32 s63, 0\n" \
  "s_cbranch_scc1 .LVA_NOF_%=\n" \
  "v_lshlrev_b32 v56, 6, v7\n" \
  "s_waitcnt vmcnt(0)\n" \
  "s_mov_b64 exec, 15\n" \
  "global_store_dword v56, v5, s[58:59] sc1\n" \
  "s_mov_b64 exec, -1\n" \
  "s_add_u32 s58, s58, 4096\n" \
  "s_addc_u32 s59, s59, 0\n" \
  ".LVA_NOF_%=:\n" \
  "s_add_u32 s74, s62, 12\n" \
  "s_lshl_b32 s76, s63, 1\n" \
  "s_and_b32 s76, s76, 12\n" \
  "s_xor_b32 s74, s74, s76\n" \
  "s_and_b32 s79, s74, 15\n" \
  "s_sub_u32 s74, s62, 4\n" \
  "s_cmp_lt_u32 s74, 48\n" \
  "s_cselect_b32 s79, s79, 1\n" \
  "s_cmp_lg_u32 s79, 0\n" \
  "s_cbranch_scc1 .LVA_NOS_%=\n" \
  "ds_read_u16 v230, v35 offset:0\n" \
  "ds_read_u16 v231, v35 offset:784\n" \
  "ds_read_u16 v232, v35 offset:1568\n" \
  "ds_read_u16 v233, v35 offset:2352\n" \
  "ds_read_u16 v234, v35 offset:3136\n" \
  "ds_read_u16 v235, v35 offset:3920\n" \
  "ds_read_u16 v236, v35 offset:4704\n" \
  "ds_read_u16 v237, v35 offset:5488\n" \
  "v_lshlrev_b32 v238, 16, v44\n" \
  "v_and_b32 v239, 0xffff0000, v44\n" \
  "v_lshlrev_b32 v240, 16, v45\n" \
  "v_and_b32 v241, 0xffff0000, v45\n" \
  "v_lshlrev_b32 v242, 16, v46\n" \
  "v_and_b32 v243, 0xffff0000, v46\n" \
  "v_lshlrev_b32 v244, 16, v47\n" \
  "v_and_b32 v245, 0xffff0000, v47\n" \
  "v_mov_b64 v[44:45], v[48:49]\n" \
  "v_mov_b64 v[46:47], v[50:51]\n" \
  "v_mov_b64 v[48:49], v[52:53]\n" \
  "v_mov_b64 v[50:51], v[54:55]\n" \
  "s_waitcnt lgkmcnt(0)\n" \
  "v_lshlrev_b32 v230, 16, v230\n" \
  "v_lshlrev_b32 v231, 16, v231\n" \
  "v_lshlrev_b32 v232, 16, v232\n" \
  "v_lshlrev_b32 v233, 16, v233\n" \
  "v_lshlrev_b32 v234, 16, v234\n" \
  "v_lshlrev_b32 v235, 16, v235\n" \
  "v_lshlrev_b32 v236, 16, v236\n" \
  "v_lshlrev_b32 v237, 16, v237\n" \
  "v_pk_add_f32 v[230:231], v[230:231], v[238:239]\n" \
  "v_pk_add_f32 v[232:233], v[232:233], v[240:241]\n" \
  "v_pk_add_f32 v[234:235], v[234:235], v[242:243]\n" \
  "v_pk_add_f32 v[236:237], v[236:237], v[244:245]\n" \
  "v_cvt_pk_bf16_f32 v56, v230, v231\n" \
  "v_cvt_pk_bf16_f32 v57, v232, v233\n" \
  "v_cvt_pk_bf16_f32 v58, v234, v235\n" \
  "v_cvt_pk_bf16_f32 v59, v236, v237\n" \
  "buffer_store_dwordx4 v[56:59], v36, s[52:55], s68 offen sc1\n" \
  "s_add_u32 s68, s68, 0x80000\n" \
  ".LVA_NOS_%=:\n" \
  "s_cmp_ge_u32 s62, 48\n" \
  "s_cbranch_scc1 .LVA_FIN_%=\n" \
  "s_lshr_b32 s78, s62, 2\n" \
  "s_and_b32 s74, s62, 3\n" \
  "ds_read2st64_b32 v[220:221], v28 offset0:13 offset1:12\n" \
  "ds_read2st64_b32 v[222:223], v29 offset0:13 offset1:12\n" \
  "ds_read2st64_b32 v[224:225], v30 offset0:13 offset1:12\n" \
  "ds_read2st64_b32 v[226:227], v31 offset0:13 offset1:12\n" \
  "ds_read2st64_b32 v[228:229], v32 offset0:13 offset1:12\n" \
  "s_waitcnt lgkmcnt(10)\n" \
  "v_alignbit_b32 v230, v202, v200, v6\n" \
  "v_alignbit_b32 v231, v204, v202, v6\n" \
  "v_alignbit_b32 v232, v206, v204, v6\n" \
  "v_alignbit_b32 v233, v208, v206, v6\n" \
  "v_alignbit_b32 v234, v203, v201, v6\n" \
  "v_alignbit_b32 v235, v205, v203, v6\n" \
  "v_alignbit_b32 v236, v207, v205, v6\n" \
  "v_alignbit_b32 v237, v209, v207, v6\n" \
  "s_cbranch_scc1 .LVA_NOZ_%=\n" \
  "v_mfma_f32_16x16x32_bf16 v[196:199], v[24:27], v[230:233], 0\n" \
  ".LVA_NOZ_%=:\n" \
  "s_nop 0\n" \
  "v_mfma_f32_16x16x32_bf16 v[196:199], v[128:131], v[234:237], v[196:199]\n" \
  "ds_read2st64_b32 v[200:201], v28 offset0:11 offset1:10\n" \
  "ds_read2st64_b32 v[202:203], v29 offset0:11 offset1:10\n" \
  "ds_read2st64_b32 v[204:205], v30 offset0:11 offset1:10\n" \
  "ds_read2st64_b32 v[206:207], v31 offset0:11 offset1:10\n" \
  "ds_read2st64_b32 v[208:209], v32 offset0:11 offset1:10\n" \
  "s_waitcnt lgkmcnt(10)\n" \
  "v_alignbit_b32 v238, v212, v210, v6\n" \
  "v_alignbit_b32 v239, v214, v212, v6\n" \
  "v_alignbit_b32 v240, v216, v214, v6\n" \
  "v_alignbit_b32 v241, v218, v216, v6\n" \
  "v_alignbit_b32 v242, v213, v211, v6\n" \
  "v_alignbit_b32 v243, v215, v213, v6\n" \
  "v_alignbit_b32 v244, v217, v215, v6\n" \
  "v_alignbit_b32 v245, v219, v217, v6\n" \
  "v_mfma_f32_16x16x32_bf16 v[196:199], v[132:135], v[238:241], v[196:199]\n" \
  "s_nop 0\n" \
  "v_mfma_f32_16x16x32_bf16 v[196:199], v[136:139], v[242:245], v[196:199]\n" \
  "ds_read2st64_b32 v[210:211], v28 offset0:9 offset1:8\n" \
  "ds_read2st64_b32 v[212:213], v29 offset0:9 offset1:8\n" \
  "ds_read2st64_b32 v[214:215], v30 offset0:9 offset1:8\n" \
  "ds_read2st64_b32 v[216:217], v31 offset0:9 offset1:8\n" \
  "ds_read2st64_b32 v[218:219], v32 offset0:9 offset1:8\n" \
  "s_waitcnt lgkmcnt(10)\n" \
  "v_alignbit_b32 v230, v222, v220, v6\n" \
  "v_alignbit_b32 v231, v224, v222, v6\n" \
  "v_alignbit_b32 v232, v226, v224, v6\n" \
  "v_alignbit_b32 v233, v228, v226, v6\n" \
  "v_alignbit_b32 v234, v223, v221, v6\n" \
  "v_alignbit_b32 v235, v225, v223, v6\n" \
  "v_alignbit_b32 v236, v227, v225, v6\n" \
  "v_alignbit_b32 v237, v229, v227, v6\n" \
  "v_mfma_f32_16x16x32_bf16 v[196:199], v[140:143], v[230:233], v[196:199]\n" \
  "s_nop 0\n" \
  "v_mfma_f32_16x16x32_bf16 v[196:199], v[144:147], v[234:237], v[196:199]\n" \
  "s_cmp_eq_u32 s78, 0\n" \
  "s_cbranch_scc1 .LVA_OUT_%=\n" \
  "ds_read2st64_b32 v[220:221], v28 offset0:7 offset1:6\n" \
  "ds_read2st64_b32 v[222:223], v29 offset0:7 offset1:6\n" \
  "ds_read2st64_b32 v[224:225], v30 offset0:7 offset1:6\n" \
  "ds_read2st64_b32 v[226:227], v31 offset0:7 offset1:6\n" \
  "ds_read2st64_b32 v[228:229], v32 offset0:7 offset1:6\n" \
  "s_waitcnt lgkmcnt(10)\n" \
  "v_alignbit_b32 v238, v202, v200, v6\n" \
  "v_alignbit_b32 v239, v204, v202, v6\n" \
  "v_alignbit_b32 v240, v206, v204, v6\n" \
  "v_alignbit_b32 v241, v208, v206, v6\n" \
  "v_alignbit_b32 v242, v203, v201, v6\n" \
  "v_alignbit_b32 v243, v205, v203, v6\n" \
  "v_alignbit_b32 v244, v207, v205, v6\n" \
  "v_alignbit_b32 v245, v209, v207, v6\n" \
  "v_mfma_f32_16x16x32_bf16 v[196:199], v[148:151], v[238:241], v[196:199]\n" \
  "s_cmp_eq_u32 s78, 1\n" \
  "s_cbranch_scc1 .LVA_OUT_%=\n" \
  "v_mfma_f32_16x16x32_bf16 v[196:199], v[152:155], v[242:245], v[196:199]\n" \
  "s_cmp_eq_u32 s78, 2\n" \
  "s_cbranch_scc1 .LVA_OUT_%=\n" \
  "ds_read2st64_b32 v[200:201], v28 offset0:5 offset1:4\n" \
  "ds_read2st64_b32 v[202:203], v29 offset0:5 offset1:4\n" \
  "ds_read2st64_b32 v[204:205], v30 offset0:5 offset1:4\n" \
  "ds_read2st64_b32 v[206:207], v31 offset0:5 offset1:4\n" \
  "ds_read2st64_b32 v[208:209], v32 offset0:5 offset1:4\n" \
  "s_waitcnt lgkmcnt(10)\n" \
  "v_alignbit_b32 v230, v212, v210, v6\n" \
  "v_alignbit_b32 v231, v214, v212, v6\n" \
  "v_alignbit_b32 v232, v216, v214, v6\n" \
  "v_alignbit_b32 v233, v218, v216, v6\n" \
  "v_alignbit_b32 v234, v213, v211, v6\n" \
  "v_alignbit_b32 v235, v215, v213, v6\n" \
  "v_alignbit_b32 v236, v217, v215, v6\n" \
  "v_alignbit_b32 v237, v219, v217, v6\n" \
  "v_mfma_f32_16x16x32_bf16 v[196:199], v[156:159], v[230:233], v[196:199]\n" \
  "s_cmp_eq_u32 s78, 3\n" \
  "s_cbranch_scc1 .LVA_OUT_%=\n" \
  "v_mfma_f32_16x16x32_bf16 v[196:199], v[160:163], v[234:237], v[196:199]\n" \
  "s_cmp_eq_u32 s78, 4\n" \
  "s_cbranch_scc1 .LVA_OUT_%=\n" \
  "ds_read2st64_b32 v[210:211], v28 offset0:3 offset1:2\n" \
  "ds_read2st64_b32 v[212:213], v29 offset0:3 offset1:2\n" \
  "ds_read2st64_b32 v[214:215], v30 offset0:3 offset1:2\n" \
  "ds_read2st64_b32 v[216:217], v31 offset0:3 offset1:2\n" \
  "ds_read2st64_b32 v[218:219], v32 offset0:3 offset1:2\n" \
  "s_waitcnt lgkmcnt(10)\n" \
  "v_alignbit_b32 v238, v222, v220, v6\n" \
  "v_alignbit_b32 v239, v224, v222, v6\n" \
  "v_alignbit_b32 v240, v226, v224, v6\n" \
  "v_alignbit_b32 v241, v228, v226, v6\n" \
  "v_alignbit_b32 v242, v223, v221, v6\n" \
  "v_alignbit_b32 v243, v225, v223, v6\n" \
  "v_alignbit_b32 v244, v227, v225, v6\n" \
  "v_alignbit_b32 v245, v229, v227, v6\n" \
  "v_mfma_f32_16x16x32_bf16 v[196:199], v[164:167], v[238:241], v[196:199]\n" \
  "s_cmp_eq_u32 s78, 5\n" \
  "s_cbranch_scc1 .LVA_OUT_%=\n" \
  "v_mfma_f32_16x16x32_bf16 v[196:199], v[168:171], v[242:245], v[196:199]\n" \
  "s_cmp_eq_u32 s78, 6\n" \
  "s_cbranch_scc1 .LVA_OUT_%=\n" \
  "ds_read2st64_b32 v[220:221], v28 offset0:1 offset1:0\n" \
  "ds_read2st64_b32 v[222:223], v29 offset0:1 offset1:0\n" \
  "ds_read2st64_b32 v[224:225], v30 offset0:1 offset1:0\n" \
  "ds_read2st64_b32 v[226:227], v31 offset0:1 offset1:0\n" \
  "ds_read2st64_b32 v[228:229], v32 offset0:1 offset1:0\n" \
  "s_waitcnt lgkmcnt(10)\n" \
  "v_alignbit_b32 v230, v202, v200, v6\n" \
  "v_alignbit_b32 v231, v204, v202, v6\n" \
  "v_alignbit_b32 v232, v206, v204, v6\n" \
  "v_alignbit_b32 v233, v208, v206, v6\n" \
  "v_alignbit_b32 v234, v203, v201, v6\n" \
  "v_alignbit_b32 v235, v205, v203, v6\n" \
  "v_alignbit_b32 v236, v207, v205, v6\n" \
  "v_alignbit_b32 v237, v209, v207, v6\n" \
  "v_mfma_f32_16x16x32_bf16 v[196:199], v[172:175], v[230:233], v[196:199]\n" \
  "s_cmp_eq_u32 s78, 7\n" \
  "s_cbranch_scc1 .LVA_OUT_%=\n" \
  "v_mfma_f32_16x16x32_bf16 v[196:199], v[176:179], v[234:237], v[196:199]\n" \
  "s_cmp_eq_u32 s78, 8\n" \
  "s_cbranch_scc1 .LVA_OUT_%=\n" \
  "s_waitcnt lgkmcnt(5)\n" \
  "v_alignbit_b32 v238, v212, v210, v6\n" \
  "v_alignbit_b32 v239, v214, v212, v6\n" \
  "v_alignbit_b32 v240, v216, v214, v6\n" \
  "v_alignbit_b32 v241, v218, v216, v6\n" \
  "v_alignbit_b32 v242, v213, v211, v6\n" \
  "v_alignbit_b32 v243, v215, v213, v6\n" \
  "v_alignbit_b32 v244, v217, v215, v6\n" \
  "v_alignbit_b32 v245, v219, v217, v6\n" \
  "v_mfma_f32_16x16x32_bf16 v[196:199], v[180:183], v[238:241], v[196:199]\n" \
  "s_cmp_eq_u32 s78, 9\n" \
  "s_cbranch_scc1 .LVA_OUT_%=\n" \
  "v_mfma_f32_16x16x32_bf16 v[196:199], v[184:187], v[242:245], v[196:199]\n" \
  "s_cmp_eq_u32 s78, 10\n" \
  "s_cbranch_scc1 .LVA_OUT_%=\n" \
  "s_waitcnt lgkmcnt(0)\n" \
  "v_alignbit_b32 v230, v222, v220, v6\n" \
  "v_alignbit_b32 v231, v224, v222, v6\n" \
  "v_alignbit_b32 v232, v226, v224, v6\n" \
  "v_alignbit_b32 v233, v228, v226, v6\n" \
  "s_nop 1\n" \
  "v_mfma_f32_16x16x32_bf16 v[196:199], v[188:191], v[230:233], v[196:199]\n" \
  ".LVA_OUT_%=:\n" \
  "v_add_u32 v28, 64, v28\n" \
  "v_add_u32 v29, 64, v29\n" \
  "v_add_u32 v30, 64, v30\n" \
  "v_add_u32 v31, 64, v31\n" \
  "v_add_u32 v32, 64, v32\n" \
  "s_cmp_lg_u32 s74, 3\n" \
  "s_cbranch_scc1 .LVA_FIN_%=\n" \
  "ds_read_u16 v56, v33 offset:0\n" \
  "ds_read_u16 v57, v33 offset:64\n" \
  "v_add_u32 v33, 256, v33\n" \
  "v_permlane32_swap_b32 v196, v197\n" \
  "v_permlane32_swap_b32 v198, v199\n" \
  "v_add_f32 v196, v196, v197\n" \
  "v_add_f32 v198, v198, v199\n" \
  "s_waitcnt lgkmcnt(0)\n" \
  "v_lshlrev_b32 v56, 16, v56\n" \
  "v_lshlrev_b32 v57, 16, v57\n" \
  "v_mul_f32 v56, s64, v56\n" \
  "v_mul_f32 v57, s64, v57\n" \
  "v_cvt_pk_bf16_f32 v197, v196, v198\n" \
  "v_cvt_pk_bf16_f32 v57, v56, v57\n" \
  "v_lshlrev_b32 v196, 16, v197\n" \
  "v_and_b32 v197, 0xffff0000, v197\n" \
  "v_lshlrev_b32 v56, 16, v57\n" \
  "v_and_b32 v57, 0xffff0000, v57\n" \
  "v_add_f32 v56, v56, v196\n" \
  "v_add_f32 v57, v57, v197\n" \
  "v_cvt_pk_bf16_f32 v58, v56, v57\n" \
  "ds_write_b16 v34, v58\n" \
  "ds_write_b16_d16_hi v34, v58 offset:64\n" \
  ".LVA_FIN_%=:\n" \
  "s_cmp_lg_u32 s79, 0\n" \
  "s_cbranch_scc1 .LVA_NOG_%=\n" \
  "v_lshlrev_b32 v60, 6, v7\n" \
  "s_waitcnt vmcnt(0)\n" \
  "s_mov_b64 exec, 255\n" \
  "global_store_dword v60, v5, s[60:61] sc1\n" \
  "s_mov_b64 exec, -1\n" \
  "s_add_u32 s60, s60, 0x10000\n" \
  "s_addc_u32 s61, s61, 0\n" \
  ".LVA_NOG_%=:\n" \
  "s_add_u32 s62, s62, 1\n" \
  "s_cmp_eq_u32 s62, 64\n" \
  "s_cbranch_scc1 .LVA_END_%=\n" \
  ".LVA_TAPS_%=:\n" \
  "s_and_b32 s74, s62, 3\n" \
  "s_lshl_b32 s76, s74, 6\n" \
  "v_add_u32 v56, s76, v5\n" \
  "s_cmp_eq_u32 s74, 3\n" \
  "s_cselect_b32 s74, 0, 256\n" \
  "v_add_u32 v57, s74, v56\n" \
  "ds_read_b128 v[128:131], v56 offset:3840\n" \
  "ds_read_b128 v[132:135], v56 offset:3584\n" \
  "ds_read_b128 v[136:139], v56 offset:3328\n" \
  "ds_read_b128 v[140:143], v56 offset:3072\n" \
  "ds_read_b128 v[192:195], v57 offset:3648\n" \
  "s_cmp_ge_u32 s62, 48\n" \
  "s_cbranch_scc1 .LVA_TOP_%=\n" \
  "ds_read_b128 v[144:147], v56 offset:2816\n" \
  "ds_read_b128 v[148:151], v56 offset:2560\n" \
  "ds_read_b128 v[152:155], v56 offset:2304\n" \
  "ds_read_b128 v[156:159], v56 offset:2048\n" \
  "ds_read_b128 v[160:163], v56 offset:1792\n" \
  "ds_read_b128 v[164:167], v56 offset:1536\n" \
  "ds_read_b128 v[168:171], v56 offset:1280\n" \
  "ds_read_b128 v[172:175], v56 offset:1024\n" \
  "ds_read_b128 v[176:179], v56 offset:768\n" \
  "ds_read_b128 v[180:183], v56 offset:512\n" \
  "ds_read_b128 v[184:187], v56 offset:256\n" \
  "ds_read_b128 v[188:191], v56 offset:0\n" \
  "s_branch .LVA_TOP_%=\n" \
  ".LVA_END_%=:\n" \
  "s_endpgm\n"
__device__ __forceinline__ v4u mk_srd(const void* p, const unsigned bytes) {
  const unsigned long long a = (unsigned long long)p;
  return v4u{(unsigned)a, (unsigned)(a >> 32) & 0xffffu, bytes, 0x00020000u};
}
template <bool ATT>
__device__ __forceinline__ void role_chain_t(const MegaArgs& A, char* smem, const int L, const int h, const int rep) {
  __builtin_amdgcn_s_waitcnt(0x0F70);
  u16* swo = reinterpret_cast<u16*>(smem);
  const int w = __builtin_amdgcn_readfirstlane(threadIdx.x >> 6);
  const int wc = (w + ROT) & 7, psw = PSWH;
  char* ws = A.ws;
  unsigned* FL = (unsigned*)(ws + OFF_FL);
  unsigned* TMO = FL + FL_TMO;
#define LANE_IDS()                         \
  const int l = lane_id();                 \
  const int c = l & 15, g = l >> 4;        \
  (void)c; (void)g;
  float* sgm = reinterpret_cast<float*>(smem + C_X);
  float* sgf = sgm + 8;
  unsigned* FQ = FL + FL_FQ + L * 65 * 32 * 32;
  v8u w0[8], w1[8], w2[8];
  {
    LANE_IDS();
    v4u m0r;
    const unsigned sb = lds_base(smem);
    const int q = (l >> 2) & 3, p = l & 3;
    const int f = (q & 1) | ((g & 1) << 1) | ((q >> 1) << 2);
    const unsigned ra = sb + (16 * (g >> 1) + 4 * (g & 1) + (q & 1) + 8 * (q >> 1)) * 256 + (w ^ f) * 32 + p * 8;
#define W_READ(W, hf, X)                                                       \
  {                                                                           \
    _Pragma("unroll") for (int t = 0; t < 4; ++t) {                           \
      const unsigned a_ = (ra ^ (X)) + (hf) * 8192 + t * 32768;               \
      const v2u r0 = trd(a_), r1 = trd(a_ + 512), r2 = trd(a_ + 16384), r3 = trd(a_ + 16384 + 512); \
      W[4 * (hf) + t] = v8u{r0.x, r0.y, r1.x, r1.y, r2.x, r2.y, r3.x, r3.y};  \
    }                                                                         \
    _Pragma("unroll") for (int t = 0; t < 4; ++t) asm volatile("" : "+v"(W[4 * (hf) + t])); \
  }
#define W_STAGE(M, hf, VM, NEXT)                                               \
  {                                                                           \
    WAIT_VM(VM);                                                              \
    RAW_BAR();                                                                \
    if ((M) == 0 && !ATT) { W_READ(w0, hf, 0); W_READ(w2, hf, 128); }         \
    if ((M) == 1 && !ATT) { W_READ(w1, hf, 0); }                              \
    if ((M) == 1 && ATT) { W_READ(w0, hf, 0); }                               \
    if ((M) == 2 && ATT) { W_READ(w1, hf, 0); W_READ(w2, hf, 128); }          \
    RAW_BAR();                                                                \
    NEXT;                                                                     \
  }
#define WO_ISSUE(hf)                                                          \
  {                                                                           \
    const char* base_ = (const char*)A.Wo + (size_t)(h * DH + 64 * (hf) + 8 * w) * (DM * 2); \
    _Pragma("unroll") for (int i = 0; i < 8; ++i) {                           \
      const int fd = (i & 1) | (((i >> 2) & 1) << 1) | ((w & 1) << 2);        \
      glds16(base_, (unsigned)(i * 1024 + ((l ^ (fd << 1)) << 4)), sb + w * 16384 + (hf) * 8192 + i * 1024); \
    }                                                                         \
  }
    wqkv_issue<true>(A.Wv, 0, h, w, l, sb);
    wqkv_issue<true>(A.Wv, 1, h, w, l, sb);
    asm volatile("s_nop 4\n\tglobal_load_dwordx4 %0, %1, %2" : "=v"(m0r) : "v"(((2 * (c >> 2) + (c & 1)) * DM + 64 * w + 16 * g + 8 * (((c >> 1) ^ (c >> 3)) & 1)) * 2), "s"(A.mem0) : "memory");
    W_STAGE(2, 0, 9, wqkv_issue<true>(A.Wk, 0, h, w, l, sb));
    SST(4);
    W_STAGE(2, 1, 8, wqkv_issue<true>(A.Wk, 1, h, w, l, sb));
    SST(5);
    asm volatile("" : "+v"(m0r));
    *reinterpret_cast<v4u*>(smem + C_SM + w * 1024 + l * 16) = m0r;
    {
      float f2[4] = {};
      if (ATT) {
#pragma unroll
        for (int t = 0; t < 8; ++t)
#pragma unroll
          for (int e = 0; e < 8; ++e) {
            f2[e & 1] = dot2sq(w1[t][e], f2[e & 1]);
            f2[2 + (e & 1)] = dot2sq(w2[t][e], f2[2 + (e & 1)]);
          }
      }
      const float f = wave_sum_dpp((f2[0] + f2[1]) + (f2[2] + f2[3]));
      if (l == 0) { sgf[w] = f; sgm[16] = 0.f; sgm[32 + w] = 0.f; }
    }
    v4u zt;
    v2u ze;
#define Z_LOAD()                                                                                                        \
  asm volatile("global_load_dwordx4 %0, %2, %3 sc1\n\tglobal_load_dwordx2 %1, %4, %5 sc1"                              \
               : "=&v"(zt), "=&v"(ze)                                                                                   \
               : "v"(l * 16), "s"(ws + OFF_TAG + (blockIdx.x & 7) * 1024), "v"(0), "s"(ws + OFF_TAG + 8192)              \
               : "memory")
    W_STAGE(1, 0, 8, Z_LOAD(); wqkv_issue<true>(A.Wq, 0, h, w, l, sb));
#undef Z_LOAD
    W_STAGE(1, 1, 8, wqkv_issue<true>(A.Wq, 1, h, w, l, sb));
    asm volatile("" : "+v"(zt), "+v"(ze));
    if (w == 0) {
      if (!zero_seen(zt, ze)) zero_wait(ws);
      const float f2 = (sgf[0] + sgf[1]) + (sgf[2] + sgf[3]);
      __builtin_amdgcn_raw_buffer_store_b32(__float_as_uint(f2) | 1u, MKRS(FL + FL_G, L ? 0 : 4 * 512), l * 512 + h * 4, 0, AUX_SC1);
    }
    SST(6);
    W_STAGE(0, 0, 8, WO_ISSUE(0));
    W_STAGE(0, 1, 8, WO_ISSUE(1));
    SST(7);
#undef W_STAGE
#undef W_READ
#undef WO_ISSUE
    const unsigned rb = sb + (2 * (g >> 1) + (q >> 1)) * 16384 + (4 * (g & 1) + (q & 1)) * 1024 + p * 8;
#pragma unroll
    for (int hf = 0; hf < 2; ++hf) {
      if (hf == 0) WAIT_VM(8); else WAIT_VM(0);
      RAW_BAR();
      v4u fr[8];
#pragma unroll
      for (int i = 0; i < 8; ++i) {
        const unsigned a_ = rb + hf * 8192 + (i & 1) * 65536 + ((4 * wc + ((i >> 1) ^ psw)) ^ f) * 32;
        const v2u r0 = trd(a_), r1 = trd(a_ + 2048);
        fr[i] = v4u{r0.x, r0.y, r1.x, r1.y};
      }
#pragma unroll
      for (int i = 0; i < 8; ++i) asm volatile("" : "+v"(fr[i]));
      RAW_BAR();
#pragma unroll
      for (int i = 0; i < 8; ++i) *reinterpret_cast<v4u*>(swo + w * 8192 + hf * 4096 + i * 512 + l * 8) = fr[i];
    }
  }
  WSTAMP2();
  const unsigned sb = lds_base(smem);
  const char* gK = ws + OFF_QK + L * SZ_QK + ((size_t)h * 16 + 2 * w) * 1024;
  const char* gV = ws + OFF_VF + L * SZ_O + ((size_t)h * 8 + 2 * w - 8) * 1024;
  float gw2;
  {
    const gu32* gp = (gu32*)(FL + FL_G + REP * 128) + (lane_id() & 31);
    unsigned g0, g1, g2;
    for (unsigned spins = 0;; ++spins) {
      g0 = __hip_atomic_load(gp, RLX_AGENT);
      g1 = __hip_atomic_load(gp + 32, RLX_AGENT);
      g2 = __hip_atomic_load(gp + 64, RLX_AGENT);
      if (__all(g0 != 0u && g1 != 0u && g2 != 0u)) break;
      if (spin_timeout(spins, TMO)) return;
      __builtin_amdgcn_s_sleep(4);
    }
    const float gm = wave_max_dpp(__uint_as_float(g0) * (__uint_as_float(g1) + __uint_as_float(g2)));
    asm volatile("s_nop 0\n\tv_readfirstlane_b32 %0, %1" : "=s"(gw2) : "v"(gm * 1.07f));
  }
  v8u pv;
  {
    LANE_IDS();
    const unsigned voff = g * 256 + c * 8, wp = sb + w * 16384 + l * 16;
    pv[0] = (((c >> 1) ^ (c >> 3)) & 1) ? 0xEEEE : 0x4444;
    pv[1] = sb + C_SM + l * 16;
    pv[2] = ATT ? sb + ((c & 2) ? C_SK : C_SQ) + ((2 * (c >> 2) + (c & 1)) * QS + 8 * g) * 2 : voff;
    pv[3] = ATT ? l * 8 : wp;
    pv[4] = sb + ((c & 2) ? C_SQ : C_SO) + w * 256 + ((c & 13) + ((g ^ (c >> 2)) & 2)) * 16 + 8 * ((g ^ (c >> 1)) & 1);
    pv[5] = voff;
    pv[6] = wp;
    pv[7] = sb + C_SO + (l >> 5) * 1024 + w * 256 + (l & 31) * 8;
  }
  if (!ATT) {
    if (!wait_n<4, 1>(FQ + h * 32, 1, TMO)) return;
    const unsigned lo = lane_id() * 16, d = sb + C_KT + (2 * w - 8) * 1024;
    glds16c(gK, lo, d);
    glds16c(gK + 1024, lo, d + 1024);
    glds16c(gV, lo, d + 8192);
    glds16c(gV + 1024, lo, d + 9216);
  }
  DRAIN();
  SST(8);
  const v4u rsA = mk_srd(ws + OFF_ACC + L * SZ_ACC, SZ_ACC);
  const v4u rsF = mk_srd(FQ, 64 * 32 * 32 * 4);
  const v4u rsMK = mk_srd(ws + OFF_MEMK + L * SZ_MEM, SZ_MEM);
  const v4u rsMV = mk_srd(ws + OFF_MEMV + L * SZ_MEM, SZ_MEM);
  const int wr = (w + RR) & 7;
  const char* pAB = ws + OFF_ACC + L * SZ_ACC + (wc - wr) * 4096;
  const char* pGK = gK + NH * 16 * 1024;
  const char* pGV = ATT ? (const char*)(size_t)(CDS + (XG(h) >> 1) * CDL) : gV + NH * 8 * 1024;
  unsigned sc[8];
  sc[0] = ATT ? L * 1024 + XG(h) * 256 : (32 + h) * 128;
  sc[1] = ATT ? 1032 - L * 1024 + h * 4096 : (h * 4 + w - 4) * 512;
  sc[2] = h * 2048 + w * 512;
  sc[3] = NSEG - 1;
  sc[4] = __float_as_uint(gw2);
  sc[5] = w;
  sc[6] = sb;
  const char* dbg = ws;
  const unsigned tg = (now10() / GR) * GR;
  const v4u rsT = mk_srd(FL + FL_TK, 2048);
#define CHAIN_OPS                                                                                                          \
  ::"{v[64:71]}"(w0[0]), "{v[72:79]}"(w0[1]), "{v[80:87]}"(w0[2]), "{v[88:95]}"(w0[3]), "{v[96:103]}"(w0[4]),              \
      "{v[104:111]}"(w0[5]), "{v[112:119]}"(w0[6]), "{v[120:127]}"(w0[7]), "{v[128:135]}"(w1[0]), "{v[136:143]}"(w1[1]),    \
      "{v[144:151]}"(w1[2]), "{v[152:159]}"(w1[3]), "{v[160:167]}"(w1[4]), "{v[168:175]}"(w1[5]), "{v[176:183]}"(w1[6]),    \
      "{v[184:191]}"(w1[7]), "{v[192:199]}"(w2[0]), "{v[200:207]}"(w2[1]), "{v[208:215]}"(w2[2]), "{v[216:223]}"(w2[3]),    \
      "{v[224:231]}"(w2[4]), "{v[232:239]}"(w2[5]), "{v[240:247]}"(w2[6]), "{v[248:255]}"(w2[7]), "{v[0:7]}"(pv),          \
      "{s[40:43]}"(rsA), "{s[44:47]}"(rsF), "{s[48:51]}"(rsMK), "{s[52:55]}"(rsMV), "{s[56:57]}"(pAB), "{s60}"(wr * 4096), "{s61}"(h * 1024 - wr * 4096), \
      "{s[62:63]}"(pGK), "{s[64:65]}"(pGV), "{s68}"(sc[0]), "{s69}"(sc[1]), "{s70}"(sc[2]),    \
      "{s71}"(sc[3]), "{s72}"(sc[4]), "{s73}"(sc[5]), "{s74}"(sc[6]), "{s[36:37]}"(dbg),                   \
      "{s34}"(tg), "{s35}"(psw * 1024), [gp] "n"(GP), [gr] "n"(GR), [gm] "n"(GM), \
      "{s[16:19]}"(rsT), "{s20}"(0), "{s22}"(sb + C_X + 128 + 8 * (w & 3)), "{s24}"(L * 1024), [cd] "n"(CD - CDS), \
      "{s25}"(APA - (APN + 1) * APP + L * (APB - APA + (2 * APN + 1) * APP)), [ka] "n"(APL), [an] "n"(APN), [d0] "n"(16 * CD0), [rq] "n"(RQM), [pp] "n"(APP) \
      : "memory", "vcc", "scc", "s99", "v63"
  if (ATT) asm volatile("s_mov_b64 exec, -1\ns_nop 4\n" CHAIN_ASM_ATT CHAIN_OPS);
  else asm volatile("s_mov_b64 exec, -1\ns_nop 4\n" CHAIN_ASM_VO CHAIN_OPS);
#undef CHAIN_OPS
#undef LANE_IDS
}

__device__ __forceinline__ void role_chain(const MegaArgs& A, char* smem, const int L, const int h, const int rep) {
  if (__builtin_amdgcn_readfirstlane(threadIdx.x >> 6) < 4) role_chain_t<true>(A, smem, L, h, rep);
  else role_chain_t<false>(A, smem, L, h, rep);
}

constexpr int AW_TK = 0, AW_SO = 114688, AW_M = AW_SO + 16384, AW_P = 24576, AW_END = AW_SO + 28672 + 16384;
__device__ __forceinline__ unsigned umin3(unsigned a, unsigned b, unsigned c) {
  return __builtin_elementwise_min(__builtin_elementwise_min(a, b), c);
}
__device__ __forceinline__ bool all_set(const unsigned r) {
  unsigned long long m;
  asm volatile("v_cmp_eq_u32 %0, 0, %1" : "=s"(m) : "v"(r) : "memory");
  return m == 0ull;
}
__device__ __forceinline__ unsigned long long zero_mask(const unsigned r) {
  unsigned long long m;
  asm volatile("v_cmp_eq_u32 %0, 0, %1" : "=s"(m) : "v"(r) : "memory");
  return m;
}
__device__ __forceinline__ int job_seg(const int n) {
  const int m = n - AWD;
  const int r = (m & 1) ? 64 + (m >> 1) : AWD + (m >> 1);
  return __builtin_amdgcn_readfirstlane(n > 127 ? 128 : (n < AWD || n >= 128 - AWD) ? n : r);
}
template <bool AT>
__device__ __forceinline__ void role_aw_t(const MegaArgs& A, char* smem, const int mode, const int a) {
  __builtin_amdgcn_s_waitcnt(0x0F70);
  const int w = __builtin_amdgcn_readfirstlane(threadIdx.x >> 6);
  const int l = threadIdx.x & 63, c = l & 15, g = l >> 4;
  const int qt = w & 1, hh = (w >> 1) & 1, h = 2 * a + hh;
  char* ws = A.ws;
  unsigned* FL = (unsigned*)(ws + OFF_FL);
  const unsigned sb = lds_base(smem);
  v4u wo[4][8];
  const unsigned* FQ = FL + FL_FQ + 1024 + h * 32;
  const v4u rsW = mk_srd(ws + OFF_PW, SZ_PW);
  const v4u rsF = mk_srd(FQ, (129 * 32 - h) * 128);
  const char* gQ = ws + OFF_QK + ((size_t)h * 16 + 8 * qt) * 1024;
  const char* gV = ws + OFF_VF + ((size_t)h * 8 + 4 * qt) * 1024;
  const char* gM = ws + (qt ? OFF_MEMV : OFF_MEMK) + h * 2048;
  const unsigned mo = sb + AW_M + hh * 4096;
  const unsigned tq0 = __builtin_amdgcn_readfirstlane(sb + AW_TK + (hh * 24 + 8 * qt) * 1024);
  const unsigned tv0 = __builtin_amdgcn_readfirstlane(sb + AW_TK + (hh * 24 + 16 + 4 * qt) * 1024);
  const int gs = job_seg(mode);
  bool got = false;
  {
    const int q = (l >> 2) & 3, p = l & 3, f = q | ((g & 1) << 2);
    const unsigned ra = sb + (4 * g + q) * 256 + (p & 1) * 16;
    unsigned n0 = 0u;
#pragma unroll
    for (int st = 0; st < 4; ++st) {
      if (st == 3 && got) WAIT_VM(12); else if (st == 3) WAIT_VM(0); else WAIT_VM(8);
      RAW_BAR();
#pragma unroll
      for (int k2 = 0; k2 < 2; ++k2) {
        const int ks = 2 * st + k2;
#pragma unroll
        for (int ct = 0; ct < 4; ++ct) {
          const int cp = 2 * w + (ct >> 1);
          const unsigned a_ = ra + (st & 1) * 65536 + (cp >> 2) * 16384 + k2 * 4096 + ((2 * (cp & 3) + (p >> 1)) ^ f) * 32 + (ct & 1) * 8;
          const v2u lo = trd(a_), hi = trd(a_ + 8192);
          wo[ct][ks] = v4u{lo.x, hi.x, lo.y, hi.y};
        }
#pragma unroll
        for (int ct = 0; ct < 4; ++ct) asm volatile("" : "+v"(wo[ct][ks]));
      }
      RAW_BAR();
      if (!AT && st == 1)
        asm volatile("s_nop 4\n\tbuffer_load_dword %0, %1, %2, %3 offen sc1" : "=v"(n0) : "v"(0), "s"(rsF), "s"(gs * 4096) : "memory");
      if (st < 2) wo_issue(A.Wo, a, st + 2, sb);
      if (!AT && st == 2 && all_set(n0)) {
#pragma unroll
        for (int i = 0; i < 8; ++i) glds16c(gQ + (size_t)gs * (NH * 16 * 1024) + i * 1024, l * 16, tq0 + i * 1024);
#pragma unroll
        for (int i = 0; i < 4; ++i) glds16c(gV + (size_t)gs * (NH * 8 * 1024) + i * 1024, l * 16, tv0 + i * 1024);
        got = true;
      }
    }
  }
  v8u pv, mk, wr[16];
  {
    const unsigned so = sb + AW_SO + l * 16, ta = sb + AW_TK + hh * 24576 + l * 16;
    const unsigned ix = (c & 2) ? 0xEEEE : 0x4444;
    pv = v8u{AT ? ix : l * 16u, so, (unsigned)(HK3 ? (l >> 3) * 2048 + (l & 7) * 16 : (c >> 1) * 2048 + (c & 1) * 64 + g * 16), AT ? ta : mo + qt * 2048 + l * 16, AT ? ta + qt * 4096 : ix,
             mo + (g * 8 + 2 * (c >> 2) + (c & 1)) * 16, sb + AW_M + 2048 + (g >> 1) * 512 + (c & 3) * 128 + (8 * (g & 1) + (c >> 2)) * 8,
             so + (hh * 2 + qt) * 4096};
#pragma unroll
    for (int i = 0; i < 8; ++i) mk[i] = 16 * (i >> 2) + 4 * g + (i & 3) > 16 * qt + c ? __float_as_uint(-3e38f) : 0u;
#pragma unroll
    for (int i = 0; i < 16; ++i) wr[i] = __builtin_shufflevector(wo[i >> 2][2 * (i & 3)], wo[i >> 2][2 * (i & 3) + 1], 0, 1, 2, 3, 4, 5, 6, 7);
  }
  const unsigned tb = now10() - 130 * GPER;
  const char* dbg = ws;
#define AW_OPS                                                                                                               \
  ::"{v[128:135]}"(wr[0]), "{v[136:143]}"(wr[1]), "{v[144:151]}"(wr[2]), "{v[152:159]}"(wr[3]), "{v[160:167]}"(wr[4]),        \
      "{v[168:175]}"(wr[5]), "{v[176:183]}"(wr[6]), "{v[184:191]}"(wr[7]), "{v[192:199]}"(wr[8]), "{v[200:207]}"(wr[9]),      \
      "{v[208:215]}"(wr[10]), "{v[216:223]}"(wr[11]), "{v[224:231]}"(wr[12]), "{v[232:239]}"(wr[13]), "{v[240:247]}"(wr[14]), \
      "{v[248:255]}"(wr[15]), "{v[0:7]}"(pv), "{v[8:15]}"(mk), "{s[40:43]}"(rsW), "{s[44:47]}"(rsF), "{s[48:49]}"(gQ),        \
      "{s[50:51]}"(gV), "{s[52:53]}"(gM), "{s[54:55]}"(FL + FL_FW + (a >> 2) * 32 + (a & 3)), "{s56}"(mode), "{s57}"(gs),      \
      "{s60}"(a * 32768 + w * 256), "{s61}"(tq0), "{s62}"(tv0), "{s63}"(AT ? mo + 2048 - (sb + AW_SO) / 2 : mo + qt * 2048), "{s64}"(tb), "{s65}"(tb),            \
      "{s66}"(AT ? AW_P + qt * 1024 + hh * 4 : a * 32768 + (w - 4) * 8192), "{s[82:83]}"(w == 4 ? 15ull : 0ull), "{s67}"(__builtin_amdgcn_readfirstlane((int)got)), "{s80}"(0x3e0293ee), "{s[36:37]}"(dbg),        \
      [wd] "n"(AWD), [w2] "n"(128 - 2 * AWD), [w3] "n"(131 - AWD), [dt] "n"(AWDT), [gp] "n"(GPER), [ga] "n"(GMA), [gs] "n"(GSTEP), [sm] "n"(SLP_M), [sa] "n"(SLP_A) \
      : "memory", "vcc", "scc", "s81", "v127"
  if (AT) asm volatile("s_mov_b64 exec, -1\ns_nop 4\n" AW_ASM_0 AW_OPS);
  else asm volatile("s_mov_b64 exec, -1\ns_nop 4\n" AW_ASM_1 AW_OPS);
#undef AW_OPS
}
__device__ __forceinline__ void role_aw(const MegaArgs& A, char* smem, const int L, const int a) {
  if (__builtin_amdgcn_readfirstlane(threadIdx.x >> 6) < 4) role_aw_t<true>(A, smem, L, a);
  else role_aw_t<false>(A, smem, L, a);
}

__device__ __forceinline__ void role_red(const MegaArgs& A, char* smem, const int idx) {
  const int w = __builtin_amdgcn_readfirstlane(threadIdx.x >> 6);
  const int l = threadIdx.x & 63;
  const int L = w >> 2, gr = (w >> 1) & 1, r = idx * 2 + (w & 1);
  char* ws = A.ws;
  unsigned* FL = (unsigned*)(ws + OFF_FL);
  unsigned* TMO = FL + FL_TMO;
  const __amdgpu_buffer_rsrc_t rsW = MKRS(ws + OFF_PW, SZ_PW);
  const __amdgpu_buffer_rsrc_t rsX = MKRS(ws + OFF_XA, OFF_X3 - OFF_XA + SZ_X);
  volatile lu32* sm32 = (lu32*)(unsigned long)lds_base(smem);
  volatile lu32* sw = sm32 + (w >> 1) * 32;
  if (threadIdx.x < 128) sm32[threadIdx.x] = 0u;
  __syncthreads();
  unsigned tb = now10() + (L * (APN + 2) + 1 + gr) * GPER;
  const int rn1 = idx < 8 ? 96 + idx : 224 + idx;
  (void)rn1;
  if (L) {
    const __amdgpu_buffer_rsrc_t rz = MKRS(ws + OFF_X2, SZ_X);
    for (int s = gr; s < NSEG; s += 2) ST128((v4u{0u, 0u, 0u, 0u}), rz, l * 16, (s * SEG + r) * DM * 2);
    DRAIN();
    if (l < 4) set_flag(FL + FL_FZ + (2 * l + (idx >> 3)) * 32 + (idx & 7) * 4 + (w & 3));
    if (l == 0) __hip_atomic_fetch_add((lu32*)(unsigned long)(lds_base(smem) + 480), 1u, __ATOMIC_RELAXED, __HIP_MEMORY_SCOPE_WORKGROUP);
  }
  for (unsigned spins = 0; sm32[120] < 4u && spins < 64u * SPIN_CAP; ++spins) __builtin_amdgcn_s_sleep(2);
  const int c = l & 15, g = l >> 4;
  const int pv = g * 32768 + (c >> 2) * 128 + (c & 3) * 16, xv = (16 * (g >> 1) + c) * 512 + (g & 1) * 8, ov = (g >> 1) * 256 + c * 16 + (g & 1) * 8;
  v4u sel0, sel1;
#pragma unroll
  for (int e = 0; e < 4; ++e) {
    const unsigned one = ((c & 7) >> 1) == e ? 0x3f80u << (16 * (c & 1)) : 0u;
    sel0[e] = c < 8 ? one : 0u;
    sel1[e] = c < 8 ? 0u : one;
  }
  int tries = SPIN_CAP;
  for (int s = gr; s < NSEG; s += 2) {
    const int gs = 64 * L + s;
    const unsigned* fw = FL + FL_FW + (gs * 4 + REP) * 4 * 32;
    RST(0);
    tb = grp_wait<SLP_R, 4>((w & 1) == 0, sw, s + 1, fw, 16, TMO, tb) + (gs >= 125 - AWD ? GATE_RT : 2 * GPER - (L ? GMS : GMR));
#define RTS(k, t) TSR(L ? A.rn : rn1, k, s, (w & 6) * 64 + (t))
    RTS(0, 0);
    RST(1);
    RTS(4, 64);
    const int xo_ = L * (int)(OFF_X3 - OFF_XA) + s * 32768 + r * 16;
    const v2u xa = LD64(rsX, xv, xo_), xb = LD64(rsX, xv + 16384, xo_);
    f32x4 a0, a1;
    for (;; --tries) {
      v4u p[16];
#pragma unroll
      for (int k = 0; k < 16; ++k)
        p[k] = LD128(rsW, pv + (k & 3) * 512, (gs * 16 + (k & 12)) * 32768 + (r >> 1) * 2048 + (r & 1) * 64);
      __builtin_amdgcn_sched_barrier(0);
      a0 = f32x4{};
      a1 = f32x4{};
#pragma unroll
      for (int k = 0; k < 16; k += 4) {
        a0 = MFMA16(BF8(sel0), BF8(p[k]), a0);
        a1 = MFMA16(BF8(sel0), BF8(p[k + 2]), a1);
        a0 = MFMA16(BF8(sel1), BF8(p[k + 1]), a0);
        a1 = MFMA16(BF8(sel1), BF8(p[k + 3]), a1);
      }
      if (!__any(__builtin_isunordered(a0[0], a1[0]) | __builtin_isunordered(a0[1], a1[1]) | __builtin_isunordered(a0[2], a1[2]) |
                 __builtin_isunordered(a0[3], a1[3])) || tries <= 0) break;
      asm volatile("s_sleep %0" ::"n"(SLP_P) : "memory");
    }
    RST(2);
    const unsigned nz = L ? 0u : 1u;
    const v4u xr = {xa.x, xa.y, xb.x, xb.y}, ar = {pk2(a0[0], a0[1]), pk2(a0[2], a0[3]), pk2(a1[0], a1[1]), pk2(a1[2], a1[3])};
    v4u xo;
#pragma unroll
    for (int e = 0; e < 4; ++e)
      xo[e] = __builtin_elementwise_max(pk2(lo16(xr[e]) + lo16(ar[e]), hi16(xr[e]) + hi16(ar[e])), nz);
    const __amdgpu_buffer_rsrc_t rsO = MKRS(L ? (char*)A.out : ws + OFF_X2, SZ_X);
    ST64((v2u{xo.x, xo.y}), rsO, ov, (s * SEG + r) * DM * 2);
    ST64((v2u{xo.z, xo.w}), rsO, ov + 512, (s * SEG + r) * DM * 2);
    RTS(1, 0);
    RST(3);
    RTS(5, 64);
    if (L == 0) {
      float sm = 0.f, sq = 0.f;
#pragma unroll
      for (int e = 0; e < 4; ++e) {
        const unsigned x = xo[e];
        sm = __builtin_amdgcn_fdot2_f32_bf16(__builtin_bit_cast(bf16x2_t, x), __builtin_bit_cast(bf16x2_t, 0x3f803f80u), sm, false);
        sq = dot2sq(x, sq);
      }
      float t = swap_add32(sm, sq);
      t += dppr<0x121>(t);
      t += dppr<0x122>(t);
      t += dppr<0x124>(t);
      t += dppr<0x128>(t);
      t = xsum16(t) * (1.0f / DM);
      const float ms = __builtin_bit_cast(float, __builtin_amdgcn_readlane(__builtin_bit_cast(int, t), 32));
      const float var = fmaxf(ms - t * t, 0.f);
      const unsigned st = pk2(t, __builtin_amdgcn_sqrtf(lo16(pk2(lo16(pk2(var, var)) + 1e-5f, 0.f))));
      if (l < 4) __hip_atomic_store((gu32*)(FL + FL_FR + ((s * 4 + l) * 4 + (r >> 3)) * 32 + (r & 7)), st, RLX_AGENT);
    }
    RTS(6, 64);
    RST(4);
    RTS(3, 0);
#undef RTS
  }
  RSD();
  if (idx == 0 && threadIdx.x == 384) {
    gu64* en = (gu64*)(ws + OFF_TAG + 8192);
    __hip_atomic_store(en, __hip_atomic_load(en, RLX_AGENT) + TAG_STEP, RLX_AGENT);
  }
}

constexpr int CUN = 2144;
constexpr int STN = 392;
__device__ __forceinline__ bf16x8 conv_bfrag(const unsigned* up, const unsigned sh) {
  const unsigned d0 = up[0], d1 = up[1], d2 = up[2], d3 = up[3], d4 = up[4];
  const v4u r = {__builtin_amdgcn_alignbit(d1, d0, sh), __builtin_amdgcn_alignbit(d2, d1, sh),
                 __builtin_amdgcn_alignbit(d3, d2, sh), __builtin_amdgcn_alignbit(d4, d3, sh)};
  return BF8(r);
}
__device__ __forceinline__ void role_conv(const MegaArgs& A, char* smem, const int cv) {
  __builtin_amdgcn_s_waitcnt(0x0F70);
  u16* Kr = reinterpret_cast<u16*>(smem);
  u16* Uc = Kr + 8 * 2 * CKN;
  u16* To = Uc + 8 * 2 * CUN;
  u16* Sx = To + 32 * 8;
  u16* St = Sx + 32 * 8;
  const int tid = threadIdx.x;
  const int w = __builtin_amdgcn_readfirstlane(tid >> 6);
  const int l = tid & 63, c = l & 15, g = l >> 4;
  const int d = 8 * cv + w;
  char* ws = A.ws;
  unsigned* FL = (unsigned*)(ws + OFF_FL);
  unsigned* TMO = FL + FL_TMO;
  u16* kr = Kr + w * 2 * CKN;
  float* prm = reinterpret_cast<float*>(kr);
  v4u tx[4];
  const v2u en = LD64(MKRS(ws + OFF_TAG, 8192 + 8), 0, 8192);
  {
    const unsigned pa = *reinterpret_cast<const unsigned*>(A.logAre + d * NST + 2 * l);
    const unsigned pb = *reinterpret_cast<const unsigned*>(A.Aim + d * NST + 2 * l);
    const unsigned pc = *reinterpret_cast<const unsigned*>(A.Cre + d * NST + 2 * l);
    const unsigned pd = *reinterpret_cast<const unsigned*>(A.Cim + d * NST + 2 * l);
    const float dt = bfr(expf(bf2f(A.logdt[d])));
    if (w < 2) tx[0] = *reinterpret_cast<const v4u*>(A.x0 + (size_t)tid * DM + 8 * cv);
    if (l < 16) reinterpret_cast<unsigned*>(Uc + w * CUN)[l] = 0u;
    if (l < 32) reinterpret_cast<unsigned*>(Uc + w * CUN + 32 + L_SEQ)[l] = 0u;
    if (l < 32) reinterpret_cast<unsigned*>(Uc + w * CUN + 32 + 128)[l] = 0u;
    if (l < 50) {
      reinterpret_cast<v4u*>(kr + 1808)[l] = v4u{0u, 0u, 0u, 0u};
      reinterpret_cast<v4u*>(kr + CKN + 1808)[l] = v4u{0u, 0u, 0u, 0u};
    }
#pragma unroll
    for (int e = 0; e < 2; ++e) {
      const float ar = -bfr(expf(e ? hi16(pa) : lo16(pa))) * dt, ai = (e ? hi16(pb) : lo16(pb)) * dt;
      const float ex = expf(ar);
      float sn, cs;
      sincosf(ai, &sn, &cs);
      prm[2 * l + e] = ar;
      prm[NST + 2 * l + e] = ai;
      prm[2 * NST + 2 * l + e] = (e ? hi16(pc) : lo16(pc)) * dt;
      prm[3 * NST + 2 * l + e] = (e ? hi16(pd) : lo16(pd)) * dt;
      prm[4 * NST + 2 * l + e] = ex * cs;
      prm[5 * NST + 2 * l + e] = ex * sn;
    }
  }
  const float dsk = bf2f(A.Dskip[d]);
  const u16* ka = kr + (c >> 3) * CKN + 2080 - 16 * (c & 7) + 8 * g;
  const unsigned ush = (c & 1) * 16;
#define KFRAG(D0) (*reinterpret_cast<const bf16x8*>(ka - (D0)))
  const __amdgpu_buffer_rsrc_t rz1 = MKRS(ws + OFF_XA, SZ_X);
  const int t0 = 16 * (4 * (g & 1) + (g >> 1)) + c;
  {
    const u16* uc = Uc + w * CUN;
    const unsigned* ub = reinterpret_cast<const unsigned*>(uc + (c & ~1) + 8 * g + 32);
    const __amdgpu_buffer_rsrc_t rst = MKRS(ws + OFF_GT, L_SEQ * 4);
#define TILE_OUT(dst, av, T)                                                                   \
  {                                                                                            \
    const u16* u_ = Uc + w * CUN + 32 + 128 * (T) + t0;                                        \
    const unsigned uu = pk2(bf2f(u_[0]) * dsk, bf2f(u_[32]) * dsk);                            \
    const unsigned yy = pk2(swap_add32(av[0], av[1]), swap_add32(av[2], av[3]));               \
    const unsigned aa = pk2(lo16(yy) + lo16(uu), hi16(yy) + hi16(uu));                         \
    (dst)[t0] = (u16)aa;                                                                       \
    (dst)[t0 + 32] = (u16)(aa >> 16);                                                          \
  }
#define CHUNK_ST(ck, xr, src, n)                                                               \
  {                                                                                            \
    v4u o;                                                                                     \
    _Pragma("unroll") for (int e = 0; e < 4; ++e)                                              \
      o[e] = pk2(lo16(xr[e]) + bf2f((src)[(2 * e) * (n) + l]), hi16(xr[e]) + bf2f((src)[(2 * e + 1) * (n) + l])); \
    ST128(o, rz1, (l >> 5) * 32768 + (l & 31) * 16, (2 * (ck) * 64 + cv) * 512);               \
  }
#define U_ROW(row, xr_) U_ROW2(row, xr_, __builtin_amdgcn_raw_buffer_load_b32(rst, (row) * 4, 0, AUX_SC1))
#define U_ROW2(row, xr_, stv)                                                                       \
  {                                                                                            \
    const unsigned st_ = (stv);                                                                \
    const float mb_ = lo16(st_), ri_ = 1.0f / hi16(st_);                                       \
    _Pragma("unroll") for (int e = 0; e < 4; ++e) {                                            \
      const unsigned t_ = pk2(lo16(xr_[e]) - mb_, hi16(xr_[e]) - mb_);                         \
      const unsigned q_ = pk2(lo16(t_) * ri_, hi16(t_) * ri_);                                 \
      Uc[(2 * e) * CUN + 32 + (row)] = (u16)q_;                                                \
      Uc[(2 * e + 1) * CUN + 32 + (row)] = (u16)(q_ >> 16);                                    \
    }                                                                                          \
  }
    const unsigned* FS = FL + FL_FS + REP * 32 * 32;
    const int gi = 4 * (g * 8 + (l & 7));
    const int tl = 32 * ((l >> 3) & 1) + 8 * g + (l & 7);
    unsigned khl = 0u;
#pragma unroll 1
    for (int ps = 0; ps < 4; ++ps) {
      float v[32];
      {
        const f32x4 ar = *reinterpret_cast<const f32x4*>(prm + gi), ai = *reinterpret_cast<const f32x4*>(prm + NST + gi);
        const f32x4 cr = *reinterpret_cast<const f32x4*>(prm + 2 * NST + gi), ci = *reinterpret_cast<const f32x4*>(prm + 3 * NST + gi);
        const f32x4 wr = *reinterpret_cast<const f32x4*>(prm + 4 * NST + gi), wi = *reinterpret_cast<const f32x4*>(prm + 5 * NST + gi);
        const float p0 = (float)(64 * ps + 32 * ((l >> 3) & 1));
        float yr0[4], yi0[4];
#pragma unroll
        for (int k = 0; k < 4; ++k) {
          const float e = __expf(ar[k] * p0);
          float sn, cs;
          __sincosf(ai[k] * p0, &sn, &cs);
          const float zr = e * cs, zi = e * sn;
          yr0[k] = cr[k] * zr - ci[k] * zi;
          yi0[k] = cr[k] * zi + ci[k] * zr;
        }
        f32x2_t yra = {yr0[0], yr0[1]}, yia = {yi0[0], yi0[1]}, wra = {wr[0], wr[1]}, wia = {wi[0], wi[1]};
        f32x2_t yrb = {yr0[2], yr0[3]}, yib = {yi0[2], yi0[3]}, wrb = {wr[2], wr[3]}, wib = {wi[2], wi[3]};
#pragma unroll
        for (int i = 0; i < 32; ++i) {
          const f32x2_t sm2 = yra + yrb;
          v[i] = sm2.x + sm2.y;
          const f32x2_t ta = yra * wra - yia * wia, tb = yrb * wrb - yib * wib;
          yia = yra * wia + yia * wra;
          yib = yrb * wib + yib * wrb;
          yra = ta;
          yrb = tb;
        }
      }
#pragma unroll
      for (int i = 0; i < 16; ++i) v[i] = swap_add32(v[i], v[i + 16]);
#pragma unroll
      for (int i = 0; i < 8; ++i) {
        v[i] = swap_add16(v[i], v[i + 8]);
        v[i] += dppf<0xB1>(0.f, v[i]);
        v[i] += dppf<0x4E>(0.f, v[i]);
        v[i] += dppf<0x141>(0.f, v[i]);
      }
      float s0 = v[0], s1 = v[1], s2 = v[2], s3 = v[3], s4 = v[4], s5 = v[5], s6 = v[6], s7 = v[7];
      asm volatile("" : "+v"(s0), "+v"(s1), "+v"(s2), "+v"(s3), "+v"(s4), "+v"(s5), "+v"(s6), "+v"(s7));
      s0 = (l & 4) ? s4 : s0; s1 = (l & 4) ? s5 : s1; s2 = (l & 4) ? s6 : s2; s3 = (l & 4) ? s7 : s3;
      s0 = (l & 2) ? s2 : s0; s1 = (l & 2) ? s3 : s1;
      const float kv = 2.0f * ((l & 1) ? s1 : s0);
      khl = pk2(kv, 0.f);
      khl = pk2(kv, kv - lo16(khl));
      kr[2080 - 64 * ps - tl] = (u16)khl;
      kr[CKN + 2080 - 64 * ps - tl] = (u16)(khl >> 16);
      SST(ps);
      if (ps < 2) {
        if (ps == 0) {
          if (w < 2) {
            const unsigned long long m = (((unsigned long long)en.y << 32) | en.x) + TAG_STEP;
            v4u g0;
            for (unsigned spins = 0; spins < 16u * SPIN_CAP; ++spins) {
              g0 = LD128(MKRS(ws + OFF_G0, 8192), tid * 16, REP * 2048);
              if (__all(((g0.x ^ g0.z) | (g0.y ^ (unsigned)m) | (g0.w ^ (unsigned)(m >> 32))) == 0u)) break;
              asm volatile("s_sleep 2" ::: "memory");
            }
            U_ROW2(tid, tx[0], g0.x);
          }
          __syncthreads();
        }
        f32x4 a0t = {};
#pragma unroll
        for (int b = -1; b < 4; ++b) a0t = MFMA16(KFRAG(-32 * b), conv_bfrag(ub + 16 * b, ush), a0t);
        TILE_OUT(St + w * STN + 128 * ps, a0t, 0);
        __syncthreads();
        if (w == ps) {
          CHUNK_ST(w, tx[0], St + 192 * w, STN);
          if (ps == 0) zero_wait(ws);
          DRAIN();
          if (l < 8) set_flag(FL + FL_FC + ((2 * w * 4 + l) * 8 + (cv >> 3)) * 32 + (cv & 7));
        }
        SST(4 + ps);
      }
      if (ps == 2) {
        if (w >= 2) tx[0] = *reinterpret_cast<const v4u*>(A.x0 + (size_t)tid * DM + 8 * cv);
#pragma unroll
        for (int i = 1; i < 4; ++i) tx[i] = *reinterpret_cast<const v4u*>(A.x0 + (size_t)(tid + 512 * i) * DM + 8 * cv);
      }
    }
    {
      unsigned spins = 0;
      while (!__all(__hip_atomic_load((gu32*)FS + (w + 8 * ((l >> 3) & 3)) * 32 + (l & 7), RLX_AGENT) != 0u)) {
        if (spin_timeout(++spins, TMO)) break;
        __builtin_amdgcn_s_sleep(4);
      }
      asm volatile("" ::: "memory");
    }
    if (w >= 2) U_ROW(tid, tx[0]);
#pragma unroll
    for (int i = 1; i < 4; ++i) U_ROW(tid + 512 * i, tx[i]);
    SST(6);
    LST(0);
    const gu32* kg = (gu32*)(FL + FL_KG + cv * 32 + (w >> 2));
    const __amdgpu_buffer_rsrc_t rk = MKRS(ws + OFF_KF + (size_t)d * 2 * CKN * 2, 2 * CKN * 2);
    v4u t[8];
#define K_LOAD(off)                                                                            \
  _Pragma("unroll") for (int i = 0; i < 4; ++i) {                                              \
    t[i] = LD128(rk, (64 * i + l) * 16, off);                                                  \
    t[4 + i] = LD128(rk, (64 * i + l) * 16, (off) + CKN * 2);                                  \
  }
    unsigned kf = __hip_atomic_load(kg, RLX_AGENT);
    RAW_BAR();
    LST(1);
    const bool early = __builtin_amdgcn_readfirstlane(kf) != 0u;
    K_LOAD(early ? 0 : 4 * CKN);
    kf = __hip_atomic_load(kg, RLX_AGENT);
    {
      f32x4 a1t = {};
#pragma unroll
      for (int b = -1; b < 8; ++b) a1t = MFMA16(KFRAG(128 - 32 * b), conv_bfrag(ub + 16 * b, ush), a1t);
      TILE_OUT(St + w * STN + 256, a1t, 1);
    }
    LST(2);
    RAW_BAR();
    if ((w >> 1) == 1) CHUNK_ST(w, tx[0], St + 128 + 64 * w, STN);
    SST(8);
    LST(3);
    if (!early) {
      if (__builtin_amdgcn_readfirstlane(kf) == 0u) (void)wait_n<8, 1>(FL + FL_KG + cv * 32 + (w >> 2), 1, TMO);
      asm volatile("" ::: "memory");
      K_LOAD(0);
    }
#undef K_LOAD
    SST(7);
    LST(4);
    {
#pragma unroll
      for (int i = 0; i < 4; ++i)
        if (64 * i + l < 229) {
          reinterpret_cast<v4u*>(kr)[64 * i + l] = t[i];
          reinterpret_cast<v4u*>(kr + CKN)[64 * i + l] = t[4 + i];
        }
      kr[2080 - 192 - tl] = (u16)khl;
      kr[CKN + 2080 - 192 - tl] = (u16)(khl >> 16);
    }
    unsigned* f1 = FL + FL_FC + (64 * w + (cv >> 3)) * 32 + (cv & 7);
    if ((w >> 1) == 1) {
      DRAIN();
      if (l < 8) set_flag(f1 + l * 256);
    }
    asm volatile("" ::: "memory");
    LST(5);
    {
      f32x4 a2 = {}, a3 = {};
#pragma unroll
      for (int b = -1; b < 16; ++b) {
        const bf16x8 bf = conv_bfrag(ub + 16 * b, ush);
        if (b < 12) a2 = MFMA16(KFRAG(256 - 32 * b), bf, a2);
        a3 = MFMA16(KFRAG(384 - 32 * b), bf, a3);
      }
      TILE_OUT(St + w * STN, a2, 2);
      TILE_OUT(St + w * STN + 128, a3, 3);
    }
    SST(10);
    LST(6);
    RAW_BAR();
    if (w >= 4) {
      CHUNK_ST(w, tx[0], St + 64 * w - 256, STN);
      DRAIN();
      if (l < 8) set_flag(f1 + l * 256);
    }
    if (w == 1) (void)(wait_n<SLP_V, 32>(FL + FL_FZ + REP * 64, 64, TMO));
    SST(9);
    LST(9);
    LSD();
#undef U_ROW
#undef U_ROW2
#undef TILE_OUT
#undef CHUNK_ST
  }
  const unsigned sb = lds_base(smem);
  const unsigned u1 = sb + (16 * CKN + w * CUN) * 2, u2 = u1 + 16 * CUN, to = sb + (16 * CKN + 16 * CUN) * 2, st = to + 1024;
  const unsigned win = ((c & ~1) + 8 * g) * 2, row = 16 * (g >> 1) + c;
  const bool nil = c < 8 && g < 2;
  if (l < 16) reinterpret_cast<unsigned*>(Uc + (8 + w) * CUN)[l] = 0u;
  bf16x8 th, tw;
#pragma unroll
  for (int i = 0; i < 8; ++i) {
    th[i] = (short)kr[2064 - c + 8 * g + i];
    tw[i] = (short)kr[CKN + 2064 - c + 8 * g + i];
  }
  v4u known;
#pragma unroll
  for (int e = 0; e < 4; ++e) known[e] = (c + 8 * g + 2 * e < 32 ? 0xffffu : 0u) | (c + 8 * g + 2 * e < 31 ? 0xffff0000u : 0u);
  const bf16x8 kz = KFRAG(-96);
  const v8u pa = {u2 + (nil ? 0 : c < 8 ? 32 + 16 * g : 64 + 16 * g), nil ? 0u : 64u, u2 + 64 + 2 * row, to + 16 * row + 2 * w, u2 + win,
                  sb + (unsigned)((w * 2 + (c >> 3)) * CKN + 2080 - 16 * (c & 7) + 8 * g) * 2 - 3904, ush, (unsigned)l * 16u};
  const unsigned b1 = u1 + win - 3072;
  const v8u pb = {b1, b1 + 4, b1 + 8, b1 + 12, b1 + 16, u1 + (32 + 512 + t0) * 2, st + (w * STN + t0) * 2, st + (64 * (w & 1) + l) * 2};
  const v4u pc = {(unsigned)((l >> 5) * 32768 + (l & 31) * 16), (unsigned)((l >> 1) * (DM * 2) + (l & 1) * 8), (unsigned)((l >> 4) * 128 + ((l >> 1) & 7) * 4),
                  sb + (16 * CKN + (8 + 4 * (l & 1)) * CUN + 32 + (l >> 1)) * 2};
  const char* dbg = ws;
  asm volatile("s_mov_b64 exec, -1\ns_nop 4\n" CONV_ASM
               ::"{v[0:7]}"(pa), "{v[8:11]}"(known), "{v[12:15]}"(th), "{v[16:19]}"(tw), "{v[24:27]}"(kz), "{v[28:35]}"(pb), "{v[36:39]}"(pc),
               "{v40}"(to + (w ? l * 8 : (l & 31) * 16)), "{v[44:47]}"(tx[1]), "{v[48:51]}"(tx[2]), "{v[52:55]}"(tx[3]),
               "{s[40:43]}"(mk_srd(ws + OFF_X2, SZ_X)), "{s[44:47]}"(mk_srd(FL + FL_FR, 64 * 16 * 128)), "{s[48:51]}"(mk_srd(ws + OFF_X3, SZ_X)),
               "{s[52:55]}"(mk_srd(ws + OFF_XA, SZ_X)), "{s[58:59]}"(FL + FL_FC + (64 * 32 + (cv >> 3)) * 32 + (cv & 7)),
               "{s[60:61]}"(FL + FL_FC + (64 * (w + 8) + (cv >> 3)) * 32 + (cv & 7)), "{s62}"(0), "{s63}"(w),
               "{s64}"(__builtin_amdgcn_readfirstlane(__float_as_uint(dsk))), "{s65}"(cv * 16), "{s66}"(REP * 512), "{s67}"(cv * 512),
               "{s68}"((w + 8) * 65536 + cv * 512), "{s69}"(now10()), "{s[72:73]}"(0x0000ffff0000ffffull), "{s[36:37]}"(dbg),
               [th] "n"(CVTHR), [gv] "n"(GATE_V), [sv] "n"(SLP_V), [fu] "n"(CVFULL)
               : "memory", "vcc", "scc", "s81", "v255");
#undef KFRAG
}

__device__ __forceinline__ v4u ln_row_packed(const v4u xr) {
  float v[8];
#pragma unroll
  for (int e = 0; e < 4; ++e) { v[2 * e] = lo16(xr[e]); v[2 * e + 1] = hi16(xr[e]); }
  float s = ((v[0] + v[1]) + (v[2] + v[3])) + ((v[4] + v[5]) + (v[6] + v[7]));
  s = wave_sum(s);
  const float mean = s * (1.0f / DM);
  float q = 0.f;
#pragma unroll
  for (int j = 0; j < 8; ++j) { float t = v[j] - mean; q += t * t; }
  q = wave_sum(q);
  const float var = q * (1.0f / DM);
  const float mb = bfr(mean);
  const float sd = bfr(sqrtf(bfr(bfr(var) + 1e-5f)));
  const float ri = 1.0f / sd;
  v4u o;
#pragma unroll
  for (int e = 0; e < 4; ++e) {
    const unsigned t = pk2(v[2 * e] - mb, v[2 * e + 1] - mb);
    const float x0 = lo16(t), x1 = hi16(t);
    float q0 = x0 * ri, q1 = x1 * ri;
    q0 = __builtin_fmaf(__builtin_fmaf(-q0, sd, x0), ri, q0);
    q1 = __builtin_fmaf(__builtin_fmaf(-q1, sd, x1), ri, q1);
    o[e] = pk2(q0, q1);
  }
  return o;
}

__device__ __forceinline__ float bfr1(float x) { return lo16(pk2(x, x)); }
__device__ __forceinline__ void ln4_packed(v4u (&x)[4]) {
  f32x2_t v[16];
#pragma unroll
  for (int e = 0; e < 16; ++e) v[e] = f32x2_t{lo16(x[e >> 2][e & 3]), hi16(x[e >> 2][e & 3])};
  const f32x2_t t = (((v[0] + v[1]) + (v[2] + v[3])) + ((v[4] + v[5]) + (v[6] + v[7]))) +
                    (((v[8] + v[9]) + (v[10] + v[11])) + ((v[12] + v[13]) + (v[14] + v[15])));
  const float mean = red_mod4(t.x + t.y) * (1.0f / DM);
  const float mb = bfr1(mean);
  f32x2_t q0 = {0.f, 0.f}, q1 = {0.f, 0.f};
#pragma unroll
  for (int e = 0; e < 16; e += 2) {
    v[e] -= mb;
    v[e + 1] -= mb;
    q0 += v[e] * v[e];
    q1 += v[e + 1] * v[e + 1];
  }
  q0 += q1;
  const float dm = mean - mb;
  const float var = fmaxf(red_mod4(q0.x + q0.y) * (1.0f / DM) - dm * dm, 0.f);
  const float sd = bfr1(sqrtf(bfr1(bfr1(var) + 1e-5f)));
  const float ri = 1.0f / sd;
#pragma unroll
  for (int e = 0; e < 16; ++e) {
    const unsigned t2 = pk2(v[e].x, v[e].y);
    const f32x2_t q = f32x2_t{lo16(t2), hi16(t2)} * ri;
    x[e >> 2][e & 3] = pk2(q.x, q.y);
  }
}

extern "C" __device__ float mul1(float, float) __asm("llvm.amdgcn.fmul.legacy");
template <bool G0>
__device__ __forceinline__ void role_qkv_t(const MegaArgs& A, char* smem, const int L, const int h) {
  __builtin_amdgcn_s_waitcnt(0x0F70);
  const int w = __builtin_amdgcn_readfirstlane(threadIdx.x >> 6);
  char* ws = A.ws;
  unsigned* FL = (unsigned*)(ws + OFF_FL);
  unsigned* TMO = FL + FL_TMO;
#define LANE_IDS()                         \
  const int l = lane_id();                 \
  const int c = l & 15, g = l >> 4;        \
  (void)c; (void)g;
  unsigned* FC = FL + FL_FC + (L * 64 * 4 + REP) * 8 * 32;
  bf16x8 wa[16], wb[16], wv[16];
  {
    LANE_IDS();
    const unsigned sb = lds_base(smem);
    const int q = (l >> 2) & 3, p = l & 3, f = q | ((g & 1) << 2);
    const unsigned ra = sb + (8 * g + q) * 256 + (w ^ f) * 32 + p * 8;
    const unsigned rb = sb + (8 * g + q) * 256 + ((2 * (w & 3) + (p >> 1)) ^ f) * 32 + (p & 1) * 16;
#define W_READ(W, hf, A0)                                                      \
  {                                                                           \
    _Pragma("unroll") for (int t = 0; t < 8; ++t) {                           \
      const unsigned a_ = (A0) + (hf) * 8192 + t * 16384;                     \
      const v2u r0 = trd(a_), r1 = trd(a_ + 1024);                            \
      W[8 * (hf) + t] = BF8((v4u{r0.x, r0.y, r1.x, r1.y}));                   \
    }                                                                         \
    _Pragma("unroll") for (int t = 0; t < 8; ++t) asm volatile("" : "+v"(W[8 * (hf) + t])); \
  }
#define W_STAGE(M, hf, VM, NEXT)                                               \
  {                                                                           \
    WAIT_VM(VM);                                                              \
    RAW_BAR();                                                                \
    if ((M) == 2) W_READ(wv, hf, ra);                                         \
    if ((M) == (G0 ? 1 : 0)) { W_READ(wa, hf, rb); W_READ(wb, hf, rb + 8); }  \
    RAW_BAR();                                                                \
    NEXT;                                                                     \
  }
    wqkv_issue<false>(A.Wq, 0, h, w, l, sb);
    wqkv_issue<false>(A.Wq, 1, h, w, l, sb);
    W_STAGE(0, 0, 8, wqkv_issue<false>(A.Wk, 0, h, w, l, sb));
    SST(4);
    W_STAGE(0, 1, 8, wqkv_issue<false>(A.Wk, 1, h, w, l, sb));
    SST(5);
    W_STAGE(1, 0, 8, wqkv_issue<false>(A.Wv, 0, h, w, l, sb));
    W_STAGE(1, 1, 8, wqkv_issue<false>(A.Wv, 1, h, w, l, sb));
    SST(6);
    W_STAGE(2, 0, 8, (void)0);
    W_STAGE(2, 1, 0, (void)0);
#undef W_STAGE
#undef W_READ
  }
  WSTAMP2();
  constexpr int Q_X = 98304;
  const unsigned sb = lds_base(smem);
  v2u ea, eb, ev;
  {
    LANE_IDS();
    const v4u zt = LD128(MKRS(ws + OFF_TAG, 8192 + 8), l * 16, (blockIdx.x & 7) * 1024);
    const v2u ze = LD64(MKRS(ws + OFF_TAG, 8192 + 8), 0, 8192);
    const v4u one = {0x3f803f80u, 0x3f803f80u, 0x3f803f80u, 0x3f803f80u};
    f32x4 t[3] = {};
#pragma unroll
    for (int ks = 0; ks < 16; ++ks) {
      t[0] = MFMA16(BF8(one), wa[ks], t[0]);
      t[1] = MFMA16(BF8(one), wb[ks], t[1]);
      t[2] = MFMA16(BF8(one), wv[ks], t[2]);
    }
    v2u e[3];
#pragma unroll
    for (int i = 0; i < 3; ++i) {
      const float x = g ? 0.f : t[i][0], x1 = x - bfr1(x);
      e[i] = v2u{pk2(x, x1), pk2(x1 - bfr1(x1), 0.f)};
    }
    ea = e[0]; eb = e[1]; ev = e[2];
    asm volatile("" : "+v"(ea), "+v"(eb), "+v"(ev));
    if (!zero_seen(zt, ze)) zero_wait(ws);
    if (l < 32) *reinterpret_cast<v4u*>(smem + Q_X + w * 2048 + (l >> 4) * 16384 + c * 16) = one;
  }
  constexpr int Q_ST = Q_X + 8 * 2048 + 256, Q_CNT = Q_X + 11 * 2048 + 256;
  if (threadIdx.x == 0) *(lu32*)(unsigned long)(sb + Q_CNT) = 0u;
  v8u pv, wr[24];
  v4u pw;
  unsigned sw;
  {
    LANE_IDS();
    const unsigned fa = sb + g * 512 + c * 16;
    pv = v8u{ea.x, ea.y, eb.x, eb.y, ev.x, ev.y, (unsigned)l * 16u, fa};
    pw = v4u{sb + Q_ST + c * 4, sb + Q_ST + g * 16,
             G0 ? (l & 7) * 4 + (l & 8) * 16 : (c & 8) ? sb + Q_X + ((w + 1) & 1) * 128 : fa + (w & 3) * 128, (c & 8) ? 0u : 0x8000u};
    sw = sb + Q_ST + (w & 3) * 32 + (l & 7) * 4;
#pragma unroll
    for (int i = 0; i < 8; ++i) {
      wr[i] = __builtin_shufflevector(__builtin_bit_cast(v4u, wa[2 * i]), __builtin_bit_cast(v4u, wa[2 * i + 1]), 0, 1, 2, 3, 4, 5, 6, 7);
      wr[8 + i] = __builtin_shufflevector(__builtin_bit_cast(v4u, wb[2 * i]), __builtin_bit_cast(v4u, wb[2 * i + 1]), 0, 1, 2, 3, 4, 5, 6, 7);
      wr[16 + i] = __builtin_shufflevector(__builtin_bit_cast(v4u, wv[2 * i]), __builtin_bit_cast(v4u, wv[2 * i + 1]), 0, 1, 2, 3, 4, 5, 6, 7);
    }
  }
  const char* Xb = ws + (L ? OFF_X3 : OFF_XA) + w * 8192;
  const v4u rsQ = mk_srd(ws + OFF_QK + L * SZ_QK, SZ_QK), rsV = mk_srd(ws + OFF_VF + L * SZ_O, SZ_O);
  const v4u rsM = mk_srd(ws + (G0 ? OFF_MEMK : OFF_MEMV) + L * SZ_MEM, SZ_MEM);
  const v4u rsF = mk_srd(FC + w * 64, (64 * 32 - REP * 8 - 2 * w) * 128);
  const unsigned* FQ = FL + FL_FQ + (L * 65 * 32 + h) * 32;
  const char* dbg = ws;
  __builtin_amdgcn_s_waitcnt(0x0F70);
  RAW_BAR();
#define QKV_OPS                                                                                                              \
  ::"{v[64:71]}"(wr[0]), "{v[72:79]}"(wr[1]), "{v[80:87]}"(wr[2]), "{v[88:95]}"(wr[3]), "{v[96:103]}"(wr[4]),                \
      "{v[104:111]}"(wr[5]), "{v[112:119]}"(wr[6]), "{v[120:127]}"(wr[7]), "{v[128:135]}"(wr[8]), "{v[136:143]}"(wr[9]),      \
      "{v[144:151]}"(wr[10]), "{v[152:159]}"(wr[11]), "{v[160:167]}"(wr[12]), "{v[168:175]}"(wr[13]), "{v[176:183]}"(wr[14]), \
      "{v[184:191]}"(wr[15]), "{v[192:199]}"(wr[16]), "{v[200:207]}"(wr[17]), "{v[208:215]}"(wr[18]), "{v[216:223]}"(wr[19]), \
      "{v[224:231]}"(wr[20]), "{v[232:239]}"(wr[21]), "{v[240:247]}"(wr[22]), "{v[248:255]}"(wr[23]), "{v[0:7]}"(pv),        \
      "{v[8:11]}"(pw), "{v62}"(sw), "{s[40:43]}"(rsQ), "{s[44:47]}"(rsV), "{s[48:51]}"(rsM), "{s[52:55]}"(rsF),              \
      "{s[56:57]}"(Xb), "{s58}"(sb + w * 8192), "{s59}"((h * 16 + (G0 ? 8 : 0) + (w & 3)) * 1024), "{s60}"((h * 8 + w) * 1024), \
      "{s61}"(h * 2048 + (w & 3) * 512), "{s62}"(0), "{s63}"(L), "{s[64:65]}"(FQ), "{s66}"(NSEG - 1), "{s67}"(0),            \
      "{s[70:71]}"(0xaaaaaaaaaaaaaaaaull), "{s[72:73]}"(0xccccccccccccccccull), "{s78}"(w), "{s80}"(sb + Q_CNT),             \
      "{s[36:37]}"(dbg), [gq] "n"(GATE_Q), [sq] "n"(SLP_Q), [s1] "n"(SLP_Q1 - SLP_Q)                                         \
      : "memory", "vcc", "scc", "s81", "v63"
  if (G0) asm volatile("s_mov_b64 exec, -1\ns_nop 4\n" QKV_ASM_0 QKV_OPS);
  else asm volatile("s_mov_b64 exec, -1\ns_nop 4\n" QKV_ASM_1 QKV_OPS);
#undef QKV_OPS
#undef LANE_IDS
}
__device__ __forceinline__ void role_qkv(const MegaArgs& A, char* smem, const int L, const int h) {
  if (__builtin_amdgcn_readfirstlane(threadIdx.x >> 6) < 4) role_qkv_t<true>(A, smem, L, h);
  else role_qkv_t<false>(A, smem, L, h);
}

constexpr int MEGA_WGS = 256;
constexpr size_t MEGA_LDS = (size_t)(32 * 4 * 64 * 8 + 8 * SMS + 15 * QS + 128 + 16 * 64 * 8 + 8 * SOS + 512) * 2;
static_assert(MEGA_LDS <= 163840 && MEGA_LDS >= 162816 + 256, "lds");
static_assert(162816 >= AW_END, "lds");
static_assert(MEGA_LDS >= (size_t)(8 * 2 * CKN + 8 * 2 * CUN + 2 * 32 * 8 + 8 * STN) * 2, "lds");
static_assert(MEGA_LDS >= (size_t)2 * 32 * SMS * 2 && MEGA_LDS >= 131072, "lds");
static_assert(131072 >= (size_t)(40 * NST + 32 * 400) * 4, "lds");
__global__ __launch_bounds__(512, 2) void mega_kernel(const u16* mem0, const u16* Dskip, char* ws, u16* out, const u16* x0,
                                                      const u16* logAre, const u16* Aim, const u16* Cre, const u16* Cim,
                                                      const u16* logdt, const u16* Wq, const u16* Wk, const u16* Wv,
                                                      const u16* Wo) {
  extern __shared__ __attribute__((aligned(16))) char smem[];
  const int b = __builtin_amdgcn_readfirstlane(role_no());
  const MegaArgs A{mem0, Dskip, ws, out, x0, logAre, Aim, Cre, Cim, logdt, Wq, Wk, Wv, Wo, b};
  {
    const int hi = (b >= 32 && b < 96) ? b - 32 : (b >= 200 && b < 248) ? b - 136 : (b >= 96 && b < 104) ? b + 16 : (b >= 248 ? b - 128 : -1);
    if (hi >= 0) start_kgen(A, smem, hi, b - 248, (b >= 64 && b < 104) || (b >= 232 && b < 240) ? b & 15 : -1);
  }
  if ((EXP_OFF & 1) && b >= 64 && b < 80) return;
  if ((EXP_OFF & 8) && b >= 200 && b < 232) return;
  if ((EXP_OFF & 16) && b >= 32 && b < 64) return;
  if ((EXP_OFF & 32) && b >= 80 && b < 96) return;
  if (b < 64) role_chain(A, smem, b >> 5, b & 31, 0);
  else if (b < 104 || (b >= 232 && b < 240)) role_aw(A, smem, b < 80 ? 0 : b < 96 ? 2 : 1, b & 15);
  else if (b < 168) role_conv(A, smem, b - 104);
  else if (b < 232) role_qkv(A, smem, (b - 168) >> 5, (b - 168) & 31);
  else role_red(A, smem, b - 240);
}

extern "C" void kernel_launch(void* const* d_in, const int* in_sizes, int n_in, void* d_out, int out_size, void* d_ws,
                              size_t ws_size, hipStream_t stream) {
  MegaArgs A;
  A.x0 = (const u16*)d_in[0];
  A.logAre = (const u16*)d_in[1];
  A.Aim = (const u16*)d_in[2];
  A.Cre = (const u16*)d_in[3];
  A.Cim = (const u16*)d_in[4];
  A.logdt = (const u16*)d_in[5];
  A.Dskip = (const u16*)d_in[6];
  A.mem0 = (const u16*)d_in[8];
  A.Wq = (const u16*)d_in[9];
  A.Wk = (const u16*)d_in[10];
  A.Wv = (const u16*)d_in[11];
  A.Wo = (const u16*)d_in[12];
  A.ws = (char*)d_ws;
  A.out = (u16*)d_out;

  (void)hipFuncSetAttribute((const void*)mega_kernel, hipFuncAttributeMaxDynamicSharedMemorySize, (int)MEGA_LDS);
  mega_kernel<<<MEGA_WGS, 512, MEGA_LDS, stream>>>(A.mem0, A.Dskip, A.ws, A.out, A.x0, A.logAre, A.Aim, A.Cre, A.Cim, A.logdt,
                                                   A.Wq, A.Wk, A.Wv, A.Wo);
}
```

```cpp
#include <hip/hip_runtime.h>
#include <hip/hip_bf16.h>
#include <stdint.h>

constexpr int L_SEQ = 2048;
constexpr int DM    = 512;
constexpr int NST   = 128;
constexpr int NH    = 32;
constexpr int DH    = 128;
constexpr int HD    = NH * DH;
constexpr int MM    = 8;
constexpr int SEG   = 32;
constexpr int NSEG  = L_SEQ / SEG;
constexpr int LK    = MM + SEG;

typedef unsigned short u16;
using bf16x8 = __attribute__((ext_vector_type(8))) short;
using f32x4  = __attribute__((ext_vector_type(4))) float;
using f32x16 = __attribute__((ext_vector_type(16))) float;

static __device__ __forceinline__ u16 f2bf(float f) {
  unsigned u = __float_as_uint(f);
  u += 0x7fffu + ((u >> 16) & 1u);
  return (u16)(u >> 16);
}
static __device__ __forceinline__ float bf2f(u16 h) { return __uint_as_float(((unsigned)h) << 16); }
static __device__ __forceinline__ float bfr(float f) { return bf2f(f2bf(f)); }

constexpr size_t SZ_X      = (size_t)L_SEQ * DM * 2;
constexpr size_t OFF_KF    = 0;
constexpr size_t OFF_UT    = OFF_KF + (size_t)DM * L_SEQ * 4;
constexpr size_t OFF_YT    = OFF_UT + SZ_X;
constexpr size_t OFF_XA    = OFF_YT + SZ_X;
constexpr size_t OFF_H     = OFF_XA + SZ_X;
constexpr size_t OFF_X2    = OFF_H + SZ_X;
constexpr size_t OFF_U2    = OFF_X2 + SZ_X;
constexpr size_t OFF_X3    = OFF_U2 + SZ_X;
constexpr size_t SZ_QK     = (size_t)L_SEQ * 2 * HD * 2;
constexpr size_t OFF_QK    = OFF_X3 + SZ_X;
constexpr size_t SZ_O      = (size_t)L_SEQ * HD * 2;
constexpr size_t OFF_VF    = OFF_QK + 2 * SZ_QK;
constexpr size_t SZ_PW     = (size_t)2 * NSEG * 16 * SEG * DM * 2;
constexpr size_t OFF_PW    = OFF_VF + 2 * SZ_O;
constexpr size_t SZ_MEM    = (size_t)NSEG * MM * HD * 2;
constexpr size_t OFF_MEMK  = OFF_PW + SZ_PW;
constexpr size_t OFF_MEMV  = OFF_MEMK + 2 * SZ_MEM;
constexpr size_t OFF_GT    = OFF_MEMV + 2 * SZ_MEM;
constexpr size_t SZ_ACC    = (size_t)2 * 32768;
constexpr size_t OFF_Z     = OFF_GT + (size_t)64 * DM * 4;
constexpr size_t OFF_ACC   = OFF_Z;
constexpr unsigned ACC_LO = 0x88000000u, ACC_HI = 0x87fffff7u;
constexpr size_t OFF_FL    = OFF_ACC + 2 * SZ_ACC;
constexpr int FL_FW  = 0;
constexpr int FL_FR  = FL_FW + 2 * 64 * 16 * 32;
constexpr int FL_TW  = FL_FR + 64 * 16 * 32;
constexpr int FL_FC  = FL_TW + 4 * 128;
constexpr int FL_FQ  = FL_FC + 2 * 64 * 32 * 32;
constexpr int FL_FS  = FL_FQ + 2 * 65 * 32 * 32;
constexpr int FL_FZ  = FL_FS + 4 * 32 * 32;
constexpr int FL_KG  = FL_FZ + 8 * 32;
constexpr int FL_G   = FL_KG + 64 * 32;
constexpr int FL_TK  = FL_G + 4 * 4 * 32;
constexpr int FL_TMO = FL_TK + 2 * 256;
constexpr int FL_END = FL_TMO + 64;
constexpr size_t Z_BYTES   = OFF_FL + (size_t)FL_END * 4 - OFF_Z;
constexpr size_t OFF_TAG   = OFF_Z + Z_BYTES;
constexpr unsigned long long TAG_STEP = 0x9e3779b97f4a7c15ull;
constexpr size_t OFF_G0    = OFF_TAG + 8 * 1024 + 256;
constexpr size_t WS_TOTAL  = OFF_G0 + 8192;
static_assert(Z_BYTES % 256 == 0 && OFF_Z % 256 == 0 && Z_BYTES <= 2 * 128 * 8192, "zero region");
#define REP ((blockIdx.x >> 1) & 3)
static_assert(WS_TOTAL + (4 << 20) <= (size_t)256 * 1024 * 1024, "workspace too large");

#define CKNP 0
constexpr int CKN = 2208 + CKNP;
static_assert((size_t)DM * 2 * CKN * 2 <= (size_t)DM * L_SEQ * 4 + SZ_X, "K buffer");
__device__ __forceinline__ float pos_of_index(int p) {
  if (p < 256) return (float)p;
  if (p < 384) return (float)(256 + 2 * (p - 256));
  if (p < 512) return (float)(512 + 4 * (p - 384));
  if (p < 640) return (float)(1024 + 8 * (p - 512));
  return 2048.0f;
}
__device__ __forceinline__ int index_of_l(int l) {
  float pf = bfr((float)l);
  int p = (int)pf;
  if (p < 256) return p;
  if (p < 512) return 256 + ((p - 256) >> 1);
  if (p < 1024) return 384 + ((p - 512) >> 2);
  if (p < 2048) return 512 + ((p - 1024) >> 3);
  return 640;
}

template <int CTRL> __device__ __forceinline__ float dppr(float x) {
  return __builtin_bit_cast(float, __builtin_amdgcn_update_dpp(0, __builtin_bit_cast(int, x), CTRL, 0xf, 0xf, false));
}
__device__ __forceinline__ float wave_sum(float v) {
  v += dppr<0x121>(v);
  v += dppr<0x122>(v);
  v += dppr<0x124>(v);
  v += dppr<0x128>(v);
  {
    auto r = __builtin_amdgcn_permlane16_swap(__float_as_uint(v), __float_as_uint(v), false, false);
    v = __uint_as_float(r[0]) + __uint_as_float(r[1]);
  }
  {
    auto r = __builtin_amdgcn_permlane32_swap(__float_as_uint(v), __float_as_uint(v), false, false);
    v = __uint_as_float(r[0]) + __uint_as_float(r[1]);
  }
  return v;
}
__device__ __forceinline__ void ln_row8(float (&v)[8]) {
  float s = 0.f;
#pragma unroll
  for (int j = 0; j < 8; ++j) s += v[j];
  s = wave_sum(s);
  const float mean = s * (1.0f / DM);
  float q = 0.f;
#pragma unroll
  for (int j = 0; j < 8; ++j) { float t = v[j] - mean; q += t * t; }
  q = wave_sum(q);
  const float var = q * (1.0f / DM);
  const float mb = bfr(mean);
  const float sd = bfr(sqrtf(bfr(bfr(var) + 1e-5f)));
#pragma unroll
  for (int j = 0; j < 8; ++j) v[j] = bfr(bfr(v[j] - mb) / sd);
}

#define RLX_AGENT __ATOMIC_RELAXED, __HIP_MEMORY_SCOPE_AGENT
typedef __attribute__((address_space(1))) unsigned gu32;
typedef __attribute__((address_space(3))) unsigned lu32;
typedef unsigned v4u __attribute__((ext_vector_type(4)));
constexpr int AUX_SC1 = 16;
constexpr unsigned SPIN_CAP = 1u << 18;

__device__ __forceinline__ bool wait_flags(const unsigned* flags, int lane, int nfl, unsigned want, unsigned* tmo) {
  for (unsigned spins = 0;; ++spins) {
    unsigned v = want;
    if (lane < nfl) v = __hip_atomic_load((gu32*)flags + lane * 32, RLX_AGENT);
    if (__all(v >= want)) break;
    if (spins > SPIN_CAP) {
      if (lane == 0) __hip_atomic_store((gu32*)tmo, 1u, RLX_AGENT);
      return false;
    }
  }
  __builtin_amdgcn_fence(__ATOMIC_ACQUIRE, "wavefront");
  return true;
}

#define STAMP(i)
#define STAMP_DUMP()
#define WSTAMP(i)
template <int CTRL> __device__ __forceinline__ float dppf(float old, float x) {
  return __builtin_bit_cast(float, __builtin_amdgcn_update_dpp(__builtin_bit_cast(int, old), __builtin_bit_cast(int, x), CTRL, 0xf, 0xf, false));
}
__device__ __forceinline__ float wave_max_dpp(float x) {
  const float ninf = -3e38f;
  x = fmaxf(x, dppf<0x111>(ninf, x));
  x = fmaxf(x, dppf<0x112>(ninf, x));
  x = fmaxf(x, dppf<0x114>(ninf, x));
  x = fmaxf(x, dppf<0x118>(ninf, x));
  x = fmaxf(x, dppf<0x142>(ninf, x));
  x = fmaxf(x, dppf<0x143>(ninf, x));
  return __builtin_bit_cast(float, __builtin_amdgcn_readlane(__builtin_bit_cast(int, x), 63));
}
__device__ __forceinline__ float wave_sum_dpp(float x) {
  x += dppf<0x111>(0.f, x);
  x += dppf<0x112>(0.f, x);
  x += dppf<0x114>(0.f, x);
  x += dppf<0x118>(0.f, x);
  x += dppf<0x142>(0.f, x);
  x += dppf<0x143>(0.f, x);
  return __builtin_bit_cast(float, __builtin_amdgcn_readlane(__builtin_bit_cast(int, x), 63));
}
constexpr int SMS = 528;
constexpr int QS = 144;
constexpr int VS = 80;
constexpr int SOS = 144;
#define VAR_NREP 4
constexpr int NREP = VAR_NREP;
__device__ __forceinline__ float xsum16(float x) {
  auto r = __builtin_amdgcn_permlane16_swap(__float_as_uint(x), __float_as_uint(x), false, false);
  return __uint_as_float(r[0]) + __uint_as_float(r[1]);
}
__device__ __forceinline__ float xsum32(float x) {
  auto r = __builtin_amdgcn_permlane32_swap(__float_as_uint(x), __float_as_uint(x), false, false);
  return __uint_as_float(r[0]) + __uint_as_float(r[1]);
}
__device__ __forceinline__ float xmax16(float x) {
  auto r = __builtin_amdgcn_permlane16_swap(__float_as_uint(x), __float_as_uint(x), false, false);
  return fmaxf(__uint_as_float(r[0]), __uint_as_float(r[1]));
}
__device__ __forceinline__ float xmax32(float x) {
  auto r = __builtin_amdgcn_permlane32_swap(__float_as_uint(x), __float_as_uint(x), false, false);
  return fmaxf(__uint_as_float(r[0]), __uint_as_float(r[1]));
}
__device__ __forceinline__ float red_mod4(float x) {
  x += dppf<0x124>(0.f, x);
  x += dppf<0x128>(0.f, x);
  return xsum32(xsum16(x));
}
typedef __bf16 bf16x2_t __attribute__((ext_vector_type(2)));
typedef float f32x2_t __attribute__((ext_vector_type(2)));
__device__ __forceinline__ unsigned pk2(float a, float b) {
  f32x2_t v = {a, b};
  bf16x2_t r = __builtin_convertvector(v, bf16x2_t);
  return __builtin_bit_cast(unsigned, r);
}
__device__ __forceinline__ float dot2sq(const unsigned x, const float acc) {
  return __builtin_amdgcn_fdot2_f32_bf16(__builtin_bit_cast(bf16x2_t, x), __builtin_bit_cast(bf16x2_t, x), acc, false);
}
__device__ __forceinline__ float lo16(unsigned u) { return __uint_as_float(u << 16); }
__device__ __forceinline__ float hi16(unsigned u) { return __uint_as_float(u & 0xffff0000u); }
__device__ __forceinline__ void scale_pair(float a, float b, float scale, float& oa, float& ob) {
  unsigned u = pk2(a, b);
  unsigned v = pk2(lo16(u) * scale, hi16(u) * scale);
  oa = lo16(v);
  ob = hi16(v);
}

#define XSPLIT 3
__device__ __forceinline__ int role_no() {
  const int b = blockIdx.x, x = b & 7, j = b >> 3;
  if (XSPLIT >= 3) {
    const int y = XSPLIT == 5 ? x & 3 : x >> 1;
    if (XSPLIT >= 6 && (j >> 2) == 6) return (x & 1) ? 104 + 16 * y + j - 16 : 80 + 4 * y + (j & 3);
    if (XSPLIT >= 7 && (j >> 2) == 4) return (x & 1) ? 104 + 16 * y + j - 16 : 64 + 4 * y + (j & 3);
    if (XSPLIT == 5 ? x < 4 : !(x & 1)) {
      if (j >= 16) return 104 + 16 * y + j - 16;
      return XSPLIT == 4 ? 16 * y + j : (j & 8) * 4 + 8 * y + (j & 7);
    }
    if (j < 16) return 168 + (j & 8) * 4 + 8 * y + (j & 7);
    const int i = 4 * y + (j & 3);
    return j < 20 ? 64 + i : j < 24 ? (i < 8 ? 96 + i : 224 + i) : j < 28 ? 80 + i : 240 + i;
  }
  const int L = x >> 2, xx = x & 3;
  if (j < 8) return 32 * L + 8 * xx + j;
  if (j < 12) return 64 + 16 * L + 4 * xx + j - 8;
  if (j < 16) { const int i = 4 * xx + j - 12; return L ? 240 + i : i < 8 ? 96 + i : 224 + i; }
  if (j < 24) return 104 + 8 * x + j - 16;
  return 168 + 32 * L + 8 * xx + j - 24;
}
#define MSTAMP()
#define WSTAMP2()
#define SST(k)
#define TSR(rn, k, ss, t)
#define TS(k, ss, t)
#define RST(k)
#define RSD()
#define LST(k)
#define LSD()
#define FSD
#define FS(k)
#define FD(ss)
struct MegaArgs {
  const u16* mem0;
  const u16* Dskip;
  char* ws;
  u16* out;
  const u16* x0;
  const u16 *logAre, *Aim, *Cre, *Cim, *logdt;
  const u16 *Wq, *Wk, *Wv, *Wo;
  int rn;
};

__device__ __forceinline__ int lane_id() {
  int l;
  asm volatile("v_mbcnt_lo_u32_b32 %0, -1, 0\n\tv_mbcnt_hi_u32_b32 %0, -1, %0" : "=v"(l));
  return l;
}
__device__ __forceinline__ bool spin_timeout(unsigned spins, unsigned* tmo) {
  if (spins > SPIN_CAP) {
    if (lane_id() == 0) __hip_atomic_store((gu32*)tmo, 1u, RLX_AGENT);
    return true;
  }
  return false;
}
#define PSWH (h & 2)
#define RR 0
#define RQM 63
#define RPM 63
#define CD0 100
#define APN 7
#define APL 300
#define APA 100
#define APB 95
#define APP 204
#define CD 30
#define CDS 30
#define CDL 0
#define XG(h) ((h) >> 3)
#define GP 230
#define GR 50
#define GM 0
#define ROT (4 * (h & 1))
#define CPRIO 1
#define P1PRIO 3
#define QLEAD 2
#define POLL_SLEEP 12
#define SLP_C POLL_SLEEP
#define SLP_A POLL_SLEEP
#define SLP_W POLL_SLEEP
#define SLP_R 4
#define SLP_V 4
#define SLP_Q 4
#define SLP_M 4
#define AWPRIO 2
#define SLP_S 8
#define SPOS 4
#define CVPRE 0
#define QHV 0
#define QCHK 0
#define LEADN 99
#define GATE_V 40
#define GATE_R 380
#define GATE_N 440
#define AWAHEAD 1
#define GATE_M 150
#define GATE_M2 380
#define QPACE 0
#define QPS 6
#define QPL 100
#define GATE_Q 150
#define SLP_Q1 24
#define QPF 3
#define QPV 6
#define QVP 3
#define QP0 2
#define QP1 0
#define QFP 15
#define QUP 7
#define QEP 3
#define AWD 9
#define GPER 200
#define GMA 60
#define GMR 60
#define GMS 0
#define GSTEP 30
#define GATE_RT 130
#define GATE_1 160
#define N1S 20
#define N1E 36
#define PRI_A 3
#define PRI_WA 2
#define PRI_WB 1
#define AWSKIP 3
#define PRI_0 3
#define AWDEF 1
#define SLP_F 2
#define EXP_OFF 0
#define AWSPEC 1
#define CVTHR 48
#define NR_C 8
#define NR_R 8
#define NR_W 8
#define NR_N 2
#define QAHEAD 0
#define NS0 4
#define CVST 16
#define CVNB 9
#define CVFULL 1
#define HK3 0
#define AWDT 120
#define AWMO 0
#define HK2 0
#define RDT 190
#define RTW 0
#define CVLT 120
#define SLP_P 2
#define SLP_N POLL_SLEEP
template <int SLP, int WPL>
__device__ __forceinline__ bool wait_n(const unsigned* f, int n, unsigned* tmo) {
  const int l = lane_id();
  for (unsigned spins = 0;; ++spins) {
    unsigned v = 1u;
    if (l < n) v = __hip_atomic_load((gu32*)f + (l / WPL) * 32 + l % WPL, RLX_AGENT);
    if (__all(v != 0u)) break;
    if (spin_timeout(spins, tmo)) return false;
    __builtin_amdgcn_s_sleep(SLP);
  }
  asm volatile("" ::: "memory");
  return true;
}
__device__ __forceinline__ unsigned now10() { return (unsigned)__builtin_amdgcn_s_memrealtime(); }
__device__ __forceinline__ void gate(const unsigned t) {
  for (int i = 0; i < 64; ++i) {
    const int d = (int)(t - now10());
    if (d <= 0) break;
    if (d > 100) __builtin_amdgcn_s_sleep(32);
    else if (d > 25) __builtin_amdgcn_s_sleep(8);
    else __builtin_amdgcn_s_sleep(2);
  }
}
template <int SLP, int WPL>
__device__ __forceinline__ unsigned grp_wait(const bool leader, volatile lu32* sw, const unsigned val, const unsigned* f, int n,
                                             unsigned* tmo, const unsigned t) {
  unsigned r;
  gate(t);
  if (leader) {
    const int l = lane_id();
    unsigned v;
    for (unsigned spins = 0;; ++spins) {
      v = __hip_atomic_load((gu32*)f + ((l & 15) / WPL) * 32 + l % WPL, RLX_AGENT);
      if (__all(v != 0u) || spin_timeout(spins, tmo)) break;
      __builtin_amdgcn_s_sleep(SLP);
    }
    int d = (int)(v - t);
    d = max(d, __builtin_amdgcn_update_dpp(0, d, 0x121, 0xf, 0xf, false));
    d = max(d, __builtin_amdgcn_update_dpp(0, d, 0x122, 0xf, 0xf, false));
    d = max(d, __builtin_amdgcn_update_dpp(0, d, 0x124, 0xf, 0xf, false));
    d = max(d, __builtin_amdgcn_update_dpp(0, d, 0x128, 0xf, 0xf, false));
    r = t + __builtin_amdgcn_readfirstlane(d);
    sw[1 + (val & 2)] = r;
    *sw = val;
  } else {
    for (unsigned spins = 0; __builtin_amdgcn_readfirstlane(*sw) < val && spins <= 64u * SPIN_CAP; ++spins) __builtin_amdgcn_s_sleep(SLP_F);
    r = __builtin_amdgcn_readfirstlane(sw[1 + (val & 2)]);
  }
  asm volatile("" ::: "memory");
  return r;
}
template <int SLP, int WPL>
__device__ __forceinline__ bool wg_wait(const unsigned* f, int n, unsigned* tmo) {
  if (threadIdx.x < 64) (void)wait_n<SLP, WPL>(f, n, tmo);
  __syncthreads();
  asm volatile("" ::: "memory");
  return true;
}
__device__ __forceinline__ void set_flag(unsigned* f) { __hip_atomic_store((gu32*)f, 1u, RLX_AGENT); }
#define DRAIN() asm volatile("s_waitcnt vmcnt(0)" ::: "memory")
#define MKRS(ptr, bytes) __builtin_amdgcn_make_buffer_rsrc((void*)(ptr), 0, (int)(bytes), 0x00020000)
typedef unsigned v2u __attribute__((ext_vector_type(2)));
#define LD128(rs, voff, soff) __builtin_amdgcn_raw_buffer_load_b128(rs, voff, soff, AUX_SC1)
#define LD64(rs, voff, soff) __builtin_amdgcn_raw_buffer_load_b64(rs, voff, soff, AUX_SC1)
#define ST128(v, rs, voff, soff)                                              \
  do {                                                                        \
    const v4u st_d_ = (v);                                                    \
    __builtin_amdgcn_raw_buffer_store_b128(st_d_, rs, voff, soff, AUX_SC1);   \
    asm volatile("s_nop 1" ::"v"(st_d_) : "memory");                          \
  } while (0)
#define ST64(v, rs, voff, soff) __builtin_amdgcn_raw_buffer_store_b64(v, rs, voff, soff, AUX_SC1)
#define BF8(x) __builtin_bit_cast(bf16x8, x)
#define MFMA16(a, b, c) __builtin_amdgcn_mfma_f32_16x16x32_bf16(a, b, c, 0, 0, 0)
typedef short bf16x4 __attribute__((ext_vector_type(4)));
#define MFMA16K(a, b, c) \
  __builtin_amdgcn_mfma_f32_16x16x16bf16_1k(__builtin_bit_cast(bf16x4, a), __builtin_bit_cast(bf16x4, b), c, 0, 0, 0)
__device__ __forceinline__ float fmax_nn(float a, float b) { return __builtin_elementwise_maximum(a, b); }
typedef __bf16 bfv8 __attribute__((ext_vector_type(8)));
typedef __bf16 bfv16 __attribute__((ext_vector_type(16)));
typedef unsigned v8u __attribute__((ext_vector_type(8)));
#define SMF(a, b, c, idx) \
  __builtin_amdgcn_smfmac_f32_16x16x64_bf16(__builtin_bit_cast(bfv8, a), __builtin_bit_cast(bfv16, b), c, idx, 0, 0)
__device__ __forceinline__ float swap_add32(float a, float b) {
  auto r = __builtin_amdgcn_permlane32_swap(__float_as_uint(a), __float_as_uint(b), false, false);
  return __uint_as_float(r[0]) + __uint_as_float(r[1]);
}
__device__ __forceinline__ float swap_add16(float a, float b) {
  auto r = __builtin_amdgcn_permlane16_swap(__float_as_uint(a), __float_as_uint(b), false, false);
  return __uint_as_float(r[0]) + __uint_as_float(r[1]);
}
__device__ __forceinline__ v4u smf_fix(const v4u d) {
  auto x = __builtin_amdgcn_permlane16_swap(d.x, d.z, false, false);
  auto y = __builtin_amdgcn_permlane16_swap(d.y, d.w, false, false);
  return v4u{x[0], x[1], y[0], y[1]};
}
__device__ __forceinline__ v8u smf_bfrag(const u16* p) {
  const v2u a = *reinterpret_cast<const v2u*>(p), b = *reinterpret_cast<const v2u*>(p + 8);
  const v2u c = *reinterpret_cast<const v2u*>(p + 32), d = *reinterpret_cast<const v2u*>(p + 40);
  return v8u{a.x, b.x, a.y, b.y, c.x, d.x, c.y, d.y};
}

#define VAR_PPA 0
#define VAR_PPB 0
#define VAR_THR 0
#define VAR_THR2 0
#define CV_LEAD 100
#define VAR_EARLY 1
#define VAR_DRAIN 1
#define VAR_D1 0
#define VAR_D2 0
#define VAR_DR2 1
#define VAR_S1 0
#define VAR_S2 0
typedef __attribute__((address_space(1))) unsigned long long gu64;
typedef __attribute__((address_space(1))) char gch;
__device__ __forceinline__ int cvt_rpi(float x) {
  int r;
  asm("v_cvt_rpi_i32_f32 %0, %1" : "=v"(r) : "v"(x));
  return r;
}
typedef short v4s __attribute__((ext_vector_type(4)));
typedef __attribute__((address_space(3))) v4s* lp4s;
__device__ __forceinline__ void glds16(const void* sbase, const unsigned voff, const unsigned lds) {
  unsigned keep;
  asm volatile("s_nop 4\n\ts_mov_b32 %0, m0\n\ts_mov_b32 m0, %3\n\ts_nop 0\n\tglobal_load_lds_dwordx4 %1, %2\n\ts_mov_b32 m0, %0"
               : "=&s"(keep)
               : "v"(voff), "s"(sbase), "s"(lds)
               : "memory");
}
__device__ __forceinline__ void glds16c(const void* sbase, const unsigned voff, const unsigned lds) {
  unsigned keep;
  asm volatile("s_nop 4\n\ts_mov_b32 %0, m0\n\ts_mov_b32 m0, %3\n\ts_nop 0\n\tglobal_load_lds_dwordx4 %1, %2 sc1\n\ts_mov_b32 m0, %0"
               : "=&s"(keep)
               : "v"(voff), "s"(sbase), "s"(lds)
               : "memory");
}
__device__ __forceinline__ v2u trd(const unsigned a) {
  return __builtin_bit_cast(v2u, __builtin_amdgcn_ds_read_tr16_b64_v4i16((lp4s)(unsigned long)a));
}
__device__ __forceinline__ unsigned lds_base(char* smem) { return (unsigned)(unsigned long)(__attribute__((address_space(3))) char*)smem; }
#define RAW_BAR()                                           \
  do {                                                      \
    asm volatile("s_waitcnt lgkmcnt(0)" ::: "memory");      \
    __builtin_amdgcn_s_barrier();                           \
    asm volatile("" ::: "memory");                          \
  } while (0)
#define WAIT_VM(n) asm volatile("s_waitcnt vmcnt(" #n ")" ::: "memory")
template <bool SP>
__device__ __forceinline__ void wqkv_issue(const u16* Wm, const int hf, const int h, const int w, const int l, const unsigned sb) {
  const char* base = (const char*)Wm + (size_t)(256 * hf + 32 * w) * (HD * 2) + h * (DH * 2);
#pragma unroll
  for (int i = 0; i < 8; ++i) {
    const int r = l >> 4;
    const int f = SP ? ((r & 1) | ((i & 1) << 1) | (((i >> 1) & 1) << 2)) : (r | (((i >> 1) & 1) << 2));
    glds16(base, (unsigned)((4 * i + r) * (HD * 2) + (((l & 15) ^ (f << 1)) << 4)), sb + w * 16384 + hf * 8192 + i * 1024);
  }
}

#define PFW 1
__device__ __forceinline__ void pull_weights(const MegaArgs& A, char* smem, const int slot, const int slots) {
  if (!PFW) return;
  const int x = blockIdx.x & 7, j = blockIdx.x >> 3, h = 8 * (x >> 1) + (j & 7), k = (j >> 3) - 1, l = lane_id();
  const bool odd = x & 1, wo = !odd && k == 1;
  const unsigned voff = wo ? l * 64 : (l >> 2) * (HD * 2) + (l & 3) * 64;
  const unsigned dst = lds_base(smem) + 162816;
  unsigned long long pk = (unsigned long long)A.Wk, pq = (unsigned long long)A.Wq, pv = (unsigned long long)A.Wv, po = (unsigned long long)A.Wo;
  asm volatile("" : "+s"(pk), "+s"(pq), "+s"(pv), "+s"(po));
  for (int i = odd ? slot * 3 + k : slot; (PFW & 1) && i < (odd ? 64 : 32); i += odd ? 3 * slots : slots) {
    const unsigned long long m = odd ? (i < 32 ? pk : pv) : (k == 0 ? pq : pk);
    const unsigned long long b = wo ? po + h * (DH * DM * 2) + i * 4096 : m + (i & 31) * (16 * HD * 2) + h * (DH * 2);
    unsigned keep;
    asm volatile("s_nop 4\n\ts_mov_b32 %0, m0\n\ts_mov_b32 m0, %3\n\ts_nop 0\n\tglobal_load_lds_dword %1, %2\n\ts_mov_b32 m0, %0"
                 : "=&s"(keep)
                 : "v"(voff), "s"(b), "s"(dst)
                 : "memory");
  }
  if (PFW & 2) {
    const unsigned long long b = po + (size_t)h * (DH * DM * 2);
    for (int i = slot * 3 + k; i < 32; i += 3 * slots) {
      unsigned keep;
      asm volatile("s_nop 4\n\ts_mov_b32 %0, m0\n\ts_mov_b32 m0, %3\n\ts_nop 0\n\tglobal_load_lds_dword %1, %2\n\ts_mov_b32 m0, %0"
                   : "=&s"(keep)
                   : "v"(l * 64), "s"(b + i * 4096), "s"(dst)
                   : "memory");
    }
  }
}
__device__ __forceinline__ void wo_issue(const u16* Wo, const int a, const int st, const unsigned sb) {
  const int w = __builtin_amdgcn_readfirstlane(threadIdx.x >> 6), l = lane_id(), c = l & 15, g = l >> 4;
  const char* base = (const char*)Wo + ((size_t)((2 * a + (st >> 1)) * DH + 64 * (w & 1) + 32 * (st & 1)) * DM + 128 * (w >> 1)) * 2;
#pragma unroll
  for (int i = 0; i < 8; ++i) {
    const int fd = g | ((i & 1) << 2);
    glds16(base, (unsigned)((4 * i + g) * (DM * 2) + ((c ^ (fd << 1)) << 4)), sb + (st & 1) * 65536 + w * 8192 + i * 1024);
  }
}
__device__ __forceinline__ bool zero_seen(const v4u t, const v2u e) {
  const unsigned long long m = (((unsigned long long)e.y << 32) | e.x) + TAG_STEP;
  const unsigned lo = (unsigned)m, hi = (unsigned)(m >> 32);
  return __all(((t.x ^ lo) | (t.y ^ hi) | (t.z ^ lo) | (t.w ^ hi)) == 0u);
}
__device__ __forceinline__ void zero_wait(char* ws) {
  const __amdgpu_buffer_rsrc_t rs = MKRS(ws + OFF_TAG, 8192 + 8);
  for (unsigned spins = 0; spins < 16u * SPIN_CAP; ++spins) {
    const v4u t = LD128(rs, lane_id() * 16, (blockIdx.x & 7) * 1024);
    const v2u e = LD64(rs, 0, 8192);
    if (zero_seen(t, e)) break;
    asm volatile("s_sleep 4" ::: "memory");
  }
  asm volatile("" ::: "memory");
}
__device__ __forceinline__ unsigned row_stats(const v4u raw) {
  float v[8];
#pragma unroll
  for (int e = 0; e < 4; ++e) { v[2 * e] = lo16(raw[e]); v[2 * e + 1] = hi16(raw[e]); }
  float s = 0.f;
#pragma unroll
  for (int e = 0; e < 8; ++e) s += v[e];
  const float mean = wave_sum(s) * (1.0f / DM);
  float q = 0.f;
#pragma unroll
  for (int e = 0; e < 8; ++e) { const float t = v[e] - mean; q += t * t; }
  const float var = wave_sum(q) * (1.0f / DM);
  const float sd = bfr(sqrtf(bfr(bfr(var) + 1e-5f)));
  return (unsigned)f2bf(mean) | ((unsigned)f2bf(sd) << 16);
}
__device__ __forceinline__ void start_kgen(const MegaArgs& A, char* smem, const int hi, const int xi, const int aw) {
  const int tid = threadIdx.x, j = tid >> 7, u = tid & 127, d = 4 * hi + j;
  const int w = __builtin_amdgcn_readfirstlane(tid >> 6), l = tid & 63;
  float* prm = reinterpret_cast<float*>(smem) + j * 10 * NST;
  float* part = reinterpret_cast<float*>(smem) + 40 * NST + j * 8 * 400;
  unsigned* kd = reinterpret_cast<unsigned*>(smem + 131072) + j * 392;
  v4u raw[8], ra, rb;
  const int r0 = 16 * xi + 2 * w;
  if (xi >= 0) {
    ra = *reinterpret_cast<const v4u*>(A.x0 + (size_t)r0 * DM + l * 8);
    rb = *reinterpret_cast<const v4u*>(A.x0 + (size_t)(r0 + 1) * DM + l * 8);
  }
  const u16 p0 = A.logdt[d], p1 = A.logAre[d * NST + u], p2 = A.Aim[d * NST + u], p3 = A.Cre[d * NST + u], p4 = A.Cim[d * NST + u];
  if (w >= 6) {
#pragma unroll
    for (int i = 0; i < 8; ++i) raw[i] = *reinterpret_cast<const v4u*>(A.x0 + (size_t)(16 * hi + 8 * (w - 6) + i) * DM + l * 8);
  }
  const v2u en = LD64(MKRS(A.ws + OFF_TAG, 8192 + 8), 0, 8192);
  {
    const __amdgpu_buffer_rsrc_t rz = MKRS(A.ws + OFF_Z, Z_BYTES);
    const unsigned i0 = hi < 16 ? ACC_LO : 0u, i1 = hi < 16 ? ACC_HI : 0u;
    ST128((v4u{i0, i1, i0, i1}), rz, tid * 16, hi * 8192);
    ST128((v4u{0u, 0u, 0u, 0u}), rz, tid * 16, (128 + hi) * 8192);
  }
  if (xi >= 0) {
    const unsigned sa = row_stats(ra), sb = row_stats(rb), st = (l & 1) ? sb : sa;
    const unsigned long long m = (((unsigned long long)en.y << 32) | en.x) + TAG_STEP;
    ST128((v4u{st, (unsigned)m, st, (unsigned)(m >> 32)}), MKRS(A.ws + OFF_G0, 8192), (l >> 1) * 2048 + (l & 1) * 16, r0 * 16);
    SST(11);
  }
  {
    const float dt = bfr(expf(bf2f(p0)));
    const float ar = -bfr(expf(bf2f(p1))) * dt, ai = bf2f(p2) * dt;
    prm[u] = ar;
    prm[NST + u] = ai;
    prm[2 * NST + u] = bf2f(p3) * dt;
    prm[3 * NST + u] = bf2f(p4) * dt;
#pragma unroll
    for (int cls = 1; cls < 4; ++cls) {
      const float st = (float)(1 << cls);
      const float e = expf(ar * st);
      float sn, cs;
      sincosf(ai * st, &sn, &cs);
      prm[(2 + 2 * cls) * NST + u] = e * cs;
      prm[(3 + 2 * cls) * NST + u] = e * sn;
    }
  }
  __builtin_amdgcn_s_waitcnt(0x0F70);
  __syncthreads();
  SST(1);
  if (w >= 6) {
    if (w == 7 && l < 8)
      __hip_atomic_store((gu64*)(A.ws + OFF_TAG) + l * 128 + hi, (((unsigned long long)en.y << 32) | en.x) + TAG_STEP, RLX_AGENT);
    __builtin_amdgcn_s_setprio(3);
    if (hi >= 32) pull_weights(A, smem, w - 6, 2);
    v4u wr[32];
    const u16* wop = A.Wo + (size_t)(hi * DH + 64 * (w - 6)) * DM + l * 8;
    if (hi < 32) {
#pragma unroll
      for (int i = 0; i < 32; ++i) wr[i] = *reinterpret_cast<const v4u*>(wop + i * DM);
    }
    unsigned stw = 0u;
#pragma unroll
    for (int i = 0; i < 8; ++i) {
      const unsigned t = row_stats(raw[i]);
      if (l == i) stw = t;
    }
    const int sx = 2 * hi + w - 6;
    if (l < 8) __hip_atomic_store((gu32*)(A.ws + OFF_GT) + 8 * sx + l, stw, RLX_AGENT);
    zero_wait(A.ws);
    DRAIN();
    if (l < 4) set_flag((unsigned*)(A.ws + OFF_FL) + FL_FS + (l * 32 + (sx >> 3)) * 32 + (sx & 7));
    if (hi < 32) {
      float c0 = 0.f, c1 = 0.f, c2 = 0.f, c3 = 0.f;
#pragma unroll
      for (int i = 0; i < 64; ++i) {
        const v4u x = wr[i & 31];
        if (i < 32) wr[i] = *reinterpret_cast<const v4u*>(wop + (32 + i) * DM);
        c0 = dot2sq(x.x, c0); c1 = dot2sq(x.y, c1); c2 = dot2sq(x.z, c2); c3 = dot2sq(x.w, c3);
      }
      const float m = wave_max_dpp(fmaxf(fmaxf(c0, c1), fmaxf(c2, c3)));
      if (l < 4) __hip_atomic_store((gu32*)(A.ws + OFF_FL) + FL_G + (l * 4 + w - 5) * 32 + hi, __float_as_uint(m) | 1u, RLX_AGENT);
    }
    __builtin_amdgcn_s_setprio(0);
  } else {
    const int jm = tid / 96, um = tid % 96;
    const int r = um % 12, ng = um / 12;
    const float* prm = reinterpret_cast<float*>(smem) + jm * 10 * NST;
    float* part = reinterpret_cast<float*>(smem) + 40 * NST + jm * 8 * 400;
    const float* pw = prm + (4 + 2 * (r >> 2)) * NST;
    const float p0 = pos_of_index(256 + 32 * r);
    f32x2_t acc[33];
#pragma unroll
    for (int i = 0; i < 33; ++i) acc[i] = f32x2_t{0.f, 0.f};
#pragma unroll 1
    for (int bt = 0; bt < 4; ++bt) {
      const int n = 16 * ng + 4 * bt;
      const f32x4 ar = *reinterpret_cast<const f32x4*>(prm + n), ai = *reinterpret_cast<const f32x4*>(prm + NST + n);
      const f32x4 cr = *reinterpret_cast<const f32x4*>(prm + 2 * NST + n), ci = *reinterpret_cast<const f32x4*>(prm + 3 * NST + n);
      const f32x4 wr = *reinterpret_cast<const f32x4*>(pw + n), wi = *reinterpret_cast<const f32x4*>(pw + NST + n);
      float yr0[4], yi0[4];
#pragma unroll
      for (int k = 0; k < 4; ++k) {
        const float e = __expf(ar[k] * p0);
        float sn, cs;
        __sincosf(ai[k] * p0, &sn, &cs);
        const float zr = e * cs, zi = e * sn;
        yr0[k] = cr[k] * zr - ci[k] * zi;
        yi0[k] = cr[k] * zi + ci[k] * zr;
      }
      f32x2_t yra = {yr0[0], yr0[1]}, yia = {yi0[0], yi0[1]}, wra = {wr[0], wr[1]}, wia = {wi[0], wi[1]};
      f32x2_t yrb = {yr0[2], yr0[3]}, yib = {yi0[2], yi0[3]}, wrb = {wr[2], wr[3]}, wib = {wi[2], wi[3]};
#pragma unroll
      for (int i = 0; i < 33; ++i) {
        acc[i] += yra + yrb;
        const f32x2_t ta = yra * wra - yia * wia, tb = yrb * wrb - yib * wib;
        yia = yra * wia + yia * wra;
        yib = yrb * wib + yib * wrb;
        yra = ta;
        yrb = tb;
      }
    }
#pragma unroll
    for (int i = 0; i < 33; ++i) part[ng * 400 + 33 * r + i] = acc[i].x + acc[i].y;
  }
  __syncthreads();
  SST(2);
  for (int i = u; i < 385; i += 128) {
    const int o = i < 384 ? 33 * (i >> 5) + (i & 31) : 33 * 11 + 32;
    float v = 0.f;
#pragma unroll
    for (int g = 0; g < 8; ++g) v += part[g * 400 + o];
    v *= 2.0f;
    kd[i] = pk2(v, v - lo16(pk2(v, v)));
  }
  if (u == 1) kd[385] = 0u;
  __syncthreads();
  if (aw >= 0) {
    wo_issue(A.Wo, aw, 0, lds_base(smem));
    wo_issue(A.Wo, aw, 1, lds_base(smem));
  }
  const __amdgpu_buffer_rsrc_t rs = MKRS(A.ws + OFF_KF, DM * 2 * CKN * 2);
  for (int un = u; un < 229; un += 128) {
    v4u oh, ol;
#pragma unroll
    for (int e = 0; e < 4; ++e) {
      const int lag = 2080 - (8 * un + 2 * e);
      const unsigned pb = pk2((float)lag, (float)(lag - 1));
      const unsigned k0 = kd[(unsigned)(lag - 256) < L_SEQ - 256u ? (pb & 0xffffu) - 0x4380u : 385u];
      const unsigned k1 = kd[(unsigned)(lag - 257) < L_SEQ - 256u ? (pb >> 16) - 0x4380u : 385u];
      oh[e] = __builtin_amdgcn_perm(k1, k0, 0x05040100u);
      ol[e] = __builtin_amdgcn_perm(k1, k0, 0x07060302u);
    }
    ST128(oh, rs, (d * 2 * CKN + 8 * un) * 2, 0);
    ST128(ol, rs, (d * 2 * CKN + CKN + 8 * un) * 2, 0);
  }
  DRAIN();
  __syncthreads();
  if (tid == 0) set_flag((unsigned*)(A.ws + OFF_FL) + FL_KG + (hi >> 1) * 32 + (hi & 1));
  SST(3);
}

constexpr int C_SM = 131072, C_SO = C_SM + 8192, C_KT = C_SO + 2048, C_X = C_KT + 16384, C_SQ = C_X + 1024;
constexpr int C_SK = C_SQ + 2432;
static_assert(C_SK + 8 * QS * 2 <= 163808, "lds");
#define CHAIN_ASM_ATT \
  "s_mov_b32 s66, %[d0]\n" \
  "s_mov_b32 s94, 576\n" \
  "s_mov_b32 s95, 288\n" \
  "s_mov_b32 s84, 0x43400000\n" \
  "s_mov_b32 s85, 0x43400000\n" \
  "s_mov_b32 s92, 0x88000000\n" \
  "s_mov_b32 s93, 0x87fffff7\n" \
  "s_mov_b32 s78, 0x3e0293ee\n" \
  "s_mov_b32 s76, 0x33333333\n" \
  "s_mov_b32 s77, 0x33333333\n" \
  "s_add_u32 s58, s56, s60\n" \
  "s_addc_u32 s59, s57, 0\n" \
  "s_xor_b32 s38, s35, 0\n" \
  "s_add_u32 s26, s58, s38\n" \
  "s_addc_u32 s27, s59, 0\n" \
  "s_xor_b32 s38, s35, 1024\n" \
  "s_add_u32 s28, s58, s38\n" \
  "s_addc_u32 s29, s59, 0\n" \
  "s_xor_b32 s38, s35, 2048\n" \
  "s_add_u32 s30, s58, s38\n" \
  "s_addc_u32 s31, s59, 0\n" \
  "s_xor_b32 s38, s35, 3072\n" \
  "s_add_u32 s32, s58, s38\n" \
  "s_addc_u32 s33, s59, 0\n" \
  "s_lshr_b32 s98, s60, 2\n" \
  "s_add_u32 s98, s98, s74\n" \
  "s_add_u32 s98, s98, 131072\n" \
  "s_lshl_b32 s97, s73, 5\n" \
  "s_add_u32 s97, s97, s74\n" \
  "s_add_u32 s97, s97, 161152\n" \
  "s_lshl_b32 s99, s73, 10\n" \
  "v_mbcnt_lo_u32_b32 v48, -1, 0\n" \
  "v_mbcnt_hi_u32_b32 v48, -1, v48\n" \
  "v_and_b32 v49, 15, v48\n" \
  "v_lshl_add_u32 v9, v49, 1, s97\n" \
  "v_lshrrev_b32 v49, 4, v48\n" \
  "v_mad_u32_u24 v9, v49, s94, v9\n" \
  "v_add_u32 v7, s99, v1\n" \
  "v_mov_b64 v[12:13], 0\n" \
  "v_mov_b64 v[14:15], 0\n" \
  "v_mov_b64 v[16:17], 0\n" \
  "v_mov_b64 v[18:19], 0\n" \
  "v_mov_b64 v[20:21], 0\n" \
  "v_mov_b64 v[22:23], 0\n" \
  "v_mov_b32 v24, 0x44870000\n" \
  "v_mul_f32 v24, s72, v24\n" \
  "s_nop 0\n" \
  "v_readfirstlane_b32 s86, v24\n" \
  "s_lshr_b32 s86, s86, 23\n" \
  "s_addk_i32 s86, 0xff83\n" \
  "s_ashr_i32 s86, s86, 1\n" \
  "s_max_i32 s86, s86, -20\n" \
  "s_min_i32 s86, s86, 100\n" \
  "s_mov_b32 s82, s86\n" \
  "s_lshr_b32 s83, s72, 23\n" \
  "s_sub_i32 s83, s83, 242\n" \
  "s_lshl_b32 s88, s73, 10\n" \
  "v_add_u32 v24, s88, v1\n" \
  "ds_read_b128 v[32:35], v24 offset:0\n" \
  "s_waitcnt lgkmcnt(0)\n" \
  "v_mov_b32 v45, 0\n" \
  "v_dot2c_f32_bf16 v45, v32, v32\n" \
  "v_dot2c_f32_bf16 v45, v33, v33\n" \
  "v_dot2c_f32_bf16 v45, v34, v34\n" \
  "v_dot2c_f32_bf16 v45, v35, v35\n" \
  "s_add_u32 s88, s74, 157760\n" \
  "v_mov_b32 v44, s88\n" \
  "s_nop 0\n" \
  "v_cmpx_lt_f32 vcc, 2.0, v45\n" \
  "ds_max_u32 v44, v45\n" \
  "s_mov_b64 exec, -1\n" \
  ".p2align 6\n" \
  ".LCA_AL_%=:\n" \
  ".LCA_TOP_%=:\n" \
  "s_waitcnt lgkmcnt(0)\n" \
  "s_barrier\n" \
  "ds_read_b32 v8, v1 offset:26688\n" \
  "s_cmp_lg_u32 s73, 3\n" \
  "s_cbranch_scc1 .LCA_NOTK_%=\n" \
  "s_memrealtime s[38:39]\n" \
  "v_mov_b32 v26, 1\n" \
  "s_mul_i32 s23, s71, %[pp]\n" \
  "s_lshr_b32 s62, s66, 4\n" \
  "s_mov_b64 exec, 1\n" \
  "s_waitcnt lgkmcnt(0)\n" \
  "s_mov_b32 s67, s38\n" \
  "s_add_u32 s38, s38, s62\n" \
  "v_mov_b32 v25, s38\n" \
  "buffer_atomic_umax v25, off, s[16:19], s68\n" \
  "buffer_atomic_add v26, off, s[16:19], s68 offset:64\n" \
  "v_add_u32 v27, s23, v25\n" \
  "buffer_store_dword v27, off, s[16:19], s69 sc1\n" \
  "s_mov_b64 exec, -1\n" \
  "s_branch .LCA_SIDE_%=\n" \
  ".LCA_NOTK_%=:\n" \
  "s_cmp_lg_u32 s73, 0\n" \
  "s_cbranch_scc1 .LCA_NOZ_%=\n" \
  "v_mov_b64 v[26:27], s[92:93]\n" \
  "s_xor_b32 s88, s60, 0x8000\n" \
  "s_add_u32 s88, s88, s61\n" \
  "buffer_store_dwordx2 v[26:27], v5, s[40:43], s88 offen sc1\n" \
  ".LCA_NOZ_%=:\n" \
  "s_nop 7\n" \
  ".LCA_SIDE_%=:\n" \
  "s_waitcnt lgkmcnt(0)\n" \
  "v_readfirstlane_b32 s86, v8\n" \
  "s_mov_b64 vcc, s[76:77]\n" \
  "s_lshr_b32 s86, s86, 23\n" \
  "s_add_i32 s86, s86, s83\n" \
  "s_ashr_i32 s86, s86, 1\n" \
  "s_max_i32 s86, s86, s82\n" \
  "s_min_i32 s86, s86, 100\n" \
  "s_lshl_b32 s86, s86, 23\n" \
  "s_add_i32 s75, s86, 0x3c800000\n" \
  "s_cmp_eq_u32 s71, 0\n" \
  "ds_read_b128 v[32:35], v1 offset:0\n" \
  "ds_read_b128 v[36:39], v1 offset:1024\n" \
  "ds_read_b128 v[48:51], v1 offset:2048\n" \
  "ds_read_b128 v[52:55], v1 offset:3072\n" \
  "ds_read_b128 v[40:43], v1 offset:4096\n" \
  "ds_read_b128 v[56:59], v1 offset:5120\n" \
  "ds_read_b128 v[60:63], v1 offset:6144\n" \
  "ds_read_b128 v[44:47], v1 offset:7168\n" \
  "s_setprio 3\n" \
  "s_waitcnt lgkmcnt(7)\n" \
  "v_smfmac_f32_16x16x64_bf16 v[12:15], v[32:35], v[64:71], v0\n" \
  "s_waitcnt lgkmcnt(6)\n" \
  "v_smfmac_f32_16x16x64_bf16 v[12:15], v[36:39], v[72:79], v0\n" \
  "s_waitcnt lgkmcnt(5)\n" \
  "v_smfmac_f32_16x16x64_bf16 v[12:15], v[48:51], v[80:87], v0\n" \
  "s_waitcnt lgkmcnt(4)\n" \
  "v_smfmac_f32_16x16x64_bf16 v[12:15], v[52:55], v[88:95], v0\n" \
  "s_waitcnt lgkmcnt(3)\n" \
  "v_smfmac_f32_16x16x64_bf16 v[12:15], v[40:43], v[96:103], v0\n" \
  "s_waitcnt lgkmcnt(2)\n" \
  "v_smfmac_f32_16x16x64_bf16 v[12:15], v[56:59], v[104:111], v0\n" \
  "s_waitcnt lgkmcnt(1)\n" \
  "v_smfmac_f32_16x16x64_bf16 v[12:15], v[60:63], v[112:119], v0\n" \
  "s_waitcnt lgkmcnt(0)\n" \
  "v_smfmac_f32_16x16x64_bf16 v[12:15], v[44:47], v[120:127], v0\n" \
  "ds_read_b128 v[24:27], v1 offset:10240\n" \
  "ds_read_b128 v[28:31], v1 offset:14336\n" \
  "s_nop 5\n" \
  "v_add_f32 v12, v12, v14\n" \
  "v_add_f32 v13, v13, v15\n" \
  "v_cvt_pk_bf16_f32 v12, v12, v13\n" \
  "ds_write_b16 v9, v12 offset:0\n" \
  "ds_write_b16_d16_hi v9, v12 offset:288\n" \
  "s_setprio 0\n" \
  "s_barrier\n" \
  "v_smfmac_f32_16x16x64_bf16 v[16:19], v[32:35], v[128:135], v0\n" \
  "v_smfmac_f32_16x16x64_bf16 v[20:23], v[32:35], v[192:199], v0\n" \
  "ds_read_b128 v[32:35], v1 offset:11264\n" \
  "v_smfmac_f32_16x16x64_bf16 v[16:19], v[36:39], v[136:143], v0\n" \
  "v_smfmac_f32_16x16x64_bf16 v[20:23], v[36:39], v[200:207], v0\n" \
  "ds_read_b128 v[36:39], v1 offset:15360\n" \
  "v_smfmac_f32_16x16x64_bf16 v[16:19], v[48:51], v[144:151], v0\n" \
  "v_smfmac_f32_16x16x64_bf16 v[20:23], v[48:51], v[208:215], v0\n" \
  "v_smfmac_f32_16x16x64_bf16 v[16:19], v[52:55], v[152:159], v0\n" \
  "v_smfmac_f32_16x16x64_bf16 v[20:23], v[52:55], v[216:223], v0\n" \
  "v_smfmac_f32_16x16x64_bf16 v[16:19], v[40:43], v[160:167], v0\n" \
  "v_smfmac_f32_16x16x64_bf16 v[20:23], v[40:43], v[224:231], v0\n" \
  ".fill ((16 - (. - .LCA_AL_%=)) & 31) >> 2, 4, 0xbf800000\n" \
  "s_waitcnt lgkmcnt(2)\n" \
  "s_barrier\n" \
  "s_setprio 3\n" \
  "ds_read_b128 v[48:51], v2 offset:0\n" \
  "ds_read_b128 v[52:55], v2 offset:64\n" \
  "v_smfmac_f32_16x16x64_bf16 v[16:19], v[56:59], v[168:175], v0\n" \
  "v_smfmac_f32_16x16x64_bf16 v[20:23], v[56:59], v[232:239], v0\n" \
  "ds_read_b128 v[56:59], v2 offset:128\n" \
  "s_cbranch_scc1 .LCA_LAST_%=\n" \
  "s_waitcnt lgkmcnt(2)\n" \
  "v_mfma_f32_16x16x32_bf16 v[12:15], v[24:27], v[48:51], 0\n" \
  "ds_read_b128 v[24:27], v1 offset:12288\n" \
  "v_mfma_f32_16x16x32_bf16 v[8:11], v[28:31], v[48:51], 0\n" \
  "ds_read_b128 v[28:31], v1 offset:16384\n" \
  "v_mfma_f32_16x16x32_bf16 v[40:43], v[48:51], v[48:51], 0\n" \
  "ds_read_b128 v[48:51], v7 offset:18432\n" \
  "v_smfmac_f32_16x16x64_bf16 v[16:19], v[60:63], v[176:183], v0\n" \
  "v_smfmac_f32_16x16x64_bf16 v[20:23], v[60:63], v[240:247], v0\n" \
  "ds_read_b128 v[60:63], v2 offset:192\n" \
  "s_waitcnt lgkmcnt(5)\n" \
  "v_mfma_f32_16x16x32_bf16 v[12:15], v[32:35], v[52:55], v[12:15]\n" \
  "ds_read_b128 v[32:35], v1 offset:13312\n" \
  "v_mfma_f32_16x16x32_bf16 v[8:11], v[36:39], v[52:55], v[8:11]\n" \
  "ds_read_b128 v[36:39], v1 offset:17408\n" \
  "v_mfma_f32_16x16x32_bf16 v[40:43], v[52:55], v[52:55], v[40:43]\n" \
  "ds_read_b128 v[52:55], v7 offset:22528\n" \
  "s_waitcnt lgkmcnt(7)\n" \
  "v_mfma_f32_16x16x32_bf16 v[40:43], v[56:59], v[56:59], v[40:43]\n" \
  "s_waitcnt lgkmcnt(5)\n" \
  "v_mfma_f32_16x16x32_bf16 v[12:15], v[24:27], v[56:59], v[12:15]\n" \
  "ds_read_b128 v[24:27], v6 offset:2048\n" \
  "v_mfma_f32_16x16x32_bf16 v[8:11], v[28:31], v[56:59], v[8:11]\n" \
  "ds_read_b128 v[28:31], v6 offset:3072\n" \
  "s_waitcnt lgkmcnt(5)\n" \
  "v_mfma_f32_16x16x32_bf16 v[40:43], v[60:63], v[60:63], v[40:43]\n" \
  "ds_read_b128 v[56:59], v6 offset:0\n" \
  "s_waitcnt lgkmcnt(4)\n" \
  "v_mfma_f32_16x16x32_bf16 v[12:15], v[32:35], v[60:63], v[12:15]\n" \
  "ds_read_b128 v[32:35], v6 offset:8192\n" \
  "v_mfma_f32_16x16x32_bf16 v[8:11], v[36:39], v[60:63], v[8:11]\n" \
  "ds_read_b128 v[36:39], v6 offset:9216\n" \
  "v_smfmac_f32_16x16x64_bf16 v[16:19], v[44:47], v[184:191], v0\n" \
  "ds_read_b128 v[60:63], v6 offset:1024\n" \
  "s_waitcnt lgkmcnt(6)\n" \
  "v_smfmac_f32_16x16x64_bf16 v[20:23], v[44:47], v[248:255], v0\n" \
  "v_cndmask_b32_dpp v43, v43, v42, vcc quad_perm:[0,1,0,1] row_mask:0xf bank_mask:0xf\n" \
  "v_mov_b32 v41, 0\n" \
  "v_mov_b32 v42, 0\n" \
  "v_cndmask_b32_dpp v12, v8, v12, vcc quad_perm:[0,1,0,1] row_mask:0xf bank_mask:0xf\n" \
  "v_cndmask_b32_dpp v13, v9, v13, vcc quad_perm:[0,1,0,1] row_mask:0xf bank_mask:0xf\n" \
  "v_cndmask_b32_dpp v14, v10, v14, vcc quad_perm:[0,1,0,1] row_mask:0xf bank_mask:0xf\n" \
  "v_cndmask_b32_dpp v15, v11, v15, vcc quad_perm:[0,1,0,1] row_mask:0xf bank_mask:0xf\n" \
  "v_maximum3_f32 v8, v43, v12, v13\n" \
  "v_maximum3_f32 v8, v8, v14, v15\n" \
  "v_add_f32 v16, v16, v18\n" \
  "v_add_f32 v17, v17, v19\n" \
  "v_max_f32_dpp v8, v8, v8 quad_perm:[2,3,0,1] row_mask:0xf bank_mask:0xf\n" \
  "v_mov_b32 v9, v8\n" \
  "v_add_f32 v20, v20, v22\n" \
  "v_add_f32 v21, v21, v23\n" \
  "v_permlane16_swap_b32 v8, v9\n" \
  "v_max_f32 v8, v8, v9\n" \
  "v_mov_b32 v9, v8\n" \
  "v_cvt_pk_bf16_f32 v16, v16, v17\n" \
  "v_cvt_pk_bf16_f32 v17, v20, v21\n" \
  "v_permlane32_swap_b32 v8, v9\n" \
  "v_max_f32 v8, v8, v9\n" \
  "s_barrier\n" \
  "buffer_load_dword v7, v5, s[16:19], s24 offen sc1\n" \
  "v_mul_f32 v8, s78, v8\n" \
  "v_pk_fma_f32 v[12:13], v[12:13], s[78:79], v[8:9] op_sel_hi:[1,0,0] neg_lo:[0,0,1] neg_hi:[0,0,1]\n" \
  "v_pk_fma_f32 v[14:15], v[14:15], s[78:79], v[8:9] op_sel_hi:[1,0,0] neg_lo:[0,0,1] neg_hi:[0,0,1]\n" \
  "v_fma_f32 v43, v43, s78, -v8\n" \
  "v_exp_f32 v12, v12\n" \
  "v_exp_f32 v13, v13\n" \
  "v_exp_f32 v14, v14\n" \
  "v_exp_f32 v15, v15\n" \
  "v_exp_f32 v43, v43\n" \
  "v_cvt_pk_bf16_f32 v44, v12, v13\n" \
  "v_cvt_pk_bf16_f32 v45, v14, v15\n" \
  "v_pk_add_f32 v[18:19], v[12:13], v[14:15]\n" \
  "v_mov_b32_dpp v46, v44 quad_perm:[2,3,0,1] row_mask:0xf bank_mask:0xf\n" \
  "v_mov_b32_dpp v9, v43 quad_perm:[2,3,0,1] row_mask:0xf bank_mask:0xf\n" \
  "v_mov_b32_dpp v47, v45 quad_perm:[2,3,0,1] row_mask:0xf bank_mask:0xf\n" \
  "v_add_f32 v10, v18, v43\n" \
  "v_cvt_pk_bf16_f32 v40, v43, v9\n" \
  "v_add_f32 v10, v10, v19\n" \
  "v_mfma_f32_16x16x32_bf16 v[12:15], v[48:51], v[44:47], 0\n" \
  "v_cvt_pk_bf16_f32 v43, v43, v9\n" \
  "v_add_f32_dpp v10, v10, v10 quad_perm:[2,3,0,1] row_mask:0xf bank_mask:0xf\n" \
  "v_mfma_f32_16x16x32_bf16 v[20:23], v[52:55], v[44:47], 0\n" \
  "v_mul_f32 v11, s75, v10\n" \
  "v_mul_f32 v10, s75, v10\n" \
  "v_mfma_f32_16x16x16_bf16 v[12:15], v[16:17], v[40:41], v[12:15]\n" \
  "ds_read_b128 v[48:51], v6 offset:10240\n" \
  "v_permlane16_swap_b32 v10, v11\n" \
  "v_add_f32 v10, v10, v11\n" \
  "v_mfma_f32_16x16x16_bf16 v[20:23], v[16:17], v[42:43], v[20:23]\n" \
  "v_mov_b32 v11, v10\n" \
  "ds_read_b128 v[52:55], v6 offset:11264\n" \
  "v_mov_b64 v[18:19], s[84:85]\n" \
  "v_permlane32_swap_b32 v10, v11\n" \
  "v_add_f32 v10, v10, v11\n" \
  "v_rcp_f32 v10, v10\n" \
  "s_nop 0\n" \
  "v_pk_mul_f32 v[12:13], v[12:13], v[10:11] op_sel_hi:[1,0]\n" \
  "v_pk_mul_f32 v[14:15], v[14:15], v[10:11] op_sel_hi:[1,0]\n" \
  "v_pk_mul_f32 v[20:21], v[20:21], v[10:11] op_sel_hi:[1,0]\n" \
  "v_pk_mul_f32 v[22:23], v[22:23], v[10:11] op_sel_hi:[1,0]\n" \
  "v_cvt_pk_bf16_f32 v40, v12, v13\n" \
  "v_cvt_pk_bf16_f32 v41, v14, v15\n" \
  "v_cvt_pk_bf16_f32 v42, v20, v21\n" \
  "ds_write_b64 v4, v[40:41]\n" \
  "v_cvt_pk_bf16_f32 v43, v22, v23\n" \
  "ds_write_b64 v4, v[42:43] offset:1024\n" \
  "v_max_u32 v16, 1, v16\n" \
  "v_max_u32 v17, 1, v17\n" \
  "buffer_store_dwordx2 v[16:17], v3, s[52:55], s70 offen sc1\n" \
  "v_mov_b64 v[12:13], s[84:85]\n" \
  "v_mov_b64 v[14:15], s[84:85]\n" \
  "v_mov_b64 v[16:17], s[84:85]\n" \
  "v_mov_b64 v[20:21], s[84:85]\n" \
  "v_mov_b64 v[22:23], s[84:85]\n" \
  "v_mov_b64 v[8:9], s[84:85]\n" \
  "v_mov_b64 v[10:11], s[84:85]\n" \
  "s_setprio 0\n" \
  "s_waitcnt lgkmcnt(0)\n" \
  "s_barrier\n" \
  "ds_read_b128 v[40:43], v1 offset:8192\n" \
  "ds_read_b128 v[44:47], v1 offset:9216\n" \
  "s_waitcnt lgkmcnt(1)\n" \
  "v_smfmac_f32_16x16x64_bf16 v[12:15], v[40:43], v[56:63], v0\n" \
  "s_waitcnt lgkmcnt(0)\n" \
  "v_smfmac_f32_16x16x64_bf16 v[12:15], v[44:47], v[32:39], v0\n" \
  "ds_read_b128 v[56:59], v6 offset:4096\n" \
  "ds_read_b128 v[60:63], v6 offset:5120\n" \
  "v_smfmac_f32_16x16x64_bf16 v[16:19], v[40:43], v[24:31], v0\n" \
  "ds_read_b128 v[32:35], v6 offset:12288\n" \
  "ds_read_b128 v[36:39], v6 offset:13312\n" \
  "v_smfmac_f32_16x16x64_bf16 v[16:19], v[44:47], v[48:55], v0\n" \
  "ds_read_b128 v[24:27], v6 offset:6144\n" \
  "ds_read_b128 v[28:31], v6 offset:7168\n" \
  "v_add_f32 v12, v12, v14\n" \
  "v_add_f32 v13, v13, v15\n" \
  "global_atomic_add_x2 v5, v[12:13], s[26:27]\n" \
  "ds_read_b128 v[48:51], v6 offset:14336\n" \
  "ds_read_b128 v[52:55], v6 offset:15360\n" \
  "s_waitcnt lgkmcnt(6)\n" \
  "v_add_f32 v16, v16, v18\n" \
  "v_add_f32 v17, v17, v19\n" \
  "global_atomic_add_x2 v5, v[16:17], s[28:29]\n" \
  "v_smfmac_f32_16x16x64_bf16 v[20:23], v[40:43], v[56:63], v0\n" \
  "s_waitcnt lgkmcnt(4)\n" \
  "v_smfmac_f32_16x16x64_bf16 v[20:23], v[44:47], v[32:39], v0\n" \
  "s_waitcnt lgkmcnt(2)\n" \
  "v_smfmac_f32_16x16x64_bf16 v[8:11], v[40:43], v[24:31], v0\n" \
  "s_waitcnt lgkmcnt(0)\n" \
  "v_smfmac_f32_16x16x64_bf16 v[8:11], v[44:47], v[48:55], v0\n" \
  "s_nop 4\n" \
  "v_add_f32 v20, v20, v22\n" \
  "v_add_f32 v21, v21, v23\n" \
  "global_atomic_add_x2 v5, v[20:21], s[30:31]\n" \
  "v_add_f32 v8, v8, v10\n" \
  "v_add_f32 v9, v9, v11\n" \
  "global_atomic_add_x2 v5, v[8:9], s[32:33]\n" \
  "s_memrealtime s[62:63]\n" \
  "v_mbcnt_lo_u32_b32 v48, -1, 0\n" \
  "v_mbcnt_hi_u32_b32 v48, -1, v48\n" \
  "v_lshrrev_b32 v49, 3, v48\n" \
  "v_and_b32 v50, 7, v48\n" \
  "v_lshlrev_b32 v50, 4, v50\n" \
  "v_lshl_or_b32 v44, v49, 8, v50\n" \
  "s_add_u32 s20, s20, 32\n" \
  "s_mov_b32 s21, 0x8000\n" \
  "v_mov_b32 v52, s22\n" \
  "v_mov_b32 v54, s20\n" \
  "s_mul_i32 s88, s71, %[pp]\n" \
  "s_lshl_b32 s89, s71, 10\n" \
  "s_sub_u32 s88, s88, s25\n" \
  "s_mul_i32 s38, s24, %[an]\n" \
  "s_cmp_gt_u32 s89, s38\n" \
  "s_cselect_b32 s89, 0x1000, 0\n" \
  "s_waitcnt vmcnt(5)\n" \
  "v_mov_b32 v46, v7\n" \
  "s_branch .LCA_TKE_%=\n" \
  ".LCA_TKR_%=:\n" \
  "s_sub_u32 s21, s21, 1\n" \
  "s_cbranch_scc1 .LCA_TKO_%=\n" \
  "s_and_b32 s38, s21, 7\n" \
  "s_cmp_lg_u32 s38, 7\n" \
  "s_cbranch_scc1 .LCA_TKS_%=\n" \
  "buffer_load_dword v46, v5, s[16:19], s24 offen sc1\n" \
  ".LCA_TKS_%=:\n" \
  "s_sleep 1\n" \
  ".LCA_TKE_%=:\n" \
  "v_readlane_b32 s38, v46, 8\n" \
  "v_readlane_b32 s39, v46, 24\n" \
  "v_readlane_b32 s23, v46, 40\n" \
  "v_readlane_b32 s34, v46, 56\n" \
  "s_add_u32 s38, s38, s39\n" \
  "s_add_u32 s23, s23, s34\n" \
  "s_add_u32 s38, s38, s23\n" \
  "s_cmp_lt_u32 s38, s20\n" \
  "s_cbranch_scc1 .LCA_TKR_%=\n" \
  ".LCA_TKO_%=:\n" \
  "v_readlane_b32 s34, v46, 0\n" \
  "v_readlane_b32 s38, v46, 16\n" \
  "v_readlane_b32 s39, v46, 32\n" \
  "v_readlane_b32 s23, v46, 48\n" \
  "s_max_u32 s34, s34, s38\n" \
  "s_max_u32 s39, s39, s23\n" \
  "s_add_u32 s34, s34, %[cd]\n" \
  "v_readlane_b32 s23, v46, 1\n" \
  "s_max_u32 s34, s34, s39\n" \
  "s_sub_u32 s23, s23, s34\n" \
  "s_sub_u32 s23, s23, s88\n" \
  "s_cmp_lt_u32 s23, s89\n" \
  "s_cselect_b32 s23, s23, 0\n" \
  "s_min_u32 s23, s23, %[ka]\n" \
  "s_add_u32 s34, s34, s23\n" \
  "s_add_u32 s34, s34, s64\n" \
  "v_mov_b32 v55, s34\n" \
  "ds_write_b64 v52, v[54:55]\n" \
  "s_branch .LCA_WAIU_%=\n" \
  ".LCA_WAIS_%=:\n" \
  "s_cmp_lt_i32 s39, -1000\n" \
  "s_cbranch_scc1 .LCA_WAIE_%=\n" \
  "s_sleep 1\n" \
  ".LCA_WAIU_%=:\n" \
  "s_memrealtime s[38:39]\n" \
  "s_waitcnt lgkmcnt(0)\n" \
  "s_sub_i32 s39, s38, s34\n" \
  "s_cmp_lt_i32 s39, -7\n" \
  "s_cbranch_scc1 .LCA_WAIS_%=\n" \
  "s_cmp_lt_i32 s39, 0\n" \
  "s_cbranch_scc1 .LCA_WAIU_%=\n" \
  ".LCA_WAIE_%=:\n" \
  "buffer_load_dwordx4 v[24:27], v44, s[40:43], s60 offen sc1\n" \
  "buffer_load_dwordx4 v[28:31], v44, s[40:43], s60 offen offset:2048 sc1\n" \
  "s_sub_u32 s62, s62, s67\n" \
  "s_lshl_b32 s62, s62, 4\n" \
  "s_sub_i32 s62, s62, s66\n" \
  "s_min_i32 s62, s62, 128\n" \
  "s_max_i32 s62, s62, -256\n" \
  "s_ashr_i32 s62, s62, 2\n" \
  "s_add_i32 s66, s66, s62\n" \
  "s_add_u32 s70, s70, 0x10000\n" \
  "v_mbcnt_lo_u32_b32 v48, -1, 0\n" \
  "v_mbcnt_hi_u32_b32 v48, -1, v48\n" \
  "v_and_b32 v49, 15, v48\n" \
  "v_lshl_add_u32 v9, v49, 1, s97\n" \
  "v_lshrrev_b32 v49, 4, v48\n" \
  "v_mad_u32_u24 v9, v49, s94, v9\n" \
  "v_add_u32 v7, s99, v1\n" \
  "v_mov_b64 v[12:13], 0\n" \
  "v_mov_b64 v[14:15], 0\n" \
  "v_mov_b64 v[16:17], 0\n" \
  "v_mov_b64 v[18:19], 0\n" \
  "v_mov_b64 v[20:21], 0\n" \
  "v_mov_b64 v[22:23], 0\n" \
  "v_and_b32 v49, 3, v48\n" \
  "v_lshl_add_u32 v40, v49, 2, s98\n" \
  "v_lshrrev_b32 v49, 2, v48\n" \
  "v_bfe_u32 v48, v48, 4, 1\n" \
  "v_xor_b32 v49, v49, v48\n" \
  "v_lshl_add_u32 v40, v49, 5, v40\n" \
  "s_add_i32 s80, s86, 0x35000000\n" \
  "s_sub_u32 s91, 0x4a350000, s86\n" \
  "s_min_u32 s91, s91, 0x4ca00000\n" \
  "s_mov_b32 s89, s60\n" \
  "s_xor_b32 s60, s60, 0x80\n" \
  "s_bitcmp0_b32 s60, 7\n" \
  "s_cselect_b32 s88, 0x8000, 0\n" \
  "s_add_u32 s60, s60, s88\n" \
  "s_and_b32 s60, s60, 0xffff\n" \
  "s_add_u32 s58, s56, s60\n" \
  "s_addc_u32 s59, s57, 0\n" \
  "s_xor_b32 s38, s35, 0\n" \
  "s_add_u32 s26, s58, s38\n" \
  "s_addc_u32 s27, s59, 0\n" \
  "s_xor_b32 s38, s35, 1024\n" \
  "s_add_u32 s28, s58, s38\n" \
  "s_addc_u32 s29, s59, 0\n" \
  "s_xor_b32 s38, s35, 2048\n" \
  "s_add_u32 s30, s58, s38\n" \
  "s_addc_u32 s31, s59, 0\n" \
  "s_xor_b32 s38, s35, 3072\n" \
  "s_add_u32 s32, s58, s38\n" \
  "s_addc_u32 s33, s59, 0\n" \
  "s_sub_u32 s71, s71, 1\n" \
  "s_mov_b32 s87, 0x40000\n" \
  "s_branch .LCA_CHK_%=\n" \
  ".LCA_POLL_%=:\n" \
  "buffer_load_dwordx4 v[24:27], v44, s[40:43], s89 offen sc1\n" \
  "buffer_load_dwordx4 v[28:31], v44, s[40:43], s89 offen offset:2048 sc1\n" \
  ".LCA_CHK_%=:\n" \
  "s_sub_u32 s87, s87, 1\n" \
  "s_cbranch_scc1 .LCA_END_%=\n" \
  "s_waitcnt vmcnt(1)\n" \
  "v_cvt_f32_i32 v32, v24\n" \
  "v_cvt_f32_i32 v33, v25\n" \
  "v_cvt_f32_i32 v34, v26\n" \
  "v_cvt_f32_i32 v35, v27\n" \
  "v_pk_mul_f32 v[24:25], v[32:33], s[80:81] op_sel_hi:[1,0]\n" \
  "v_pk_mul_f32 v[26:27], v[34:35], s[80:81] op_sel_hi:[1,0]\n" \
  "v_max3_f32 v45, |v32|, |v33|, |v34|\n" \
  "v_cvt_pk_bf16_f32 v24, v24, v26\n" \
  "v_cvt_pk_bf16_f32 v25, v25, v27\n" \
  "ds_write2_b32 v40, v24, v25 offset0:0 offset1:4\n" \
  "v_max3_f32 v45, |v45|, |v35|, |v35|\n" \
  "s_waitcnt vmcnt(0)\n" \
  "v_cvt_f32_i32 v32, v28\n" \
  "v_cvt_f32_i32 v33, v29\n" \
  "v_cvt_f32_i32 v34, v30\n" \
  "v_cvt_f32_i32 v35, v31\n" \
  "v_pk_mul_f32 v[28:29], v[32:33], s[80:81] op_sel_hi:[1,0]\n" \
  "v_pk_mul_f32 v[30:31], v[34:35], s[80:81] op_sel_hi:[1,0]\n" \
  "v_max3_f32 v45, |v45|, |v32|, |v33|\n" \
  "v_cvt_pk_bf16_f32 v28, v28, v30\n" \
  "v_cvt_pk_bf16_f32 v29, v29, v31\n" \
  "ds_write2_b32 v40, v28, v29 offset0:128 offset1:132\n" \
  "v_max3_f32 v45, |v45|, |v34|, |v35|\n" \
  "v_cmp_lt_f32 vcc, s91, v45\n" \
  "s_cbranch_vccnz .LCA_BIG_%=\n" \
  ".LCA_SML_%=:\n" \
  "s_branch .LCA_TOP_%=\n" \
  ".LCA_BIG_%=:\n" \
  "v_cmp_lt_f32 vcc, 0x4ca00000, v45\n" \
  "s_cbranch_vccnz .LCA_POLL_%=\n" \
  "v_mul_f32 v45, s80, v45\n" \
  "v_mul_f32 v45, v45, v45\n" \
  "s_add_u32 s88, s74, 157760\n" \
  "v_mov_b32 v44, s88\n" \
  "s_nop 0\n" \
  "v_cmpx_lt_f32 vcc, 2.0, v45\n" \
  "ds_max_u32 v44, v45\n" \
  "s_mov_b64 exec, -1\n" \
  "s_branch .LCA_SML_%=\n" \
  ".LCA_LAST_%=:\n" \
  "s_nop 0\n" \
  "v_smfmac_f32_16x16x64_bf16 v[16:19], v[60:63], v[176:183], v0\n" \
  "v_smfmac_f32_16x16x64_bf16 v[20:23], v[60:63], v[240:247], v0\n" \
  "v_smfmac_f32_16x16x64_bf16 v[16:19], v[44:47], v[184:191], v0\n" \
  "v_smfmac_f32_16x16x64_bf16 v[20:23], v[44:47], v[248:255], v0\n" \
  "s_nop 7\n" \
  "v_add_f32 v16, v16, v18\n" \
  "v_add_f32 v17, v17, v19\n" \
  "v_add_f32 v20, v20, v22\n" \
  "v_add_f32 v21, v21, v23\n" \
  "v_cvt_pk_bf16_f32 v16, v16, v17\n" \
  "v_cvt_pk_bf16_f32 v17, v20, v21\n" \
  "s_nop 1\n" \
  "s_nop 0\n" \
  "v_max_u32 v16, 1, v16\n" \
  "v_max_u32 v17, 1, v17\n" \
  "buffer_store_dwordx2 v[16:17], v3, s[52:55], s70 offen sc1\n" \
  "s_waitcnt vmcnt(0)\n" \
  ".LCA_END_%=:\n" \
  "s_endpgm\n"
#define CHAIN_ASM_VO \
  "s_mov_b32 s66, %[d0]\n" \
  "s_mov_b32 s94, 576\n" \
  "s_mov_b32 s95, 288\n" \
  "s_mov_b32 s84, 0x43400000\n" \
  "s_mov_b32 s85, 0x43400000\n" \
  "s_mov_b32 s92, 0x88000000\n" \
  "s_mov_b32 s93, 0x87fffff7\n" \
  "s_mov_b32 s78, 0x3e0293ee\n" \
  "s_mov_b32 s76, 0x33333333\n" \
  "s_mov_b32 s77, 0x33333333\n" \
  "s_add_u32 s58, s56, s60\n" \
  "s_addc_u32 s59, s57, 0\n" \
  "s_xor_b32 s38, s35, 0\n" \
  "s_add_u32 s26, s58, s38\n" \
  "s_addc_u32 s27, s59, 0\n" \
  "s_xor_b32 s38, s35, 1024\n" \
  "s_add_u32 s28, s58, s38\n" \
  "s_addc_u32 s29, s59, 0\n" \
  "s_xor_b32 s38, s35, 2048\n" \
  "s_add_u32 s30, s58, s38\n" \
  "s_addc_u32 s31, s59, 0\n" \
  "s_xor_b32 s38, s35, 3072\n" \
  "s_add_u32 s32, s58, s38\n" \
  "s_addc_u32 s33, s59, 0\n" \
  "s_lshr_b32 s98, s60, 2\n" \
  "s_add_u32 s98, s98, s74\n" \
  "s_add_u32 s98, s98, 131072\n" \
  "s_lshl_b32 s97, s73, 5\n" \
  "s_add_u32 s97, s97, s74\n" \
  "s_add_u32 s97, s97, 158592\n" \
  "s_lshl_b32 s99, s73, 11\n" \
  "s_add_u32 s99, s99, s74\n" \
  "s_add_u32 s99, s99, 133120\n" \
  "v_mbcnt_lo_u32_b32 v48, -1, 0\n" \
  "v_mbcnt_hi_u32_b32 v48, -1, v48\n" \
  "s_sub_u32 s88, s73, 4\n" \
  "v_and_b32 v51, 15, v48\n" \
  "v_lshl_add_u32 v4, v51, 1, s97\n" \
  "v_lshrrev_b32 v49, 4, v48\n" \
  "v_mad_u32_u24 v4, v49, s94, v4\n" \
  "v_lshrrev_b32 v49, 2, v51\n" \
  "v_and_b32 v50, 5, v51\n" \
  "v_and_or_b32 v49, v49, 2, v50\n" \
  "s_lshl_b32 s89, s88, 6\n" \
  "s_add_u32 s89, s89, s74\n" \
  "s_add_u32 s89, s89, 161152\n" \
  "v_and_b32 v50, 48, v48\n" \
  "v_add_u32 v6, s89, v50\n" \
  "v_mad_u32_u24 v6, v49, s95, v6\n" \
  "v_lshlrev_b32 v7, 3, v50\n" \
  "v_lshl_add_u32 v7, v49, 4, v7\n" \
  "v_and_b32 v49, 2, v51\n" \
  "v_lshl_add_u32 v7, v49, 29, v7\n" \
  "v_lshlrev_b32 v10, 4, v48\n" \
  "v_mov_b64 v[12:13], 0\n" \
  "v_mov_b64 v[14:15], 0\n" \
  "v_mov_b64 v[16:17], 0\n" \
  "v_mov_b64 v[18:19], 0\n" \
  "v_mov_b64 v[20:21], 0\n" \
  "v_mov_b64 v[22:23], 0\n" \
  "v_mov_b32 v24, 0x44870000\n" \
  "v_mul_f32 v24, s72, v24\n" \
  "s_nop 0\n" \
  "v_readfirstlane_b32 s86, v24\n" \
  "s_lshr_b32 s86, s86, 23\n" \
  "s_addk_i32 s86, 0xff83\n" \
  "s_ashr_i32 s86, s86, 1\n" \
  "s_max_i32 s86, s86, -20\n" \
  "s_min_i32 s86, s86, 100\n" \
  "s_mov_b32 s82, s86\n" \
  "s_lshr_b32 s83, s72, 23\n" \
  "s_sub_i32 s83, s83, 242\n" \
  "s_lshl_b32 s88, s73, 10\n" \
  "v_add_u32 v24, s88, v1\n" \
  "ds_read_b128 v[32:35], v24 offset:0\n" \
  "s_waitcnt lgkmcnt(0)\n" \
  "v_mov_b32 v45, 0\n" \
  "v_dot2c_f32_bf16 v45, v32, v32\n" \
  "v_dot2c_f32_bf16 v45, v33, v33\n" \
  "v_dot2c_f32_bf16 v45, v34, v34\n" \
  "v_dot2c_f32_bf16 v45, v35, v35\n" \
  "s_add_u32 s88, s74, 157760\n" \
  "v_mov_b32 v44, s88\n" \
  "s_nop 0\n" \
  "v_cmpx_lt_f32 vcc, 2.0, v45\n" \
  "ds_max_u32 v44, v45\n" \
  "s_mov_b64 exec, -1\n" \
  ".p2align 6\n" \
  ".LCV_AL_%=:\n" \
  ".LCV_TOP_%=:\n" \
  "s_waitcnt lgkmcnt(0)\n" \
  "s_barrier\n" \
  "s_setprio 2\n" \
  "ds_read_b128 v[24:27], v1 offset:0\n" \
  "ds_read_b128 v[28:31], v1 offset:1024\n" \
  "ds_read_b128 v[32:35], v1 offset:2048\n" \
  "ds_read_b128 v[36:39], v1 offset:3072\n" \
  "buffer_load_dword v5, off, s[44:47], s68 sc1\n" \
  "s_waitcnt lgkmcnt(3)\n" \
  "v_smfmac_f32_16x16x64_bf16 v[20:23], v[24:27], v[192:199], v0\n" \
  "ds_read_b128 v[40:43], v1 offset:4096\n" \
  "s_waitcnt lgkmcnt(3)\n" \
  "v_smfmac_f32_16x16x64_bf16 v[20:23], v[28:31], v[200:207], v0\n" \
  "ds_read_b128 v[44:47], v1 offset:5120\n" \
  "s_waitcnt lgkmcnt(3)\n" \
  "v_smfmac_f32_16x16x64_bf16 v[20:23], v[32:35], v[208:215], v0\n" \
  "ds_read_b128 v[48:51], v1 offset:6144\n" \
  "s_waitcnt lgkmcnt(3)\n" \
  "v_smfmac_f32_16x16x64_bf16 v[20:23], v[36:39], v[216:223], v0\n" \
  "ds_read_b128 v[52:55], v1 offset:7168\n" \
  "s_waitcnt lgkmcnt(3)\n" \
  "v_smfmac_f32_16x16x64_bf16 v[20:23], v[40:43], v[224:231], v0\n" \
  "s_waitcnt lgkmcnt(2)\n" \
  "v_smfmac_f32_16x16x64_bf16 v[20:23], v[44:47], v[232:239], v0\n" \
  "s_waitcnt lgkmcnt(1)\n" \
  "v_smfmac_f32_16x16x64_bf16 v[20:23], v[48:51], v[240:247], v0\n" \
  "s_waitcnt lgkmcnt(0)\n" \
  "v_smfmac_f32_16x16x64_bf16 v[20:23], v[52:55], v[248:255], v0\n" \
  "s_nop 0\n" \
  "v_smfmac_f32_16x16x64_bf16 v[12:15], v[24:27], v[64:71], v0\n" \
  "s_nop 0\n" \
  "v_smfmac_f32_16x16x64_bf16 v[12:15], v[28:31], v[72:79], v0\n" \
  "s_nop 0\n" \
  "v_smfmac_f32_16x16x64_bf16 v[12:15], v[32:35], v[80:87], v0\n" \
  "ds_read_b32 v11, v1 offset:26688\n" \
  "v_smfmac_f32_16x16x64_bf16 v[12:15], v[36:39], v[88:95], v0\n" \
  "v_add_f32 v20, v20, v22\n" \
  "v_smfmac_f32_16x16x64_bf16 v[12:15], v[40:43], v[96:103], v0\n" \
  "v_add_f32 v21, v21, v23\n" \
  "v_smfmac_f32_16x16x64_bf16 v[12:15], v[44:47], v[104:111], v0\n" \
  "v_cvt_pk_bf16_f32 v20, v20, v21\n" \
  "v_smfmac_f32_16x16x64_bf16 v[12:15], v[48:51], v[112:119], v0\n" \
  "ds_write_b16 v4, v20 offset:0\n" \
  "v_smfmac_f32_16x16x64_bf16 v[12:15], v[52:55], v[120:127], v0\n" \
  "ds_write_b16_d16_hi v4, v20 offset:288\n" \
  "v_smfmac_f32_16x16x64_bf16 v[16:19], v[24:27], v[128:135], v0\n" \
  "s_nop 1\n" \
  "v_smfmac_f32_16x16x64_bf16 v[16:19], v[28:31], v[136:143], v0\n" \
  "s_nop 1\n" \
  "v_smfmac_f32_16x16x64_bf16 v[16:19], v[32:35], v[144:151], v0\n" \
  "v_add_f32 v12, v12, v14\n" \
  "v_smfmac_f32_16x16x64_bf16 v[16:19], v[36:39], v[152:159], v0\n" \
  "v_add_f32 v13, v13, v15\n" \
  "v_smfmac_f32_16x16x64_bf16 v[16:19], v[40:43], v[160:167], v0\n" \
  "v_cvt_pk_bf16_f32 v12, v12, v13\n" \
  "v_smfmac_f32_16x16x64_bf16 v[16:19], v[44:47], v[168:175], v0\n" \
  "ds_write_b16 v4, v12 offset:128\n" \
  "v_smfmac_f32_16x16x64_bf16 v[16:19], v[48:51], v[176:183], v0\n" \
  "ds_write_b16_d16_hi v4, v12 offset:416\n" \
  "v_smfmac_f32_16x16x64_bf16 v[16:19], v[52:55], v[184:191], v0\n" \
  "s_barrier\n" \
  "s_nop 6\n" \
  "v_add_f32 v16, v16, v18\n" \
  "v_add_f32 v17, v17, v19\n" \
  "v_cvt_pk_bf16_f32 v16, v16, v17\n" \
  "ds_write_b16 v4, v16 offset:2560\n" \
  "ds_write_b16_d16_hi v4, v16 offset:2848\n" \
  "s_waitcnt lgkmcnt(0)\n" \
  "s_barrier\n" \
  "s_setprio 0\n" \
  "ds_read_b128 v[60:63], v6 offset:0\n" \
  "v_readfirstlane_b32 s86, v11\n" \
  "s_add_u32 s88, s74, 157760\n" \
  "v_mov_b32 v8, s88\n" \
  "v_mov_b32 v9, 0\n" \
  "ds_write_b32 v8, v9\n" \
  "s_lshr_b32 s86, s86, 23\n" \
  "s_add_i32 s86, s86, s83\n" \
  "s_ashr_i32 s86, s86, 1\n" \
  "s_max_i32 s86, s86, s82\n" \
  "s_min_i32 s86, s86, 100\n" \
  "s_lshl_b32 s86, s86, 23\n" \
  "s_cmp_eq_u32 s71, 0\n" \
  "s_waitcnt lgkmcnt(1)\n" \
  "v_max_u32 v60, 1, v60\n" \
  "v_max_u32 v61, 1, v61\n" \
  "v_max_u32 v62, 1, v62\n" \
  "v_max_u32 v63, 1, v63\n" \
  "buffer_store_dwordx4 v[60:63], v7, s[48:51], s69 offen sc1\n" \
  "s_cbranch_scc1 .LCV_LAST_%=\n" \
  "s_waitcnt vmcnt(1)\n" \
  "v_cmp_eq_u32 vcc, 0, v5\n" \
  "s_cmp_eq_u64 vcc, 0\n" \
  "s_cselect_b32 s90, 1, 0\n" \
  "s_barrier\n" \
  "ds_read_b128 v[24:27], v3 offset:0\n" \
  "ds_read_b128 v[28:31], v3 offset:1024\n" \
  "ds_read_b128 v[32:35], v3 offset:2048\n" \
  "ds_read_b128 v[36:39], v3 offset:3072\n" \
  "ds_read_b128 v[40:43], v3 offset:8192\n" \
  "ds_read_b128 v[44:47], v3 offset:9216\n" \
  "ds_read_b128 v[48:51], v3 offset:10240\n" \
  "ds_read_b128 v[52:55], v3 offset:11264\n" \
  "ds_read_b128 v[56:59], v3 offset:4096\n" \
  "ds_read_b128 v[60:63], v3 offset:5120\n" \
  "s_cbranch_scc0 .LCV_NOTOK_%=\n" \
  "s_mov_b32 m0, s99\n" \
  "s_add_u32 s88, s99, 8192\n" \
  "global_load_lds_dwordx4 v10, s[62:63] sc1\n" \
  "global_load_lds_dwordx4 v10, s[62:63] offset:1024 sc1\n" \
  "s_mov_b32 m0, s88\n" \
  "s_nop 0\n" \
  "global_load_lds_dwordx4 v10, s[64:65] sc1\n" \
  "global_load_lds_dwordx4 v10, s[64:65] offset:1024 sc1\n" \
  ".LCV_NOTOK_%=:\n" \
  "v_mov_b64 v[12:13], s[84:85]\n" \
  "v_mov_b64 v[14:15], s[84:85]\n" \
  "v_mov_b64 v[16:17], s[84:85]\n" \
  "v_mov_b64 v[18:19], s[84:85]\n" \
  "v_mov_b64 v[20:21], s[84:85]\n" \
  "v_mov_b64 v[22:23], s[84:85]\n" \
  "s_waitcnt lgkmcnt(0)\n" \
  "s_barrier\n" \
  "ds_read_b128 v[4:7], v1 offset:8192\n" \
  "ds_read_b128 v[8:11], v1 offset:9216\n" \
  "s_waitcnt lgkmcnt(1)\n" \
  "v_smfmac_f32_16x16x64_bf16 v[12:15], v[4:7], v[24:31], v0\n" \
  "s_waitcnt lgkmcnt(0)\n" \
  "v_smfmac_f32_16x16x64_bf16 v[12:15], v[8:11], v[40:47], v0\n" \
  "ds_read_b128 v[24:27], v3 offset:12288\n" \
  "ds_read_b128 v[28:31], v3 offset:13312\n" \
  "v_smfmac_f32_16x16x64_bf16 v[16:19], v[4:7], v[32:39], v0\n" \
  "ds_read_b128 v[32:35], v3 offset:6144\n" \
  "ds_read_b128 v[36:39], v3 offset:7168\n" \
  "v_smfmac_f32_16x16x64_bf16 v[16:19], v[8:11], v[48:55], v0\n" \
  "s_cmp_eq_u32 s90, 1\n" \
  "s_cbranch_scc1 .LCV_NMT_%=\n" \
  "buffer_load_dword v55, off, s[44:47], s68 sc1\n" \
  ".LCV_NMT_%=:\n" \
  "v_add_f32 v12, v12, v14\n" \
  "v_add_f32 v13, v13, v15\n" \
  "global_atomic_add_x2 v2, v[12:13], s[26:27]\n" \
  "v_smfmac_f32_16x16x64_bf16 v[20:23], v[4:7], v[56:63], v0\n" \
  "ds_read_b128 v[56:59], v3 offset:14336\n" \
  "ds_read_b128 v[60:63], v3 offset:15360\n" \
  "v_add_f32 v16, v16, v18\n" \
  "v_add_f32 v17, v17, v19\n" \
  "global_atomic_add_x2 v2, v[16:17], s[28:29]\n" \
  "v_mov_b64 v[40:41], s[84:85]\n" \
  "v_mov_b64 v[42:43], s[84:85]\n" \
  "s_waitcnt lgkmcnt(4)\n" \
  "v_smfmac_f32_16x16x64_bf16 v[20:23], v[8:11], v[24:31], v0\n" \
  "s_waitcnt lgkmcnt(2)\n" \
  "v_smfmac_f32_16x16x64_bf16 v[40:43], v[4:7], v[32:39], v0\n" \
  "s_waitcnt lgkmcnt(0)\n" \
  "v_smfmac_f32_16x16x64_bf16 v[40:43], v[8:11], v[56:63], v0\n" \
  "s_nop 3\n" \
  "v_add_f32 v20, v20, v22\n" \
  "v_add_f32 v21, v21, v23\n" \
  "global_atomic_add_x2 v2, v[20:21], s[30:31]\n" \
  "s_nop 0\n" \
  "v_add_f32 v40, v40, v42\n" \
  "v_add_f32 v41, v41, v43\n" \
  "global_atomic_add_x2 v2, v[40:41], s[32:33]\n" \
  "v_mbcnt_lo_u32_b32 v48, -1, 0\n" \
  "v_mbcnt_hi_u32_b32 v48, -1, v48\n" \
  "s_add_u32 s20, s20, 32\n" \
  "s_cmp_eq_u32 s90, 1\n" \
  "s_cbranch_scc1 .LCV_HAVE_%=\n" \
  "s_mov_b32 s87, 0x40000\n" \
  "v_lshlrev_b32 v10, 4, v48\n" \
  "s_waitcnt vmcnt(4)\n" \
  "v_cmp_eq_u32 vcc, 0, v55\n" \
  "s_cmp_eq_u64 vcc, 0\n" \
  "s_cbranch_scc1 .LCV_GOT_%=\n" \
  ".LCV_LATE_%=:\n" \
  "buffer_load_dword v58, off, s[44:47], s68 sc1\n" \
  "s_sub_u32 s87, s87, 1\n" \
  "s_cbranch_scc1 .LCV_END_%=\n" \
  "s_waitcnt vmcnt(0)\n" \
  "v_cmp_eq_u32 vcc, 0, v58\n" \
  "s_cmp_eq_u64 vcc, 0\n" \
  "s_cbranch_scc1 .LCV_GOT_%=\n" \
  "s_sleep 12\n" \
  "s_branch .LCV_LATE_%=\n" \
  ".LCV_GOT_%=:\n" \
  "s_mov_b32 m0, s99\n" \
  "s_add_u32 s88, s99, 8192\n" \
  "global_load_lds_dwordx4 v10, s[62:63] sc1\n" \
  "global_load_lds_dwordx4 v10, s[62:63] offset:1024 sc1\n" \
  "s_mov_b32 m0, s88\n" \
  "s_nop 0\n" \
  "global_load_lds_dwordx4 v10, s[64:65] sc1\n" \
  "global_load_lds_dwordx4 v10, s[64:65] offset:1024 sc1\n" \
  ".LCV_HAVE_%=:\n" \
  "v_lshrrev_b32 v49, 3, v48\n" \
  "v_and_b32 v50, 7, v48\n" \
  "v_lshlrev_b32 v50, 4, v50\n" \
  "v_lshl_or_b32 v44, v49, 8, v50\n" \
  "s_mov_b32 s21, 0x8000\n" \
  "v_mov_b32 v52, s22\n" \
  "v_mov_b32 v54, s20\n" \
  "s_branch .LCV_TKF_%=\n" \
  ".LCV_TKE_%=:\n" \
  "s_sleep 1\n" \
  ".LCV_TKF_%=:\n" \
  "ds_read_b64 v[54:55], v52\n" \
  "s_sub_u32 s21, s21, 1\n" \
  "s_cbranch_scc1 .LCV_TKO_%=\n" \
  "s_waitcnt lgkmcnt(0)\n" \
  "v_readfirstlane_b32 s38, v54\n" \
  "v_readfirstlane_b32 s34, v55\n" \
  "s_cmp_eq_u32 s38, s20\n" \
  "s_cbranch_scc0 .LCV_TKE_%=\n" \
  ".LCV_TKO_%=:\n" \
  "s_branch .LCV_WAIU_%=\n" \
  ".LCV_WAIS_%=:\n" \
  "s_cmp_lt_i32 s39, -1000\n" \
  "s_cbranch_scc1 .LCV_WAIE_%=\n" \
  "s_sleep 1\n" \
  ".LCV_WAIU_%=:\n" \
  "s_memrealtime s[38:39]\n" \
  "s_waitcnt lgkmcnt(0)\n" \
  "s_sub_i32 s39, s38, s34\n" \
  "s_cmp_lt_i32 s39, -7\n" \
  "s_cbranch_scc1 .LCV_WAIS_%=\n" \
  "s_cmp_lt_i32 s39, 0\n" \
  "s_cbranch_scc1 .LCV_WAIU_%=\n" \
  ".LCV_WAIE_%=:\n" \
  "buffer_load_dwordx4 v[24:27], v44, s[40:43], s60 offen sc1\n" \
  "buffer_load_dwordx4 v[28:31], v44, s[40:43], s60 offen offset:2048 sc1\n" \
  "v_mbcnt_lo_u32_b32 v48, -1, 0\n" \
  "v_mbcnt_hi_u32_b32 v48, -1, v48\n" \
  "s_sub_u32 s88, s73, 4\n" \
  "v_and_b32 v51, 15, v48\n" \
  "v_lshl_add_u32 v4, v51, 1, s97\n" \
  "v_lshrrev_b32 v49, 4, v48\n" \
  "v_mad_u32_u24 v4, v49, s94, v4\n" \
  "v_lshrrev_b32 v49, 2, v51\n" \
  "v_and_b32 v50, 5, v51\n" \
  "v_and_or_b32 v49, v49, 2, v50\n" \
  "s_lshl_b32 s89, s88, 6\n" \
  "s_add_u32 s89, s89, s74\n" \
  "s_add_u32 s89, s89, 161152\n" \
  "v_and_b32 v50, 48, v48\n" \
  "v_add_u32 v6, s89, v50\n" \
  "v_mad_u32_u24 v6, v49, s95, v6\n" \
  "v_lshlrev_b32 v7, 3, v50\n" \
  "v_lshl_add_u32 v7, v49, 4, v7\n" \
  "v_and_b32 v49, 2, v51\n" \
  "v_lshl_add_u32 v7, v49, 29, v7\n" \
  "v_lshlrev_b32 v10, 4, v48\n" \
  "v_mov_b64 v[12:13], 0\n" \
  "v_mov_b64 v[14:15], 0\n" \
  "v_mov_b64 v[16:17], 0\n" \
  "v_mov_b64 v[18:19], 0\n" \
  "v_mov_b64 v[20:21], 0\n" \
  "v_mov_b64 v[22:23], 0\n" \
  "v_and_b32 v49, 3, v48\n" \
  "v_lshl_add_u32 v40, v49, 2, s98\n" \
  "v_lshrrev_b32 v49, 2, v48\n" \
  "v_bfe_u32 v48, v48, 4, 1\n" \
  "v_xor_b32 v49, v49, v48\n" \
  "v_lshl_add_u32 v40, v49, 5, v40\n" \
  "s_add_u32 s62, s62, 0x80000\n" \
  "s_addc_u32 s63, s63, 0\n" \
  "s_add_u32 s64, s64, 0x40000\n" \
  "s_addc_u32 s65, s65, 0\n" \
  "s_add_u32 s68, s68, 0x1000\n" \
  "s_add_u32 s69, s69, 0x10000\n" \
  "s_add_i32 s80, s86, 0x35000000\n" \
  "s_sub_u32 s91, 0x4a350000, s86\n" \
  "s_min_u32 s91, s91, 0x4ca00000\n" \
  "s_mov_b32 s89, s60\n" \
  "s_xor_b32 s60, s60, 0x80\n" \
  "s_bitcmp0_b32 s60, 7\n" \
  "s_cselect_b32 s88, 0x8000, 0\n" \
  "s_add_u32 s60, s60, s88\n" \
  "s_and_b32 s60, s60, 0xffff\n" \
  "s_add_u32 s58, s56, s60\n" \
  "s_addc_u32 s59, s57, 0\n" \
  "s_xor_b32 s38, s35, 0\n" \
  "s_add_u32 s26, s58, s38\n" \
  "s_addc_u32 s27, s59, 0\n" \
  "s_xor_b32 s38, s35, 1024\n" \
  "s_add_u32 s28, s58, s38\n" \
  "s_addc_u32 s29, s59, 0\n" \
  "s_xor_b32 s38, s35, 2048\n" \
  "s_add_u32 s30, s58, s38\n" \
  "s_addc_u32 s31, s59, 0\n" \
  "s_xor_b32 s38, s35, 3072\n" \
  "s_add_u32 s32, s58, s38\n" \
  "s_addc_u32 s33, s59, 0\n" \
  "s_sub_u32 s71, s71, 1\n" \
  "s_mov_b32 s87, 0x40000\n" \
  "s_branch .LCV_CHK_%=\n" \
  ".LCV_POLL_%=:\n" \
  "buffer_load_dwordx4 v[24:27], v44, s[40:43], s89 offen sc1\n" \
  "buffer_load_dwordx4 v[28:31], v44, s[40:43], s89 offen offset:2048 sc1\n" \
  ".LCV_CHK_%=:\n" \
  "s_sub_u32 s87, s87, 1\n" \
  "s_cbranch_scc1 .LCV_END_%=\n" \
  "s_waitcnt vmcnt(1)\n" \
  "v_cvt_f32_i32 v32, v24\n" \
  "v_cvt_f32_i32 v33, v25\n" \
  "v_cvt_f32_i32 v34, v26\n" \
  "v_cvt_f32_i32 v35, v27\n" \
  "v_pk_mul_f32 v[24:25], v[32:33], s[80:81] op_sel_hi:[1,0]\n" \
  "v_pk_mul_f32 v[26:27], v[34:35], s[80:81] op_sel_hi:[1,0]\n" \
  "v_max3_f32 v45, |v32|, |v33|, |v34|\n" \
  "v_cvt_pk_bf16_f32 v24, v24, v26\n" \
  "v_cvt_pk_bf16_f32 v25, v25, v27\n" \
  "ds_write2_b32 v40, v24, v25 offset0:0 offset1:4\n" \
  "v_max3_f32 v45, |v45|, |v35|, |v35|\n" \
  "s_waitcnt vmcnt(0)\n" \
  "v_cvt_f32_i32 v32, v28\n" \
  "v_cvt_f32_i32 v33, v29\n" \
  "v_cvt_f32_i32 v34, v30\n" \
  "v_cvt_f32_i32 v35, v31\n" \
  "v_pk_mul_f32 v[28:29], v[32:33], s[80:81] op_sel_hi:[1,0]\n" \
  "v_pk_mul_f32 v[30:31], v[34:35], s[80:81] op_sel_hi:[1,0]\n" \
  "v_max3_f32 v45, |v45|, |v32|, |v33|\n" \
  "v_cvt_pk_bf16_f32 v28, v28, v30\n" \
  "v_cvt_pk_bf16_f32 v29, v29, v31\n" \
  "ds_write2_b32 v40, v28, v29 offset0:128 offset1:132\n" \
  "v_max3_f32 v45, |v45|, |v34|, |v35|\n" \
  "v_cmp_lt_f32 vcc, s91, v45\n" \
  "s_cbranch_vccnz .LCV_BIG_%=\n" \
  ".LCV_SML_%=:\n" \
  "s_branch .LCV_TOP_%=\n" \
  ".LCV_BIG_%=:\n" \
  "v_cmp_lt_f32 vcc, 0x4ca00000, v45\n" \
  "s_cbranch_vccnz .LCV_POLL_%=\n" \
  "v_mul_f32 v45, s80, v45\n" \
  "v_mul_f32 v45, v45, v45\n" \
  "s_add_u32 s88, s74, 157760\n" \
  "v_mov_b32 v44, s88\n" \
  "s_nop 0\n" \
  "v_cmpx_lt_f32 vcc, 2.0, v45\n" \
  "ds_max_u32 v44, v45\n" \
  "s_mov_b64 exec, -1\n" \
  "s_branch .LCV_SML_%=\n" \
  ".LCV_LAST_%=:\n" \
  "s_waitcnt vmcnt(0)\n" \
  ".LCV_END_%=:\n" \
  "s_endpgm\n"
#define QKV_ASM_0 \
  "s_mov_b32 s69, 0\n" \
  "s_branch .LQA_POLL_%=\n" \
  ".p2align 6\n" \
  ".LQA_AL_%=:\n" \
  ".LQA_TOP_%=:\n" \
  "s_barrier\n" \
  "ds_read_b128 v[36:39], v7 offset:0\n" \
  "ds_read_b128 v[40:43], v7 offset:2048\n" \
  "ds_read_b128 v[44:47], v7 offset:4096\n" \
  "s_setprio 2\n" \
  "s_waitcnt lgkmcnt(2)\n" \
  "v_mfma_f32_16x16x32_bf16 v[12:15], v[64:67], v[36:39], 0\n" \
  "ds_read_b128 v[48:51], v7 offset:6144\n" \
  "v_mfma_f32_16x16x32_bf16 v[16:19], v[128:131], v[36:39], 0\n" \
  "v_mfma_f32_16x16x32_bf16 v[20:23], v[36:39], v[192:195], 0\n" \
  "s_cmp_lg_u32 s69, 1\n" \
  "s_cbranch_scc1 .LQA_FDN_%=\n" \
  "s_waitcnt vmcnt(1)\n" \
  "v_mov_b32 v52, s80\n" \
  "v_mov_b32 v53, 0x10000\n" \
  "s_mov_b64 exec, 1\n" \
  "ds_add_rtn_u32 v54, v52, v53\n" \
  "s_mov_b64 exec, -1\n" \
  "s_add_u32 s67, s67, 4\n" \
  "s_add_u32 s64, s64, 4096\n" \
  "s_addc_u32 s65, s65, 0\n" \
  "v_mov_b32 v55, 0\n" \
  "s_waitcnt lgkmcnt(0)\n" \
  "v_readfirstlane_b32 s74, v54\n" \
  "s_and_b32 s76, s74, 0x30000\n" \
  "s_cmp_lg_u32 s76, 0x30000\n" \
  "s_cbranch_scc1 .LQA_FDN_%=\n" \
  "s_mov_b32 s79, 0x100000\n" \
  "s_branch .LQA_FDT_%=\n" \
  ".LQA_FD_%=:\n" \
  "ds_read_b32 v54, v52\n" \
  "s_sub_u32 s79, s79, 1\n" \
  "s_cbranch_scc1 .LQA_FDE_%=\n" \
  "s_waitcnt lgkmcnt(0)\n" \
  "v_readfirstlane_b32 s74, v54\n" \
  ".LQA_FDT_%=:\n" \
  "s_and_b32 s74, s74, 0xffff\n" \
  "s_cmp_lt_u32 s74, s67\n" \
  "s_cbranch_scc1 .LQA_FD_%=\n" \
  ".LQA_FDE_%=:\n" \
  "s_mov_b64 exec, 1\n" \
  "global_store_dword v55, v53, s[64:65] offset:-4096 sc1\n" \
  "s_mov_b64 exec, -1\n" \
  "s_waitcnt vmcnt(1)\n" \
  "s_branch .LQA_LOOK_%=\n" \
  ".LQA_FDN_%=:\n" \
  "s_waitcnt vmcnt(0)\n" \
  ".LQA_LOOK_%=:\n" \
  "v_cmp_eq_u32 vcc, 0, v11\n" \
  "s_mov_b32 m0, s58\n" \
  "s_add_u32 s74, s56, 4096\n" \
  "s_addc_u32 s75, s57, 0\n" \
  "s_add_u32 s76, s58, 4096\n" \
  "s_cmp_eq_u64 vcc, 0\n" \
  "s_cselect_b32 s69, 1, 0\n" \
  "s_waitcnt lgkmcnt(2)\n" \
  "v_mfma_f32_16x16x32_bf16 v[12:15], v[68:71], v[40:43], v[12:15]\n" \
  "ds_read_b128 v[36:39], v7 offset:8192\n" \
  "s_cbranch_scc0 2\n" \
  "global_load_lds_dwordx4 v6, s[56:57] offset:0 sc1\n" \
  "v_mfma_f32_16x16x32_bf16 v[16:19], v[132:135], v[40:43], v[16:19]\n" \
  "s_cbranch_scc0 2\n" \
  "global_load_lds_dwordx4 v6, s[56:57] offset:1024 sc1\n" \
  "v_mfma_f32_16x16x32_bf16 v[20:23], v[40:43], v[196:199], v[20:23]\n" \
  "s_cbranch_scc0 2\n" \
  "global_load_lds_dwordx4 v6, s[56:57] offset:2048 sc1\n" \
  "s_waitcnt lgkmcnt(2)\n" \
  "v_mfma_f32_16x16x32_bf16 v[12:15], v[72:75], v[44:47], v[12:15]\n" \
  "ds_read_b128 v[40:43], v7 offset:10240\n" \
  "s_cbranch_scc0 2\n" \
  "global_load_lds_dwordx4 v6, s[56:57] offset:3072 sc1\n" \
  "v_mfma_f32_16x16x32_bf16 v[16:19], v[136:139], v[44:47], v[16:19]\n" \
  "s_mov_b32 m0, s76\n" \
  "s_cbranch_scc0 2\n" \
  "global_load_lds_dwordx4 v6, s[74:75] offset:0 sc1\n" \
  "v_mfma_f32_16x16x32_bf16 v[20:23], v[44:47], v[200:203], v[20:23]\n" \
  "s_cbranch_scc0 2\n" \
  "global_load_lds_dwordx4 v6, s[74:75] offset:1024 sc1\n" \
  "s_waitcnt lgkmcnt(2)\n" \
  "v_mfma_f32_16x16x32_bf16 v[12:15], v[76:79], v[48:51], v[12:15]\n" \
  "ds_read_b128 v[44:47], v7 offset:12288\n" \
  "s_cbranch_scc0 2\n" \
  "global_load_lds_dwordx4 v6, s[74:75] offset:2048 sc1\n" \
  "v_mfma_f32_16x16x32_bf16 v[16:19], v[140:143], v[48:51], v[16:19]\n" \
  "s_cbranch_scc0 2\n" \
  "global_load_lds_dwordx4 v6, s[74:75] offset:3072 sc1\n" \
  "v_mfma_f32_16x16x32_bf16 v[20:23], v[48:51], v[204:207], v[20:23]\n" \
  "s_cselect_b32 s76, 0x40000, s62\n" \
  "s_add_u32 s74, s59, 4096\n" \
  "s_waitcnt lgkmcnt(2)\n" \
  "v_mfma_f32_16x16x32_bf16 v[12:15], v[80:83], v[36:39], v[12:15]\n" \
  "ds_read_b128 v[48:51], v7 offset:14336\n" \
  "v_mov_b64 v[24:25], 0\n" \
  "v_lshrrev_b32 v26, 1, v6\n" \
  "buffer_store_dwordx2 v[24:25], v26, s[48:51], s61 offen sc1\n" \
  "v_mfma_f32_16x16x32_bf16 v[16:19], v[144:147], v[36:39], v[16:19]\n" \
  "v_mfma_f32_16x16x32_bf16 v[20:23], v[36:39], v[208:211], v[20:23]\n" \
  "s_waitcnt lgkmcnt(2)\n" \
  "v_mfma_f32_16x16x32_bf16 v[12:15], v[84:87], v[40:43], v[12:15]\n" \
  "ds_read_b128 v[36:39], v7 offset:16384\n" \
  "v_mfma_f32_16x16x32_bf16 v[16:19], v[148:151], v[40:43], v[16:19]\n" \
  "v_mfma_f32_16x16x32_bf16 v[20:23], v[40:43], v[212:215], v[20:23]\n" \
  "s_waitcnt lgkmcnt(2)\n" \
  "v_mfma_f32_16x16x32_bf16 v[12:15], v[88:91], v[44:47], v[12:15]\n" \
  "ds_read_b128 v[40:43], v7 offset:18432\n" \
  "v_mfma_f32_16x16x32_bf16 v[16:19], v[152:155], v[44:47], v[16:19]\n" \
  "v_mfma_f32_16x16x32_bf16 v[20:23], v[44:47], v[216:219], v[20:23]\n" \
  "s_waitcnt lgkmcnt(2)\n" \
  "v_mfma_f32_16x16x32_bf16 v[12:15], v[92:95], v[48:51], v[12:15]\n" \
  "ds_read_b128 v[44:47], v7 offset:20480\n" \
  "v_mfma_f32_16x16x32_bf16 v[16:19], v[156:159], v[48:51], v[16:19]\n" \
  "v_mfma_f32_16x16x32_bf16 v[20:23], v[48:51], v[220:223], v[20:23]\n" \
  "s_waitcnt lgkmcnt(2)\n" \
  "v_mfma_f32_16x16x32_bf16 v[12:15], v[96:99], v[36:39], v[12:15]\n" \
  "ds_read_b128 v[48:51], v7 offset:22528\n" \
  "v_mfma_f32_16x16x32_bf16 v[16:19], v[160:163], v[36:39], v[16:19]\n" \
  "v_mfma_f32_16x16x32_bf16 v[20:23], v[36:39], v[224:227], v[20:23]\n" \
  "s_waitcnt lgkmcnt(2)\n" \
  "v_mfma_f32_16x16x32_bf16 v[12:15], v[100:103], v[40:43], v[12:15]\n" \
  "ds_read_b128 v[36:39], v7 offset:24576\n" \
  "v_mfma_f32_16x16x32_bf16 v[16:19], v[164:167], v[40:43], v[16:19]\n" \
  "v_mfma_f32_16x16x32_bf16 v[20:23], v[40:43], v[228:231], v[20:23]\n" \
  "s_waitcnt lgkmcnt(2)\n" \
  "v_mfma_f32_16x16x32_bf16 v[12:15], v[104:107], v[44:47], v[12:15]\n" \
  "ds_read_b128 v[40:43], v7 offset:26624\n" \
  "v_mfma_f32_16x16x32_bf16 v[16:19], v[168:171], v[44:47], v[16:19]\n" \
  "v_mfma_f32_16x16x32_bf16 v[20:23], v[44:47], v[232:235], v[20:23]\n" \
  "s_waitcnt lgkmcnt(2)\n" \
  "v_mfma_f32_16x16x32_bf16 v[12:15], v[108:111], v[48:51], v[12:15]\n" \
  "ds_read_b128 v[44:47], v7 offset:28672\n" \
  "v_mfma_f32_16x16x32_bf16 v[16:19], v[172:175], v[48:51], v[16:19]\n" \
  "v_mfma_f32_16x16x32_bf16 v[20:23], v[48:51], v[236:239], v[20:23]\n" \
  "s_waitcnt lgkmcnt(2)\n" \
  "v_mfma_f32_16x16x32_bf16 v[12:15], v[112:115], v[36:39], v[12:15]\n" \
  "ds_read_b128 v[48:51], v7 offset:30720\n" \
  "v_mfma_f32_16x16x32_bf16 v[16:19], v[176:179], v[36:39], v[16:19]\n" \
  "v_mfma_f32_16x16x32_bf16 v[20:23], v[36:39], v[240:243], v[20:23]\n" \
  "s_waitcnt lgkmcnt(2)\n" \
  "v_mfma_f32_16x16x32_bf16 v[12:15], v[116:119], v[40:43], v[12:15]\n" \
  "ds_read_b128 v[36:39], v7 offset:256\n" \
  "v_mfma_f32_16x16x32_bf16 v[16:19], v[180:183], v[40:43], v[16:19]\n" \
  "v_mfma_f32_16x16x32_bf16 v[20:23], v[40:43], v[244:247], v[20:23]\n" \
  "s_waitcnt lgkmcnt(2)\n" \
  "v_mfma_f32_16x16x32_bf16 v[12:15], v[120:123], v[44:47], v[12:15]\n" \
  "ds_read_b128 v[40:43], v7 offset:2304\n" \
  "v_mfma_f32_16x16x32_bf16 v[16:19], v[184:187], v[44:47], v[16:19]\n" \
  "v_mfma_f32_16x16x32_bf16 v[20:23], v[44:47], v[248:251], v[20:23]\n" \
  "s_waitcnt lgkmcnt(2)\n" \
  "v_mfma_f32_16x16x32_bf16 v[12:15], v[124:127], v[48:51], v[12:15]\n" \
  "ds_read_b128 v[44:47], v7 offset:4352\n" \
  "v_mfma_f32_16x16x32_bf16 v[16:19], v[188:191], v[48:51], v[16:19]\n" \
  "v_mfma_f32_16x16x32_bf16 v[20:23], v[48:51], v[252:255], v[20:23]\n" \
  "s_barrier\n" \
  "s_waitcnt lgkmcnt(2)\n" \
  "v_mfma_f32_16x16x32_bf16 v[24:27], v[64:67], v[36:39], 0\n" \
  "ds_read_b128 v[48:51], v7 offset:6400\n" \
  "v_mfma_f32_16x16x32_bf16 v[28:31], v[128:131], v[36:39], 0\n" \
  "ds_read2_b32 v[52:53], v8 offset0:0 offset1:0\n" \
  "v_mfma_f32_16x16x32_bf16 v[32:35], v[36:39], v[192:195], 0\n" \
  "s_waitcnt lgkmcnt(3)\n" \
  "v_mfma_f32_16x16x32_bf16 v[24:27], v[68:71], v[40:43], v[24:27]\n" \
  "ds_read_b128 v[36:39], v7 offset:8448\n" \
  "v_mfma_f32_16x16x32_bf16 v[28:31], v[132:135], v[40:43], v[28:31]\n" \
  "ds_read2_b32 v[54:55], v8 offset0:16 offset1:16\n" \
  "v_mfma_f32_16x16x32_bf16 v[32:35], v[40:43], v[196:199], v[32:35]\n" \
  "s_waitcnt lgkmcnt(4)\n" \
  "v_mfma_f32_16x16x32_bf16 v[24:27], v[72:75], v[44:47], v[24:27]\n" \
  "ds_read_b128 v[40:43], v7 offset:10496\n" \
  "v_mfma_f32_16x16x32_bf16 v[28:31], v[136:139], v[44:47], v[28:31]\n" \
  "ds_read2_b32 v[56:57], v8 offset0:32 offset1:48\n" \
  "v_mfma_f32_16x16x32_bf16 v[32:35], v[44:47], v[200:203], v[32:35]\n" \
  "s_waitcnt lgkmcnt(5)\n" \
  "v_mfma_f32_16x16x32_bf16 v[24:27], v[76:79], v[48:51], v[24:27]\n" \
  "ds_read_b128 v[44:47], v7 offset:12544\n" \
  "v_mfma_f32_16x16x32_bf16 v[28:31], v[140:143], v[48:51], v[28:31]\n" \
  "ds_read_b128 v[58:61], v9 offset:128\n" \
  "v_mfma_f32_16x16x32_bf16 v[32:35], v[48:51], v[204:207], v[32:35]\n" \
  "s_waitcnt lgkmcnt(6)\n" \
  "v_mfma_f32_16x16x16_bf16 v[12:15], v[0:1], v[52:53], v[12:15]\n" \
  "v_mfma_f32_16x16x16_bf16 v[16:19], v[2:3], v[52:53], v[16:19]\n" \
  "v_mfma_f32_16x16x16_bf16 v[20:23], v[52:53], v[4:5], v[20:23]\n" \
  "s_waitcnt lgkmcnt(4)\n" \
  "v_mfma_f32_16x16x16_bf16 v[24:27], v[0:1], v[54:55], v[24:27]\n" \
  "v_mfma_f32_16x16x16_bf16 v[28:31], v[2:3], v[54:55], v[28:31]\n" \
  "v_mfma_f32_16x16x16_bf16 v[32:35], v[54:55], v[4:5], v[32:35]\n" \
  "v_mfma_f32_16x16x32_bf16 v[24:27], v[80:83], v[36:39], v[24:27]\n" \
  "ds_read_b128 v[48:51], v7 offset:14592\n" \
  "v_mfma_f32_16x16x32_bf16 v[28:31], v[144:147], v[36:39], v[28:31]\n" \
  "ds_read_b128 v[52:55], v9 offset:192\n" \
  "v_mfma_f32_16x16x32_bf16 v[32:35], v[36:39], v[208:211], v[32:35]\n" \
  "s_waitcnt lgkmcnt(5)\n" \
  "v_mfma_f32_16x16x32_bf16 v[24:27], v[84:87], v[40:43], v[24:27]\n" \
  "ds_read_b128 v[36:39], v7 offset:16640\n" \
  "v_mfma_f32_16x16x32_bf16 v[28:31], v[148:151], v[40:43], v[28:31]\n" \
  "v_mfma_f32_16x16x32_bf16 v[32:35], v[40:43], v[212:215], v[32:35]\n" \
  "s_waitcnt lgkmcnt(4)\n" \
  "v_mfma_f32_16x16x32_bf16 v[24:27], v[88:91], v[44:47], v[24:27]\n" \
  "ds_read_b128 v[40:43], v7 offset:18688\n" \
  "v_mfma_f32_16x16x32_bf16 v[28:31], v[152:155], v[44:47], v[28:31]\n" \
  "v_mfma_f32_16x16x32_bf16 v[32:35], v[44:47], v[216:219], v[32:35]\n" \
  "s_waitcnt lgkmcnt(3)\n" \
  "v_mfma_f32_16x16x32_bf16 v[24:27], v[92:95], v[48:51], v[24:27]\n" \
  "ds_read_b128 v[44:47], v7 offset:20736\n" \
  "v_mfma_f32_16x16x32_bf16 v[28:31], v[156:159], v[48:51], v[28:31]\n" \
  "v_mfma_f32_16x16x32_bf16 v[32:35], v[48:51], v[220:223], v[32:35]\n" \
  "s_waitcnt lgkmcnt(2)\n" \
  "v_mfma_f32_16x16x32_bf16 v[24:27], v[96:99], v[36:39], v[24:27]\n" \
  "ds_read_b128 v[48:51], v7 offset:22784\n" \
  "v_mfma_f32_16x16x32_bf16 v[28:31], v[160:163], v[36:39], v[28:31]\n" \
  "v_mfma_f32_16x16x32_bf16 v[32:35], v[36:39], v[224:227], v[32:35]\n" \
  "s_waitcnt lgkmcnt(2)\n" \
  "v_mfma_f32_16x16x32_bf16 v[24:27], v[100:103], v[40:43], v[24:27]\n" \
  "ds_read_b128 v[36:39], v7 offset:24832\n" \
  "v_mfma_f32_16x16x32_bf16 v[28:31], v[164:167], v[40:43], v[28:31]\n" \
  "buffer_load_dword v11, v10, s[52:55], s76 offen sc1\n" \
  "v_mfma_f32_16x16x32_bf16 v[32:35], v[40:43], v[228:231], v[32:35]\n" \
  "s_waitcnt lgkmcnt(2)\n" \
  "v_mfma_f32_16x16x32_bf16 v[24:27], v[104:107], v[44:47], v[24:27]\n" \
  "ds_read_b128 v[40:43], v7 offset:26880\n" \
  "v_mfma_f32_16x16x32_bf16 v[28:31], v[168:171], v[44:47], v[28:31]\n" \
  "v_mfma_f32_16x16x32_bf16 v[32:35], v[44:47], v[232:235], v[32:35]\n" \
  "s_waitcnt lgkmcnt(2)\n" \
  "v_mfma_f32_16x16x32_bf16 v[24:27], v[108:111], v[48:51], v[24:27]\n" \
  "ds_read_b128 v[44:47], v7 offset:28928\n" \
  "v_mfma_f32_16x16x32_bf16 v[28:31], v[172:175], v[48:51], v[28:31]\n" \
  "v_mfma_f32_16x16x32_bf16 v[32:35], v[48:51], v[236:239], v[32:35]\n" \
  "s_waitcnt lgkmcnt(2)\n" \
  "v_mfma_f32_16x16x32_bf16 v[24:27], v[112:115], v[36:39], v[24:27]\n" \
  "ds_read_b128 v[48:51], v7 offset:30976\n" \
  "v_mfma_f32_16x16x32_bf16 v[28:31], v[176:179], v[36:39], v[28:31]\n" \
  "v_mfma_f32_16x16x32_bf16 v[32:35], v[36:39], v[240:243], v[32:35]\n" \
  "s_waitcnt lgkmcnt(2)\n" \
  "v_mfma_f32_16x16x32_bf16 v[24:27], v[116:119], v[40:43], v[24:27]\n" \
  "v_mfma_f32_16x16x32_bf16 v[28:31], v[180:183], v[40:43], v[28:31]\n" \
  "v_mfma_f32_16x16x32_bf16 v[32:35], v[40:43], v[244:247], v[32:35]\n" \
  "s_waitcnt lgkmcnt(1)\n" \
  "v_mfma_f32_16x16x32_bf16 v[24:27], v[120:123], v[44:47], v[24:27]\n" \
  "v_mfma_f32_16x16x32_bf16 v[28:31], v[184:187], v[44:47], v[28:31]\n" \
  "v_mfma_f32_16x16x32_bf16 v[32:35], v[44:47], v[248:251], v[32:35]\n" \
  "s_waitcnt lgkmcnt(0)\n" \
  "v_mfma_f32_16x16x32_bf16 v[24:27], v[124:127], v[48:51], v[24:27]\n" \
  "v_mfma_f32_16x16x32_bf16 v[28:31], v[188:191], v[48:51], v[28:31]\n" \
  "v_mfma_f32_16x16x32_bf16 v[32:35], v[48:51], v[252:255], v[32:35]\n" \
  "s_setprio 2\n" \
  "v_mul_f32 v12, v12, v56\n" \
  "v_mul_f32 v13, v13, v56\n" \
  "v_mul_f32 v14, v14, v56\n" \
  "v_cvt_pk_bf16_f32 v12, v12, v13\n" \
  "v_mul_f32 v15, v15, v56\n" \
  "v_mul_f32 v16, v16, v56\n" \
  "v_cvt_pk_bf16_f32 v13, v14, v15\n" \
  "v_mul_f32 v17, v17, v56\n" \
  "v_mul_f32 v18, v18, v56\n" \
  "v_cvt_pk_bf16_f32 v14, v16, v17\n" \
  "v_mul_f32 v19, v19, v56\n" \
  "v_cvt_pk_bf16_f32 v15, v18, v19\n" \
  "buffer_store_dwordx4 v[12:15], v6, s[40:43], s59 offen offset:0 sc1\n" \
  "v_mul_f32 v24, v24, v57\n" \
  "v_mul_f32 v25, v25, v57\n" \
  "v_mul_f32 v26, v26, v57\n" \
  "v_cvt_pk_bf16_f32 v24, v24, v25\n" \
  "v_mul_f32 v27, v27, v57\n" \
  "v_mul_f32 v28, v28, v57\n" \
  "v_cvt_pk_bf16_f32 v25, v26, v27\n" \
  "v_mul_f32 v29, v29, v57\n" \
  "v_mul_f32 v30, v30, v57\n" \
  "v_cvt_pk_bf16_f32 v26, v28, v29\n" \
  "v_mul_f32 v31, v31, v57\n" \
  "v_cvt_pk_bf16_f32 v27, v30, v31\n" \
  "buffer_store_dwordx4 v[24:27], v6, s[40:43], s74 offen offset:0 sc1\n" \
  "v_mul_f32 v20, v20, v58\n" \
  "v_mul_f32 v21, v21, v59\n" \
  "v_mul_f32 v22, v22, v60\n" \
  "v_mul_f32 v23, v23, v61\n" \
  "v_cvt_pk_bf16_f32 v20, v20, v21\n" \
  "v_cvt_pk_bf16_f32 v21, v22, v23\n" \
  "v_mul_f32 v32, v32, v52\n" \
  "v_mul_f32 v33, v33, v53\n" \
  "v_mul_f32 v34, v34, v54\n" \
  "v_mul_f32 v35, v35, v55\n" \
  "v_cvt_pk_bf16_f32 v22, v32, v33\n" \
  "v_cvt_pk_bf16_f32 v23, v34, v35\n" \
  "buffer_store_dwordx4 v[20:23], v6, s[44:47], s60 offen offset:0 sc1\n" \
  "s_setprio 0\n" \
  "s_add_u32 s59, s59, 0x80000\n" \
  "s_add_u32 s60, s60, 0x40000\n" \
  "s_add_u32 s61, s61, 0x10000\n" \
  "v_xor_b32 v7, 0x8000, v7\n" \
  "s_cmp_eq_u32 s69, 1\n" \
  "s_waitcnt vmcnt(3)\n" \
  "s_cbranch_scc1 .LQA_GO_%=\n" \
  "v_cmp_eq_u32 vcc, 0, v11\n" \
  "s_cmp_eq_u64 vcc, 0\n" \
  "s_cbranch_scc0 .LQA_NOX_%=\n" \
  "s_mov_b32 m0, s58\n" \
  "s_add_u32 s74, s56, 4096\n" \
  "s_addc_u32 s75, s57, 0\n" \
  "s_add_u32 s76, s58, 4096\n" \
  "global_load_lds_dwordx4 v6, s[56:57] offset:0 sc1\n" \
  "global_load_lds_dwordx4 v6, s[56:57] offset:1024 sc1\n" \
  "global_load_lds_dwordx4 v6, s[56:57] offset:2048 sc1\n" \
  "global_load_lds_dwordx4 v6, s[56:57] offset:3072 sc1\n" \
  "s_mov_b32 m0, s76\n" \
  "s_nop 0\n" \
  "global_load_lds_dwordx4 v6, s[74:75] offset:0 sc1\n" \
  "global_load_lds_dwordx4 v6, s[74:75] offset:1024 sc1\n" \
  "global_load_lds_dwordx4 v6, s[74:75] offset:2048 sc1\n" \
  "global_load_lds_dwordx4 v6, s[74:75] offset:3072 sc1\n" \
  "s_mov_b32 s69, 2\n" \
  "s_waitcnt vmcnt(8)\n" \
  "s_branch .LQA_CNT_%=\n" \
  ".LQA_NOX_%=:\n" \
  "s_waitcnt vmcnt(0)\n" \
  ".LQA_CNT_%=:\n" \
  "v_mov_b32 v52, s80\n" \
  "v_mov_b32 v53, 0x10000\n" \
  "s_mov_b64 exec, 1\n" \
  "ds_add_rtn_u32 v54, v52, v53\n" \
  "s_mov_b64 exec, -1\n" \
  "s_add_u32 s67, s67, 4\n" \
  "s_add_u32 s64, s64, 4096\n" \
  "s_addc_u32 s65, s65, 0\n" \
  "v_mov_b32 v55, 0\n" \
  "s_waitcnt lgkmcnt(0)\n" \
  "v_readfirstlane_b32 s74, v54\n" \
  "s_and_b32 s76, s74, 0x30000\n" \
  "s_cmp_lg_u32 s76, 0x30000\n" \
  "s_cbranch_scc1 .LQA_FWN_%=\n" \
  "s_mov_b32 s79, 0x100000\n" \
  "s_branch .LQA_FWT_%=\n" \
  ".LQA_FW_%=:\n" \
  "ds_read_b32 v54, v52\n" \
  "s_sub_u32 s79, s79, 1\n" \
  "s_cbranch_scc1 .LQA_FWE_%=\n" \
  "s_waitcnt lgkmcnt(0)\n" \
  "v_readfirstlane_b32 s74, v54\n" \
  ".LQA_FWT_%=:\n" \
  "s_and_b32 s74, s74, 0xffff\n" \
  "s_cmp_lt_u32 s74, s67\n" \
  "s_cbranch_scc1 .LQA_FW_%=\n" \
  ".LQA_FWE_%=:\n" \
  "s_mov_b64 exec, 1\n" \
  "global_store_dword v55, v53, s[64:65] offset:-4096 sc1\n" \
  "s_mov_b64 exec, -1\n" \
  "s_waitcnt vmcnt(1)\n" \
  "s_branch .LQA_GO_%=\n" \
  ".LQA_FWN_%=:\n" \
  "s_waitcnt vmcnt(0)\n" \
  ".LQA_GO_%=:\n" \
  "s_sub_u32 s66, s66, 1\n" \
  "s_cbranch_scc1 .LQA_LAST_%=\n" \
  "s_cmp_lg_u32 s69, 0\n" \
  "s_cbranch_scc1 .LQA_NEXT_%=\n" \
  ".LQA_GATE_%=:\n" \
  "s_memrealtime s[74:75]\n" \
  "s_waitcnt lgkmcnt(0)\n" \
  "s_sub_i32 s74, s68, s74\n" \
  "s_cmp_gt_u32 s74, %[gq]\n" \
  "s_cbranch_scc1 .LQA_POLL_%=\n" \
  "s_sleep 3\n" \
  "s_branch .LQA_GATE_%=\n" \
  ".LQA_POLL_%=:\n" \
  "s_mov_b32 s79, 0x40000\n" \
  ".LQA_AGAIN_%=:\n" \
  "buffer_load_dword v11, v10, s[52:55], s62 offen sc1\n" \
  "s_sub_u32 s79, s79, 1\n" \
  "s_cbranch_scc1 .LQA_SEEN_%=\n" \
  "s_waitcnt vmcnt(0)\n" \
  "v_cmp_eq_u32 vcc, 0, v11\n" \
  "s_cmp_eq_u64 vcc, 0\n" \
  "s_cbranch_scc1 .LQA_SEEN_%=\n" \
  "s_sleep %[sq]\n" \
  "s_cmp_lg_u32 s63, 0\n" \
  "s_cbranch_scc1 .LQA_AGAIN_%=\n" \
  "s_sleep %[s1]\n" \
  "s_branch .LQA_AGAIN_%=\n" \
  ".LQA_SEEN_%=:\n" \
  "s_mov_b32 m0, s58\n" \
  "s_add_u32 s74, s56, 4096\n" \
  "s_addc_u32 s75, s57, 0\n" \
  "s_add_u32 s76, s58, 4096\n" \
  "global_load_lds_dwordx4 v6, s[56:57] offset:0 sc1\n" \
  "global_load_lds_dwordx4 v6, s[56:57] offset:1024 sc1\n" \
  "global_load_lds_dwordx4 v6, s[56:57] offset:2048 sc1\n" \
  "global_load_lds_dwordx4 v6, s[56:57] offset:3072 sc1\n" \
  "s_mov_b32 m0, s76\n" \
  "s_nop 0\n" \
  "global_load_lds_dwordx4 v6, s[74:75] offset:0 sc1\n" \
  "global_load_lds_dwordx4 v6, s[74:75] offset:1024 sc1\n" \
  "global_load_lds_dwordx4 v6, s[74:75] offset:2048 sc1\n" \
  "global_load_lds_dwordx4 v6, s[74:75] offset:3072 sc1\n" \
  "s_memrealtime s[76:77]\n" \
  "s_waitcnt lgkmcnt(0)\n" \
  "s_add_u32 s68, s76, %[gq]\n" \
  "s_waitcnt vmcnt(0)\n" \
  ".LQA_NEXT_%=:\n" \
  "s_add_u32 s56, s56, 0x8000\n" \
  "s_addc_u32 s57, s57, 0\n" \
  "s_xor_b32 s58, s58, 0x8000\n" \
  "s_add_u32 s62, s62, 4096\n" \
  "buffer_load_dword v11, v10, s[52:55], s62 offen sc1\n" \
  "s_branch .LQA_TOP_%=\n" \
  ".LQA_LAST_%=:\n" \
  "s_barrier\n" \
  "s_cmp_lg_u32 s78, 0\n" \
  "s_cbranch_scc1 .LQA_END_%=\n" \
  "s_add_u32 s67, s67, 4\n" \
  "s_add_u32 s64, s64, 4096\n" \
  "s_addc_u32 s65, s65, 0\n" \
  "s_mov_b32 s74, 0\n" \
  "s_mov_b32 s79, 0x100000\n" \
  "s_branch .LQA_FLT_%=\n" \
  ".LQA_FL_%=:\n" \
  "ds_read_b32 v54, v52\n" \
  "s_sub_u32 s79, s79, 1\n" \
  "s_cbranch_scc1 .LQA_FLE_%=\n" \
  "s_waitcnt lgkmcnt(0)\n" \
  "v_readfirstlane_b32 s74, v54\n" \
  ".LQA_FLT_%=:\n" \
  "s_and_b32 s74, s74, 0xffff\n" \
  "s_cmp_lt_u32 s74, s67\n" \
  "s_cbranch_scc1 .LQA_FL_%=\n" \
  ".LQA_FLE_%=:\n" \
  "s_mov_b64 exec, 1\n" \
  "global_store_dword v55, v53, s[64:65] offset:-4096 sc1\n" \
  "s_mov_b64 exec, -1\n" \
  ".LQA_END_%=:\n" \
  "s_endpgm\n"
#define QKV_ASM_1 \
  "s_barrier\n" \
  "ds_read_b128 v[36:39], v10 offset:0\n" \
  "ds_read_b128 v[40:43], v7 offset:0\n" \
  "ds_read_b128 v[44:47], v10 offset:2048\n" \
  "ds_read_b128 v[48:51], v7 offset:2048\n" \
  ".p2align 6\n" \
  ".LQB_AL_%=:\n" \
  ".LQB_TOP_%=:\n" \
  "s_setprio 3\n" \
  "s_waitcnt lgkmcnt(3)\n" \
  "v_mfma_f32_16x16x32_bf16 v[56:59], v[36:39], v[36:39], 0\n" \
  "ds_read_b128 v[52:55], v10 offset:4096\n" \
  "s_waitcnt lgkmcnt(3)\n" \
  "v_mfma_f32_16x16x32_bf16 v[12:15], v[40:43], v[192:195], 0\n" \
  "ds_read_b128 v[36:39], v7 offset:4096\n" \
  "s_waitcnt lgkmcnt(3)\n" \
  "v_mfma_f32_16x16x32_bf16 v[56:59], v[44:47], v[44:47], v[56:59]\n" \
  "ds_read_b128 v[40:43], v10 offset:6144\n" \
  "s_waitcnt lgkmcnt(3)\n" \
  "v_mfma_f32_16x16x32_bf16 v[12:15], v[48:51], v[196:199], v[12:15]\n" \
  "ds_read_b128 v[44:47], v7 offset:6144\n" \
  "s_waitcnt lgkmcnt(3)\n" \
  "v_mfma_f32_16x16x32_bf16 v[56:59], v[52:55], v[52:55], v[56:59]\n" \
  "ds_read_b128 v[48:51], v10 offset:8192\n" \
  "s_waitcnt lgkmcnt(3)\n" \
  "v_mfma_f32_16x16x32_bf16 v[12:15], v[36:39], v[200:203], v[12:15]\n" \
  "ds_read_b128 v[52:55], v7 offset:8192\n" \
  "s_waitcnt lgkmcnt(3)\n" \
  "v_mfma_f32_16x16x32_bf16 v[56:59], v[40:43], v[40:43], v[56:59]\n" \
  "ds_read_b128 v[36:39], v10 offset:10240\n" \
  "s_waitcnt lgkmcnt(3)\n" \
  "v_mfma_f32_16x16x32_bf16 v[12:15], v[44:47], v[204:207], v[12:15]\n" \
  "ds_read_b128 v[40:43], v7 offset:10240\n" \
  "s_waitcnt lgkmcnt(3)\n" \
  "v_mfma_f32_16x16x32_bf16 v[56:59], v[48:51], v[48:51], v[56:59]\n" \
  "ds_read_b128 v[44:47], v10 offset:12288\n" \
  "s_waitcnt lgkmcnt(3)\n" \
  "v_mfma_f32_16x16x32_bf16 v[12:15], v[52:55], v[208:211], v[12:15]\n" \
  "ds_read_b128 v[48:51], v7 offset:12288\n" \
  "s_waitcnt lgkmcnt(3)\n" \
  "v_mfma_f32_16x16x32_bf16 v[56:59], v[36:39], v[36:39], v[56:59]\n" \
  "ds_read_b128 v[52:55], v10 offset:14336\n" \
  "s_waitcnt lgkmcnt(3)\n" \
  "v_mfma_f32_16x16x32_bf16 v[12:15], v[40:43], v[212:215], v[12:15]\n" \
  "ds_read_b128 v[36:39], v7 offset:14336\n" \
  "s_waitcnt lgkmcnt(3)\n" \
  "v_mfma_f32_16x16x32_bf16 v[56:59], v[44:47], v[44:47], v[56:59]\n" \
  "ds_read_b128 v[40:43], v10 offset:16384\n" \
  "s_waitcnt lgkmcnt(3)\n" \
  "v_mfma_f32_16x16x32_bf16 v[12:15], v[48:51], v[216:219], v[12:15]\n" \
  "ds_read_b128 v[44:47], v7 offset:16384\n" \
  "s_waitcnt lgkmcnt(3)\n" \
  "v_mfma_f32_16x16x32_bf16 v[56:59], v[52:55], v[52:55], v[56:59]\n" \
  "ds_read_b128 v[48:51], v10 offset:18432\n" \
  "s_waitcnt lgkmcnt(3)\n" \
  "v_mfma_f32_16x16x32_bf16 v[12:15], v[36:39], v[220:223], v[12:15]\n" \
  "ds_read_b128 v[52:55], v7 offset:18432\n" \
  "s_waitcnt lgkmcnt(3)\n" \
  "v_mfma_f32_16x16x32_bf16 v[56:59], v[40:43], v[40:43], v[56:59]\n" \
  "ds_read_b128 v[36:39], v10 offset:20480\n" \
  "s_waitcnt lgkmcnt(3)\n" \
  "v_mfma_f32_16x16x32_bf16 v[12:15], v[44:47], v[224:227], v[12:15]\n" \
  "ds_read_b128 v[40:43], v7 offset:20480\n" \
  "s_waitcnt lgkmcnt(3)\n" \
  "v_mfma_f32_16x16x32_bf16 v[56:59], v[48:51], v[48:51], v[56:59]\n" \
  "ds_read_b128 v[44:47], v10 offset:22528\n" \
  "s_waitcnt lgkmcnt(3)\n" \
  "v_mfma_f32_16x16x32_bf16 v[12:15], v[52:55], v[228:231], v[12:15]\n" \
  "ds_read_b128 v[48:51], v7 offset:22528\n" \
  "s_waitcnt lgkmcnt(3)\n" \
  "v_mfma_f32_16x16x32_bf16 v[56:59], v[36:39], v[36:39], v[56:59]\n" \
  "ds_read_b128 v[52:55], v10 offset:24576\n" \
  "s_waitcnt lgkmcnt(3)\n" \
  "v_mfma_f32_16x16x32_bf16 v[12:15], v[40:43], v[232:235], v[12:15]\n" \
  "ds_read_b128 v[36:39], v7 offset:24576\n" \
  "s_waitcnt lgkmcnt(3)\n" \
  "v_mfma_f32_16x16x32_bf16 v[56:59], v[44:47], v[44:47], v[56:59]\n" \
  "ds_read_b128 v[40:43], v10 offset:26624\n" \
  "s_waitcnt lgkmcnt(3)\n" \
  "v_mfma_f32_16x16x32_bf16 v[12:15], v[48:51], v[236:239], v[12:15]\n" \
  "ds_read_b128 v[44:47], v7 offset:26624\n" \
  "s_waitcnt lgkmcnt(3)\n" \
  "v_mfma_f32_16x16x32_bf16 v[56:59], v[52:55], v[52:55], v[56:59]\n" \
  "ds_read_b128 v[48:51], v10 offset:28672\n" \
  "s_waitcnt lgkmcnt(3)\n" \
  "v_mfma_f32_16x16x32_bf16 v[12:15], v[36:39], v[240:243], v[12:15]\n" \
  "ds_read_b128 v[52:55], v7 offset:28672\n" \
  "s_waitcnt lgkmcnt(3)\n" \
  "v_mfma_f32_16x16x32_bf16 v[56:59], v[40:43], v[40:43], v[56:59]\n" \
  "ds_read_b128 v[36:39], v10 offset:30720\n" \
  "s_waitcnt lgkmcnt(3)\n" \
  "v_mfma_f32_16x16x32_bf16 v[12:15], v[44:47], v[244:247], v[12:15]\n" \
  "ds_read_b128 v[40:43], v7 offset:30720\n" \
  "s_waitcnt lgkmcnt(3)\n" \
  "v_mfma_f32_16x16x32_bf16 v[56:59], v[48:51], v[48:51], v[56:59]\n" \
  "ds_read_b128 v[44:47], v7 offset:256\n" \
  "s_waitcnt lgkmcnt(3)\n" \
  "v_mfma_f32_16x16x32_bf16 v[12:15], v[52:55], v[248:251], v[12:15]\n" \
  "ds_read_b128 v[48:51], v7 offset:2304\n" \
  "s_waitcnt lgkmcnt(3)\n" \
  "v_mfma_f32_16x16x32_bf16 v[56:59], v[36:39], v[36:39], v[56:59]\n" \
  "ds_read_b128 v[52:55], v7 offset:4352\n" \
  "s_waitcnt lgkmcnt(3)\n" \
  "v_mfma_f32_16x16x32_bf16 v[12:15], v[40:43], v[252:255], v[12:15]\n" \
  "ds_read_b128 v[36:39], v7 offset:6400\n" \
  "s_waitcnt lgkmcnt(3)\n" \
  "v_mfma_f32_16x16x32_bf16 v[16:19], v[44:47], v[192:195], 0\n" \
  "ds_read_b128 v[40:43], v7 offset:8448\n" \
  "s_waitcnt lgkmcnt(3)\n" \
  "v_mfma_f32_16x16x32_bf16 v[16:19], v[48:51], v[196:199], v[16:19]\n" \
  "ds_read_b128 v[44:47], v7 offset:10496\n" \
  "s_waitcnt lgkmcnt(3)\n" \
  "v_mfma_f32_16x16x32_bf16 v[16:19], v[52:55], v[200:203], v[16:19]\n" \
  "ds_read_b128 v[48:51], v7 offset:12544\n" \
  "s_waitcnt lgkmcnt(3)\n" \
  "v_mfma_f32_16x16x32_bf16 v[16:19], v[36:39], v[204:207], v[16:19]\n" \
  "ds_read_b128 v[52:55], v7 offset:14592\n" \
  "s_waitcnt lgkmcnt(3)\n" \
  "v_mfma_f32_16x16x32_bf16 v[16:19], v[40:43], v[208:211], v[16:19]\n" \
  "ds_read_b128 v[36:39], v7 offset:16640\n" \
  "s_waitcnt lgkmcnt(3)\n" \
  "v_mfma_f32_16x16x32_bf16 v[16:19], v[44:47], v[212:215], v[16:19]\n" \
  "ds_read_b128 v[40:43], v7 offset:18688\n" \
  "s_waitcnt lgkmcnt(3)\n" \
  "v_mfma_f32_16x16x32_bf16 v[16:19], v[48:51], v[216:219], v[16:19]\n" \
  "ds_read_b128 v[44:47], v7 offset:20736\n" \
  "s_waitcnt lgkmcnt(3)\n" \
  "v_mfma_f32_16x16x32_bf16 v[16:19], v[52:55], v[220:223], v[16:19]\n" \
  "ds_read_b128 v[48:51], v7 offset:22784\n" \
  "s_waitcnt lgkmcnt(3)\n" \
  "v_mfma_f32_16x16x32_bf16 v[16:19], v[36:39], v[224:227], v[16:19]\n" \
  "ds_read_b128 v[52:55], v7 offset:24832\n" \
  "s_waitcnt lgkmcnt(3)\n" \
  "v_mfma_f32_16x16x32_bf16 v[16:19], v[40:43], v[228:231], v[16:19]\n" \
  "ds_read_b128 v[36:39], v7 offset:26880\n" \
  "s_waitcnt lgkmcnt(3)\n" \
  "v_mfma_f32_16x16x32_bf16 v[16:19], v[44:47], v[232:235], v[16:19]\n" \
  "ds_read_b128 v[40:43], v7 offset:28928\n" \
  "s_waitcnt lgkmcnt(3)\n" \
  "v_mfma_f32_16x16x32_bf16 v[16:19], v[48:51], v[236:239], v[16:19]\n" \
  "ds_read_b128 v[44:47], v7 offset:30976\n" \
  "s_waitcnt lgkmcnt(3)\n" \
  "v_mfma_f32_16x16x32_bf16 v[16:19], v[52:55], v[240:243], v[16:19]\n" \
  "ds_read_b128 v[48:51], v7 offset:0\n" \
  "s_waitcnt lgkmcnt(3)\n" \
  "v_mfma_f32_16x16x32_bf16 v[16:19], v[36:39], v[244:247], v[16:19]\n" \
  "ds_read_b128 v[52:55], v7 offset:2048\n" \
  "s_waitcnt lgkmcnt(3)\n" \
  "v_mfma_f32_16x16x32_bf16 v[16:19], v[40:43], v[248:251], v[16:19]\n" \
  "ds_read_b128 v[36:39], v7 offset:4096\n" \
  "s_waitcnt lgkmcnt(3)\n" \
  "v_mfma_f32_16x16x32_bf16 v[16:19], v[44:47], v[252:255], v[16:19]\n" \
  "ds_read_b128 v[40:43], v7 offset:6144\n" \
  "s_setprio 0\n" \
  "v_cndmask_b32 v56, v56, v57, s[70:71]\n" \
  "v_cndmask_b32 v57, v58, v59, s[70:71]\n" \
  "v_cndmask_b32 v56, v56, v57, s[72:73]\n" \
  "v_mov_b32 v57, v56\n" \
  "v_mov_b32 v58, v56\n" \
  "v_mul_f32 v56, 0x3b000000, v56\n" \
  "v_xor_b32 v10, v11, v10\n" \
  "v_permlane32_swap_b32 v57, v58\n" \
  "v_mul_f32 v57, 0xbb000000, v58\n" \
  "v_fma_f32 v56, -v57, v57, v56\n" \
  "v_max_f32 v56, 0, v56\n" \
  "v_cvt_pk_bf16_f32 v56, v56, v56\n" \
  "v_lshlrev_b32 v56, 16, v56\n" \
  "v_add_f32 v56, 0x3727c5ac, v56\n" \
  "v_cvt_pk_bf16_f32 v56, v56, v56\n" \
  "v_lshlrev_b32 v56, 16, v56\n" \
  "v_sqrt_f32 v56, v56\n" \
  "v_cvt_pk_bf16_f32 v57, v57, v57\n" \
  "v_cvt_pk_bf16_f32 v56, v56, v56\n" \
  "v_lshlrev_b32 v56, 16, v56\n" \
  "v_rcp_f32 v56, v56\n" \
  "s_mov_b64 exec, 0xf0000f\n" \
  "ds_write2_b32 v62, v57, v56 offset1:32\n" \
  "s_mov_b64 exec, -1\n" \
  "s_cmp_eq_u32 s66, 63\n" \
  "s_cbranch_scc1 .LQB_NOE_%=\n" \
  "v_mul_f32 v20, v20, v60\n" \
  "v_mul_f32 v21, v21, v60\n" \
  "v_mul_f32 v22, v22, v60\n" \
  "v_cvt_pk_bf16_f32 v20, v20, v21\n" \
  "v_mul_f32 v23, v23, v60\n" \
  "v_mul_f32 v24, v24, v60\n" \
  "v_cvt_pk_bf16_f32 v21, v22, v23\n" \
  "v_mul_f32 v25, v25, v60\n" \
  "v_mul_f32 v26, v26, v60\n" \
  "v_cvt_pk_bf16_f32 v22, v24, v25\n" \
  "v_mul_f32 v27, v27, v60\n" \
  "v_cvt_pk_bf16_f32 v23, v26, v27\n" \
  "buffer_store_dwordx4 v[20:23], v6, s[40:43], s59 offen offset:0 sc1\n" \
  "s_add_u32 s76, s59, 4096\n" \
  "v_mul_f32 v28, v28, v61\n" \
  "v_mul_f32 v29, v29, v61\n" \
  "v_mul_f32 v30, v30, v61\n" \
  "v_cvt_pk_bf16_f32 v28, v28, v29\n" \
  "v_mul_f32 v31, v31, v61\n" \
  "v_mul_f32 v32, v32, v61\n" \
  "v_cvt_pk_bf16_f32 v29, v30, v31\n" \
  "v_mul_f32 v33, v33, v61\n" \
  "v_mul_f32 v34, v34, v61\n" \
  "v_cvt_pk_bf16_f32 v30, v32, v33\n" \
  "v_mul_f32 v35, v35, v61\n" \
  "v_cvt_pk_bf16_f32 v31, v34, v35\n" \
  "buffer_store_dwordx4 v[28:31], v6, s[40:43], s76 offen offset:0 sc1\n" \
  "s_add_u32 s59, s59, 0x80000\n" \
  ".LQB_NOE_%=:\n" \
  "s_waitcnt lgkmcnt(0)\n" \
  "s_barrier\n" \
  "ds_read2_b32 v[56:57], v8 offset0:0 offset1:0\n" \
  "ds_read2_b32 v[58:59], v8 offset0:16 offset1:16\n" \
  "ds_read_b128 v[28:31], v9 offset:128\n" \
  "ds_read_b128 v[32:35], v9 offset:192\n" \
  "ds_read2_b32 v[60:61], v8 offset0:32 offset1:48\n" \
  "s_setprio 3\n" \
  "s_waitcnt lgkmcnt(3)\n" \
  "v_mfma_f32_16x16x16_bf16 v[12:15], v[56:57], v[4:5], v[12:15]\n" \
  "v_mfma_f32_16x16x16_bf16 v[16:19], v[58:59], v[4:5], v[16:19]\n" \
  "s_setprio 0\n" \
  "s_waitcnt lgkmcnt(0)\n" \
  "s_nop 4\n" \
  "v_mul_f32 v12, v12, v28\n" \
  "v_mul_f32 v13, v13, v29\n" \
  "v_mul_f32 v14, v14, v30\n" \
  "v_mul_f32 v15, v15, v31\n" \
  "v_cvt_pk_bf16_f32 v12, v12, v13\n" \
  "v_cvt_pk_bf16_f32 v13, v14, v15\n" \
  "v_mul_f32 v16, v16, v32\n" \
  "v_mul_f32 v17, v17, v33\n" \
  "v_mul_f32 v18, v18, v34\n" \
  "v_mul_f32 v19, v19, v35\n" \
  "v_cvt_pk_bf16_f32 v14, v16, v17\n" \
  "v_cvt_pk_bf16_f32 v15, v18, v19\n" \
  "buffer_store_dwordx4 v[12:15], v6, s[44:47], s60 offen offset:0 sc1\n" \
  "v_mov_b64 v[16:17], 0\n" \
  "v_lshrrev_b32 v18, 1, v6\n" \
  "buffer_store_dwordx2 v[16:17], v18, s[48:51], s61 offen sc1\n" \
  "s_setprio 0\n" \
  "s_add_u32 s60, s60, 0x40000\n" \
  "s_add_u32 s61, s61, 0x10000\n" \
  "s_sub_u32 s66, s66, 1\n" \
  "v_mov_b32 v12, s80\n" \
  "v_mov_b32 v13, 1\n" \
  "v_mfma_f32_16x16x16_bf16 v[20:23], v[0:1], v[56:57], 0\n" \
  "v_mfma_f32_16x16x16_bf16 v[24:27], v[2:3], v[56:57], 0\n" \
  "v_mfma_f32_16x16x16_bf16 v[28:31], v[0:1], v[58:59], 0\n" \
  "v_mfma_f32_16x16x16_bf16 v[32:35], v[2:3], v[58:59], 0\n" \
  "v_mfma_f32_16x16x32_bf16 v[20:23], v[64:67], v[48:51], v[20:23]\n" \
  "ds_read_b128 v[44:47], v7 offset:8192\n" \
  "v_mfma_f32_16x16x32_bf16 v[24:27], v[128:131], v[48:51], v[24:27]\n" \
  "v_mfma_f32_16x16x32_bf16 v[20:23], v[68:71], v[52:55], v[20:23]\n" \
  "ds_read_b128 v[48:51], v7 offset:10240\n" \
  "v_mfma_f32_16x16x32_bf16 v[24:27], v[132:135], v[52:55], v[24:27]\n" \
  "v_mfma_f32_16x16x32_bf16 v[20:23], v[72:75], v[36:39], v[20:23]\n" \
  "ds_read_b128 v[52:55], v7 offset:12288\n" \
  "v_mfma_f32_16x16x32_bf16 v[24:27], v[136:139], v[36:39], v[24:27]\n" \
  "v_mfma_f32_16x16x32_bf16 v[20:23], v[76:79], v[40:43], v[20:23]\n" \
  "ds_read_b128 v[36:39], v7 offset:14336\n" \
  "v_mfma_f32_16x16x32_bf16 v[24:27], v[140:143], v[40:43], v[24:27]\n" \
  "s_waitcnt lgkmcnt(3)\n" \
  "v_mfma_f32_16x16x32_bf16 v[20:23], v[80:83], v[44:47], v[20:23]\n" \
  "ds_read_b128 v[40:43], v7 offset:16384\n" \
  "v_mfma_f32_16x16x32_bf16 v[24:27], v[144:147], v[44:47], v[24:27]\n" \
  "s_waitcnt lgkmcnt(3)\n" \
  "v_mfma_f32_16x16x32_bf16 v[20:23], v[84:87], v[48:51], v[20:23]\n" \
  "ds_read_b128 v[44:47], v7 offset:18432\n" \
  "v_mfma_f32_16x16x32_bf16 v[24:27], v[148:151], v[48:51], v[24:27]\n" \
  "s_waitcnt lgkmcnt(3)\n" \
  "v_mfma_f32_16x16x32_bf16 v[20:23], v[88:91], v[52:55], v[20:23]\n" \
  "ds_read_b128 v[48:51], v7 offset:20480\n" \
  "s_waitcnt vmcnt(0)\n" \
  "s_mov_b64 exec, 1\n" \
  "ds_add_u32 v12, v13\n" \
  "s_mov_b64 exec, -1\n" \
  "v_mfma_f32_16x16x32_bf16 v[24:27], v[152:155], v[52:55], v[24:27]\n" \
  "s_waitcnt lgkmcnt(4)\n" \
  "v_mfma_f32_16x16x32_bf16 v[20:23], v[92:95], v[36:39], v[20:23]\n" \
  "ds_read_b128 v[52:55], v7 offset:22528\n" \
  "v_mfma_f32_16x16x32_bf16 v[24:27], v[156:159], v[36:39], v[24:27]\n" \
  "s_waitcnt lgkmcnt(4)\n" \
  "v_mfma_f32_16x16x32_bf16 v[20:23], v[96:99], v[40:43], v[20:23]\n" \
  "ds_read_b128 v[36:39], v7 offset:24576\n" \
  "v_mfma_f32_16x16x32_bf16 v[24:27], v[160:163], v[40:43], v[24:27]\n" \
  "s_waitcnt lgkmcnt(4)\n" \
  "v_mfma_f32_16x16x32_bf16 v[20:23], v[100:103], v[44:47], v[20:23]\n" \
  "ds_read_b128 v[40:43], v7 offset:26624\n" \
  "v_mfma_f32_16x16x32_bf16 v[24:27], v[164:167], v[44:47], v[24:27]\n" \
  "s_waitcnt lgkmcnt(4)\n" \
  "v_mfma_f32_16x16x32_bf16 v[20:23], v[104:107], v[48:51], v[20:23]\n" \
  "ds_read_b128 v[44:47], v7 offset:28672\n" \
  "v_mfma_f32_16x16x32_bf16 v[24:27], v[168:171], v[48:51], v[24:27]\n" \
  "s_waitcnt lgkmcnt(3)\n" \
  "v_mfma_f32_16x16x32_bf16 v[20:23], v[108:111], v[52:55], v[20:23]\n" \
  "ds_read_b128 v[48:51], v7 offset:30720\n" \
  "v_mfma_f32_16x16x32_bf16 v[24:27], v[172:175], v[52:55], v[24:27]\n" \
  "s_waitcnt lgkmcnt(3)\n" \
  "v_mfma_f32_16x16x32_bf16 v[20:23], v[112:115], v[36:39], v[20:23]\n" \
  "ds_read_b128 v[52:55], v7 offset:256\n" \
  "v_mfma_f32_16x16x32_bf16 v[24:27], v[176:179], v[36:39], v[24:27]\n" \
  "s_waitcnt lgkmcnt(3)\n" \
  "v_mfma_f32_16x16x32_bf16 v[20:23], v[116:119], v[40:43], v[20:23]\n" \
  "ds_read_b128 v[36:39], v7 offset:2304\n" \
  "v_mfma_f32_16x16x32_bf16 v[24:27], v[180:183], v[40:43], v[24:27]\n" \
  "s_waitcnt lgkmcnt(3)\n" \
  "v_mfma_f32_16x16x32_bf16 v[20:23], v[120:123], v[44:47], v[20:23]\n" \
  "ds_read_b128 v[40:43], v7 offset:4352\n" \
  "v_mfma_f32_16x16x32_bf16 v[24:27], v[184:187], v[44:47], v[24:27]\n" \
  "s_waitcnt lgkmcnt(3)\n" \
  "v_mfma_f32_16x16x32_bf16 v[20:23], v[124:127], v[48:51], v[20:23]\n" \
  "ds_read_b128 v[44:47], v7 offset:6400\n" \
  "v_mfma_f32_16x16x32_bf16 v[24:27], v[188:191], v[48:51], v[24:27]\n" \
  "s_waitcnt lgkmcnt(3)\n" \
  "v_mfma_f32_16x16x32_bf16 v[28:31], v[64:67], v[52:55], v[28:31]\n" \
  "ds_read_b128 v[48:51], v7 offset:8448\n" \
  "v_mfma_f32_16x16x32_bf16 v[32:35], v[128:131], v[52:55], v[32:35]\n" \
  "s_waitcnt lgkmcnt(3)\n" \
  "v_mfma_f32_16x16x32_bf16 v[28:31], v[68:71], v[36:39], v[28:31]\n" \
  "ds_read_b128 v[52:55], v7 offset:10496\n" \
  "v_mfma_f32_16x16x32_bf16 v[32:35], v[132:135], v[36:39], v[32:35]\n" \
  "s_waitcnt lgkmcnt(3)\n" \
  "v_mfma_f32_16x16x32_bf16 v[28:31], v[72:75], v[40:43], v[28:31]\n" \
  "ds_read_b128 v[36:39], v7 offset:12544\n" \
  "v_mfma_f32_16x16x32_bf16 v[32:35], v[136:139], v[40:43], v[32:35]\n" \
  "s_waitcnt lgkmcnt(3)\n" \
  "v_mfma_f32_16x16x32_bf16 v[28:31], v[76:79], v[44:47], v[28:31]\n" \
  "ds_read_b128 v[40:43], v7 offset:14592\n" \
  "v_mfma_f32_16x16x32_bf16 v[32:35], v[140:143], v[44:47], v[32:35]\n" \
  "s_waitcnt lgkmcnt(3)\n" \
  "v_mfma_f32_16x16x32_bf16 v[28:31], v[80:83], v[48:51], v[28:31]\n" \
  "ds_read_b128 v[44:47], v7 offset:16640\n" \
  "v_mfma_f32_16x16x32_bf16 v[32:35], v[144:147], v[48:51], v[32:35]\n" \
  "s_waitcnt lgkmcnt(3)\n" \
  "v_mfma_f32_16x16x32_bf16 v[28:31], v[84:87], v[52:55], v[28:31]\n" \
  "ds_read_b128 v[48:51], v7 offset:18688\n" \
  "v_mfma_f32_16x16x32_bf16 v[32:35], v[148:151], v[52:55], v[32:35]\n" \
  "s_waitcnt lgkmcnt(3)\n" \
  "v_mfma_f32_16x16x32_bf16 v[28:31], v[88:91], v[36:39], v[28:31]\n" \
  "ds_read_b128 v[52:55], v7 offset:20736\n" \
  "v_mfma_f32_16x16x32_bf16 v[32:35], v[152:155], v[36:39], v[32:35]\n" \
  "s_waitcnt lgkmcnt(3)\n" \
  "v_mfma_f32_16x16x32_bf16 v[28:31], v[92:95], v[40:43], v[28:31]\n" \
  "ds_read_b128 v[36:39], v7 offset:22784\n" \
  "v_mfma_f32_16x16x32_bf16 v[32:35], v[156:159], v[40:43], v[32:35]\n" \
  "s_waitcnt lgkmcnt(3)\n" \
  "v_mfma_f32_16x16x32_bf16 v[28:31], v[96:99], v[44:47], v[28:31]\n" \
  "ds_read_b128 v[40:43], v7 offset:24832\n" \
  "v_mfma_f32_16x16x32_bf16 v[32:35], v[160:163], v[44:47], v[32:35]\n" \
  "s_waitcnt lgkmcnt(3)\n" \
  "v_mfma_f32_16x16x32_bf16 v[28:31], v[100:103], v[48:51], v[28:31]\n" \
  "ds_read_b128 v[44:47], v7 offset:26880\n" \
  "v_mfma_f32_16x16x32_bf16 v[32:35], v[164:167], v[48:51], v[32:35]\n" \
  "s_waitcnt lgkmcnt(3)\n" \
  "v_mfma_f32_16x16x32_bf16 v[28:31], v[104:107], v[52:55], v[28:31]\n" \
  "ds_read_b128 v[48:51], v7 offset:28928\n" \
  "v_mfma_f32_16x16x32_bf16 v[32:35], v[168:171], v[52:55], v[32:35]\n" \
  "s_waitcnt lgkmcnt(3)\n" \
  "v_mfma_f32_16x16x32_bf16 v[28:31], v[108:111], v[36:39], v[28:31]\n" \
  "ds_read_b128 v[52:55], v7 offset:30976\n" \
  "v_mfma_f32_16x16x32_bf16 v[32:35], v[172:175], v[36:39], v[32:35]\n" \
  "s_waitcnt lgkmcnt(3)\n" \
  "v_mfma_f32_16x16x32_bf16 v[28:31], v[112:115], v[40:43], v[28:31]\n" \
  "v_mfma_f32_16x16x32_bf16 v[32:35], v[176:179], v[40:43], v[32:35]\n" \
  "v_xor_b32 v7, 0x8000, v7\n" \
  "s_waitcnt lgkmcnt(0)\n" \
  "s_barrier\n" \
  "ds_read_b128 v[36:39], v10 offset:0\n" \
  "ds_read_b128 v[40:43], v7 offset:0\n" \
  "v_mfma_f32_16x16x32_bf16 v[28:31], v[116:119], v[44:47], v[28:31]\n" \
  "v_mfma_f32_16x16x32_bf16 v[32:35], v[180:183], v[44:47], v[32:35]\n" \
  "ds_read_b128 v[44:47], v10 offset:2048\n" \
  "v_mfma_f32_16x16x32_bf16 v[28:31], v[120:123], v[48:51], v[28:31]\n" \
  "v_mfma_f32_16x16x32_bf16 v[32:35], v[184:187], v[48:51], v[32:35]\n" \
  "ds_read_b128 v[48:51], v7 offset:2048\n" \
  "v_mfma_f32_16x16x32_bf16 v[28:31], v[124:127], v[52:55], v[28:31]\n" \
  "v_mfma_f32_16x16x32_bf16 v[32:35], v[188:191], v[52:55], v[32:35]\n" \
  "s_cbranch_scc1 .LQB_LAST_%=\n" \
  "s_branch .LQB_TOP_%=\n" \
  ".LQB_LAST_%=:\n" \
  "s_nop 7\n" \
  "v_mul_f32 v20, v20, v60\n" \
  "v_mul_f32 v21, v21, v60\n" \
  "v_mul_f32 v22, v22, v60\n" \
  "v_cvt_pk_bf16_f32 v20, v20, v21\n" \
  "v_mul_f32 v23, v23, v60\n" \
  "v_mul_f32 v24, v24, v60\n" \
  "v_cvt_pk_bf16_f32 v21, v22, v23\n" \
  "v_mul_f32 v25, v25, v60\n" \
  "v_mul_f32 v26, v26, v60\n" \
  "v_cvt_pk_bf16_f32 v22, v24, v25\n" \
  "v_mul_f32 v27, v27, v60\n" \
  "v_cvt_pk_bf16_f32 v23, v26, v27\n" \
  "buffer_store_dwordx4 v[20:23], v6, s[40:43], s59 offen offset:0 sc1\n" \
  "s_add_u32 s76, s59, 4096\n" \
  "v_mul_f32 v28, v28, v61\n" \
  "v_mul_f32 v29, v29, v61\n" \
  "v_mul_f32 v30, v30, v61\n" \
  "v_cvt_pk_bf16_f32 v28, v28, v29\n" \
  "v_mul_f32 v31, v31, v61\n" \
  "v_mul_f32 v32, v32, v61\n" \
  "v_cvt_pk_bf16_f32 v29, v30, v31\n" \
  "v_mul_f32 v33, v33, v61\n" \
  "v_mul_f32 v34, v34, v61\n" \
  "v_cvt_pk_bf16_f32 v30, v32, v33\n" \
  "v_mul_f32 v35, v35, v61\n" \
  "v_cvt_pk_bf16_f32 v31, v34, v35\n" \
  "buffer_store_dwordx4 v[28:31], v6, s[40:43], s76 offen offset:0 sc1\n" \
  "s_add_u32 s59, s59, 0x80000\n" \
  "s_waitcnt vmcnt(0)\n" \
  "v_mov_b32 v12, s80\n" \
  "v_mov_b32 v13, 1\n" \
  "s_mov_b64 exec, 1\n" \
  "ds_add_u32 v12, v13\n" \
  "s_mov_b64 exec, -1\n" \
  "s_waitcnt lgkmcnt(0)\n" \
  "s_endpgm\n"
#define AW_ASM_0 \
  "ds_write_b128 v7, v[8:11] offset:28672\n" \
  "ds_write_b128 v7, v[12:15] offset:29696\n" \
  ".p2align 6\n" \
  ".LWA_AL_%=:\n" \
  ".LWA_TOP_%=:\n" \
  "ds_read_b128 v[64:67], v7 offset:28672\n" \
  "ds_read_b128 v[68:71], v7 offset:29696\n" \
  "s_barrier\n" \
  "s_setprio 3\n" \
  "ds_read_b128 v[16:19], v4 offset:0\n" \
  "ds_read_b128 v[32:35], v3 offset:8192\n" \
  "ds_read_b128 v[48:51], v3 offset:12288\n" \
  "ds_read_b128 v[20:23], v4 offset:1024\n" \
  "ds_read_b128 v[36:39], v3 offset:9216\n" \
  "ds_read_b128 v[52:55], v3 offset:13312\n" \
  "ds_read_b128 v[24:27], v4 offset:2048\n" \
  "ds_read_b128 v[40:43], v3 offset:10240\n" \
  "ds_read_b128 v[56:59], v3 offset:14336\n" \
  "ds_read_b128 v[28:31], v4 offset:3072\n" \
  "ds_read_b128 v[44:47], v3 offset:11264\n" \
  "ds_read_b128 v[60:63], v3 offset:15360\n" \
  "s_waitcnt lgkmcnt(10)\n" \
  "v_mfma_f32_16x16x32_bf16 v[64:67], v[32:35], v[16:19], v[64:67]\n" \
  "ds_read_b128 v[72:75], v3 offset:16384\n" \
  "s_waitcnt lgkmcnt(10)\n" \
  "v_mfma_f32_16x16x32_bf16 v[68:71], v[48:51], v[16:19], v[68:71]\n" \
  "ds_read_b128 v[76:79], v3 offset:17408\n" \
  "s_waitcnt lgkmcnt(9)\n" \
  "v_mfma_f32_16x16x32_bf16 v[64:67], v[36:39], v[20:23], v[64:67]\n" \
  "ds_read_b128 v[80:83], v3 offset:18432\n" \
  "s_waitcnt lgkmcnt(9)\n" \
  "v_mfma_f32_16x16x32_bf16 v[68:71], v[52:55], v[20:23], v[68:71]\n" \
  "ds_read_b128 v[84:87], v3 offset:19456\n" \
  "s_waitcnt lgkmcnt(8)\n" \
  "v_mfma_f32_16x16x32_bf16 v[64:67], v[40:43], v[24:27], v[64:67]\n" \
  "ds_read_b128 v[88:91], v3 offset:20480\n" \
  "s_waitcnt lgkmcnt(8)\n" \
  "v_mfma_f32_16x16x32_bf16 v[68:71], v[56:59], v[24:27], v[68:71]\n" \
  "ds_read_b128 v[92:95], v3 offset:21504\n" \
  "s_waitcnt lgkmcnt(7)\n" \
  "v_mfma_f32_16x16x32_bf16 v[64:67], v[44:47], v[28:31], v[64:67]\n" \
  "ds_read_b128 v[96:99], v3 offset:22528\n" \
  "s_waitcnt lgkmcnt(7)\n" \
  "v_mfma_f32_16x16x32_bf16 v[68:71], v[60:63], v[28:31], v[68:71]\n" \
  "ds_read_b128 v[100:103], v3 offset:23552\n" \
  "s_add_u32 s56, s56, 3\n" \
  "s_sub_u32 s68, s56, %[wd]\n" \
  "s_bitcmp1_b32 s68, 0\n" \
  "s_cselect_b32 s58, 64, %[wd]\n" \
  "v_maximum3_f32 v10, v64, v65, v66\n" \
  "s_lshr_b32 s70, s68, 1\n" \
  "s_add_u32 s58, s58, s70\n" \
  "v_maximum3_f32 v10, v10, v67, v68\n" \
  "v_maximum3_f32 v10, v10, v69, v70\n" \
  "v_max_f32 v8, v10, v71\n" \
  "v_mov_b32 v9, v8\n" \
  "s_cmp_lt_u32 s68, %[w2]\n" \
  "s_cselect_b32 s58, s58, s56\n" \
  "v_permlane16_swap_b32 v8, v9\n" \
  "v_max_f32 v8, v8, v9\n" \
  "v_mov_b32 v9, v8\n" \
  "s_min_u32 s58, s58, 128\n" \
  "s_nop 0\n" \
  "v_permlane32_swap_b32 v8, v9\n" \
  "v_max_f32 v8, v8, v9\n" \
  "v_mul_f32 v10, s80, v8\n" \
  "v_pk_fma_f32 v[64:65], v[64:65], s[80:81], v[10:11] op_sel_hi:[1,0,0] neg_lo:[0,0,1] neg_hi:[0,0,1]\n" \
  "v_pk_fma_f32 v[66:67], v[66:67], s[80:81], v[10:11] op_sel_hi:[1,0,0] neg_lo:[0,0,1] neg_hi:[0,0,1]\n" \
  "v_pk_fma_f32 v[68:69], v[68:69], s[80:81], v[10:11] op_sel_hi:[1,0,0] neg_lo:[0,0,1] neg_hi:[0,0,1]\n" \
  "v_pk_fma_f32 v[70:71], v[70:71], s[80:81], v[10:11] op_sel_hi:[1,0,0] neg_lo:[0,0,1] neg_hi:[0,0,1]\n" \
  "v_exp_f32 v64, v64\n" \
  "v_exp_f32 v65, v65\n" \
  "v_exp_f32 v66, v66\n" \
  "v_exp_f32 v67, v67\n" \
  "v_exp_f32 v68, v68\n" \
  "v_exp_f32 v69, v69\n" \
  "v_exp_f32 v70, v70\n" \
  "v_exp_f32 v71, v71\n" \
  "v_cvt_pk_bf16_f32 v104, v64, v65\n" \
  "v_cvt_pk_bf16_f32 v105, v66, v67\n" \
  "v_cvt_pk_bf16_f32 v106, v68, v69\n" \
  "v_cvt_pk_bf16_f32 v107, v70, v71\n" \
  "v_pk_add_f32 v[64:65], v[64:65], v[66:67]\n" \
  "v_pk_add_f32 v[68:69], v[68:69], v[70:71]\n" \
  "s_waitcnt lgkmcnt(0)\n" \
  "v_pk_add_f32 v[64:65], v[64:65], v[68:69]\n" \
  "v_mfma_f32_16x16x32_bf16 v[32:35], v[72:75], v[104:107], 0\n" \
  "v_mfma_f32_16x16x32_bf16 v[48:51], v[88:91], v[104:107], 0\n" \
  "v_mfma_f32_16x16x32_bf16 v[36:39], v[76:79], v[104:107], 0\n" \
  "v_mfma_f32_16x16x32_bf16 v[52:55], v[92:95], v[104:107], 0\n" \
  "v_mfma_f32_16x16x32_bf16 v[40:43], v[80:83], v[104:107], 0\n" \
  "v_mfma_f32_16x16x32_bf16 v[56:59], v[96:99], v[104:107], 0\n" \
  "v_mfma_f32_16x16x32_bf16 v[44:47], v[84:87], v[104:107], 0\n" \
  "v_mfma_f32_16x16x32_bf16 v[60:63], v[100:103], v[104:107], 0\n" \
  "v_add_f32 v11, v64, v65\n" \
  "s_cmp_lt_u32 s56, %[w3]\n" \
  "s_cselect_b32 s68, s57, 0\n" \
  "s_cmp_ge_u32 s68, 64\n" \
  "s_cbranch_scc1 .LWA_EASY_%=\n" \
  "v_cvt_pk_bf16_f32 v108, v32, v33\n" \
  "v_cvt_pk_bf16_f32 v109, v48, v49\n" \
  "v_cvt_pk_bf16_f32 v110, v34, v35\n" \
  "v_cvt_pk_bf16_f32 v111, v50, v51\n" \
  "ds_write_b128 v7, v[108:111] offset:0\n" \
  "v_cvt_pk_bf16_f32 v112, v36, v37\n" \
  "v_cvt_pk_bf16_f32 v113, v52, v53\n" \
  "v_cvt_pk_bf16_f32 v114, v38, v39\n" \
  "v_cvt_pk_bf16_f32 v115, v54, v55\n" \
  "ds_write_b128 v7, v[112:115] offset:1024\n" \
  "v_cvt_pk_bf16_f32 v116, v40, v41\n" \
  "v_cvt_pk_bf16_f32 v117, v56, v57\n" \
  "v_cvt_pk_bf16_f32 v118, v42, v43\n" \
  "v_cvt_pk_bf16_f32 v119, v58, v59\n" \
  "ds_write_b128 v7, v[116:119] offset:2048\n" \
  "v_cvt_pk_bf16_f32 v120, v44, v45\n" \
  "v_cvt_pk_bf16_f32 v121, v60, v61\n" \
  "v_cvt_pk_bf16_f32 v122, v46, v47\n" \
  "v_cvt_pk_bf16_f32 v123, v62, v63\n" \
  "ds_write_b128 v7, v[120:123] offset:3072\n" \
  "s_waitcnt vmcnt(0)\n" \
  "s_waitcnt lgkmcnt(0)\n" \
  "s_barrier\n" \
  "s_setprio 2\n" \
  "ds_read_b128 v[48:51], v1 offset:0\n" \
  "ds_read_b128 v[52:55], v1 offset:1024\n" \
  "ds_read_b128 v[56:59], v1 offset:2048\n" \
  "s_waitcnt lgkmcnt(2)\n" \
  "v_mfma_f32_16x16x32_bf16 v[64:67], v[128:131], v[48:51], 0\n" \
  "ds_read_b128 v[60:63], v1 offset:3072\n" \
  "s_lshl_b32 s75, s57, 19\n" \
  "v_mfma_f32_16x16x32_bf16 v[68:71], v[160:163], v[48:51], 0\n" \
  "s_add_u32 s75, s75, s60\n" \
  "v_mfma_f32_16x16x32_bf16 v[72:75], v[192:195], v[48:51], 0\n" \
  "s_add_u32 s76, s75, 16384\n" \
  "v_mfma_f32_16x16x32_bf16 v[76:79], v[224:227], v[48:51], 0\n" \
  "s_waitcnt lgkmcnt(2)\n" \
  "v_mfma_f32_16x16x32_bf16 v[64:67], v[132:135], v[52:55], v[64:67]\n" \
  "ds_read_b128 v[48:51], v1 offset:8192\n" \
  "v_mfma_f32_16x16x32_bf16 v[68:71], v[164:167], v[52:55], v[68:71]\n" \
  "v_mfma_f32_16x16x32_bf16 v[72:75], v[196:199], v[52:55], v[72:75]\n" \
  "v_mfma_f32_16x16x32_bf16 v[76:79], v[228:231], v[52:55], v[76:79]\n" \
  "s_waitcnt lgkmcnt(2)\n" \
  "v_mfma_f32_16x16x32_bf16 v[64:67], v[136:139], v[56:59], v[64:67]\n" \
  "ds_read_b128 v[52:55], v1 offset:9216\n" \
  "v_mfma_f32_16x16x32_bf16 v[68:71], v[168:171], v[56:59], v[68:71]\n" \
  "v_mfma_f32_16x16x32_bf16 v[72:75], v[200:203], v[56:59], v[72:75]\n" \
  "v_mfma_f32_16x16x32_bf16 v[76:79], v[232:235], v[56:59], v[76:79]\n" \
  "s_waitcnt lgkmcnt(2)\n" \
  "v_mfma_f32_16x16x32_bf16 v[64:67], v[140:143], v[60:63], v[64:67]\n" \
  "ds_read_b128 v[56:59], v1 offset:10240\n" \
  "v_mfma_f32_16x16x32_bf16 v[68:71], v[172:175], v[60:63], v[68:71]\n" \
  "v_mfma_f32_16x16x32_bf16 v[72:75], v[204:207], v[60:63], v[72:75]\n" \
  "v_mfma_f32_16x16x32_bf16 v[76:79], v[236:239], v[60:63], v[76:79]\n" \
  "s_waitcnt lgkmcnt(2)\n" \
  "v_mfma_f32_16x16x32_bf16 v[96:99], v[144:147], v[48:51], 0\n" \
  "ds_read_b128 v[60:63], v1 offset:11264\n" \
  "v_mfma_f32_16x16x32_bf16 v[100:103], v[176:179], v[48:51], 0\n" \
  "v_mfma_f32_16x16x32_bf16 v[104:107], v[208:211], v[48:51], 0\n" \
  "v_mfma_f32_16x16x32_bf16 v[108:111], v[240:243], v[48:51], 0\n" \
  "s_waitcnt lgkmcnt(2)\n" \
  "v_mfma_f32_16x16x32_bf16 v[96:99], v[148:151], v[52:55], v[96:99]\n" \
  "ds_read_b128 v[48:51], v1 offset:4096\n" \
  "v_mfma_f32_16x16x32_bf16 v[100:103], v[180:183], v[52:55], v[100:103]\n" \
  "v_mfma_f32_16x16x32_bf16 v[104:107], v[212:215], v[52:55], v[104:107]\n" \
  "v_mfma_f32_16x16x32_bf16 v[108:111], v[244:247], v[52:55], v[108:111]\n" \
  "s_waitcnt lgkmcnt(2)\n" \
  "v_mfma_f32_16x16x32_bf16 v[96:99], v[152:155], v[56:59], v[96:99]\n" \
  "ds_read_b128 v[52:55], v1 offset:5120\n" \
  "v_mfma_f32_16x16x32_bf16 v[100:103], v[184:187], v[56:59], v[100:103]\n" \
  "v_mfma_f32_16x16x32_bf16 v[104:107], v[216:219], v[56:59], v[104:107]\n" \
  "v_mfma_f32_16x16x32_bf16 v[108:111], v[248:251], v[56:59], v[108:111]\n" \
  "s_waitcnt lgkmcnt(2)\n" \
  "v_mfma_f32_16x16x32_bf16 v[96:99], v[156:159], v[60:63], v[96:99]\n" \
  "ds_read_b128 v[56:59], v1 offset:6144\n" \
  "v_mfma_f32_16x16x32_bf16 v[100:103], v[188:191], v[60:63], v[100:103]\n" \
  "v_mfma_f32_16x16x32_bf16 v[104:107], v[220:223], v[60:63], v[104:107]\n" \
  "v_mfma_f32_16x16x32_bf16 v[108:111], v[252:255], v[60:63], v[108:111]\n" \
  "s_waitcnt lgkmcnt(2)\n" \
  "v_mfma_f32_16x16x32_bf16 v[80:83], v[128:131], v[48:51], 0\n" \
  "ds_read_b128 v[60:63], v1 offset:7168\n" \
  "v_mfma_f32_16x16x32_bf16 v[84:87], v[160:163], v[48:51], 0\n" \
  "v_mfma_f32_16x16x32_bf16 v[88:91], v[192:195], v[48:51], 0\n" \
  "v_mfma_f32_16x16x32_bf16 v[92:95], v[224:227], v[48:51], 0\n" \
  "s_waitcnt lgkmcnt(2)\n" \
  "v_mfma_f32_16x16x32_bf16 v[80:83], v[132:135], v[52:55], v[80:83]\n" \
  "ds_read_b128 v[48:51], v1 offset:12288\n" \
  "v_mfma_f32_16x16x32_bf16 v[84:87], v[164:167], v[52:55], v[84:87]\n" \
  "v_mfma_f32_16x16x32_bf16 v[88:91], v[196:199], v[52:55], v[88:91]\n" \
  "v_mfma_f32_16x16x32_bf16 v[92:95], v[228:231], v[52:55], v[92:95]\n" \
  "s_waitcnt lgkmcnt(2)\n" \
  "v_mfma_f32_16x16x32_bf16 v[80:83], v[136:139], v[56:59], v[80:83]\n" \
  "ds_read_b128 v[52:55], v1 offset:13312\n" \
  "v_mfma_f32_16x16x32_bf16 v[84:87], v[168:171], v[56:59], v[84:87]\n" \
  "v_mfma_f32_16x16x32_bf16 v[88:91], v[200:203], v[56:59], v[88:91]\n" \
  "v_mfma_f32_16x16x32_bf16 v[92:95], v[232:235], v[56:59], v[92:95]\n" \
  "s_waitcnt lgkmcnt(2)\n" \
  "v_mfma_f32_16x16x32_bf16 v[80:83], v[140:143], v[60:63], v[80:83]\n" \
  "ds_read_b128 v[56:59], v1 offset:14336\n" \
  "v_mfma_f32_16x16x32_bf16 v[84:87], v[172:175], v[60:63], v[84:87]\n" \
  "v_mfma_f32_16x16x32_bf16 v[88:91], v[204:207], v[60:63], v[88:91]\n" \
  "v_mfma_f32_16x16x32_bf16 v[92:95], v[236:239], v[60:63], v[92:95]\n" \
  "s_waitcnt lgkmcnt(2)\n" \
  "v_mfma_f32_16x16x32_bf16 v[112:115], v[144:147], v[48:51], 0\n" \
  "ds_read_b128 v[60:63], v1 offset:15360\n" \
  "v_mfma_f32_16x16x32_bf16 v[116:119], v[176:179], v[48:51], 0\n" \
  "v_mfma_f32_16x16x32_bf16 v[120:123], v[208:211], v[48:51], 0\n" \
  "v_mfma_f32_16x16x32_bf16 v[124:127], v[240:243], v[48:51], 0\n" \
  "s_waitcnt lgkmcnt(2)\n" \
  "v_mfma_f32_16x16x32_bf16 v[112:115], v[148:151], v[52:55], v[112:115]\n" \
  "v_mfma_f32_16x16x32_bf16 v[116:119], v[180:183], v[52:55], v[116:119]\n" \
  "v_mfma_f32_16x16x32_bf16 v[120:123], v[212:215], v[52:55], v[120:123]\n" \
  "v_mfma_f32_16x16x32_bf16 v[124:127], v[244:247], v[52:55], v[124:127]\n" \
  "s_waitcnt lgkmcnt(1)\n" \
  "v_mfma_f32_16x16x32_bf16 v[112:115], v[152:155], v[56:59], v[112:115]\n" \
  "v_mfma_f32_16x16x32_bf16 v[116:119], v[184:187], v[56:59], v[116:119]\n" \
  "v_mfma_f32_16x16x32_bf16 v[120:123], v[216:219], v[56:59], v[120:123]\n" \
  "v_mfma_f32_16x16x32_bf16 v[124:127], v[248:251], v[56:59], v[124:127]\n" \
  "s_waitcnt lgkmcnt(0)\n" \
  "v_mfma_f32_16x16x32_bf16 v[112:115], v[156:159], v[60:63], v[112:115]\n" \
  "v_mfma_f32_16x16x32_bf16 v[116:119], v[188:191], v[60:63], v[116:119]\n" \
  "v_mfma_f32_16x16x32_bf16 v[120:123], v[220:223], v[60:63], v[120:123]\n" \
  "v_mfma_f32_16x16x32_bf16 v[124:127], v[252:255], v[60:63], v[124:127]\n" \
  "s_setprio 3\n" \
  "s_barrier\n" \
  "ds_read_b128 v[32:35], v5 offset:0\n" \
  "ds_read_b128 v[36:39], v5 offset:512\n" \
  "ds_read_b128 v[40:43], v5 offset:1024\n" \
  "ds_read_b128 v[44:47], v5 offset:1536\n" \
  "ds_read_b64_tr_b16 v[48:49], v6 offset:0\n" \
  "ds_read_b64_tr_b16 v[50:51], v6 offset:32\n" \
  "ds_read_b64_tr_b16 v[52:53], v6 offset:1024\n" \
  "ds_read_b64_tr_b16 v[54:55], v6 offset:1056\n" \
  "ds_read_b64_tr_b16 v[56:57], v6 offset:4096\n" \
  "ds_read_b64_tr_b16 v[58:59], v6 offset:4128\n" \
  "ds_read_b64_tr_b16 v[60:61], v6 offset:5120\n" \
  "ds_read_b64_tr_b16 v[62:63], v6 offset:5152\n" \
  "s_waitcnt lgkmcnt(11)\n" \
  "v_mfma_f32_16x16x32_bf16 v[12:15], v[32:35], v[16:19], 0\n" \
  "s_waitcnt lgkmcnt(10)\n" \
  "v_mfma_f32_16x16x32_bf16 v[12:15], v[36:39], v[20:23], v[12:15]\n" \
  "s_waitcnt lgkmcnt(9)\n" \
  "v_mfma_f32_16x16x32_bf16 v[12:15], v[40:43], v[24:27], v[12:15]\n" \
  "s_waitcnt lgkmcnt(8)\n" \
  "v_mfma_f32_16x16x32_bf16 v[12:15], v[44:47], v[28:31], v[12:15]\n" \
  "v_mov_b64 v[16:17], 0\n" \
  "v_mov_b64 v[18:19], 0\n" \
  "v_mov_b64 v[20:21], 0\n" \
  "v_mov_b64 v[22:23], 0\n" \
  "v_mov_b64 v[24:25], 0\n" \
  "v_mov_b64 v[26:27], 0\n" \
  "v_mov_b64 v[28:29], 0\n" \
  "v_mov_b64 v[30:31], 0\n" \
  "v_maximum3_f32 v8, v8, v12, v13\n" \
  "v_mov_b32 v9, v8\n" \
  "v_mov_b64 v[32:33], 0\n" \
  "v_mov_b64 v[34:35], 0\n" \
  "v_permlane16_swap_b32 v8, v9\n" \
  "v_max_f32 v8, v8, v9\n" \
  "v_mov_b32 v9, v8\n" \
  "v_mov_b64 v[36:37], 0\n" \
  "v_mov_b64 v[38:39], 0\n" \
  "v_permlane32_swap_b32 v8, v9\n" \
  "v_max_f32 v8, v8, v9\n" \
  "v_mul_f32 v8, s80, v8\n" \
  "v_sub_f32 v14, v10, v8\n" \
  "v_fma_f32 v12, v12, s80, -v8\n" \
  "v_fma_f32 v13, v13, s80, -v8\n" \
  "v_exp_f32 v14, v14\n" \
  "v_exp_f32 v12, v12\n" \
  "v_exp_f32 v13, v13\n" \
  "v_fma_f32 v11, v11, v14, v12\n" \
  "v_add_f32 v11, v11, v13\n" \
  "v_mov_b32 v9, v11\n" \
  "v_mov_b64 v[40:41], 0\n" \
  "v_mov_b64 v[42:43], 0\n" \
  "v_permlane16_swap_b32 v11, v9\n" \
  "v_add_f32 v11, v11, v9\n" \
  "v_mov_b32 v9, v11\n" \
  "v_mov_b64 v[44:45], 0\n" \
  "v_mov_b64 v[46:47], 0\n" \
  "v_permlane32_swap_b32 v11, v9\n" \
  "v_add_f32 v11, v11, v9\n" \
  "v_rcp_f32 v15, v11\n" \
  "v_add_u32 v9, s66, v1\n" \
  "v_mul_f32 v12, v12, v15\n" \
  "v_mul_f32 v13, v13, v15\n" \
  "v_mul_f32 v14, v14, v15\n" \
  "v_cvt_pk_bf16_f32 v12, v12, v13\n" \
  "ds_write2_b32 v9, v12, v14 offset1:2\n" \
  "s_waitcnt lgkmcnt(0)\n" \
  "s_barrier\n" \
  "ds_read_b128 v[8:11], v1 offset:24576\n" \
  "ds_read_b128 v[12:15], v1 offset:25600\n" \
  "s_setprio 0\n" \
  "v_smfmac_f32_16x16x64_bf16 v[16:19], v[48:51], v[128:135], v0\n" \
  "v_smfmac_f32_16x16x64_bf16 v[20:23], v[48:51], v[160:167], v0\n" \
  "v_smfmac_f32_16x16x64_bf16 v[24:27], v[48:51], v[192:199], v0\n" \
  "v_smfmac_f32_16x16x64_bf16 v[28:31], v[48:51], v[224:231], v0\n" \
  "v_smfmac_f32_16x16x64_bf16 v[16:19], v[52:55], v[136:143], v0\n" \
  "v_smfmac_f32_16x16x64_bf16 v[20:23], v[52:55], v[168:175], v0\n" \
  "v_smfmac_f32_16x16x64_bf16 v[24:27], v[52:55], v[200:207], v0\n" \
  "v_smfmac_f32_16x16x64_bf16 v[28:31], v[52:55], v[232:239], v0\n" \
  "v_smfmac_f32_16x16x64_bf16 v[32:35], v[56:59], v[144:151], v0\n" \
  "s_waitcnt lgkmcnt(0)\n" \
  "v_pk_mul_f32 v[64:65], v[64:65], v[10:11] op_sel_hi:[1,0]\n" \
  "v_pk_mul_f32 v[66:67], v[66:67], v[10:11] op_sel_hi:[1,0]\n" \
  "v_smfmac_f32_16x16x64_bf16 v[36:39], v[56:59], v[176:183], v0\n" \
  "v_pk_mul_f32 v[68:69], v[68:69], v[10:11] op_sel_hi:[1,0]\n" \
  "v_pk_mul_f32 v[70:71], v[70:71], v[10:11] op_sel_hi:[1,0]\n" \
  "v_pk_fma_f32 v[64:65], v[96:97], v[10:11], v[64:65] op_sel:[0,1,0]\n" \
  "v_smfmac_f32_16x16x64_bf16 v[40:43], v[56:59], v[208:215], v0\n" \
  "v_pk_fma_f32 v[66:67], v[98:99], v[10:11], v[66:67] op_sel:[0,1,0]\n" \
  "v_pk_fma_f32 v[68:69], v[100:101], v[10:11], v[68:69] op_sel:[0,1,0]\n" \
  "v_pk_fma_f32 v[70:71], v[102:103], v[10:11], v[70:71] op_sel:[0,1,0]\n" \
  "v_smfmac_f32_16x16x64_bf16 v[44:47], v[56:59], v[240:247], v0\n" \
  "v_pk_mul_f32 v[72:73], v[72:73], v[10:11] op_sel_hi:[1,0]\n" \
  "v_pk_mul_f32 v[74:75], v[74:75], v[10:11] op_sel_hi:[1,0]\n" \
  "v_pk_mul_f32 v[76:77], v[76:77], v[10:11] op_sel_hi:[1,0]\n" \
  "v_smfmac_f32_16x16x64_bf16 v[32:35], v[60:63], v[152:159], v0\n" \
  "v_pk_mul_f32 v[78:79], v[78:79], v[10:11] op_sel_hi:[1,0]\n" \
  "v_pk_fma_f32 v[72:73], v[104:105], v[10:11], v[72:73] op_sel:[0,1,0]\n" \
  "v_pk_fma_f32 v[74:75], v[106:107], v[10:11], v[74:75] op_sel:[0,1,0]\n" \
  "v_smfmac_f32_16x16x64_bf16 v[36:39], v[60:63], v[184:191], v0\n" \
  "v_pk_fma_f32 v[76:77], v[108:109], v[10:11], v[76:77] op_sel:[0,1,0]\n" \
  "v_pk_fma_f32 v[78:79], v[110:111], v[10:11], v[78:79] op_sel:[0,1,0]\n" \
  "v_pk_mul_f32 v[80:81], v[80:81], v[14:15] op_sel_hi:[1,0]\n" \
  "v_smfmac_f32_16x16x64_bf16 v[40:43], v[60:63], v[216:223], v0\n" \
  "v_pk_mul_f32 v[82:83], v[82:83], v[14:15] op_sel_hi:[1,0]\n" \
  "v_pk_mul_f32 v[84:85], v[84:85], v[14:15] op_sel_hi:[1,0]\n" \
  "v_pk_mul_f32 v[86:87], v[86:87], v[14:15] op_sel_hi:[1,0]\n" \
  "v_smfmac_f32_16x16x64_bf16 v[44:47], v[60:63], v[248:255], v0\n" \
  "v_pk_fma_f32 v[80:81], v[112:113], v[14:15], v[80:81] op_sel:[0,1,0]\n" \
  "v_pk_fma_f32 v[82:83], v[114:115], v[14:15], v[82:83] op_sel:[0,1,0]\n" \
  "v_pk_fma_f32 v[84:85], v[116:117], v[14:15], v[84:85] op_sel:[0,1,0]\n" \
  "v_pk_fma_f32 v[86:87], v[118:119], v[14:15], v[86:87] op_sel:[0,1,0]\n" \
  "v_pk_mul_f32 v[88:89], v[88:89], v[14:15] op_sel_hi:[1,0]\n" \
  "v_pk_mul_f32 v[90:91], v[90:91], v[14:15] op_sel_hi:[1,0]\n" \
  "v_pk_mul_f32 v[92:93], v[92:93], v[14:15] op_sel_hi:[1,0]\n" \
  "v_pk_mul_f32 v[94:95], v[94:95], v[14:15] op_sel_hi:[1,0]\n" \
  "v_pk_fma_f32 v[88:89], v[120:121], v[14:15], v[88:89] op_sel:[0,1,0]\n" \
  "v_pk_fma_f32 v[90:91], v[122:123], v[14:15], v[90:91] op_sel:[0,1,0]\n" \
  "v_pk_fma_f32 v[92:93], v[124:125], v[14:15], v[92:93] op_sel:[0,1,0]\n" \
  "v_pk_fma_f32 v[94:95], v[126:127], v[14:15], v[94:95] op_sel:[0,1,0]\n" \
  "v_pk_add_f32 v[16:17], v[16:17], v[18:19]\n" \
  "v_pk_add_f32 v[32:33], v[32:33], v[34:35]\n" \
  "v_cvt_pk_bf16_f32 v16, v16, v17\n" \
  "v_cvt_pk_bf16_f32 v17, v32, v33\n" \
  "v_pk_add_f32 v[20:21], v[20:21], v[22:23]\n" \
  "v_pk_add_f32 v[36:37], v[36:37], v[38:39]\n" \
  "v_cvt_pk_bf16_f32 v20, v20, v21\n" \
  "v_cvt_pk_bf16_f32 v21, v36, v37\n" \
  "v_pk_add_f32 v[24:25], v[24:25], v[26:27]\n" \
  "v_pk_add_f32 v[40:41], v[40:41], v[42:43]\n" \
  "v_mfma_f32_16x16x16_bf16 v[64:67], v[16:17], v[8:9], v[64:67]\n" \
  "v_mfma_f32_16x16x16_bf16 v[68:71], v[20:21], v[8:9], v[68:71]\n" \
  "v_cvt_pk_bf16_f32 v24, v24, v25\n" \
  "v_cvt_pk_bf16_f32 v25, v40, v41\n" \
  "v_mfma_f32_16x16x16_bf16 v[80:83], v[16:17], v[12:13], v[80:83]\n" \
  "v_mfma_f32_16x16x16_bf16 v[84:87], v[20:21], v[12:13], v[84:87]\n" \
  "v_pk_add_f32 v[28:29], v[28:29], v[30:31]\n" \
  "v_pk_add_f32 v[44:45], v[44:45], v[46:47]\n" \
  "v_cvt_pk_bf16_f32 v28, v28, v29\n" \
  "v_cvt_pk_bf16_f32 v29, v44, v45\n" \
  "s_nop 1\n" \
  "v_mfma_f32_16x16x16_bf16 v[72:75], v[24:25], v[8:9], v[72:75]\n" \
  "v_mfma_f32_16x16x16_bf16 v[76:79], v[28:29], v[8:9], v[76:79]\n" \
  "v_mfma_f32_16x16x16_bf16 v[88:91], v[24:25], v[12:13], v[88:91]\n" \
  "v_mfma_f32_16x16x16_bf16 v[92:95], v[28:29], v[12:13], v[92:95]\n" \
  "v_cvt_pk_bf16_f32 v64, v64, v65\n" \
  "v_cvt_pk_bf16_f32 v65, v66, v67\n" \
  "v_cvt_pk_bf16_f32 v66, v68, v69\n" \
  "v_cvt_pk_bf16_f32 v67, v70, v71\n" \
  "buffer_store_dwordx4 v[64:67], v2, s[40:43], s75 offen offset:0 sc1\n" \
  "v_cvt_pk_bf16_f32 v80, v80, v81\n" \
  "v_cvt_pk_bf16_f32 v81, v82, v83\n" \
  "v_cvt_pk_bf16_f32 v82, v84, v85\n" \
  "v_cvt_pk_bf16_f32 v83, v86, v87\n" \
  "buffer_store_dwordx4 v[80:83], v2, s[40:43], s76 offen offset:0 sc1\n" \
  "v_cvt_pk_bf16_f32 v72, v72, v73\n" \
  "v_cvt_pk_bf16_f32 v73, v74, v75\n" \
  "v_cvt_pk_bf16_f32 v74, v76, v77\n" \
  "v_cvt_pk_bf16_f32 v75, v78, v79\n" \
  "buffer_store_dwordx4 v[72:75], v2, s[40:43], s75 offen offset:128 sc1\n" \
  "v_cvt_pk_bf16_f32 v88, v88, v89\n" \
  "v_cvt_pk_bf16_f32 v89, v90, v91\n" \
  "v_cvt_pk_bf16_f32 v90, v92, v93\n" \
  "v_cvt_pk_bf16_f32 v91, v94, v95\n" \
  "buffer_store_dwordx4 v[88:91], v2, s[40:43], s76 offen offset:128 sc1\n" \
  ".LWA_NEXT_%=:\n" \
  "v_xor_b32 v3, 0x10000, v3\n" \
  "v_xor_b32 v4, 0x10000, v4\n" \
  "s_mov_b32 s57, s58\n" \
  "s_cmp_eq_u32 s57, 128\n" \
  "s_cbranch_scc1 .LWA_END_%=\n" \
  "s_branch .LWA_TOP_%=\n" \
  ".LWA_END_%=:\n" \
  "s_endpgm\n" \
  ".LWA_EASY_%=:\n" \
  "v_lshrrev_b32 v9, 1, v1\n" \
  "v_mov_b32 v105, 0\n" \
  "v_add_u32 v64, s63, v9\n" \
  "v_mov_b32 v106, 0\n" \
  "s_barrier\n" \
  "ds_read_b128 v[72:75], v5 offset:0\n" \
  "ds_read_b128 v[76:79], v5 offset:512\n" \
  "ds_read_b128 v[80:83], v5 offset:1024\n" \
  "ds_read_b128 v[84:87], v5 offset:1536\n" \
  "ds_read_b64 v[88:89], v64 offset:0\n" \
  "ds_read_b64 v[90:91], v64 offset:512\n" \
  "ds_read_b64 v[92:93], v64 offset:1024\n" \
  "ds_read_b64 v[94:95], v64 offset:1536\n" \
  "s_waitcnt lgkmcnt(7)\n" \
  "v_mfma_f32_16x16x32_bf16 v[64:67], v[72:75], v[16:19], 0\n" \
  "s_waitcnt lgkmcnt(6)\n" \
  "v_mfma_f32_16x16x32_bf16 v[64:67], v[76:79], v[20:23], v[64:67]\n" \
  "s_waitcnt lgkmcnt(5)\n" \
  "v_mfma_f32_16x16x32_bf16 v[64:67], v[80:83], v[24:27], v[64:67]\n" \
  "s_waitcnt lgkmcnt(4)\n" \
  "v_mfma_f32_16x16x32_bf16 v[64:67], v[84:87], v[28:31], v[64:67]\n" \
  "s_nop 7\n" \
  "v_maximum3_f32 v8, v8, v64, v65\n" \
  "v_mov_b32 v9, v8\n" \
  "s_nop 1\n" \
  "v_permlane16_swap_b32 v8, v9\n" \
  "v_max_f32 v8, v8, v9\n" \
  "v_mov_b32 v9, v8\n" \
  "s_nop 1\n" \
  "v_permlane32_swap_b32 v8, v9\n" \
  "v_max_f32 v8, v8, v9\n" \
  "v_mul_f32 v8, s80, v8\n" \
  "v_sub_f32 v66, v10, v8\n" \
  "v_fma_f32 v64, v64, s80, -v8\n" \
  "v_fma_f32 v65, v65, s80, -v8\n" \
  "v_exp_f32 v66, v66\n" \
  "v_exp_f32 v64, v64\n" \
  "v_exp_f32 v65, v65\n" \
  "v_fma_f32 v11, v11, v66, v64\n" \
  "v_add_f32 v11, v11, v65\n" \
  "v_mov_b32 v9, v11\n" \
  "s_nop 1\n" \
  "v_permlane16_swap_b32 v11, v9\n" \
  "v_add_f32 v11, v11, v9\n" \
  "v_mov_b32 v9, v11\n" \
  "s_nop 1\n" \
  "v_permlane32_swap_b32 v11, v9\n" \
  "v_add_f32 v11, v11, v9\n" \
  "v_rcp_f32 v68, v11\n" \
  "s_waitcnt lgkmcnt(0)\n" \
  "v_mul_f32 v66, v66, v68\n" \
  "v_mul_f32 v64, v64, v68\n" \
  "v_mul_f32 v65, v65, v68\n" \
  "v_pk_mul_f32 v[32:33], v[32:33], v[66:67] op_sel_hi:[1,0]\n" \
  "v_cvt_pk_bf16_f32 v104, v64, v65\n" \
  "v_pk_mul_f32 v[34:35], v[34:35], v[66:67] op_sel_hi:[1,0]\n" \
  "v_cvt_pk_bf16_f32 v107, v64, v65\n" \
  "v_mul_f32 v48, v48, v66\n" \
  "v_mul_f32 v49, v49, v66\n" \
  "v_mul_f32 v50, v50, v66\n" \
  "v_mul_f32 v51, v51, v66\n" \
  "v_mfma_f32_16x16x16_bf16 v[32:35], v[88:89], v[104:105], v[32:35]\n" \
  "v_mul_f32 v36, v36, v66\n" \
  "v_mul_f32 v37, v37, v66\n" \
  "v_mul_f32 v38, v38, v66\n" \
  "v_mul_f32 v39, v39, v66\n" \
  "v_mfma_f32_16x16x16_bf16 v[48:51], v[88:89], v[106:107], v[48:51]\n" \
  "v_mul_f32 v52, v52, v66\n" \
  "v_mul_f32 v53, v53, v66\n" \
  "v_mul_f32 v54, v54, v66\n" \
  "v_mul_f32 v55, v55, v66\n" \
  "v_mfma_f32_16x16x16_bf16 v[36:39], v[90:91], v[104:105], v[36:39]\n" \
  "v_mul_f32 v40, v40, v66\n" \
  "v_mul_f32 v41, v41, v66\n" \
  "v_mul_f32 v42, v42, v66\n" \
  "v_mul_f32 v43, v43, v66\n" \
  "v_mfma_f32_16x16x16_bf16 v[52:55], v[90:91], v[106:107], v[52:55]\n" \
  "v_mul_f32 v56, v56, v66\n" \
  "v_mul_f32 v57, v57, v66\n" \
  "v_mul_f32 v58, v58, v66\n" \
  "v_mul_f32 v59, v59, v66\n" \
  "v_mfma_f32_16x16x16_bf16 v[40:43], v[92:93], v[104:105], v[40:43]\n" \
  "v_mul_f32 v44, v44, v66\n" \
  "v_mul_f32 v45, v45, v66\n" \
  "v_mul_f32 v46, v46, v66\n" \
  "v_mul_f32 v47, v47, v66\n" \
  "v_mfma_f32_16x16x16_bf16 v[56:59], v[92:93], v[106:107], v[56:59]\n" \
  "v_mul_f32 v60, v60, v66\n" \
  "v_mul_f32 v61, v61, v66\n" \
  "v_mul_f32 v62, v62, v66\n" \
  "v_mul_f32 v63, v63, v66\n" \
  "v_mfma_f32_16x16x16_bf16 v[44:47], v[94:95], v[104:105], v[44:47]\n" \
  "v_cvt_pk_bf16_f32 v108, v32, v33\n" \
  "v_cvt_pk_bf16_f32 v109, v48, v49\n" \
  "v_cvt_pk_bf16_f32 v110, v34, v35\n" \
  "v_cvt_pk_bf16_f32 v111, v50, v51\n" \
  "ds_write_b128 v7, v[108:111] offset:0\n" \
  "v_mfma_f32_16x16x16_bf16 v[60:63], v[94:95], v[106:107], v[60:63]\n" \
  "v_cvt_pk_bf16_f32 v112, v36, v37\n" \
  "v_cvt_pk_bf16_f32 v113, v52, v53\n" \
  "v_cvt_pk_bf16_f32 v114, v38, v39\n" \
  "v_cvt_pk_bf16_f32 v115, v54, v55\n" \
  "ds_write_b128 v7, v[112:115] offset:1024\n" \
  "v_cvt_pk_bf16_f32 v116, v40, v41\n" \
  "v_cvt_pk_bf16_f32 v117, v56, v57\n" \
  "v_cvt_pk_bf16_f32 v118, v42, v43\n" \
  "v_cvt_pk_bf16_f32 v119, v58, v59\n" \
  "ds_write_b128 v7, v[116:119] offset:2048\n" \
  "v_cvt_pk_bf16_f32 v120, v44, v45\n" \
  "v_cvt_pk_bf16_f32 v121, v60, v61\n" \
  "v_cvt_pk_bf16_f32 v122, v46, v47\n" \
  "v_cvt_pk_bf16_f32 v123, v62, v63\n" \
  "ds_write_b128 v7, v[120:123] offset:3072\n" \
  "s_waitcnt lgkmcnt(0)\n" \
  "s_barrier\n" \
  "s_setprio 2\n" \
  "ds_read_b128 v[48:51], v1 offset:0\n" \
  "ds_read_b128 v[52:55], v1 offset:1024\n" \
  "ds_read_b128 v[56:59], v1 offset:2048\n" \
  "s_waitcnt lgkmcnt(2)\n" \
  "v_mfma_f32_16x16x32_bf16 v[64:67], v[128:131], v[48:51], 0\n" \
  "ds_read_b128 v[60:63], v1 offset:3072\n" \
  "s_lshl_b32 s75, s57, 19\n" \
  "v_mfma_f32_16x16x32_bf16 v[68:71], v[160:163], v[48:51], 0\n" \
  "s_add_u32 s75, s75, s60\n" \
  "v_mfma_f32_16x16x32_bf16 v[72:75], v[192:195], v[48:51], 0\n" \
  "s_add_u32 s76, s75, 16384\n" \
  "v_mfma_f32_16x16x32_bf16 v[76:79], v[224:227], v[48:51], 0\n" \
  "s_waitcnt lgkmcnt(2)\n" \
  "v_mfma_f32_16x16x32_bf16 v[64:67], v[132:135], v[52:55], v[64:67]\n" \
  "ds_read_b128 v[48:51], v1 offset:8192\n" \
  "v_mfma_f32_16x16x32_bf16 v[68:71], v[164:167], v[52:55], v[68:71]\n" \
  "v_mfma_f32_16x16x32_bf16 v[72:75], v[196:199], v[52:55], v[72:75]\n" \
  "v_mfma_f32_16x16x32_bf16 v[76:79], v[228:231], v[52:55], v[76:79]\n" \
  "s_waitcnt lgkmcnt(2)\n" \
  "v_mfma_f32_16x16x32_bf16 v[64:67], v[136:139], v[56:59], v[64:67]\n" \
  "ds_read_b128 v[52:55], v1 offset:9216\n" \
  "v_mfma_f32_16x16x32_bf16 v[68:71], v[168:171], v[56:59], v[68:71]\n" \
  "v_mfma_f32_16x16x32_bf16 v[72:75], v[200:203], v[56:59], v[72:75]\n" \
  "v_mfma_f32_16x16x32_bf16 v[76:79], v[232:235], v[56:59], v[76:79]\n" \
  "s_waitcnt lgkmcnt(2)\n" \
  "v_mfma_f32_16x16x32_bf16 v[64:67], v[140:143], v[60:63], v[64:67]\n" \
  "ds_read_b128 v[56:59], v1 offset:10240\n" \
  "v_mfma_f32_16x16x32_bf16 v[68:71], v[172:175], v[60:63], v[68:71]\n" \
  "v_mfma_f32_16x16x32_bf16 v[72:75], v[204:207], v[60:63], v[72:75]\n" \
  "v_mfma_f32_16x16x32_bf16 v[76:79], v[236:239], v[60:63], v[76:79]\n" \
  "s_waitcnt lgkmcnt(2)\n" \
  "v_mfma_f32_16x16x32_bf16 v[64:67], v[144:147], v[48:51], v[64:67]\n" \
  "ds_read_b128 v[60:63], v1 offset:11264\n" \
  "v_mfma_f32_16x16x32_bf16 v[68:71], v[176:179], v[48:51], v[68:71]\n" \
  "v_mfma_f32_16x16x32_bf16 v[72:75], v[208:211], v[48:51], v[72:75]\n" \
  "v_mfma_f32_16x16x32_bf16 v[76:79], v[240:243], v[48:51], v[76:79]\n" \
  "s_waitcnt lgkmcnt(2)\n" \
  "v_mfma_f32_16x16x32_bf16 v[64:67], v[148:151], v[52:55], v[64:67]\n" \
  "ds_read_b128 v[48:51], v1 offset:4096\n" \
  "v_mfma_f32_16x16x32_bf16 v[68:71], v[180:183], v[52:55], v[68:71]\n" \
  "v_mfma_f32_16x16x32_bf16 v[72:75], v[212:215], v[52:55], v[72:75]\n" \
  "v_mfma_f32_16x16x32_bf16 v[76:79], v[244:247], v[52:55], v[76:79]\n" \
  "s_waitcnt lgkmcnt(2)\n" \
  "v_mfma_f32_16x16x32_bf16 v[64:67], v[152:155], v[56:59], v[64:67]\n" \
  "ds_read_b128 v[52:55], v1 offset:5120\n" \
  "v_mfma_f32_16x16x32_bf16 v[68:71], v[184:187], v[56:59], v[68:71]\n" \
  "v_mfma_f32_16x16x32_bf16 v[72:75], v[216:219], v[56:59], v[72:75]\n" \
  "v_mfma_f32_16x16x32_bf16 v[76:79], v[248:251], v[56:59], v[76:79]\n" \
  "s_waitcnt lgkmcnt(2)\n" \
  "v_mfma_f32_16x16x32_bf16 v[64:67], v[156:159], v[60:63], v[64:67]\n" \
  "ds_read_b128 v[56:59], v1 offset:6144\n" \
  "v_mfma_f32_16x16x32_bf16 v[68:71], v[188:191], v[60:63], v[68:71]\n" \
  "v_mfma_f32_16x16x32_bf16 v[72:75], v[220:223], v[60:63], v[72:75]\n" \
  "v_mfma_f32_16x16x32_bf16 v[76:79], v[252:255], v[60:63], v[76:79]\n" \
  "s_waitcnt lgkmcnt(2)\n" \
  "v_mfma_f32_16x16x32_bf16 v[80:83], v[128:131], v[48:51], 0\n" \
  "ds_read_b128 v[60:63], v1 offset:7168\n" \
  "v_mfma_f32_16x16x32_bf16 v[84:87], v[160:163], v[48:51], 0\n" \
  "v_mfma_f32_16x16x32_bf16 v[88:91], v[192:195], v[48:51], 0\n" \
  "v_mfma_f32_16x16x32_bf16 v[92:95], v[224:227], v[48:51], 0\n" \
  "s_waitcnt lgkmcnt(2)\n" \
  "v_mfma_f32_16x16x32_bf16 v[80:83], v[132:135], v[52:55], v[80:83]\n" \
  "ds_read_b128 v[48:51], v1 offset:12288\n" \
  "v_mfma_f32_16x16x32_bf16 v[84:87], v[164:167], v[52:55], v[84:87]\n" \
  "v_cvt_pk_bf16_f32 v64, v64, v65\n" \
  "v_mfma_f32_16x16x32_bf16 v[88:91], v[196:199], v[52:55], v[88:91]\n" \
  "v_cvt_pk_bf16_f32 v65, v66, v67\n" \
  "v_mfma_f32_16x16x32_bf16 v[92:95], v[228:231], v[52:55], v[92:95]\n" \
  "s_waitcnt lgkmcnt(2)\n" \
  "v_mfma_f32_16x16x32_bf16 v[80:83], v[136:139], v[56:59], v[80:83]\n" \
  "ds_read_b128 v[52:55], v1 offset:13312\n" \
  "v_mfma_f32_16x16x32_bf16 v[84:87], v[168:171], v[56:59], v[84:87]\n" \
  "v_cvt_pk_bf16_f32 v66, v68, v69\n" \
  "v_mfma_f32_16x16x32_bf16 v[88:91], v[200:203], v[56:59], v[88:91]\n" \
  "v_cvt_pk_bf16_f32 v67, v70, v71\n" \
  "v_mfma_f32_16x16x32_bf16 v[92:95], v[232:235], v[56:59], v[92:95]\n" \
  "s_waitcnt lgkmcnt(2)\n" \
  "v_mfma_f32_16x16x32_bf16 v[80:83], v[140:143], v[60:63], v[80:83]\n" \
  "ds_read_b128 v[56:59], v1 offset:14336\n" \
  "v_mfma_f32_16x16x32_bf16 v[84:87], v[172:175], v[60:63], v[84:87]\n" \
  "buffer_store_dwordx4 v[64:67], v2, s[40:43], s75 offen offset:0 sc1\n" \
  "v_mfma_f32_16x16x32_bf16 v[88:91], v[204:207], v[60:63], v[88:91]\n" \
  "v_cvt_pk_bf16_f32 v72, v72, v73\n" \
  "v_mfma_f32_16x16x32_bf16 v[92:95], v[236:239], v[60:63], v[92:95]\n" \
  "s_waitcnt lgkmcnt(2)\n" \
  "v_mfma_f32_16x16x32_bf16 v[80:83], v[144:147], v[48:51], v[80:83]\n" \
  "ds_read_b128 v[60:63], v1 offset:15360\n" \
  "v_mfma_f32_16x16x32_bf16 v[84:87], v[176:179], v[48:51], v[84:87]\n" \
  "v_cvt_pk_bf16_f32 v73, v74, v75\n" \
  "v_mfma_f32_16x16x32_bf16 v[88:91], v[208:211], v[48:51], v[88:91]\n" \
  "v_cvt_pk_bf16_f32 v74, v76, v77\n" \
  "v_mfma_f32_16x16x32_bf16 v[92:95], v[240:243], v[48:51], v[92:95]\n" \
  "s_waitcnt lgkmcnt(2)\n" \
  "v_mfma_f32_16x16x32_bf16 v[80:83], v[148:151], v[52:55], v[80:83]\n" \
  "v_mfma_f32_16x16x32_bf16 v[84:87], v[180:183], v[52:55], v[84:87]\n" \
  "v_cvt_pk_bf16_f32 v75, v78, v79\n" \
  "v_mfma_f32_16x16x32_bf16 v[88:91], v[212:215], v[52:55], v[88:91]\n" \
  "buffer_store_dwordx4 v[72:75], v2, s[40:43], s75 offen offset:128 sc1\n" \
  "v_mfma_f32_16x16x32_bf16 v[92:95], v[244:247], v[52:55], v[92:95]\n" \
  "s_waitcnt lgkmcnt(1)\n" \
  "v_mfma_f32_16x16x32_bf16 v[80:83], v[152:155], v[56:59], v[80:83]\n" \
  "v_mfma_f32_16x16x32_bf16 v[84:87], v[184:187], v[56:59], v[84:87]\n" \
  "v_mfma_f32_16x16x32_bf16 v[88:91], v[216:219], v[56:59], v[88:91]\n" \
  "v_mfma_f32_16x16x32_bf16 v[92:95], v[248:251], v[56:59], v[92:95]\n" \
  "s_waitcnt lgkmcnt(0)\n" \
  "v_mfma_f32_16x16x32_bf16 v[80:83], v[156:159], v[60:63], v[80:83]\n" \
  "v_mfma_f32_16x16x32_bf16 v[84:87], v[188:191], v[60:63], v[84:87]\n" \
  "v_mfma_f32_16x16x32_bf16 v[88:91], v[220:223], v[60:63], v[88:91]\n" \
  "v_mfma_f32_16x16x32_bf16 v[92:95], v[252:255], v[60:63], v[92:95]\n" \
  "s_nop 4\n" \
  "v_cvt_pk_bf16_f32 v80, v80, v81\n" \
  "v_cvt_pk_bf16_f32 v81, v82, v83\n" \
  "v_cvt_pk_bf16_f32 v82, v84, v85\n" \
  "v_cvt_pk_bf16_f32 v83, v86, v87\n" \
  "buffer_store_dwordx4 v[80:83], v2, s[40:43], s76 offen offset:0 sc1\n" \
  "v_cvt_pk_bf16_f32 v88, v88, v89\n" \
  "v_cvt_pk_bf16_f32 v89, v90, v91\n" \
  "v_cvt_pk_bf16_f32 v90, v92, v93\n" \
  "v_cvt_pk_bf16_f32 v91, v94, v95\n" \
  "buffer_store_dwordx4 v[88:91], v2, s[40:43], s76 offen offset:128 sc1\n" \
  "s_branch .LWA_NEXT_%=\n"
#define AW_ASM_1 \
  "v_lshlrev_b32 v7, 5, v0\n" \
  "s_mov_b32 s79, -1\n" \
  "s_cmp_eq_u32 s67, 0\n" \
  "s_cbranch_scc1 .LWB_LATE_%=\n" \
  "s_waitcnt vmcnt(0)\n" \
  ".p2align 6\n" \
  ".LWB_AL_%=:\n" \
  ".LWB_TOP_%=:\n" \
  "s_barrier\n" \
  "s_add_u32 s56, s56, 3\n" \
  "s_sub_u32 s68, s56, %[wd]\n" \
  "s_bitcmp1_b32 s68, 0\n" \
  "s_cselect_b32 s58, 64, %[wd]\n" \
  "s_lshr_b32 s70, s68, 1\n" \
  "s_add_u32 s58, s58, s70\n" \
  "s_cmp_lt_u32 s68, %[w2]\n" \
  "s_cselect_b32 s58, s58, s56\n" \
  "s_min_u32 s58, s58, 128\n" \
  "s_lshr_b32 s68, s58, 6\n" \
  "s_add_u32 s68, s68, s58\n" \
  "s_lshl_b32 s68, s68, 12\n" \
  "buffer_load_dword v5, off, s[44:47], s68 sc1\n" \
  "s_xor_b32 s61, s61, 0x10000\n" \
  "s_xor_b32 s62, s62, 0x10000\n" \
  "s_mov_b32 s67, 0\n" \
  "s_mov_b32 s77, 0\n" \
  "s_cmp_lt_u32 s56, %[w3]\n" \
  "s_cselect_b32 s68, s57, 0\n" \
  "s_cmp_ge_u32 s68, 64\n" \
  "s_cbranch_scc1 .LWB_ASKE_%=\n" \
  "s_and_b32 s75, s57, 64\n" \
  "s_lshl_b32 s75, s75, 4\n" \
  "buffer_load_dword v14, off, s[84:87], s75 sc1\n" \
  "s_and_b32 s76, s57, 63\n" \
  "s_mul_i32 s76, s76, %[pp]\n" \
  "s_add_u32 s76, s76, %[mo]\n" \
  "s_lshl_b32 s59, s57, 19\n" \
  "s_add_u32 s59, s59, s66\n" \
  "s_add_u32 s78, s59, 4096\n" \
  "v_mov_b64 v[8:9], -1\n" \
  "v_mov_b64 v[10:11], -1\n" \
  "buffer_store_dwordx4 v[8:11], v0, s[40:43], s59 offen offset:0 sc1\n" \
  "buffer_store_dwordx4 v[8:11], v0, s[40:43], s59 offen offset:1024 sc1\n" \
  "buffer_store_dwordx4 v[8:11], v0, s[40:43], s59 offen offset:2048 sc1\n" \
  "buffer_store_dwordx4 v[8:11], v0, s[40:43], s59 offen offset:3072 sc1\n" \
  "buffer_store_dwordx4 v[8:11], v0, s[40:43], s78 offen offset:0 sc1\n" \
  "buffer_store_dwordx4 v[8:11], v0, s[40:43], s78 offen offset:1024 sc1\n" \
  "v_mov_b64 v[16:17], 0\n" \
  "v_mov_b64 v[18:19], 0\n" \
  "v_mov_b64 v[20:21], 0\n" \
  "v_mov_b64 v[22:23], 0\n" \
  "v_mov_b64 v[24:25], 0\n" \
  "v_mov_b64 v[26:27], 0\n" \
  "v_mov_b64 v[28:29], 0\n" \
  "v_mov_b64 v[30:31], 0\n" \
  "v_mov_b64 v[32:33], 0\n" \
  "v_mov_b64 v[34:35], 0\n" \
  "v_mov_b64 v[36:37], 0\n" \
  "v_mov_b64 v[38:39], 0\n" \
  "v_mov_b64 v[40:41], 0\n" \
  "v_mov_b64 v[42:43], 0\n" \
  "v_mov_b64 v[44:45], 0\n" \
  "v_mov_b64 v[46:47], 0\n" \
  "s_waitcnt vmcnt(6)\n" \
  "v_readfirstlane_b32 s74, v14\n" \
  "s_add_u32 s74, s74, s76\n" \
  "s_cmp_lt_i32 s79, 0\n" \
  "s_barrier\n" \
  "s_cbranch_scc1 .LWB_NOD_%=\n" \
  "s_memrealtime s[68:69]\n" \
  "s_lshl_b32 s72, s79, 11\n" \
  "s_add_u32 s72, s72, s54\n" \
  "s_addc_u32 s73, s55, 0\n" \
  "s_waitcnt lgkmcnt(0)\n" \
  "s_add_u32 s70, s68, 0\n" \
  "s_or_b32 s70, s70, 1\n" \
  "v_mov_b32 v13, s70\n" \
  "s_mov_b64 exec, s[82:83]\n" \
  "global_store_dword v7, v13, s[72:73] sc1\n" \
  "s_mov_b64 exec, -1\n" \
  "s_mov_b32 s79, -1\n" \
  ".LWB_NOD_%=:\n" \
  "s_setprio 0\n" \
  "buffer_store_dwordx4 v[8:11], v0, s[40:43], s78 offen offset:2048 sc1\n" \
  "buffer_store_dwordx4 v[8:11], v0, s[40:43], s78 offen offset:3072 sc1\n" \
  "v_cmp_eq_u32 vcc, 0, v5\n" \
  "s_cmp_eq_u64 vcc, 0\n" \
  "s_cbranch_scc0 .LWB_NOT_%=\n" \
  "s_mov_b32 m0, s61\n" \
  "s_lshl_b32 s68, s58, 19\n" \
  "s_add_u32 s68, s68, s48\n" \
  "s_addc_u32 s69, s49, 0\n" \
  "global_load_lds_dwordx4 v0, s[68:69] offset:0 sc1\n" \
  "global_load_lds_dwordx4 v0, s[68:69] offset:1024 sc1\n" \
  "global_load_lds_dwordx4 v0, s[68:69] offset:2048 sc1\n" \
  "global_load_lds_dwordx4 v0, s[68:69] offset:3072 sc1\n" \
  "s_add_u32 m0, s61, 4096\n" \
  "s_add_u32 s70, s68, 4096\n" \
  "s_addc_u32 s71, s69, 0\n" \
  "global_load_lds_dwordx4 v0, s[70:71] offset:0 sc1\n" \
  "global_load_lds_dwordx4 v0, s[70:71] offset:1024 sc1\n" \
  "global_load_lds_dwordx4 v0, s[70:71] offset:2048 sc1\n" \
  "global_load_lds_dwordx4 v0, s[70:71] offset:3072 sc1\n" \
  "s_mov_b32 m0, s62\n" \
  "s_lshl_b32 s72, s58, 18\n" \
  "s_add_u32 s72, s72, s50\n" \
  "s_addc_u32 s73, s51, 0\n" \
  "global_load_lds_dwordx4 v0, s[72:73] offset:0 sc1\n" \
  "global_load_lds_dwordx4 v0, s[72:73] offset:1024 sc1\n" \
  "global_load_lds_dwordx4 v0, s[72:73] offset:2048 sc1\n" \
  "global_load_lds_dwordx4 v0, s[72:73] offset:3072 sc1\n" \
  "s_mov_b32 s67, 1\n" \
  ".LWB_NOT_%=:\n" \
  "ds_read_b128 v[48:51], v1 offset:0\n" \
  "ds_read_b128 v[52:55], v1 offset:1024\n" \
  "ds_read_b128 v[56:59], v1 offset:2048\n" \
  "s_waitcnt lgkmcnt(2)\n" \
  "v_mfma_f32_16x16x32_bf16 v[64:67], v[128:131], v[48:51], 0\n" \
  "ds_read_b128 v[60:63], v1 offset:3072\n" \
  "v_mfma_f32_16x16x32_bf16 v[68:71], v[160:163], v[48:51], 0\n" \
  "v_mfma_f32_16x16x32_bf16 v[72:75], v[192:195], v[48:51], 0\n" \
  "v_mfma_f32_16x16x32_bf16 v[76:79], v[224:227], v[48:51], 0\n" \
  "s_waitcnt lgkmcnt(2)\n" \
  "v_mfma_f32_16x16x32_bf16 v[64:67], v[132:135], v[52:55], v[64:67]\n" \
  "ds_read_b128 v[48:51], v1 offset:8192\n" \
  "v_mfma_f32_16x16x32_bf16 v[68:71], v[164:167], v[52:55], v[68:71]\n" \
  "v_mfma_f32_16x16x32_bf16 v[72:75], v[196:199], v[52:55], v[72:75]\n" \
  "v_mfma_f32_16x16x32_bf16 v[76:79], v[228:231], v[52:55], v[76:79]\n" \
  "s_waitcnt lgkmcnt(2)\n" \
  "v_mfma_f32_16x16x32_bf16 v[64:67], v[136:139], v[56:59], v[64:67]\n" \
  "ds_read_b128 v[52:55], v1 offset:9216\n" \
  "s_memrealtime s[68:69]\n" \
  "v_mfma_f32_16x16x32_bf16 v[68:71], v[168:171], v[56:59], v[68:71]\n" \
  "v_mfma_f32_16x16x32_bf16 v[72:75], v[200:203], v[56:59], v[72:75]\n" \
  "v_mfma_f32_16x16x32_bf16 v[76:79], v[232:235], v[56:59], v[76:79]\n" \
  "s_waitcnt lgkmcnt(2)\n" \
  "v_mfma_f32_16x16x32_bf16 v[64:67], v[140:143], v[60:63], v[64:67]\n" \
  "ds_read_b128 v[56:59], v1 offset:10240\n" \
  "s_waitcnt lgkmcnt(0)\n" \
  "s_sub_i32 s70, s74, s68\n" \
  "s_cmp_gt_i32 s70, 0\n" \
  "s_cbranch_scc1 .LWB_NOTYET_%=\n" \
  "s_mov_b32 m0, s63\n" \
  "s_lshl_b32 s72, s57, 16\n" \
  "s_add_u32 s72, s72, s52\n" \
  "s_addc_u32 s73, s53, 0\n" \
  "global_load_lds_dwordx4 v0, s[72:73] offset:0 sc1\n" \
  "global_load_lds_dwordx4 v0, s[72:73] offset:1024 sc1\n" \
  "s_mov_b32 s77, 1\n" \
  ".LWB_NOTYET_%=:\n" \
  "s_lshl_b32 s70, s67, 7\n" \
  "s_or_b32 s70, s70, s58\n" \
  "s_lshr_b32 s68, s70, 6\n" \
  "s_add_u32 s68, s68, s70\n" \
  "s_lshl_b32 s68, s68, 12\n" \
  "buffer_load_dword v5, off, s[44:47], s68 sc1\n" \
  "v_mfma_f32_16x16x32_bf16 v[68:71], v[172:175], v[60:63], v[68:71]\n" \
  "v_mfma_f32_16x16x32_bf16 v[72:75], v[204:207], v[60:63], v[72:75]\n" \
  "v_mfma_f32_16x16x32_bf16 v[76:79], v[236:239], v[60:63], v[76:79]\n" \
  "s_waitcnt lgkmcnt(2)\n" \
  "v_mfma_f32_16x16x32_bf16 v[96:99], v[144:147], v[48:51], 0\n" \
  "ds_read_b128 v[60:63], v1 offset:11264\n" \
  "v_mfma_f32_16x16x32_bf16 v[100:103], v[176:179], v[48:51], 0\n" \
  "v_mfma_f32_16x16x32_bf16 v[104:107], v[208:211], v[48:51], 0\n" \
  "v_mfma_f32_16x16x32_bf16 v[108:111], v[240:243], v[48:51], 0\n" \
  "s_waitcnt lgkmcnt(2)\n" \
  "v_mfma_f32_16x16x32_bf16 v[96:99], v[148:151], v[52:55], v[96:99]\n" \
  "ds_read_b128 v[48:51], v1 offset:4096\n" \
  "v_mfma_f32_16x16x32_bf16 v[100:103], v[180:183], v[52:55], v[100:103]\n" \
  "v_mfma_f32_16x16x32_bf16 v[104:107], v[212:215], v[52:55], v[104:107]\n" \
  "v_mfma_f32_16x16x32_bf16 v[108:111], v[244:247], v[52:55], v[108:111]\n" \
  "s_waitcnt lgkmcnt(2)\n" \
  "v_mfma_f32_16x16x32_bf16 v[96:99], v[152:155], v[56:59], v[96:99]\n" \
  "ds_read_b128 v[52:55], v1 offset:5120\n" \
  "v_mfma_f32_16x16x32_bf16 v[100:103], v[184:187], v[56:59], v[100:103]\n" \
  "v_mfma_f32_16x16x32_bf16 v[104:107], v[216:219], v[56:59], v[104:107]\n" \
  "v_mfma_f32_16x16x32_bf16 v[108:111], v[248:251], v[56:59], v[108:111]\n" \
  "s_waitcnt lgkmcnt(2)\n" \
  "v_mfma_f32_16x16x32_bf16 v[96:99], v[156:159], v[60:63], v[96:99]\n" \
  "ds_read_b128 v[56:59], v1 offset:6144\n" \
  "v_mfma_f32_16x16x32_bf16 v[100:103], v[188:191], v[60:63], v[100:103]\n" \
  "v_mfma_f32_16x16x32_bf16 v[104:107], v[220:223], v[60:63], v[104:107]\n" \
  "v_mfma_f32_16x16x32_bf16 v[108:111], v[252:255], v[60:63], v[108:111]\n" \
  "s_waitcnt lgkmcnt(2)\n" \
  "v_mfma_f32_16x16x32_bf16 v[80:83], v[128:131], v[48:51], 0\n" \
  "ds_read_b128 v[60:63], v1 offset:7168\n" \
  "v_mfma_f32_16x16x32_bf16 v[84:87], v[160:163], v[48:51], 0\n" \
  "v_mfma_f32_16x16x32_bf16 v[88:91], v[192:195], v[48:51], 0\n" \
  "v_mfma_f32_16x16x32_bf16 v[92:95], v[224:227], v[48:51], 0\n" \
  "s_waitcnt lgkmcnt(2)\n" \
  "v_mfma_f32_16x16x32_bf16 v[80:83], v[132:135], v[52:55], v[80:83]\n" \
  "ds_read_b128 v[48:51], v1 offset:12288\n" \
  "v_mfma_f32_16x16x32_bf16 v[84:87], v[164:167], v[52:55], v[84:87]\n" \
  "v_mfma_f32_16x16x32_bf16 v[88:91], v[196:199], v[52:55], v[88:91]\n" \
  "v_mfma_f32_16x16x32_bf16 v[92:95], v[228:231], v[52:55], v[92:95]\n" \
  "s_waitcnt lgkmcnt(2)\n" \
  "v_mfma_f32_16x16x32_bf16 v[80:83], v[136:139], v[56:59], v[80:83]\n" \
  "ds_read_b128 v[52:55], v1 offset:13312\n" \
  "v_mfma_f32_16x16x32_bf16 v[84:87], v[168:171], v[56:59], v[84:87]\n" \
  "v_mfma_f32_16x16x32_bf16 v[88:91], v[200:203], v[56:59], v[88:91]\n" \
  "v_mfma_f32_16x16x32_bf16 v[92:95], v[232:235], v[56:59], v[92:95]\n" \
  "s_waitcnt lgkmcnt(2)\n" \
  "v_mfma_f32_16x16x32_bf16 v[80:83], v[140:143], v[60:63], v[80:83]\n" \
  "ds_read_b128 v[56:59], v1 offset:14336\n" \
  "v_mfma_f32_16x16x32_bf16 v[84:87], v[172:175], v[60:63], v[84:87]\n" \
  "v_mfma_f32_16x16x32_bf16 v[88:91], v[204:207], v[60:63], v[88:91]\n" \
  "v_mfma_f32_16x16x32_bf16 v[92:95], v[236:239], v[60:63], v[92:95]\n" \
  "s_waitcnt lgkmcnt(2)\n" \
  "v_mfma_f32_16x16x32_bf16 v[112:115], v[144:147], v[48:51], 0\n" \
  "ds_read_b128 v[60:63], v1 offset:15360\n" \
  "v_mfma_f32_16x16x32_bf16 v[116:119], v[176:179], v[48:51], 0\n" \
  "v_mfma_f32_16x16x32_bf16 v[120:123], v[208:211], v[48:51], 0\n" \
  "v_mfma_f32_16x16x32_bf16 v[124:127], v[240:243], v[48:51], 0\n" \
  "s_waitcnt lgkmcnt(2)\n" \
  "v_mfma_f32_16x16x32_bf16 v[112:115], v[148:151], v[52:55], v[112:115]\n" \
  "v_mfma_f32_16x16x32_bf16 v[116:119], v[180:183], v[52:55], v[116:119]\n" \
  "v_mfma_f32_16x16x32_bf16 v[120:123], v[212:215], v[52:55], v[120:123]\n" \
  "s_waitcnt vmcnt(0)\n" \
  "ds_read_b128 v[8:11], v3 offset:0\n" \
  "ds_read_b128 v[12:15], v3 offset:1024\n" \
  "v_mfma_f32_16x16x32_bf16 v[124:127], v[244:247], v[52:55], v[124:127]\n" \
  "s_waitcnt lgkmcnt(3)\n" \
  "v_mfma_f32_16x16x32_bf16 v[112:115], v[152:155], v[56:59], v[112:115]\n" \
  "v_mfma_f32_16x16x32_bf16 v[116:119], v[184:187], v[56:59], v[116:119]\n" \
  "v_mfma_f32_16x16x32_bf16 v[120:123], v[216:219], v[56:59], v[120:123]\n" \
  "v_mfma_f32_16x16x32_bf16 v[124:127], v[248:251], v[56:59], v[124:127]\n" \
  "s_waitcnt lgkmcnt(2)\n" \
  "v_mfma_f32_16x16x32_bf16 v[112:115], v[156:159], v[60:63], v[112:115]\n" \
  "s_waitcnt lgkmcnt(0)\n" \
  "v_min3_u32 v8, v8, v9, v10\n" \
  "v_mfma_f32_16x16x32_bf16 v[116:119], v[188:191], v[60:63], v[116:119]\n" \
  "v_min3_u32 v12, v12, v13, v14\n" \
  "v_mfma_f32_16x16x32_bf16 v[120:123], v[220:223], v[60:63], v[120:123]\n" \
  "v_min3_u32 v8, v8, v11, v15\n" \
  "v_mfma_f32_16x16x32_bf16 v[124:127], v[252:255], v[60:63], v[124:127]\n" \
  "v_min_u32 v8, v8, v12\n" \
  "v_cmp_eq_u32 vcc, 0, v5\n" \
  "s_cmp_eq_u64 vcc, 0\n" \
  "s_cbranch_scc0 .LWB_NOU_%=\n" \
  "s_mov_b32 m0, s61\n" \
  "s_lshl_b32 s68, s58, 19\n" \
  "s_add_u32 s68, s68, s48\n" \
  "s_addc_u32 s69, s49, 0\n" \
  "global_load_lds_dwordx4 v0, s[68:69] offset:0 sc1\n" \
  "global_load_lds_dwordx4 v0, s[68:69] offset:1024 sc1\n" \
  "global_load_lds_dwordx4 v0, s[68:69] offset:2048 sc1\n" \
  "global_load_lds_dwordx4 v0, s[68:69] offset:3072 sc1\n" \
  "s_add_u32 m0, s61, 4096\n" \
  "s_add_u32 s70, s68, 4096\n" \
  "s_addc_u32 s71, s69, 0\n" \
  "global_load_lds_dwordx4 v0, s[70:71] offset:0 sc1\n" \
  "global_load_lds_dwordx4 v0, s[70:71] offset:1024 sc1\n" \
  "global_load_lds_dwordx4 v0, s[70:71] offset:2048 sc1\n" \
  "global_load_lds_dwordx4 v0, s[70:71] offset:3072 sc1\n" \
  "s_mov_b32 m0, s62\n" \
  "s_lshl_b32 s72, s58, 18\n" \
  "s_add_u32 s72, s72, s50\n" \
  "s_addc_u32 s73, s51, 0\n" \
  "global_load_lds_dwordx4 v0, s[72:73] offset:0 sc1\n" \
  "global_load_lds_dwordx4 v0, s[72:73] offset:1024 sc1\n" \
  "global_load_lds_dwordx4 v0, s[72:73] offset:2048 sc1\n" \
  "global_load_lds_dwordx4 v0, s[72:73] offset:3072 sc1\n" \
  "s_mov_b32 s67, 1\n" \
  ".LWB_NOU_%=:\n" \
  "s_cmp_eq_u32 s77, 1\n" \
  "s_cbranch_scc1 .LWB_TEST_%=\n" \
  "s_memrealtime s[68:69]\n" \
  "s_waitcnt lgkmcnt(0)\n" \
  "s_sub_i32 s70, s74, s68\n" \
  "s_cmp_lt_i32 s70, 50\n" \
  "s_cbranch_scc1 .LWB_GATEB_%=\n" \
  ".LWB_GATEA_%=:\n" \
  "s_memrealtime s[68:69]\n" \
  "s_waitcnt lgkmcnt(0)\n" \
  "s_sub_i32 s70, s74, s68\n" \
  "s_sub_u32 s70, s70, 65\n" \
  "s_cmp_gt_u32 s70, 2000\n" \
  "s_cbranch_scc1 .LWB_NEWS_%=\n" \
  "s_sleep 1\n" \
  "s_cmp_lt_u32 s70, 40\n" \
  "s_cbranch_scc1 .LWB_GATEA_%=\n" \
  "s_sleep 8\n" \
  "s_branch .LWB_GATEA_%=\n" \
  ".LWB_NEWS_%=:\n" \
  "buffer_load_dword v14, off, s[84:87], s75 sc1\n" \
  "s_waitcnt vmcnt(0)\n" \
  "v_readfirstlane_b32 s74, v14\n" \
  "s_add_u32 s74, s74, s76\n" \
  ".LWB_GATEB_%=:\n" \
  "s_memrealtime s[68:69]\n" \
  "s_waitcnt lgkmcnt(0)\n" \
  "s_sub_i32 s70, s74, s68\n" \
  "s_sub_u32 s70, s70, 0\n" \
  "s_cmp_gt_u32 s70, 2000\n" \
  "s_cbranch_scc1 .LWB_ASK_%=\n" \
  "s_sleep 1\n" \
  "s_cmp_lt_u32 s70, 40\n" \
  "s_cbranch_scc1 .LWB_GATEB_%=\n" \
  "s_sleep 8\n" \
  "s_branch .LWB_GATEB_%=\n" \
  ".LWB_AGAIN_%=:\n" \
  "s_sleep %[sm]\n" \
  ".LWB_ASK_%=:\n" \
  "s_mov_b32 m0, s63\n" \
  "s_lshl_b32 s72, s57, 16\n" \
  "s_add_u32 s72, s72, s52\n" \
  "s_addc_u32 s73, s53, 0\n" \
  "global_load_lds_dwordx4 v0, s[72:73] offset:0 sc1\n" \
  "global_load_lds_dwordx4 v0, s[72:73] offset:1024 sc1\n" \
  "s_add_u32 s77, s77, 1\n" \
  "s_waitcnt vmcnt(0)\n" \
  "ds_read_b128 v[8:11], v3 offset:0\n" \
  "ds_read_b128 v[12:15], v3 offset:1024\n" \
  "s_cmp_gt_u32 s77, 0x40000\n" \
  "s_cbranch_scc1 .LWB_OK_%=\n" \
  "s_waitcnt lgkmcnt(1)\n" \
  "v_min3_u32 v8, v8, v9, v10\n" \
  "s_waitcnt lgkmcnt(0)\n" \
  "v_min3_u32 v12, v12, v13, v14\n" \
  "v_min3_u32 v8, v8, v11, v15\n" \
  "v_min_u32 v8, v8, v12\n" \
  ".LWB_TEST_%=:\n" \
  "v_cmp_eq_u32 vcc, 0, v8\n" \
  "s_cmp_eq_u64 vcc, 0\n" \
  "s_cbranch_scc0 .LWB_AGAIN_%=\n" \
  ".LWB_OK_%=:\n" \
  "s_waitcnt lgkmcnt(0)\n" \
  "s_barrier\n" \
  "ds_read_b64_tr_b16 v[48:49], v6 offset:0\n" \
  "ds_read_b64_tr_b16 v[50:51], v6 offset:32\n" \
  "ds_read_b64_tr_b16 v[52:53], v6 offset:1024\n" \
  "ds_read_b64_tr_b16 v[54:55], v6 offset:1056\n" \
  "ds_read_b64_tr_b16 v[56:57], v6 offset:4096\n" \
  "ds_read_b64_tr_b16 v[58:59], v6 offset:4128\n" \
  "ds_read_b64_tr_b16 v[60:61], v6 offset:5120\n" \
  "ds_read_b64_tr_b16 v[62:63], v6 offset:5152\n" \
  "s_memrealtime s[68:69]\n" \
  "s_lshl_b32 s72, s57, 11\n" \
  "s_add_u32 s72, s72, s54\n" \
  "s_addc_u32 s73, s55, 0\n" \
  "s_waitcnt lgkmcnt(0)\n" \
  "s_add_u32 s70, s68, %[dt]\n" \
  "s_or_b32 s70, s70, 1\n" \
  "v_mov_b32 v13, s70\n" \
  "s_mov_b64 exec, s[82:83]\n" \
  "global_store_dword v7, v13, s[72:73] sc1\n" \
  "s_mov_b64 exec, -1\n" \
  "s_waitcnt lgkmcnt(6)\n" \
  "v_smfmac_f32_16x16x64_bf16 v[16:19], v[48:51], v[128:135], v4\n" \
  "v_smfmac_f32_16x16x64_bf16 v[20:23], v[48:51], v[160:167], v4\n" \
  "v_smfmac_f32_16x16x64_bf16 v[24:27], v[48:51], v[192:199], v4\n" \
  "v_smfmac_f32_16x16x64_bf16 v[28:31], v[48:51], v[224:231], v4\n" \
  "s_waitcnt lgkmcnt(4)\n" \
  "v_smfmac_f32_16x16x64_bf16 v[16:19], v[52:55], v[136:143], v4\n" \
  "v_smfmac_f32_16x16x64_bf16 v[20:23], v[52:55], v[168:175], v4\n" \
  "v_smfmac_f32_16x16x64_bf16 v[24:27], v[52:55], v[200:207], v4\n" \
  "v_smfmac_f32_16x16x64_bf16 v[28:31], v[52:55], v[232:239], v4\n" \
  "s_waitcnt lgkmcnt(2)\n" \
  "v_smfmac_f32_16x16x64_bf16 v[32:35], v[56:59], v[144:151], v4\n" \
  "v_smfmac_f32_16x16x64_bf16 v[36:39], v[56:59], v[176:183], v4\n" \
  "v_smfmac_f32_16x16x64_bf16 v[40:43], v[56:59], v[208:215], v4\n" \
  "v_smfmac_f32_16x16x64_bf16 v[44:47], v[56:59], v[240:247], v4\n" \
  "s_waitcnt lgkmcnt(0)\n" \
  "v_smfmac_f32_16x16x64_bf16 v[32:35], v[60:63], v[152:159], v4\n" \
  "v_smfmac_f32_16x16x64_bf16 v[36:39], v[60:63], v[184:191], v4\n" \
  "v_smfmac_f32_16x16x64_bf16 v[40:43], v[60:63], v[216:223], v4\n" \
  "v_smfmac_f32_16x16x64_bf16 v[44:47], v[60:63], v[248:255], v4\n" \
  "s_nop 1\n" \
  "s_lshl_b32 s75, s57, 19\n" \
  "s_add_u32 s75, s75, s60\n" \
  "s_add_u32 s76, s75, 16384\n" \
  "v_pk_add_f32 v[16:17], v[16:17], v[18:19]\n" \
  "v_pk_add_f32 v[32:33], v[32:33], v[34:35]\n" \
  "v_cvt_pk_bf16_f32 v16, v16, v17\n" \
  "v_cvt_pk_bf16_f32 v17, v32, v33\n" \
  "v_pk_add_f32 v[20:21], v[20:21], v[22:23]\n" \
  "v_pk_add_f32 v[36:37], v[36:37], v[38:39]\n" \
  "v_cvt_pk_bf16_f32 v20, v20, v21\n" \
  "v_cvt_pk_bf16_f32 v21, v36, v37\n" \
  "v_pk_add_f32 v[24:25], v[24:25], v[26:27]\n" \
  "v_pk_add_f32 v[40:41], v[40:41], v[42:43]\n" \
  "v_cvt_pk_bf16_f32 v24, v24, v25\n" \
  "v_cvt_pk_bf16_f32 v25, v40, v41\n" \
  "v_pk_add_f32 v[28:29], v[28:29], v[30:31]\n" \
  "v_pk_add_f32 v[44:45], v[44:45], v[46:47]\n" \
  "v_cvt_pk_bf16_f32 v28, v28, v29\n" \
  "v_cvt_pk_bf16_f32 v29, v44, v45\n" \
  "s_barrier\n" \
  "ds_read_b128 v[32:35], v1 offset:24576\n" \
  "ds_read_b128 v[36:39], v1 offset:25600\n" \
  "s_setprio 1\n" \
  "s_waitcnt lgkmcnt(0)\n" \
  "v_pk_mul_f32 v[64:65], v[64:65], v[34:35] op_sel_hi:[1,0]\n" \
  "v_pk_mul_f32 v[66:67], v[66:67], v[34:35] op_sel_hi:[1,0]\n" \
  "v_pk_mul_f32 v[68:69], v[68:69], v[34:35] op_sel_hi:[1,0]\n" \
  "v_pk_mul_f32 v[70:71], v[70:71], v[34:35] op_sel_hi:[1,0]\n" \
  "v_pk_fma_f32 v[64:65], v[96:97], v[34:35], v[64:65] op_sel:[0,1,0]\n" \
  "v_pk_fma_f32 v[66:67], v[98:99], v[34:35], v[66:67] op_sel:[0,1,0]\n" \
  "v_pk_fma_f32 v[68:69], v[100:101], v[34:35], v[68:69] op_sel:[0,1,0]\n" \
  "v_pk_fma_f32 v[70:71], v[102:103], v[34:35], v[70:71] op_sel:[0,1,0]\n" \
  "s_nop 1\n" \
  "v_mfma_f32_16x16x16_bf16 v[64:67], v[16:17], v[32:33], v[64:67]\n" \
  "v_mfma_f32_16x16x16_bf16 v[68:71], v[20:21], v[32:33], v[68:71]\n" \
  "v_pk_mul_f32 v[72:73], v[72:73], v[34:35] op_sel_hi:[1,0]\n" \
  "v_pk_mul_f32 v[74:75], v[74:75], v[34:35] op_sel_hi:[1,0]\n" \
  "v_pk_mul_f32 v[76:77], v[76:77], v[34:35] op_sel_hi:[1,0]\n" \
  "v_pk_mul_f32 v[78:79], v[78:79], v[34:35] op_sel_hi:[1,0]\n" \
  "v_pk_fma_f32 v[72:73], v[104:105], v[34:35], v[72:73] op_sel:[0,1,0]\n" \
  "v_pk_fma_f32 v[74:75], v[106:107], v[34:35], v[74:75] op_sel:[0,1,0]\n" \
  "v_pk_fma_f32 v[76:77], v[108:109], v[34:35], v[76:77] op_sel:[0,1,0]\n" \
  "v_pk_fma_f32 v[78:79], v[110:111], v[34:35], v[78:79] op_sel:[0,1,0]\n" \
  "s_nop 1\n" \
  "v_mfma_f32_16x16x16_bf16 v[72:75], v[24:25], v[32:33], v[72:75]\n" \
  "v_mfma_f32_16x16x16_bf16 v[76:79], v[28:29], v[32:33], v[76:79]\n" \
  "v_cvt_pk_bf16_f32 v64, v64, v65\n" \
  "v_cvt_pk_bf16_f32 v65, v66, v67\n" \
  "v_cvt_pk_bf16_f32 v66, v68, v69\n" \
  "v_cvt_pk_bf16_f32 v67, v70, v71\n" \
  "buffer_store_dwordx4 v[64:67], v2, s[40:43], s75 offen offset:0 sc1\n" \
  "v_pk_mul_f32 v[80:81], v[80:81], v[38:39] op_sel_hi:[1,0]\n" \
  "v_pk_mul_f32 v[82:83], v[82:83], v[38:39] op_sel_hi:[1,0]\n" \
  "v_pk_mul_f32 v[84:85], v[84:85], v[38:39] op_sel_hi:[1,0]\n" \
  "v_pk_mul_f32 v[86:87], v[86:87], v[38:39] op_sel_hi:[1,0]\n" \
  "v_pk_fma_f32 v[80:81], v[112:113], v[38:39], v[80:81] op_sel:[0,1,0]\n" \
  "v_pk_fma_f32 v[82:83], v[114:115], v[38:39], v[82:83] op_sel:[0,1,0]\n" \
  "v_pk_fma_f32 v[84:85], v[116:117], v[38:39], v[84:85] op_sel:[0,1,0]\n" \
  "v_pk_fma_f32 v[86:87], v[118:119], v[38:39], v[86:87] op_sel:[0,1,0]\n" \
  "s_nop 1\n" \
  "v_mfma_f32_16x16x16_bf16 v[80:83], v[16:17], v[36:37], v[80:83]\n" \
  "v_mfma_f32_16x16x16_bf16 v[84:87], v[20:21], v[36:37], v[84:87]\n" \
  "v_cvt_pk_bf16_f32 v72, v72, v73\n" \
  "v_cvt_pk_bf16_f32 v73, v74, v75\n" \
  "v_cvt_pk_bf16_f32 v74, v76, v77\n" \
  "v_cvt_pk_bf16_f32 v75, v78, v79\n" \
  "buffer_store_dwordx4 v[72:75], v2, s[40:43], s75 offen offset:128 sc1\n" \
  "v_pk_mul_f32 v[88:89], v[88:89], v[38:39] op_sel_hi:[1,0]\n" \
  "v_pk_mul_f32 v[90:91], v[90:91], v[38:39] op_sel_hi:[1,0]\n" \
  "v_pk_mul_f32 v[92:93], v[92:93], v[38:39] op_sel_hi:[1,0]\n" \
  "v_pk_mul_f32 v[94:95], v[94:95], v[38:39] op_sel_hi:[1,0]\n" \
  "v_pk_fma_f32 v[88:89], v[120:121], v[38:39], v[88:89] op_sel:[0,1,0]\n" \
  "v_pk_fma_f32 v[90:91], v[122:123], v[38:39], v[90:91] op_sel:[0,1,0]\n" \
  "v_pk_fma_f32 v[92:93], v[124:125], v[38:39], v[92:93] op_sel:[0,1,0]\n" \
  "v_pk_fma_f32 v[94:95], v[126:127], v[38:39], v[94:95] op_sel:[0,1,0]\n" \
  "s_nop 1\n" \
  "v_mfma_f32_16x16x16_bf16 v[88:91], v[24:25], v[36:37], v[88:91]\n" \
  "v_mfma_f32_16x16x16_bf16 v[92:95], v[28:29], v[36:37], v[92:95]\n" \
  "v_cvt_pk_bf16_f32 v80, v80, v81\n" \
  "v_cvt_pk_bf16_f32 v81, v82, v83\n" \
  "v_cvt_pk_bf16_f32 v82, v84, v85\n" \
  "v_cvt_pk_bf16_f32 v83, v86, v87\n" \
  "buffer_store_dwordx4 v[80:83], v2, s[40:43], s76 offen offset:0 sc1\n" \
  "s_nop 2\n" \
  "v_cvt_pk_bf16_f32 v88, v88, v89\n" \
  "v_cvt_pk_bf16_f32 v89, v90, v91\n" \
  "v_cvt_pk_bf16_f32 v90, v92, v93\n" \
  "v_cvt_pk_bf16_f32 v91, v94, v95\n" \
  "buffer_store_dwordx4 v[88:91], v2, s[40:43], s76 offen offset:128 sc1\n" \
  "s_waitcnt vmcnt(4)\n" \
  ".LWB_NEXT_%=:\n" \
  "s_setprio 0\n" \
  "s_mov_b32 s57, s58\n" \
  "s_cmp_eq_u32 s67, 0\n" \
  "s_cbranch_scc1 .LWB_MISS_%=\n" \
  "s_branch .LWB_TOP_%=\n" \
  ".LWB_MISS_%=:\n" \
  "s_cmp_eq_u32 s57, 128\n" \
  "s_cbranch_scc1 .LWB_END_%=\n" \
  ".LWB_LATE_%=:\n" \
  "s_mov_b32 s77, 0x40000\n" \
  ".LWB_POLL_%=:\n" \
  "s_lshr_b32 s68, s57, 6\n" \
  "s_add_u32 s68, s68, s57\n" \
  "s_lshl_b32 s68, s68, 12\n" \
  "buffer_load_dword v5, off, s[44:47], s68 sc1\n" \
  "s_sub_u32 s77, s77, 1\n" \
  "s_cbranch_scc1 .LWB_SEEN_%=\n" \
  "s_waitcnt vmcnt(0)\n" \
  "v_cmp_eq_u32 vcc, 0, v5\n" \
  "s_cmp_eq_u64 vcc, 0\n" \
  "s_cbranch_scc1 .LWB_SEEN_%=\n" \
  "s_sleep %[sa]\n" \
  "s_branch .LWB_POLL_%=\n" \
  ".LWB_SEEN_%=:\n" \
  "s_mov_b32 m0, s61\n" \
  "s_lshl_b32 s68, s57, 19\n" \
  "s_add_u32 s68, s68, s48\n" \
  "s_addc_u32 s69, s49, 0\n" \
  "global_load_lds_dwordx4 v0, s[68:69] offset:0 sc1\n" \
  "global_load_lds_dwordx4 v0, s[68:69] offset:1024 sc1\n" \
  "global_load_lds_dwordx4 v0, s[68:69] offset:2048 sc1\n" \
  "global_load_lds_dwordx4 v0, s[68:69] offset:3072 sc1\n" \
  "s_add_u32 m0, s61, 4096\n" \
  "s_add_u32 s70, s68, 4096\n" \
  "s_addc_u32 s71, s69, 0\n" \
  "global_load_lds_dwordx4 v0, s[70:71] offset:0 sc1\n" \
  "global_load_lds_dwordx4 v0, s[70:71] offset:1024 sc1\n" \
  "global_load_lds_dwordx4 v0, s[70:71] offset:2048 sc1\n" \
  "global_load_lds_dwordx4 v0, s[70:71] offset:3072 sc1\n" \
  "s_mov_b32 m0, s62\n" \
  "s_lshl_b32 s72, s57, 18\n" \
  "s_add_u32 s72, s72, s50\n" \
  "s_addc_u32 s73, s51, 0\n" \
  "global_load_lds_dwordx4 v0, s[72:73] offset:0 sc1\n" \
  "global_load_lds_dwordx4 v0, s[72:73] offset:1024 sc1\n" \
  "global_load_lds_dwordx4 v0, s[72:73] offset:2048 sc1\n" \
  "global_load_lds_dwordx4 v0, s[72:73] offset:3072 sc1\n" \
  "s_waitcnt vmcnt(0)\n" \
  "s_branch .LWB_TOP_%=\n" \
  ".LWB_END_%=:\n" \
  "s_endpgm\n" \
  ".LWB_AGAINE_%=:\n" \
  "s_sleep %[sm]\n" \
  ".LWB_ASKE_%=:\n" \
  "s_mov_b32 m0, s63\n" \
  "s_lshl_b32 s72, s57, 16\n" \
  "s_add_u32 s72, s72, s52\n" \
  "s_addc_u32 s73, s53, 0\n" \
  "global_load_lds_dwordx4 v0, s[72:73] offset:0 sc1\n" \
  "global_load_lds_dwordx4 v0, s[72:73] offset:1024 sc1\n" \
  "s_add_u32 s77, s77, 1\n" \
  "s_waitcnt vmcnt(0)\n" \
  "ds_read_b128 v[8:11], v3 offset:0\n" \
  "ds_read_b128 v[12:15], v3 offset:1024\n" \
  "s_cmp_gt_u32 s77, 0x40000\n" \
  "s_cbranch_scc1 .LWB_OKE_%=\n" \
  "s_waitcnt lgkmcnt(1)\n" \
  "v_min3_u32 v8, v8, v9, v10\n" \
  "s_waitcnt lgkmcnt(0)\n" \
  "v_min3_u32 v12, v12, v13, v14\n" \
  "v_min3_u32 v8, v8, v11, v15\n" \
  "v_min_u32 v8, v8, v12\n" \
  ".LWB_TESTE_%=:\n" \
  "v_cmp_eq_u32 vcc, 0, v8\n" \
  "s_cmp_eq_u64 vcc, 0\n" \
  "s_cbranch_scc0 .LWB_AGAINE_%=\n" \
  ".LWB_OKE_%=:\n" \
  "s_waitcnt lgkmcnt(0)\n" \
  "s_barrier\n" \
  "v_cmp_eq_u32 vcc, 0, v5\n" \
  "s_cmp_eq_u64 vcc, 0\n" \
  "s_cbranch_scc0 .LWB_NOE_%=\n" \
  "s_mov_b32 m0, s61\n" \
  "s_lshl_b32 s68, s58, 19\n" \
  "s_add_u32 s68, s68, s48\n" \
  "s_addc_u32 s69, s49, 0\n" \
  "global_load_lds_dwordx4 v0, s[68:69] offset:0 sc1\n" \
  "global_load_lds_dwordx4 v0, s[68:69] offset:1024 sc1\n" \
  "global_load_lds_dwordx4 v0, s[68:69] offset:2048 sc1\n" \
  "global_load_lds_dwordx4 v0, s[68:69] offset:3072 sc1\n" \
  "s_add_u32 m0, s61, 4096\n" \
  "s_add_u32 s70, s68, 4096\n" \
  "s_addc_u32 s71, s69, 0\n" \
  "global_load_lds_dwordx4 v0, s[70:71] offset:0 sc1\n" \
  "global_load_lds_dwordx4 v0, s[70:71] offset:1024 sc1\n" \
  "global_load_lds_dwordx4 v0, s[70:71] offset:2048 sc1\n" \
  "global_load_lds_dwordx4 v0, s[70:71] offset:3072 sc1\n" \
  "s_mov_b32 m0, s62\n" \
  "s_lshl_b32 s72, s58, 18\n" \
  "s_add_u32 s72, s72, s50\n" \
  "s_addc_u32 s73, s51, 0\n" \
  "global_load_lds_dwordx4 v0, s[72:73] offset:0 sc1\n" \
  "global_load_lds_dwordx4 v0, s[72:73] offset:1024 sc1\n" \
  "global_load_lds_dwordx4 v0, s[72:73] offset:2048 sc1\n" \
  "global_load_lds_dwordx4 v0, s[72:73] offset:3072 sc1\n" \
  "s_mov_b32 s67, 1\n" \
  ".LWB_NOE_%=:\n" \
  "s_barrier\n" \
  "s_setprio 0\n" \
  "ds_read_b128 v[48:51], v1 offset:0\n" \
  "ds_read_b128 v[52:55], v1 offset:1024\n" \
  "ds_read_b128 v[56:59], v1 offset:2048\n" \
  "s_waitcnt lgkmcnt(2)\n" \
  "v_mfma_f32_16x16x32_bf16 v[64:67], v[128:131], v[48:51], 0\n" \
  "ds_read_b128 v[60:63], v1 offset:3072\n" \
  "s_lshl_b32 s75, s57, 19\n" \
  "v_mfma_f32_16x16x32_bf16 v[68:71], v[160:163], v[48:51], 0\n" \
  "s_add_u32 s75, s75, s60\n" \
  "v_mfma_f32_16x16x32_bf16 v[72:75], v[192:195], v[48:51], 0\n" \
  "s_add_u32 s76, s75, 16384\n" \
  "v_mfma_f32_16x16x32_bf16 v[76:79], v[224:227], v[48:51], 0\n" \
  "s_waitcnt lgkmcnt(2)\n" \
  "v_mfma_f32_16x16x32_bf16 v[64:67], v[132:135], v[52:55], v[64:67]\n" \
  "ds_read_b128 v[48:51], v1 offset:8192\n" \
  "v_mfma_f32_16x16x32_bf16 v[68:71], v[164:167], v[52:55], v[68:71]\n" \
  "v_mfma_f32_16x16x32_bf16 v[72:75], v[196:199], v[52:55], v[72:75]\n" \
  "v_mfma_f32_16x16x32_bf16 v[76:79], v[228:231], v[52:55], v[76:79]\n" \
  "s_waitcnt lgkmcnt(2)\n" \
  "v_mfma_f32_16x16x32_bf16 v[64:67], v[136:139], v[56:59], v[64:67]\n" \
  "ds_read_b128 v[52:55], v1 offset:9216\n" \
  "v_mfma_f32_16x16x32_bf16 v[68:71], v[168:171], v[56:59], v[68:71]\n" \
  "v_mfma_f32_16x16x32_bf16 v[72:75], v[200:203], v[56:59], v[72:75]\n" \
  "v_mfma_f32_16x16x32_bf16 v[76:79], v[232:235], v[56:59], v[76:79]\n" \
  "s_waitcnt lgkmcnt(2)\n" \
  "v_mfma_f32_16x16x32_bf16 v[64:67], v[140:143], v[60:63], v[64:67]\n" \
  "ds_read_b128 v[56:59], v1 offset:10240\n" \
  "v_mfma_f32_16x16x32_bf16 v[68:71], v[172:175], v[60:63], v[68:71]\n" \
  "v_mfma_f32_16x16x32_bf16 v[72:75], v[204:207], v[60:63], v[72:75]\n" \
  "v_mfma_f32_16x16x32_bf16 v[76:79], v[236:239], v[60:63], v[76:79]\n" \
  "s_waitcnt lgkmcnt(2)\n" \
  "v_mfma_f32_16x16x32_bf16 v[64:67], v[144:147], v[48:51], v[64:67]\n" \
  "ds_read_b128 v[60:63], v1 offset:11264\n" \
  "v_mfma_f32_16x16x32_bf16 v[68:71], v[176:179], v[48:51], v[68:71]\n" \
  "v_mfma_f32_16x16x32_bf16 v[72:75], v[208:211], v[48:51], v[72:75]\n" \
  "v_mfma_f32_16x16x32_bf16 v[76:79], v[240:243], v[48:51], v[76:79]\n" \
  "s_waitcnt lgkmcnt(2)\n" \
  "v_mfma_f32_16x16x32_bf16 v[64:67], v[148:151], v[52:55], v[64:67]\n" \
  "ds_read_b128 v[48:51], v1 offset:4096\n" \
  "v_mfma_f32_16x16x32_bf16 v[68:71], v[180:183], v[52:55], v[68:71]\n" \
  "v_mfma_f32_16x16x32_bf16 v[72:75], v[212:215], v[52:55], v[72:75]\n" \
  "v_mfma_f32_16x16x32_bf16 v[76:79], v[244:247], v[52:55], v[76:79]\n" \
  "s_waitcnt lgkmcnt(2)\n" \
  "v_mfma_f32_16x16x32_bf16 v[64:67], v[152:155], v[56:59], v[64:67]\n" \
  "ds_read_b128 v[52:55], v1 offset:5120\n" \
  "v_mfma_f32_16x16x32_bf16 v[68:71], v[184:187], v[56:59], v[68:71]\n" \
  "v_mfma_f32_16x16x32_bf16 v[72:75], v[216:219], v[56:59], v[72:75]\n" \
  "v_mfma_f32_16x16x32_bf16 v[76:79], v[248:251], v[56:59], v[76:79]\n" \
  "s_waitcnt lgkmcnt(2)\n" \
  "v_mfma_f32_16x16x32_bf16 v[64:67], v[156:159], v[60:63], v[64:67]\n" \
  "ds_read_b128 v[56:59], v1 offset:6144\n" \
  "v_mfma_f32_16x16x32_bf16 v[68:71], v[188:191], v[60:63], v[68:71]\n" \
  "v_mfma_f32_16x16x32_bf16 v[72:75], v[220:223], v[60:63], v[72:75]\n" \
  "v_mfma_f32_16x16x32_bf16 v[76:79], v[252:255], v[60:63], v[76:79]\n" \
  "s_waitcnt lgkmcnt(2)\n" \
  "v_mfma_f32_16x16x32_bf16 v[80:83], v[128:131], v[48:51], 0\n" \
  "ds_read_b128 v[60:63], v1 offset:7168\n" \
  "v_mfma_f32_16x16x32_bf16 v[84:87], v[160:163], v[48:51], 0\n" \
  "v_mfma_f32_16x16x32_bf16 v[88:91], v[192:195], v[48:51], 0\n" \
  "v_mfma_f32_16x16x32_bf16 v[92:95], v[224:227], v[48:51], 0\n" \
  "s_waitcnt lgkmcnt(2)\n" \
  "v_mfma_f32_16x16x32_bf16 v[80:83], v[132:135], v[52:55], v[80:83]\n" \
  "ds_read_b128 v[48:51], v1 offset:12288\n" \
  "v_mfma_f32_16x16x32_bf16 v[84:87], v[164:167], v[52:55], v[84:87]\n" \
  "v_cvt_pk_bf16_f32 v64, v64, v65\n" \
  "v_mfma_f32_16x16x32_bf16 v[88:91], v[196:199], v[52:55], v[88:91]\n" \
  "v_cvt_pk_bf16_f32 v65, v66, v67\n" \
  "v_mfma_f32_16x16x32_bf16 v[92:95], v[228:231], v[52:55], v[92:95]\n" \
  "s_waitcnt lgkmcnt(2)\n" \
  "v_mfma_f32_16x16x32_bf16 v[80:83], v[136:139], v[56:59], v[80:83]\n" \
  "ds_read_b128 v[52:55], v1 offset:13312\n" \
  "v_mfma_f32_16x16x32_bf16 v[84:87], v[168:171], v[56:59], v[84:87]\n" \
  "v_cvt_pk_bf16_f32 v66, v68, v69\n" \
  "v_mfma_f32_16x16x32_bf16 v[88:91], v[200:203], v[56:59], v[88:91]\n" \
  "v_cvt_pk_bf16_f32 v67, v70, v71\n" \
  "v_mfma_f32_16x16x32_bf16 v[92:95], v[232:235], v[56:59], v[92:95]\n" \
  "s_waitcnt lgkmcnt(2)\n" \
  "v_mfma_f32_16x16x32_bf16 v[80:83], v[140:143], v[60:63], v[80:83]\n" \
  "ds_read_b128 v[56:59], v1 offset:14336\n" \
  "v_mfma_f32_16x16x32_bf16 v[84:87], v[172:175], v[60:63], v[84:87]\n" \
  "buffer_store_dwordx4 v[64:67], v2, s[40:43], s75 offen offset:0 sc1\n" \
  "v_mfma_f32_16x16x32_bf16 v[88:91], v[204:207], v[60:63], v[88:91]\n" \
  "v_cvt_pk_bf16_f32 v72, v72, v73\n" \
  "v_mfma_f32_16x16x32_bf16 v[92:95], v[236:239], v[60:63], v[92:95]\n" \
  "s_waitcnt lgkmcnt(2)\n" \
  "v_mfma_f32_16x16x32_bf16 v[80:83], v[144:147], v[48:51], v[80:83]\n" \
  "ds_read_b128 v[60:63], v1 offset:15360\n" \
  "v_mfma_f32_16x16x32_bf16 v[84:87], v[176:179], v[48:51], v[84:87]\n" \
  "v_cvt_pk_bf16_f32 v73, v74, v75\n" \
  "v_mfma_f32_16x16x32_bf16 v[88:91], v[208:211], v[48:51], v[88:91]\n" \
  "v_cvt_pk_bf16_f32 v74, v76, v77\n" \
  "v_mfma_f32_16x16x32_bf16 v[92:95], v[240:243], v[48:51], v[92:95]\n" \
  "s_waitcnt lgkmcnt(2)\n" \
  "v_mfma_f32_16x16x32_bf16 v[80:83], v[148:151], v[52:55], v[80:83]\n" \
  "v_mfma_f32_16x16x32_bf16 v[84:87], v[180:183], v[52:55], v[84:87]\n" \
  "v_cvt_pk_bf16_f32 v75, v78, v79\n" \
  "v_mfma_f32_16x16x32_bf16 v[88:91], v[212:215], v[52:55], v[88:91]\n" \
  "buffer_store_dwordx4 v[72:75], v2, s[40:43], s75 offen offset:128 sc1\n" \
  "v_mfma_f32_16x16x32_bf16 v[92:95], v[244:247], v[52:55], v[92:95]\n" \
  "s_waitcnt lgkmcnt(1)\n" \
  "v_mfma_f32_16x16x32_bf16 v[80:83], v[152:155], v[56:59], v[80:83]\n" \
  "v_mfma_f32_16x16x32_bf16 v[84:87], v[184:187], v[56:59], v[84:87]\n" \
  "v_mfma_f32_16x16x32_bf16 v[88:91], v[216:219], v[56:59], v[88:91]\n" \
  "v_mfma_f32_16x16x32_bf16 v[92:95], v[248:251], v[56:59], v[92:95]\n" \
  "s_waitcnt lgkmcnt(0)\n" \
  "v_mfma_f32_16x16x32_bf16 v[80:83], v[156:159], v[60:63], v[80:83]\n" \
  "v_mfma_f32_16x16x32_bf16 v[84:87], v[188:191], v[60:63], v[84:87]\n" \
  "v_mfma_f32_16x16x32_bf16 v[88:91], v[220:223], v[60:63], v[88:91]\n" \
  "v_mfma_f32_16x16x32_bf16 v[92:95], v[252:255], v[60:63], v[92:95]\n" \
  "s_nop 4\n" \
  "v_cvt_pk_bf16_f32 v80, v80, v81\n" \
  "v_cvt_pk_bf16_f32 v81, v82, v83\n" \
  "v_cvt_pk_bf16_f32 v82, v84, v85\n" \
  "v_cvt_pk_bf16_f32 v83, v86, v87\n" \
  "buffer_store_dwordx4 v[80:83], v2, s[40:43], s76 offen offset:0 sc1\n" \
  "v_cvt_pk_bf16_f32 v88, v88, v89\n" \
  "v_cvt_pk_bf16_f32 v89, v90, v91\n" \
  "v_cvt_pk_bf16_f32 v90, v92, v93\n" \
  "v_cvt_pk_bf16_f32 v91, v94, v95\n" \
  "buffer_store_dwordx4 v[88:91], v2, s[40:43], s76 offen offset:128 sc1\n" \
  "s_mov_b32 s79, s57\n" \
  "s_waitcnt vmcnt(4)\n" \
  "s_branch .LWB_NEXT_%=\n"
#define CONV_ASM \
  "v_mov_b64 v[64:65], 0\n" \
  "v_mov_b64 v[66:67], 0\n" \
  "v_mov_b64 v[68:69], 0\n" \
  "v_mov_b64 v[70:71], 0\n" \
  "v_mov_b64 v[72:73], 0\n" \
  "v_mov_b64 v[74:75], 0\n" \
  "v_mov_b64 v[76:77], 0\n" \
  "v_mov_b64 v[78:79], 0\n" \
  "v_mov_b64 v[80:81], 0\n" \
  "v_mov_b64 v[82:83], 0\n" \
  "v_mov_b64 v[84:85], 0\n" \
  "v_mov_b64 v[86:87], 0\n" \
  "v_mov_b64 v[88:89], 0\n" \
  "v_mov_b64 v[90:91], 0\n" \
  "v_mov_b64 v[92:93], 0\n" \
  "v_mov_b64 v[94:95], 0\n" \
  "v_mov_b64 v[96:97], 0\n" \
  "v_mov_b64 v[98:99], 0\n" \
  "v_mov_b64 v[100:101], 0\n" \
  "v_mov_b64 v[102:103], 0\n" \
  "v_mov_b64 v[104:105], 0\n" \
  "v_mov_b64 v[106:107], 0\n" \
  "v_mov_b64 v[108:109], 0\n" \
  "v_mov_b64 v[110:111], 0\n" \
  "v_mov_b64 v[112:113], 0\n" \
  "v_mov_b64 v[114:115], 0\n" \
  "v_mov_b64 v[116:117], 0\n" \
  "v_mov_b64 v[118:119], 0\n" \
  "v_mov_b64 v[120:121], 0\n" \
  "v_mov_b64 v[122:123], 0\n" \
  "v_mov_b64 v[124:125], 0\n" \
  "v_mov_b64 v[126:127], 0\n" \
  "v_mov_b32 v20, 0\n" \
  "v_mov_b32 v41, 0\n" \
  "v_mov_b32 v254, 0\n" \
  "s_branch .LVA_TAPS_%=\n" \
  ".p2align 6\n" \
  ".LVA_AL_%=:\n" \
  ".LVA_TOP_%=:\n" \
  "s_cmp_lg_u32 s63, 1\n" \
  "s_cbranch_scc1 .LVA_BAR_%=\n" \
  "s_waitcnt vmcnt(0)\n" \
  "v_min3_u32 v56, v42, v43, v41\n" \
  "v_readfirstlane_b32 s78, v254\n" \
  "v_cmp_eq_u32 vcc, 0, v56\n" \
  "s_cbranch_vccz .LVA_SEEN_%=\n" \
  "s_mov_b32 s79, 0\n" \
  "s_cmp_lg_u32 s78, 0\n" \
  "s_cbranch_scc1 .LVA_KNOWN_%=\n" \
  ".LVA_GATE_%=:\n" \
  "s_memrealtime s[74:75]\n" \
  "s_waitcnt lgkmcnt(0)\n" \
  "s_sub_i32 s74, s69, s74\n" \
  "s_cmp_gt_u32 s74, %[gv]\n" \
  "s_cbranch_scc1 .LVA_WHEN_%=\n" \
  "s_sleep 2\n" \
  "s_branch .LVA_GATE_%=\n" \
  ".LVA_NONE_%=:\n" \
  "s_sleep %[sv]\n" \
  ".LVA_WHEN_%=:\n" \
  "buffer_load_dword v254, off, s[44:47], s71 sc1\n" \
  "s_add_u32 s79, s79, 1\n" \
  "s_cmp_gt_u32 s79, 0x40000\n" \
  "s_cbranch_scc1 .LVA_KNOWN_%=\n" \
  "s_waitcnt vmcnt(0)\n" \
  "v_readfirstlane_b32 s78, v254\n" \
  "s_cmp_eq_u32 s78, 0\n" \
  "s_cbranch_scc1 .LVA_NONE_%=\n" \
  ".LVA_KNOWN_%=:\n" \
  "s_memrealtime s[74:75]\n" \
  "s_waitcnt lgkmcnt(0)\n" \
  "s_sub_i32 s74, s78, s74\n" \
  "s_cmp_lt_i32 s74, %[lt]\n" \
  "s_cselect_b32 s70, 0, 0x200000\n" \
  "s_branch .LVA_DUE_%=\n" \
  ".LVA_SOON_%=:\n" \
  "s_cmp_lt_u32 s74, 8\n" \
  "s_cbranch_scc1 .LVA_NOW_%=\n" \
  "s_sleep 1\n" \
  ".LVA_NOW_%=:\n" \
  "s_memrealtime s[74:75]\n" \
  "s_waitcnt lgkmcnt(0)\n" \
  "s_sub_i32 s74, s78, s74\n" \
  ".LVA_DUE_%=:\n" \
  "s_cmp_lt_u32 s74, 1000\n" \
  "s_cbranch_scc1 .LVA_SOON_%=\n" \
  "s_branch .LVA_ASK_%=\n" \
  ".LVA_AGAIN_%=:\n" \
  "s_sleep %[sv]\n" \
  ".LVA_ASK_%=:\n" \
  "buffer_load_dwordx2 v[42:43], v37, s[40:43], s65 offen sc1\n" \
  "buffer_load_dword v41, v38, s[44:47], s66 offen sc1\n" \
  "s_add_u32 s79, s79, 1\n" \
  "s_cmp_gt_u32 s79, 0x40000\n" \
  "s_cbranch_scc1 .LVA_SEEN_%=\n" \
  "s_waitcnt vmcnt(0)\n" \
  "v_min3_u32 v56, v42, v43, v41\n" \
  "v_cmp_eq_u32 vcc, 0, v56\n" \
  "s_cbranch_vccnz .LVA_AGAIN_%=\n" \
  ".LVA_SEEN_%=:\n" \
  "v_and_b32 v62, 0xffff0000, v41\n" \
  "v_lshlrev_b32 v60, 16, v41\n" \
  "v_lshlrev_b32 v56, 16, v42\n" \
  "v_and_b32 v57, 0xffff0000, v42\n" \
  "v_lshlrev_b32 v58, 16, v43\n" \
  "v_and_b32 v59, 0xffff0000, v43\n" \
  "v_rcp_f32 v62, v62\n" \
  "v_pk_add_f32 v[56:57], v[56:57], v[60:61] op_sel_hi:[1,0] neg_lo:[0,1] neg_hi:[0,1]\n" \
  "v_pk_add_f32 v[58:59], v[58:59], v[60:61] op_sel_hi:[1,0] neg_lo:[0,1] neg_hi:[0,1]\n" \
  "v_cvt_pk_bf16_f32 v57, v56, v57\n" \
  "v_cvt_pk_bf16_f32 v59, v58, v59\n" \
  "v_lshlrev_b32 v56, 16, v57\n" \
  "v_and_b32 v57, 0xffff0000, v57\n" \
  "v_lshlrev_b32 v58, 16, v59\n" \
  "v_and_b32 v59, 0xffff0000, v59\n" \
  "v_pk_mul_f32 v[56:57], v[56:57], v[62:63] op_sel_hi:[1,0]\n" \
  "v_pk_mul_f32 v[58:59], v[58:59], v[62:63] op_sel_hi:[1,0]\n" \
  "v_cvt_pk_bf16_f32 v56, v56, v57\n" \
  "v_cvt_pk_bf16_f32 v58, v58, v59\n" \
  "ds_write_b16 v39, v56\n" \
  "ds_write_b16_d16_hi v39, v56 offset:4288\n" \
  "ds_write_b16 v39, v58 offset:8576\n" \
  "ds_write_b16_d16_hi v39, v58 offset:12864\n" \
  "ds_write_b64 v40, v[42:43] offset:512\n" \
  ".LVA_BAR_%=:\n" \
  "s_waitcnt lgkmcnt(0)\n" \
  "s_barrier\n" \
  "ds_read_b128 v[56:59], v0 offset:0\n" \
  "ds_read_u16 v60, v2 offset:0\n" \
  "ds_read_u16 v61, v3 offset:512\n" \
  "ds_read2_b32 v[200:201], v4 offset0:0 offset1:16\n" \
  "ds_read2_b32 v[202:203], v4 offset0:1 offset1:17\n" \
  "ds_read2_b32 v[204:205], v4 offset0:2 offset1:18\n" \
  "ds_read2_b32 v[206:207], v4 offset0:3 offset1:19\n" \
  "ds_read2_b32 v[208:209], v4 offset0:4 offset1:20\n" \
  "s_setprio 3\n" \
  "s_waitcnt lgkmcnt(7)\n" \
  "v_mfma_f32_16x16x32_bf16 v[20:23], v[56:59], v[12:15], v[20:23]\n" \
  "v_mfma_f32_16x16x32_bf16 v[20:23], v[56:59], v[16:19], v[20:23]\n" \
  "s_waitcnt lgkmcnt(5)\n" \
  "v_lshlrev_b32 v60, 16, v60\n" \
  "v_lshlrev_b32 v61, 16, v61\n" \
  "v_mul_f32 v60, s64, v60\n" \
  "v_add_u32 v0, v1, v0\n" \
  "v_add_u32 v2, 64, v2\n" \
  "v_add_u32 v4, 64, v4\n" \
  "s_nop 0\n" \
  "v_cvt_pk_bf16_f32 v62, v20, v60\n" \
  "v_lshlrev_b32 v63, 16, v62\n" \
  "v_and_b32 v62, 0xffff0000, v62\n" \
  "v_add_f32 v62, v62, v63\n" \
  "v_cvt_pk_bf16_f32 v62, v62, v62\n" \
  "v_lshlrev_b32 v62, 16, v62\n" \
  "v_add_f32 v62, v62, v61\n" \
  "v_cvt_pk_bf16_f32 v62, v62, v62\n" \
  "s_mov_b64 exec, s[72:73]\n" \
  "ds_write_b16 v3, v62\n" \
  "s_mov_b64 exec, -1\n" \
  "s_cmp_gt_u32 s63, 1\n" \
  "s_waitcnt lgkmcnt(0)\n" \
  "s_barrier\n" \
  "s_cbranch_scc1 .LVA_REST_%=\n" \
  "s_cmp_eq_u32 s63, 1\n" \
  "s_cbranch_scc1 .LVA_ONE_%=\n" \
  "ds_read_b128 v[56:59], v40 offset:0\n" \
  "s_mov_b32 exec_hi, 0\n" \
  "s_waitcnt lgkmcnt(0)\n" \
  "buffer_store_dwordx4 v[56:59], v7, s[48:51], s67 offen sc1\n" \
  "s_mov_b32 exec_hi, -1\n" \
  "s_add_u32 s67, s67, 0x8000\n" \
  "s_branch .LVA_GO_%=\n" \
  ".LVA_ONE_%=:\n" \
  "s_add_u32 s65, s65, 0x8000\n" \
  "s_add_u32 s66, s66, 2048\n" \
  "s_add_u32 s71, s71, 4\n" \
  "s_max_u32 s76, s65, s70\n" \
  "s_max_u32 s78, s66, s70\n" \
  "buffer_load_dword v254, off, s[44:47], s71 sc1\n" \
  "buffer_load_dwordx2 v[42:43], v37, s[40:43], s76 offen sc1\n" \
  "buffer_load_dword v41, v38, s[44:47], s78 offen sc1\n" \
  "s_memrealtime s[74:75]\n" \
  "v_add_u32 v39, 64, v39\n" \
  "s_waitcnt lgkmcnt(0)\n" \
  "s_add_u32 s69, s74, %[gv]\n" \
  "s_branch .LVA_GO_%=\n" \
  ".LVA_REST_%=:\n" \
  "s_setprio 0\n" \
  ".LVA_GO_%=:\n" \
  "v_alignbit_b32 v246, v202, v200, v6\n" \
  "v_alignbit_b32 v247, v204, v202, v6\n" \
  "v_alignbit_b32 v248, v206, v204, v6\n" \
  "v_alignbit_b32 v249, v208, v206, v6\n" \
  "v_alignbit_b32 v250, v203, v201, v6\n" \
  "v_alignbit_b32 v251, v205, v203, v6\n" \
  "v_alignbit_b32 v252, v207, v205, v6\n" \
  "v_alignbit_b32 v253, v209, v207, v6\n" \
  "ds_read2st64_b32 v[200:201], v28 offset0:17 offset1:16\n" \
  "ds_read2st64_b32 v[202:203], v29 offset0:17 offset1:16\n" \
  "ds_read2st64_b32 v[204:205], v30 offset0:17 offset1:16\n" \
  "ds_read2st64_b32 v[206:207], v31 offset0:17 offset1:16\n" \
  "ds_read2st64_b32 v[208:209], v32 offset0:17 offset1:16\n" \
  "ds_read2st64_b32 v[210:211], v28 offset0:15 offset1:14\n" \
  "ds_read2st64_b32 v[212:213], v29 offset0:15 offset1:14\n" \
  "ds_read2st64_b32 v[214:215], v30 offset0:15 offset1:14\n" \
  "ds_read2st64_b32 v[216:217], v31 offset0:15 offset1:14\n" \
  "ds_read2st64_b32 v[218:219], v32 offset0:15 offset1:14\n" \
  "v_and_b32 v250, v250, v8\n" \
  "v_and_b32 v251, v251, v9\n" \
  "v_and_b32 v252, v252, v10\n" \
  "v_and_b32 v253, v253, v11\n" \
  "s_bitcmp1_b32 s62, 1\n" \
  "s_cbranch_scc1 .LVA_QH_%=\n" \
  "s_bitcmp1_b32 s62, 0\n" \
  "s_cbranch_scc1 .LVA_QB_%=\n" \
  ".LVA_QA_%=:\n" \
  "v_mfma_f32_16x16x32_bf16 v[64:67], v[128:131], v[246:249], v[68:71]\n" \
  "v_mfma_f32_16x16x32_bf16 v[68:71], v[132:135], v[246:249], v[72:75]\n" \
  "v_mfma_f32_16x16x32_bf16 v[72:75], v[136:139], v[246:249], v[76:79]\n" \
  "v_mfma_f32_16x16x32_bf16 v[76:79], v[140:143], v[246:249], v[80:83]\n" \
  "s_cmp_ge_u32 s62, 48\n" \
  "s_cbranch_scc1 .LVA_EA_%=\n" \
  "v_mfma_f32_16x16x32_bf16 v[80:83], v[144:147], v[246:249], v[84:87]\n" \
  "v_mfma_f32_16x16x32_bf16 v[84:87], v[148:151], v[246:249], v[88:91]\n" \
  "v_mfma_f32_16x16x32_bf16 v[88:91], v[152:155], v[246:249], v[92:95]\n" \
  "v_mfma_f32_16x16x32_bf16 v[92:95], v[156:159], v[246:249], v[96:99]\n" \
  "s_cmp_ge_u32 s62, 32\n" \
  "s_cbranch_scc1 .LVA_EA_%=\n" \
  "v_mfma_f32_16x16x32_bf16 v[96:99], v[160:163], v[246:249], v[100:103]\n" \
  "v_mfma_f32_16x16x32_bf16 v[100:103], v[164:167], v[246:249], v[104:107]\n" \
  "v_mfma_f32_16x16x32_bf16 v[104:107], v[168:171], v[246:249], v[108:111]\n" \
  "v_mfma_f32_16x16x32_bf16 v[108:111], v[172:175], v[246:249], v[112:115]\n" \
  "s_cmp_ge_u32 s62, 16\n" \
  "s_cbranch_scc1 .LVA_EA_%=\n" \
  "v_mfma_f32_16x16x32_bf16 v[112:115], v[176:179], v[246:249], v[116:119]\n" \
  "v_mfma_f32_16x16x32_bf16 v[116:119], v[180:183], v[246:249], v[120:123]\n" \
  "v_mfma_f32_16x16x32_bf16 v[120:123], v[184:187], v[246:249], v[124:127]\n" \
  "v_mfma_f32_16x16x32_bf16 v[124:127], v[188:191], v[246:249], 0\n" \
  ".LVA_EA_%=:\n" \
  "s_nop 1\n" \
  "v_mfma_f32_16x16x32_bf16 v[20:23], v[192:195], v[250:253], v[64:67]\n" \
  "s_nop 7\n" \
  "v_permlane32_swap_b32 v22, v23\n" \
  "v_add_f32 v20, v22, v23\n" \
  "s_branch .LVA_PD_%=\n" \
  ".LVA_QB_%=:\n" \
  "v_mfma_f32_16x16x32_bf16 v[64:67], v[128:131], v[246:249], v[64:67]\n" \
  "v_mfma_f32_16x16x32_bf16 v[68:71], v[132:135], v[246:249], v[68:71]\n" \
  "v_mfma_f32_16x16x32_bf16 v[72:75], v[136:139], v[246:249], v[72:75]\n" \
  "v_mfma_f32_16x16x32_bf16 v[76:79], v[140:143], v[246:249], v[76:79]\n" \
  "s_cmp_ge_u32 s62, 48\n" \
  "s_cbranch_scc1 .LVA_EB_%=\n" \
  "v_mfma_f32_16x16x32_bf16 v[80:83], v[144:147], v[246:249], v[80:83]\n" \
  "v_mfma_f32_16x16x32_bf16 v[84:87], v[148:151], v[246:249], v[84:87]\n" \
  "v_mfma_f32_16x16x32_bf16 v[88:91], v[152:155], v[246:249], v[88:91]\n" \
  "v_mfma_f32_16x16x32_bf16 v[92:95], v[156:159], v[246:249], v[92:95]\n" \
  "s_cmp_ge_u32 s62, 32\n" \
  "s_cbranch_scc1 .LVA_EB_%=\n" \
  "v_mfma_f32_16x16x32_bf16 v[96:99], v[160:163], v[246:249], v[96:99]\n" \
  "v_mfma_f32_16x16x32_bf16 v[100:103], v[164:167], v[246:249], v[100:103]\n" \
  "v_mfma_f32_16x16x32_bf16 v[104:107], v[168:171], v[246:249], v[104:107]\n" \
  "v_mfma_f32_16x16x32_bf16 v[108:111], v[172:175], v[246:249], v[108:111]\n" \
  "s_cmp_ge_u32 s62, 16\n" \
  "s_cbranch_scc1 .LVA_EB_%=\n" \
  "v_mfma_f32_16x16x32_bf16 v[112:115], v[176:179], v[246:249], v[112:115]\n" \
  "v_mfma_f32_16x16x32_bf16 v[116:119], v[180:183], v[246:249], v[116:119]\n" \
  "v_mfma_f32_16x16x32_bf16 v[120:123], v[184:187], v[246:249], v[120:123]\n" \
  "v_mfma_f32_16x16x32_bf16 v[124:127], v[188:191], v[246:249], v[124:127]\n" \
  ".LVA_EB_%=:\n" \
  "s_nop 1\n" \
  "v_mfma_f32_16x16x32_bf16 v[20:23], v[192:195], v[250:253], v[64:67]\n" \
  "s_not_b64 s[72:73], s[72:73]\n" \
  "s_nop 6\n" \
  "v_permlane32_swap_b32 v20, v21\n" \
  "v_add_f32 v20, v20, v21\n" \
  "s_branch .LVA_PD_%=\n" \
  ".LVA_QH_%=:\n" \
  "s_bitcmp1_b32 s62, 0\n" \
  "s_cbranch_scc1 .LVA_QD_%=\n" \
  ".LVA_QC_%=:\n" \
  "v_mfma_f32_16x16x32_bf16 v[64:67], v[128:131], v[246:249], v[64:67]\n" \
  "v_mfma_f32_16x16x32_bf16 v[68:71], v[132:135], v[246:249], v[68:71]\n" \
  "v_mfma_f32_16x16x32_bf16 v[72:75], v[136:139], v[246:249], v[72:75]\n" \
  "v_mfma_f32_16x16x32_bf16 v[76:79], v[140:143], v[246:249], v[76:79]\n" \
  "s_cmp_ge_u32 s62, 48\n" \
  "s_cbranch_scc1 .LVA_EC_%=\n" \
  "v_mfma_f32_16x16x32_bf16 v[80:83], v[144:147], v[246:249], v[80:83]\n" \
  "v_mfma_f32_16x16x32_bf16 v[84:87], v[148:151], v[246:249], v[84:87]\n" \
  "v_mfma_f32_16x16x32_bf16 v[88:91], v[152:155], v[246:249], v[88:91]\n" \
  "v_mfma_f32_16x16x32_bf16 v[92:95], v[156:159], v[246:249], v[92:95]\n" \
  "s_cmp_ge_u32 s62, 32\n" \
  "s_cbranch_scc1 .LVA_EC_%=\n" \
  "v_mfma_f32_16x16x32_bf16 v[96:99], v[160:163], v[246:249], v[96:99]\n" \
  "v_mfma_f32_16x16x32_bf16 v[100:103], v[164:167], v[246:249], v[100:103]\n" \
  "v_mfma_f32_16x16x32_bf16 v[104:107], v[168:171], v[246:249], v[104:107]\n" \
  "v_mfma_f32_16x16x32_bf16 v[108:111], v[172:175], v[246:249], v[108:111]\n" \
  "s_cmp_ge_u32 s62, 16\n" \
  "s_cbranch_scc1 .LVA_EC_%=\n" \
  "v_mfma_f32_16x16x32_bf16 v[112:115], v[176:179], v[246:249], v[112:115]\n" \
  "v_mfma_f32_16x16x32_bf16 v[116:119], v[180:183], v[246:249], v[116:119]\n" \
  "v_mfma_f32_16x16x32_bf16 v[120:123], v[184:187], v[246:249], v[120:123]\n" \
  "v_mfma_f32_16x16x32_bf16 v[124:127], v[188:191], v[246:249], v[124:127]\n" \
  ".LVA_EC_%=:\n" \
  "s_nop 1\n" \
  "v_mfma_f32_16x16x32_bf16 v[20:23], v[192:195], v[250:253], v[64:67]\n" \
  "s_nop 7\n" \
  "v_permlane32_swap_b32 v22, v23\n" \
  "v_add_f32 v20, v22, v23\n" \
  "s_branch .LVA_PD_%=\n" \
  ".LVA_QD_%=:\n" \
  "v_mfma_f32_16x16x32_bf16 v[64:67], v[128:131], v[246:249], v[64:67]\n" \
  "v_mfma_f32_16x16x32_bf16 v[68:71], v[132:135], v[246:249], v[68:71]\n" \
  "v_mfma_f32_16x16x32_bf16 v[72:75], v[136:139], v[246:249], v[72:75]\n" \
  "v_mfma_f32_16x16x32_bf16 v[76:79], v[140:143], v[246:249], v[76:79]\n" \
  "s_cmp_ge_u32 s62, 48\n" \
  "s_cbranch_scc1 .LVA_ED_%=\n" \
  "v_mfma_f32_16x16x32_bf16 v[80:83], v[144:147], v[246:249], v[80:83]\n" \
  "v_mfma_f32_16x16x32_bf16 v[84:87], v[148:151], v[246:249], v[84:87]\n" \
  "v_mfma_f32_16x16x32_bf16 v[88:91], v[152:155], v[246:249], v[88:91]\n" \
  "v_mfma_f32_16x16x32_bf16 v[92:95], v[156:159], v[246:249], v[92:95]\n" \
  "s_cmp_ge_u32 s62, 32\n" \
  "s_cbranch_scc1 .LVA_ED_%=\n" \
  "v_mfma_f32_16x16x32_bf16 v[96:99], v[160:163], v[246:249], v[96:99]\n" \
  "v_mfma_f32_16x16x32_bf16 v[100:103], v[164:167], v[246:249], v[100:103]\n" \
  "v_mfma_f32_16x16x32_bf16 v[104:107], v[168:171], v[246:249], v[104:107]\n" \
  "v_mfma_f32_16x16x32_bf16 v[108:111], v[172:175], v[246:249], v[108:111]\n" \
  "s_cmp_ge_u32 s62, 16\n" \
  "s_cbranch_scc1 .LVA_ED_%=\n" \
  "v_mfma_f32_16x16x32_bf16 v[112:115], v[176:179], v[246:249], v[112:115]\n" \
  "v_mfma_f32_16x16x32_bf16 v[116:119], v[180:183], v[246:249], v[116:119]\n" \
  "v_mfma_f32_16x16x32_bf16 v[120:123], v[184:187], v[246:249], v[120:123]\n" \
  "v_mfma_f32_16x16x32_bf16 v[124:127], v[188:191], v[246:249], v[124:127]\n" \
  ".LVA_ED_%=:\n" \
  "s_nop 1\n" \
  "v_mfma_f32_16x16x32_bf16 v[20:23], v[192:195], v[250:253], v[68:71]\n" \
  "s_not_b64 s[72:73], s[72:73]\n" \
  "s_nop 6\n" \
  "v_permlane32_swap_b32 v20, v21\n" \
  "v_add_f32 v20, v20, v21\n" \
  ".LVA_PD_%=:\n" \
  "s_cmp_lg_u32 s63, 0\n" \
  "s_cbranch_scc1 .LVA_NOF_%=\n" \
  "v_lshlrev_b32 v56, 6, v7\n" \
  "s_waitcnt vmcnt(0)\n" \
  "s_mov_b64 exec, 15\n" \
  "global_store_dword v56, v5, s[58:59] sc1\n" \
  "s_mov_b64 exec, -1\n" \
  "s_add_u32 s58, s58, 4096\n" \
  "s_addc_u32 s59, s59, 0\n" \
  ".LVA_NOF_%=:\n" \
  "s_add_u32 s74, s62, 12\n" \
  "s_lshl_b32 s76, s63, 1\n" \
  "s_and_b32 s76, s76, 12\n" \
  "s_xor_b32 s74, s74, s76\n" \
  "s_and_b32 s79, s74, 15\n" \
  "s_sub_u32 s74, s62, 4\n" \
  "s_cmp_lt_u32 s74, 48\n" \
  "s_cselect_b32 s79, s79, 1\n" \
  "s_cmp_lg_u32 s79, 0\n" \
  "s_cbranch_scc1 .LVA_NOS_%=\n" \
  "ds_read_u16 v230, v35 offset:0\n" \
  "ds_read_u16 v231, v35 offset:784\n" \
  "ds_read_u16 v232, v35 offset:1568\n" \
  "ds_read_u16 v233, v35 offset:2352\n" \
  "ds_read_u16 v234, v35 offset:3136\n" \
  "ds_read_u16 v235, v35 offset:3920\n" \
  "ds_read_u16 v236, v35 offset:4704\n" \
  "ds_read_u16 v237, v35 offset:5488\n" \
  "v_lshlrev_b32 v238, 16, v44\n" \
  "v_and_b32 v239, 0xffff0000, v44\n" \
  "v_lshlrev_b32 v240, 16, v45\n" \
  "v_and_b32 v241, 0xffff0000, v45\n" \
  "v_lshlrev_b32 v242, 16, v46\n" \
  "v_and_b32 v243, 0xffff0000, v46\n" \
  "v_lshlrev_b32 v244, 16, v47\n" \
  "v_and_b32 v245, 0xffff0000, v47\n" \
  "v_mov_b64 v[44:45], v[48:49]\n" \
  "v_mov_b64 v[46:47], v[50:51]\n" \
  "v_mov_b64 v[48:49], v[52:53]\n" \
  "v_mov_b64 v[50:51], v[54:55]\n" \
  "s_waitcnt lgkmcnt(0)\n" \
  "v_lshlrev_b32 v230, 16, v230\n" \
  "v_lshlrev_b32 v231, 16, v231\n" \
  "v_lshlrev_b32 v232, 16, v232\n" \
  "v_lshlrev_b32 v233, 16, v233\n" \
  "v_lshlrev_b32 v234, 16, v234\n" \
  "v_lshlrev_b32 v235, 16, v235\n" \
  "v_lshlrev_b32 v236, 16, v236\n" \
  "v_lshlrev_b32 v237, 16, v237\n" \
  "v_pk_add_f32 v[230:231], v[230:231], v[238:239]\n" \
  "v_pk_add_f32 v[232:233], v[232:233], v[240:241]\n" \
  "v_pk_add_f32 v[234:235], v[234:235], v[242:243]\n" \
  "v_pk_add_f32 v[236:237], v[236:237], v[244:245]\n" \
  "v_cvt_pk_bf16_f32 v56, v230, v231\n" \
  "v_cvt_pk_bf16_f32 v57, v232, v233\n" \
  "v_cvt_pk_bf16_f32 v58, v234, v235\n" \
  "v_cvt_pk_bf16_f32 v59, v236, v237\n" \
  "buffer_store_dwordx4 v[56:59], v36, s[52:55], s68 offen sc1\n" \
  "s_add_u32 s68, s68, 0x80000\n" \
  ".LVA_NOS_%=:\n" \
  "s_cmp_ge_u32 s62, 48\n" \
  "s_cbranch_scc1 .LVA_FIN_%=\n" \
  "s_lshr_b32 s78, s62, 2\n" \
  "s_and_b32 s74, s62, 3\n" \
  "ds_read2st64_b32 v[220:221], v28 offset0:13 offset1:12\n" \
  "ds_read2st64_b32 v[222:223], v29 offset0:13 offset1:12\n" \
  "ds_read2st64_b32 v[224:225], v30 offset0:13 offset1:12\n" \
  "ds_read2st64_b32 v[226:227], v31 offset0:13 offset1:12\n" \
  "ds_read2st64_b32 v[228:229], v32 offset0:13 offset1:12\n" \
  "s_waitcnt lgkmcnt(10)\n" \
  "v_alignbit_b32 v230, v202, v200, v6\n" \
  "v_alignbit_b32 v231, v204, v202, v6\n" \
  "v_alignbit_b32 v232, v206, v204, v6\n" \
  "v_alignbit_b32 v233, v208, v206, v6\n" \
  "v_alignbit_b32 v234, v203, v201, v6\n" \
  "v_alignbit_b32 v235, v205, v203, v6\n" \
  "v_alignbit_b32 v236, v207, v205, v6\n" \
  "v_alignbit_b32 v237, v209, v207, v6\n" \
  "s_cbranch_scc1 .LVA_NOZ_%=\n" \
  "v_mfma_f32_16x16x32_bf16 v[196:199], v[24:27], v[230:233], 0\n" \
  ".LVA_NOZ_%=:\n" \
  "s_nop 0\n" \
  "v_mfma_f32_16x16x32_bf16 v[196:199], v[128:131], v[234:237], v[196:199]\n" \
  "ds_read2st64_b32 v[200:201], v28 offset0:11 offset1:10\n" \
  "ds_read2st64_b32 v[202:203], v29 offset0:11 offset1:10\n" \
  "ds_read2st64_b32 v[204:205], v30 offset0:11 offset1:10\n" \
  "ds_read2st64_b32 v[206:207], v31 offset0:11 offset1:10\n" \
  "ds_read2st64_b32 v[208:209], v32 offset0:11 offset1:10\n" \
  "s_waitcnt lgkmcnt(10)\n" \
  "v_alignbit_b32 v238, v212, v210, v6\n" \
  "v_alignbit_b32 v239, v214, v212, v6\n" \
  "v_alignbit_b32 v240, v216, v214, v6\n" \
  "v_alignbit_b32 v241, v218, v216, v6\n" \
  "v_alignbit_b32 v242, v213, v211, v6\n" \
  "v_alignbit_b32 v243, v215, v213, v6\n" \
  "v_alignbit_b32 v244, v217, v215, v6\n" \
  "v_alignbit_b32 v245, v219, v217, v6\n" \
  "v_mfma_f32_16x16x32_bf16 v[196:199], v[132:135], v[238:241], v[196:199]\n" \
  "s_nop 0\n" \
  "v_mfma_f32_16x16x32_bf16 v[196:199], v[136:139], v[242:245], v[196:199]\n" \
  "ds_read2st64_b32 v[210:211], v28 offset0:9 offset1:8\n" \
  "ds_read2st64_b32 v[212:213], v29 offset0:9 offset1:8\n" \
  "ds_read2st64_b32 v[214:215], v30 offset0:9 offset1:8\n" \
  "ds_read2st64_b32 v[216:217], v31 offset0:9 offset1:8\n" \
  "ds_read2st64_b32 v[218:219], v32 offset0:9 offset1:8\n" \
  "s_waitcnt lgkmcnt(10)\n" \
  "v_alignbit_b32 v230, v222, v220, v6\n" \
  "v_alignbit_b32 v231, v224, v222, v6\n" \
  "v_alignbit_b32 v232, v226, v224, v6\n" \
  "v_alignbit_b32 v233, v228, v226, v6\n" \
  "v_alignbit_b32 v234, v223, v221, v6\n" \
  "v_alignbit_b32 v235, v225, v223, v6\n" \
  "v_alignbit_b32 v236, v227, v225, v6\n" \
  "v_alignbit_b32 v237, v229, v227, v6\n" \
  "v_mfma_f32_16x16x32_bf16 v[196:199], v[140:143], v[230:233], v[196:199]\n" \
  "s_nop 0\n" \
  "v_mfma_f32_16x16x32_bf16 v[196:199], v[144:147], v[234:237], v[196:199]\n" \
  "s_cmp_eq_u32 s78, 0\n" \
  "s_cbranch_scc1 .LVA_OUT_%=\n" \
  "ds_read2st64_b32 v[220:221], v28 offset0:7 offset1:6\n" \
  "ds_read2st64_b32 v[222:223], v29 offset0:7 offset1:6\n" \
  "ds_read2st64_b32 v[224:225], v30 offset0:7 offset1:6\n" \
  "ds_read2st64_b32 v[226:227], v31 offset0:7 offset1:6\n" \
  "ds_read2st64_b32 v[228:229], v32 offset0:7 offset1:6\n" \
  "s_waitcnt lgkmcnt(10)\n" \
  "v_alignbit_b32 v238, v202, v200, v6\n" \
  "v_alignbit_b32 v239, v204, v202, v6\n" \
  "v_alignbit_b32 v240, v206, v204, v6\n" \
  "v_alignbit_b32 v241, v208, v206, v6\n" \
  "v_alignbit_b32 v242, v203, v201, v6\n" \
  "v_alignbit_b32 v243, v205, v203, v6\n" \
  "v_alignbit_b32 v244, v207, v205, v6\n" \
  "v_alignbit_b32 v245, v209, v207, v6\n" \
  "v_mfma_f32_16x16x32_bf16 v[196:199], v[148:151], v[238:241], v[196:199]\n" \
  "s_cmp_eq_u32 s78, 1\n" \
  "s_cbranch_scc1 .LVA_OUT_%=\n" \
  "v_mfma_f32_16x16x32_bf16 v[196:199], v[152:155], v[242:245], v[196:199]\n" \
  "s_cmp_eq_u32 s78, 2\n" \
  "s_cbranch_scc1 .LVA_OUT_%=\n" \
  "ds_read2st64_b32 v[200:201], v28 offset0:5 offset1:4\n" \
  "ds_read2st64_b32 v[202:203], v29 offset0:5 offset1:4\n" \
  "ds_read2st64_b32 v[204:205], v30 offset0:5 offset1:4\n" \
  "ds_read2st64_b32 v[206:207], v31 offset0:5 offset1:4\n" \
  "ds_read2st64_b32 v[208:209], v32 offset0:5 offset1:4\n" \
  "s_waitcnt lgkmcnt(10)\n" \
  "v_alignbit_b32 v230, v212, v210, v6\n" \
  "v_alignbit_b32 v231, v214, v212, v6\n" \
  "v_alignbit_b32 v232, v216, v214, v6\n" \
  "v_alignbit_b32 v233, v218, v216, v6\n" \
  "v_alignbit_b32 v234, v213, v211, v6\n" \
  "v_alignbit_b32 v235, v215, v213, v6\n" \
  "v_alignbit_b32 v236, v217, v215, v6\n" \
  "v_alignbit_b32 v237, v219, v217, v6\n" \
  "v_mfma_f32_16x16x32_bf16 v[196:199], v[156:159], v[230:233], v[196:199]\n" \
  "s_cmp_eq_u32 s78, 3\n" \
  "s_cbranch_scc1 .LVA_OUT_%=\n" \
  "v_mfma_f32_16x16x32_bf16 v[196:199], v[160:163], v[234:237], v[196:199]\n" \
  "s_cmp_eq_u32 s78, 4\n" \
  "s_cbranch_scc1 .LVA_OUT_%=\n" \
  "ds_read2st64_b32 v[210:211], v28 offset0:3 offset1:2\n" \
  "ds_read2st64_b32 v[212:213], v29 offset0:3 offset1:2\n" \
  "ds_read2st64_b32 v[214:215], v30 offset0:3 offset1:2\n" \
  "ds_read2st64_b32 v[216:217], v31 offset0:3 offset1:2\n" \
  "ds_read2st64_b32 v[218:219], v32 offset0:3 offset1:2\n" \
  "s_waitcnt lgkmcnt(10)\n" \
  "v_alignbit_b32 v238, v222, v220, v6\n" \
  "v_alignbit_b32 v239, v224, v222, v6\n" \
  "v_alignbit_b32 v240, v226, v224, v6\n" \
  "v_alignbit_b32 v241, v228, v226, v6\n" \
  "v_alignbit_b32 v242, v223, v221, v6\n" \
  "v_alignbit_b32 v243, v225, v223, v6\n" \
  "v_alignbit_b32 v244, v227, v225, v6\n" \
  "v_alignbit_b32 v245, v229, v227, v6\n" \
  "v_mfma_f32_16x16x32_bf16 v[196:199], v[164:167], v[238:241], v[196:199]\n" \
  "s_cmp_eq_u32 s78, 5\n" \
  "s_cbranch_scc1 .LVA_OUT_%=\n" \
  "v_mfma_f32_16x16x32_bf16 v[196:199], v[168:171], v[242:245], v[196:199]\n" \
  "s_cmp_eq_u32 s78, 6\n" \
  "s_cbranch_scc1 .LVA_OUT_%=\n" \
  "ds_read2st64_b32 v[220:221], v28 offset0:1 offset1:0\n" \
  "ds_read2st64_b32 v[222:223], v29 offset0:1 offset1:0\n" \
  "ds_read2st64_b32 v[224:225], v30 offset0:1 offset1:0\n" \
  "ds_read2st64_b32 v[226:227], v31 offset0:1 offset1:0\n" \
  "ds_read2st64_b32 v[228:229], v32 offset0:1 offset1:0\n" \
  "s_waitcnt lgkmcnt(10)\n" \
  "v_alignbit_b32 v230, v202, v200, v6\n" \
  "v_alignbit_b32 v231, v204, v202, v6\n" \
  "v_alignbit_b32 v232, v206, v204, v6\n" \
  "v_alignbit_b32 v233, v208, v206, v6\n" \
  "v_alignbit_b32 v234, v203, v201, v6\n" \
  "v_alignbit_b32 v235, v205, v203, v6\n" \
  "v_alignbit_b32 v236, v207, v205, v6\n" \
  "v_alignbit_b32 v237, v209, v207, v6\n" \
  "v_mfma_f32_16x16x32_bf16 v[196:199], v[172:175], v[230:233], v[196:199]\n" \
  "s_cmp_eq_u32 s78, 7\n" \
  "s_cbranch_scc1 .LVA_OUT_%=\n" \
  "v_mfma_f32_16x16x32_bf16 v[196:199], v[176:179], v[234:237], v[196:199]\n" \
  "s_cmp_eq_u32 s78, 8\n" \
  "s_cbranch_scc1 .LVA_OUT_%=\n" \
  "s_waitcnt lgkmcnt(5)\n" \
  "v_alignbit_b32 v238, v212, v210, v6\n" \
  "v_alignbit_b32 v239, v214, v212, v6\n" \
  "v_alignbit_b32 v240, v216, v214, v6\n" \
  "v_alignbit_b32 v241, v218, v216, v6\n" \
  "v_alignbit_b32 v242, v213, v211, v6\n" \
  "v_alignbit_b32 v243, v215, v213, v6\n" \
  "v_alignbit_b32 v244, v217, v215, v6\n" \
  "v_alignbit_b32 v245, v219, v217, v6\n" \
  "v_mfma_f32_16x16x32_bf16 v[196:199], v[180:183], v[238:241], v[196:199]\n" \
  "s_cmp_eq_u32 s78, 9\n" \
  "s_cbranch_scc1 .LVA_OUT_%=\n" \
  "v_mfma_f32_16x16x32_bf16 v[196:199], v[184:187], v[242:245], v[196:199]\n" \
  "s_cmp_eq_u32 s78, 10\n" \
  "s_cbranch_scc1 .LVA_OUT_%=\n" \
  "s_waitcnt lgkmcnt(0)\n" \
  "v_alignbit_b32 v230, v222, v220, v6\n" \
  "v_alignbit_b32 v231, v224, v222, v6\n" \
  "v_alignbit_b32 v232, v226, v224, v6\n" \
  "v_alignbit_b32 v233, v228, v226, v6\n" \
  "s_nop 1\n" \
  "v_mfma_f32_16x16x32_bf16 v[196:199], v[188:191], v[230:233], v[196:199]\n" \
  ".LVA_OUT_%=:\n" \
  "v_add_u32 v28, 64, v28\n" \
  "v_add_u32 v29, 64, v29\n" \
  "v_add_u32 v30, 64, v30\n" \
  "v_add_u32 v31, 64, v31\n" \
  "v_add_u32 v32, 64, v32\n" \
  "s_cmp_lg_u32 s74, 3\n" \
  "s_cbranch_scc1 .LVA_FIN_%=\n" \
  "ds_read_u16 v56, v33 offset:0\n" \
  "ds_read_u16 v57, v33 offset:64\n" \
  "v_add_u32 v33, 256, v33\n" \
  "v_permlane32_swap_b32 v196, v197\n" \
  "v_permlane32_swap_b32 v198, v199\n" \
  "v_add_f32 v196, v196, v197\n" \
  "v_add_f32 v198, v198, v199\n" \
  "s_waitcnt lgkmcnt(0)\n" \
  "v_lshlrev_b32 v56, 16, v56\n" \
  "v_lshlrev_b32 v57, 16, v57\n" \
  "v_mul_f32 v56, s64, v56\n" \
  "v_mul_f32 v57, s64, v57\n" \
  "v_cvt_pk_bf16_f32 v197, v196, v198\n" \
  "v_cvt_pk_bf16_f32 v57, v56, v57\n" \
  "v_lshlrev_b32 v196, 16, v197\n" \
  "v_and_b32 v197, 0xffff0000, v197\n" \
  "v_lshlrev_b32 v56, 16, v57\n" \
  "v_and_b32 v57, 0xffff0000, v57\n" \
  "v_add_f32 v56, v56, v196\n" \
  "v_add_f32 v57, v57, v197\n" \
  "v_cvt_pk_bf16_f32 v58, v56, v57\n" \
  "ds_write_b16 v34, v58\n" \
  "ds_write_b16_d16_hi v34, v58 offset:64\n" \
  ".LVA_FIN_%=:\n" \
  "s_cmp_lg_u32 s79, 0\n" \
  "s_cbranch_scc1 .LVA_NOG_%=\n" \
  "v_lshlrev_b32 v60, 6, v7\n" \
  "s_waitcnt vmcnt(0)\n" \
  "s_mov_b64 exec, 255\n" \
  "global_store_dword v60, v5, s[60:61] sc1\n" \
  "s_mov_b64 exec, -1\n" \
  "s_add_u32 s60, s60, 0x10000\n" \
  "s_addc_u32 s61, s61, 0\n" \
  ".LVA_NOG_%=:\n" \
  "s_add_u32 s62, s62, 1\n" \
  "s_cmp_eq_u32 s62, 64\n" \
  "s_cbranch_scc1 .LVA_END_%=\n" \
  ".LVA_TAPS_%=:\n" \
  "s_and_b32 s74, s62, 3\n" \
  "s_lshl_b32 s76, s74, 6\n" \
  "v_add_u32 v56, s76, v5\n" \
  "s_cmp_eq_u32 s74, 3\n" \
  "s_cselect_b32 s74, 0, 256\n" \
  "v_add_u32 v57, s74, v56\n" \
  "ds_read_b128 v[128:131], v56 offset:3840\n" \
  "ds_read_b128 v[132:135], v56 offset:3584\n" \
  "ds_read_b128 v[136:139], v56 offset:3328\n" \
  "ds_read_b128 v[140:143], v56 offset:3072\n" \
  "ds_read_b128 v[192:195], v57 offset:3648\n" \
  "s_cmp_ge_u32 s62, 48\n" \
  "s_cbranch_scc1 .LVA_TOP_%=\n" \
  "ds_read_b128 v[144:147], v56 offset:2816\n" \
  "ds_read_b128 v[148:151], v56 offset:2560\n" \
  "ds_read_b128 v[152:155], v56 offset:2304\n" \
  "ds_read_b128 v[156:159], v56 offset:2048\n" \
  "ds_read_b128 v[160:163], v56 offset:1792\n" \
  "ds_read_b128 v[164:167], v56 offset:1536\n" \
  "ds_read_b128 v[168:171], v56 offset:1280\n" \
  "ds_read_b128 v[172:175], v56 offset:1024\n" \
  "ds_read_b128 v[176:179], v56 offset:768\n" \
  "ds_read_b128 v[180:183], v56 offset:512\n" \
  "ds_read_b128 v[184:187], v56 offset:256\n" \
  "ds_read_b128 v[188:191], v56 offset:0\n" \
  "s_branch .LVA_TOP_%=\n" \
  ".LVA_END_%=:\n" \
  "s_endpgm\n"
__device__ __forceinline__ v4u mk_srd(const void* p, const unsigned bytes) {
  const unsigned long long a = (unsigned long long)p;
  return v4u{(unsigned)a, (unsigned)(a >> 32) & 0xffffu, bytes, 0x00020000u};
}
template <bool ATT>
__device__ __forceinline__ void role_chain_t(const MegaArgs& A, char* smem, const int L, const int h, const int rep) {
  __builtin_amdgcn_s_waitcnt(0x0F70);
  u16* swo = reinterpret_cast<u16*>(smem);
  const int w = __builtin_amdgcn_readfirstlane(threadIdx.x >> 6);
  const int wc = (w + ROT) & 7, psw = PSWH;
  char* ws = A.ws;
  unsigned* FL = (unsigned*)(ws + OFF_FL);
  unsigned* TMO = FL + FL_TMO;
#define LANE_IDS()                         \
  const int l = lane_id();                 \
  const int c = l & 15, g = l >> 4;        \
  (void)c; (void)g;
  float* sgm = reinterpret_cast<float*>(smem + C_X);
  float* sgf = sgm + 8;
  unsigned* FQ = FL + FL_FQ + L * 65 * 32 * 32;
  v8u w0[8], w1[8], w2[8];
  {
    LANE_IDS();
    v4u m0r;
    const unsigned sb = lds_base(smem);
    const int q = (l >> 2) & 3, p = l & 3;
    const int f = (q & 1) | ((g & 1) << 1) | ((q >> 1) << 2);
    const unsigned ra = sb + (16 * (g >> 1) + 4 * (g & 1) + (q & 1) + 8 * (q >> 1)) * 256 + (w ^ f) * 32 + p * 8;
#define W_READ(W, hf, X)                                                       \
  {                                                                           \
    _Pragma("unroll") for (int t = 0; t < 4; ++t) {                           \
      const unsigned a_ = (ra ^ (X)) + (hf) * 8192 + t * 32768;               \
      const v2u r0 = trd(a_), r1 = trd(a_ + 512), r2 = trd(a_ + 16384), r3 = trd(a_ + 16384 + 512); \
      W[4 * (hf) + t] = v8u{r0.x, r0.y, r1.x, r1.y, r2.x, r2.y, r3.x, r3.y};  \
    }                                                                         \
    _Pragma("unroll") for (int t = 0; t < 4; ++t) asm volatile("" : "+v"(W[4 * (hf) + t])); \
  }
#define W_STAGE(M, hf, VM, NEXT)                                               \
  {                                                                           \
    WAIT_VM(VM);                                                              \
    RAW_BAR();                                                                \
    if ((M) == 0 && !ATT) { W_READ(w0, hf, 0); W_READ(w2, hf, 128); }         \
    if ((M) == 1 && !ATT) { W_READ(w1, hf, 0); }                              \
    if ((M) == 1 && ATT) { W_READ(w0, hf, 0); }                               \
    if ((M) == 2 && ATT) { W_READ(w1, hf, 0); W_READ(w2, hf, 128); }          \
    RAW_BAR();                                                                \
    NEXT;                                                                     \
  }
#define WO_ISSUE(hf)                                                          \
  {                                                                           \
    const char* base_ = (const char*)A.Wo + (size_t)(h * DH + 64 * (hf) + 8 * w) * (DM * 2); \
    _Pragma("unroll") for (int i = 0; i < 8; ++i) {                           \
      const int fd = (i & 1) | (((i >> 2) & 1) << 1) | ((w & 1) << 2);        \
      glds16(base_, (unsigned)(i * 1024 + ((l ^ (fd << 1)) << 4)), sb + w * 16384 + (hf) * 8192 + i * 1024); \
    }                                                                         \
  }
    wqkv_issue<true>(A.Wv, 0, h, w, l, sb);
    wqkv_issue<true>(A.Wv, 1, h, w, l, sb);
    asm volatile("s_nop 4\n\tglobal_load_dwordx4 %0, %1, %2" : "=v"(m0r) : "v"(((2 * (c >> 2) + (c & 1)) * DM + 64 * w + 16 * g + 8 * (((c >> 1) ^ (c >> 3)) & 1)) * 2), "s"(A.mem0) : "memory");
    W_STAGE(2, 0, 9, wqkv_issue<true>(A.Wk, 0, h, w, l, sb));
    SST(4);
    W_STAGE(2, 1, 8, wqkv_issue<true>(A.Wk, 1, h, w, l, sb));
    SST(5);
    asm volatile("" : "+v"(m0r));
    *reinterpret_cast<v4u*>(smem + C_SM + w * 1024 + l * 16) = m0r;
    {
      float f2[4] = {};
      if (ATT) {
#pragma unroll
        for (int t = 0; t < 8; ++t)
#pragma unroll
          for (int e = 0; e < 8; ++e) {
            f2[e & 1] = dot2sq(w1[t][e], f2[e & 1]);
            f2[2 + (e & 1)] = dot2sq(w2[t][e], f2[2 + (e & 1)]);
          }
      }
      const float f = wave_sum_dpp((f2[0] + f2[1]) + (f2[2] + f2[3]));
      if (l == 0) { sgf[w] = f; sgm[16] = 0.f; sgm[32 + w] = 0.f; }
    }
    v4u zt;
    v2u ze;
#define Z_LOAD()                                                                                                        \
  asm volatile("global_load_dwordx4 %0, %2, %3 sc1\n\tglobal_load_dwordx2 %1, %4, %5 sc1"                              \
               : "=&v"(zt), "=&v"(ze)                                                                                   \
               : "v"(l * 16), "s"(ws + OFF_TAG + (blockIdx.x & 7) * 1024), "v"(0), "s"(ws + OFF_TAG + 8192)              \
               : "memory")
    W_STAGE(1, 0, 8, Z_LOAD(); wqkv_issue<true>(A.Wq, 0, h, w, l, sb));
#undef Z_LOAD
    W_STAGE(1, 1, 8, wqkv_issue<true>(A.Wq, 1, h, w, l, sb));
    asm volatile("" : "+v"(zt), "+v"(ze));
    if (w == 0) {
      if (!zero_seen(zt, ze)) zero_wait(ws);
      const float f2 = (sgf[0] + sgf[1]) + (sgf[2] + sgf[3]);
      __builtin_amdgcn_raw_buffer_store_b32(__float_as_uint(f2) | 1u, MKRS(FL + FL_G, L ? 0 : 4 * 512), l * 512 + h * 4, 0, AUX_SC1);
    }
    SST(6);
    W_STAGE(0, 0, 8, WO_ISSUE(0));
    W_STAGE(0, 1, 8, WO_ISSUE(1));
    SST(7);
#undef W_STAGE
#undef W_READ
#undef WO_ISSUE
    const unsigned rb = sb + (2 * (g >> 1) + (q >> 1)) * 16384 + (4 * (g & 1) + (q & 1)) * 1024 + p * 8;
#pragma unroll
    for (int hf = 0; hf < 2; ++hf) {
      if (hf == 0) WAIT_VM(8); else WAIT_VM(0);
      RAW_BAR();
      v4u fr[8];
#pragma unroll
      for (int i = 0; i < 8; ++i) {
        const unsigned a_ = rb + hf * 8192 + (i & 1) * 65536 + ((4 * wc + ((i >> 1) ^ psw)) ^ f) * 32;
        const v2u r0 = trd(a_), r1 = trd(a_ + 2048);
        fr[i] = v4u{r0.x, r0.y, r1.x, r1.y};
      }
#pragma unroll
      for (int i = 0; i < 8; ++i) asm volatile("" : "+v"(fr[i]));
      RAW_BAR();
#pragma unroll
      for (int i = 0; i < 8; ++i) *reinterpret_cast<v4u*>(swo + w * 8192 + hf * 4096 + i * 512 + l * 8) = fr[i];
    }
  }
  WSTAMP2();
  const unsigned sb = lds_base(smem);
  const char* gK = ws + OFF_QK + L * SZ_QK + ((size_t)h * 16 + 2 * w) * 1024;
  const char* gV = ws + OFF_VF + L * SZ_O + ((size_t)h * 8 + 2 * w - 8) * 1024;
  float gw2;
  {
    const gu32* gp = (gu32*)(FL + FL_G + REP * 128) + (lane_id() & 31);
    unsigned g0, g1, g2;
    for (unsigned spins = 0;; ++spins) {
      g0 = __hip_atomic_load(gp, RLX_AGENT);
      g1 = __hip_atomic_load(gp + 32, RLX_AGENT);
      g2 = __hip_atomic_load(gp + 64, RLX_AGENT);
      if (__all(g0 != 0u && g1 != 0u && g2 != 0u)) break;
      if (spin_timeout(spins, TMO)) return;
      __builtin_amdgcn_s_sleep(4);
    }
    const float gm = wave_max_dpp(__uint_as_float(g0) * (__uint_as_float(g1) + __uint_as_float(g2)));
    asm volatile("s_nop 0\n\tv_readfirstlane_b32 %0, %1" : "=s"(gw2) : "v"(gm * 1.07f));
  }
  v8u pv;
  {
    LANE_IDS();
    const unsigned voff = g * 256 + c * 8, wp = sb + w * 16384 + l * 16;
    pv[0] = (((c >> 1) ^ (c >> 3)) & 1) ? 0xEEEE : 0x4444;
    pv[1] = sb + C_SM + l * 16;
    pv[2] = ATT ? sb + ((c & 2) ? C_SK : C_SQ) + ((2 * (c >> 2) + (c & 1)) * QS + 8 * g) * 2 : voff;
    pv[3] = ATT ? l * 8 : wp;
    pv[4] = sb + ((c & 2) ? C_SQ : C_SO) + w * 256 + ((c & 13) + ((g ^ (c >> 2)) & 2)) * 16 + 8 * ((g ^ (c >> 1)) & 1);
    pv[5] = voff;
    pv[6] = wp;
    pv[7] = sb + C_SO + (l >> 5) * 1024 + w * 256 + (l & 31) * 8;
  }
  if (!ATT) {
    if (!wait_n<4, 1>(FQ + h * 32, 1, TMO)) return;
    const unsigned lo = lane_id() * 16, d = sb + C_KT + (2 * w - 8) * 1024;
    glds16c(gK, lo, d);
    glds16c(gK + 1024, lo, d + 1024);
    glds16c(gV, lo, d + 8192);
    glds16c(gV + 1024, lo, d + 9216);
  }
  DRAIN();
  SST(8);
  const v4u rsA = mk_srd(ws + OFF_ACC + L * SZ_ACC, SZ_ACC);
  const v4u rsF = mk_srd(FQ, 64 * 32 * 32 * 4);
  const v4u rsMK = mk_srd(ws + OFF_MEMK + L * SZ_MEM, SZ_MEM);
  const v4u rsMV = mk_srd(ws + OFF_MEMV + L * SZ_MEM, SZ_MEM);
  const int wr = (w + RR) & 7;
  const char* pAB = ws + OFF_ACC + L * SZ_ACC + (wc - wr) * 4096;
  const char* pGK = gK + NH * 16 * 1024;
  const char* pGV = ATT ? (const char*)(size_t)(CDS + (XG(h) >> 1) * CDL) : gV + NH * 8 * 1024;
  unsigned sc[8];
  sc[0] = ATT ? L * 1024 + XG(h) * 256 : (32 + h) * 128;
  sc[1] = !ATT ? (h * 4 + w - 4) * 512 : (h & 7) == 1 ? L * 1024 + (h >> 3) * 256 + 128 : 1032 - L * 1024 + h * 4096;
  sc[2] = h * 2048 + w * 512;
  sc[3] = NSEG - 1;
  sc[4] = __float_as_uint(gw2);
  sc[5] = w;
  sc[6] = sb;
  const char* dbg = ws;
  const unsigned tg = (now10() / GR) * GR;
  const v4u rsT = mk_srd(FL + FL_TK, 2048);
#define CHAIN_OPS                                                                                                          \
  ::"{v[64:71]}"(w0[0]), "{v[72:79]}"(w0[1]), "{v[80:87]}"(w0[2]), "{v[88:95]}"(w0[3]), "{v[96:103]}"(w0[4]),              \
      "{v[104:111]}"(w0[5]), "{v[112:119]}"(w0[6]), "{v[120:127]}"(w0[7]), "{v[128:135]}"(w1[0]), "{v[136:143]}"(w1[1]),    \
      "{v[144:151]}"(w1[2]), "{v[152:159]}"(w1[3]), "{v[160:167]}"(w1[4]), "{v[168:175]}"(w1[5]), "{v[176:183]}"(w1[6]),    \
      "{v[184:191]}"(w1[7]), "{v[192:199]}"(w2[0]), "{v[200:207]}"(w2[1]), "{v[208:215]}"(w2[2]), "{v[216:223]}"(w2[3]),    \
      "{v[224:231]}"(w2[4]), "{v[232:239]}"(w2[5]), "{v[240:247]}"(w2[6]), "{v[248:255]}"(w2[7]), "{v[0:7]}"(pv),          \
      "{s[40:43]}"(rsA), "{s[44:47]}"(rsF), "{s[48:51]}"(rsMK), "{s[52:55]}"(rsMV), "{s[56:57]}"(pAB), "{s60}"(wr * 4096), "{s61}"(h * 1024 - wr * 4096), \
      "{s[62:63]}"(pGK), "{s[64:65]}"(pGV), "{s68}"(sc[0]), "{s69}"(sc[1]), "{s70}"(sc[2]),    \
      "{s71}"(sc[3]), "{s72}"(sc[4]), "{s73}"(sc[5]), "{s74}"(sc[6]), "{s[36:37]}"(dbg),                   \
      "{s34}"(tg), "{s35}"(psw * 1024), [gp] "n"(GP), [gr] "n"(GR), [gm] "n"(GM), \
      "{s[16:19]}"(rsT), "{s20}"(0), "{s22}"(sb + C_X + 128 + 8 * (w & 3)), "{s24}"(L * 1024), [cd] "n"(CD - CDS), \
      "{s25}"(APA - (APN + 1) * APP + L * (APB - APA + (2 * APN + 1) * APP)), [ka] "n"(APL), [an] "n"(APN), [d0] "n"(16 * CD0), [rq] "n"(RQM), [pp] "n"(APP) \
      : "memory", "vcc", "scc", "s99", "v63"
  if (ATT) asm volatile("s_mov_b64 exec, -1\ns_nop 4\n" CHAIN_ASM_ATT CHAIN_OPS);
  else asm volatile("s_mov_b64 exec, -1\ns_nop 4\n" CHAIN_ASM_VO CHAIN_OPS);
#undef CHAIN_OPS
#undef LANE_IDS
}

__device__ __forceinline__ void role_chain(const MegaArgs& A, char* smem, const int L, const int h, const int rep) {
  if (__builtin_amdgcn_readfirstlane(threadIdx.x >> 6) < 4) role_chain_t<true>(A, smem, L, h, rep);
  else role_chain_t<false>(A, smem, L, h, rep);
}

constexpr int AW_TK = 0, AW_SO = 114688, AW_M = AW_SO + 16384, AW_P = 24576, AW_END = AW_SO + 28672 + 16384;
__device__ __forceinline__ unsigned umin3(unsigned a, unsigned b, unsigned c) {
  return __builtin_elementwise_min(__builtin_elementwise_min(a, b), c);
}
__device__ __forceinline__ bool all_set(const unsigned r) {
  unsigned long long m;
  asm volatile("v_cmp_eq_u32 %0, 0, %1" : "=s"(m) : "v"(r) : "memory");
  return m == 0ull;
}
__device__ __forceinline__ unsigned long long zero_mask(const unsigned r) {
  unsigned long long m;
  asm volatile("v_cmp_eq_u32 %0, 0, %1" : "=s"(m) : "v"(r) : "memory");
  return m;
}
__device__ __forceinline__ int job_seg(const int n) {
  const int m = n - AWD;
  const int r = (m & 1) ? 64 + (m >> 1) : AWD + (m >> 1);
  return __builtin_amdgcn_readfirstlane(n > 127 ? 128 : (n < AWD || n >= 128 - AWD) ? n : r);
}
template <bool AT>
__device__ __forceinline__ void role_aw_t(const MegaArgs& A, char* smem, const int mode, const int a) {
  __builtin_amdgcn_s_waitcnt(0x0F70);
  const int w = __builtin_amdgcn_readfirstlane(threadIdx.x >> 6);
  const int l = threadIdx.x & 63, c = l & 15, g = l >> 4;
  const int qt = w & 1, hh = (w >> 1) & 1, h = 2 * a + hh;
  char* ws = A.ws;
  unsigned* FL = (unsigned*)(ws + OFF_FL);
  const unsigned sb = lds_base(smem);
  v4u wo[4][8];
  const unsigned* FQ = FL + FL_FQ + 1024 + h * 32;
  const v4u rsW = mk_srd(ws + OFF_PW, SZ_PW);
  const v4u rsF = mk_srd(FQ, (129 * 32 - h) * 128);
  const char* gQ = ws + OFF_QK + ((size_t)h * 16 + 8 * qt) * 1024;
  const char* gV = ws + OFF_VF + ((size_t)h * 8 + 4 * qt) * 1024;
  const char* gM = ws + (qt ? OFF_MEMV : OFF_MEMK) + h * 2048;
  const unsigned mo = sb + AW_M + hh * 4096;
  const unsigned tq0 = __builtin_amdgcn_readfirstlane(sb + AW_TK + (hh * 24 + 8 * qt) * 1024);
  const unsigned tv0 = __builtin_amdgcn_readfirstlane(sb + AW_TK + (hh * 24 + 16 + 4 * qt) * 1024);
  const int gs = job_seg(mode);
  bool got = false;
  {
    const int q = (l >> 2) & 3, p = l & 3, f = q | ((g & 1) << 2);
    const unsigned ra = sb + (4 * g + q) * 256 + (p & 1) * 16;
    unsigned n0 = 0u;
#pragma unroll
    for (int st = 0; st < 4; ++st) {
      if (st == 3 && got) WAIT_VM(12); else if (st == 3) WAIT_VM(0); else WAIT_VM(8);
      RAW_BAR();
#pragma unroll
      for (int k2 = 0; k2 < 2; ++k2) {
        const int ks = 2 * st + k2;
#pragma unroll
        for (int ct = 0; ct < 4; ++ct) {
          const int cp = 2 * w + (ct >> 1);
          const unsigned a_ = ra + (st & 1) * 65536 + (cp >> 2) * 16384 + k2 * 4096 + ((2 * (cp & 3) + (p >> 1)) ^ f) * 32 + (ct & 1) * 8;
          const v2u lo = trd(a_), hi = trd(a_ + 8192);
          wo[ct][ks] = v4u{lo.x, hi.x, lo.y, hi.y};
        }
#pragma unroll
        for (int ct = 0; ct < 4; ++ct) asm volatile("" : "+v"(wo[ct][ks]));
      }
      RAW_BAR();
      if (!AT && st == 1)
        asm volatile("s_nop 4\n\tbuffer_load_dword %0, %1, %2, %3 offen sc1" : "=v"(n0) : "v"(0), "s"(rsF), "s"(gs * 4096) : "memory");
      if (st < 2) wo_issue(A.Wo, a, st + 2, sb);
      if (!AT && st == 2 && all_set(n0)) {
#pragma unroll
        for (int i = 0; i < 8; ++i) glds16c(gQ + (size_t)gs * (NH * 16 * 1024) + i * 1024, l * 16, tq0 + i * 1024);
#pragma unroll
        for (int i = 0; i < 4; ++i) glds16c(gV + (size_t)gs * (NH * 8 * 1024) + i * 1024, l * 16, tv0 + i * 1024);
        got = true;
      }
    }
  }
  v8u pv, mk, wr[16];
  {
    const unsigned so = sb + AW_SO + l * 16, ta = sb + AW_TK + hh * 24576 + l * 16;
    const unsigned ix = (c & 2) ? 0xEEEE : 0x4444;
    pv = v8u{AT ? ix : l * 16u, so, (unsigned)(HK3 ? (l >> 3) * 2048 + (l & 7) * 16 : (c >> 1) * 2048 + (c & 1) * 64 + g * 16), AT ? ta : mo + qt * 2048 + l * 16, AT ? ta + qt * 4096 : ix,
             mo + (g * 8 + 2 * (c >> 2) + (c & 1)) * 16, sb + AW_M + 2048 + (g >> 1) * 512 + (c & 3) * 128 + (8 * (g & 1) + (c >> 2)) * 8,
             so + (hh * 2 + qt) * 4096};
#pragma unroll
    for (int i = 0; i < 8; ++i) mk[i] = 16 * (i >> 2) + 4 * g + (i & 3) > 16 * qt + c ? __float_as_uint(-3e38f) : 0u;
#pragma unroll
    for (int i = 0; i < 16; ++i) wr[i] = __builtin_shufflevector(wo[i >> 2][2 * (i & 3)], wo[i >> 2][2 * (i & 3) + 1], 0, 1, 2, 3, 4, 5, 6, 7);
  }
  const char* dbg = ws;
#define AW_OPS                                                                                                               \
  ::"{v[128:135]}"(wr[0]), "{v[136:143]}"(wr[1]), "{v[144:151]}"(wr[2]), "{v[152:159]}"(wr[3]), "{v[160:167]}"(wr[4]),        \
      "{v[168:175]}"(wr[5]), "{v[176:183]}"(wr[6]), "{v[184:191]}"(wr[7]), "{v[192:199]}"(wr[8]), "{v[200:207]}"(wr[9]),      \
      "{v[208:215]}"(wr[10]), "{v[216:223]}"(wr[11]), "{v[224:231]}"(wr[12]), "{v[232:239]}"(wr[13]), "{v[240:247]}"(wr[14]), \
      "{v[248:255]}"(wr[15]), "{v[0:7]}"(pv), "{v[8:15]}"(mk), "{s[40:43]}"(rsW), "{s[44:47]}"(rsF), "{s[48:49]}"(gQ),        \
      "{s[50:51]}"(gV), "{s[52:53]}"(gM), "{s[54:55]}"(FL + FL_FW + (a >> 2) * 32 + (a & 3)), "{s56}"(mode), "{s57}"(gs),      \
      "{s60}"(HK2 ? (a >> 1) * 32768 + w * 256 + (a & 1) * 128 : a * 32768 + w * 256), "{s[92:95]}"(mk_srd(ws, 0)), "{s61}"(tq0), "{s62}"(tv0), "{s63}"(AT ? mo + 2048 - (sb + AW_SO) / 2 : mo + qt * 2048), "{s[84:87]}"(mk_srd(FL + FL_TK + (a >> 2) * 64 + 32, 1028)), "{s[88:89]}"(ws + WS_TOTAL + (28 << 20) + 1024 + qt * 4),            \
      "{s66}"(AT ? AW_P + qt * 1024 + hh * 4 : HK2 ? (a >> 1) * 32768 + (w - 4) * 256 + (a & 1) * 128 : a * 32768 + (w - 4) * 8192), "{s[82:83]}"(w == 4 ? 15ull : 0ull), "{s67}"(__builtin_amdgcn_readfirstlane((int)got)), "{s80}"(0x3e0293ee), "{s[36:37]}"(dbg),        \
      [wd] "n"(AWD), [w2] "n"(128 - 2 * AWD), [w3] "n"(131 - AWD), [dt] "n"(AWDT), [pp] "n"(APP), [mo] "n"(AWMO - 63 * APP), [sm] "n"(SLP_M), [sa] "n"(SLP_A) \
      : "memory", "vcc", "scc", "s81", "v127"
  if (AT) asm volatile("s_mov_b64 exec, -1\ns_nop 4\n" AW_ASM_0 AW_OPS);
  else asm volatile("s_mov_b64 exec, -1\ns_nop 4\n" AW_ASM_1 AW_OPS);
#undef AW_OPS
}
__device__ __forceinline__ void role_aw(const MegaArgs& A, char* smem, const int L, const int a) {
  if (__builtin_amdgcn_readfirstlane(threadIdx.x >> 6) < 4) role_aw_t<true>(A, smem, L, a);
  else role_aw_t<false>(A, smem, L, a);
}

__device__ __forceinline__ void role_red(const MegaArgs& A, char* smem, const int idx) {
  const int w = __builtin_amdgcn_readfirstlane(threadIdx.x >> 6);
  const int l = threadIdx.x & 63;
  const int L = w >> 2, gr = (w >> 1) & 1, r = idx * 2 + (w & 1);
  char* ws = A.ws;
  unsigned* FL = (unsigned*)(ws + OFF_FL);
  unsigned* TMO = FL + FL_TMO;
  const __amdgpu_buffer_rsrc_t rsW = MKRS(ws + OFF_PW, SZ_PW);
  const __amdgpu_buffer_rsrc_t rsX = MKRS(ws + OFF_XA, OFF_X3 - OFF_XA + SZ_X);
  volatile lu32* sm32 = (lu32*)(unsigned long)lds_base(smem);
  volatile lu32* sw = sm32 + (w >> 1) * 32;
  if (threadIdx.x < 128) sm32[threadIdx.x] = 0u;
  __syncthreads();
  unsigned tb = now10() + (L * (APN + 2) + 1 + gr) * GPER;
  const int rn1 = idx < 8 ? 96 + idx : 224 + idx;
  (void)rn1;
  if (L) {
    const __amdgpu_buffer_rsrc_t rz = MKRS(ws + OFF_X2, SZ_X);
    for (int s = gr; s < NSEG; s += 2) ST128((v4u{0u, 0u, 0u, 0u}), rz, l * 16, (s * SEG + r) * DM * 2);
    DRAIN();
    if (l < 4) set_flag(FL + FL_FZ + (2 * l + (idx >> 3)) * 32 + (idx & 7) * 4 + (w & 3));
    if (l == 0) __hip_atomic_fetch_add((lu32*)(unsigned long)(lds_base(smem) + 480), 1u, __ATOMIC_RELAXED, __HIP_MEMORY_SCOPE_WORKGROUP);
  }
  for (unsigned spins = 0; sm32[120] < 4u && spins < 64u * SPIN_CAP; ++spins) __builtin_amdgcn_s_sleep(2);
  const int c = l & 15, g = l >> 4;
  const int pv = g * 32768 + (c >> 2) * 128 + (c & 3) * 16, xv = (16 * (g >> 1) + c) * 512 + (g & 1) * 8, ov = (g >> 1) * 256 + c * 16 + (g & 1) * 8;
  v4u sel0, sel1;
#pragma unroll
  for (int e = 0; e < 4; ++e) {
    const unsigned one = ((c & 7) >> 1) == e ? 0x3f80u << (16 * (c & 1)) : 0u;
    sel0[e] = c < 8 ? one : 0u;
    sel1[e] = c < 8 ? 0u : one;
  }
  int tries = SPIN_CAP;
  for (int s = gr; s < NSEG; s += 2) {
    const int gs = 64 * L + s;
    const unsigned* fw = FL + FL_FW + (gs * 4 + REP) * 4 * 32;
    RST(0);
    const unsigned due = grp_wait<SLP_R, 4>((w & 1) == 0, sw, s + 1, fw, 16, TMO, tb);
    tb = due + (gs >= 125 - AWD ? GATE_RT : 2 * GPER - (L ? GMS : GMR));
    {
      const unsigned t = now10(), e = (int)(due - t) > 0 ? due : t;
      __builtin_amdgcn_raw_buffer_store_b32((e + RDT) | 1u, MKRS(FL + FL_TW, r == 2 * RTW && !L ? 2048 : 0), l * 512, s * 4, AUX_SC1);
    }
#define RTS(k, t) TSR(L ? A.rn : rn1, k, s, (w & 6) * 64 + (t))
    RTS(0, 0);
    RTS(4, 64);
    const int xo_ = L * (int)(OFF_X3 - OFF_XA) + s * 32768 + r * 16;
    const v2u xa = LD64(rsX, xv, xo_), xb = LD64(rsX, xv + 16384, xo_);
    for (int i = 0; i < 256; ++i) {
      const int d = (int)(due - now10());
      if (d <= 0) break;
      if (d > 8) __builtin_amdgcn_s_sleep(1);
    }
    RST(1);
    f32x4 a0, a1;
    for (;; --tries) {
      v4u p[16];
#pragma unroll
      for (int k = 0; k < 16; ++k) {
        p[k] = LD128(rsW, pv + ((k & 1) + (k < 8 ? 2 : 0)) * 512, (gs * 16 + 2 * (k & 6)) * 32768 + (r >> 1) * 2048 + (r & 1) * 64);
        if ((k & 7) == 7) __builtin_amdgcn_sched_barrier(0);
      }
      a0 = f32x4{};
      a1 = f32x4{};
#pragma unroll
      for (int k = 0; k < 8; ++k) a1 = MFMA16(BF8((k & 1) ? sel1 : sel0), BF8(p[k]), a1);
#pragma unroll
      for (int k = 8; k < 16; ++k) a0 = MFMA16(BF8((k & 1) ? sel1 : sel0), BF8(p[k]), a0);
      if (!__any(__builtin_isunordered(a0[0], a1[0]) | __builtin_isunordered(a0[1], a1[1]) | __builtin_isunordered(a0[2], a1[2]) |
                 __builtin_isunordered(a0[3], a1[3])) || tries <= 0) break;
      asm volatile("s_sleep %0" ::"n"(SLP_P) : "memory");
    }
    RST(2);
    const unsigned nz = L ? 0u : 1u;
    const v4u xr = {xa.x, xa.y, xb.x, xb.y}, ar = {pk2(a0[0], a0[1]), pk2(a0[2], a0[3]), pk2(a1[0], a1[1]), pk2(a1[2], a1[3])};
    v4u xo;
#pragma unroll
    for (int e = 0; e < 4; ++e)
      xo[e] = __builtin_elementwise_max(pk2(lo16(xr[e]) + lo16(ar[e]), hi16(xr[e]) + hi16(ar[e])), nz);
    const __amdgpu_buffer_rsrc_t rsO = MKRS(L ? (char*)A.out : ws + OFF_X2, SZ_X);
    ST64((v2u{xo.x, xo.y}), rsO, ov, (s * SEG + r) * DM * 2);
    ST64((v2u{xo.z, xo.w}), rsO, ov + 512, (s * SEG + r) * DM * 2);
    RTS(1, 0);
    RST(3);
    RTS(5, 64);
    if (L == 0) {
      float sm = 0.f, sq = 0.f;
#pragma unroll
      for (int e = 0; e < 4; ++e) {
        const unsigned x = xo[e];
        sm = __builtin_amdgcn_fdot2_f32_bf16(__builtin_bit_cast(bf16x2_t, x), __builtin_bit_cast(bf16x2_t, 0x3f803f80u), sm, false);
        sq = dot2sq(x, sq);
      }
      float t = swap_add32(sm, sq);
      t += dppr<0x121>(t);
      t += dppr<0x122>(t);
      t += dppr<0x124>(t);
      t += dppr<0x128>(t);
      t = xsum16(t) * (1.0f / DM);
      const float ms = __builtin_bit_cast(float, __builtin_amdgcn_readlane(__builtin_bit_cast(int, t), 32));
      const float var = fmaxf(ms - t * t, 0.f);
      const unsigned st = pk2(t, __builtin_amdgcn_sqrtf(lo16(pk2(lo16(pk2(var, var)) + 1e-5f, 0.f))));
      if (l < 4) __hip_atomic_store((gu32*)(FL + FL_FR + ((s * 4 + l) * 4 + (r >> 3)) * 32 + (r & 7)), st, RLX_AGENT);
    }
    RTS(6, 64);
    RST(4);
    RTS(3, 0);
#undef RTS
  }
  RSD();
  if (idx == 0 && threadIdx.x == 384) {
    gu64* en = (gu64*)(ws + OFF_TAG + 8192);
    __hip_atomic_store(en, __hip_atomic_load(en, RLX_AGENT) + TAG_STEP, RLX_AGENT);
  }
}

constexpr int CUN = 2144;
constexpr int STN = 392;
__device__ __forceinline__ bf16x8 conv_bfrag(const unsigned* up, const unsigned sh) {
  const unsigned d0 = up[0], d1 = up[1], d2 = up[2], d3 = up[3], d4 = up[4];
  const v4u r = {__builtin_amdgcn_alignbit(d1, d0, sh), __builtin_amdgcn_alignbit(d2, d1, sh),
                 __builtin_amdgcn_alignbit(d3, d2, sh), __builtin_amdgcn_alignbit(d4, d3, sh)};
  return BF8(r);
}
__device__ __forceinline__ void role_conv(const MegaArgs& A, char* smem, const int cv) {
  __builtin_amdgcn_s_waitcnt(0x0F70);
  u16* Kr = reinterpret_cast<u16*>(smem);
  u16* Uc = Kr + 8 * 2 * CKN;
  u16* To = Uc + 8 * 2 * CUN;
  u16* Sx = To + 32 * 8;
  u16* St = Sx + 32 * 8;
  const int tid = threadIdx.x;
  const int w = __builtin_amdgcn_readfirstlane(tid >> 6);
  const int l = tid & 63, c = l & 15, g = l >> 4;
  const int d = 8 * cv + w;
  char* ws = A.ws;
  unsigned* FL = (unsigned*)(ws + OFF_FL);
  unsigned* TMO = FL + FL_TMO;
  u16* kr = Kr + w * 2 * CKN;
  float* prm = reinterpret_cast<float*>(kr);
  v4u tx[4];
  const v2u en = LD64(MKRS(ws + OFF_TAG, 8192 + 8), 0, 8192);
  {
    const unsigned pa = *reinterpret_cast<const unsigned*>(A.logAre + d * NST + 2 * l);
    const unsigned pb = *reinterpret_cast<const unsigned*>(A.Aim + d * NST + 2 * l);
    const unsigned pc = *reinterpret_cast<const unsigned*>(A.Cre + d * NST + 2 * l);
    const unsigned pd = *reinterpret_cast<const unsigned*>(A.Cim + d * NST + 2 * l);
    const float dt = bfr(expf(bf2f(A.logdt[d])));
    if (w < 2) tx[0] = *reinterpret_cast<const v4u*>(A.x0 + (size_t)tid * DM + 8 * cv);
    if (l < 16) reinterpret_cast<unsigned*>(Uc + w * CUN)[l] = 0u;
    if (l < 32) reinterpret_cast<unsigned*>(Uc + w * CUN + 32 + L_SEQ)[l] = 0u;
    if (l < 32) reinterpret_cast<unsigned*>(Uc + w * CUN + 32 + 128)[l] = 0u;
    if (l < 50) {
      reinterpret_cast<v4u*>(kr + 1808)[l] = v4u{0u, 0u, 0u, 0u};
      reinterpret_cast<v4u*>(kr + CKN + 1808)[l] = v4u{0u, 0u, 0u, 0u};
    }
#pragma unroll
    for (int e = 0; e < 2; ++e) {
      const float ar = -bfr(expf(e ? hi16(pa) : lo16(pa))) * dt, ai = (e ? hi16(pb) : lo16(pb)) * dt;
      const float ex = expf(ar);
      float sn, cs;
      sincosf(ai, &sn, &cs);
      prm[2 * l + e] = ar;
      prm[NST + 2 * l + e] = ai;
      prm[2 * NST + 2 * l + e] = (e ? hi16(pc) : lo16(pc)) * dt;
      prm[3 * NST + 2 * l + e] = (e ? hi16(pd) : lo16(pd)) * dt;
      prm[4 * NST + 2 * l + e] = ex * cs;
      prm[5 * NST + 2 * l + e] = ex * sn;
    }
  }
  const float dsk = bf2f(A.Dskip[d]);
  const u16* ka = kr + (c >> 3) * CKN + 2080 - 16 * (c & 7) + 8 * g;
  const unsigned ush = (c & 1) * 16;
#define KFRAG(D0) (*reinterpret_cast<const bf16x8*>(ka - (D0)))
  const __amdgpu_buffer_rsrc_t rz1 = MKRS(ws + OFF_XA, SZ_X);
  const int t0 = 16 * (4 * (g & 1) + (g >> 1)) + c;
  {
    const u16* uc = Uc + w * CUN;
    const unsigned* ub = reinterpret_cast<const unsigned*>(uc + (c & ~1) + 8 * g + 32);
    const __amdgpu_buffer_rsrc_t rst = MKRS(ws + OFF_GT, L_SEQ * 4);
#define TILE_OUT(dst, av, T)                                                                   \
  {                                                                                            \
    const u16* u_ = Uc + w * CUN + 32 + 128 * (T) + t0;                                        \
    const unsigned uu = pk2(bf2f(u_[0]) * dsk, bf2f(u_[32]) * dsk);                            \
    const unsigned yy = pk2(swap_add32(av[0], av[1]), swap_add32(av[2], av[3]));               \
    const unsigned aa = pk2(lo16(yy) + lo16(uu), hi16(yy) + hi16(uu));                         \
    (dst)[t0] = (u16)aa;                                                                       \
    (dst)[t0 + 32] = (u16)(aa >> 16);                                                          \
  }
#define CHUNK_ST(ck, xr, src, n)                                                               \
  {                                                                                            \
    v4u o;                                                                                     \
    _Pragma("unroll") for (int e = 0; e < 4; ++e)                                              \
      o[e] = pk2(lo16(xr[e]) + bf2f((src)[(2 * e) * (n) + l]), hi16(xr[e]) + bf2f((src)[(2 * e + 1) * (n) + l])); \
    ST128(o, rz1, (l >> 5) * 32768 + (l & 31) * 16, (2 * (ck) * 64 + cv) * 512);               \
  }
#define U_ROW(row, xr_) U_ROW2(row, xr_, __builtin_amdgcn_raw_buffer_load_b32(rst, (row) * 4, 0, AUX_SC1))
#define U_ROW2(row, xr_, stv)                                                                       \
  {                                                                                            \
    const unsigned st_ = (stv);                                                                \
    const float mb_ = lo16(st_), ri_ = 1.0f / hi16(st_);                                       \
    _Pragma("unroll") for (int e = 0; e < 4; ++e) {                                            \
      const unsigned t_ = pk2(lo16(xr_[e]) - mb_, hi16(xr_[e]) - mb_);                         \
      const unsigned q_ = pk2(lo16(t_) * ri_, hi16(t_) * ri_);                                 \
      Uc[(2 * e) * CUN + 32 + (row)] = (u16)q_;                                                \
      Uc[(2 * e + 1) * CUN + 32 + (row)] = (u16)(q_ >> 16);                                    \
    }                                                                                          \
  }
    const unsigned* FS = FL + FL_FS + REP * 32 * 32;
    const int gi = 4 * (g * 8 + (l & 7));
    const int tl = 32 * ((l >> 3) & 1) + 8 * g + (l & 7);
    unsigned khl = 0u;
#pragma unroll 1
    for (int ps = 0; ps < 4; ++ps) {
      float v[32];
      {
        const f32x4 ar = *reinterpret_cast<const f32x4*>(prm + gi), ai = *reinterpret_cast<const f32x4*>(prm + NST + gi);
        const f32x4 cr = *reinterpret_cast<const f32x4*>(prm + 2 * NST + gi), ci = *reinterpret_cast<const f32x4*>(prm + 3 * NST + gi);
        const f32x4 wr = *reinterpret_cast<const f32x4*>(prm + 4 * NST + gi), wi = *reinterpret_cast<const f32x4*>(prm + 5 * NST + gi);
        const float p0 = (float)(64 * ps + 32 * ((l >> 3) & 1));
        float yr0[4], yi0[4];
#pragma unroll
        for (int k = 0; k < 4; ++k) {
          const float e = __expf(ar[k] * p0);
          float sn, cs;
          __sincosf(ai[k] * p0, &sn, &cs);
          const float zr = e * cs, zi = e * sn;
          yr0[k] = cr[k] * zr - ci[k] * zi;
          yi0[k] = cr[k] * zi + ci[k] * zr;
        }
        f32x2_t yra = {yr0[0], yr0[1]}, yia = {yi0[0], yi0[1]}, wra = {wr[0], wr[1]}, wia = {wi[0], wi[1]};
        f32x2_t yrb = {yr0[2], yr0[3]}, yib = {yi0[2], yi0[3]}, wrb = {wr[2], wr[3]}, wib = {wi[2], wi[3]};
#pragma unroll
        for (int i = 0; i < 32; ++i) {
          const f32x2_t sm2 = yra + yrb;
          v[i] = sm2.x + sm2.y;
          const f32x2_t ta = yra * wra - yia * wia, tb = yrb * wrb - yib * wib;
          yia = yra * wia + yia * wra;
          yib = yrb * wib + yib * wrb;
          yra = ta;
          yrb = tb;
        }
      }
#pragma unroll
      for (int i = 0; i < 16; ++i) v[i] = swap_add32(v[i], v[i + 16]);
#pragma unroll
      for (int i = 0; i < 8; ++i) {
        v[i] = swap_add16(v[i], v[i + 8]);
        v[i] += dppf<0xB1>(0.f, v[i]);
        v[i] += dppf<0x4E>(0.f, v[i]);
        v[i] += dppf<0x141>(0.f, v[i]);
      }
      float s0 = v[0], s1 = v[1], s2 = v[2], s3 = v[3], s4 = v[4], s5 = v[5], s6 = v[6], s7 = v[7];
      asm volatile("" : "+v"(s0), "+v"(s1), "+v"(s2), "+v"(s3), "+v"(s4), "+v"(s5), "+v"(s6), "+v"(s7));
      s0 = (l & 4) ? s4 : s0; s1 = (l & 4) ? s5 : s1; s2 = (l & 4) ? s6 : s2; s3 = (l & 4) ? s7 : s3;
      s0 = (l & 2) ? s2 : s0; s1 = (l & 2) ? s3 : s1;
      const float kv = 2.0f * ((l & 1) ? s1 : s0);
      khl = pk2(kv, 0.f);
      khl = pk2(kv, kv - lo16(khl));
      kr[2080 - 64 * ps - tl] = (u16)khl;
      kr[CKN + 2080 - 64 * ps - tl] = (u16)(khl >> 16);
      SST(ps);
      if (ps < 2) {
        if (ps == 0) {
          if (w < 2) {
            const unsigned long long m = (((unsigned long long)en.y << 32) | en.x) + TAG_STEP;
            v4u g0;
            for (unsigned spins = 0; spins < 16u * SPIN_CAP; ++spins) {
              g0 = LD128(MKRS(ws + OFF_G0, 8192), tid * 16, REP * 2048);
              if (__all(((g0.x ^ g0.z) | (g0.y ^ (unsigned)m) | (g0.w ^ (unsigned)(m >> 32))) == 0u)) break;
              asm volatile("s_sleep 2" ::: "memory");
            }
            U_ROW2(tid, tx[0], g0.x);
          }
          __syncthreads();
        }
        f32x4 a0t = {};
#pragma unroll
        for (int b = -1; b < 4; ++b) a0t = MFMA16(KFRAG(-32 * b), conv_bfrag(ub + 16 * b, ush), a0t);
        TILE_OUT(St + w * STN + 128 * ps, a0t, 0);
        __syncthreads();
        if (w == ps) {
          CHUNK_ST(w, tx[0], St + 192 * w, STN);
          if (ps == 0) zero_wait(ws);
          DRAIN();
          if (l < 8) set_flag(FL + FL_FC + ((2 * w * 4 + l) * 8 + (cv >> 3)) * 32 + (cv & 7));
        }
        SST(4 + ps);
      }
      if (ps == 2) {
        if (w >= 2) tx[0] = *reinterpret_cast<const v4u*>(A.x0 + (size_t)tid * DM + 8 * cv);
#pragma unroll
        for (int i = 1; i < 4; ++i) tx[i] = *reinterpret_cast<const v4u*>(A.x0 + (size_t)(tid + 512 * i) * DM + 8 * cv);
      }
    }
    {
      unsigned spins = 0;
      while (!__all(__hip_atomic_load((gu32*)FS + (w + 8 * ((l >> 3) & 3)) * 32 + (l & 7), RLX_AGENT) != 0u)) {
        if (spin_timeout(++spins, TMO)) break;
        __builtin_amdgcn_s_sleep(4);
      }
      asm volatile("" ::: "memory");
    }
    if (w >= 2) U_ROW(tid, tx[0]);
#pragma unroll
    for (int i = 1; i < 4; ++i) U_ROW(tid + 512 * i, tx[i]);
    SST(6);
    LST(0);
    const gu32* kg = (gu32*)(FL + FL_KG + cv * 32 + (w >> 2));
    const __amdgpu_buffer_rsrc_t rk = MKRS(ws + OFF_KF + (size_t)d * 2 * CKN * 2, 2 * CKN * 2);
    v4u t[8];
#define K_LOAD(off)                                                                            \
  _Pragma("unroll") for (int i = 0; i < 4; ++i) {                                              \
    t[i] = LD128(rk, (64 * i + l) * 16, off);                                                  \
    t[4 + i] = LD128(rk, (64 * i + l) * 16, (off) + CKN * 2);                                  \
  }
    unsigned kf = __hip_atomic_load(kg, RLX_AGENT);
    RAW_BAR();
    LST(1);
    const bool early = __builtin_amdgcn_readfirstlane(kf) != 0u;
    K_LOAD(early ? 0 : 4 * CKN);
    kf = __hip_atomic_load(kg, RLX_AGENT);
    {
      f32x4 a1t = {};
#pragma unroll
      for (int b = -1; b < 8; ++b) a1t = MFMA16(KFRAG(128 - 32 * b), conv_bfrag(ub + 16 * b, ush), a1t);
      TILE_OUT(St + w * STN + 256, a1t, 1);
    }
    LST(2);
    RAW_BAR();
    if ((w >> 1) == 1) CHUNK_ST(w, tx[0], St + 128 + 64 * w, STN);
    SST(8);
    LST(3);
    if (!early) {
      if (__builtin_amdgcn_readfirstlane(kf) == 0u) (void)wait_n<8, 1>(FL + FL_KG + cv * 32 + (w >> 2), 1, TMO);
      asm volatile("" ::: "memory");
      K_LOAD(0);
    }
#undef K_LOAD
    SST(7);
    LST(4);
    {
#pragma unroll
      for (int i = 0; i < 4; ++i)
        if (64 * i + l < 229) {
          reinterpret_cast<v4u*>(kr)[64 * i + l] = t[i];
          reinterpret_cast<v4u*>(kr + CKN)[64 * i + l] = t[4 + i];
        }
      kr[2080 - 192 - tl] = (u16)khl;
      kr[CKN + 2080 - 192 - tl] = (u16)(khl >> 16);
    }
    unsigned* f1 = FL + FL_FC + (64 * w + (cv >> 3)) * 32 + (cv & 7);
    if ((w >> 1) == 1) {
      DRAIN();
      if (l < 8) set_flag(f1 + l * 256);
    }
    asm volatile("" ::: "memory");
    LST(5);
    {
      f32x4 a2 = {}, a3 = {};
#pragma unroll
      for (int b = -1; b < 16; ++b) {
        const bf16x8 bf = conv_bfrag(ub + 16 * b, ush);
        if (b < 12) a2 = MFMA16(KFRAG(256 - 32 * b), bf, a2);
        a3 = MFMA16(KFRAG(384 - 32 * b), bf, a3);
      }
      TILE_OUT(St + w * STN, a2, 2);
      TILE_OUT(St + w * STN + 128, a3, 3);
    }
    SST(10);
    LST(6);
    RAW_BAR();
    if (w >= 4) {
      CHUNK_ST(w, tx[0], St + 64 * w - 256, STN);
      DRAIN();
      if (l < 8) set_flag(f1 + l * 256);
    }
    if (w == 1) (void)(wait_n<SLP_V, 32>(FL + FL_FZ + REP * 64, 64, TMO));
    SST(9);
    LST(9);
    LSD();
#undef U_ROW
#undef U_ROW2
#undef TILE_OUT
#undef CHUNK_ST
  }
  const unsigned sb = lds_base(smem);
  const unsigned u1 = sb + (16 * CKN + w * CUN) * 2, u2 = u1 + 16 * CUN, to = sb + (16 * CKN + 16 * CUN) * 2, st = to + 1024;
  const unsigned win = ((c & ~1) + 8 * g) * 2, row = 16 * (g >> 1) + c;
  const bool nil = c < 8 && g < 2;
  if (l < 16) reinterpret_cast<unsigned*>(Uc + (8 + w) * CUN)[l] = 0u;
  bf16x8 th, tw;
#pragma unroll
  for (int i = 0; i < 8; ++i) {
    th[i] = (short)kr[2064 - c + 8 * g + i];
    tw[i] = (short)kr[CKN + 2064 - c + 8 * g + i];
  }
  v4u known;
#pragma unroll
  for (int e = 0; e < 4; ++e) known[e] = (c + 8 * g + 2 * e < 32 ? 0xffffu : 0u) | (c + 8 * g + 2 * e < 31 ? 0xffff0000u : 0u);
  const bf16x8 kz = KFRAG(-96);
  const v8u pa = {u2 + (nil ? 0 : c < 8 ? 32 + 16 * g : 64 + 16 * g), nil ? 0u : 64u, u2 + 64 + 2 * row, to + 16 * row + 2 * w, u2 + win,
                  sb + (unsigned)((w * 2 + (c >> 3)) * CKN + 2080 - 16 * (c & 7) + 8 * g) * 2 - 3904, ush, (unsigned)l * 16u};
  const unsigned b1 = u1 + win - 3072;
  const v8u pb = {b1, b1 + 4, b1 + 8, b1 + 12, b1 + 16, u1 + (32 + 512 + t0) * 2, st + (w * STN + t0) * 2, st + (64 * (w & 1) + l) * 2};
  const v4u pc = {(unsigned)((l >> 5) * 32768 + (l & 31) * 16), (unsigned)((l >> 1) * (DM * 2) + (l & 1) * 8), (unsigned)((l >> 4) * 128 + ((l >> 1) & 7) * 4),
                  sb + (16 * CKN + (8 + 4 * (l & 1)) * CUN + 32 + (l >> 1)) * 2};
  const char* dbg = ws;
  asm volatile("s_mov_b64 exec, -1\ns_nop 4\n" CONV_ASM
               ::"{v[0:7]}"(pa), "{v[8:11]}"(known), "{v[12:15]}"(th), "{v[16:19]}"(tw), "{v[24:27]}"(kz), "{v[28:35]}"(pb), "{v[36:39]}"(pc),
               "{v40}"(to + (w ? l * 8 : (l & 31) * 16)), "{v[44:47]}"(tx[1]), "{v[48:51]}"(tx[2]), "{v[52:55]}"(tx[3]),
               "{s[40:43]}"(mk_srd(ws + OFF_X2, SZ_X)), "{s[44:47]}"(mk_srd(FL + FL_FR, 64 * 16 * 128 + 2048)), "{s71}"(64 * 16 * 128 + REP * 512), "{s[48:51]}"(mk_srd(ws + OFF_X3, SZ_X)),
               "{s[52:55]}"(mk_srd(ws + OFF_XA, SZ_X)), "{s[58:59]}"(FL + FL_FC + (64 * 32 + (cv >> 3)) * 32 + (cv & 7)),
               "{s[60:61]}"(FL + FL_FC + (64 * (w + 8) + (cv >> 3)) * 32 + (cv & 7)), "{s62}"(0), "{s63}"(w),
               "{s64}"(__builtin_amdgcn_readfirstlane(__float_as_uint(dsk))), "{s65}"(cv * 16), "{s66}"(REP * 512), "{s67}"(cv * 512),
               "{s68}"((w + 8) * 65536 + cv * 512), "{s69}"(now10()), "{s[72:73]}"(0x0000ffff0000ffffull), "{s[36:37]}"(dbg),
               [lt] "n"(-CVLT), [gv] "n"(GATE_V), [sv] "n"(SLP_V)
               : "memory", "vcc", "scc", "s81", "v255");
#undef KFRAG
}

__device__ __forceinline__ v4u ln_row_packed(const v4u xr) {
  float v[8];
#pragma unroll
  for (int e = 0; e < 4; ++e) { v[2 * e] = lo16(xr[e]); v[2 * e + 1] = hi16(xr[e]); }
  float s = ((v[0] + v[1]) + (v[2] + v[3])) + ((v[4] + v[5]) + (v[6] + v[7]));
  s = wave_sum(s);
  const float mean = s * (1.0f / DM);
  float q = 0.f;
#pragma unroll
  for (int j = 0; j < 8; ++j) { float t = v[j] - mean; q += t * t; }
  q = wave_sum(q);
  const float var = q * (1.0f / DM);
  const float mb = bfr(mean);
  const float sd = bfr(sqrtf(bfr(bfr(var) + 1e-5f)));
  const float ri = 1.0f / sd;
  v4u o;
#pragma unroll
  for (int e = 0; e < 4; ++e) {
    const unsigned t = pk2(v[2 * e] - mb, v[2 * e + 1] - mb);
    const float x0 = lo16(t), x1 = hi16(t);
    float q0 = x0 * ri, q1 = x1 * ri;
    q0 = __builtin_fmaf(__builtin_fmaf(-q0, sd, x0), ri, q0);
    q1 = __builtin_fmaf(__builtin_fmaf(-q1, sd, x1), ri, q1);
    o[e] = pk2(q0, q1);
  }
  return o;
}

__device__ __forceinline__ float bfr1(float x) { return lo16(pk2(x, x)); }
__device__ __forceinline__ void ln4_packed(v4u (&x)[4]) {
  f32x2_t v[16];
#pragma unroll
  for (int e = 0; e < 16; ++e) v[e] = f32x2_t{lo16(x[e >> 2][e & 3]), hi16(x[e >> 2][e & 3])};
  const f32x2_t t = (((v[0] + v[1]) + (v[2] + v[3])) + ((v[4] + v[5]) + (v[6] + v[7]))) +
                    (((v[8] + v[9]) + (v[10] + v[11])) + ((v[12] + v[13]) + (v[14] + v[15])));
  const float mean = red_mod4(t.x + t.y) * (1.0f / DM);
  const float mb = bfr1(mean);
  f32x2_t q0 = {0.f, 0.f}, q1 = {0.f, 0.f};
#pragma unroll
  for (int e = 0; e < 16; e += 2) {
    v[e] -= mb;
    v[e + 1] -= mb;
    q0 += v[e] * v[e];
    q1 += v[e + 1] * v[e + 1];
  }
  q0 += q1;
  const float dm = mean - mb;
  const float var = fmaxf(red_mod4(q0.x + q0.y) * (1.0f / DM) - dm * dm, 0.f);
  const float sd = bfr1(sqrtf(bfr1(bfr1(var) + 1e-5f)));
  const float ri = 1.0f / sd;
#pragma unroll
  for (int e = 0; e < 16; ++e) {
    const unsigned t2 = pk2(v[e].x, v[e].y);
    const f32x2_t q = f32x2_t{lo16(t2), hi16(t2)} * ri;
    x[e >> 2][e & 3] = pk2(q.x, q.y);
  }
}

extern "C" __device__ float mul1(float, float) __asm("llvm.amdgcn.fmul.legacy");
template <bool G0>
__device__ __forceinline__ void role_qkv_t(const MegaArgs& A, char* smem, const int L, const int h) {
  __builtin_amdgcn_s_waitcnt(0x0F70);
  const int w = __builtin_amdgcn_readfirstlane(threadIdx.x >> 6);
  char* ws = A.ws;
  unsigned* FL = (unsigned*)(ws + OFF_FL);
  unsigned* TMO = FL + FL_TMO;
#define LANE_IDS()                         \
  const int l = lane_id();                 \
  const int c = l & 15, g = l >> 4;        \
  (void)c; (void)g;
  unsigned* FC = FL + FL_FC + (L * 64 * 4 + REP) * 8 * 32;
  bf16x8 wa[16], wb[16], wv[16];
  {
    LANE_IDS();
    const unsigned sb = lds_base(smem);
    const int q = (l >> 2) & 3, p = l & 3, f = q | ((g & 1) << 2);
    const unsigned ra = sb + (8 * g + q) * 256 + (w ^ f) * 32 + p * 8;
    const unsigned rb = sb + (8 * g + q) * 256 + ((2 * (w & 3) + (p >> 1)) ^ f) * 32 + (p & 1) * 16;
#define W_READ(W, hf, A0)                                                      \
  {                                                                           \
    _Pragma("unroll") for (int t = 0; t < 8; ++t) {                           \
      const unsigned a_ = (A0) + (hf) * 8192 + t * 16384;                     \
      const v2u r0 = trd(a_), r1 = trd(a_ + 1024);                            \
      W[8 * (hf) + t] = BF8((v4u{r0.x, r0.y, r1.x, r1.y}));                   \
    }                                                                         \
    _Pragma("unroll") for (int t = 0; t < 8; ++t) asm volatile("" : "+v"(W[8 * (hf) + t])); \
  }
#define W_STAGE(M, hf, VM, NEXT)                                               \
  {                                                                           \
    WAIT_VM(VM);                                                              \
    RAW_BAR();                                                                \
    if ((M) == 2) W_READ(wv, hf, ra);                                         \
    if ((M) == (G0 ? 1 : 0)) { W_READ(wa, hf, rb); W_READ(wb, hf, rb + 8); }  \
    RAW_BAR();                                                                \
    NEXT;                                                                     \
  }
    wqkv_issue<false>(A.Wq, 0, h, w, l, sb);
    wqkv_issue<false>(A.Wq, 1, h, w, l, sb);
    W_STAGE(0, 0, 8, wqkv_issue<false>(A.Wk, 0, h, w, l, sb));
    SST(4);
    W_STAGE(0, 1, 8, wqkv_issue<false>(A.Wk, 1, h, w, l, sb));
    SST(5);
    W_STAGE(1, 0, 8, wqkv_issue<false>(A.Wv, 0, h, w, l, sb));
    W_STAGE(1, 1, 8, wqkv_issue<false>(A.Wv, 1, h, w, l, sb));
    SST(6);
    W_STAGE(2, 0, 8, (void)0);
    W_STAGE(2, 1, 0, (void)0);
#undef W_STAGE
#undef W_READ
  }
  WSTAMP2();
  constexpr int Q_X = 98304;
  const unsigned sb = lds_base(smem);
  v2u ea, eb, ev;
  {
    LANE_IDS();
    const v4u zt = LD128(MKRS(ws + OFF_TAG, 8192 + 8), l * 16, (blockIdx.x & 7) * 1024);
    const v2u ze = LD64(MKRS(ws + OFF_TAG, 8192 + 8), 0, 8192);
    const v4u one = {0x3f803f80u, 0x3f803f80u, 0x3f803f80u, 0x3f803f80u};
    f32x4 t[3] = {};
#pragma unroll
    for (int ks = 0; ks < 16; ++ks) {
      t[0] = MFMA16(BF8(one), wa[ks], t[0]);
      t[1] = MFMA16(BF8(one), wb[ks], t[1]);
      t[2] = MFMA16(BF8(one), wv[ks], t[2]);
    }
    v2u e[3];
#pragma unroll
    for (int i = 0; i < 3; ++i) {
      const float x = g ? 0.f : t[i][0], x1 = x - bfr1(x);
      e[i] = v2u{pk2(x, x1), pk2(x1 - bfr1(x1), 0.f)};
    }
    ea = e[0]; eb = e[1]; ev = e[2];
    asm volatile("" : "+v"(ea), "+v"(eb), "+v"(ev));
    if (!zero_seen(zt, ze)) zero_wait(ws);
    if (l < 32) *reinterpret_cast<v4u*>(smem + Q_X + w * 2048 + (l >> 4) * 16384 + c * 16) = one;
  }
  constexpr int Q_ST = Q_X + 8 * 2048 + 256, Q_CNT = Q_X + 11 * 2048 + 256;
  if (threadIdx.x == 0) *(lu32*)(unsigned long)(sb + Q_CNT) = 0u;
  v8u pv, wr[24];
  v4u pw;
  unsigned sw;
  {
    LANE_IDS();
    const unsigned fa = sb + g * 512 + c * 16;
    pv = v8u{ea.x, ea.y, eb.x, eb.y, ev.x, ev.y, (unsigned)l * 16u, fa};
    pw = v4u{sb + Q_ST + c * 4, sb + Q_ST + g * 16,
             G0 ? (l & 7) * 4 + (l & 8) * 16 : (c & 8) ? sb + Q_X + ((w + 1) & 1) * 128 : fa + (w & 3) * 128, (c & 8) ? 0u : 0x8000u};
    sw = sb + Q_ST + (w & 3) * 32 + (l & 7) * 4;
#pragma unroll
    for (int i = 0; i < 8; ++i) {
      wr[i] = __builtin_shufflevector(__builtin_bit_cast(v4u, wa[2 * i]), __builtin_bit_cast(v4u, wa[2 * i + 1]), 0, 1, 2, 3, 4, 5, 6, 7);
      wr[8 + i] = __builtin_shufflevector(__builtin_bit_cast(v4u, wb[2 * i]), __builtin_bit_cast(v4u, wb[2 * i + 1]), 0, 1, 2, 3, 4, 5, 6, 7);
      wr[16 + i] = __builtin_shufflevector(__builtin_bit_cast(v4u, wv[2 * i]), __builtin_bit_cast(v4u, wv[2 * i + 1]), 0, 1, 2, 3, 4, 5, 6, 7);
    }
  }
  const char* Xb = ws + (L ? OFF_X3 : OFF_XA) + w * 8192;
  const v4u rsQ = mk_srd(ws + OFF_QK + L * SZ_QK, SZ_QK), rsV = mk_srd(ws + OFF_VF + L * SZ_O, SZ_O);
  const v4u rsM = mk_srd(ws + (G0 ? OFF_MEMK : OFF_MEMV) + L * SZ_MEM, SZ_MEM);
  const v4u rsF = mk_srd(FC + w * 64, (64 * 32 - REP * 8 - 2 * w) * 128);
  const unsigned* FQ = FL + FL_FQ + (L * 65 * 32 + h) * 32;
  const char* dbg = ws;
  __builtin_amdgcn_s_waitcnt(0x0F70);
  RAW_BAR();
#define QKV_OPS                                                                                                              \
  ::"{v[64:71]}"(wr[0]), "{v[72:79]}"(wr[1]), "{v[80:87]}"(wr[2]), "{v[88:95]}"(wr[3]), "{v[96:103]}"(wr[4]),                \
      "{v[104:111]}"(wr[5]), "{v[112:119]}"(wr[6]), "{v[120:127]}"(wr[7]), "{v[128:135]}"(wr[8]), "{v[136:143]}"(wr[9]),      \
      "{v[144:151]}"(wr[10]), "{v[152:159]}"(wr[11]), "{v[160:167]}"(wr[12]), "{v[168:175]}"(wr[13]), "{v[176:183]}"(wr[14]), \
      "{v[184:191]}"(wr[15]), "{v[192:199]}"(wr[16]), "{v[200:207]}"(wr[17]), "{v[208:215]}"(wr[18]), "{v[216:223]}"(wr[19]), \
      "{v[224:231]}"(wr[20]), "{v[232:239]}"(wr[21]), "{v[240:247]}"(wr[22]), "{v[248:255]}"(wr[23]), "{v[0:7]}"(pv),        \
      "{v[8:11]}"(pw), "{v62}"(sw), "{s[40:43]}"(rsQ), "{s[44:47]}"(rsV), "{s[48:51]}"(rsM), "{s[52:55]}"(rsF),              \
      "{s[56:57]}"(Xb), "{s58}"(sb + w * 8192), "{s59}"((h * 16 + (G0 ? 8 : 0) + (w & 3)) * 1024), "{s60}"((h * 8 + w) * 1024), \
      "{s61}"(h * 2048 + (w & 3) * 512), "{s62}"(0), "{s63}"(L), "{s[64:65]}"(FQ), "{s66}"(NSEG - 1), "{s67}"(0),            \
      "{s[70:71]}"(0xaaaaaaaaaaaaaaaaull), "{s[72:73]}"(0xccccccccccccccccull), "{s78}"(w), "{s80}"(sb + Q_CNT),             \
      "{s[36:37]}"(dbg), [gq] "n"(GATE_Q), [sq] "n"(SLP_Q), [s1] "n"(SLP_Q1 - SLP_Q)                                         \
      : "memory", "vcc", "scc", "s81", "v63"
  if (G0) asm volatile("s_mov_b64 exec, -1\ns_nop 4\n" QKV_ASM_0 QKV_OPS);
  else asm volatile("s_mov_b64 exec, -1\ns_nop 4\n" QKV_ASM_1 QKV_OPS);
#undef QKV_OPS
#undef LANE_IDS
}
__device__ __forceinline__ void role_qkv(const MegaArgs& A, char* smem, const int L, const int h) {
  if (__builtin_amdgcn_readfirstlane(threadIdx.x >> 6) < 4) role_qkv_t<true>(A, smem, L, h);
  else role_qkv_t<false>(A, smem, L, h);
}

constexpr int MEGA_WGS = 256;
constexpr size_t MEGA_LDS = (size_t)(32 * 4 * 64 * 8 + 8 * SMS + 15 * QS + 128 + 16 * 64 * 8 + 8 * SOS + 512) * 2;
static_assert(MEGA_LDS <= 163840 && MEGA_LDS >= 162816 + 256, "lds");
static_assert(162816 >= AW_END, "lds");
static_assert(MEGA_LDS >= (size_t)(8 * 2 * CKN + 8 * 2 * CUN + 2 * 32 * 8 + 8 * STN) * 2, "lds");
static_assert(MEGA_LDS >= (size_t)2 * 32 * SMS * 2 && MEGA_LDS >= 131072, "lds");
static_assert(131072 >= (size_t)(40 * NST + 32 * 400) * 4, "lds");
__global__ __launch_bounds__(512, 2) void mega_kernel(const u16* mem0, const u16* Dskip, char* ws, u16* out, const u16* x0,
                                                      const u16* logAre, const u16* Aim, const u16* Cre, const u16* Cim,
                                                      const u16* logdt, const u16* Wq, const u16* Wk, const u16* Wv,
                                                      const u16* Wo) {
  extern __shared__ __attribute__((aligned(16))) char smem[];
  const int b = __builtin_amdgcn_readfirstlane(role_no());
  const MegaArgs A{mem0, Dskip, ws, out, x0, logAre, Aim, Cre, Cim, logdt, Wq, Wk, Wv, Wo, b};
  {
    const int hi = (b >= 32 && b < 96) ? b - 32 : (b >= 200 && b < 248) ? b - 136 : (b >= 96 && b < 104) ? b + 16 : (b >= 248 ? b - 128 : -1);
    if (hi >= 0) start_kgen(A, smem, hi, b - 248, (b >= 64 && b < 104) || (b >= 232 && b < 240) ? b & 15 : -1);
  }
  if ((EXP_OFF & 1) && b >= 64 && b < 80) return;
  if ((EXP_OFF & 8) && b >= 200 && b < 232) return;
  if ((EXP_OFF & 16) && b >= 32 && b < 64) return;
  if ((EXP_OFF & 32) && b >= 80 && b < 96) return;
  if (b < 64) role_chain(A, smem, b >> 5, b & 31, 0);
  else if (b < 104 || (b >= 232 && b < 240)) role_aw(A, smem, b < 80 ? 0 : b < 96 ? 2 : 1, b & 15);
  else if (b < 168) role_conv(A, smem, b - 104);
  else if (b < 232) role_qkv(A, smem, (b - 168) >> 5, (b - 168) & 31);
  else role_red(A, smem, b - 240);
}

extern "C" void kernel_launch(void* const* d_in, const int* in_sizes, int n_in, void* d_out, int out_size, void* d_ws,
                              size_t ws_size, hipStream_t stream) {
  MegaArgs A;
  A.x0 = (const u16*)d_in[0];
  A.logAre = (const u16*)d_in[1];
  A.Aim = (const u16*)d_in[2];
  A.Cre = (const u16*)d_in[3];
  A.Cim = (const u16*)d_in[4];
  A.logdt = (const u16*)d_in[5];
  A.Dskip = (const u16*)d_in[6];
  A.mem0 = (const u16*)d_in[8];
  A.Wq = (const u16*)d_in[9];
  A.Wk = (const u16*)d_in[10];
  A.Wv = (const u16*)d_in[11];
  A.Wo = (const u16*)d_in[12];
  A.ws = (char*)d_ws;
  A.out = (u16*)d_out;

  (void)hipFuncSetAttribute((const void*)mega_kernel, hipFuncAttributeMaxDynamicSharedMemorySize, (int)MEGA_LDS);
  mega_kernel<<<MEGA_WGS, 512, MEGA_LDS, stream>>>(A.mem0, A.Dskip, A.ws, A.out, A.x0, A.logAre, A.Aim, A.Cre, A.Cim, A.logdt,
                                                   A.Wq, A.Wk, A.Wv, A.Wo);
}
```

```cpp
#define PFW 1
#define PFS 0
#include <hip/hip_runtime.h>
#include <hip/hip_bf16.h>
#include <stdint.h>

constexpr int L_SEQ = 2048;
constexpr int DM    = 512;
constexpr int NST   = 128;
constexpr int NH    = 32;
constexpr int DH    = 128;
constexpr int HD    = NH * DH;
constexpr int MM    = 8;
constexpr int SEG   = 32;
constexpr int NSEG  = L_SEQ / SEG;
constexpr int LK    = MM + SEG;

typedef unsigned short u16;
using bf16x8 = __attribute__((ext_vector_type(8))) short;
using f32x4  = __attribute__((ext_vector_type(4))) float;
using f32x16 = __attribute__((ext_vector_type(16))) float;

static __device__ __forceinline__ u16 f2bf(float f) {
  unsigned u = __float_as_uint(f);
  u += 0x7fffu + ((u >> 16) & 1u);
  return (u16)(u >> 16);
}
static __device__ __forceinline__ float bf2f(u16 h) { return __uint_as_float(((unsigned)h) << 16); }
static __device__ __forceinline__ float bfr(float f) { return bf2f(f2bf(f)); }

constexpr size_t SZ_X      = (size_t)L_SEQ * DM * 2;
constexpr size_t OFF_KF    = 0;
constexpr size_t OFF_UT    = OFF_KF + (size_t)DM * L_SEQ * 4;
constexpr size_t OFF_YT    = OFF_UT + SZ_X;
constexpr size_t OFF_XA    = OFF_YT + SZ_X;
constexpr size_t OFF_H     = OFF_XA + SZ_X;
constexpr size_t OFF_X2    = OFF_H + SZ_X;
constexpr size_t OFF_U2    = OFF_X2 + SZ_X;
constexpr size_t OFF_X3    = OFF_U2 + SZ_X;
constexpr size_t SZ_QK     = (size_t)L_SEQ * 2 * HD * 2;
constexpr size_t OFF_QK    = OFF_X3 + SZ_X;
constexpr size_t SZ_O      = (size_t)L_SEQ * HD * 2;
constexpr size_t OFF_VF    = OFF_QK + 2 * SZ_QK;
constexpr size_t SZ_PW     = (size_t)2 * NSEG * 16 * SEG * DM * 2;
constexpr size_t OFF_PW    = OFF_VF + 2 * SZ_O;
constexpr size_t SZ_MEM    = (size_t)NSEG * MM * HD * 2;
constexpr size_t OFF_MEMK  = OFF_PW + SZ_PW;
constexpr size_t OFF_MEMV  = OFF_MEMK + 2 * SZ_MEM;
constexpr size_t OFF_GT    = OFF_MEMV + 2 * SZ_MEM;
constexpr size_t SZ_ACC    = (size_t)2 * 32768;
constexpr size_t OFF_Z     = OFF_GT + (size_t)64 * DM * 4;
constexpr size_t OFF_ACC   = OFF_Z;
constexpr unsigned ACC_LO = 0x88000000u, ACC_HI = 0x87fffff7u;
constexpr size_t OFF_FL    = OFF_ACC + 2 * SZ_ACC;
constexpr int FL_FW  = 0;
constexpr int FL_FR  = FL_FW + 2 * 64 * 16 * 32;
constexpr int FL_TW  = FL_FR + 64 * 16 * 32;
constexpr int FL_FC  = FL_TW + 4 * 128;
constexpr int FL_FQ  = FL_FC + 2 * 64 * 32 * 32;
constexpr int FL_FS  = FL_FQ + 2 * 65 * 32 * 32;
constexpr int FL_FZ  = FL_FS + 4 * 32 * 32;
constexpr int FL_KG  = FL_FZ + 8 * 32;
constexpr int FL_G   = FL_KG + 64 * 32;
constexpr int FL_TK  = FL_G + 4 * 4 * 32;
constexpr int FL_TMO = FL_TK + 2 * 256;
constexpr int FL_END = FL_TMO + 64;
constexpr size_t Z_BYTES   = OFF_FL + (size_t)FL_END * 4 - OFF_Z;
constexpr size_t OFF_TAG   = OFF_Z + Z_BYTES;
constexpr unsigned long long TAG_STEP = 0x9e3779b97f4a7c15ull;
constexpr size_t OFF_G0    = OFF_TAG + 8 * 1024 + 256;
constexpr size_t WS_TOTAL  = OFF_G0 + 8192;
static_assert(Z_BYTES % 256 == 0 && OFF_Z % 256 == 0 && Z_BYTES <= 2 * 128 * 8192, "zero region");
#define REP ((blockIdx.x >> 1) & 3)
static_assert(WS_TOTAL + (4 << 20) <= (size_t)256 * 1024 * 1024, "workspace too large");

#define CKNP 0
constexpr int CKN = 2208 + CKNP;
static_assert((size_t)DM * 2 * CKN * 2 <= (size_t)DM * L_SEQ * 4 + SZ_X, "K buffer");
__device__ __forceinline__ float pos_of_index(int p) {
  if (p < 256) return (float)p;
  if (p < 384) return (float)(256 + 2 * (p - 256));
  if (p < 512) return (float)(512 + 4 * (p - 384));
  if (p < 640) return (float)(1024 + 8 * (p - 512));
  return 2048.0f;
}
__device__ __forceinline__ int index_of_l(int l) {
  float pf = bfr((float)l);
  int p = (int)pf;
  if (p < 256) return p;
  if (p < 512) return 256 + ((p - 256) >> 1);
  if (p < 1024) return 384 + ((p - 512) >> 2);
  if (p < 2048) return 512 + ((p - 1024) >> 3);
  return 640;
}

template <int CTRL> __device__ __forceinline__ float dppr(float x) {
  return __builtin_bit_cast(float, __builtin_amdgcn_update_dpp(0, __builtin_bit_cast(int, x), CTRL, 0xf, 0xf, false));
}
__device__ __forceinline__ float wave_sum(float v) {
  v += dppr<0x121>(v);
  v += dppr<0x122>(v);
  v += dppr<0x124>(v);
  v += dppr<0x128>(v);
  {
    auto r = __builtin_amdgcn_permlane16_swap(__float_as_uint(v), __float_as_uint(v), false, false);
    v = __uint_as_float(r[0]) + __uint_as_float(r[1]);
  }
  {
    auto r = __builtin_amdgcn_permlane32_swap(__float_as_uint(v), __float_as_uint(v), false, false);
    v = __uint_as_float(r[0]) + __uint_as_float(r[1]);
  }
  return v;
}
__device__ __forceinline__ void ln_row8(float (&v)[8]) {
  float s = 0.f;
#pragma unroll
  for (int j = 0; j < 8; ++j) s += v[j];
  s = wave_sum(s);
  const float mean = s * (1.0f / DM);
  float q = 0.f;
#pragma unroll
  for (int j = 0; j < 8; ++j) { float t = v[j] - mean; q += t * t; }
  q = wave_sum(q);
  const float var = q * (1.0f / DM);
  const float mb = bfr(mean);
  const float sd = bfr(sqrtf(bfr(bfr(var) + 1e-5f)));
#pragma unroll
  for (int j = 0; j < 8; ++j) v[j] = bfr(bfr(v[j] - mb) / sd);
}

#define RLX_AGENT __ATOMIC_RELAXED, __HIP_MEMORY_SCOPE_AGENT
typedef __attribute__((address_space(1))) unsigned gu32;
typedef __attribute__((address_space(3))) unsigned lu32;
typedef unsigned v4u __attribute__((ext_vector_type(4)));
constexpr int AUX_SC1 = 16;
constexpr unsigned SPIN_CAP = 1u << 18;

__device__ __forceinline__ bool wait_flags(const unsigned* flags, int lane, int nfl, unsigned want, unsigned* tmo) {
  for (unsigned spins = 0;; ++spins) {
    unsigned v = want;
    if (lane < nfl) v = __hip_atomic_load((gu32*)flags + lane * 32, RLX_AGENT);
    if (__all(v >= want)) break;
    if (spins > SPIN_CAP) {
      if (lane == 0) __hip_atomic_store((gu32*)tmo, 1u, RLX_AGENT);
      return false;
    }
  }
  __builtin_amdgcn_fence(__ATOMIC_ACQUIRE, "wavefront");
  return true;
}

#define STAMP(i)
#define STAMP_DUMP()
#define WSTAMP(i)
template <int CTRL> __device__ __forceinline__ float dppf(float old, float x) {
  return __builtin_bit_cast(float, __builtin_amdgcn_update_dpp(__builtin_bit_cast(int, old), __builtin_bit_cast(int, x), CTRL, 0xf, 0xf, false));
}
__device__ __forceinline__ float wave_max_dpp(float x) {
  const float ninf = -3e38f;
  x = fmaxf(x, dppf<0x111>(ninf, x));
  x = fmaxf(x, dppf<0x112>(ninf, x));
  x = fmaxf(x, dppf<0x114>(ninf, x));
  x = fmaxf(x, dppf<0x118>(ninf, x));
  x = fmaxf(x, dppf<0x142>(ninf, x));
  x = fmaxf(x, dppf<0x143>(ninf, x));
  return __builtin_bit_cast(float, __builtin_amdgcn_readlane(__builtin_bit_cast(int, x), 63));
}
__device__ __forceinline__ float wave_sum_dpp(float x) {
  x += dppf<0x111>(0.f, x);
  x += dppf<0x112>(0.f, x);
  x += dppf<0x114>(0.f, x);
  x += dppf<0x118>(0.f, x);
  x += dppf<0x142>(0.f, x);
  x += dppf<0x143>(0.f, x);
  return __builtin_bit_cast(float, __builtin_amdgcn_readlane(__builtin_bit_cast(int, x), 63));
}
constexpr int SMS = 528;
constexpr int QS = 144;
constexpr int VS = 80;
constexpr int SOS = 144;
#define VAR_NREP 4
constexpr int NREP = VAR_NREP;
__device__ __forceinline__ float xsum16(float x) {
  auto r = __builtin_amdgcn_permlane16_swap(__float_as_uint(x), __float_as_uint(x), false, false);
  return __uint_as_float(r[0]) + __uint_as_float(r[1]);
}
__device__ __forceinline__ float xsum32(float x) {
  auto r = __builtin_amdgcn_permlane32_swap(__float_as_uint(x), __float_as_uint(x), false, false);
  return __uint_as_float(r[0]) + __uint_as_float(r[1]);
}
__device__ __forceinline__ float xmax16(float x) {
  auto r = __builtin_amdgcn_permlane16_swap(__float_as_uint(x), __float_as_uint(x), false, false);
  return fmaxf(__uint_as_float(r[0]), __uint_as_float(r[1]));
}
__device__ __forceinline__ float xmax32(float x) {
  auto r = __builtin_amdgcn_permlane32_swap(__float_as_uint(x), __float_as_uint(x), false, false);
  return fmaxf(__uint_as_float(r[0]), __uint_as_float(r[1]));
}
__device__ __forceinline__ float red_mod4(float x) {
  x += dppf<0x124>(0.f, x);
  x += dppf<0x128>(0.f, x);
  return xsum32(xsum16(x));
}
typedef __bf16 bf16x2_t __attribute__((ext_vector_type(2)));
typedef float f32x2_t __attribute__((ext_vector_type(2)));
__device__ __forceinline__ unsigned pk2(float a, float b) {
  f32x2_t v = {a, b};
  bf16x2_t r = __builtin_convertvector(v, bf16x2_t);
  return __builtin_bit_cast(unsigned, r);
}
__device__ __forceinline__ float dot2sq(const unsigned x, const float acc) {
  return __builtin_amdgcn_fdot2_f32_bf16(__builtin_bit_cast(bf16x2_t, x), __builtin_bit_cast(bf16x2_t, x), acc, false);
}
__device__ __forceinline__ float lo16(unsigned u) { return __uint_as_float(u << 16); }
__device__ __forceinline__ float hi16(unsigned u) { return __uint_as_float(u & 0xffff0000u); }
__device__ __forceinline__ void scale_pair(float a, float b, float scale, float& oa, float& ob) {
  unsigned u = pk2(a, b);
  unsigned v = pk2(lo16(u) * scale, hi16(u) * scale);
  oa = lo16(v);
  ob = hi16(v);
}

#define XSPLIT 3
__device__ __forceinline__ int role_no() {
  const int b = blockIdx.x, x = b & 7, j = b >> 3;
  if (XSPLIT >= 3) {
    const int y = XSPLIT == 5 ? x & 3 : x >> 1;
    if (XSPLIT >= 6 && (j >> 2) == 6) return (x & 1) ? 104 + 16 * y + j - 16 : 80 + 4 * y + (j & 3);
    if (XSPLIT >= 7 && (j >> 2) == 4) return (x & 1) ? 104 + 16 * y + j - 16 : 64 + 4 * y + (j & 3);
    if (XSPLIT == 5 ? x < 4 : !(x & 1)) {
      if (j >= 16) return 104 + 16 * y + j - 16;
      return XSPLIT == 4 ? 16 * y + j : (j & 8) * 4 + 8 * y + (j & 7);
    }
    if (j < 16) return 168 + (j & 8) * 4 + 8 * y + (j & 7);
    const int i = 4 * y + (j & 3);
    return j < 20 ? 64 + i : j < 24 ? (i < 8 ? 96 + i : 224 + i) : j < 28 ? 80 + i : 240 + i;
  }
  const int L = x >> 2, xx = x & 3;
  if (j < 8) return 32 * L + 8 * xx + j;
  if (j < 12) return 64 + 16 * L + 4 * xx + j - 8;
  if (j < 16) { const int i = 4 * xx + j - 12; return L ? 240 + i : i < 8 ? 96 + i : 224 + i; }
  if (j < 24) return 104 + 8 * x + j - 16;
  return 168 + 32 * L + 8 * xx + j - 24;
}
#define MSTAMP()
#define WSTAMP2()
#define SST(k)
#define TSR(rn, k, ss, t)
#define TS(k, ss, t)
#define RST(k)
#define RSD()
#define LST(k)
#define LSD()
#define PSV
#define PST(k)
#define PSD()
#define FSD
#define FS(k)
#define FD(ss)
struct MegaArgs {
  const u16* mem0;
  const u16* Dskip;
  char* ws;
  u16* out;
  const u16* x0;
  const u16 *logAre, *Aim, *Cre, *Cim, *logdt;
  const u16 *Wq, *Wk, *Wv, *Wo;
  int rn;
};

__device__ __forceinline__ int lane_id() {
  int l;
  asm volatile("v_mbcnt_lo_u32_b32 %0, -1, 0\n\tv_mbcnt_hi_u32_b32 %0, -1, %0" : "=v"(l));
  return l;
}
__device__ __forceinline__ bool spin_timeout(unsigned spins, unsigned* tmo) {
  if (spins > SPIN_CAP) {
    if (lane_id() == 0) __hip_atomic_store((gu32*)tmo, 1u, RLX_AGENT);
    return true;
  }
  return false;
}
#define PSWH (h & 2)
#define RR 0
#define RQM 63
#define RPM 63
#define CD0 100
#define APN 7
#define APL 300
#define APA 100
#define APB 95
#define APP 204
#define CD 30
#define CDS 30
#define CDL 0
#define XG(h) ((h) >> 3)
#define GP 230
#define GR 50
#define GM 0
#define ROT (4 * (h & 1))
#define CPRIO 1
#define P1PRIO 3
#define QLEAD 2
#define POLL_SLEEP 12
#define SLP_C POLL_SLEEP
#define SLP_A POLL_SLEEP
#define SLP_W POLL_SLEEP
#define SLP_R 4
#define SLP_V 4
#define SLP_Q 4
#define SLP_M 4
#define AWPRIO 2
#define SLP_S 8
#define SPOS 4
#define CVPRE 0
#define QHV 0
#define QCHK 0
#define LEADN 99
#define GATE_V 40
#define GATE_R 380
#define GATE_N 440
#define AWAHEAD 1
#define GATE_M 150
#define GATE_M2 380
#define QPACE 0
#define QPS 6
#define QPL 100
#define GATE_Q 150
#define SLP_Q1 24
#define QPF 3
#define QPV 6
#define QVP 3
#define QP0 2
#define QP1 0
#define QFP 15
#define QUP 7
#define QEP 3
#define AWD 9
#define GPER 200
#define GMA 60
#define GMR 60
#define GMS 0
#define GSTEP 30
#define GATE_RT 130
#define GATE_1 160
#define N1S 20
#define N1E 36
#define PRI_A 3
#define PRI_WA 2
#define PRI_WB 1
#define AWSKIP 3
#define PRI_0 3
#define AWDEF 1
#define SLP_F 2
#define EXP_OFF 0
#define AWSPEC 1
#define CVTHR 48
#define NR_C 8
#define NR_R 8
#define NR_W 8
#define NR_N 2
#define QAHEAD 0
#define NS0 4
#define CVST 16
#define CVNB 9
#define CVFULL 1
#define HK3 0
#define AWDT 110
#define AWMO 0
#define HK2 0
#define RDT 185
#define RQ0 4
#define RGW 40
#define ZK 7
#define ZC 10
#define CD1 90
#define SLP_1 4
#define CSL 0
#define QSL 0
#define RSL 0
#define CTH 0
#define WORD 0
#define WXC 0
#define XI0 8
#define QFAST 58
#define CG1 110
#define RTW 0
#define CVLT 120
#define SLP_P 2
#define SLP_N POLL_SLEEP
template <int SLP, int WPL>
__device__ __forceinline__ bool wait_n(const unsigned* f, int n, unsigned* tmo) {
  const int l = lane_id();
  for (unsigned spins = 0;; ++spins) {
    unsigned v = 1u;
    if (l < n) v = __hip_atomic_load((gu32*)f + (l / WPL) * 32 + l % WPL, RLX_AGENT);
    if (__all(v != 0u)) break;
    if (spin_timeout(spins, tmo)) return false;
    __builtin_amdgcn_s_sleep(SLP);
  }
  asm volatile("" ::: "memory");
  return true;
}
__device__ __forceinline__ unsigned now10() { return (unsigned)__builtin_amdgcn_s_memrealtime(); }
__device__ __forceinline__ void gate(const unsigned t) {
#pragma unroll 1
  for (int i = 0; i < 64; ++i) {
    const int d = (int)(t - now10());
    if (d <= 0) break;
    if (d > 100) __builtin_amdgcn_s_sleep(32);
    else if (d > 25) __builtin_amdgcn_s_sleep(8);
    else __builtin_amdgcn_s_sleep(2);
  }
}
template <int SLP, int WPL>
__device__ __forceinline__ unsigned grp_wait(const bool leader, volatile lu32* sw, const unsigned val, const unsigned* f, int n,
                                             unsigned* tmo, const unsigned t) {
  unsigned r;
  gate(t);
  if (leader) {
    const int l = lane_id();
    unsigned v;
    for (unsigned spins = 0;; ++spins) {
      v = __hip_atomic_load((gu32*)f + ((l & 15) / WPL) * 32 + l % WPL, RLX_AGENT);
      if (__all(v != 0u) || spin_timeout(spins, tmo)) break;
      __builtin_amdgcn_s_sleep(SLP);
    }
    int d = (int)(v - t);
    d = max(d, __builtin_amdgcn_update_dpp(0, d, 0x121, 0xf, 0xf, false));
    d = max(d, __builtin_amdgcn_update_dpp(0, d, 0x122, 0xf, 0xf, false));
    d = max(d, __builtin_amdgcn_update_dpp(0, d, 0x124, 0xf, 0xf, false));
    d = max(d, __builtin_amdgcn_update_dpp(0, d, 0x128, 0xf, 0xf, false));
    r = t + __builtin_amdgcn_readfirstlane(d);
    sw[1 + (val & 2)] = r;
    *sw = val;
  } else {
    for (unsigned spins = 0; __builtin_amdgcn_readfirstlane(*sw) < val && spins <= 64u * SPIN_CAP; ++spins) __builtin_amdgcn_s_sleep(SLP_F);
    r = __builtin_amdgcn_readfirstlane(sw[1 + (val & 2)]);
  }
  asm volatile("" ::: "memory");
  return r;
}
template <int SLP, int WPL>
__device__ __forceinline__ bool wg_wait(const unsigned* f, int n, unsigned* tmo) {
  if (threadIdx.x < 64) (void)wait_n<SLP, WPL>(f, n, tmo);
  __syncthreads();
  asm volatile("" ::: "memory");
  return true;
}
__device__ __forceinline__ void set_flag(unsigned* f) { __hip_atomic_store((gu32*)f, 1u, RLX_AGENT); }
#define DRAIN() asm volatile("s_waitcnt vmcnt(0)" ::: "memory")
#define MKRS(ptr, bytes) __builtin_amdgcn_make_buffer_rsrc((void*)(ptr), 0, (int)(bytes), 0x00020000)
typedef unsigned v2u __attribute__((ext_vector_type(2)));
#define LD128(rs, voff, soff) __builtin_amdgcn_raw_buffer_load_b128(rs, voff, soff, AUX_SC1)
#define LD64(rs, voff, soff) __builtin_amdgcn_raw_buffer_load_b64(rs, voff, soff, AUX_SC1)
#define ST128(v, rs, voff, soff)                                              \
  do {                                                                        \
    const v4u st_d_ = (v);                                                    \
    __builtin_amdgcn_raw_buffer_store_b128(st_d_, rs, voff, soff, AUX_SC1);   \
    asm volatile("s_nop 1" ::"v"(st_d_) : "memory");                          \
  } while (0)
#define ST64(v, rs, voff, soff) __builtin_amdgcn_raw_buffer_store_b64(v, rs, voff, soff, AUX_SC1)
#define BF8(x) __builtin_bit_cast(bf16x8, x)
#define MFMA16(a, b, c) __builtin_amdgcn_mfma_f32_16x16x32_bf16(a, b, c, 0, 0, 0)
typedef short bf16x4 __attribute__((ext_vector_type(4)));
#define MFMA16K(a, b, c) \
  __builtin_amdgcn_mfma_f32_16x16x16bf16_1k(__builtin_bit_cast(bf16x4, a), __builtin_bit_cast(bf16x4, b), c, 0, 0, 0)
__device__ __forceinline__ float fmax_nn(float a, float b) { return __builtin_elementwise_maximum(a, b); }
typedef __bf16 bfv8 __attribute__((ext_vector_type(8)));
typedef __bf16 bfv16 __attribute__((ext_vector_type(16)));
typedef unsigned v8u __attribute__((ext_vector_type(8)));
#define SMF(a, b, c, idx) \
  __builtin_amdgcn_smfmac_f32_16x16x64_bf16(__builtin_bit_cast(bfv8, a), __builtin_bit_cast(bfv16, b), c, idx, 0, 0)
__device__ __forceinline__ float swap_add32(float a, float b) {
  auto r = __builtin_amdgcn_permlane32_swap(__float_as_uint(a), __float_as_uint(b), false, false);
  return __uint_as_float(r[0]) + __uint_as_float(r[1]);
}
__device__ __forceinline__ float swap_add16(float a, float b) {
  auto r = __builtin_amdgcn_permlane16_swap(__float_as_uint(a), __float_as_uint(b), false, false);
  return __uint_as_float(r[0]) + __uint_as_float(r[1]);
}
__device__ __forceinline__ v4u smf_fix(const v4u d) {
  auto x = __builtin_amdgcn_permlane16_swap(d.x, d.z, false, false);
  auto y = __builtin_amdgcn_permlane16_swap(d.y, d.w, false, false);
  return v4u{x[0], x[1], y[0], y[1]};
}
__device__ __forceinline__ v8u smf_bfrag(const u16* p) {
  const v2u a = *reinterpret_cast<const v2u*>(p), b = *reinterpret_cast<const v2u*>(p + 8);
  const v2u c = *reinterpret_cast<const v2u*>(p + 32), d = *reinterpret_cast<const v2u*>(p + 40);
  return v8u{a.x, b.x, a.y, b.y, c.x, d.x, c.y, d.y};
}

#define VAR_PPA 0
#define VAR_PPB 0
#define VAR_THR 0
#define VAR_THR2 0
#define CV_LEAD 100
#define VAR_EARLY 1
#define VAR_DRAIN 1
#define VAR_D1 0
#define VAR_D2 0
#define VAR_DR2 1
#define VAR_S1 0
#define VAR_S2 0
typedef __attribute__((address_space(1))) unsigned long long gu64;
typedef __attribute__((address_space(1))) char gch;
__device__ __forceinline__ int cvt_rpi(float x) {
  int r;
  asm("v_cvt_rpi_i32_f32 %0, %1" : "=v"(r) : "v"(x));
  return r;
}
typedef short v4s __attribute__((ext_vector_type(4)));
typedef __attribute__((address_space(3))) v4s* lp4s;
__device__ __forceinline__ void glds16(const void* sbase, const unsigned voff, const unsigned lds) {
  unsigned keep;
  asm volatile("s_nop 4\n\ts_mov_b32 %0, m0\n\ts_mov_b32 m0, %3\n\ts_nop 0\n\tglobal_load_lds_dwordx4 %1, %2\n\ts_mov_b32 m0, %0"
               : "=&s"(keep)
               : "v"(voff), "s"(sbase), "s"(lds)
               : "memory");
}
__device__ __forceinline__ void glds16c(const void* sbase, const unsigned voff, const unsigned lds) {
  unsigned keep;
  asm volatile("s_nop 4\n\ts_mov_b32 %0, m0\n\ts_mov_b32 m0, %3\n\ts_nop 0\n\tglobal_load_lds_dwordx4 %1, %2 sc1\n\ts_mov_b32 m0, %0"
               : "=&s"(keep)
               : "v"(voff), "s"(sbase), "s"(lds)
               : "memory");
}
__device__ __forceinline__ v2u trd(const unsigned a) {
  return __builtin_bit_cast(v2u, __builtin_amdgcn_ds_read_tr16_b64_v4i16((lp4s)(unsigned long)a));
}
__device__ __forceinline__ unsigned lds_base(char* smem) { return (unsigned)(unsigned long)(__attribute__((address_space(3))) char*)smem; }
#define RAW_BAR()                                           \
  do {                                                      \
    asm volatile("s_waitcnt lgkmcnt(0)" ::: "memory");      \
    __builtin_amdgcn_s_barrier();                           \
    asm volatile("" ::: "memory");                          \
  } while (0)
#define WAIT_VM(n) asm volatile("s_waitcnt vmcnt(" #n ")" ::: "memory")
template <bool SP>
__device__ __forceinline__ void w_issue(const u16* Wm, const int j, const int b, const int i, const int h, const int w, const int l, const unsigned sb) {
  const char* base = (const char*)Wm + (size_t)(128 * j + 16 * w) * (HD * 2) + h * (DH * 2);
  const int r = l >> 4;
  const int f = SP ? ((r & 1) | ((i & 1) << 1) | (((i >> 1) & 1) << 2)) : (r | (((i >> 1) & 1) << 2));
  glds16(base, (unsigned)((4 * i + r) * (HD * 2) + (((l & 15) ^ (f << 1)) << 4)), sb + w * 16384 + b * 4096 + i * 1024);
}
#define WM(k) (WORD ? (k) % 3 : (k) >> 2)
#define WJ(k) (WORD ? (k) / 3 : (k) & 3)
template <int N>
__device__ __forceinline__ void wait_vm() { asm volatile("s_waitcnt vmcnt(%0)" ::"n"(N) : "memory"); }

#define IPF 1
__device__ __forceinline__ void pull_weights(const MegaArgs& A, char* smem, const int slot, const int slots) {
  if (!PFW) return;
  const int x = blockIdx.x & 7, j = blockIdx.x >> 3, h = 8 * (x >> 1) + (j & 7), k = (j >> 3) - 1, l = lane_id();
  const bool odd = x & 1, wo = !odd && k == 1;
  const unsigned voff = wo ? l * 64 : (l >> 2) * (HD * 2) + (l & 3) * 64;
  const unsigned dst = lds_base(smem) + 162816;
  unsigned long long pk = (unsigned long long)A.Wk, pq = (unsigned long long)A.Wq, pv = (unsigned long long)A.Wv, po = (unsigned long long)A.Wo;
  asm volatile("" : "+s"(pk), "+s"(pq), "+s"(pv), "+s"(po));
  for (int i = odd ? slot * 3 + k : slot; (PFW & 1) && i < (odd ? 64 : 32); i += odd ? 3 * slots : slots) {
    const unsigned long long m = odd ? (i < 32 ? pk : pv) : (k == 0 ? pq : pk);
    const unsigned long long b = wo ? po + h * (DH * DM * 2) + i * 4096 : m + (i & 31) * (16 * HD * 2) + h * (DH * 2);
    unsigned keep;
    asm volatile("s_nop 4\n\ts_mov_b32 %0, m0\n\ts_mov_b32 m0, %3\n\ts_nop 0\n\tglobal_load_lds_dword %1, %2\n\ts_mov_b32 m0, %0"
                 : "=&s"(keep)
                 : "v"(voff), "s"(b), "s"(dst)
                 : "memory");
  }
  if (PFW & 2) {
    const unsigned long long b = po + (size_t)h * (DH * DM * 2);
    for (int i = slot * 3 + k; i < 32; i += 3 * slots) {
      unsigned keep;
      asm volatile("s_nop 4\n\ts_mov_b32 %0, m0\n\ts_mov_b32 m0, %3\n\ts_nop 0\n\tglobal_load_lds_dword %1, %2\n\ts_mov_b32 m0, %0"
                   : "=&s"(keep)
                   : "v"(l * 64), "s"(b + i * 4096), "s"(dst)
                   : "memory");
    }
  }
}
__device__ __forceinline__ void pull_slow(const MegaArgs& A, char* smem, const int w) {
  const int x = blockIdx.x & 7, l = lane_id();
  const unsigned voff = (l >> 2) * (HD * 2) + (l & 3) * 64, dst = lds_base(smem) + 162816;
#pragma unroll
  for (int e = 0; e < 4; ++e) {
    const int i = ((blockIdx.x >> 3) & 7) * 6 + w + 48 * e, pc = i / 3, sel = i - 3 * pc;
    const unsigned long long m = (unsigned long long)(e < 2 ? A.Wk : (x & 1) ? A.Wv : A.Wq);
    const int o = (int)(m >> 8), h2 = (31 - o) & 31;
    const int h = sel == 2 && (h2 >> 3) == (x >> 1) ? h2 : 8 * (x >> 1) + ((1 - o) & 3) + 4 * (sel & 1);
    unsigned keep;
    asm volatile("s_nop 4\n\ts_mov_b32 %0, m0\n\ts_mov_b32 m0, %3\n\ts_nop 0\n\tglobal_load_lds_dword %1, %2\n\ts_mov_b32 m0, %0"
                 : "=&s"(keep)
                 : "v"(voff), "s"(m + (pc & 31) * (16 * HD * 2) + h * (DH * 2)), "s"(dst)
                 : "memory");
  }
}
__device__ __forceinline__ void wo_issue(const u16* Wo, const int a, const int st, const unsigned sb) {
  const int w = __builtin_amdgcn_readfirstlane(threadIdx.x >> 6), l = lane_id(), c = l & 15, g = l >> 4;
  const char* base = (const char*)Wo + ((size_t)((2 * a + (st >> 1)) * DH + 64 * (w & 1) + 32 * (st & 1)) * DM + 128 * (w >> 1)) * 2;
#pragma unroll
  for (int i = 0; i < 8; ++i) {
    const int fd = g | ((i & 1) << 2);
    glds16(base, (unsigned)((4 * i + g) * (DM * 2) + ((c ^ (fd << 1)) << 4)), sb + (st & 1) * 65536 + w * 8192 + i * 1024);
  }
}
__device__ __forceinline__ bool zero_seen(const v4u t, const v2u e) {
  const unsigned long long m = (((unsigned long long)e.y << 32) | e.x) + TAG_STEP;
  const unsigned lo = (unsigned)m, hi = (unsigned)(m >> 32);
  return __all(((t.x ^ lo) | (t.y ^ hi) | (t.z ^ lo) | (t.w ^ hi)) == 0u);
}
__device__ __forceinline__ void zero_wait(char* ws) {
  const __amdgpu_buffer_rsrc_t rs = MKRS(ws + OFF_TAG, 8192 + 8);
  for (unsigned spins = 0; spins < 16u * SPIN_CAP; ++spins) {
    const v4u t = LD128(rs, lane_id() * 16, (blockIdx.x & 7) * 1024);
    const v2u e = LD64(rs, 0, 8192);
    if (zero_seen(t, e)) break;
    asm volatile("s_sleep 4" ::: "memory");
  }
  asm volatile("" ::: "memory");
}
__device__ __forceinline__ unsigned row_stats(const v4u raw) {
  float v[8];
#pragma unroll
  for (int e = 0; e < 4; ++e) { v[2 * e] = lo16(raw[e]); v[2 * e + 1] = hi16(raw[e]); }
  float s = 0.f;
#pragma unroll
  for (int e = 0; e < 8; ++e) s += v[e];
  const float mean = wave_sum(s) * (1.0f / DM);
  float q = 0.f;
#pragma unroll
  for (int e = 0; e < 8; ++e) { const float t = v[e] - mean; q += t * t; }
  const float var = wave_sum(q) * (1.0f / DM);
  const float sd = bfr(sqrtf(bfr(bfr(var) + 1e-5f)));
  return (unsigned)f2bf(mean) | ((unsigned)f2bf(sd) << 16);
}
__device__ __forceinline__ void sincos1(const float x, float* sn, float* cs) {
  const float t = __builtin_amdgcn_sinf(x * 0.07957747155f);
  *sn = __builtin_amdgcn_sinf(x * 0.1591549431f);
  *cs = __builtin_fmaf(-2.0f * t, t, 1.0f);
}
#define RUN_START                                                                                                   \
  const f32x2_t wra = {wr[0], wr[1]}, wia = {wi[0], wi[1]}, wrb = {wr[2], wr[3]}, wib = {wi[2], wi[3]};             \
  const f32x2_t pa = wra + wra, pb = wrb + wrb, qa = wra * wra + wia * wia, qb = wrb * wrb + wib * wib;             \
  f32x2_t ya = {yr0[0], yr0[1]}, yb = {yr0[2], yr0[3]};                                                             \
  f32x2_t za = ya * wra - f32x2_t{yi0[0], yi0[1]} * wia, zb = yb * wrb - f32x2_t{yi0[2], yi0[3]} * wib;
#define RUN_STEP                                                                                                    \
  {                                                                                                                 \
    const f32x2_t na = __builtin_elementwise_fma(pa, za, -(qa * ya)), nb = __builtin_elementwise_fma(pb, zb, -(qb * yb)); \
    ya = za; yb = zb; za = na; zb = nb;                                                                             \
  }
__device__ __forceinline__ void start_kgen(const MegaArgs& A, char* smem, const int hi, const int xi, const int aw) {
  const int tid = threadIdx.x, j = tid >> 7, u = tid & 127, d = 4 * hi + j;
  const int w = __builtin_amdgcn_readfirstlane(tid >> 6), l = tid & 63;
  float* prm = reinterpret_cast<float*>(smem) + j * 10 * NST;
  float* part = reinterpret_cast<float*>(smem) + 40 * NST + j * 8 * 400;
  unsigned* kd = reinterpret_cast<unsigned*>(smem + 131072) + j * 392;
  v4u raw[8], ra, rb;
  unsigned txt = 0u;
  if (IPF) {
    unsigned long long pc;
    int len;
    asm volatile("s_getpc_b64 %0\n.Lcode_at:\n\ts_max_i32 %1, 0, .Lcode_end-.Lcode_at" : "=s"(pc), "=s"(len)::"scc");
    const unsigned slot = ((blockIdx.x >> 3) - 8) + 8 * w;
    if ((blockIdx.x >> 6) == 1) txt = __builtin_amdgcn_raw_buffer_load_b32(MKRS(pc, len), l * 64 + slot * 4096, 0, 0);
  }
  const int r0 = 16 * xi + 2 * w;
  if (xi >= 0) {
    ra = *reinterpret_cast<const v4u*>(A.x0 + (size_t)r0 * DM + l * 8);
    rb = *reinterpret_cast<const v4u*>(A.x0 + (size_t)(r0 + 1) * DM + l * 8);
  }
  const u16 p0 = A.logdt[d], p1 = A.logAre[d * NST + u], p2 = A.Aim[d * NST + u], p3 = A.Cre[d * NST + u], p4 = A.Cim[d * NST + u];
  if (w >= 6) {
#pragma unroll
    for (int i = 0; i < 8; ++i) raw[i] = *reinterpret_cast<const v4u*>(A.x0 + (size_t)(16 * hi + 8 * (w - 6) + i) * DM + l * 8);
  }
  const v2u en = LD64(MKRS(A.ws + OFF_TAG, 8192 + 8), 0, 8192);
  {
    const __amdgpu_buffer_rsrc_t rz = MKRS(A.ws + OFF_Z, Z_BYTES);
    const unsigned i0 = hi < 16 ? ACC_LO : 0u, i1 = hi < 16 ? ACC_HI : 0u;
    ST128((v4u{i0, i1, i0, i1}), rz, tid * 16, hi * 8192);
    ST128((v4u{0u, 0u, 0u, 0u}), rz, tid * 16, (128 + hi) * 8192);
  }
  if (xi >= 0) {
    const unsigned sa = row_stats(ra), sb = row_stats(rb), st = (l & 1) ? sb : sa;
    const unsigned long long m = (((unsigned long long)en.y << 32) | en.x) + TAG_STEP;
    ST128((v4u{st, (unsigned)m, st, (unsigned)(m >> 32)}), MKRS(A.ws + OFF_G0, 8192), (l >> 1) * 2048 + (l & 1) * 16, r0 * 16);
    SST(11);
  }
  const bool ps = PFS && w < 6 && (blockIdx.x >> 6) == 1;
  asm volatile("" ::"v"(txt), "v"(p0), "v"(p1), "v"(p2), "v"(p3), "v"(p4) : "memory");
  if (ps) pull_slow(A, smem, w);
  {
    const float dt = bfr(expf(bf2f(p0)));
    const float ar = -bfr(expf(bf2f(p1))) * dt, ai = bf2f(p2) * dt;
    prm[u] = ar;
    prm[NST + u] = ai;
    prm[2 * NST + u] = bf2f(p3) * dt;
    prm[3 * NST + u] = bf2f(p4) * dt;
#pragma unroll
    for (int cls = 1; cls < 4; ++cls) {
      const float st = (float)(1 << cls);
      const float e = expf(ar * st);
      float sn, cs;
      sincos1(ai * st, &sn, &cs);
      prm[(2 + 2 * cls) * NST + u] = e * cs;
      prm[(3 + 2 * cls) * NST + u] = e * sn;
    }
  }
  if (ps) WAIT_VM(4); else WAIT_VM(0);
  RAW_BAR();
  SST(1);
  if (w >= 6) {
    __builtin_amdgcn_s_waitcnt(0x0F70);
    if (w == 7 && l < 8)
      __hip_atomic_store((gu64*)(A.ws + OFF_TAG) + l * 128 + hi, (((unsigned long long)en.y << 32) | en.x) + TAG_STEP, RLX_AGENT);
    __builtin_amdgcn_s_setprio(3);
    if (hi >= 32) pull_weights(A, smem, w - 6, 2);
    v4u wr[32];
    const u16* wop = A.Wo + (size_t)(hi * DH + 64 * (w - 6)) * DM + l * 8;
    if (hi < 32) {
#pragma unroll
      for (int i = 0; i < 32; ++i) wr[i] = *reinterpret_cast<const v4u*>(wop + i * DM);
    }
    unsigned stw = 0u;
#pragma unroll
    for (int i = 0; i < 8; ++i) {
      const unsigned t = row_stats(raw[i]);
      if (l == i) stw = t;
    }
    const int sx = 2 * hi + w - 6;
    if (l < 8) __hip_atomic_store((gu32*)(A.ws + OFF_GT) + 8 * sx + l, stw, RLX_AGENT);
    zero_wait(A.ws);
    DRAIN();
    if (l < 4) set_flag((unsigned*)(A.ws + OFF_FL) + FL_FS + (l * 32 + (sx >> 3)) * 32 + (sx & 7));
    if (hi < 32) {
      float c0 = 0.f, c1 = 0.f, c2 = 0.f, c3 = 0.f;
#pragma unroll
      for (int i = 0; i < 64; ++i) {
        const v4u x = wr[i & 31];
        if (i < 32) wr[i] = *reinterpret_cast<const v4u*>(wop + (32 + i) * DM);
        c0 = dot2sq(x.x, c0); c1 = dot2sq(x.y, c1); c2 = dot2sq(x.z, c2); c3 = dot2sq(x.w, c3);
      }
      const float m = wave_max_dpp(fmaxf(fmaxf(c0, c1), fmaxf(c2, c3)));
      if (l < 4) __hip_atomic_store((gu32*)(A.ws + OFF_FL) + FL_G + (l * 4 + w - 5) * 32 + hi, __float_as_uint(m) | 1u, RLX_AGENT);
    }
    __builtin_amdgcn_s_setprio(0);
  } else {
    const int jm = tid / 96, um = tid % 96;
    const int r = um % 12, ng = um / 12;
    const float* prm = reinterpret_cast<float*>(smem) + jm * 10 * NST;
    float* part = reinterpret_cast<float*>(smem) + 40 * NST + jm * 8 * 400;
    const float* pw = prm + (4 + 2 * (r >> 2)) * NST;
    const float p0 = pos_of_index(256 + 32 * r);
    f32x2_t acc[33];
#pragma unroll
    for (int i = 0; i < 33; ++i) acc[i] = f32x2_t{0.f, 0.f};
#pragma unroll 1
    for (int bt = 0; bt < 4; ++bt) {
      const int n = 16 * ng + 4 * bt;
      const f32x4 ar = *reinterpret_cast<const f32x4*>(prm + n), ai = *reinterpret_cast<const f32x4*>(prm + NST + n);
      const f32x4 cr = *reinterpret_cast<const f32x4*>(prm + 2 * NST + n), ci = *reinterpret_cast<const f32x4*>(prm + 3 * NST + n);
      const f32x4 wr = *reinterpret_cast<const f32x4*>(pw + n), wi = *reinterpret_cast<const f32x4*>(pw + NST + n);
      float yr0[4], yi0[4];
#pragma unroll
      for (int k = 0; k < 4; ++k) {
        const float e = __expf(ar[k] * p0);
        float sn, cs;
        __sincosf(ai[k] * p0, &sn, &cs);
        const float zr = e * cs, zi = e * sn;
        yr0[k] = cr[k] * zr - ci[k] * zi;
        yi0[k] = cr[k] * zi + ci[k] * zr;
      }
      RUN_START
#pragma unroll
      for (int i = 0; i < 33; ++i) {
        acc[i] += ya + yb;
        RUN_STEP
      }
    }
#pragma unroll
    for (int i = 0; i < 33; ++i) part[ng * 400 + 33 * r + i] = acc[i].x + acc[i].y;
  }
  __syncthreads();
  SST(2);
  for (int i = u; i < 385; i += 128) {
    const int o = i < 384 ? 33 * (i >> 5) + (i & 31) : 33 * 11 + 32;
    float v = 0.f;
#pragma unroll
    for (int g = 0; g < 8; ++g) v += part[g * 400 + o];
    v *= 2.0f;
    kd[i] = pk2(v, v - lo16(pk2(v, v)));
  }
  if (u == 1) kd[385] = 0u;
  __syncthreads();
  if (aw >= 0) {
    wo_issue(A.Wo, aw, 0, lds_base(smem));
    wo_issue(A.Wo, aw, 1, lds_base(smem));
  }
  const __amdgpu_buffer_rsrc_t rs = MKRS(A.ws + OFF_KF, DM * 2 * CKN * 2);
  for (int un = u; un < 229; un += 128) {
    v4u oh, ol;
#pragma unroll
    for (int e = 0; e < 4; ++e) {
      const int lag = 2080 - (8 * un + 2 * e);
      const unsigned pb = pk2((float)lag, (float)(lag - 1));
      const unsigned k0 = kd[(unsigned)(lag - 256) < L_SEQ - 256u ? (pb & 0xffffu) - 0x4380u : 385u];
      const unsigned k1 = kd[(unsigned)(lag - 257) < L_SEQ - 256u ? (pb >> 16) - 0x4380u : 385u];
      oh[e] = __builtin_amdgcn_perm(k1, k0, 0x05040100u);
      ol[e] = __builtin_amdgcn_perm(k1, k0, 0x07060302u);
    }
    ST128(oh, rs, (d * 2 * CKN + 8 * un) * 2, 0);
    ST128(ol, rs, (d * 2 * CKN + CKN + 8 * un) * 2, 0);
  }
  DRAIN();
  __syncthreads();
  if (tid == 0) set_flag((unsigned*)(A.ws + OFF_FL) + FL_KG + (hi >> 1) * 32 + (hi & 1));
  SST(3);
}

constexpr int C_SM = 131072, C_SO = C_SM + 8192, C_KT = C_SO + 2048, C_X = C_KT + 16384, C_SQ = C_X + 1024;
constexpr int C_SK = C_SQ + 2432;
static_assert(C_SK + 8 * QS * 2 <= 163808, "lds");
#define CHAIN_ASM_ATT \
  "s_mov_b32 s66, %[d0]\n" \
  "s_mov_b32 s94, 576\n" \
  "s_mov_b32 s95, 288\n" \
  "s_mov_b32 s84, 0x43400000\n" \
  "s_mov_b32 s85, 0x43400000\n" \
  "s_mov_b32 s92, 0x88000000\n" \
  "s_mov_b32 s93, 0x87fffff7\n" \
  "s_mov_b32 s78, 0x3e0293ee\n" \
  "s_mov_b32 s76, 0x33333333\n" \
  "s_mov_b32 s77, 0x33333333\n" \
  "s_add_u32 s58, s56, s60\n" \
  "s_addc_u32 s59, s57, 0\n" \
  "s_xor_b32 s38, s35, 0\n" \
  "s_add_u32 s26, s58, s38\n" \
  "s_addc_u32 s27, s59, 0\n" \
  "s_xor_b32 s38, s35, 1024\n" \
  "s_add_u32 s28, s58, s38\n" \
  "s_addc_u32 s29, s59, 0\n" \
  "s_xor_b32 s38, s35, 2048\n" \
  "s_add_u32 s30, s58, s38\n" \
  "s_addc_u32 s31, s59, 0\n" \
  "s_xor_b32 s38, s35, 3072\n" \
  "s_add_u32 s32, s58, s38\n" \
  "s_addc_u32 s33, s59, 0\n" \
  "s_lshr_b32 s98, s60, 2\n" \
  "s_add_u32 s98, s98, s74\n" \
  "s_add_u32 s98, s98, 131072\n" \
  "s_lshl_b32 s97, s73, 5\n" \
  "s_add_u32 s97, s97, s74\n" \
  "s_add_u32 s97, s97, 161152\n" \
  "s_lshl_b32 s99, s73, 10\n" \
  "s_mov_b32 s65, 9\n" \
  "v_mbcnt_lo_u32_b32 v48, -1, 0\n" \
  "v_mbcnt_hi_u32_b32 v48, -1, v48\n" \
  "v_and_b32 v49, 15, v48\n" \
  "v_lshl_add_u32 v9, v49, 1, s97\n" \
  "v_lshrrev_b32 v49, 4, v48\n" \
  "v_mad_u32_u24 v9, v49, s94, v9\n" \
  "v_add_u32 v7, s99, v1\n" \
  "v_mov_b64 v[12:13], 0\n" \
  "v_mov_b64 v[14:15], 0\n" \
  "v_mov_b64 v[16:17], 0\n" \
  "v_mov_b64 v[18:19], 0\n" \
  "v_mov_b64 v[20:21], 0\n" \
  "v_mov_b64 v[22:23], 0\n" \
  "v_mov_b32 v24, 0x44870000\n" \
  "v_mul_f32 v24, s72, v24\n" \
  "s_nop 0\n" \
  "v_readfirstlane_b32 s86, v24\n" \
  "s_lshr_b32 s86, s86, 23\n" \
  "s_addk_i32 s86, 0xff83\n" \
  "s_ashr_i32 s86, s86, 1\n" \
  "s_max_i32 s86, s86, -20\n" \
  "s_min_i32 s86, s86, 100\n" \
  "s_mov_b32 s82, s86\n" \
  "s_lshr_b32 s83, s72, 23\n" \
  "s_sub_i32 s83, s83, 242\n" \
  "s_lshl_b32 s88, s73, 10\n" \
  "v_add_u32 v24, s88, v1\n" \
  "ds_read_b128 v[32:35], v24 offset:0\n" \
  "s_waitcnt lgkmcnt(0)\n" \
  "v_mov_b32 v45, 0\n" \
  "v_dot2c_f32_bf16 v45, v32, v32\n" \
  "v_dot2c_f32_bf16 v45, v33, v33\n" \
  "v_dot2c_f32_bf16 v45, v34, v34\n" \
  "v_dot2c_f32_bf16 v45, v35, v35\n" \
  "s_add_u32 s88, s74, 157760\n" \
  "v_mov_b32 v44, s88\n" \
  "s_nop 0\n" \
  "v_cmpx_lt_f32 vcc, 2.0, v45\n" \
  "ds_max_u32 v44, v45\n" \
  "s_mov_b64 exec, -1\n" \
  ".p2align 6\n" \
  ".LCA_AL_%=:\n" \
  ".LCA_TOP_%=:\n" \
  "s_waitcnt lgkmcnt(0)\n" \
  "s_barrier\n" \
  "ds_read_b32 v8, v1 offset:26688\n" \
  "s_cmp_lg_u32 s73, s65\n" \
  "s_cbranch_scc1 .LCA_NOTK_%=\n" \
  "s_memrealtime s[38:39]\n" \
  "v_mov_b32 v26, 1\n" \
  "s_mul_i32 s23, s71, %[pp]\n" \
  "s_lshr_b32 s62, s66, 4\n" \
  "s_mov_b64 exec, 1\n" \
  "s_waitcnt lgkmcnt(0)\n" \
  "s_mov_b32 s67, s38\n" \
  "s_add_u32 s38, s38, s62\n" \
  "v_mov_b32 v25, s38\n" \
  "buffer_atomic_umax v25, off, s[16:19], s68\n" \
  "buffer_atomic_add v26, off, s[16:19], s68 offset:64\n" \
  "v_add_u32 v27, s23, v25\n" \
  "buffer_store_dword v27, off, s[16:19], s69 sc1\n" \
  "s_mov_b64 exec, -1\n" \
  "s_branch .LCA_SIDE_%=\n" \
  ".LCA_NOTK_%=:\n" \
  "s_cmp_lg_u32 s73, 0\n" \
  "s_cbranch_scc1 .LCA_NOZ_%=\n" \
  "v_mov_b64 v[26:27], s[92:93]\n" \
  "s_xor_b32 s88, s60, 0x8000\n" \
  "s_add_u32 s88, s88, s61\n" \
  "buffer_store_dwordx2 v[26:27], v5, s[40:43], s88 offen sc1\n" \
  ".LCA_NOZ_%=:\n" \
  "s_nop 7\n" \
  ".LCA_SIDE_%=:\n" \
  "s_waitcnt lgkmcnt(0)\n" \
  "v_readfirstlane_b32 s86, v8\n" \
  "s_mov_b64 vcc, s[76:77]\n" \
  "s_lshr_b32 s86, s86, 23\n" \
  "s_add_i32 s86, s86, s83\n" \
  "s_ashr_i32 s86, s86, 1\n" \
  "s_max_i32 s86, s86, s82\n" \
  "s_min_i32 s86, s86, 100\n" \
  "s_lshl_b32 s86, s86, 23\n" \
  "s_add_i32 s75, s86, 0x3c800000\n" \
  "s_sub_u32 s38, s71, 1\n" \
  "s_cmp_ge_u32 s38, 62\n" \
  "ds_read_b128 v[32:35], v1 offset:0\n" \
  "ds_read_b128 v[36:39], v1 offset:1024\n" \
  "ds_read_b128 v[48:51], v1 offset:2048\n" \
  "ds_read_b128 v[52:55], v1 offset:3072\n" \
  "ds_read_b128 v[40:43], v1 offset:4096\n" \
  "ds_read_b128 v[56:59], v1 offset:5120\n" \
  "ds_read_b128 v[60:63], v1 offset:6144\n" \
  "ds_read_b128 v[44:47], v1 offset:7168\n" \
  "s_setprio 3\n" \
  "s_waitcnt lgkmcnt(7)\n" \
  "v_smfmac_f32_16x16x64_bf16 v[12:15], v[32:35], v[64:71], v0\n" \
  "s_waitcnt lgkmcnt(6)\n" \
  "v_smfmac_f32_16x16x64_bf16 v[12:15], v[36:39], v[72:79], v0\n" \
  "s_waitcnt lgkmcnt(5)\n" \
  "v_smfmac_f32_16x16x64_bf16 v[12:15], v[48:51], v[80:87], v0\n" \
  "s_waitcnt lgkmcnt(4)\n" \
  "v_smfmac_f32_16x16x64_bf16 v[12:15], v[52:55], v[88:95], v0\n" \
  "s_waitcnt lgkmcnt(3)\n" \
  "v_smfmac_f32_16x16x64_bf16 v[12:15], v[40:43], v[96:103], v0\n" \
  "s_waitcnt lgkmcnt(2)\n" \
  "v_smfmac_f32_16x16x64_bf16 v[12:15], v[56:59], v[104:111], v0\n" \
  "s_waitcnt lgkmcnt(1)\n" \
  "v_smfmac_f32_16x16x64_bf16 v[12:15], v[60:63], v[112:119], v0\n" \
  "s_waitcnt lgkmcnt(0)\n" \
  "v_smfmac_f32_16x16x64_bf16 v[12:15], v[44:47], v[120:127], v0\n" \
  "ds_read_b128 v[24:27], v1 offset:10240\n" \
  "ds_read_b128 v[28:31], v1 offset:14336\n" \
  "s_nop 5\n" \
  "v_add_f32 v12, v12, v14\n" \
  "v_add_f32 v13, v13, v15\n" \
  "v_cvt_pk_bf16_f32 v12, v12, v13\n" \
  "ds_write_b16 v9, v12 offset:0\n" \
  "ds_write_b16_d16_hi v9, v12 offset:288\n" \
  "s_setprio 0\n" \
  "s_barrier\n" \
  "v_smfmac_f32_16x16x64_bf16 v[16:19], v[32:35], v[128:135], v0\n" \
  "v_smfmac_f32_16x16x64_bf16 v[20:23], v[32:35], v[192:199], v0\n" \
  "ds_read_b128 v[32:35], v1 offset:11264\n" \
  "v_smfmac_f32_16x16x64_bf16 v[16:19], v[36:39], v[136:143], v0\n" \
  "v_smfmac_f32_16x16x64_bf16 v[20:23], v[36:39], v[200:207], v0\n" \
  "ds_read_b128 v[36:39], v1 offset:15360\n" \
  "v_smfmac_f32_16x16x64_bf16 v[16:19], v[48:51], v[144:151], v0\n" \
  "v_smfmac_f32_16x16x64_bf16 v[20:23], v[48:51], v[208:215], v0\n" \
  "v_smfmac_f32_16x16x64_bf16 v[16:19], v[52:55], v[152:159], v0\n" \
  "v_smfmac_f32_16x16x64_bf16 v[20:23], v[52:55], v[216:223], v0\n" \
  "v_smfmac_f32_16x16x64_bf16 v[16:19], v[40:43], v[160:167], v0\n" \
  "v_smfmac_f32_16x16x64_bf16 v[20:23], v[40:43], v[224:231], v0\n" \
  ".fill ((16 - (. - .LCA_AL_%=)) & 31) >> 2, 4, 0xbf800000\n" \
  "s_waitcnt lgkmcnt(2)\n" \
  "s_barrier\n" \
  "s_setprio 3\n" \
  "ds_read_b128 v[48:51], v2 offset:0\n" \
  "ds_read_b128 v[52:55], v2 offset:64\n" \
  "v_smfmac_f32_16x16x64_bf16 v[16:19], v[56:59], v[168:175], v0\n" \
  "v_smfmac_f32_16x16x64_bf16 v[20:23], v[56:59], v[232:239], v0\n" \
  "ds_read_b128 v[56:59], v2 offset:128\n" \
  "s_cbranch_scc1 .LCA_LAST_%=\n" \
  ".LCA_BACK_%=:\n" \
  "s_waitcnt lgkmcnt(2)\n" \
  "v_mfma_f32_16x16x32_bf16 v[12:15], v[24:27], v[48:51], 0\n" \
  "ds_read_b128 v[24:27], v1 offset:12288\n" \
  "v_mfma_f32_16x16x32_bf16 v[8:11], v[28:31], v[48:51], 0\n" \
  "ds_read_b128 v[28:31], v1 offset:16384\n" \
  "v_mfma_f32_16x16x32_bf16 v[40:43], v[48:51], v[48:51], 0\n" \
  "ds_read_b128 v[48:51], v7 offset:18432\n" \
  "v_smfmac_f32_16x16x64_bf16 v[16:19], v[60:63], v[176:183], v0\n" \
  "v_smfmac_f32_16x16x64_bf16 v[20:23], v[60:63], v[240:247], v0\n" \
  "ds_read_b128 v[60:63], v2 offset:192\n" \
  "s_waitcnt lgkmcnt(5)\n" \
  "v_mfma_f32_16x16x32_bf16 v[12:15], v[32:35], v[52:55], v[12:15]\n" \
  "ds_read_b128 v[32:35], v1 offset:13312\n" \
  "v_mfma_f32_16x16x32_bf16 v[8:11], v[36:39], v[52:55], v[8:11]\n" \
  "ds_read_b128 v[36:39], v1 offset:17408\n" \
  "v_mfma_f32_16x16x32_bf16 v[40:43], v[52:55], v[52:55], v[40:43]\n" \
  "ds_read_b128 v[52:55], v7 offset:22528\n" \
  "s_waitcnt lgkmcnt(7)\n" \
  "v_mfma_f32_16x16x32_bf16 v[40:43], v[56:59], v[56:59], v[40:43]\n" \
  "s_waitcnt lgkmcnt(5)\n" \
  "v_mfma_f32_16x16x32_bf16 v[12:15], v[24:27], v[56:59], v[12:15]\n" \
  "ds_read_b128 v[24:27], v6 offset:2048\n" \
  "v_mfma_f32_16x16x32_bf16 v[8:11], v[28:31], v[56:59], v[8:11]\n" \
  "ds_read_b128 v[28:31], v6 offset:3072\n" \
  "s_waitcnt lgkmcnt(5)\n" \
  "v_mfma_f32_16x16x32_bf16 v[40:43], v[60:63], v[60:63], v[40:43]\n" \
  "ds_read_b128 v[56:59], v6 offset:0\n" \
  "s_waitcnt lgkmcnt(4)\n" \
  "v_mfma_f32_16x16x32_bf16 v[12:15], v[32:35], v[60:63], v[12:15]\n" \
  "ds_read_b128 v[32:35], v6 offset:8192\n" \
  "v_mfma_f32_16x16x32_bf16 v[8:11], v[36:39], v[60:63], v[8:11]\n" \
  "ds_read_b128 v[36:39], v6 offset:9216\n" \
  "v_smfmac_f32_16x16x64_bf16 v[16:19], v[44:47], v[184:191], v0\n" \
  "ds_read_b128 v[60:63], v6 offset:1024\n" \
  "s_waitcnt lgkmcnt(6)\n" \
  "v_smfmac_f32_16x16x64_bf16 v[20:23], v[44:47], v[248:255], v0\n" \
  "v_cndmask_b32_dpp v43, v43, v42, vcc quad_perm:[0,1,0,1] row_mask:0xf bank_mask:0xf\n" \
  "v_mov_b32 v41, 0\n" \
  "v_mov_b32 v42, 0\n" \
  "v_cndmask_b32_dpp v12, v8, v12, vcc quad_perm:[0,1,0,1] row_mask:0xf bank_mask:0xf\n" \
  "v_cndmask_b32_dpp v13, v9, v13, vcc quad_perm:[0,1,0,1] row_mask:0xf bank_mask:0xf\n" \
  "v_cndmask_b32_dpp v14, v10, v14, vcc quad_perm:[0,1,0,1] row_mask:0xf bank_mask:0xf\n" \
  "v_cndmask_b32_dpp v15, v11, v15, vcc quad_perm:[0,1,0,1] row_mask:0xf bank_mask:0xf\n" \
  "v_maximum3_f32 v8, v43, v12, v13\n" \
  "v_maximum3_f32 v8, v8, v14, v15\n" \
  "v_add_f32 v16, v16, v18\n" \
  "v_add_f32 v17, v17, v19\n" \
  "v_max_f32_dpp v8, v8, v8 quad_perm:[2,3,0,1] row_mask:0xf bank_mask:0xf\n" \
  "v_mov_b32 v9, v8\n" \
  "v_add_f32 v20, v20, v22\n" \
  "v_add_f32 v21, v21, v23\n" \
  "v_permlane16_swap_b32 v8, v9\n" \
  "v_max_f32 v8, v8, v9\n" \
  "v_mov_b32 v9, v8\n" \
  "v_cvt_pk_bf16_f32 v16, v16, v17\n" \
  "v_cvt_pk_bf16_f32 v17, v20, v21\n" \
  "v_permlane32_swap_b32 v8, v9\n" \
  "v_max_f32 v8, v8, v9\n" \
  "s_barrier\n" \
  "buffer_load_dword v7, v5, s[16:19], s24 offen sc1\n" \
  "v_mul_f32 v8, s78, v8\n" \
  "v_pk_fma_f32 v[12:13], v[12:13], s[78:79], v[8:9] op_sel_hi:[1,0,0] neg_lo:[0,0,1] neg_hi:[0,0,1]\n" \
  "v_pk_fma_f32 v[14:15], v[14:15], s[78:79], v[8:9] op_sel_hi:[1,0,0] neg_lo:[0,0,1] neg_hi:[0,0,1]\n" \
  "v_fma_f32 v43, v43, s78, -v8\n" \
  "v_exp_f32 v12, v12\n" \
  "v_exp_f32 v13, v13\n" \
  "v_exp_f32 v14, v14\n" \
  "v_exp_f32 v15, v15\n" \
  "v_exp_f32 v43, v43\n" \
  "v_cvt_pk_bf16_f32 v44, v12, v13\n" \
  "v_cvt_pk_bf16_f32 v45, v14, v15\n" \
  "v_pk_add_f32 v[18:19], v[12:13], v[14:15]\n" \
  "v_mov_b32_dpp v46, v44 quad_perm:[2,3,0,1] row_mask:0xf bank_mask:0xf\n" \
  "v_mov_b32_dpp v9, v43 quad_perm:[2,3,0,1] row_mask:0xf bank_mask:0xf\n" \
  "v_mov_b32_dpp v47, v45 quad_perm:[2,3,0,1] row_mask:0xf bank_mask:0xf\n" \
  "v_add_f32 v10, v18, v43\n" \
  "v_cvt_pk_bf16_f32 v40, v43, v9\n" \
  "v_add_f32 v10, v10, v19\n" \
  "v_mfma_f32_16x16x32_bf16 v[12:15], v[48:51], v[44:47], 0\n" \
  "v_cvt_pk_bf16_f32 v43, v43, v9\n" \
  "v_add_f32_dpp v10, v10, v10 quad_perm:[2,3,0,1] row_mask:0xf bank_mask:0xf\n" \
  "v_mfma_f32_16x16x32_bf16 v[20:23], v[52:55], v[44:47], 0\n" \
  "v_mul_f32 v11, s75, v10\n" \
  "v_mul_f32 v10, s75, v10\n" \
  "v_mfma_f32_16x16x16_bf16 v[12:15], v[16:17], v[40:41], v[12:15]\n" \
  "ds_read_b128 v[48:51], v6 offset:10240\n" \
  "v_permlane16_swap_b32 v10, v11\n" \
  "v_add_f32 v10, v10, v11\n" \
  "v_mfma_f32_16x16x16_bf16 v[20:23], v[16:17], v[42:43], v[20:23]\n" \
  "v_mov_b32 v11, v10\n" \
  "ds_read_b128 v[52:55], v6 offset:11264\n" \
  "v_mov_b64 v[18:19], s[84:85]\n" \
  "v_permlane32_swap_b32 v10, v11\n" \
  "v_add_f32 v10, v10, v11\n" \
  "v_rcp_f32 v10, v10\n" \
  "s_nop 0\n" \
  "v_pk_mul_f32 v[12:13], v[12:13], v[10:11] op_sel_hi:[1,0]\n" \
  "v_pk_mul_f32 v[14:15], v[14:15], v[10:11] op_sel_hi:[1,0]\n" \
  "v_pk_mul_f32 v[20:21], v[20:21], v[10:11] op_sel_hi:[1,0]\n" \
  "v_pk_mul_f32 v[22:23], v[22:23], v[10:11] op_sel_hi:[1,0]\n" \
  "v_cvt_pk_bf16_f32 v40, v12, v13\n" \
  "v_cvt_pk_bf16_f32 v41, v14, v15\n" \
  "v_cvt_pk_bf16_f32 v42, v20, v21\n" \
  "ds_write_b64 v4, v[40:41]\n" \
  "v_cvt_pk_bf16_f32 v43, v22, v23\n" \
  "ds_write_b64 v4, v[42:43] offset:1024\n" \
  "v_max_u32 v16, 1, v16\n" \
  "v_max_u32 v17, 1, v17\n" \
  "buffer_store_dwordx2 v[16:17], v3, s[52:55], s70 offen sc1\n" \
  "v_mov_b64 v[12:13], s[84:85]\n" \
  "v_mov_b64 v[14:15], s[84:85]\n" \
  "v_mov_b64 v[16:17], s[84:85]\n" \
  "v_mov_b64 v[20:21], s[84:85]\n" \
  "v_mov_b64 v[22:23], s[84:85]\n" \
  "v_mov_b64 v[8:9], s[84:85]\n" \
  "v_mov_b64 v[10:11], s[84:85]\n" \
  "s_setprio 0\n" \
  "s_waitcnt lgkmcnt(0)\n" \
  "s_barrier\n" \
  "ds_read_b128 v[40:43], v1 offset:8192\n" \
  "ds_read_b128 v[44:47], v1 offset:9216\n" \
  "s_waitcnt lgkmcnt(1)\n" \
  "v_smfmac_f32_16x16x64_bf16 v[12:15], v[40:43], v[56:63], v0\n" \
  "s_waitcnt lgkmcnt(0)\n" \
  "v_smfmac_f32_16x16x64_bf16 v[12:15], v[44:47], v[32:39], v0\n" \
  "ds_read_b128 v[56:59], v6 offset:4096\n" \
  "ds_read_b128 v[60:63], v6 offset:5120\n" \
  "v_smfmac_f32_16x16x64_bf16 v[16:19], v[40:43], v[24:31], v0\n" \
  "ds_read_b128 v[32:35], v6 offset:12288\n" \
  "ds_read_b128 v[36:39], v6 offset:13312\n" \
  "v_smfmac_f32_16x16x64_bf16 v[16:19], v[44:47], v[48:55], v0\n" \
  "ds_read_b128 v[24:27], v6 offset:6144\n" \
  "ds_read_b128 v[28:31], v6 offset:7168\n" \
  "v_add_f32 v12, v12, v14\n" \
  "v_add_f32 v13, v13, v15\n" \
  "global_atomic_add_x2 v5, v[12:13], s[26:27]\n" \
  "ds_read_b128 v[48:51], v6 offset:14336\n" \
  "ds_read_b128 v[52:55], v6 offset:15360\n" \
  "s_waitcnt lgkmcnt(6)\n" \
  "v_add_f32 v16, v16, v18\n" \
  "v_add_f32 v17, v17, v19\n" \
  "global_atomic_add_x2 v5, v[16:17], s[28:29]\n" \
  "v_smfmac_f32_16x16x64_bf16 v[20:23], v[40:43], v[56:63], v0\n" \
  "s_waitcnt lgkmcnt(4)\n" \
  "v_smfmac_f32_16x16x64_bf16 v[20:23], v[44:47], v[32:39], v0\n" \
  "s_waitcnt lgkmcnt(2)\n" \
  "v_smfmac_f32_16x16x64_bf16 v[8:11], v[40:43], v[24:31], v0\n" \
  "s_waitcnt lgkmcnt(0)\n" \
  "v_smfmac_f32_16x16x64_bf16 v[8:11], v[44:47], v[48:55], v0\n" \
  "s_nop 4\n" \
  "v_add_f32 v20, v20, v22\n" \
  "v_add_f32 v21, v21, v23\n" \
  "global_atomic_add_x2 v5, v[20:21], s[30:31]\n" \
  "v_add_f32 v8, v8, v10\n" \
  "v_add_f32 v9, v9, v11\n" \
  "global_atomic_add_x2 v5, v[8:9], s[32:33]\n" \
  "s_memrealtime s[62:63]\n" \
  "v_mbcnt_lo_u32_b32 v48, -1, 0\n" \
  "v_mbcnt_hi_u32_b32 v48, -1, v48\n" \
  "v_lshrrev_b32 v49, 3, v48\n" \
  "v_and_b32 v50, 7, v48\n" \
  "v_lshlrev_b32 v50, 4, v50\n" \
  "v_lshl_or_b32 v44, v49, 8, v50\n" \
  "s_add_u32 s20, s20, 32\n" \
  "s_mov_b32 s21, 0x8000\n" \
  "v_mov_b32 v52, s22\n" \
  "v_mov_b32 v54, s20\n" \
  "s_mul_i32 s88, s71, %[pp]\n" \
  "s_lshl_b32 s89, s71, 10\n" \
  "s_sub_u32 s88, s88, s25\n" \
  "s_mul_i32 s38, s24, %[an]\n" \
  "s_cmp_gt_u32 s89, s38\n" \
  "s_cselect_b32 s89, 0x1000, 0\n" \
  "s_waitcnt vmcnt(5)\n" \
  "v_mov_b32 v46, v7\n" \
  "s_branch .LCA_TKE_%=\n" \
  ".LCA_TKR_%=:\n" \
  "s_sub_u32 s21, s21, 1\n" \
  "s_cbranch_scc1 .LCA_TKO_%=\n" \
  "s_and_b32 s38, s21, 7\n" \
  "s_cmp_lg_u32 s38, 7\n" \
  "s_cbranch_scc1 .LCA_TKS_%=\n" \
  "buffer_load_dword v46, v5, s[16:19], s24 offen sc1\n" \
  ".LCA_TKS_%=:\n" \
  "s_sleep 1\n" \
  ".LCA_TKE_%=:\n" \
  "v_readlane_b32 s38, v46, 8\n" \
  "v_readlane_b32 s39, v46, 24\n" \
  "v_readlane_b32 s23, v46, 40\n" \
  "v_readlane_b32 s34, v46, 56\n" \
  "s_add_u32 s38, s38, s39\n" \
  "s_add_u32 s23, s23, s34\n" \
  "s_add_u32 s38, s38, s23\n" \
  "s_cmp_lt_u32 s38, s20\n" \
  "s_cbranch_scc1 .LCA_TKR_%=\n" \
  ".LCA_TKO_%=:\n" \
  "v_readlane_b32 s34, v46, 0\n" \
  "v_readlane_b32 s38, v46, 16\n" \
  "v_readlane_b32 s39, v46, 32\n" \
  "v_readlane_b32 s23, v46, 48\n" \
  "s_max_u32 s34, s34, s38\n" \
  "s_max_u32 s39, s39, s23\n" \
  "s_add_u32 s34, s34, %[cd]\n" \
  "v_readlane_b32 s23, v46, 1\n" \
  "s_max_u32 s34, s34, s39\n" \
  "s_sub_u32 s23, s23, s34\n" \
  "s_sub_u32 s23, s23, s88\n" \
  "s_cmp_lt_u32 s23, s89\n" \
  "s_cselect_b32 s23, s23, 0\n" \
  "s_min_u32 s23, s23, %[ka]\n" \
  "s_add_u32 s34, s34, s23\n" \
  "s_add_u32 s34, s34, s64\n" \
  "v_mov_b32 v55, s34\n" \
  "ds_write_b64 v52, v[54:55]\n" \
  "s_branch .LCA_WAIU_%=\n" \
  ".LCA_WAIS_%=:\n" \
  "s_cmp_lt_i32 s39, -1000\n" \
  "s_cbranch_scc1 .LCA_WAIE_%=\n" \
  "s_sleep 1\n" \
  ".LCA_WAIU_%=:\n" \
  "s_memrealtime s[38:39]\n" \
  "s_waitcnt lgkmcnt(0)\n" \
  "s_sub_i32 s39, s38, s34\n" \
  "s_cmp_lt_i32 s39, -7\n" \
  "s_cbranch_scc1 .LCA_WAIS_%=\n" \
  "s_cmp_lt_i32 s39, 0\n" \
  "s_cbranch_scc1 .LCA_WAIU_%=\n" \
  ".LCA_WAIE_%=:\n" \
  "buffer_load_dwordx4 v[24:27], v44, s[40:43], s60 offen sc1\n" \
  "buffer_load_dwordx4 v[28:31], v44, s[40:43], s60 offen offset:2048 sc1\n" \
  "s_sub_u32 s62, s62, s67\n" \
  "s_lshl_b32 s62, s62, 4\n" \
  "s_sub_i32 s62, s62, s66\n" \
  "s_min_i32 s62, s62, 128\n" \
  "s_max_i32 s62, s62, -256\n" \
  "s_ashr_i32 s62, s62, 2\n" \
  "s_add_i32 s66, s66, s62\n" \
  "s_add_u32 s70, s70, 0x10000\n" \
  "v_mbcnt_lo_u32_b32 v48, -1, 0\n" \
  "v_mbcnt_hi_u32_b32 v48, -1, v48\n" \
  "v_and_b32 v49, 15, v48\n" \
  "v_lshl_add_u32 v9, v49, 1, s97\n" \
  "v_lshrrev_b32 v49, 4, v48\n" \
  "v_mad_u32_u24 v9, v49, s94, v9\n" \
  "v_add_u32 v7, s99, v1\n" \
  "v_mov_b64 v[12:13], 0\n" \
  "v_mov_b64 v[14:15], 0\n" \
  "v_mov_b64 v[16:17], 0\n" \
  "v_mov_b64 v[18:19], 0\n" \
  "v_mov_b64 v[20:21], 0\n" \
  "v_mov_b64 v[22:23], 0\n" \
  "v_and_b32 v49, 3, v48\n" \
  "v_lshl_add_u32 v40, v49, 2, s98\n" \
  "v_lshrrev_b32 v49, 2, v48\n" \
  "v_bfe_u32 v48, v48, 4, 1\n" \
  "v_xor_b32 v49, v49, v48\n" \
  "v_lshl_add_u32 v40, v49, 5, v40\n" \
  "s_add_i32 s80, s86, 0x35000000\n" \
  "s_sub_u32 s91, 0x4a350000, s86\n" \
  "s_min_u32 s91, s91, 0x4ca00000\n" \
  "s_mov_b32 s89, s60\n" \
  "s_xor_b32 s60, s60, 0x80\n" \
  "s_bitcmp0_b32 s60, 7\n" \
  "s_cselect_b32 s88, 0x8000, 0\n" \
  "s_add_u32 s60, s60, s88\n" \
  "s_and_b32 s60, s60, 0xffff\n" \
  "s_add_u32 s58, s56, s60\n" \
  "s_addc_u32 s59, s57, 0\n" \
  "s_xor_b32 s38, s35, 0\n" \
  "s_add_u32 s26, s58, s38\n" \
  "s_addc_u32 s27, s59, 0\n" \
  "s_xor_b32 s38, s35, 1024\n" \
  "s_add_u32 s28, s58, s38\n" \
  "s_addc_u32 s29, s59, 0\n" \
  "s_xor_b32 s38, s35, 2048\n" \
  "s_add_u32 s30, s58, s38\n" \
  "s_addc_u32 s31, s59, 0\n" \
  "s_xor_b32 s38, s35, 3072\n" \
  "s_add_u32 s32, s58, s38\n" \
  "s_addc_u32 s33, s59, 0\n" \
  "s_sub_u32 s71, s71, 1\n" \
  "s_mov_b32 s87, 0x40000\n" \
  "s_branch .LCA_CHK_%=\n" \
  ".LCA_POLL_%=:\n" \
  "buffer_load_dwordx4 v[24:27], v44, s[40:43], s89 offen sc1\n" \
  "buffer_load_dwordx4 v[28:31], v44, s[40:43], s89 offen offset:2048 sc1\n" \
  ".LCA_CHK_%=:\n" \
  "s_sub_u32 s87, s87, 1\n" \
  "s_cbranch_scc1 .LCA_END_%=\n" \
  "s_waitcnt vmcnt(1)\n" \
  "v_cvt_f32_i32 v32, v24\n" \
  "v_cvt_f32_i32 v33, v25\n" \
  "v_cvt_f32_i32 v34, v26\n" \
  "v_cvt_f32_i32 v35, v27\n" \
  "v_pk_mul_f32 v[24:25], v[32:33], s[80:81] op_sel_hi:[1,0]\n" \
  "v_pk_mul_f32 v[26:27], v[34:35], s[80:81] op_sel_hi:[1,0]\n" \
  "v_max3_f32 v45, |v32|, |v33|, |v34|\n" \
  "v_cvt_pk_bf16_f32 v24, v24, v26\n" \
  "v_cvt_pk_bf16_f32 v25, v25, v27\n" \
  "ds_write2_b32 v40, v24, v25 offset0:0 offset1:4\n" \
  "v_max3_f32 v45, |v45|, |v35|, |v35|\n" \
  "s_waitcnt vmcnt(0)\n" \
  "v_cvt_f32_i32 v32, v28\n" \
  "v_cvt_f32_i32 v33, v29\n" \
  "v_cvt_f32_i32 v34, v30\n" \
  "v_cvt_f32_i32 v35, v31\n" \
  "v_pk_mul_f32 v[28:29], v[32:33], s[80:81] op_sel_hi:[1,0]\n" \
  "v_pk_mul_f32 v[30:31], v[34:35], s[80:81] op_sel_hi:[1,0]\n" \
  "v_max3_f32 v45, |v45|, |v32|, |v33|\n" \
  "v_cvt_pk_bf16_f32 v28, v28, v30\n" \
  "v_cvt_pk_bf16_f32 v29, v29, v31\n" \
  "ds_write2_b32 v40, v28, v29 offset0:128 offset1:132\n" \
  "v_max3_f32 v45, |v45|, |v34|, |v35|\n" \
  "v_cmp_lt_f32 vcc, s91, v45\n" \
  "s_cbranch_vccnz .LCA_BIG_%=\n" \
  ".LCA_SML_%=:\n" \
  "s_branch .LCA_TOP_%=\n" \
  ".LCA_BIG_%=:\n" \
  "v_cmp_lt_f32 vcc, 0x4ca00000, v45\n" \
  "s_cbranch_vccnz .LCA_POLL_%=\n" \
  "v_mul_f32 v45, s80, v45\n" \
  "v_mul_f32 v45, v45, v45\n" \
  "s_add_u32 s88, s74, 157760\n" \
  "v_mov_b32 v44, s88\n" \
  "s_nop 0\n" \
  "v_cmpx_lt_f32 vcc, 2.0, v45\n" \
  "ds_max_u32 v44, v45\n" \
  "s_mov_b64 exec, -1\n" \
  "s_branch .LCA_SML_%=\n" \
  ".LCA_FIRST_%=:\n" \
  "s_mov_b32 s65, 3\n" \
  "s_cmp_lg_u32 s73, s65\n" \
  "s_cbranch_scc1 .LCA_NOTF_%=\n" \
  "s_memrealtime s[38:39]\n" \
  "v_mov_b32 v10, 1\n" \
  "s_mul_i32 s23, s71, %[pp]\n" \
  "s_mov_b32 s62, %[d1]\n" \
  "s_mov_b64 exec, 1\n" \
  "s_waitcnt lgkmcnt(0)\n" \
  "s_mov_b32 s67, s38\n" \
  "s_add_u32 s38, s38, s62\n" \
  "v_mov_b32 v9, s38\n" \
  "buffer_atomic_umax v9, off, s[16:19], s68\n" \
  "buffer_atomic_add v10, off, s[16:19], s68 offset:64\n" \
  "v_add_u32 v11, s23, v9\n" \
  "buffer_store_dword v11, off, s[16:19], s69 sc1\n" \
  "s_mov_b64 exec, -1\n" \
  "s_branch .LCA_NOTF_%=\n" \
  ".LCA_NOTF_%=:\n" \
  "s_sub_u32 s67, s67, 40\n" \
  "ds_read_b128 v[24:27], v1 offset:10240\n" \
  "ds_read_b128 v[28:31], v1 offset:14336\n" \
  "ds_read_b128 v[32:35], v1 offset:11264\n" \
  "ds_read_b128 v[36:39], v1 offset:15360\n" \
  "s_waitcnt lgkmcnt(0)\n" \
  "s_branch .LCA_BACK_%=\n" \
  ".LCA_LAST_%=:\n" \
  "s_cmp_lg_u32 s71, 0\n" \
  "s_cbranch_scc1 .LCA_FIRST_%=\n" \
  "v_smfmac_f32_16x16x64_bf16 v[16:19], v[60:63], v[176:183], v0\n" \
  "v_smfmac_f32_16x16x64_bf16 v[20:23], v[60:63], v[240:247], v0\n" \
  "v_smfmac_f32_16x16x64_bf16 v[16:19], v[44:47], v[184:191], v0\n" \
  "v_smfmac_f32_16x16x64_bf16 v[20:23], v[44:47], v[248:255], v0\n" \
  "s_nop 7\n" \
  "v_add_f32 v16, v16, v18\n" \
  "v_add_f32 v17, v17, v19\n" \
  "v_add_f32 v20, v20, v22\n" \
  "v_add_f32 v21, v21, v23\n" \
  "v_cvt_pk_bf16_f32 v16, v16, v17\n" \
  "v_cvt_pk_bf16_f32 v17, v20, v21\n" \
  "s_nop 1\n" \
  "s_nop 0\n" \
  "v_max_u32 v16, 1, v16\n" \
  "v_max_u32 v17, 1, v17\n" \
  "buffer_store_dwordx2 v[16:17], v3, s[52:55], s70 offen sc1\n" \
  "s_waitcnt vmcnt(0)\n" \
  ".LCA_END_%=:\n" \
  "s_endpgm\n"
#define CHAIN_ASM_VO \
  "s_mov_b32 s66, %[d0]\n" \
  "s_mov_b32 s94, 576\n" \
  "s_mov_b32 s95, 288\n" \
  "s_mov_b32 s84, 0x43400000\n" \
  "s_mov_b32 s85, 0x43400000\n" \
  "s_mov_b32 s92, 0x88000000\n" \
  "s_mov_b32 s93, 0x87fffff7\n" \
  "s_mov_b32 s78, 0x3e0293ee\n" \
  "s_mov_b32 s76, 0x33333333\n" \
  "s_mov_b32 s77, 0x33333333\n" \
  "s_add_u32 s58, s56, s60\n" \
  "s_addc_u32 s59, s57, 0\n" \
  "s_xor_b32 s38, s35, 0\n" \
  "s_add_u32 s26, s58, s38\n" \
  "s_addc_u32 s27, s59, 0\n" \
  "s_xor_b32 s38, s35, 1024\n" \
  "s_add_u32 s28, s58, s38\n" \
  "s_addc_u32 s29, s59, 0\n" \
  "s_xor_b32 s38, s35, 2048\n" \
  "s_add_u32 s30, s58, s38\n" \
  "s_addc_u32 s31, s59, 0\n" \
  "s_xor_b32 s38, s35, 3072\n" \
  "s_add_u32 s32, s58, s38\n" \
  "s_addc_u32 s33, s59, 0\n" \
  "s_lshr_b32 s98, s60, 2\n" \
  "s_add_u32 s98, s98, s74\n" \
  "s_add_u32 s98, s98, 131072\n" \
  "s_lshl_b32 s97, s73, 5\n" \
  "s_add_u32 s97, s97, s74\n" \
  "s_add_u32 s97, s97, 158592\n" \
  "s_lshl_b32 s99, s73, 11\n" \
  "s_add_u32 s99, s99, s74\n" \
  "s_add_u32 s99, s99, 133120\n" \
  "v_mbcnt_lo_u32_b32 v48, -1, 0\n" \
  "v_mbcnt_hi_u32_b32 v48, -1, v48\n" \
  "s_sub_u32 s88, s73, 4\n" \
  "v_and_b32 v51, 15, v48\n" \
  "v_lshl_add_u32 v4, v51, 1, s97\n" \
  "v_lshrrev_b32 v49, 4, v48\n" \
  "v_mad_u32_u24 v4, v49, s94, v4\n" \
  "v_lshrrev_b32 v49, 2, v51\n" \
  "v_and_b32 v50, 5, v51\n" \
  "v_and_or_b32 v49, v49, 2, v50\n" \
  "s_lshl_b32 s89, s88, 6\n" \
  "s_add_u32 s89, s89, s74\n" \
  "s_add_u32 s89, s89, 161152\n" \
  "v_and_b32 v50, 48, v48\n" \
  "v_add_u32 v6, s89, v50\n" \
  "v_mad_u32_u24 v6, v49, s95, v6\n" \
  "v_lshlrev_b32 v7, 3, v50\n" \
  "v_lshl_add_u32 v7, v49, 4, v7\n" \
  "v_and_b32 v49, 2, v51\n" \
  "v_lshl_add_u32 v7, v49, 29, v7\n" \
  "v_lshlrev_b32 v10, 4, v48\n" \
  "v_mov_b64 v[12:13], 0\n" \
  "v_mov_b64 v[14:15], 0\n" \
  "v_mov_b64 v[16:17], 0\n" \
  "v_mov_b64 v[18:19], 0\n" \
  "v_mov_b64 v[20:21], 0\n" \
  "v_mov_b64 v[22:23], 0\n" \
  "v_mov_b32 v24, 0x44870000\n" \
  "v_mul_f32 v24, s72, v24\n" \
  "s_nop 0\n" \
  "v_readfirstlane_b32 s86, v24\n" \
  "s_lshr_b32 s86, s86, 23\n" \
  "s_addk_i32 s86, 0xff83\n" \
  "s_ashr_i32 s86, s86, 1\n" \
  "s_max_i32 s86, s86, -20\n" \
  "s_min_i32 s86, s86, 100\n" \
  "s_mov_b32 s82, s86\n" \
  "s_lshr_b32 s83, s72, 23\n" \
  "s_sub_i32 s83, s83, 242\n" \
  "s_lshl_b32 s88, s73, 10\n" \
  "v_add_u32 v24, s88, v1\n" \
  "ds_read_b128 v[32:35], v24 offset:0\n" \
  "s_waitcnt lgkmcnt(0)\n" \
  "v_mov_b32 v45, 0\n" \
  "v_dot2c_f32_bf16 v45, v32, v32\n" \
  "v_dot2c_f32_bf16 v45, v33, v33\n" \
  "v_dot2c_f32_bf16 v45, v34, v34\n" \
  "v_dot2c_f32_bf16 v45, v35, v35\n" \
  "s_add_u32 s88, s74, 157760\n" \
  "v_mov_b32 v44, s88\n" \
  "s_nop 0\n" \
  "v_cmpx_lt_f32 vcc, 2.0, v45\n" \
  "ds_max_u32 v44, v45\n" \
  "s_mov_b64 exec, -1\n" \
  ".p2align 6\n" \
  ".LCV_AL_%=:\n" \
  ".LCV_TOP_%=:\n" \
  "s_waitcnt lgkmcnt(0)\n" \
  "s_barrier\n" \
  "s_setprio 2\n" \
  "ds_read_b128 v[24:27], v1 offset:0\n" \
  "ds_read_b128 v[28:31], v1 offset:1024\n" \
  "ds_read_b128 v[32:35], v1 offset:2048\n" \
  "ds_read_b128 v[36:39], v1 offset:3072\n" \
  "buffer_load_dword v5, off, s[44:47], s68 sc1\n" \
  "s_cmp_eq_u32 s71, 63\n" \
  "s_waitcnt lgkmcnt(3)\n" \
  "v_smfmac_f32_16x16x64_bf16 v[20:23], v[24:27], v[192:199], v0\n" \
  "ds_read_b128 v[40:43], v1 offset:4096\n" \
  "s_waitcnt lgkmcnt(3)\n" \
  "v_smfmac_f32_16x16x64_bf16 v[20:23], v[28:31], v[200:207], v0\n" \
  "ds_read_b128 v[44:47], v1 offset:5120\n" \
  "s_waitcnt lgkmcnt(3)\n" \
  "v_smfmac_f32_16x16x64_bf16 v[20:23], v[32:35], v[208:215], v0\n" \
  "ds_read_b128 v[48:51], v1 offset:6144\n" \
  "s_waitcnt lgkmcnt(3)\n" \
  "v_smfmac_f32_16x16x64_bf16 v[20:23], v[36:39], v[216:223], v0\n" \
  "ds_read_b128 v[52:55], v1 offset:7168\n" \
  "s_waitcnt lgkmcnt(3)\n" \
  "v_smfmac_f32_16x16x64_bf16 v[20:23], v[40:43], v[224:231], v0\n" \
  "s_waitcnt lgkmcnt(2)\n" \
  "v_smfmac_f32_16x16x64_bf16 v[20:23], v[44:47], v[232:239], v0\n" \
  "s_waitcnt lgkmcnt(1)\n" \
  "v_smfmac_f32_16x16x64_bf16 v[20:23], v[48:51], v[240:247], v0\n" \
  "s_waitcnt lgkmcnt(0)\n" \
  "v_smfmac_f32_16x16x64_bf16 v[20:23], v[52:55], v[248:255], v0\n" \
  "s_nop 0\n" \
  "v_smfmac_f32_16x16x64_bf16 v[12:15], v[24:27], v[64:71], v0\n" \
  "s_nop 0\n" \
  "v_smfmac_f32_16x16x64_bf16 v[12:15], v[28:31], v[72:79], v0\n" \
  "s_nop 0\n" \
  "v_smfmac_f32_16x16x64_bf16 v[12:15], v[32:35], v[80:87], v0\n" \
  "ds_read_b32 v11, v1 offset:26688\n" \
  "v_smfmac_f32_16x16x64_bf16 v[12:15], v[36:39], v[88:95], v0\n" \
  "v_add_f32 v20, v20, v22\n" \
  "v_smfmac_f32_16x16x64_bf16 v[12:15], v[40:43], v[96:103], v0\n" \
  "v_add_f32 v21, v21, v23\n" \
  "v_smfmac_f32_16x16x64_bf16 v[12:15], v[44:47], v[104:111], v0\n" \
  "v_cvt_pk_bf16_f32 v20, v20, v21\n" \
  "v_smfmac_f32_16x16x64_bf16 v[12:15], v[48:51], v[112:119], v0\n" \
  "ds_write_b16 v4, v20 offset:0\n" \
  "v_smfmac_f32_16x16x64_bf16 v[12:15], v[52:55], v[120:127], v0\n" \
  "ds_write_b16_d16_hi v4, v20 offset:288\n" \
  "v_smfmac_f32_16x16x64_bf16 v[16:19], v[24:27], v[128:135], v0\n" \
  "s_nop 1\n" \
  "v_smfmac_f32_16x16x64_bf16 v[16:19], v[28:31], v[136:143], v0\n" \
  "s_nop 1\n" \
  "v_smfmac_f32_16x16x64_bf16 v[16:19], v[32:35], v[144:151], v0\n" \
  "v_add_f32 v12, v12, v14\n" \
  "v_smfmac_f32_16x16x64_bf16 v[16:19], v[36:39], v[152:159], v0\n" \
  "v_add_f32 v13, v13, v15\n" \
  "v_smfmac_f32_16x16x64_bf16 v[16:19], v[40:43], v[160:167], v0\n" \
  "v_cvt_pk_bf16_f32 v12, v12, v13\n" \
  "v_smfmac_f32_16x16x64_bf16 v[16:19], v[44:47], v[168:175], v0\n" \
  "ds_write_b16 v4, v12 offset:128\n" \
  "v_smfmac_f32_16x16x64_bf16 v[16:19], v[48:51], v[176:183], v0\n" \
  "ds_write_b16_d16_hi v4, v12 offset:416\n" \
  "v_smfmac_f32_16x16x64_bf16 v[16:19], v[52:55], v[184:191], v0\n" \
  "s_barrier\n" \
  "s_nop 6\n" \
  "v_add_f32 v16, v16, v18\n" \
  "v_add_f32 v17, v17, v19\n" \
  "v_cvt_pk_bf16_f32 v16, v16, v17\n" \
  "ds_write_b16 v4, v16 offset:2560\n" \
  "ds_write_b16_d16_hi v4, v16 offset:2848\n" \
  "s_cbranch_scc1 .LCV_FIRST_%=\n" \
  ".LCV_BACK_%=:\n" \
  "s_waitcnt lgkmcnt(0)\n" \
  "s_barrier\n" \
  "s_setprio 0\n" \
  "ds_read_b128 v[60:63], v6 offset:0\n" \
  "v_readfirstlane_b32 s86, v11\n" \
  "s_add_u32 s88, s74, 157760\n" \
  "v_mov_b32 v8, s88\n" \
  "v_mov_b32 v9, 0\n" \
  "ds_write_b32 v8, v9\n" \
  "s_lshr_b32 s86, s86, 23\n" \
  "s_add_i32 s86, s86, s83\n" \
  "s_ashr_i32 s86, s86, 1\n" \
  "s_max_i32 s86, s86, s82\n" \
  "s_min_i32 s86, s86, 100\n" \
  "s_lshl_b32 s86, s86, 23\n" \
  "s_cmp_eq_u32 s71, 0\n" \
  "s_waitcnt lgkmcnt(1)\n" \
  "v_max_u32 v60, 1, v60\n" \
  "v_max_u32 v61, 1, v61\n" \
  "v_max_u32 v62, 1, v62\n" \
  "v_max_u32 v63, 1, v63\n" \
  "buffer_store_dwordx4 v[60:63], v7, s[48:51], s69 offen sc1\n" \
  "s_cbranch_scc1 .LCV_LAST_%=\n" \
  "s_waitcnt vmcnt(1)\n" \
  "v_cmp_eq_u32 vcc, 0, v5\n" \
  "s_cmp_eq_u64 vcc, 0\n" \
  "s_cselect_b32 s90, 1, 0\n" \
  "s_barrier\n" \
  "ds_read_b128 v[24:27], v3 offset:0\n" \
  "ds_read_b128 v[28:31], v3 offset:1024\n" \
  "ds_read_b128 v[32:35], v3 offset:2048\n" \
  "ds_read_b128 v[36:39], v3 offset:3072\n" \
  "ds_read_b128 v[40:43], v3 offset:8192\n" \
  "ds_read_b128 v[44:47], v3 offset:9216\n" \
  "ds_read_b128 v[48:51], v3 offset:10240\n" \
  "ds_read_b128 v[52:55], v3 offset:11264\n" \
  "ds_read_b128 v[56:59], v3 offset:4096\n" \
  "ds_read_b128 v[60:63], v3 offset:5120\n" \
  "s_cbranch_scc0 .LCV_NOTOK_%=\n" \
  "s_mov_b32 m0, s99\n" \
  "s_add_u32 s88, s99, 8192\n" \
  "global_load_lds_dwordx4 v10, s[62:63] sc1\n" \
  "global_load_lds_dwordx4 v10, s[62:63] offset:1024 sc1\n" \
  "s_mov_b32 m0, s88\n" \
  "s_nop 0\n" \
  "global_load_lds_dwordx4 v10, s[64:65] sc1\n" \
  "global_load_lds_dwordx4 v10, s[64:65] offset:1024 sc1\n" \
  ".LCV_NOTOK_%=:\n" \
  "v_mov_b64 v[12:13], s[84:85]\n" \
  "v_mov_b64 v[14:15], s[84:85]\n" \
  "v_mov_b64 v[16:17], s[84:85]\n" \
  "v_mov_b64 v[18:19], s[84:85]\n" \
  "v_mov_b64 v[20:21], s[84:85]\n" \
  "v_mov_b64 v[22:23], s[84:85]\n" \
  "s_waitcnt lgkmcnt(0)\n" \
  "s_barrier\n" \
  "ds_read_b128 v[4:7], v1 offset:8192\n" \
  "ds_read_b128 v[8:11], v1 offset:9216\n" \
  "s_waitcnt lgkmcnt(1)\n" \
  "v_smfmac_f32_16x16x64_bf16 v[12:15], v[4:7], v[24:31], v0\n" \
  "s_waitcnt lgkmcnt(0)\n" \
  "v_smfmac_f32_16x16x64_bf16 v[12:15], v[8:11], v[40:47], v0\n" \
  "ds_read_b128 v[24:27], v3 offset:12288\n" \
  "ds_read_b128 v[28:31], v3 offset:13312\n" \
  "v_smfmac_f32_16x16x64_bf16 v[16:19], v[4:7], v[32:39], v0\n" \
  "ds_read_b128 v[32:35], v3 offset:6144\n" \
  "ds_read_b128 v[36:39], v3 offset:7168\n" \
  "v_smfmac_f32_16x16x64_bf16 v[16:19], v[8:11], v[48:55], v0\n" \
  "s_cmp_eq_u32 s90, 1\n" \
  "s_cbranch_scc1 .LCV_NMT_%=\n" \
  "buffer_load_dword v55, off, s[44:47], s68 sc1\n" \
  ".LCV_NMT_%=:\n" \
  "v_add_f32 v12, v12, v14\n" \
  "v_add_f32 v13, v13, v15\n" \
  "global_atomic_add_x2 v2, v[12:13], s[26:27]\n" \
  "v_smfmac_f32_16x16x64_bf16 v[20:23], v[4:7], v[56:63], v0\n" \
  "ds_read_b128 v[56:59], v3 offset:14336\n" \
  "ds_read_b128 v[60:63], v3 offset:15360\n" \
  "v_add_f32 v16, v16, v18\n" \
  "v_add_f32 v17, v17, v19\n" \
  "global_atomic_add_x2 v2, v[16:17], s[28:29]\n" \
  "v_mov_b64 v[40:41], s[84:85]\n" \
  "v_mov_b64 v[42:43], s[84:85]\n" \
  "s_waitcnt lgkmcnt(4)\n" \
  "v_smfmac_f32_16x16x64_bf16 v[20:23], v[8:11], v[24:31], v0\n" \
  "s_waitcnt lgkmcnt(2)\n" \
  "v_smfmac_f32_16x16x64_bf16 v[40:43], v[4:7], v[32:39], v0\n" \
  "s_waitcnt lgkmcnt(0)\n" \
  "v_smfmac_f32_16x16x64_bf16 v[40:43], v[8:11], v[56:63], v0\n" \
  "s_nop 3\n" \
  "v_add_f32 v20, v20, v22\n" \
  "v_add_f32 v21, v21, v23\n" \
  "global_atomic_add_x2 v2, v[20:21], s[30:31]\n" \
  "s_nop 0\n" \
  "v_add_f32 v40, v40, v42\n" \
  "v_add_f32 v41, v41, v43\n" \
  "global_atomic_add_x2 v2, v[40:41], s[32:33]\n" \
  "v_mbcnt_lo_u32_b32 v48, -1, 0\n" \
  "v_mbcnt_hi_u32_b32 v48, -1, v48\n" \
  "s_add_u32 s20, s20, 32\n" \
  "s_cmp_eq_u32 s90, 1\n" \
  "s_cbranch_scc1 .LCV_HAVE_%=\n" \
  "s_mov_b32 s87, 0x40000\n" \
  "v_lshlrev_b32 v10, 4, v48\n" \
  "s_waitcnt vmcnt(4)\n" \
  "v_cmp_eq_u32 vcc, 0, v55\n" \
  "s_cmp_eq_u64 vcc, 0\n" \
  "s_cbranch_scc1 .LCV_GOT_%=\n" \
  ".LCV_LATE_%=:\n" \
  "buffer_load_dword v58, off, s[44:47], s68 sc1\n" \
  "s_sub_u32 s87, s87, 1\n" \
  "s_cbranch_scc1 .LCV_END_%=\n" \
  "s_waitcnt vmcnt(0)\n" \
  "v_cmp_eq_u32 vcc, 0, v58\n" \
  "s_cmp_eq_u64 vcc, 0\n" \
  "s_cbranch_scc1 .LCV_GOT_%=\n" \
  "s_sleep 12\n" \
  "s_branch .LCV_LATE_%=\n" \
  ".LCV_GOT_%=:\n" \
  "s_mov_b32 m0, s99\n" \
  "s_add_u32 s88, s99, 8192\n" \
  "global_load_lds_dwordx4 v10, s[62:63] sc1\n" \
  "global_load_lds_dwordx4 v10, s[62:63] offset:1024 sc1\n" \
  "s_mov_b32 m0, s88\n" \
  "s_nop 0\n" \
  "global_load_lds_dwordx4 v10, s[64:65] sc1\n" \
  "global_load_lds_dwordx4 v10, s[64:65] offset:1024 sc1\n" \
  ".LCV_HAVE_%=:\n" \
  "v_lshrrev_b32 v49, 3, v48\n" \
  "v_and_b32 v50, 7, v48\n" \
  "v_lshlrev_b32 v50, 4, v50\n" \
  "v_lshl_or_b32 v44, v49, 8, v50\n" \
  "s_mov_b32 s21, 0x8000\n" \
  "v_mov_b32 v52, s22\n" \
  "v_mov_b32 v54, s20\n" \
  "s_branch .LCV_TKF_%=\n" \
  ".LCV_TKE_%=:\n" \
  "s_sleep 1\n" \
  ".LCV_TKF_%=:\n" \
  "ds_read_b64 v[54:55], v52\n" \
  "s_sub_u32 s21, s21, 1\n" \
  "s_cbranch_scc1 .LCV_TKO_%=\n" \
  "s_waitcnt lgkmcnt(0)\n" \
  "v_readfirstlane_b32 s38, v54\n" \
  "v_readfirstlane_b32 s34, v55\n" \
  "s_cmp_eq_u32 s38, s20\n" \
  "s_cbranch_scc0 .LCV_TKE_%=\n" \
  ".LCV_TKO_%=:\n" \
  "s_branch .LCV_WAIU_%=\n" \
  ".LCV_WAIS_%=:\n" \
  "s_cmp_lt_i32 s39, -1000\n" \
  "s_cbranch_scc1 .LCV_WAIE_%=\n" \
  "s_sleep 1\n" \
  ".LCV_WAIU_%=:\n" \
  "s_memrealtime s[38:39]\n" \
  "s_waitcnt lgkmcnt(0)\n" \
  "s_sub_i32 s39, s38, s34\n" \
  "s_cmp_lt_i32 s39, -7\n" \
  "s_cbranch_scc1 .LCV_WAIS_%=\n" \
  "s_cmp_lt_i32 s39, 0\n" \
  "s_cbranch_scc1 .LCV_WAIU_%=\n" \
  ".LCV_WAIE_%=:\n" \
  "buffer_load_dwordx4 v[24:27], v44, s[40:43], s60 offen sc1\n" \
  "buffer_load_dwordx4 v[28:31], v44, s[40:43], s60 offen offset:2048 sc1\n" \
  "v_mbcnt_lo_u32_b32 v48, -1, 0\n" \
  "v_mbcnt_hi_u32_b32 v48, -1, v48\n" \
  "s_sub_u32 s88, s73, 4\n" \
  "v_and_b32 v51, 15, v48\n" \
  "v_lshl_add_u32 v4, v51, 1, s97\n" \
  "v_lshrrev_b32 v49, 4, v48\n" \
  "v_mad_u32_u24 v4, v49, s94, v4\n" \
  "v_lshrrev_b32 v49, 2, v51\n" \
  "v_and_b32 v50, 5, v51\n" \
  "v_and_or_b32 v49, v49, 2, v50\n" \
  "s_lshl_b32 s89, s88, 6\n" \
  "s_add_u32 s89, s89, s74\n" \
  "s_add_u32 s89, s89, 161152\n" \
  "v_and_b32 v50, 48, v48\n" \
  "v_add_u32 v6, s89, v50\n" \
  "v_mad_u32_u24 v6, v49, s95, v6\n" \
  "v_lshlrev_b32 v7, 3, v50\n" \
  "v_lshl_add_u32 v7, v49, 4, v7\n" \
  "v_and_b32 v49, 2, v51\n" \
  "v_lshl_add_u32 v7, v49, 29, v7\n" \
  "v_lshlrev_b32 v10, 4, v48\n" \
  "v_mov_b64 v[12:13], 0\n" \
  "v_mov_b64 v[14:15], 0\n" \
  "v_mov_b64 v[16:17], 0\n" \
  "v_mov_b64 v[18:19], 0\n" \
  "v_mov_b64 v[20:21], 0\n" \
  "v_mov_b64 v[22:23], 0\n" \
  "v_and_b32 v49, 3, v48\n" \
  "v_lshl_add_u32 v40, v49, 2, s98\n" \
  "v_lshrrev_b32 v49, 2, v48\n" \
  "v_bfe_u32 v48, v48, 4, 1\n" \
  "v_xor_b32 v49, v49, v48\n" \
  "v_lshl_add_u32 v40, v49, 5, v40\n" \
  "s_add_u32 s62, s62, 0x80000\n" \
  "s_addc_u32 s63, s63, 0\n" \
  "s_add_u32 s64, s64, 0x40000\n" \
  "s_addc_u32 s65, s65, 0\n" \
  "s_add_u32 s68, s68, 0x1000\n" \
  "s_add_u32 s69, s69, 0x10000\n" \
  "s_add_i32 s80, s86, 0x35000000\n" \
  "s_sub_u32 s91, 0x4a350000, s86\n" \
  "s_min_u32 s91, s91, 0x4ca00000\n" \
  "s_mov_b32 s89, s60\n" \
  "s_xor_b32 s60, s60, 0x80\n" \
  "s_bitcmp0_b32 s60, 7\n" \
  "s_cselect_b32 s88, 0x8000, 0\n" \
  "s_add_u32 s60, s60, s88\n" \
  "s_and_b32 s60, s60, 0xffff\n" \
  "s_add_u32 s58, s56, s60\n" \
  "s_addc_u32 s59, s57, 0\n" \
  "s_xor_b32 s38, s35, 0\n" \
  "s_add_u32 s26, s58, s38\n" \
  "s_addc_u32 s27, s59, 0\n" \
  "s_xor_b32 s38, s35, 1024\n" \
  "s_add_u32 s28, s58, s38\n" \
  "s_addc_u32 s29, s59, 0\n" \
  "s_xor_b32 s38, s35, 2048\n" \
  "s_add_u32 s30, s58, s38\n" \
  "s_addc_u32 s31, s59, 0\n" \
  "s_xor_b32 s38, s35, 3072\n" \
  "s_add_u32 s32, s58, s38\n" \
  "s_addc_u32 s33, s59, 0\n" \
  "s_sub_u32 s71, s71, 1\n" \
  "s_mov_b32 s87, 0x40000\n" \
  "s_branch .LCV_CHK_%=\n" \
  ".LCV_POLL_%=:\n" \
  "buffer_load_dwordx4 v[24:27], v44, s[40:43], s89 offen sc1\n" \
  "buffer_load_dwordx4 v[28:31], v44, s[40:43], s89 offen offset:2048 sc1\n" \
  ".LCV_CHK_%=:\n" \
  "s_sub_u32 s87, s87, 1\n" \
  "s_cbranch_scc1 .LCV_END_%=\n" \
  "s_waitcnt vmcnt(1)\n" \
  "v_cvt_f32_i32 v32, v24\n" \
  "v_cvt_f32_i32 v33, v25\n" \
  "v_cvt_f32_i32 v34, v26\n" \
  "v_cvt_f32_i32 v35, v27\n" \
  "v_pk_mul_f32 v[24:25], v[32:33], s[80:81] op_sel_hi:[1,0]\n" \
  "v_pk_mul_f32 v[26:27], v[34:35], s[80:81] op_sel_hi:[1,0]\n" \
  "v_max3_f32 v45, |v32|, |v33|, |v34|\n" \
  "v_cvt_pk_bf16_f32 v24, v24, v26\n" \
  "v_cvt_pk_bf16_f32 v25, v25, v27\n" \
  "ds_write2_b32 v40, v24, v25 offset0:0 offset1:4\n" \
  "v_max3_f32 v45, |v45|, |v35|, |v35|\n" \
  "s_waitcnt vmcnt(0)\n" \
  "v_cvt_f32_i32 v32, v28\n" \
  "v_cvt_f32_i32 v33, v29\n" \
  "v_cvt_f32_i32 v34, v30\n" \
  "v_cvt_f32_i32 v35, v31\n" \
  "v_pk_mul_f32 v[28:29], v[32:33], s[80:81] op_sel_hi:[1,0]\n" \
  "v_pk_mul_f32 v[30:31], v[34:35], s[80:81] op_sel_hi:[1,0]\n" \
  "v_max3_f32 v45, |v45|, |v32|, |v33|\n" \
  "v_cvt_pk_bf16_f32 v28, v28, v30\n" \
  "v_cvt_pk_bf16_f32 v29, v29, v31\n" \
  "ds_write2_b32 v40, v28, v29 offset0:128 offset1:132\n" \
  "v_max3_f32 v45, |v45|, |v34|, |v35|\n" \
  "v_cmp_lt_f32 vcc, s91, v45\n" \
  "s_cbranch_vccnz .LCV_BIG_%=\n" \
  ".LCV_SML_%=:\n" \
  "s_branch .LCV_TOP_%=\n" \
  ".LCV_BIG_%=:\n" \
  "v_cmp_lt_f32 vcc, 0x4ca00000, v45\n" \
  "s_cbranch_vccnz .LCV_POLL_%=\n" \
  "v_mul_f32 v45, s80, v45\n" \
  "v_mul_f32 v45, v45, v45\n" \
  "s_add_u32 s88, s74, 157760\n" \
  "v_mov_b32 v44, s88\n" \
  "s_nop 0\n" \
  "v_cmpx_lt_f32 vcc, 2.0, v45\n" \
  "ds_max_u32 v44, v45\n" \
  "s_mov_b64 exec, -1\n" \
  "s_branch .LCV_SML_%=\n" \
  ".LCV_FIRST_%=:\n" \
  "s_mov_b32 s87, 0x100000\n" \
  "s_branch .LCV_FTEST_%=\n" \
  ".LCV_FASK_%=:\n" \
  "s_sleep %[sf]\n" \
  "buffer_load_dword v5, off, s[44:47], s68 sc1\n" \
  "s_sub_u32 s87, s87, 1\n" \
  "s_cbranch_scc1 .LCV_END_%=\n" \
  ".LCV_FTEST_%=:\n" \
  "s_waitcnt vmcnt(0)\n" \
  "v_cmp_eq_u32 vcc, 0, v5\n" \
  "s_cmp_eq_u64 vcc, 0\n" \
  "s_cbranch_scc0 .LCV_FASK_%=\n" \
  "s_mov_b32 m0, s99\n" \
  "s_add_u32 s88, s99, 8192\n" \
  "global_load_lds_dwordx4 v10, s[62:63] sc1\n" \
  "global_load_lds_dwordx4 v10, s[62:63] offset:1024 sc1\n" \
  "s_mov_b32 m0, s88\n" \
  "s_nop 0\n" \
  "global_load_lds_dwordx4 v10, s[64:65] sc1\n" \
  "global_load_lds_dwordx4 v10, s[64:65] offset:1024 sc1\n" \
  "buffer_load_dword v9, off, s[16:19], s24 offset:8 sc1\n" \
  "s_add_u32 s62, s62, 0x80000\n" \
  "s_addc_u32 s63, s63, 0\n" \
  "s_add_u32 s64, s64, 0x40000\n" \
  "s_addc_u32 s65, s65, 0\n" \
  "s_add_u32 s68, s68, 0x1000\n" \
  "s_waitcnt vmcnt(0)\n" \
  "v_readfirstlane_b32 s89, v9\n" \
  "s_add_u32 s89, s89, s25\n" \
  "s_sub_u32 s89, s89, %[g1]\n" \
  ".LCV_FWAIT_%=:\n" \
  "s_memrealtime s[38:39]\n" \
  "s_waitcnt lgkmcnt(0)\n" \
  "s_sub_u32 s38, s89, s38\n" \
  "s_cmp_lt_u32 s38, 600\n" \
  "s_cbranch_scc1 .LCV_FWAIT_%=\n" \
  "buffer_load_dword v5, off, s[44:47], s68 sc1\n" \
  "s_branch .LCV_BACK_%=\n" \
  ".LCV_LAST_%=:\n" \
  "s_waitcnt vmcnt(0)\n" \
  ".LCV_END_%=:\n" \
  "s_endpgm\n"
#define QKV_ASM_0 \
  "s_cmp_lg_u32 s69, 0\n" \
  "s_mov_b32 s69, 0\n" \
  "s_cbranch_scc1 .LQA_GOT_%=\n" \
  "s_branch .LQA_POLL_%=\n" \
  ".p2align 6\n" \
  ".LQA_AL_%=:\n" \
  ".LQA_TOP_%=:\n" \
  "s_barrier\n" \
  "ds_read_b128 v[36:39], v7 offset:0\n" \
  "ds_read_b128 v[40:43], v7 offset:2048\n" \
  "ds_read_b128 v[44:47], v7 offset:4096\n" \
  "s_setprio 2\n" \
  "s_waitcnt lgkmcnt(2)\n" \
  "v_mfma_f32_16x16x32_bf16 v[12:15], v[64:67], v[36:39], 0\n" \
  "ds_read_b128 v[48:51], v7 offset:6144\n" \
  "v_mfma_f32_16x16x32_bf16 v[16:19], v[128:131], v[36:39], 0\n" \
  "v_mfma_f32_16x16x32_bf16 v[20:23], v[36:39], v[192:195], 0\n" \
  "s_cmp_lg_u32 s69, 1\n" \
  "s_cbranch_scc1 .LQA_FDN_%=\n" \
  "s_waitcnt vmcnt(1)\n" \
  "v_mov_b32 v52, s80\n" \
  "v_mov_b32 v53, 0x10000\n" \
  "s_mov_b64 exec, 1\n" \
  "ds_add_rtn_u32 v54, v52, v53\n" \
  "s_mov_b64 exec, -1\n" \
  "s_add_u32 s67, s67, 4\n" \
  "s_add_u32 s64, s64, 4096\n" \
  "s_addc_u32 s65, s65, 0\n" \
  "v_mov_b32 v55, 0\n" \
  "s_waitcnt lgkmcnt(0)\n" \
  "v_readfirstlane_b32 s74, v54\n" \
  "s_and_b32 s76, s74, 0x30000\n" \
  "s_cmp_lg_u32 s76, 0x30000\n" \
  "s_cbranch_scc1 .LQA_FDN_%=\n" \
  "s_mov_b32 s79, 0x100000\n" \
  "s_branch .LQA_FDT_%=\n" \
  ".LQA_FD_%=:\n" \
  "ds_read_b32 v54, v52\n" \
  "s_sub_u32 s79, s79, 1\n" \
  "s_cbranch_scc1 .LQA_FDE_%=\n" \
  "s_waitcnt lgkmcnt(0)\n" \
  "v_readfirstlane_b32 s74, v54\n" \
  ".LQA_FDT_%=:\n" \
  "s_and_b32 s74, s74, 0xffff\n" \
  "s_cmp_lt_u32 s74, s67\n" \
  "s_cbranch_scc1 .LQA_FD_%=\n" \
  ".LQA_FDE_%=:\n" \
  "s_mov_b64 exec, 1\n" \
  "global_store_dword v55, v53, s[64:65] offset:-4096 sc1\n" \
  "s_mov_b64 exec, -1\n" \
  "s_waitcnt vmcnt(1)\n" \
  "s_branch .LQA_LOOK_%=\n" \
  ".LQA_FDN_%=:\n" \
  "s_waitcnt vmcnt(0)\n" \
  ".LQA_LOOK_%=:\n" \
  "v_cmp_eq_u32 vcc, 0, v11\n" \
  "s_mov_b32 m0, s58\n" \
  "s_add_u32 s74, s56, 4096\n" \
  "s_addc_u32 s75, s57, 0\n" \
  "s_add_u32 s76, s58, 4096\n" \
  "s_cmp_eq_u64 vcc, 0\n" \
  "s_cselect_b32 s69, 1, 0\n" \
  "s_waitcnt lgkmcnt(2)\n" \
  "v_mfma_f32_16x16x32_bf16 v[12:15], v[68:71], v[40:43], v[12:15]\n" \
  "ds_read_b128 v[36:39], v7 offset:8192\n" \
  "s_cbranch_scc0 2\n" \
  "global_load_lds_dwordx4 v6, s[56:57] offset:0 sc1\n" \
  "v_mfma_f32_16x16x32_bf16 v[16:19], v[132:135], v[40:43], v[16:19]\n" \
  "s_cbranch_scc0 2\n" \
  "global_load_lds_dwordx4 v6, s[56:57] offset:1024 sc1\n" \
  "v_mfma_f32_16x16x32_bf16 v[20:23], v[40:43], v[196:199], v[20:23]\n" \
  "s_cbranch_scc0 2\n" \
  "global_load_lds_dwordx4 v6, s[56:57] offset:2048 sc1\n" \
  "s_waitcnt lgkmcnt(2)\n" \
  "v_mfma_f32_16x16x32_bf16 v[12:15], v[72:75], v[44:47], v[12:15]\n" \
  "ds_read_b128 v[40:43], v7 offset:10240\n" \
  "s_cbranch_scc0 2\n" \
  "global_load_lds_dwordx4 v6, s[56:57] offset:3072 sc1\n" \
  "v_mfma_f32_16x16x32_bf16 v[16:19], v[136:139], v[44:47], v[16:19]\n" \
  "s_mov_b32 m0, s76\n" \
  "s_cbranch_scc0 2\n" \
  "global_load_lds_dwordx4 v6, s[74:75] offset:0 sc1\n" \
  "v_mfma_f32_16x16x32_bf16 v[20:23], v[44:47], v[200:203], v[20:23]\n" \
  "s_cbranch_scc0 2\n" \
  "global_load_lds_dwordx4 v6, s[74:75] offset:1024 sc1\n" \
  "s_waitcnt lgkmcnt(2)\n" \
  "v_mfma_f32_16x16x32_bf16 v[12:15], v[76:79], v[48:51], v[12:15]\n" \
  "ds_read_b128 v[44:47], v7 offset:12288\n" \
  "s_cbranch_scc0 2\n" \
  "global_load_lds_dwordx4 v6, s[74:75] offset:2048 sc1\n" \
  "v_mfma_f32_16x16x32_bf16 v[16:19], v[140:143], v[48:51], v[16:19]\n" \
  "s_cbranch_scc0 2\n" \
  "global_load_lds_dwordx4 v6, s[74:75] offset:3072 sc1\n" \
  "v_mfma_f32_16x16x32_bf16 v[20:23], v[48:51], v[204:207], v[20:23]\n" \
  "s_cselect_b32 s76, 0x40000, s62\n" \
  "s_add_u32 s74, s59, 4096\n" \
  "s_waitcnt lgkmcnt(2)\n" \
  "v_mfma_f32_16x16x32_bf16 v[12:15], v[80:83], v[36:39], v[12:15]\n" \
  "ds_read_b128 v[48:51], v7 offset:14336\n" \
  "v_mov_b64 v[24:25], 0\n" \
  "v_lshrrev_b32 v26, 1, v6\n" \
  "buffer_store_dwordx2 v[24:25], v26, s[48:51], s61 offen sc1\n" \
  "v_mfma_f32_16x16x32_bf16 v[16:19], v[144:147], v[36:39], v[16:19]\n" \
  "v_mfma_f32_16x16x32_bf16 v[20:23], v[36:39], v[208:211], v[20:23]\n" \
  "s_waitcnt lgkmcnt(2)\n" \
  "v_mfma_f32_16x16x32_bf16 v[12:15], v[84:87], v[40:43], v[12:15]\n" \
  "ds_read_b128 v[36:39], v7 offset:16384\n" \
  "v_mfma_f32_16x16x32_bf16 v[16:19], v[148:151], v[40:43], v[16:19]\n" \
  "v_mfma_f32_16x16x32_bf16 v[20:23], v[40:43], v[212:215], v[20:23]\n" \
  "s_waitcnt lgkmcnt(2)\n" \
  "v_mfma_f32_16x16x32_bf16 v[12:15], v[88:91], v[44:47], v[12:15]\n" \
  "ds_read_b128 v[40:43], v7 offset:18432\n" \
  "v_mfma_f32_16x16x32_bf16 v[16:19], v[152:155], v[44:47], v[16:19]\n" \
  "v_mfma_f32_16x16x32_bf16 v[20:23], v[44:47], v[216:219], v[20:23]\n" \
  "s_waitcnt lgkmcnt(2)\n" \
  "v_mfma_f32_16x16x32_bf16 v[12:15], v[92:95], v[48:51], v[12:15]\n" \
  "ds_read_b128 v[44:47], v7 offset:20480\n" \
  "v_mfma_f32_16x16x32_bf16 v[16:19], v[156:159], v[48:51], v[16:19]\n" \
  "v_mfma_f32_16x16x32_bf16 v[20:23], v[48:51], v[220:223], v[20:23]\n" \
  "s_waitcnt lgkmcnt(2)\n" \
  "v_mfma_f32_16x16x32_bf16 v[12:15], v[96:99], v[36:39], v[12:15]\n" \
  "ds_read_b128 v[48:51], v7 offset:22528\n" \
  "v_mfma_f32_16x16x32_bf16 v[16:19], v[160:163], v[36:39], v[16:19]\n" \
  "v_mfma_f32_16x16x32_bf16 v[20:23], v[36:39], v[224:227], v[20:23]\n" \
  "s_waitcnt lgkmcnt(2)\n" \
  "v_mfma_f32_16x16x32_bf16 v[12:15], v[100:103], v[40:43], v[12:15]\n" \
  "ds_read_b128 v[36:39], v7 offset:24576\n" \
  "v_mfma_f32_16x16x32_bf16 v[16:19], v[164:167], v[40:43], v[16:19]\n" \
  "v_mfma_f32_16x16x32_bf16 v[20:23], v[40:43], v[228:231], v[20:23]\n" \
  "s_waitcnt lgkmcnt(2)\n" \
  "v_mfma_f32_16x16x32_bf16 v[12:15], v[104:107], v[44:47], v[12:15]\n" \
  "ds_read_b128 v[40:43], v7 offset:26624\n" \
  "v_mfma_f32_16x16x32_bf16 v[16:19], v[168:171], v[44:47], v[16:19]\n" \
  "v_mfma_f32_16x16x32_bf16 v[20:23], v[44:47], v[232:235], v[20:23]\n" \
  "s_waitcnt lgkmcnt(2)\n" \
  "v_mfma_f32_16x16x32_bf16 v[12:15], v[108:111], v[48:51], v[12:15]\n" \
  "ds_read_b128 v[44:47], v7 offset:28672\n" \
  "v_mfma_f32_16x16x32_bf16 v[16:19], v[172:175], v[48:51], v[16:19]\n" \
  "v_mfma_f32_16x16x32_bf16 v[20:23], v[48:51], v[236:239], v[20:23]\n" \
  "s_waitcnt lgkmcnt(2)\n" \
  "v_mfma_f32_16x16x32_bf16 v[12:15], v[112:115], v[36:39], v[12:15]\n" \
  "ds_read_b128 v[48:51], v7 offset:30720\n" \
  "v_mfma_f32_16x16x32_bf16 v[16:19], v[176:179], v[36:39], v[16:19]\n" \
  "v_mfma_f32_16x16x32_bf16 v[20:23], v[36:39], v[240:243], v[20:23]\n" \
  "s_waitcnt lgkmcnt(2)\n" \
  "v_mfma_f32_16x16x32_bf16 v[12:15], v[116:119], v[40:43], v[12:15]\n" \
  "ds_read_b128 v[36:39], v7 offset:256\n" \
  "v_mfma_f32_16x16x32_bf16 v[16:19], v[180:183], v[40:43], v[16:19]\n" \
  "v_mfma_f32_16x16x32_bf16 v[20:23], v[40:43], v[244:247], v[20:23]\n" \
  "s_waitcnt lgkmcnt(2)\n" \
  "v_mfma_f32_16x16x32_bf16 v[12:15], v[120:123], v[44:47], v[12:15]\n" \
  "ds_read_b128 v[40:43], v7 offset:2304\n" \
  "v_mfma_f32_16x16x32_bf16 v[16:19], v[184:187], v[44:47], v[16:19]\n" \
  "v_mfma_f32_16x16x32_bf16 v[20:23], v[44:47], v[248:251], v[20:23]\n" \
  "s_waitcnt lgkmcnt(2)\n" \
  "v_mfma_f32_16x16x32_bf16 v[12:15], v[124:127], v[48:51], v[12:15]\n" \
  "ds_read_b128 v[44:47], v7 offset:4352\n" \
  "v_mfma_f32_16x16x32_bf16 v[16:19], v[188:191], v[48:51], v[16:19]\n" \
  "v_mfma_f32_16x16x32_bf16 v[20:23], v[48:51], v[252:255], v[20:23]\n" \
  "s_barrier\n" \
  "s_waitcnt lgkmcnt(2)\n" \
  "v_mfma_f32_16x16x32_bf16 v[24:27], v[64:67], v[36:39], 0\n" \
  "ds_read_b128 v[48:51], v7 offset:6400\n" \
  "v_mfma_f32_16x16x32_bf16 v[28:31], v[128:131], v[36:39], 0\n" \
  "ds_read2_b32 v[52:53], v8 offset0:0 offset1:0\n" \
  "v_mfma_f32_16x16x32_bf16 v[32:35], v[36:39], v[192:195], 0\n" \
  "s_waitcnt lgkmcnt(3)\n" \
  "v_mfma_f32_16x16x32_bf16 v[24:27], v[68:71], v[40:43], v[24:27]\n" \
  "ds_read_b128 v[36:39], v7 offset:8448\n" \
  "v_mfma_f32_16x16x32_bf16 v[28:31], v[132:135], v[40:43], v[28:31]\n" \
  "ds_read2_b32 v[54:55], v8 offset0:16 offset1:16\n" \
  "v_mfma_f32_16x16x32_bf16 v[32:35], v[40:43], v[196:199], v[32:35]\n" \
  "s_waitcnt lgkmcnt(4)\n" \
  "v_mfma_f32_16x16x32_bf16 v[24:27], v[72:75], v[44:47], v[24:27]\n" \
  "ds_read_b128 v[40:43], v7 offset:10496\n" \
  "v_mfma_f32_16x16x32_bf16 v[28:31], v[136:139], v[44:47], v[28:31]\n" \
  "ds_read2_b32 v[56:57], v8 offset0:32 offset1:48\n" \
  "v_mfma_f32_16x16x32_bf16 v[32:35], v[44:47], v[200:203], v[32:35]\n" \
  "s_waitcnt lgkmcnt(5)\n" \
  "v_mfma_f32_16x16x32_bf16 v[24:27], v[76:79], v[48:51], v[24:27]\n" \
  "ds_read_b128 v[44:47], v7 offset:12544\n" \
  "v_mfma_f32_16x16x32_bf16 v[28:31], v[140:143], v[48:51], v[28:31]\n" \
  "ds_read_b128 v[58:61], v9 offset:128\n" \
  "v_mfma_f32_16x16x32_bf16 v[32:35], v[48:51], v[204:207], v[32:35]\n" \
  "s_waitcnt lgkmcnt(6)\n" \
  "v_mfma_f32_16x16x16_bf16 v[12:15], v[0:1], v[52:53], v[12:15]\n" \
  "v_mfma_f32_16x16x16_bf16 v[16:19], v[2:3], v[52:53], v[16:19]\n" \
  "v_mfma_f32_16x16x16_bf16 v[20:23], v[52:53], v[4:5], v[20:23]\n" \
  "s_waitcnt lgkmcnt(4)\n" \
  "v_mfma_f32_16x16x16_bf16 v[24:27], v[0:1], v[54:55], v[24:27]\n" \
  "v_mfma_f32_16x16x16_bf16 v[28:31], v[2:3], v[54:55], v[28:31]\n" \
  "v_mfma_f32_16x16x16_bf16 v[32:35], v[54:55], v[4:5], v[32:35]\n" \
  "v_mfma_f32_16x16x32_bf16 v[24:27], v[80:83], v[36:39], v[24:27]\n" \
  "ds_read_b128 v[48:51], v7 offset:14592\n" \
  "v_mfma_f32_16x16x32_bf16 v[28:31], v[144:147], v[36:39], v[28:31]\n" \
  "ds_read_b128 v[52:55], v9 offset:192\n" \
  "v_mfma_f32_16x16x32_bf16 v[32:35], v[36:39], v[208:211], v[32:35]\n" \
  "s_waitcnt lgkmcnt(5)\n" \
  "v_mfma_f32_16x16x32_bf16 v[24:27], v[84:87], v[40:43], v[24:27]\n" \
  "ds_read_b128 v[36:39], v7 offset:16640\n" \
  "v_mfma_f32_16x16x32_bf16 v[28:31], v[148:151], v[40:43], v[28:31]\n" \
  "v_mfma_f32_16x16x32_bf16 v[32:35], v[40:43], v[212:215], v[32:35]\n" \
  "s_waitcnt lgkmcnt(4)\n" \
  "v_mfma_f32_16x16x32_bf16 v[24:27], v[88:91], v[44:47], v[24:27]\n" \
  "ds_read_b128 v[40:43], v7 offset:18688\n" \
  "v_mfma_f32_16x16x32_bf16 v[28:31], v[152:155], v[44:47], v[28:31]\n" \
  "v_mfma_f32_16x16x32_bf16 v[32:35], v[44:47], v[216:219], v[32:35]\n" \
  "s_waitcnt lgkmcnt(3)\n" \
  "v_mfma_f32_16x16x32_bf16 v[24:27], v[92:95], v[48:51], v[24:27]\n" \
  "ds_read_b128 v[44:47], v7 offset:20736\n" \
  "v_mfma_f32_16x16x32_bf16 v[28:31], v[156:159], v[48:51], v[28:31]\n" \
  "v_mfma_f32_16x16x32_bf16 v[32:35], v[48:51], v[220:223], v[32:35]\n" \
  "s_waitcnt lgkmcnt(2)\n" \
  "v_mfma_f32_16x16x32_bf16 v[24:27], v[96:99], v[36:39], v[24:27]\n" \
  "ds_read_b128 v[48:51], v7 offset:22784\n" \
  "v_mfma_f32_16x16x32_bf16 v[28:31], v[160:163], v[36:39], v[28:31]\n" \
  "v_mfma_f32_16x16x32_bf16 v[32:35], v[36:39], v[224:227], v[32:35]\n" \
  "s_waitcnt lgkmcnt(2)\n" \
  "v_mfma_f32_16x16x32_bf16 v[24:27], v[100:103], v[40:43], v[24:27]\n" \
  "ds_read_b128 v[36:39], v7 offset:24832\n" \
  "v_mfma_f32_16x16x32_bf16 v[28:31], v[164:167], v[40:43], v[28:31]\n" \
  "buffer_load_dword v11, v10, s[52:55], s76 offen sc1\n" \
  "v_mfma_f32_16x16x32_bf16 v[32:35], v[40:43], v[228:231], v[32:35]\n" \
  "s_waitcnt lgkmcnt(2)\n" \
  "v_mfma_f32_16x16x32_bf16 v[24:27], v[104:107], v[44:47], v[24:27]\n" \
  "ds_read_b128 v[40:43], v7 offset:26880\n" \
  "v_mfma_f32_16x16x32_bf16 v[28:31], v[168:171], v[44:47], v[28:31]\n" \
  "v_mfma_f32_16x16x32_bf16 v[32:35], v[44:47], v[232:235], v[32:35]\n" \
  "s_waitcnt lgkmcnt(2)\n" \
  "v_mfma_f32_16x16x32_bf16 v[24:27], v[108:111], v[48:51], v[24:27]\n" \
  "ds_read_b128 v[44:47], v7 offset:28928\n" \
  "v_mfma_f32_16x16x32_bf16 v[28:31], v[172:175], v[48:51], v[28:31]\n" \
  "v_mfma_f32_16x16x32_bf16 v[32:35], v[48:51], v[236:239], v[32:35]\n" \
  "s_waitcnt lgkmcnt(2)\n" \
  "v_mfma_f32_16x16x32_bf16 v[24:27], v[112:115], v[36:39], v[24:27]\n" \
  "ds_read_b128 v[48:51], v7 offset:30976\n" \
  "v_mfma_f32_16x16x32_bf16 v[28:31], v[176:179], v[36:39], v[28:31]\n" \
  "v_mfma_f32_16x16x32_bf16 v[32:35], v[36:39], v[240:243], v[32:35]\n" \
  "s_waitcnt lgkmcnt(2)\n" \
  "v_mfma_f32_16x16x32_bf16 v[24:27], v[116:119], v[40:43], v[24:27]\n" \
  "v_mfma_f32_16x16x32_bf16 v[28:31], v[180:183], v[40:43], v[28:31]\n" \
  "v_mfma_f32_16x16x32_bf16 v[32:35], v[40:43], v[244:247], v[32:35]\n" \
  "s_waitcnt lgkmcnt(1)\n" \
  "v_mfma_f32_16x16x32_bf16 v[24:27], v[120:123], v[44:47], v[24:27]\n" \
  "v_mfma_f32_16x16x32_bf16 v[28:31], v[184:187], v[44:47], v[28:31]\n" \
  "v_mfma_f32_16x16x32_bf16 v[32:35], v[44:47], v[248:251], v[32:35]\n" \
  "s_waitcnt lgkmcnt(0)\n" \
  "v_mfma_f32_16x16x32_bf16 v[24:27], v[124:127], v[48:51], v[24:27]\n" \
  "v_mfma_f32_16x16x32_bf16 v[28:31], v[188:191], v[48:51], v[28:31]\n" \
  "v_mfma_f32_16x16x32_bf16 v[32:35], v[48:51], v[252:255], v[32:35]\n" \
  "s_setprio 2\n" \
  "v_mul_f32 v12, v12, v56\n" \
  "v_mul_f32 v13, v13, v56\n" \
  "v_mul_f32 v14, v14, v56\n" \
  "v_cvt_pk_bf16_f32 v12, v12, v13\n" \
  "v_mul_f32 v15, v15, v56\n" \
  "v_mul_f32 v16, v16, v56\n" \
  "v_cvt_pk_bf16_f32 v13, v14, v15\n" \
  "v_mul_f32 v17, v17, v56\n" \
  "v_mul_f32 v18, v18, v56\n" \
  "v_cvt_pk_bf16_f32 v14, v16, v17\n" \
  "v_mul_f32 v19, v19, v56\n" \
  "v_cvt_pk_bf16_f32 v15, v18, v19\n" \
  "buffer_store_dwordx4 v[12:15], v6, s[40:43], s59 offen offset:0 sc1\n" \
  "v_mul_f32 v24, v24, v57\n" \
  "v_mul_f32 v25, v25, v57\n" \
  "v_mul_f32 v26, v26, v57\n" \
  "v_cvt_pk_bf16_f32 v24, v24, v25\n" \
  "v_mul_f32 v27, v27, v57\n" \
  "v_mul_f32 v28, v28, v57\n" \
  "v_cvt_pk_bf16_f32 v25, v26, v27\n" \
  "v_mul_f32 v29, v29, v57\n" \
  "v_mul_f32 v30, v30, v57\n" \
  "v_cvt_pk_bf16_f32 v26, v28, v29\n" \
  "v_mul_f32 v31, v31, v57\n" \
  "v_cvt_pk_bf16_f32 v27, v30, v31\n" \
  "buffer_store_dwordx4 v[24:27], v6, s[40:43], s74 offen offset:0 sc1\n" \
  "v_mul_f32 v20, v20, v58\n" \
  "v_mul_f32 v21, v21, v59\n" \
  "v_mul_f32 v22, v22, v60\n" \
  "v_mul_f32 v23, v23, v61\n" \
  "v_cvt_pk_bf16_f32 v20, v20, v21\n" \
  "v_cvt_pk_bf16_f32 v21, v22, v23\n" \
  "v_mul_f32 v32, v32, v52\n" \
  "v_mul_f32 v33, v33, v53\n" \
  "v_mul_f32 v34, v34, v54\n" \
  "v_mul_f32 v35, v35, v55\n" \
  "v_cvt_pk_bf16_f32 v22, v32, v33\n" \
  "v_cvt_pk_bf16_f32 v23, v34, v35\n" \
  "buffer_store_dwordx4 v[20:23], v6, s[44:47], s60 offen offset:0 sc1\n" \
  "s_setprio 0\n" \
  "s_add_u32 s59, s59, 0x80000\n" \
  "s_add_u32 s60, s60, 0x40000\n" \
  "s_add_u32 s61, s61, 0x10000\n" \
  "v_xor_b32 v7, 0x20000, v7\n" \
  "s_cmp_eq_u32 s69, 1\n" \
  "s_waitcnt vmcnt(3)\n" \
  "s_cbranch_scc0 .LQA_TST_%=\n" \
  "s_cmp_lt_u32 s66, 64\n" \
  "s_cbranch_scc1 .LQA_GO_%=\n" \
  "s_mov_b32 s69, 2\n" \
  "s_branch .LQA_NOX_%=\n" \
  ".LQA_TST_%=:\n" \
  "v_cmp_eq_u32 vcc, 0, v11\n" \
  "s_cmp_eq_u64 vcc, 0\n" \
  "s_cbranch_scc0 .LQA_NOX_%=\n" \
  "s_mov_b32 m0, s58\n" \
  "s_add_u32 s74, s56, 4096\n" \
  "s_addc_u32 s75, s57, 0\n" \
  "s_add_u32 s76, s58, 4096\n" \
  "global_load_lds_dwordx4 v6, s[56:57] offset:0 sc1\n" \
  "global_load_lds_dwordx4 v6, s[56:57] offset:1024 sc1\n" \
  "global_load_lds_dwordx4 v6, s[56:57] offset:2048 sc1\n" \
  "global_load_lds_dwordx4 v6, s[56:57] offset:3072 sc1\n" \
  "s_mov_b32 m0, s76\n" \
  "s_nop 0\n" \
  "global_load_lds_dwordx4 v6, s[74:75] offset:0 sc1\n" \
  "global_load_lds_dwordx4 v6, s[74:75] offset:1024 sc1\n" \
  "global_load_lds_dwordx4 v6, s[74:75] offset:2048 sc1\n" \
  "global_load_lds_dwordx4 v6, s[74:75] offset:3072 sc1\n" \
  "s_mov_b32 s69, 2\n" \
  "s_waitcnt vmcnt(8)\n" \
  "s_branch .LQA_CNT_%=\n" \
  ".LQA_NOX_%=:\n" \
  "s_waitcnt vmcnt(0)\n" \
  ".LQA_CNT_%=:\n" \
  "v_mov_b32 v52, s80\n" \
  "v_mov_b32 v53, 0x10000\n" \
  "s_mov_b64 exec, 1\n" \
  "ds_add_rtn_u32 v54, v52, v53\n" \
  "s_mov_b64 exec, -1\n" \
  "s_add_u32 s67, s67, 4\n" \
  "s_add_u32 s64, s64, 4096\n" \
  "s_addc_u32 s65, s65, 0\n" \
  "v_mov_b32 v55, 0\n" \
  "s_waitcnt lgkmcnt(0)\n" \
  "v_readfirstlane_b32 s74, v54\n" \
  "s_and_b32 s76, s74, 0x30000\n" \
  "s_cmp_lg_u32 s76, 0x30000\n" \
  "s_cbranch_scc1 .LQA_FWN_%=\n" \
  "s_mov_b32 s79, 0x100000\n" \
  "s_branch .LQA_FWT_%=\n" \
  ".LQA_FW_%=:\n" \
  "ds_read_b32 v54, v52\n" \
  "s_sub_u32 s79, s79, 1\n" \
  "s_cbranch_scc1 .LQA_FWE_%=\n" \
  "s_waitcnt lgkmcnt(0)\n" \
  "v_readfirstlane_b32 s74, v54\n" \
  ".LQA_FWT_%=:\n" \
  "s_and_b32 s74, s74, 0xffff\n" \
  "s_cmp_lt_u32 s74, s67\n" \
  "s_cbranch_scc1 .LQA_FW_%=\n" \
  ".LQA_FWE_%=:\n" \
  "s_mov_b64 exec, 1\n" \
  "global_store_dword v55, v53, s[64:65] offset:-4096 sc1\n" \
  "s_mov_b64 exec, -1\n" \
  "s_waitcnt vmcnt(1)\n" \
  "s_branch .LQA_GO_%=\n" \
  ".LQA_FWN_%=:\n" \
  "s_waitcnt vmcnt(0)\n" \
  ".LQA_GO_%=:\n" \
  "s_sub_u32 s66, s66, 1\n" \
  "s_cbranch_scc1 .LQA_LAST_%=\n" \
  "s_cmp_lg_u32 s69, 0\n" \
  "s_cbranch_scc1 .LQA_NEXT_%=\n" \
  ".LQA_GATE_%=:\n" \
  "s_memrealtime s[74:75]\n" \
  "s_waitcnt lgkmcnt(0)\n" \
  "s_sub_i32 s74, s68, s74\n" \
  "s_cmp_gt_u32 s74, %[gq]\n" \
  "s_cbranch_scc1 .LQA_POLL_%=\n" \
  "s_sleep 3\n" \
  "s_branch .LQA_GATE_%=\n" \
  ".LQA_POLL_%=:\n" \
  "s_mov_b32 s79, 0x40000\n" \
  ".LQA_AGAIN_%=:\n" \
  "buffer_load_dword v11, v10, s[52:55], s62 offen sc1\n" \
  "s_sub_u32 s79, s79, 1\n" \
  "s_cbranch_scc1 .LQA_SEEN_%=\n" \
  "s_waitcnt vmcnt(0)\n" \
  "v_cmp_eq_u32 vcc, 0, v11\n" \
  "s_cmp_eq_u64 vcc, 0\n" \
  "s_cbranch_scc1 .LQA_SEEN_%=\n" \
  "s_sleep %[sq]\n" \
  "s_cmp_ge_u32 s66, s63\n" \
  "s_cbranch_scc1 .LQA_AGAIN_%=\n" \
  "s_sleep %[s1]\n" \
  "s_branch .LQA_AGAIN_%=\n" \
  ".LQA_SEEN_%=:\n" \
  "s_mov_b32 m0, s58\n" \
  "s_add_u32 s74, s56, 4096\n" \
  "s_addc_u32 s75, s57, 0\n" \
  "s_add_u32 s76, s58, 4096\n" \
  "global_load_lds_dwordx4 v6, s[56:57] offset:0 sc1\n" \
  "global_load_lds_dwordx4 v6, s[56:57] offset:1024 sc1\n" \
  "global_load_lds_dwordx4 v6, s[56:57] offset:2048 sc1\n" \
  "global_load_lds_dwordx4 v6, s[56:57] offset:3072 sc1\n" \
  "s_mov_b32 m0, s76\n" \
  "s_nop 0\n" \
  "global_load_lds_dwordx4 v6, s[74:75] offset:0 sc1\n" \
  "global_load_lds_dwordx4 v6, s[74:75] offset:1024 sc1\n" \
  "global_load_lds_dwordx4 v6, s[74:75] offset:2048 sc1\n" \
  "global_load_lds_dwordx4 v6, s[74:75] offset:3072 sc1\n" \
  ".LQA_GOT_%=:\n" \
  "s_memrealtime s[76:77]\n" \
  "s_waitcnt lgkmcnt(0)\n" \
  "s_add_u32 s68, s76, %[gq]\n" \
  "s_waitcnt vmcnt(0)\n" \
  ".LQA_NEXT_%=:\n" \
  "s_add_u32 s56, s56, 0x8000\n" \
  "s_addc_u32 s57, s57, 0\n" \
  "s_xor_b32 s58, s58, 0x20000\n" \
  "s_add_u32 s62, s62, 4096\n" \
  "buffer_load_dword v11, v10, s[52:55], s62 offen sc1\n" \
  "s_branch .LQA_TOP_%=\n" \
  ".LQA_LAST_%=:\n" \
  "s_barrier\n" \
  "s_endpgm\n"
#define QKV_ASM_1 \
  "s_barrier\n" \
  "ds_read_b128 v[36:39], v10 offset:0\n" \
  "ds_read_b128 v[40:43], v7 offset:0\n" \
  "ds_read_b128 v[44:47], v10 offset:2048\n" \
  "ds_read_b128 v[48:51], v7 offset:2048\n" \
  ".p2align 6\n" \
  ".LQB_AL_%=:\n" \
  ".LQB_TOP_%=:\n" \
  "s_setprio 3\n" \
  "s_waitcnt lgkmcnt(3)\n" \
  "v_mfma_f32_16x16x32_bf16 v[56:59], v[36:39], v[36:39], 0\n" \
  "ds_read_b128 v[52:55], v10 offset:4096\n" \
  "s_waitcnt lgkmcnt(3)\n" \
  "v_mfma_f32_16x16x32_bf16 v[12:15], v[40:43], v[192:195], 0\n" \
  "ds_read_b128 v[36:39], v7 offset:4096\n" \
  "s_waitcnt lgkmcnt(3)\n" \
  "v_mfma_f32_16x16x32_bf16 v[56:59], v[44:47], v[44:47], v[56:59]\n" \
  "ds_read_b128 v[40:43], v10 offset:6144\n" \
  "s_waitcnt lgkmcnt(3)\n" \
  "v_mfma_f32_16x16x32_bf16 v[12:15], v[48:51], v[196:199], v[12:15]\n" \
  "ds_read_b128 v[44:47], v7 offset:6144\n" \
  "s_waitcnt lgkmcnt(3)\n" \
  "v_mfma_f32_16x16x32_bf16 v[56:59], v[52:55], v[52:55], v[56:59]\n" \
  "ds_read_b128 v[48:51], v10 offset:8192\n" \
  "s_waitcnt lgkmcnt(3)\n" \
  "v_mfma_f32_16x16x32_bf16 v[12:15], v[36:39], v[200:203], v[12:15]\n" \
  "ds_read_b128 v[52:55], v7 offset:8192\n" \
  "s_waitcnt lgkmcnt(3)\n" \
  "v_mfma_f32_16x16x32_bf16 v[56:59], v[40:43], v[40:43], v[56:59]\n" \
  "ds_read_b128 v[36:39], v10 offset:10240\n" \
  "s_waitcnt lgkmcnt(3)\n" \
  "v_mfma_f32_16x16x32_bf16 v[12:15], v[44:47], v[204:207], v[12:15]\n" \
  "ds_read_b128 v[40:43], v7 offset:10240\n" \
  "s_waitcnt lgkmcnt(3)\n" \
  "v_mfma_f32_16x16x32_bf16 v[56:59], v[48:51], v[48:51], v[56:59]\n" \
  "ds_read_b128 v[44:47], v10 offset:12288\n" \
  "s_waitcnt lgkmcnt(3)\n" \
  "v_mfma_f32_16x16x32_bf16 v[12:15], v[52:55], v[208:211], v[12:15]\n" \
  "ds_read_b128 v[48:51], v7 offset:12288\n" \
  "s_waitcnt lgkmcnt(3)\n" \
  "v_mfma_f32_16x16x32_bf16 v[56:59], v[36:39], v[36:39], v[56:59]\n" \
  "ds_read_b128 v[52:55], v10 offset:14336\n" \
  "s_waitcnt lgkmcnt(3)\n" \
  "v_mfma_f32_16x16x32_bf16 v[12:15], v[40:43], v[212:215], v[12:15]\n" \
  "ds_read_b128 v[36:39], v7 offset:14336\n" \
  "s_waitcnt lgkmcnt(3)\n" \
  "v_mfma_f32_16x16x32_bf16 v[56:59], v[44:47], v[44:47], v[56:59]\n" \
  "ds_read_b128 v[40:43], v10 offset:16384\n" \
  "s_waitcnt lgkmcnt(3)\n" \
  "v_mfma_f32_16x16x32_bf16 v[12:15], v[48:51], v[216:219], v[12:15]\n" \
  "ds_read_b128 v[44:47], v7 offset:16384\n" \
  "s_waitcnt lgkmcnt(3)\n" \
  "v_mfma_f32_16x16x32_bf16 v[56:59], v[52:55], v[52:55], v[56:59]\n" \
  "ds_read_b128 v[48:51], v10 offset:18432\n" \
  "s_waitcnt lgkmcnt(3)\n" \
  "v_mfma_f32_16x16x32_bf16 v[12:15], v[36:39], v[220:223], v[12:15]\n" \
  "ds_read_b128 v[52:55], v7 offset:18432\n" \
  "s_waitcnt lgkmcnt(3)\n" \
  "v_mfma_f32_16x16x32_bf16 v[56:59], v[40:43], v[40:43], v[56:59]\n" \
  "ds_read_b128 v[36:39], v10 offset:20480\n" \
  "s_waitcnt lgkmcnt(3)\n" \
  "v_mfma_f32_16x16x32_bf16 v[12:15], v[44:47], v[224:227], v[12:15]\n" \
  "ds_read_b128 v[40:43], v7 offset:20480\n" \
  "s_waitcnt lgkmcnt(3)\n" \
  "v_mfma_f32_16x16x32_bf16 v[56:59], v[48:51], v[48:51], v[56:59]\n" \
  "ds_read_b128 v[44:47], v10 offset:22528\n" \
  "s_waitcnt lgkmcnt(3)\n" \
  "v_mfma_f32_16x16x32_bf16 v[12:15], v[52:55], v[228:231], v[12:15]\n" \
  "ds_read_b128 v[48:51], v7 offset:22528\n" \
  "s_waitcnt lgkmcnt(3)\n" \
  "v_mfma_f32_16x16x32_bf16 v[56:59], v[36:39], v[36:39], v[56:59]\n" \
  "ds_read_b128 v[52:55], v10 offset:24576\n" \
  "s_waitcnt lgkmcnt(3)\n" \
  "v_mfma_f32_16x16x32_bf16 v[12:15], v[40:43], v[232:235], v[12:15]\n" \
  "ds_read_b128 v[36:39], v7 offset:24576\n" \
  "s_waitcnt lgkmcnt(3)\n" \
  "v_mfma_f32_16x16x32_bf16 v[56:59], v[44:47], v[44:47], v[56:59]\n" \
  "ds_read_b128 v[40:43], v10 offset:26624\n" \
  "s_waitcnt lgkmcnt(3)\n" \
  "v_mfma_f32_16x16x32_bf16 v[12:15], v[48:51], v[236:239], v[12:15]\n" \
  "ds_read_b128 v[44:47], v7 offset:26624\n" \
  "s_waitcnt lgkmcnt(3)\n" \
  "v_mfma_f32_16x16x32_bf16 v[56:59], v[52:55], v[52:55], v[56:59]\n" \
  "ds_read_b128 v[48:51], v10 offset:28672\n" \
  "s_waitcnt lgkmcnt(3)\n" \
  "v_mfma_f32_16x16x32_bf16 v[12:15], v[36:39], v[240:243], v[12:15]\n" \
  "ds_read_b128 v[52:55], v7 offset:28672\n" \
  "s_waitcnt lgkmcnt(3)\n" \
  "v_mfma_f32_16x16x32_bf16 v[56:59], v[40:43], v[40:43], v[56:59]\n" \
  "ds_read_b128 v[36:39], v10 offset:30720\n" \
  "s_waitcnt lgkmcnt(3)\n" \
  "v_mfma_f32_16x16x32_bf16 v[12:15], v[44:47], v[244:247], v[12:15]\n" \
  "ds_read_b128 v[40:43], v7 offset:30720\n" \
  "s_waitcnt lgkmcnt(3)\n" \
  "v_mfma_f32_16x16x32_bf16 v[56:59], v[48:51], v[48:51], v[56:59]\n" \
  "ds_read_b128 v[44:47], v7 offset:256\n" \
  "s_waitcnt lgkmcnt(3)\n" \
  "v_mfma_f32_16x16x32_bf16 v[12:15], v[52:55], v[248:251], v[12:15]\n" \
  "ds_read_b128 v[48:51], v7 offset:2304\n" \
  "s_waitcnt lgkmcnt(3)\n" \
  "v_mfma_f32_16x16x32_bf16 v[56:59], v[36:39], v[36:39], v[56:59]\n" \
  "ds_read_b128 v[52:55], v7 offset:4352\n" \
  "s_waitcnt lgkmcnt(3)\n" \
  "v_mfma_f32_16x16x32_bf16 v[12:15], v[40:43], v[252:255], v[12:15]\n" \
  "ds_read_b128 v[36:39], v7 offset:6400\n" \
  "s_waitcnt lgkmcnt(3)\n" \
  "v_mfma_f32_16x16x32_bf16 v[16:19], v[44:47], v[192:195], 0\n" \
  "ds_read_b128 v[40:43], v7 offset:8448\n" \
  "s_waitcnt lgkmcnt(3)\n" \
  "v_mfma_f32_16x16x32_bf16 v[16:19], v[48:51], v[196:199], v[16:19]\n" \
  "ds_read_b128 v[44:47], v7 offset:10496\n" \
  "s_waitcnt lgkmcnt(3)\n" \
  "v_mfma_f32_16x16x32_bf16 v[16:19], v[52:55], v[200:203], v[16:19]\n" \
  "ds_read_b128 v[48:51], v7 offset:12544\n" \
  "s_waitcnt lgkmcnt(3)\n" \
  "v_mfma_f32_16x16x32_bf16 v[16:19], v[36:39], v[204:207], v[16:19]\n" \
  "ds_read_b128 v[52:55], v7 offset:14592\n" \
  "s_waitcnt lgkmcnt(3)\n" \
  "v_mfma_f32_16x16x32_bf16 v[16:19], v[40:43], v[208:211], v[16:19]\n" \
  "ds_read_b128 v[36:39], v7 offset:16640\n" \
  "s_waitcnt lgkmcnt(3)\n" \
  "v_mfma_f32_16x16x32_bf16 v[16:19], v[44:47], v[212:215], v[16:19]\n" \
  "ds_read_b128 v[40:43], v7 offset:18688\n" \
  "s_waitcnt lgkmcnt(3)\n" \
  "v_mfma_f32_16x16x32_bf16 v[16:19], v[48:51], v[216:219], v[16:19]\n" \
  "ds_read_b128 v[44:47], v7 offset:20736\n" \
  "s_waitcnt lgkmcnt(3)\n" \
  "v_mfma_f32_16x16x32_bf16 v[16:19], v[52:55], v[220:223], v[16:19]\n" \
  "ds_read_b128 v[48:51], v7 offset:22784\n" \
  "s_waitcnt lgkmcnt(3)\n" \
  "v_mfma_f32_16x16x32_bf16 v[16:19], v[36:39], v[224:227], v[16:19]\n" \
  "ds_read_b128 v[52:55], v7 offset:24832\n" \
  "s_waitcnt lgkmcnt(3)\n" \
  "v_mfma_f32_16x16x32_bf16 v[16:19], v[40:43], v[228:231], v[16:19]\n" \
  "ds_read_b128 v[36:39], v7 offset:26880\n" \
  "s_waitcnt lgkmcnt(3)\n" \
  "v_mfma_f32_16x16x32_bf16 v[16:19], v[44:47], v[232:235], v[16:19]\n" \
  "ds_read_b128 v[40:43], v7 offset:28928\n" \
  "s_waitcnt lgkmcnt(3)\n" \
  "v_mfma_f32_16x16x32_bf16 v[16:19], v[48:51], v[236:239], v[16:19]\n" \
  "ds_read_b128 v[44:47], v7 offset:30976\n" \
  "s_waitcnt lgkmcnt(3)\n" \
  "v_mfma_f32_16x16x32_bf16 v[16:19], v[52:55], v[240:243], v[16:19]\n" \
  "ds_read_b128 v[48:51], v7 offset:0\n" \
  "s_waitcnt lgkmcnt(3)\n" \
  "v_mfma_f32_16x16x32_bf16 v[16:19], v[36:39], v[244:247], v[16:19]\n" \
  "ds_read_b128 v[52:55], v7 offset:2048\n" \
  "s_waitcnt lgkmcnt(3)\n" \
  "v_mfma_f32_16x16x32_bf16 v[16:19], v[40:43], v[248:251], v[16:19]\n" \
  "ds_read_b128 v[36:39], v7 offset:4096\n" \
  "s_waitcnt lgkmcnt(3)\n" \
  "v_mfma_f32_16x16x32_bf16 v[16:19], v[44:47], v[252:255], v[16:19]\n" \
  "ds_read_b128 v[40:43], v7 offset:6144\n" \
  "s_setprio 0\n" \
  "v_cndmask_b32 v56, v56, v57, s[70:71]\n" \
  "v_cndmask_b32 v57, v58, v59, s[70:71]\n" \
  "v_cndmask_b32 v56, v56, v57, s[72:73]\n" \
  "v_mov_b32 v57, v56\n" \
  "v_mov_b32 v58, v56\n" \
  "v_mul_f32 v56, 0x3b000000, v56\n" \
  "v_xor_b32 v10, v11, v10\n" \
  "v_permlane32_swap_b32 v57, v58\n" \
  "v_mul_f32 v57, 0xbb000000, v58\n" \
  "v_fma_f32 v56, -v57, v57, v56\n" \
  "v_max_f32 v56, 0, v56\n" \
  "v_cvt_pk_bf16_f32 v56, v56, v56\n" \
  "v_lshlrev_b32 v56, 16, v56\n" \
  "v_add_f32 v56, 0x3727c5ac, v56\n" \
  "v_cvt_pk_bf16_f32 v56, v56, v56\n" \
  "v_lshlrev_b32 v56, 16, v56\n" \
  "v_sqrt_f32 v56, v56\n" \
  "v_cvt_pk_bf16_f32 v57, v57, v57\n" \
  "v_cvt_pk_bf16_f32 v56, v56, v56\n" \
  "v_lshlrev_b32 v56, 16, v56\n" \
  "v_rcp_f32 v56, v56\n" \
  "s_mov_b64 exec, 0xf0000f\n" \
  "ds_write2_b32 v62, v57, v56 offset1:32\n" \
  "s_mov_b64 exec, -1\n" \
  "s_cmp_eq_u32 s66, 63\n" \
  "s_cbranch_scc1 .LQB_NOE_%=\n" \
  "v_mul_f32 v20, v20, v60\n" \
  "v_mul_f32 v21, v21, v60\n" \
  "v_mul_f32 v22, v22, v60\n" \
  "v_cvt_pk_bf16_f32 v20, v20, v21\n" \
  "v_mul_f32 v23, v23, v60\n" \
  "v_mul_f32 v24, v24, v60\n" \
  "v_cvt_pk_bf16_f32 v21, v22, v23\n" \
  "v_mul_f32 v25, v25, v60\n" \
  "v_mul_f32 v26, v26, v60\n" \
  "v_cvt_pk_bf16_f32 v22, v24, v25\n" \
  "v_mul_f32 v27, v27, v60\n" \
  "v_cvt_pk_bf16_f32 v23, v26, v27\n" \
  "buffer_store_dwordx4 v[20:23], v6, s[40:43], s59 offen offset:0 sc1\n" \
  "s_add_u32 s76, s59, 4096\n" \
  "v_mul_f32 v28, v28, v61\n" \
  "v_mul_f32 v29, v29, v61\n" \
  "v_mul_f32 v30, v30, v61\n" \
  "v_cvt_pk_bf16_f32 v28, v28, v29\n" \
  "v_mul_f32 v31, v31, v61\n" \
  "v_mul_f32 v32, v32, v61\n" \
  "v_cvt_pk_bf16_f32 v29, v30, v31\n" \
  "v_mul_f32 v33, v33, v61\n" \
  "v_mul_f32 v34, v34, v61\n" \
  "v_cvt_pk_bf16_f32 v30, v32, v33\n" \
  "v_mul_f32 v35, v35, v61\n" \
  "v_cvt_pk_bf16_f32 v31, v34, v35\n" \
  "buffer_store_dwordx4 v[28:31], v6, s[40:43], s76 offen offset:0 sc1\n" \
  "s_add_u32 s59, s59, 0x80000\n" \
  ".LQB_NOE_%=:\n" \
  "s_waitcnt lgkmcnt(0)\n" \
  "s_barrier\n" \
  "ds_read2_b32 v[56:57], v8 offset0:0 offset1:0\n" \
  "ds_read2_b32 v[58:59], v8 offset0:16 offset1:16\n" \
  "ds_read_b128 v[28:31], v9 offset:128\n" \
  "ds_read_b128 v[32:35], v9 offset:192\n" \
  "ds_read2_b32 v[60:61], v8 offset0:32 offset1:48\n" \
  "s_setprio 3\n" \
  "s_waitcnt lgkmcnt(3)\n" \
  "v_mfma_f32_16x16x16_bf16 v[12:15], v[56:57], v[4:5], v[12:15]\n" \
  "v_mfma_f32_16x16x16_bf16 v[16:19], v[58:59], v[4:5], v[16:19]\n" \
  "s_setprio 0\n" \
  "s_waitcnt lgkmcnt(0)\n" \
  "s_nop 4\n" \
  "v_mul_f32 v12, v12, v28\n" \
  "v_mul_f32 v13, v13, v29\n" \
  "v_mul_f32 v14, v14, v30\n" \
  "v_mul_f32 v15, v15, v31\n" \
  "v_cvt_pk_bf16_f32 v12, v12, v13\n" \
  "v_cvt_pk_bf16_f32 v13, v14, v15\n" \
  "v_mul_f32 v16, v16, v32\n" \
  "v_mul_f32 v17, v17, v33\n" \
  "v_mul_f32 v18, v18, v34\n" \
  "v_mul_f32 v19, v19, v35\n" \
  "v_cvt_pk_bf16_f32 v14, v16, v17\n" \
  "v_cvt_pk_bf16_f32 v15, v18, v19\n" \
  "buffer_store_dwordx4 v[12:15], v6, s[44:47], s60 offen offset:0 sc1\n" \
  "v_mov_b64 v[16:17], 0\n" \
  "v_lshrrev_b32 v18, 1, v6\n" \
  "buffer_store_dwordx2 v[16:17], v18, s[48:51], s61 offen sc1\n" \
  "s_setprio 0\n" \
  "s_add_u32 s60, s60, 0x40000\n" \
  "s_add_u32 s61, s61, 0x10000\n" \
  "v_mov_b32 v12, s80\n" \
  "v_mov_b32 v13, 1\n" \
  "v_mfma_f32_16x16x16_bf16 v[20:23], v[0:1], v[56:57], 0\n" \
  "v_mfma_f32_16x16x16_bf16 v[24:27], v[2:3], v[56:57], 0\n" \
  "v_mfma_f32_16x16x16_bf16 v[28:31], v[0:1], v[58:59], 0\n" \
  "v_mfma_f32_16x16x16_bf16 v[32:35], v[2:3], v[58:59], 0\n" \
  "v_mfma_f32_16x16x32_bf16 v[20:23], v[64:67], v[48:51], v[20:23]\n" \
  "ds_read_b128 v[44:47], v7 offset:8192\n" \
  "v_mfma_f32_16x16x32_bf16 v[24:27], v[128:131], v[48:51], v[24:27]\n" \
  "v_mfma_f32_16x16x32_bf16 v[20:23], v[68:71], v[52:55], v[20:23]\n" \
  "ds_read_b128 v[48:51], v7 offset:10240\n" \
  "v_mfma_f32_16x16x32_bf16 v[24:27], v[132:135], v[52:55], v[24:27]\n" \
  "v_mfma_f32_16x16x32_bf16 v[20:23], v[72:75], v[36:39], v[20:23]\n" \
  "ds_read_b128 v[52:55], v7 offset:12288\n" \
  "v_mfma_f32_16x16x32_bf16 v[24:27], v[136:139], v[36:39], v[24:27]\n" \
  "v_mfma_f32_16x16x32_bf16 v[20:23], v[76:79], v[40:43], v[20:23]\n" \
  "ds_read_b128 v[36:39], v7 offset:14336\n" \
  "v_mfma_f32_16x16x32_bf16 v[24:27], v[140:143], v[40:43], v[24:27]\n" \
  "s_waitcnt lgkmcnt(3)\n" \
  "v_mfma_f32_16x16x32_bf16 v[20:23], v[80:83], v[44:47], v[20:23]\n" \
  "ds_read_b128 v[40:43], v7 offset:16384\n" \
  "v_mfma_f32_16x16x32_bf16 v[24:27], v[144:147], v[44:47], v[24:27]\n" \
  "s_waitcnt lgkmcnt(3)\n" \
  "v_mfma_f32_16x16x32_bf16 v[20:23], v[84:87], v[48:51], v[20:23]\n" \
  "ds_read_b128 v[44:47], v7 offset:18432\n" \
  "v_mfma_f32_16x16x32_bf16 v[24:27], v[148:151], v[48:51], v[24:27]\n" \
  "s_waitcnt lgkmcnt(3)\n" \
  "v_mfma_f32_16x16x32_bf16 v[20:23], v[88:91], v[52:55], v[20:23]\n" \
  "ds_read_b128 v[48:51], v7 offset:20480\n" \
  "s_waitcnt vmcnt(0)\n" \
  "s_mov_b64 exec, 1\n" \
  "ds_add_rtn_u32 v14, v12, v13\n" \
  "s_mov_b64 exec, -1\n" \
  "v_mfma_f32_16x16x32_bf16 v[24:27], v[152:155], v[52:55], v[24:27]\n" \
  "s_waitcnt lgkmcnt(4)\n" \
  "v_mfma_f32_16x16x32_bf16 v[20:23], v[92:95], v[36:39], v[20:23]\n" \
  "ds_read_b128 v[52:55], v7 offset:22528\n" \
  "v_mfma_f32_16x16x32_bf16 v[24:27], v[156:159], v[36:39], v[24:27]\n" \
  "s_waitcnt lgkmcnt(4)\n" \
  "v_mfma_f32_16x16x32_bf16 v[20:23], v[96:99], v[40:43], v[20:23]\n" \
  "ds_read_b128 v[36:39], v7 offset:24576\n" \
  "s_waitcnt lgkmcnt(2)\n" \
  "v_mfma_f32_16x16x32_bf16 v[24:27], v[160:163], v[40:43], v[24:27]\n" \
  "v_readfirstlane_b32 s74, v14\n" \
  "s_and_b32 s74, s74, 3\n" \
  "s_cmp_eq_u32 s74, 3\n" \
  "s_cbranch_scc0 4\n" \
  "s_mov_b64 exec, 1\n" \
  "global_store_dword v16, v13, s[64:65] offset:64 sc1\n" \
  "s_mov_b64 exec, -1\n" \
  "v_mfma_f32_16x16x32_bf16 v[20:23], v[100:103], v[44:47], v[20:23]\n" \
  "ds_read_b128 v[40:43], v7 offset:26624\n" \
  "s_add_u32 s64, s64, 4096\n" \
  "s_addc_u32 s65, s65, 0\n" \
  "v_mfma_f32_16x16x32_bf16 v[24:27], v[164:167], v[44:47], v[24:27]\n" \
  "s_sub_u32 s66, s66, 1\n" \
  "v_mfma_f32_16x16x32_bf16 v[20:23], v[104:107], v[48:51], v[20:23]\n" \
  "ds_read_b128 v[44:47], v7 offset:28672\n" \
  "v_mfma_f32_16x16x32_bf16 v[24:27], v[168:171], v[48:51], v[24:27]\n" \
  "s_waitcnt lgkmcnt(3)\n" \
  "v_mfma_f32_16x16x32_bf16 v[20:23], v[108:111], v[52:55], v[20:23]\n" \
  "ds_read_b128 v[48:51], v7 offset:30720\n" \
  "v_mfma_f32_16x16x32_bf16 v[24:27], v[172:175], v[52:55], v[24:27]\n" \
  "s_waitcnt lgkmcnt(3)\n" \
  "v_mfma_f32_16x16x32_bf16 v[20:23], v[112:115], v[36:39], v[20:23]\n" \
  "ds_read_b128 v[52:55], v7 offset:256\n" \
  "v_mfma_f32_16x16x32_bf16 v[24:27], v[176:179], v[36:39], v[24:27]\n" \
  "s_waitcnt lgkmcnt(3)\n" \
  "v_mfma_f32_16x16x32_bf16 v[20:23], v[116:119], v[40:43], v[20:23]\n" \
  "ds_read_b128 v[36:39], v7 offset:2304\n" \
  "v_mfma_f32_16x16x32_bf16 v[24:27], v[180:183], v[40:43], v[24:27]\n" \
  "s_waitcnt lgkmcnt(3)\n" \
  "v_mfma_f32_16x16x32_bf16 v[20:23], v[120:123], v[44:47], v[20:23]\n" \
  "ds_read_b128 v[40:43], v7 offset:4352\n" \
  "v_mfma_f32_16x16x32_bf16 v[24:27], v[184:187], v[44:47], v[24:27]\n" \
  "s_waitcnt lgkmcnt(3)\n" \
  "v_mfma_f32_16x16x32_bf16 v[20:23], v[124:127], v[48:51], v[20:23]\n" \
  "ds_read_b128 v[44:47], v7 offset:6400\n" \
  "v_mfma_f32_16x16x32_bf16 v[24:27], v[188:191], v[48:51], v[24:27]\n" \
  "s_waitcnt lgkmcnt(3)\n" \
  "v_mfma_f32_16x16x32_bf16 v[28:31], v[64:67], v[52:55], v[28:31]\n" \
  "ds_read_b128 v[48:51], v7 offset:8448\n" \
  "v_mfma_f32_16x16x32_bf16 v[32:35], v[128:131], v[52:55], v[32:35]\n" \
  "s_waitcnt lgkmcnt(3)\n" \
  "v_mfma_f32_16x16x32_bf16 v[28:31], v[68:71], v[36:39], v[28:31]\n" \
  "ds_read_b128 v[52:55], v7 offset:10496\n" \
  "v_mfma_f32_16x16x32_bf16 v[32:35], v[132:135], v[36:39], v[32:35]\n" \
  "s_waitcnt lgkmcnt(3)\n" \
  "v_mfma_f32_16x16x32_bf16 v[28:31], v[72:75], v[40:43], v[28:31]\n" \
  "ds_read_b128 v[36:39], v7 offset:12544\n" \
  "v_mfma_f32_16x16x32_bf16 v[32:35], v[136:139], v[40:43], v[32:35]\n" \
  "s_waitcnt lgkmcnt(3)\n" \
  "v_mfma_f32_16x16x32_bf16 v[28:31], v[76:79], v[44:47], v[28:31]\n" \
  "ds_read_b128 v[40:43], v7 offset:14592\n" \
  "v_mfma_f32_16x16x32_bf16 v[32:35], v[140:143], v[44:47], v[32:35]\n" \
  "s_waitcnt lgkmcnt(3)\n" \
  "v_mfma_f32_16x16x32_bf16 v[28:31], v[80:83], v[48:51], v[28:31]\n" \
  "ds_read_b128 v[44:47], v7 offset:16640\n" \
  "v_mfma_f32_16x16x32_bf16 v[32:35], v[144:147], v[48:51], v[32:35]\n" \
  "s_waitcnt lgkmcnt(3)\n" \
  "v_mfma_f32_16x16x32_bf16 v[28:31], v[84:87], v[52:55], v[28:31]\n" \
  "ds_read_b128 v[48:51], v7 offset:18688\n" \
  "v_mfma_f32_16x16x32_bf16 v[32:35], v[148:151], v[52:55], v[32:35]\n" \
  "s_waitcnt lgkmcnt(3)\n" \
  "v_mfma_f32_16x16x32_bf16 v[28:31], v[88:91], v[36:39], v[28:31]\n" \
  "ds_read_b128 v[52:55], v7 offset:20736\n" \
  "v_mfma_f32_16x16x32_bf16 v[32:35], v[152:155], v[36:39], v[32:35]\n" \
  "s_waitcnt lgkmcnt(3)\n" \
  "v_mfma_f32_16x16x32_bf16 v[28:31], v[92:95], v[40:43], v[28:31]\n" \
  "ds_read_b128 v[36:39], v7 offset:22784\n" \
  "v_mfma_f32_16x16x32_bf16 v[32:35], v[156:159], v[40:43], v[32:35]\n" \
  "s_waitcnt lgkmcnt(3)\n" \
  "v_mfma_f32_16x16x32_bf16 v[28:31], v[96:99], v[44:47], v[28:31]\n" \
  "ds_read_b128 v[40:43], v7 offset:24832\n" \
  "v_mfma_f32_16x16x32_bf16 v[32:35], v[160:163], v[44:47], v[32:35]\n" \
  "s_waitcnt lgkmcnt(3)\n" \
  "v_mfma_f32_16x16x32_bf16 v[28:31], v[100:103], v[48:51], v[28:31]\n" \
  "ds_read_b128 v[44:47], v7 offset:26880\n" \
  "v_mfma_f32_16x16x32_bf16 v[32:35], v[164:167], v[48:51], v[32:35]\n" \
  "s_waitcnt lgkmcnt(3)\n" \
  "v_mfma_f32_16x16x32_bf16 v[28:31], v[104:107], v[52:55], v[28:31]\n" \
  "ds_read_b128 v[48:51], v7 offset:28928\n" \
  "v_mfma_f32_16x16x32_bf16 v[32:35], v[168:171], v[52:55], v[32:35]\n" \
  "s_waitcnt lgkmcnt(3)\n" \
  "v_mfma_f32_16x16x32_bf16 v[28:31], v[108:111], v[36:39], v[28:31]\n" \
  "ds_read_b128 v[52:55], v7 offset:30976\n" \
  "v_mfma_f32_16x16x32_bf16 v[32:35], v[172:175], v[36:39], v[32:35]\n" \
  "s_waitcnt lgkmcnt(3)\n" \
  "v_mfma_f32_16x16x32_bf16 v[28:31], v[112:115], v[40:43], v[28:31]\n" \
  "v_mfma_f32_16x16x32_bf16 v[32:35], v[176:179], v[40:43], v[32:35]\n" \
  "v_xor_b32 v7, 0x20000, v7\n" \
  "s_waitcnt lgkmcnt(0)\n" \
  "s_barrier\n" \
  "ds_read_b128 v[36:39], v10 offset:0\n" \
  "ds_read_b128 v[40:43], v7 offset:0\n" \
  "v_mfma_f32_16x16x32_bf16 v[28:31], v[116:119], v[44:47], v[28:31]\n" \
  "v_mfma_f32_16x16x32_bf16 v[32:35], v[180:183], v[44:47], v[32:35]\n" \
  "ds_read_b128 v[44:47], v10 offset:2048\n" \
  "v_mfma_f32_16x16x32_bf16 v[28:31], v[120:123], v[48:51], v[28:31]\n" \
  "v_mfma_f32_16x16x32_bf16 v[32:35], v[184:187], v[48:51], v[32:35]\n" \
  "ds_read_b128 v[48:51], v7 offset:2048\n" \
  "v_mfma_f32_16x16x32_bf16 v[28:31], v[124:127], v[52:55], v[28:31]\n" \
  "v_mfma_f32_16x16x32_bf16 v[32:35], v[188:191], v[52:55], v[32:35]\n" \
  "s_cbranch_scc1 .LQB_LAST_%=\n" \
  "s_branch .LQB_TOP_%=\n" \
  ".LQB_LAST_%=:\n" \
  "s_nop 7\n" \
  "v_mul_f32 v20, v20, v60\n" \
  "v_mul_f32 v21, v21, v60\n" \
  "v_mul_f32 v22, v22, v60\n" \
  "v_cvt_pk_bf16_f32 v20, v20, v21\n" \
  "v_mul_f32 v23, v23, v60\n" \
  "v_mul_f32 v24, v24, v60\n" \
  "v_cvt_pk_bf16_f32 v21, v22, v23\n" \
  "v_mul_f32 v25, v25, v60\n" \
  "v_mul_f32 v26, v26, v60\n" \
  "v_cvt_pk_bf16_f32 v22, v24, v25\n" \
  "v_mul_f32 v27, v27, v60\n" \
  "v_cvt_pk_bf16_f32 v23, v26, v27\n" \
  "buffer_store_dwordx4 v[20:23], v6, s[40:43], s59 offen offset:0 sc1\n" \
  "s_add_u32 s76, s59, 4096\n" \
  "v_mul_f32 v28, v28, v61\n" \
  "v_mul_f32 v29, v29, v61\n" \
  "v_mul_f32 v30, v30, v61\n" \
  "v_cvt_pk_bf16_f32 v28, v28, v29\n" \
  "v_mul_f32 v31, v31, v61\n" \
  "v_mul_f32 v32, v32, v61\n" \
  "v_cvt_pk_bf16_f32 v29, v30, v31\n" \
  "v_mul_f32 v33, v33, v61\n" \
  "v_mul_f32 v34, v34, v61\n" \
  "v_cvt_pk_bf16_f32 v30, v32, v33\n" \
  "v_mul_f32 v35, v35, v61\n" \
  "v_cvt_pk_bf16_f32 v31, v34, v35\n" \
  "buffer_store_dwordx4 v[28:31], v6, s[40:43], s76 offen offset:0 sc1\n" \
  "s_add_u32 s59, s59, 0x80000\n" \
  "s_waitcnt vmcnt(0)\n" \
  "s_mov_b64 exec, 1\n" \
  "ds_add_rtn_u32 v14, v12, v13\n" \
  "s_mov_b64 exec, -1\n" \
  "s_waitcnt lgkmcnt(0)\n" \
  "v_readfirstlane_b32 s74, v14\n" \
  "s_and_b32 s74, s74, 3\n" \
  "s_cmp_eq_u32 s74, 3\n" \
  "s_cbranch_scc0 4\n" \
  "s_mov_b64 exec, 1\n" \
  "global_store_dword v16, v13, s[64:65] offset:64 sc1\n" \
  "s_mov_b64 exec, -1\n" \
  "s_endpgm\n"
#define AW_ASM_0 \
  "v_add_u32 v17, s61, v1\n" \
  "ds_write_b128 v17, v[8:11]\n" \
  "ds_write_b128 v17, v[12:15] offset:1024\n" \
  ".p2align 6\n" \
  ".LWA_AL_%=:\n" \
  ".LWA_TOP_%=:\n" \
  "v_mov_b32 v16, s65\n" \
  "v_add_u32 v17, s61, v1\n" \
  "s_add_u32 s64, s64, 2\n" \
  "s_mov_b32 s77, 0x1000000\n" \
  "s_branch .LWA_LAND_%=\n" \
  ".LWA_WAIT_%=:\n" \
  "s_setprio 0\n" \
  "s_sleep 1\n" \
  "s_sub_u32 s77, s77, 1\n" \
  "s_cbranch_scc1 .LWA_GO_%=\n" \
  ".LWA_LAND_%=:\n" \
  "ds_read_b32 v18, v16\n" \
  "s_waitcnt lgkmcnt(0)\n" \
  "v_readfirstlane_b32 s68, v18\n" \
  "s_cmp_lt_u32 s68, s64\n" \
  "s_cbranch_scc1 .LWA_WAIT_%=\n" \
  ".LWA_GO_%=:\n" \
  "ds_read_b128 v[64:67], v17 offset:0\n" \
  "ds_read_b128 v[68:71], v17 offset:1024\n" \
  "s_setprio 3\n" \
  "ds_read_b128 v[16:19], v4 offset:0\n" \
  "ds_read_b128 v[32:35], v3 offset:8192\n" \
  "ds_read_b128 v[48:51], v3 offset:12288\n" \
  "ds_read_b128 v[20:23], v4 offset:1024\n" \
  "ds_read_b128 v[36:39], v3 offset:9216\n" \
  "ds_read_b128 v[52:55], v3 offset:13312\n" \
  "ds_read_b128 v[24:27], v4 offset:2048\n" \
  "ds_read_b128 v[40:43], v3 offset:10240\n" \
  "ds_read_b128 v[56:59], v3 offset:14336\n" \
  "ds_read_b128 v[28:31], v4 offset:3072\n" \
  "ds_read_b128 v[44:47], v3 offset:11264\n" \
  "ds_read_b128 v[60:63], v3 offset:15360\n" \
  "s_waitcnt lgkmcnt(10)\n" \
  "v_mfma_f32_16x16x32_bf16 v[64:67], v[32:35], v[16:19], v[64:67]\n" \
  "ds_read_b128 v[72:75], v3 offset:16384\n" \
  "s_waitcnt lgkmcnt(10)\n" \
  "v_mfma_f32_16x16x32_bf16 v[68:71], v[48:51], v[16:19], v[68:71]\n" \
  "ds_read_b128 v[76:79], v3 offset:17408\n" \
  "s_waitcnt lgkmcnt(9)\n" \
  "v_mfma_f32_16x16x32_bf16 v[64:67], v[36:39], v[20:23], v[64:67]\n" \
  "ds_read_b128 v[80:83], v3 offset:18432\n" \
  "s_waitcnt lgkmcnt(9)\n" \
  "v_mfma_f32_16x16x32_bf16 v[68:71], v[52:55], v[20:23], v[68:71]\n" \
  "ds_read_b128 v[84:87], v3 offset:19456\n" \
  "s_waitcnt lgkmcnt(8)\n" \
  "v_mfma_f32_16x16x32_bf16 v[64:67], v[40:43], v[24:27], v[64:67]\n" \
  "ds_read_b128 v[88:91], v3 offset:20480\n" \
  "s_waitcnt lgkmcnt(8)\n" \
  "v_mfma_f32_16x16x32_bf16 v[68:71], v[56:59], v[24:27], v[68:71]\n" \
  "ds_read_b128 v[92:95], v3 offset:21504\n" \
  "s_waitcnt lgkmcnt(7)\n" \
  "v_mfma_f32_16x16x32_bf16 v[64:67], v[44:47], v[28:31], v[64:67]\n" \
  "ds_read_b128 v[96:99], v3 offset:22528\n" \
  "s_waitcnt lgkmcnt(7)\n" \
  "v_mfma_f32_16x16x32_bf16 v[68:71], v[60:63], v[28:31], v[68:71]\n" \
  "ds_read_b128 v[100:103], v3 offset:23552\n" \
  "s_add_u32 s56, s56, 3\n" \
  "s_sub_u32 s68, s56, %[wd]\n" \
  "s_bitcmp1_b32 s68, 0\n" \
  "s_cselect_b32 s58, 64, %[wd]\n" \
  "v_maximum3_f32 v10, v64, v65, v66\n" \
  "s_lshr_b32 s70, s68, 1\n" \
  "s_add_u32 s58, s58, s70\n" \
  "v_maximum3_f32 v10, v10, v67, v68\n" \
  "v_maximum3_f32 v10, v10, v69, v70\n" \
  "v_max_f32 v8, v10, v71\n" \
  "v_mov_b32 v9, v8\n" \
  "s_cmp_lt_u32 s68, %[w2]\n" \
  "s_cselect_b32 s58, s58, s56\n" \
  "v_permlane16_swap_b32 v8, v9\n" \
  "v_max_f32 v8, v8, v9\n" \
  "v_mov_b32 v9, v8\n" \
  "s_min_u32 s58, s58, 128\n" \
  "s_nop 0\n" \
  "v_permlane32_swap_b32 v8, v9\n" \
  "v_max_f32 v8, v8, v9\n" \
  "v_mul_f32 v10, s80, v8\n" \
  "v_pk_fma_f32 v[64:65], v[64:65], s[80:81], v[10:11] op_sel_hi:[1,0,0] neg_lo:[0,0,1] neg_hi:[0,0,1]\n" \
  "v_pk_fma_f32 v[66:67], v[66:67], s[80:81], v[10:11] op_sel_hi:[1,0,0] neg_lo:[0,0,1] neg_hi:[0,0,1]\n" \
  "v_pk_fma_f32 v[68:69], v[68:69], s[80:81], v[10:11] op_sel_hi:[1,0,0] neg_lo:[0,0,1] neg_hi:[0,0,1]\n" \
  "v_pk_fma_f32 v[70:71], v[70:71], s[80:81], v[10:11] op_sel_hi:[1,0,0] neg_lo:[0,0,1] neg_hi:[0,0,1]\n" \
  "v_exp_f32 v64, v64\n" \
  "v_exp_f32 v65, v65\n" \
  "v_exp_f32 v66, v66\n" \
  "v_exp_f32 v67, v67\n" \
  "v_exp_f32 v68, v68\n" \
  "v_exp_f32 v69, v69\n" \
  "v_exp_f32 v70, v70\n" \
  "v_exp_f32 v71, v71\n" \
  "v_cvt_pk_bf16_f32 v104, v64, v65\n" \
  "v_cvt_pk_bf16_f32 v105, v66, v67\n" \
  "v_cvt_pk_bf16_f32 v106, v68, v69\n" \
  "v_cvt_pk_bf16_f32 v107, v70, v71\n" \
  "v_pk_add_f32 v[64:65], v[64:65], v[66:67]\n" \
  "v_pk_add_f32 v[68:69], v[68:69], v[70:71]\n" \
  "s_waitcnt lgkmcnt(0)\n" \
  "v_pk_add_f32 v[64:65], v[64:65], v[68:69]\n" \
  "v_mfma_f32_16x16x32_bf16 v[32:35], v[72:75], v[104:107], 0\n" \
  "v_mfma_f32_16x16x32_bf16 v[48:51], v[88:91], v[104:107], 0\n" \
  "v_mfma_f32_16x16x32_bf16 v[36:39], v[76:79], v[104:107], 0\n" \
  "v_mfma_f32_16x16x32_bf16 v[52:55], v[92:95], v[104:107], 0\n" \
  "v_mfma_f32_16x16x32_bf16 v[40:43], v[80:83], v[104:107], 0\n" \
  "v_mfma_f32_16x16x32_bf16 v[56:59], v[96:99], v[104:107], 0\n" \
  "v_mfma_f32_16x16x32_bf16 v[44:47], v[84:87], v[104:107], 0\n" \
  "v_mfma_f32_16x16x32_bf16 v[60:63], v[100:103], v[104:107], 0\n" \
  "v_add_f32 v11, v64, v65\n" \
  "s_cmp_lt_u32 s56, %[w3]\n" \
  "s_cselect_b32 s68, s57, 0\n" \
  "s_cmp_ge_u32 s68, 64\n" \
  "s_cbranch_scc1 .LWA_EASY_%=\n" \
  "v_cvt_pk_bf16_f32 v108, v32, v33\n" \
  "v_cvt_pk_bf16_f32 v109, v48, v49\n" \
  "v_cvt_pk_bf16_f32 v110, v34, v35\n" \
  "v_cvt_pk_bf16_f32 v111, v50, v51\n" \
  "ds_write_b128 v7, v[108:111] offset:0\n" \
  "v_cvt_pk_bf16_f32 v112, v36, v37\n" \
  "v_cvt_pk_bf16_f32 v113, v52, v53\n" \
  "v_cvt_pk_bf16_f32 v114, v38, v39\n" \
  "v_cvt_pk_bf16_f32 v115, v54, v55\n" \
  "ds_write_b128 v7, v[112:115] offset:1024\n" \
  "v_cvt_pk_bf16_f32 v116, v40, v41\n" \
  "v_cvt_pk_bf16_f32 v117, v56, v57\n" \
  "v_cvt_pk_bf16_f32 v118, v42, v43\n" \
  "v_cvt_pk_bf16_f32 v119, v58, v59\n" \
  "ds_write_b128 v7, v[116:119] offset:2048\n" \
  "v_cvt_pk_bf16_f32 v120, v44, v45\n" \
  "v_cvt_pk_bf16_f32 v121, v60, v61\n" \
  "v_cvt_pk_bf16_f32 v122, v46, v47\n" \
  "v_cvt_pk_bf16_f32 v123, v62, v63\n" \
  "ds_write_b128 v7, v[120:123] offset:3072\n" \
  "s_waitcnt vmcnt(0)\n" \
  "s_waitcnt lgkmcnt(0)\n" \
  "s_barrier\n" \
  "s_setprio 2\n" \
  "ds_read_b128 v[48:51], v1 offset:0\n" \
  "ds_read_b128 v[52:55], v1 offset:1024\n" \
  "ds_read_b128 v[56:59], v1 offset:2048\n" \
  "s_waitcnt lgkmcnt(2)\n" \
  "v_mfma_f32_16x16x32_bf16 v[64:67], v[128:131], v[48:51], 0\n" \
  "ds_read_b128 v[60:63], v1 offset:3072\n" \
  "s_lshl_b32 s75, s57, 19\n" \
  "v_mfma_f32_16x16x32_bf16 v[68:71], v[160:163], v[48:51], 0\n" \
  "s_add_u32 s75, s75, s60\n" \
  "v_mfma_f32_16x16x32_bf16 v[72:75], v[192:195], v[48:51], 0\n" \
  "s_add_u32 s76, s75, 16384\n" \
  "v_mfma_f32_16x16x32_bf16 v[76:79], v[224:227], v[48:51], 0\n" \
  "s_waitcnt lgkmcnt(2)\n" \
  "v_mfma_f32_16x16x32_bf16 v[64:67], v[132:135], v[52:55], v[64:67]\n" \
  "ds_read_b128 v[48:51], v1 offset:8192\n" \
  "v_mfma_f32_16x16x32_bf16 v[68:71], v[164:167], v[52:55], v[68:71]\n" \
  "v_mfma_f32_16x16x32_bf16 v[72:75], v[196:199], v[52:55], v[72:75]\n" \
  "v_mfma_f32_16x16x32_bf16 v[76:79], v[228:231], v[52:55], v[76:79]\n" \
  "s_waitcnt lgkmcnt(2)\n" \
  "v_mfma_f32_16x16x32_bf16 v[64:67], v[136:139], v[56:59], v[64:67]\n" \
  "ds_read_b128 v[52:55], v1 offset:9216\n" \
  "v_mfma_f32_16x16x32_bf16 v[68:71], v[168:171], v[56:59], v[68:71]\n" \
  "v_mfma_f32_16x16x32_bf16 v[72:75], v[200:203], v[56:59], v[72:75]\n" \
  "v_mfma_f32_16x16x32_bf16 v[76:79], v[232:235], v[56:59], v[76:79]\n" \
  "s_waitcnt lgkmcnt(2)\n" \
  "v_mfma_f32_16x16x32_bf16 v[64:67], v[140:143], v[60:63], v[64:67]\n" \
  "ds_read_b128 v[56:59], v1 offset:10240\n" \
  "v_mfma_f32_16x16x32_bf16 v[68:71], v[172:175], v[60:63], v[68:71]\n" \
  "v_mfma_f32_16x16x32_bf16 v[72:75], v[204:207], v[60:63], v[72:75]\n" \
  "v_mfma_f32_16x16x32_bf16 v[76:79], v[236:239], v[60:63], v[76:79]\n" \
  "s_waitcnt lgkmcnt(2)\n" \
  "v_mfma_f32_16x16x32_bf16 v[96:99], v[144:147], v[48:51], 0\n" \
  "ds_read_b128 v[60:63], v1 offset:11264\n" \
  "v_mfma_f32_16x16x32_bf16 v[100:103], v[176:179], v[48:51], 0\n" \
  "v_mfma_f32_16x16x32_bf16 v[104:107], v[208:211], v[48:51], 0\n" \
  "v_mfma_f32_16x16x32_bf16 v[108:111], v[240:243], v[48:51], 0\n" \
  "s_waitcnt lgkmcnt(2)\n" \
  "v_mfma_f32_16x16x32_bf16 v[96:99], v[148:151], v[52:55], v[96:99]\n" \
  "ds_read_b128 v[48:51], v1 offset:4096\n" \
  "v_mfma_f32_16x16x32_bf16 v[100:103], v[180:183], v[52:55], v[100:103]\n" \
  "v_mfma_f32_16x16x32_bf16 v[104:107], v[212:215], v[52:55], v[104:107]\n" \
  "v_mfma_f32_16x16x32_bf16 v[108:111], v[244:247], v[52:55], v[108:111]\n" \
  "s_waitcnt lgkmcnt(2)\n" \
  "v_mfma_f32_16x16x32_bf16 v[96:99], v[152:155], v[56:59], v[96:99]\n" \
  "ds_read_b128 v[52:55], v1 offset:5120\n" \
  "v_mfma_f32_16x16x32_bf16 v[100:103], v[184:187], v[56:59], v[100:103]\n" \
  "v_mfma_f32_16x16x32_bf16 v[104:107], v[216:219], v[56:59], v[104:107]\n" \
  "v_mfma_f32_16x16x32_bf16 v[108:111], v[248:251], v[56:59], v[108:111]\n" \
  "s_waitcnt lgkmcnt(2)\n" \
  "v_mfma_f32_16x16x32_bf16 v[96:99], v[156:159], v[60:63], v[96:99]\n" \
  "ds_read_b128 v[56:59], v1 offset:6144\n" \
  "v_mfma_f32_16x16x32_bf16 v[100:103], v[188:191], v[60:63], v[100:103]\n" \
  "v_mfma_f32_16x16x32_bf16 v[104:107], v[220:223], v[60:63], v[104:107]\n" \
  "v_mfma_f32_16x16x32_bf16 v[108:111], v[252:255], v[60:63], v[108:111]\n" \
  "s_waitcnt lgkmcnt(2)\n" \
  "v_mfma_f32_16x16x32_bf16 v[80:83], v[128:131], v[48:51], 0\n" \
  "ds_read_b128 v[60:63], v1 offset:7168\n" \
  "v_mfma_f32_16x16x32_bf16 v[84:87], v[160:163], v[48:51], 0\n" \
  "v_mfma_f32_16x16x32_bf16 v[88:91], v[192:195], v[48:51], 0\n" \
  "v_mfma_f32_16x16x32_bf16 v[92:95], v[224:227], v[48:51], 0\n" \
  "s_waitcnt lgkmcnt(2)\n" \
  "v_mfma_f32_16x16x32_bf16 v[80:83], v[132:135], v[52:55], v[80:83]\n" \
  "ds_read_b128 v[48:51], v1 offset:12288\n" \
  "v_mfma_f32_16x16x32_bf16 v[84:87], v[164:167], v[52:55], v[84:87]\n" \
  "v_mfma_f32_16x16x32_bf16 v[88:91], v[196:199], v[52:55], v[88:91]\n" \
  "v_mfma_f32_16x16x32_bf16 v[92:95], v[228:231], v[52:55], v[92:95]\n" \
  "s_waitcnt lgkmcnt(2)\n" \
  "v_mfma_f32_16x16x32_bf16 v[80:83], v[136:139], v[56:59], v[80:83]\n" \
  "ds_read_b128 v[52:55], v1 offset:13312\n" \
  "v_mfma_f32_16x16x32_bf16 v[84:87], v[168:171], v[56:59], v[84:87]\n" \
  "v_mfma_f32_16x16x32_bf16 v[88:91], v[200:203], v[56:59], v[88:91]\n" \
  "v_mfma_f32_16x16x32_bf16 v[92:95], v[232:235], v[56:59], v[92:95]\n" \
  "s_waitcnt lgkmcnt(2)\n" \
  "v_mfma_f32_16x16x32_bf16 v[80:83], v[140:143], v[60:63], v[80:83]\n" \
  "ds_read_b128 v[56:59], v1 offset:14336\n" \
  "v_mfma_f32_16x16x32_bf16 v[84:87], v[172:175], v[60:63], v[84:87]\n" \
  "v_mfma_f32_16x16x32_bf16 v[88:91], v[204:207], v[60:63], v[88:91]\n" \
  "v_mfma_f32_16x16x32_bf16 v[92:95], v[236:239], v[60:63], v[92:95]\n" \
  "s_waitcnt lgkmcnt(2)\n" \
  "v_mfma_f32_16x16x32_bf16 v[112:115], v[144:147], v[48:51], 0\n" \
  "ds_read_b128 v[60:63], v1 offset:15360\n" \
  "v_mfma_f32_16x16x32_bf16 v[116:119], v[176:179], v[48:51], 0\n" \
  "v_mfma_f32_16x16x32_bf16 v[120:123], v[208:211], v[48:51], 0\n" \
  "v_mfma_f32_16x16x32_bf16 v[124:127], v[240:243], v[48:51], 0\n" \
  "s_waitcnt lgkmcnt(2)\n" \
  "v_mfma_f32_16x16x32_bf16 v[112:115], v[148:151], v[52:55], v[112:115]\n" \
  "v_mfma_f32_16x16x32_bf16 v[116:119], v[180:183], v[52:55], v[116:119]\n" \
  "v_mfma_f32_16x16x32_bf16 v[120:123], v[212:215], v[52:55], v[120:123]\n" \
  "v_mfma_f32_16x16x32_bf16 v[124:127], v[244:247], v[52:55], v[124:127]\n" \
  "s_waitcnt lgkmcnt(1)\n" \
  "v_mfma_f32_16x16x32_bf16 v[112:115], v[152:155], v[56:59], v[112:115]\n" \
  "v_mfma_f32_16x16x32_bf16 v[116:119], v[184:187], v[56:59], v[116:119]\n" \
  "v_mfma_f32_16x16x32_bf16 v[120:123], v[216:219], v[56:59], v[120:123]\n" \
  "v_mfma_f32_16x16x32_bf16 v[124:127], v[248:251], v[56:59], v[124:127]\n" \
  "s_waitcnt lgkmcnt(0)\n" \
  "v_mfma_f32_16x16x32_bf16 v[112:115], v[156:159], v[60:63], v[112:115]\n" \
  "v_mfma_f32_16x16x32_bf16 v[116:119], v[188:191], v[60:63], v[116:119]\n" \
  "v_mfma_f32_16x16x32_bf16 v[120:123], v[220:223], v[60:63], v[120:123]\n" \
  "v_mfma_f32_16x16x32_bf16 v[124:127], v[252:255], v[60:63], v[124:127]\n" \
  "s_setprio 3\n" \
  "s_barrier\n" \
  "ds_read_b128 v[32:35], v5 offset:0\n" \
  "ds_read_b128 v[36:39], v5 offset:512\n" \
  "ds_read_b128 v[40:43], v5 offset:1024\n" \
  "ds_read_b128 v[44:47], v5 offset:1536\n" \
  "ds_read_b64_tr_b16 v[48:49], v6 offset:0\n" \
  "ds_read_b64_tr_b16 v[50:51], v6 offset:32\n" \
  "ds_read_b64_tr_b16 v[52:53], v6 offset:1024\n" \
  "ds_read_b64_tr_b16 v[54:55], v6 offset:1056\n" \
  "ds_read_b64_tr_b16 v[56:57], v6 offset:4096\n" \
  "ds_read_b64_tr_b16 v[58:59], v6 offset:4128\n" \
  "ds_read_b64_tr_b16 v[60:61], v6 offset:5120\n" \
  "ds_read_b64_tr_b16 v[62:63], v6 offset:5152\n" \
  "s_waitcnt lgkmcnt(11)\n" \
  "v_mfma_f32_16x16x32_bf16 v[12:15], v[32:35], v[16:19], 0\n" \
  "s_waitcnt lgkmcnt(10)\n" \
  "v_mfma_f32_16x16x32_bf16 v[12:15], v[36:39], v[20:23], v[12:15]\n" \
  "s_waitcnt lgkmcnt(9)\n" \
  "v_mfma_f32_16x16x32_bf16 v[12:15], v[40:43], v[24:27], v[12:15]\n" \
  "s_waitcnt lgkmcnt(8)\n" \
  "v_mfma_f32_16x16x32_bf16 v[12:15], v[44:47], v[28:31], v[12:15]\n" \
  "v_mov_b64 v[16:17], 0\n" \
  "v_mov_b64 v[18:19], 0\n" \
  "v_mov_b64 v[20:21], 0\n" \
  "v_mov_b64 v[22:23], 0\n" \
  "v_mov_b64 v[24:25], 0\n" \
  "v_mov_b64 v[26:27], 0\n" \
  "v_mov_b64 v[28:29], 0\n" \
  "v_mov_b64 v[30:31], 0\n" \
  "v_maximum3_f32 v8, v8, v12, v13\n" \
  "v_mov_b32 v9, v8\n" \
  "v_mov_b64 v[32:33], 0\n" \
  "v_mov_b64 v[34:35], 0\n" \
  "v_permlane16_swap_b32 v8, v9\n" \
  "v_max_f32 v8, v8, v9\n" \
  "v_mov_b32 v9, v8\n" \
  "v_mov_b64 v[36:37], 0\n" \
  "v_mov_b64 v[38:39], 0\n" \
  "v_permlane32_swap_b32 v8, v9\n" \
  "v_max_f32 v8, v8, v9\n" \
  "v_mul_f32 v8, s80, v8\n" \
  "v_sub_f32 v14, v10, v8\n" \
  "v_fma_f32 v12, v12, s80, -v8\n" \
  "v_fma_f32 v13, v13, s80, -v8\n" \
  "v_exp_f32 v14, v14\n" \
  "v_exp_f32 v12, v12\n" \
  "v_exp_f32 v13, v13\n" \
  "v_fma_f32 v11, v11, v14, v12\n" \
  "v_add_f32 v11, v11, v13\n" \
  "v_mov_b32 v9, v11\n" \
  "v_mov_b64 v[40:41], 0\n" \
  "v_mov_b64 v[42:43], 0\n" \
  "v_permlane16_swap_b32 v11, v9\n" \
  "v_add_f32 v11, v11, v9\n" \
  "v_mov_b32 v9, v11\n" \
  "v_mov_b64 v[44:45], 0\n" \
  "v_mov_b64 v[46:47], 0\n" \
  "v_permlane32_swap_b32 v11, v9\n" \
  "v_add_f32 v11, v11, v9\n" \
  "v_rcp_f32 v15, v11\n" \
  "v_add_u32 v9, s66, v1\n" \
  "v_mul_f32 v12, v12, v15\n" \
  "v_mul_f32 v13, v13, v15\n" \
  "v_mul_f32 v14, v14, v15\n" \
  "v_cvt_pk_bf16_f32 v12, v12, v13\n" \
  "ds_write2_b32 v9, v12, v14 offset1:2\n" \
  "s_waitcnt lgkmcnt(0)\n" \
  "s_barrier\n" \
  "ds_read_b128 v[8:11], v1 offset:24576\n" \
  "ds_read_b128 v[12:15], v1 offset:25600\n" \
  "s_setprio 0\n" \
  "v_smfmac_f32_16x16x64_bf16 v[16:19], v[48:51], v[128:135], v0\n" \
  "v_smfmac_f32_16x16x64_bf16 v[20:23], v[48:51], v[160:167], v0\n" \
  "v_smfmac_f32_16x16x64_bf16 v[24:27], v[48:51], v[192:199], v0\n" \
  "v_smfmac_f32_16x16x64_bf16 v[28:31], v[48:51], v[224:231], v0\n" \
  "v_smfmac_f32_16x16x64_bf16 v[16:19], v[52:55], v[136:143], v0\n" \
  "v_smfmac_f32_16x16x64_bf16 v[20:23], v[52:55], v[168:175], v0\n" \
  "v_smfmac_f32_16x16x64_bf16 v[24:27], v[52:55], v[200:207], v0\n" \
  "v_smfmac_f32_16x16x64_bf16 v[28:31], v[52:55], v[232:239], v0\n" \
  "v_smfmac_f32_16x16x64_bf16 v[32:35], v[56:59], v[144:151], v0\n" \
  "s_waitcnt lgkmcnt(0)\n" \
  "v_pk_mul_f32 v[64:65], v[64:65], v[10:11] op_sel_hi:[1,0]\n" \
  "v_pk_mul_f32 v[66:67], v[66:67], v[10:11] op_sel_hi:[1,0]\n" \
  "v_smfmac_f32_16x16x64_bf16 v[36:39], v[56:59], v[176:183], v0\n" \
  "v_pk_mul_f32 v[68:69], v[68:69], v[10:11] op_sel_hi:[1,0]\n" \
  "v_pk_mul_f32 v[70:71], v[70:71], v[10:11] op_sel_hi:[1,0]\n" \
  "v_pk_fma_f32 v[64:65], v[96:97], v[10:11], v[64:65] op_sel:[0,1,0]\n" \
  "v_smfmac_f32_16x16x64_bf16 v[40:43], v[56:59], v[208:215], v0\n" \
  "v_pk_fma_f32 v[66:67], v[98:99], v[10:11], v[66:67] op_sel:[0,1,0]\n" \
  "v_pk_fma_f32 v[68:69], v[100:101], v[10:11], v[68:69] op_sel:[0,1,0]\n" \
  "v_pk_fma_f32 v[70:71], v[102:103], v[10:11], v[70:71] op_sel:[0,1,0]\n" \
  "v_smfmac_f32_16x16x64_bf16 v[44:47], v[56:59], v[240:247], v0\n" \
  "v_pk_mul_f32 v[72:73], v[72:73], v[10:11] op_sel_hi:[1,0]\n" \
  "v_pk_mul_f32 v[74:75], v[74:75], v[10:11] op_sel_hi:[1,0]\n" \
  "v_pk_mul_f32 v[76:77], v[76:77], v[10:11] op_sel_hi:[1,0]\n" \
  "v_smfmac_f32_16x16x64_bf16 v[32:35], v[60:63], v[152:159], v0\n" \
  "v_pk_mul_f32 v[78:79], v[78:79], v[10:11] op_sel_hi:[1,0]\n" \
  "v_pk_fma_f32 v[72:73], v[104:105], v[10:11], v[72:73] op_sel:[0,1,0]\n" \
  "v_pk_fma_f32 v[74:75], v[106:107], v[10:11], v[74:75] op_sel:[0,1,0]\n" \
  "v_smfmac_f32_16x16x64_bf16 v[36:39], v[60:63], v[184:191], v0\n" \
  "v_pk_fma_f32 v[76:77], v[108:109], v[10:11], v[76:77] op_sel:[0,1,0]\n" \
  "v_pk_fma_f32 v[78:79], v[110:111], v[10:11], v[78:79] op_sel:[0,1,0]\n" \
  "v_pk_mul_f32 v[80:81], v[80:81], v[14:15] op_sel_hi:[1,0]\n" \
  "v_smfmac_f32_16x16x64_bf16 v[40:43], v[60:63], v[216:223], v0\n" \
  "v_pk_mul_f32 v[82:83], v[82:83], v[14:15] op_sel_hi:[1,0]\n" \
  "v_pk_mul_f32 v[84:85], v[84:85], v[14:15] op_sel_hi:[1,0]\n" \
  "v_pk_mul_f32 v[86:87], v[86:87], v[14:15] op_sel_hi:[1,0]\n" \
  "v_smfmac_f32_16x16x64_bf16 v[44:47], v[60:63], v[248:255], v0\n" \
  "v_pk_fma_f32 v[80:81], v[112:113], v[14:15], v[80:81] op_sel:[0,1,0]\n" \
  "v_pk_fma_f32 v[82:83], v[114:115], v[14:15], v[82:83] op_sel:[0,1,0]\n" \
  "v_pk_fma_f32 v[84:85], v[116:117], v[14:15], v[84:85] op_sel:[0,1,0]\n" \
  "v_pk_fma_f32 v[86:87], v[118:119], v[14:15], v[86:87] op_sel:[0,1,0]\n" \
  "v_pk_mul_f32 v[88:89], v[88:89], v[14:15] op_sel_hi:[1,0]\n" \
  "v_pk_mul_f32 v[90:91], v[90:91], v[14:15] op_sel_hi:[1,0]\n" \
  "v_pk_mul_f32 v[92:93], v[92:93], v[14:15] op_sel_hi:[1,0]\n" \
  "v_pk_mul_f32 v[94:95], v[94:95], v[14:15] op_sel_hi:[1,0]\n" \
  "v_pk_fma_f32 v[88:89], v[120:121], v[14:15], v[88:89] op_sel:[0,1,0]\n" \
  "v_pk_fma_f32 v[90:91], v[122:123], v[14:15], v[90:91] op_sel:[0,1,0]\n" \
  "v_pk_fma_f32 v[92:93], v[124:125], v[14:15], v[92:93] op_sel:[0,1,0]\n" \
  "v_pk_fma_f32 v[94:95], v[126:127], v[14:15], v[94:95] op_sel:[0,1,0]\n" \
  "v_pk_add_f32 v[16:17], v[16:17], v[18:19]\n" \
  "v_pk_add_f32 v[32:33], v[32:33], v[34:35]\n" \
  "v_cvt_pk_bf16_f32 v16, v16, v17\n" \
  "v_cvt_pk_bf16_f32 v17, v32, v33\n" \
  "v_pk_add_f32 v[20:21], v[20:21], v[22:23]\n" \
  "v_pk_add_f32 v[36:37], v[36:37], v[38:39]\n" \
  "v_cvt_pk_bf16_f32 v20, v20, v21\n" \
  "v_cvt_pk_bf16_f32 v21, v36, v37\n" \
  "v_pk_add_f32 v[24:25], v[24:25], v[26:27]\n" \
  "v_pk_add_f32 v[40:41], v[40:41], v[42:43]\n" \
  "v_mfma_f32_16x16x16_bf16 v[64:67], v[16:17], v[8:9], v[64:67]\n" \
  "v_mfma_f32_16x16x16_bf16 v[68:71], v[20:21], v[8:9], v[68:71]\n" \
  "v_cvt_pk_bf16_f32 v24, v24, v25\n" \
  "v_cvt_pk_bf16_f32 v25, v40, v41\n" \
  "v_mfma_f32_16x16x16_bf16 v[80:83], v[16:17], v[12:13], v[80:83]\n" \
  "v_mfma_f32_16x16x16_bf16 v[84:87], v[20:21], v[12:13], v[84:87]\n" \
  "v_pk_add_f32 v[28:29], v[28:29], v[30:31]\n" \
  "v_pk_add_f32 v[44:45], v[44:45], v[46:47]\n" \
  "v_cvt_pk_bf16_f32 v28, v28, v29\n" \
  "v_cvt_pk_bf16_f32 v29, v44, v45\n" \
  "s_nop 1\n" \
  "v_mfma_f32_16x16x16_bf16 v[72:75], v[24:25], v[8:9], v[72:75]\n" \
  "v_mfma_f32_16x16x16_bf16 v[76:79], v[28:29], v[8:9], v[76:79]\n" \
  "v_mfma_f32_16x16x16_bf16 v[88:91], v[24:25], v[12:13], v[88:91]\n" \
  "v_mfma_f32_16x16x16_bf16 v[92:95], v[28:29], v[12:13], v[92:95]\n" \
  "v_cvt_pk_bf16_f32 v64, v64, v65\n" \
  "v_cvt_pk_bf16_f32 v65, v66, v67\n" \
  "v_cvt_pk_bf16_f32 v66, v68, v69\n" \
  "v_cvt_pk_bf16_f32 v67, v70, v71\n" \
  "buffer_store_dwordx4 v[64:67], v2, s[40:43], s75 offen offset:0 sc1\n" \
  "v_cvt_pk_bf16_f32 v80, v80, v81\n" \
  "v_cvt_pk_bf16_f32 v81, v82, v83\n" \
  "v_cvt_pk_bf16_f32 v82, v84, v85\n" \
  "v_cvt_pk_bf16_f32 v83, v86, v87\n" \
  "buffer_store_dwordx4 v[80:83], v2, s[40:43], s76 offen offset:0 sc1\n" \
  "v_cvt_pk_bf16_f32 v72, v72, v73\n" \
  "v_cvt_pk_bf16_f32 v73, v74, v75\n" \
  "v_cvt_pk_bf16_f32 v74, v76, v77\n" \
  "v_cvt_pk_bf16_f32 v75, v78, v79\n" \
  "buffer_store_dwordx4 v[72:75], v2, s[40:43], s75 offen offset:128 sc1\n" \
  "v_cvt_pk_bf16_f32 v88, v88, v89\n" \
  "v_cvt_pk_bf16_f32 v89, v90, v91\n" \
  "v_cvt_pk_bf16_f32 v90, v92, v93\n" \
  "v_cvt_pk_bf16_f32 v91, v94, v95\n" \
  "buffer_store_dwordx4 v[88:91], v2, s[40:43], s76 offen offset:128 sc1\n" \
  ".LWA_NEXT_%=:\n" \
  "v_xor_b32 v3, 0x10000, v3\n" \
  "v_xor_b32 v4, 0x10000, v4\n" \
  "s_mov_b32 s57, s58\n" \
  "s_cmp_eq_u32 s57, 128\n" \
  "s_cbranch_scc1 .LWA_END_%=\n" \
  "s_branch .LWA_TOP_%=\n" \
  ".LWA_END_%=:\n" \
  "s_endpgm\n" \
  ".LWA_EASY_%=:\n" \
  "v_lshrrev_b32 v9, 1, v1\n" \
  "v_mov_b32 v105, 0\n" \
  "v_add_u32 v64, s63, v9\n" \
  "v_mov_b32 v106, 0\n" \
  "s_barrier\n" \
  "s_setprio 3\n" \
  "ds_read_b128 v[72:75], v5 offset:0\n" \
  "ds_read_b128 v[76:79], v5 offset:512\n" \
  "ds_read_b128 v[80:83], v5 offset:1024\n" \
  "ds_read_b128 v[84:87], v5 offset:1536\n" \
  "ds_read_b64 v[88:89], v64 offset:0\n" \
  "ds_read_b64 v[90:91], v64 offset:512\n" \
  "ds_read_b64 v[92:93], v64 offset:1024\n" \
  "ds_read_b64 v[94:95], v64 offset:1536\n" \
  "s_waitcnt lgkmcnt(7)\n" \
  "v_mfma_f32_16x16x32_bf16 v[64:67], v[72:75], v[16:19], 0\n" \
  "s_waitcnt lgkmcnt(6)\n" \
  "v_mfma_f32_16x16x32_bf16 v[64:67], v[76:79], v[20:23], v[64:67]\n" \
  "s_waitcnt lgkmcnt(5)\n" \
  "v_mfma_f32_16x16x32_bf16 v[64:67], v[80:83], v[24:27], v[64:67]\n" \
  "s_waitcnt lgkmcnt(4)\n" \
  "v_mfma_f32_16x16x32_bf16 v[64:67], v[84:87], v[28:31], v[64:67]\n" \
  "s_nop 7\n" \
  "v_maximum3_f32 v8, v8, v64, v65\n" \
  "v_mov_b32 v9, v8\n" \
  "s_nop 1\n" \
  "v_permlane16_swap_b32 v8, v9\n" \
  "v_max_f32 v8, v8, v9\n" \
  "v_mov_b32 v9, v8\n" \
  "s_nop 1\n" \
  "v_permlane32_swap_b32 v8, v9\n" \
  "v_max_f32 v8, v8, v9\n" \
  "v_mul_f32 v8, s80, v8\n" \
  "v_sub_f32 v66, v10, v8\n" \
  "v_fma_f32 v64, v64, s80, -v8\n" \
  "v_fma_f32 v65, v65, s80, -v8\n" \
  "v_exp_f32 v66, v66\n" \
  "v_exp_f32 v64, v64\n" \
  "v_exp_f32 v65, v65\n" \
  "v_fma_f32 v11, v11, v66, v64\n" \
  "v_add_f32 v11, v11, v65\n" \
  "v_mov_b32 v9, v11\n" \
  "s_nop 1\n" \
  "v_permlane16_swap_b32 v11, v9\n" \
  "v_add_f32 v11, v11, v9\n" \
  "v_mov_b32 v9, v11\n" \
  "s_nop 1\n" \
  "v_permlane32_swap_b32 v11, v9\n" \
  "v_add_f32 v11, v11, v9\n" \
  "v_rcp_f32 v68, v11\n" \
  "s_waitcnt lgkmcnt(0)\n" \
  "v_mul_f32 v66, v66, v68\n" \
  "v_mul_f32 v64, v64, v68\n" \
  "v_mul_f32 v65, v65, v68\n" \
  "v_pk_mul_f32 v[32:33], v[32:33], v[66:67] op_sel_hi:[1,0]\n" \
  "v_cvt_pk_bf16_f32 v104, v64, v65\n" \
  "v_pk_mul_f32 v[34:35], v[34:35], v[66:67] op_sel_hi:[1,0]\n" \
  "v_cvt_pk_bf16_f32 v107, v64, v65\n" \
  "v_mul_f32 v48, v48, v66\n" \
  "v_mul_f32 v49, v49, v66\n" \
  "v_mul_f32 v50, v50, v66\n" \
  "v_mul_f32 v51, v51, v66\n" \
  "v_mfma_f32_16x16x16_bf16 v[32:35], v[88:89], v[104:105], v[32:35]\n" \
  "v_mul_f32 v36, v36, v66\n" \
  "v_mul_f32 v37, v37, v66\n" \
  "v_mul_f32 v38, v38, v66\n" \
  "v_mul_f32 v39, v39, v66\n" \
  "v_mfma_f32_16x16x16_bf16 v[48:51], v[88:89], v[106:107], v[48:51]\n" \
  "v_mul_f32 v52, v52, v66\n" \
  "v_mul_f32 v53, v53, v66\n" \
  "v_mul_f32 v54, v54, v66\n" \
  "v_mul_f32 v55, v55, v66\n" \
  "v_mfma_f32_16x16x16_bf16 v[36:39], v[90:91], v[104:105], v[36:39]\n" \
  "v_mul_f32 v40, v40, v66\n" \
  "v_mul_f32 v41, v41, v66\n" \
  "v_mul_f32 v42, v42, v66\n" \
  "v_mul_f32 v43, v43, v66\n" \
  "v_mfma_f32_16x16x16_bf16 v[52:55], v[90:91], v[106:107], v[52:55]\n" \
  "v_mul_f32 v56, v56, v66\n" \
  "v_mul_f32 v57, v57, v66\n" \
  "v_mul_f32 v58, v58, v66\n" \
  "v_mul_f32 v59, v59, v66\n" \
  "v_mfma_f32_16x16x16_bf16 v[40:43], v[92:93], v[104:105], v[40:43]\n" \
  "v_mul_f32 v44, v44, v66\n" \
  "v_mul_f32 v45, v45, v66\n" \
  "v_mul_f32 v46, v46, v66\n" \
  "v_mul_f32 v47, v47, v66\n" \
  "v_mfma_f32_16x16x16_bf16 v[56:59], v[92:93], v[106:107], v[56:59]\n" \
  "v_mul_f32 v60, v60, v66\n" \
  "v_mul_f32 v61, v61, v66\n" \
  "v_mul_f32 v62, v62, v66\n" \
  "v_mul_f32 v63, v63, v66\n" \
  "v_mfma_f32_16x16x16_bf16 v[44:47], v[94:95], v[104:105], v[44:47]\n" \
  "v_cvt_pk_bf16_f32 v108, v32, v33\n" \
  "v_cvt_pk_bf16_f32 v109, v48, v49\n" \
  "v_cvt_pk_bf16_f32 v110, v34, v35\n" \
  "v_cvt_pk_bf16_f32 v111, v50, v51\n" \
  "ds_write_b128 v7, v[108:111] offset:32768\n" \
  "v_mfma_f32_16x16x16_bf16 v[60:63], v[94:95], v[106:107], v[60:63]\n" \
  "v_cvt_pk_bf16_f32 v112, v36, v37\n" \
  "v_cvt_pk_bf16_f32 v113, v52, v53\n" \
  "v_cvt_pk_bf16_f32 v114, v38, v39\n" \
  "v_cvt_pk_bf16_f32 v115, v54, v55\n" \
  "ds_write_b128 v7, v[112:115] offset:33792\n" \
  "v_cvt_pk_bf16_f32 v116, v40, v41\n" \
  "v_cvt_pk_bf16_f32 v117, v56, v57\n" \
  "v_cvt_pk_bf16_f32 v118, v42, v43\n" \
  "v_cvt_pk_bf16_f32 v119, v58, v59\n" \
  "ds_write_b128 v7, v[116:119] offset:34816\n" \
  "v_cvt_pk_bf16_f32 v120, v44, v45\n" \
  "v_cvt_pk_bf16_f32 v121, v60, v61\n" \
  "v_cvt_pk_bf16_f32 v122, v46, v47\n" \
  "v_cvt_pk_bf16_f32 v123, v62, v63\n" \
  "ds_write_b128 v7, v[120:123] offset:35840\n" \
  "s_waitcnt lgkmcnt(0)\n" \
  "s_barrier\n" \
  "s_setprio 2\n" \
  "ds_read_b128 v[48:51], v1 offset:32768\n" \
  "ds_read_b128 v[52:55], v1 offset:33792\n" \
  "ds_read_b128 v[56:59], v1 offset:34816\n" \
  "s_waitcnt lgkmcnt(2)\n" \
  "v_mfma_f32_16x16x32_bf16 v[64:67], v[128:131], v[48:51], 0\n" \
  "ds_read_b128 v[60:63], v1 offset:35840\n" \
  "s_lshl_b32 s75, s57, 19\n" \
  "v_mfma_f32_16x16x32_bf16 v[68:71], v[160:163], v[48:51], 0\n" \
  "s_add_u32 s75, s75, s60\n" \
  "v_mfma_f32_16x16x32_bf16 v[72:75], v[192:195], v[48:51], 0\n" \
  "s_add_u32 s76, s75, 16384\n" \
  "v_mfma_f32_16x16x32_bf16 v[76:79], v[224:227], v[48:51], 0\n" \
  "s_waitcnt lgkmcnt(2)\n" \
  "v_mfma_f32_16x16x32_bf16 v[64:67], v[132:135], v[52:55], v[64:67]\n" \
  "ds_read_b128 v[48:51], v1 offset:40960\n" \
  "v_mfma_f32_16x16x32_bf16 v[68:71], v[164:167], v[52:55], v[68:71]\n" \
  "v_mfma_f32_16x16x32_bf16 v[72:75], v[196:199], v[52:55], v[72:75]\n" \
  "v_mfma_f32_16x16x32_bf16 v[76:79], v[228:231], v[52:55], v[76:79]\n" \
  "s_waitcnt lgkmcnt(2)\n" \
  "v_mfma_f32_16x16x32_bf16 v[64:67], v[136:139], v[56:59], v[64:67]\n" \
  "ds_read_b128 v[52:55], v1 offset:41984\n" \
  "v_mfma_f32_16x16x32_bf16 v[68:71], v[168:171], v[56:59], v[68:71]\n" \
  "v_mfma_f32_16x16x32_bf16 v[72:75], v[200:203], v[56:59], v[72:75]\n" \
  "v_mfma_f32_16x16x32_bf16 v[76:79], v[232:235], v[56:59], v[76:79]\n" \
  "s_waitcnt lgkmcnt(2)\n" \
  "v_mfma_f32_16x16x32_bf16 v[64:67], v[140:143], v[60:63], v[64:67]\n" \
  "ds_read_b128 v[56:59], v1 offset:43008\n" \
  "v_mfma_f32_16x16x32_bf16 v[68:71], v[172:175], v[60:63], v[68:71]\n" \
  "v_mfma_f32_16x16x32_bf16 v[72:75], v[204:207], v[60:63], v[72:75]\n" \
  "v_mfma_f32_16x16x32_bf16 v[76:79], v[236:239], v[60:63], v[76:79]\n" \
  "s_waitcnt lgkmcnt(2)\n" \
  "v_mfma_f32_16x16x32_bf16 v[64:67], v[144:147], v[48:51], v[64:67]\n" \
  "ds_read_b128 v[60:63], v1 offset:44032\n" \
  "v_mfma_f32_16x16x32_bf16 v[68:71], v[176:179], v[48:51], v[68:71]\n" \
  "v_mfma_f32_16x16x32_bf16 v[72:75], v[208:211], v[48:51], v[72:75]\n" \
  "v_mfma_f32_16x16x32_bf16 v[76:79], v[240:243], v[48:51], v[76:79]\n" \
  "s_waitcnt lgkmcnt(2)\n" \
  "v_mfma_f32_16x16x32_bf16 v[64:67], v[148:151], v[52:55], v[64:67]\n" \
  "ds_read_b128 v[48:51], v1 offset:36864\n" \
  "v_mfma_f32_16x16x32_bf16 v[68:71], v[180:183], v[52:55], v[68:71]\n" \
  "v_mfma_f32_16x16x32_bf16 v[72:75], v[212:215], v[52:55], v[72:75]\n" \
  "v_mfma_f32_16x16x32_bf16 v[76:79], v[244:247], v[52:55], v[76:79]\n" \
  "s_waitcnt lgkmcnt(2)\n" \
  "v_mfma_f32_16x16x32_bf16 v[64:67], v[152:155], v[56:59], v[64:67]\n" \
  "ds_read_b128 v[52:55], v1 offset:37888\n" \
  "v_mfma_f32_16x16x32_bf16 v[68:71], v[184:187], v[56:59], v[68:71]\n" \
  "v_mfma_f32_16x16x32_bf16 v[72:75], v[216:219], v[56:59], v[72:75]\n" \
  "v_mfma_f32_16x16x32_bf16 v[76:79], v[248:251], v[56:59], v[76:79]\n" \
  "s_waitcnt lgkmcnt(2)\n" \
  "v_mfma_f32_16x16x32_bf16 v[64:67], v[156:159], v[60:63], v[64:67]\n" \
  "ds_read_b128 v[56:59], v1 offset:38912\n" \
  "v_mfma_f32_16x16x32_bf16 v[68:71], v[188:191], v[60:63], v[68:71]\n" \
  "v_mfma_f32_16x16x32_bf16 v[72:75], v[220:223], v[60:63], v[72:75]\n" \
  "v_mfma_f32_16x16x32_bf16 v[76:79], v[252:255], v[60:63], v[76:79]\n" \
  "s_waitcnt lgkmcnt(2)\n" \
  "v_mfma_f32_16x16x32_bf16 v[80:83], v[128:131], v[48:51], 0\n" \
  "ds_read_b128 v[60:63], v1 offset:39936\n" \
  "v_mfma_f32_16x16x32_bf16 v[84:87], v[160:163], v[48:51], 0\n" \
  "v_mfma_f32_16x16x32_bf16 v[88:91], v[192:195], v[48:51], 0\n" \
  "v_mfma_f32_16x16x32_bf16 v[92:95], v[224:227], v[48:51], 0\n" \
  "s_waitcnt lgkmcnt(2)\n" \
  "v_mfma_f32_16x16x32_bf16 v[80:83], v[132:135], v[52:55], v[80:83]\n" \
  "ds_read_b128 v[48:51], v1 offset:45056\n" \
  "v_mfma_f32_16x16x32_bf16 v[84:87], v[164:167], v[52:55], v[84:87]\n" \
  "v_cvt_pk_bf16_f32 v64, v64, v65\n" \
  "v_mfma_f32_16x16x32_bf16 v[88:91], v[196:199], v[52:55], v[88:91]\n" \
  "v_cvt_pk_bf16_f32 v65, v66, v67\n" \
  "v_mfma_f32_16x16x32_bf16 v[92:95], v[228:231], v[52:55], v[92:95]\n" \
  "s_waitcnt lgkmcnt(2)\n" \
  "v_mfma_f32_16x16x32_bf16 v[80:83], v[136:139], v[56:59], v[80:83]\n" \
  "ds_read_b128 v[52:55], v1 offset:46080\n" \
  "v_mfma_f32_16x16x32_bf16 v[84:87], v[168:171], v[56:59], v[84:87]\n" \
  "v_cvt_pk_bf16_f32 v66, v68, v69\n" \
  "v_mfma_f32_16x16x32_bf16 v[88:91], v[200:203], v[56:59], v[88:91]\n" \
  "v_cvt_pk_bf16_f32 v67, v70, v71\n" \
  "v_mfma_f32_16x16x32_bf16 v[92:95], v[232:235], v[56:59], v[92:95]\n" \
  "s_waitcnt lgkmcnt(2)\n" \
  "v_mfma_f32_16x16x32_bf16 v[80:83], v[140:143], v[60:63], v[80:83]\n" \
  "ds_read_b128 v[56:59], v1 offset:47104\n" \
  "v_mfma_f32_16x16x32_bf16 v[84:87], v[172:175], v[60:63], v[84:87]\n" \
  "buffer_store_dwordx4 v[64:67], v2, s[40:43], s75 offen offset:0 sc1\n" \
  "v_mfma_f32_16x16x32_bf16 v[88:91], v[204:207], v[60:63], v[88:91]\n" \
  "v_cvt_pk_bf16_f32 v72, v72, v73\n" \
  "v_mfma_f32_16x16x32_bf16 v[92:95], v[236:239], v[60:63], v[92:95]\n" \
  "s_waitcnt lgkmcnt(2)\n" \
  "v_mfma_f32_16x16x32_bf16 v[80:83], v[144:147], v[48:51], v[80:83]\n" \
  "ds_read_b128 v[60:63], v1 offset:48128\n" \
  "v_mfma_f32_16x16x32_bf16 v[84:87], v[176:179], v[48:51], v[84:87]\n" \
  "v_cvt_pk_bf16_f32 v73, v74, v75\n" \
  "v_mfma_f32_16x16x32_bf16 v[88:91], v[208:211], v[48:51], v[88:91]\n" \
  "v_cvt_pk_bf16_f32 v74, v76, v77\n" \
  "v_mfma_f32_16x16x32_bf16 v[92:95], v[240:243], v[48:51], v[92:95]\n" \
  "s_waitcnt lgkmcnt(2)\n" \
  "v_mfma_f32_16x16x32_bf16 v[80:83], v[148:151], v[52:55], v[80:83]\n" \
  "v_mfma_f32_16x16x32_bf16 v[84:87], v[180:183], v[52:55], v[84:87]\n" \
  "v_cvt_pk_bf16_f32 v75, v78, v79\n" \
  "v_mfma_f32_16x16x32_bf16 v[88:91], v[212:215], v[52:55], v[88:91]\n" \
  "buffer_store_dwordx4 v[72:75], v2, s[40:43], s75 offen offset:128 sc1\n" \
  "v_mfma_f32_16x16x32_bf16 v[92:95], v[244:247], v[52:55], v[92:95]\n" \
  "s_waitcnt lgkmcnt(1)\n" \
  "v_mfma_f32_16x16x32_bf16 v[80:83], v[152:155], v[56:59], v[80:83]\n" \
  "v_mfma_f32_16x16x32_bf16 v[84:87], v[184:187], v[56:59], v[84:87]\n" \
  "v_mfma_f32_16x16x32_bf16 v[88:91], v[216:219], v[56:59], v[88:91]\n" \
  "v_mfma_f32_16x16x32_bf16 v[92:95], v[248:251], v[56:59], v[92:95]\n" \
  "s_waitcnt lgkmcnt(0)\n" \
  "v_mfma_f32_16x16x32_bf16 v[80:83], v[156:159], v[60:63], v[80:83]\n" \
  "v_mfma_f32_16x16x32_bf16 v[84:87], v[188:191], v[60:63], v[84:87]\n" \
  "v_mfma_f32_16x16x32_bf16 v[88:91], v[220:223], v[60:63], v[88:91]\n" \
  "v_mfma_f32_16x16x32_bf16 v[92:95], v[252:255], v[60:63], v[92:95]\n" \
  "s_nop 4\n" \
  "v_cvt_pk_bf16_f32 v80, v80, v81\n" \
  "v_cvt_pk_bf16_f32 v81, v82, v83\n" \
  "v_cvt_pk_bf16_f32 v82, v84, v85\n" \
  "v_cvt_pk_bf16_f32 v83, v86, v87\n" \
  "buffer_store_dwordx4 v[80:83], v2, s[40:43], s76 offen offset:0 sc1\n" \
  "v_cvt_pk_bf16_f32 v88, v88, v89\n" \
  "v_cvt_pk_bf16_f32 v89, v90, v91\n" \
  "v_cvt_pk_bf16_f32 v90, v92, v93\n" \
  "v_cvt_pk_bf16_f32 v91, v94, v95\n" \
  "buffer_store_dwordx4 v[88:91], v2, s[40:43], s76 offen offset:128 sc1\n" \
  "s_branch .LWA_NEXT_%=\n"
#define AW_ASM_1 \
  "v_lshlrev_b32 v7, 5, v0\n" \
  "s_mov_b32 s79, -1\n" \
  "s_cmp_eq_u32 s67, 0\n" \
  "s_cbranch_scc1 .LWB_LATE_%=\n" \
  "s_waitcnt vmcnt(0)\n" \
  "v_mov_b32 v12, s65\n" \
  "v_mov_b32 v13, s67\n" \
  "s_mov_b64 exec, 1\n" \
  "ds_add_u32 v12, v13\n" \
  "s_mov_b64 exec, -1\n" \
  ".p2align 6\n" \
  ".LWB_AL_%=:\n" \
  ".LWB_TOP_%=:\n" \
  "s_add_u32 s56, s56, 3\n" \
  "s_mov_b32 s77, 0\n" \
  "s_cmp_lt_u32 s56, %[w3]\n" \
  "s_cselect_b32 s68, s57, 0\n" \
  "s_cmp_ge_u32 s68, 64\n" \
  "s_cbranch_scc0 .LWB_HARD_%=\n" \
  "s_mov_b32 m0, s63\n" \
  "s_lshl_b32 s72, s57, 16\n" \
  "s_add_u32 s72, s72, s52\n" \
  "s_addc_u32 s73, s53, 0\n" \
  "global_load_lds_dwordx4 v0, s[72:73] offset:0 sc1\n" \
  "global_load_lds_dwordx4 v0, s[72:73] offset:1024 sc1\n" \
  ".LWB_HARD_%=:\n" \
  "s_sub_u32 s68, s56, %[wd]\n" \
  "s_bitcmp1_b32 s68, 0\n" \
  "s_cselect_b32 s58, 64, %[wd]\n" \
  "s_lshr_b32 s70, s68, 1\n" \
  "s_add_u32 s58, s58, s70\n" \
  "s_cmp_lt_u32 s68, %[w2]\n" \
  "s_cselect_b32 s58, s58, s56\n" \
  "s_min_u32 s58, s58, 128\n" \
  "s_lshr_b32 s68, s58, 6\n" \
  "s_add_u32 s68, s68, s58\n" \
  "s_lshl_b32 s68, s68, 12\n" \
  "buffer_load_dword v5, off, s[44:47], s68 sc1\n" \
  "s_xor_b32 s61, s61, 0x10000\n" \
  "s_xor_b32 s62, s62, 0x10000\n" \
  "s_mov_b32 s67, 0\n" \
  "s_cmp_lt_u32 s56, %[w3]\n" \
  "s_cselect_b32 s68, s57, 0\n" \
  "s_cmp_ge_u32 s68, 64\n" \
  "s_cbranch_scc1 .LWB_ASKEDE_%=\n" \
  "s_and_b32 s75, s57, 64\n" \
  "s_lshl_b32 s75, s75, 4\n" \
  "buffer_load_dword v14, off, s[84:87], s75 sc1\n" \
  "s_and_b32 s76, s57, 63\n" \
  "s_mul_i32 s76, s76, %[pp]\n" \
  "s_add_u32 s76, s76, %[mo]\n" \
  "s_lshl_b32 s59, s57, 19\n" \
  "s_add_u32 s59, s59, s66\n" \
  "s_add_u32 s78, s59, 4096\n" \
  "v_mov_b64 v[8:9], -1\n" \
  "v_mov_b64 v[10:11], -1\n" \
  "s_barrier\n" \
  "s_setprio 0\n" \
  "buffer_store_dwordx4 v[8:11], v0, s[40:43], s59 offen offset:0 sc1\n" \
  "buffer_store_dwordx4 v[8:11], v0, s[40:43], s59 offen offset:1024 sc1\n" \
  "buffer_store_dwordx4 v[8:11], v0, s[40:43], s59 offen offset:2048 sc1\n" \
  "buffer_store_dwordx4 v[8:11], v0, s[40:43], s59 offen offset:3072 sc1\n" \
  "buffer_store_dwordx4 v[8:11], v0, s[40:43], s78 offen offset:0 sc1\n" \
  "buffer_store_dwordx4 v[8:11], v0, s[40:43], s78 offen offset:1024 sc1\n" \
  "buffer_store_dwordx4 v[8:11], v0, s[40:43], s78 offen offset:2048 sc1\n" \
  "buffer_store_dwordx4 v[8:11], v0, s[40:43], s78 offen offset:3072 sc1\n" \
  "v_mov_b64 v[16:17], 0\n" \
  "v_mov_b64 v[18:19], 0\n" \
  "v_mov_b64 v[20:21], 0\n" \
  "v_mov_b64 v[22:23], 0\n" \
  "v_mov_b64 v[24:25], 0\n" \
  "v_mov_b64 v[26:27], 0\n" \
  "v_mov_b64 v[28:29], 0\n" \
  "v_mov_b64 v[30:31], 0\n" \
  "v_mov_b64 v[32:33], 0\n" \
  "v_mov_b64 v[34:35], 0\n" \
  "v_mov_b64 v[36:37], 0\n" \
  "v_mov_b64 v[38:39], 0\n" \
  "v_mov_b64 v[40:41], 0\n" \
  "v_mov_b64 v[42:43], 0\n" \
  "v_mov_b64 v[44:45], 0\n" \
  "v_mov_b64 v[46:47], 0\n" \
  "s_waitcnt vmcnt(8)\n" \
  "v_readfirstlane_b32 s74, v14\n" \
  "s_add_u32 s74, s74, s76\n" \
  "s_cmp_lt_i32 s79, 0\n" \
  "s_cbranch_scc1 .LWB_NOD_%=\n" \
  "v_mov_b32 v12, s64\n" \
  "v_mov_b32 v13, 1\n" \
  "s_mov_b64 exec, 1\n" \
  "ds_add_rtn_u32 v15, v12, v13\n" \
  "s_mov_b64 exec, -1\n" \
  "s_waitcnt lgkmcnt(0)\n" \
  "v_readfirstlane_b32 s68, v15\n" \
  "s_and_b32 s68, s68, 3\n" \
  "s_cmp_lg_u32 s68, 3\n" \
  "s_cbranch_scc1 .LWB_NOD_%=\n" \
  "s_memrealtime s[68:69]\n" \
  "s_lshl_b32 s72, s79, 11\n" \
  "s_add_u32 s72, s72, s54\n" \
  "s_addc_u32 s73, s55, 0\n" \
  "s_waitcnt lgkmcnt(0)\n" \
  "s_add_u32 s70, s68, 0\n" \
  "s_or_b32 s70, s70, 1\n" \
  "v_mov_b32 v13, s70\n" \
  "s_mov_b64 exec, 15\n" \
  "global_store_dword v7, v13, s[72:73] sc1\n" \
  "s_mov_b64 exec, -1\n" \
  ".LWB_NOD_%=:\n" \
  "s_mov_b32 s79, -1\n" \
  "v_cmp_eq_u32 vcc, 0, v5\n" \
  "s_cmp_eq_u64 vcc, 0\n" \
  "s_cbranch_scc0 .LWB_NOT_%=\n" \
  "s_mov_b32 m0, s61\n" \
  "s_lshl_b32 s68, s58, 19\n" \
  "s_add_u32 s68, s68, s48\n" \
  "s_addc_u32 s69, s49, 0\n" \
  "global_load_lds_dwordx4 v0, s[68:69] offset:0 sc1\n" \
  "global_load_lds_dwordx4 v0, s[68:69] offset:1024 sc1\n" \
  "global_load_lds_dwordx4 v0, s[68:69] offset:2048 sc1\n" \
  "global_load_lds_dwordx4 v0, s[68:69] offset:3072 sc1\n" \
  "s_add_u32 m0, s61, 4096\n" \
  "s_add_u32 s70, s68, 4096\n" \
  "s_addc_u32 s71, s69, 0\n" \
  "global_load_lds_dwordx4 v0, s[70:71] offset:0 sc1\n" \
  "global_load_lds_dwordx4 v0, s[70:71] offset:1024 sc1\n" \
  "global_load_lds_dwordx4 v0, s[70:71] offset:2048 sc1\n" \
  "global_load_lds_dwordx4 v0, s[70:71] offset:3072 sc1\n" \
  "s_mov_b32 m0, s62\n" \
  "s_lshl_b32 s72, s58, 18\n" \
  "s_add_u32 s72, s72, s50\n" \
  "s_addc_u32 s73, s51, 0\n" \
  "global_load_lds_dwordx4 v0, s[72:73] offset:0 sc1\n" \
  "global_load_lds_dwordx4 v0, s[72:73] offset:1024 sc1\n" \
  "global_load_lds_dwordx4 v0, s[72:73] offset:2048 sc1\n" \
  "global_load_lds_dwordx4 v0, s[72:73] offset:3072 sc1\n" \
  "s_mov_b32 s67, 1\n" \
  ".LWB_NOT_%=:\n" \
  "ds_read_b128 v[48:51], v1 offset:0\n" \
  "ds_read_b128 v[52:55], v1 offset:1024\n" \
  "ds_read_b128 v[56:59], v1 offset:2048\n" \
  "s_waitcnt lgkmcnt(2)\n" \
  "v_mfma_f32_16x16x32_bf16 v[64:67], v[128:131], v[48:51], 0\n" \
  "ds_read_b128 v[60:63], v1 offset:3072\n" \
  "v_mfma_f32_16x16x32_bf16 v[68:71], v[160:163], v[48:51], 0\n" \
  "v_mfma_f32_16x16x32_bf16 v[72:75], v[192:195], v[48:51], 0\n" \
  "v_mfma_f32_16x16x32_bf16 v[76:79], v[224:227], v[48:51], 0\n" \
  "s_waitcnt lgkmcnt(2)\n" \
  "v_mfma_f32_16x16x32_bf16 v[64:67], v[132:135], v[52:55], v[64:67]\n" \
  "ds_read_b128 v[48:51], v1 offset:8192\n" \
  "v_mfma_f32_16x16x32_bf16 v[68:71], v[164:167], v[52:55], v[68:71]\n" \
  "v_mfma_f32_16x16x32_bf16 v[72:75], v[196:199], v[52:55], v[72:75]\n" \
  "v_mfma_f32_16x16x32_bf16 v[76:79], v[228:231], v[52:55], v[76:79]\n" \
  "s_waitcnt lgkmcnt(2)\n" \
  "v_mfma_f32_16x16x32_bf16 v[64:67], v[136:139], v[56:59], v[64:67]\n" \
  "ds_read_b128 v[52:55], v1 offset:9216\n" \
  "s_memrealtime s[68:69]\n" \
  "v_mfma_f32_16x16x32_bf16 v[68:71], v[168:171], v[56:59], v[68:71]\n" \
  "v_mfma_f32_16x16x32_bf16 v[72:75], v[200:203], v[56:59], v[72:75]\n" \
  "v_mfma_f32_16x16x32_bf16 v[76:79], v[232:235], v[56:59], v[76:79]\n" \
  "s_waitcnt lgkmcnt(2)\n" \
  "v_mfma_f32_16x16x32_bf16 v[64:67], v[140:143], v[60:63], v[64:67]\n" \
  "ds_read_b128 v[56:59], v1 offset:10240\n" \
  "s_waitcnt lgkmcnt(0)\n" \
  "s_sub_i32 s70, s74, s68\n" \
  "s_cmp_gt_i32 s70, 0\n" \
  "s_cbranch_scc1 .LWB_NOTYET_%=\n" \
  "s_mov_b32 m0, s63\n" \
  "s_lshl_b32 s72, s57, 16\n" \
  "s_add_u32 s72, s72, s52\n" \
  "s_addc_u32 s73, s53, 0\n" \
  "global_load_lds_dwordx4 v0, s[72:73] offset:0 sc1\n" \
  "global_load_lds_dwordx4 v0, s[72:73] offset:1024 sc1\n" \
  "s_mov_b32 s77, 1\n" \
  ".LWB_NOTYET_%=:\n" \
  "s_lshl_b32 s70, s67, 7\n" \
  "s_or_b32 s70, s70, s58\n" \
  "s_lshr_b32 s68, s70, 6\n" \
  "s_add_u32 s68, s68, s70\n" \
  "s_lshl_b32 s68, s68, 12\n" \
  "buffer_load_dword v5, off, s[44:47], s68 sc1\n" \
  "v_mfma_f32_16x16x32_bf16 v[68:71], v[172:175], v[60:63], v[68:71]\n" \
  "v_mfma_f32_16x16x32_bf16 v[72:75], v[204:207], v[60:63], v[72:75]\n" \
  "v_mfma_f32_16x16x32_bf16 v[76:79], v[236:239], v[60:63], v[76:79]\n" \
  "s_waitcnt lgkmcnt(2)\n" \
  "v_mfma_f32_16x16x32_bf16 v[96:99], v[144:147], v[48:51], 0\n" \
  "ds_read_b128 v[60:63], v1 offset:11264\n" \
  "v_mfma_f32_16x16x32_bf16 v[100:103], v[176:179], v[48:51], 0\n" \
  "v_mfma_f32_16x16x32_bf16 v[104:107], v[208:211], v[48:51], 0\n" \
  "v_mfma_f32_16x16x32_bf16 v[108:111], v[240:243], v[48:51], 0\n" \
  "s_waitcnt lgkmcnt(2)\n" \
  "v_mfma_f32_16x16x32_bf16 v[96:99], v[148:151], v[52:55], v[96:99]\n" \
  "ds_read_b128 v[48:51], v1 offset:4096\n" \
  "v_mfma_f32_16x16x32_bf16 v[100:103], v[180:183], v[52:55], v[100:103]\n" \
  "v_mfma_f32_16x16x32_bf16 v[104:107], v[212:215], v[52:55], v[104:107]\n" \
  "v_mfma_f32_16x16x32_bf16 v[108:111], v[244:247], v[52:55], v[108:111]\n" \
  "s_waitcnt lgkmcnt(2)\n" \
  "v_mfma_f32_16x16x32_bf16 v[96:99], v[152:155], v[56:59], v[96:99]\n" \
  "ds_read_b128 v[52:55], v1 offset:5120\n" \
  "v_mfma_f32_16x16x32_bf16 v[100:103], v[184:187], v[56:59], v[100:103]\n" \
  "v_mfma_f32_16x16x32_bf16 v[104:107], v[216:219], v[56:59], v[104:107]\n" \
  "v_mfma_f32_16x16x32_bf16 v[108:111], v[248:251], v[56:59], v[108:111]\n" \
  "s_waitcnt lgkmcnt(2)\n" \
  "v_mfma_f32_16x16x32_bf16 v[96:99], v[156:159], v[60:63], v[96:99]\n" \
  "ds_read_b128 v[56:59], v1 offset:6144\n" \
  "v_mfma_f32_16x16x32_bf16 v[100:103], v[188:191], v[60:63], v[100:103]\n" \
  "v_mfma_f32_16x16x32_bf16 v[104:107], v[220:223], v[60:63], v[104:107]\n" \
  "v_mfma_f32_16x16x32_bf16 v[108:111], v[252:255], v[60:63], v[108:111]\n" \
  "s_waitcnt lgkmcnt(2)\n" \
  "v_mfma_f32_16x16x32_bf16 v[80:83], v[128:131], v[48:51], 0\n" \
  "ds_read_b128 v[60:63], v1 offset:7168\n" \
  "v_mfma_f32_16x16x32_bf16 v[84:87], v[160:163], v[48:51], 0\n" \
  "v_mfma_f32_16x16x32_bf16 v[88:91], v[192:195], v[48:51], 0\n" \
  "v_mfma_f32_16x16x32_bf16 v[92:95], v[224:227], v[48:51], 0\n" \
  "s_waitcnt lgkmcnt(2)\n" \
  "v_mfma_f32_16x16x32_bf16 v[80:83], v[132:135], v[52:55], v[80:83]\n" \
  "ds_read_b128 v[48:51], v1 offset:12288\n" \
  "v_mfma_f32_16x16x32_bf16 v[84:87], v[164:167], v[52:55], v[84:87]\n" \
  "v_mfma_f32_16x16x32_bf16 v[88:91], v[196:199], v[52:55], v[88:91]\n" \
  "v_mfma_f32_16x16x32_bf16 v[92:95], v[228:231], v[52:55], v[92:95]\n" \
  "s_waitcnt lgkmcnt(2)\n" \
  "v_mfma_f32_16x16x32_bf16 v[80:83], v[136:139], v[56:59], v[80:83]\n" \
  "ds_read_b128 v[52:55], v1 offset:13312\n" \
  "v_mfma_f32_16x16x32_bf16 v[84:87], v[168:171], v[56:59], v[84:87]\n" \
  "v_mfma_f32_16x16x32_bf16 v[88:91], v[200:203], v[56:59], v[88:91]\n" \
  "v_mfma_f32_16x16x32_bf16 v[92:95], v[232:235], v[56:59], v[92:95]\n" \
  "s_waitcnt lgkmcnt(2)\n" \
  "v_mfma_f32_16x16x32_bf16 v[80:83], v[140:143], v[60:63], v[80:83]\n" \
  "ds_read_b128 v[56:59], v1 offset:14336\n" \
  "v_mfma_f32_16x16x32_bf16 v[84:87], v[172:175], v[60:63], v[84:87]\n" \
  "v_mfma_f32_16x16x32_bf16 v[88:91], v[204:207], v[60:63], v[88:91]\n" \
  "v_mfma_f32_16x16x32_bf16 v[92:95], v[236:239], v[60:63], v[92:95]\n" \
  "s_waitcnt lgkmcnt(2)\n" \
  "v_mfma_f32_16x16x32_bf16 v[112:115], v[144:147], v[48:51], 0\n" \
  "ds_read_b128 v[60:63], v1 offset:15360\n" \
  "v_mfma_f32_16x16x32_bf16 v[116:119], v[176:179], v[48:51], 0\n" \
  "v_mfma_f32_16x16x32_bf16 v[120:123], v[208:211], v[48:51], 0\n" \
  "v_mfma_f32_16x16x32_bf16 v[124:127], v[240:243], v[48:51], 0\n" \
  "s_waitcnt lgkmcnt(2)\n" \
  "v_mfma_f32_16x16x32_bf16 v[112:115], v[148:151], v[52:55], v[112:115]\n" \
  "v_mfma_f32_16x16x32_bf16 v[116:119], v[180:183], v[52:55], v[116:119]\n" \
  "v_mfma_f32_16x16x32_bf16 v[120:123], v[212:215], v[52:55], v[120:123]\n" \
  "s_waitcnt vmcnt(0)\n" \
  "ds_read_b128 v[8:11], v3 offset:0\n" \
  "ds_read_b128 v[12:15], v3 offset:1024\n" \
  "v_mfma_f32_16x16x32_bf16 v[124:127], v[244:247], v[52:55], v[124:127]\n" \
  "s_waitcnt lgkmcnt(3)\n" \
  "v_mfma_f32_16x16x32_bf16 v[112:115], v[152:155], v[56:59], v[112:115]\n" \
  "v_mfma_f32_16x16x32_bf16 v[116:119], v[184:187], v[56:59], v[116:119]\n" \
  "v_mfma_f32_16x16x32_bf16 v[120:123], v[216:219], v[56:59], v[120:123]\n" \
  "v_mfma_f32_16x16x32_bf16 v[124:127], v[248:251], v[56:59], v[124:127]\n" \
  "s_waitcnt lgkmcnt(2)\n" \
  "v_mfma_f32_16x16x32_bf16 v[112:115], v[156:159], v[60:63], v[112:115]\n" \
  "s_waitcnt lgkmcnt(0)\n" \
  "v_min3_u32 v8, v8, v9, v10\n" \
  "v_mfma_f32_16x16x32_bf16 v[116:119], v[188:191], v[60:63], v[116:119]\n" \
  "v_min3_u32 v12, v12, v13, v14\n" \
  "v_mfma_f32_16x16x32_bf16 v[120:123], v[220:223], v[60:63], v[120:123]\n" \
  "v_min3_u32 v8, v8, v11, v15\n" \
  "v_mfma_f32_16x16x32_bf16 v[124:127], v[252:255], v[60:63], v[124:127]\n" \
  "v_min_u32 v8, v8, v12\n" \
  "v_cmp_eq_u32 vcc, 0, v5\n" \
  "s_cmp_eq_u64 vcc, 0\n" \
  "s_cbranch_scc0 .LWB_NOU_%=\n" \
  "s_mov_b32 m0, s61\n" \
  "s_lshl_b32 s68, s58, 19\n" \
  "s_add_u32 s68, s68, s48\n" \
  "s_addc_u32 s69, s49, 0\n" \
  "global_load_lds_dwordx4 v0, s[68:69] offset:0 sc1\n" \
  "global_load_lds_dwordx4 v0, s[68:69] offset:1024 sc1\n" \
  "global_load_lds_dwordx4 v0, s[68:69] offset:2048 sc1\n" \
  "global_load_lds_dwordx4 v0, s[68:69] offset:3072 sc1\n" \
  "s_add_u32 m0, s61, 4096\n" \
  "s_add_u32 s70, s68, 4096\n" \
  "s_addc_u32 s71, s69, 0\n" \
  "global_load_lds_dwordx4 v0, s[70:71] offset:0 sc1\n" \
  "global_load_lds_dwordx4 v0, s[70:71] offset:1024 sc1\n" \
  "global_load_lds_dwordx4 v0, s[70:71] offset:2048 sc1\n" \
  "global_load_lds_dwordx4 v0, s[70:71] offset:3072 sc1\n" \
  "s_mov_b32 m0, s62\n" \
  "s_lshl_b32 s72, s58, 18\n" \
  "s_add_u32 s72, s72, s50\n" \
  "s_addc_u32 s73, s51, 0\n" \
  "global_load_lds_dwordx4 v0, s[72:73] offset:0 sc1\n" \
  "global_load_lds_dwordx4 v0, s[72:73] offset:1024 sc1\n" \
  "global_load_lds_dwordx4 v0, s[72:73] offset:2048 sc1\n" \
  "global_load_lds_dwordx4 v0, s[72:73] offset:3072 sc1\n" \
  "s_mov_b32 s67, 1\n" \
  ".LWB_NOU_%=:\n" \
  "s_cmp_eq_u32 s77, 1\n" \
  "s_cbranch_scc1 .LWB_TEST_%=\n" \
  "s_memrealtime s[68:69]\n" \
  "s_waitcnt lgkmcnt(0)\n" \
  "s_sub_i32 s70, s74, s68\n" \
  "s_cmp_lt_i32 s70, 50\n" \
  "s_cbranch_scc1 .LWB_GATEB_%=\n" \
  ".LWB_GATEA_%=:\n" \
  "s_memrealtime s[68:69]\n" \
  "s_waitcnt lgkmcnt(0)\n" \
  "s_sub_i32 s70, s74, s68\n" \
  "s_sub_u32 s70, s70, 65\n" \
  "s_cmp_gt_u32 s70, 2000\n" \
  "s_cbranch_scc1 .LWB_NEWS_%=\n" \
  "s_sleep 1\n" \
  "s_cmp_lt_u32 s70, 40\n" \
  "s_cbranch_scc1 .LWB_GATEA_%=\n" \
  "s_sleep 8\n" \
  "s_branch .LWB_GATEA_%=\n" \
  ".LWB_NEWS_%=:\n" \
  "buffer_load_dword v14, off, s[84:87], s75 sc1\n" \
  "s_waitcnt vmcnt(0)\n" \
  "v_readfirstlane_b32 s74, v14\n" \
  "s_add_u32 s74, s74, s76\n" \
  ".LWB_GATEB_%=:\n" \
  "s_memrealtime s[68:69]\n" \
  "s_waitcnt lgkmcnt(0)\n" \
  "s_sub_i32 s70, s74, s68\n" \
  "s_sub_u32 s70, s70, 0\n" \
  "s_cmp_gt_u32 s70, 2000\n" \
  "s_cbranch_scc1 .LWB_ASK_%=\n" \
  "s_sleep 1\n" \
  "s_cmp_lt_u32 s70, 40\n" \
  "s_cbranch_scc1 .LWB_GATEB_%=\n" \
  "s_sleep 8\n" \
  "s_branch .LWB_GATEB_%=\n" \
  ".LWB_AGAIN_%=:\n" \
  "s_sleep %[sm]\n" \
  ".LWB_ASK_%=:\n" \
  "s_mov_b32 m0, s63\n" \
  "s_lshl_b32 s72, s57, 16\n" \
  "s_add_u32 s72, s72, s52\n" \
  "s_addc_u32 s73, s53, 0\n" \
  "global_load_lds_dwordx4 v0, s[72:73] offset:0 sc1\n" \
  "global_load_lds_dwordx4 v0, s[72:73] offset:1024 sc1\n" \
  "s_add_u32 s77, s77, 1\n" \
  ".LWB_ASKED_%=:\n" \
  "s_waitcnt vmcnt(0)\n" \
  "ds_read_b128 v[8:11], v3 offset:0\n" \
  "ds_read_b128 v[12:15], v3 offset:1024\n" \
  "s_cmp_gt_u32 s77, 0x40000\n" \
  "s_cbranch_scc1 .LWB_OK_%=\n" \
  "s_waitcnt lgkmcnt(1)\n" \
  "v_min3_u32 v8, v8, v9, v10\n" \
  "s_waitcnt lgkmcnt(0)\n" \
  "v_min3_u32 v12, v12, v13, v14\n" \
  "v_min3_u32 v8, v8, v11, v15\n" \
  "v_min_u32 v8, v8, v12\n" \
  ".LWB_TEST_%=:\n" \
  "v_cmp_eq_u32 vcc, 0, v8\n" \
  "s_cmp_eq_u64 vcc, 0\n" \
  "s_cbranch_scc0 .LWB_AGAIN_%=\n" \
  ".LWB_OK_%=:\n" \
  "s_waitcnt lgkmcnt(0)\n" \
  "s_barrier\n" \
  "ds_read_b64_tr_b16 v[48:49], v6 offset:0\n" \
  "ds_read_b64_tr_b16 v[50:51], v6 offset:32\n" \
  "ds_read_b64_tr_b16 v[52:53], v6 offset:1024\n" \
  "ds_read_b64_tr_b16 v[54:55], v6 offset:1056\n" \
  "ds_read_b64_tr_b16 v[56:57], v6 offset:4096\n" \
  "ds_read_b64_tr_b16 v[58:59], v6 offset:4128\n" \
  "ds_read_b64_tr_b16 v[60:61], v6 offset:5120\n" \
  "ds_read_b64_tr_b16 v[62:63], v6 offset:5152\n" \
  "s_memrealtime s[68:69]\n" \
  "s_lshl_b32 s72, s57, 11\n" \
  "s_add_u32 s72, s72, s54\n" \
  "s_addc_u32 s73, s55, 0\n" \
  "s_waitcnt lgkmcnt(0)\n" \
  "s_add_u32 s70, s68, %[dt]\n" \
  "s_or_b32 s70, s70, 1\n" \
  "v_mov_b32 v13, s70\n" \
  "s_mov_b64 exec, s[82:83]\n" \
  "global_store_dword v7, v13, s[72:73] sc1\n" \
  "s_mov_b64 exec, -1\n" \
  "s_waitcnt lgkmcnt(6)\n" \
  "v_smfmac_f32_16x16x64_bf16 v[16:19], v[48:51], v[128:135], v4\n" \
  "v_smfmac_f32_16x16x64_bf16 v[20:23], v[48:51], v[160:167], v4\n" \
  "v_smfmac_f32_16x16x64_bf16 v[24:27], v[48:51], v[192:199], v4\n" \
  "v_smfmac_f32_16x16x64_bf16 v[28:31], v[48:51], v[224:231], v4\n" \
  "s_waitcnt lgkmcnt(4)\n" \
  "v_smfmac_f32_16x16x64_bf16 v[16:19], v[52:55], v[136:143], v4\n" \
  "v_smfmac_f32_16x16x64_bf16 v[20:23], v[52:55], v[168:175], v4\n" \
  "v_smfmac_f32_16x16x64_bf16 v[24:27], v[52:55], v[200:207], v4\n" \
  "v_smfmac_f32_16x16x64_bf16 v[28:31], v[52:55], v[232:239], v4\n" \
  "s_waitcnt lgkmcnt(2)\n" \
  "v_smfmac_f32_16x16x64_bf16 v[32:35], v[56:59], v[144:151], v4\n" \
  "v_smfmac_f32_16x16x64_bf16 v[36:39], v[56:59], v[176:183], v4\n" \
  "v_smfmac_f32_16x16x64_bf16 v[40:43], v[56:59], v[208:215], v4\n" \
  "v_smfmac_f32_16x16x64_bf16 v[44:47], v[56:59], v[240:247], v4\n" \
  "s_waitcnt lgkmcnt(0)\n" \
  "v_smfmac_f32_16x16x64_bf16 v[32:35], v[60:63], v[152:159], v4\n" \
  "v_smfmac_f32_16x16x64_bf16 v[36:39], v[60:63], v[184:191], v4\n" \
  "v_smfmac_f32_16x16x64_bf16 v[40:43], v[60:63], v[216:223], v4\n" \
  "v_smfmac_f32_16x16x64_bf16 v[44:47], v[60:63], v[248:255], v4\n" \
  "s_nop 1\n" \
  "s_lshl_b32 s75, s57, 19\n" \
  "s_add_u32 s75, s75, s60\n" \
  "s_add_u32 s76, s75, 16384\n" \
  "v_pk_add_f32 v[16:17], v[16:17], v[18:19]\n" \
  "v_pk_add_f32 v[32:33], v[32:33], v[34:35]\n" \
  "v_cvt_pk_bf16_f32 v16, v16, v17\n" \
  "v_cvt_pk_bf16_f32 v17, v32, v33\n" \
  "v_pk_add_f32 v[20:21], v[20:21], v[22:23]\n" \
  "v_pk_add_f32 v[36:37], v[36:37], v[38:39]\n" \
  "v_cvt_pk_bf16_f32 v20, v20, v21\n" \
  "v_cvt_pk_bf16_f32 v21, v36, v37\n" \
  "v_pk_add_f32 v[24:25], v[24:25], v[26:27]\n" \
  "v_pk_add_f32 v[40:41], v[40:41], v[42:43]\n" \
  "v_cvt_pk_bf16_f32 v24, v24, v25\n" \
  "v_cvt_pk_bf16_f32 v25, v40, v41\n" \
  "v_pk_add_f32 v[28:29], v[28:29], v[30:31]\n" \
  "v_pk_add_f32 v[44:45], v[44:45], v[46:47]\n" \
  "v_cvt_pk_bf16_f32 v28, v28, v29\n" \
  "v_cvt_pk_bf16_f32 v29, v44, v45\n" \
  "s_barrier\n" \
  "ds_read_b128 v[32:35], v1 offset:24576\n" \
  "ds_read_b128 v[36:39], v1 offset:25600\n" \
  "s_setprio 1\n" \
  "s_waitcnt lgkmcnt(0)\n" \
  "v_pk_mul_f32 v[64:65], v[64:65], v[34:35] op_sel_hi:[1,0]\n" \
  "v_pk_mul_f32 v[66:67], v[66:67], v[34:35] op_sel_hi:[1,0]\n" \
  "v_pk_mul_f32 v[68:69], v[68:69], v[34:35] op_sel_hi:[1,0]\n" \
  "v_pk_mul_f32 v[70:71], v[70:71], v[34:35] op_sel_hi:[1,0]\n" \
  "v_pk_fma_f32 v[64:65], v[96:97], v[34:35], v[64:65] op_sel:[0,1,0]\n" \
  "v_pk_fma_f32 v[66:67], v[98:99], v[34:35], v[66:67] op_sel:[0,1,0]\n" \
  "v_pk_fma_f32 v[68:69], v[100:101], v[34:35], v[68:69] op_sel:[0,1,0]\n" \
  "v_pk_fma_f32 v[70:71], v[102:103], v[34:35], v[70:71] op_sel:[0,1,0]\n" \
  "s_nop 1\n" \
  "v_mfma_f32_16x16x16_bf16 v[64:67], v[16:17], v[32:33], v[64:67]\n" \
  "v_mfma_f32_16x16x16_bf16 v[68:71], v[20:21], v[32:33], v[68:71]\n" \
  "v_pk_mul_f32 v[72:73], v[72:73], v[34:35] op_sel_hi:[1,0]\n" \
  "v_pk_mul_f32 v[74:75], v[74:75], v[34:35] op_sel_hi:[1,0]\n" \
  "v_pk_mul_f32 v[76:77], v[76:77], v[34:35] op_sel_hi:[1,0]\n" \
  "v_pk_mul_f32 v[78:79], v[78:79], v[34:35] op_sel_hi:[1,0]\n" \
  "v_pk_fma_f32 v[72:73], v[104:105], v[34:35], v[72:73] op_sel:[0,1,0]\n" \
  "v_pk_fma_f32 v[74:75], v[106:107], v[34:35], v[74:75] op_sel:[0,1,0]\n" \
  "v_pk_fma_f32 v[76:77], v[108:109], v[34:35], v[76:77] op_sel:[0,1,0]\n" \
  "v_pk_fma_f32 v[78:79], v[110:111], v[34:35], v[78:79] op_sel:[0,1,0]\n" \
  "s_nop 1\n" \
  "v_mfma_f32_16x16x16_bf16 v[72:75], v[24:25], v[32:33], v[72:75]\n" \
  "v_mfma_f32_16x16x16_bf16 v[76:79], v[28:29], v[32:33], v[76:79]\n" \
  "v_cvt_pk_bf16_f32 v64, v64, v65\n" \
  "v_cvt_pk_bf16_f32 v65, v66, v67\n" \
  "v_cvt_pk_bf16_f32 v66, v68, v69\n" \
  "v_cvt_pk_bf16_f32 v67, v70, v71\n" \
  "buffer_store_dwordx4 v[64:67], v2, s[40:43], s75 offen offset:0 sc1\n" \
  "v_pk_mul_f32 v[80:81], v[80:81], v[38:39] op_sel_hi:[1,0]\n" \
  "v_pk_mul_f32 v[82:83], v[82:83], v[38:39] op_sel_hi:[1,0]\n" \
  "v_pk_mul_f32 v[84:85], v[84:85], v[38:39] op_sel_hi:[1,0]\n" \
  "v_pk_mul_f32 v[86:87], v[86:87], v[38:39] op_sel_hi:[1,0]\n" \
  "v_pk_fma_f32 v[80:81], v[112:113], v[38:39], v[80:81] op_sel:[0,1,0]\n" \
  "v_pk_fma_f32 v[82:83], v[114:115], v[38:39], v[82:83] op_sel:[0,1,0]\n" \
  "v_pk_fma_f32 v[84:85], v[116:117], v[38:39], v[84:85] op_sel:[0,1,0]\n" \
  "v_pk_fma_f32 v[86:87], v[118:119], v[38:39], v[86:87] op_sel:[0,1,0]\n" \
  "s_nop 1\n" \
  "v_mfma_f32_16x16x16_bf16 v[80:83], v[16:17], v[36:37], v[80:83]\n" \
  "v_mfma_f32_16x16x16_bf16 v[84:87], v[20:21], v[36:37], v[84:87]\n" \
  "v_cvt_pk_bf16_f32 v72, v72, v73\n" \
  "v_cvt_pk_bf16_f32 v73, v74, v75\n" \
  "v_cvt_pk_bf16_f32 v74, v76, v77\n" \
  "v_cvt_pk_bf16_f32 v75, v78, v79\n" \
  "buffer_store_dwordx4 v[72:75], v2, s[40:43], s75 offen offset:128 sc1\n" \
  "v_pk_mul_f32 v[88:89], v[88:89], v[38:39] op_sel_hi:[1,0]\n" \
  "v_pk_mul_f32 v[90:91], v[90:91], v[38:39] op_sel_hi:[1,0]\n" \
  "v_pk_mul_f32 v[92:93], v[92:93], v[38:39] op_sel_hi:[1,0]\n" \
  "v_pk_mul_f32 v[94:95], v[94:95], v[38:39] op_sel_hi:[1,0]\n" \
  "v_pk_fma_f32 v[88:89], v[120:121], v[38:39], v[88:89] op_sel:[0,1,0]\n" \
  "v_pk_fma_f32 v[90:91], v[122:123], v[38:39], v[90:91] op_sel:[0,1,0]\n" \
  "v_pk_fma_f32 v[92:93], v[124:125], v[38:39], v[92:93] op_sel:[0,1,0]\n" \
  "v_pk_fma_f32 v[94:95], v[126:127], v[38:39], v[94:95] op_sel:[0,1,0]\n" \
  "s_nop 1\n" \
  "v_mfma_f32_16x16x16_bf16 v[88:91], v[24:25], v[36:37], v[88:91]\n" \
  "v_mfma_f32_16x16x16_bf16 v[92:95], v[28:29], v[36:37], v[92:95]\n" \
  "v_cvt_pk_bf16_f32 v80, v80, v81\n" \
  "v_cvt_pk_bf16_f32 v81, v82, v83\n" \
  "v_cvt_pk_bf16_f32 v82, v84, v85\n" \
  "v_cvt_pk_bf16_f32 v83, v86, v87\n" \
  "buffer_store_dwordx4 v[80:83], v2, s[40:43], s76 offen offset:0 sc1\n" \
  "s_nop 2\n" \
  "v_cvt_pk_bf16_f32 v88, v88, v89\n" \
  "v_cvt_pk_bf16_f32 v89, v90, v91\n" \
  "v_cvt_pk_bf16_f32 v90, v92, v93\n" \
  "v_cvt_pk_bf16_f32 v91, v94, v95\n" \
  "buffer_store_dwordx4 v[88:91], v2, s[40:43], s76 offen offset:128 sc1\n" \
  "s_waitcnt vmcnt(4)\n" \
  "v_mov_b32 v12, s65\n" \
  "v_mov_b32 v13, s67\n" \
  "s_mov_b64 exec, 1\n" \
  "ds_add_u32 v12, v13\n" \
  "s_mov_b64 exec, -1\n" \
  ".LWB_NEXT_%=:\n" \
  "s_setprio 0\n" \
  "s_mov_b32 s57, s58\n" \
  "s_cmp_eq_u32 s67, 0\n" \
  "s_cbranch_scc1 .LWB_MISS_%=\n" \
  "s_branch .LWB_TOP_%=\n" \
  ".LWB_MISS_%=:\n" \
  "s_cmp_eq_u32 s57, 128\n" \
  "s_cbranch_scc1 .LWB_END_%=\n" \
  ".LWB_LATE_%=:\n" \
  "s_mov_b32 s77, 0x40000\n" \
  ".LWB_POLL_%=:\n" \
  "s_lshr_b32 s68, s57, 6\n" \
  "s_add_u32 s68, s68, s57\n" \
  "s_lshl_b32 s68, s68, 12\n" \
  "buffer_load_dword v5, off, s[44:47], s68 sc1\n" \
  "s_sub_u32 s77, s77, 1\n" \
  "s_cbranch_scc1 .LWB_SEEN_%=\n" \
  "s_waitcnt vmcnt(0)\n" \
  "v_cmp_eq_u32 vcc, 0, v5\n" \
  "s_cmp_eq_u64 vcc, 0\n" \
  "s_cbranch_scc1 .LWB_SEEN_%=\n" \
  "s_sleep %[sa]\n" \
  "s_branch .LWB_POLL_%=\n" \
  ".LWB_SEEN_%=:\n" \
  "s_mov_b32 m0, s61\n" \
  "s_lshl_b32 s68, s57, 19\n" \
  "s_add_u32 s68, s68, s48\n" \
  "s_addc_u32 s69, s49, 0\n" \
  "global_load_lds_dwordx4 v0, s[68:69] offset:0 sc1\n" \
  "global_load_lds_dwordx4 v0, s[68:69] offset:1024 sc1\n" \
  "global_load_lds_dwordx4 v0, s[68:69] offset:2048 sc1\n" \
  "global_load_lds_dwordx4 v0, s[68:69] offset:3072 sc1\n" \
  "s_add_u32 m0, s61, 4096\n" \
  "s_add_u32 s70, s68, 4096\n" \
  "s_addc_u32 s71, s69, 0\n" \
  "global_load_lds_dwordx4 v0, s[70:71] offset:0 sc1\n" \
  "global_load_lds_dwordx4 v0, s[70:71] offset:1024 sc1\n" \
  "global_load_lds_dwordx4 v0, s[70:71] offset:2048 sc1\n" \
  "global_load_lds_dwordx4 v0, s[70:71] offset:3072 sc1\n" \
  "s_mov_b32 m0, s62\n" \
  "s_lshl_b32 s72, s57, 18\n" \
  "s_add_u32 s72, s72, s50\n" \
  "s_addc_u32 s73, s51, 0\n" \
  "global_load_lds_dwordx4 v0, s[72:73] offset:0 sc1\n" \
  "global_load_lds_dwordx4 v0, s[72:73] offset:1024 sc1\n" \
  "global_load_lds_dwordx4 v0, s[72:73] offset:2048 sc1\n" \
  "global_load_lds_dwordx4 v0, s[72:73] offset:3072 sc1\n" \
  "s_mov_b32 s67, 1\n" \
  "s_waitcnt vmcnt(0)\n" \
  "v_mov_b32 v12, s65\n" \
  "v_mov_b32 v13, s67\n" \
  "s_mov_b64 exec, 1\n" \
  "ds_add_u32 v12, v13\n" \
  "s_mov_b64 exec, -1\n" \
  "s_branch .LWB_TOP_%=\n" \
  ".LWB_END_%=:\n" \
  "s_endpgm\n" \
  ".LWB_AGAINE_%=:\n" \
  "s_sleep %[sm]\n" \
  ".LWB_ASKE_%=:\n" \
  "s_mov_b32 m0, s63\n" \
  "s_lshl_b32 s72, s57, 16\n" \
  "s_add_u32 s72, s72, s52\n" \
  "s_addc_u32 s73, s53, 0\n" \
  "global_load_lds_dwordx4 v0, s[72:73] offset:0 sc1\n" \
  "global_load_lds_dwordx4 v0, s[72:73] offset:1024 sc1\n" \
  "s_add_u32 s77, s77, 1\n" \
  ".LWB_ASKEDE_%=:\n" \
  "s_waitcnt vmcnt(0)\n" \
  "ds_read_b128 v[8:11], v3 offset:0\n" \
  "ds_read_b128 v[12:15], v3 offset:1024\n" \
  "s_cmp_gt_u32 s77, 0x40000\n" \
  "s_cbranch_scc1 .LWB_OKE_%=\n" \
  "s_waitcnt lgkmcnt(1)\n" \
  "v_min3_u32 v8, v8, v9, v10\n" \
  "s_waitcnt lgkmcnt(0)\n" \
  "v_min3_u32 v12, v12, v13, v14\n" \
  "v_min3_u32 v8, v8, v11, v15\n" \
  "v_min_u32 v8, v8, v12\n" \
  ".LWB_TESTE_%=:\n" \
  "v_cmp_eq_u32 vcc, 0, v8\n" \
  "s_cmp_eq_u64 vcc, 0\n" \
  "s_cbranch_scc0 .LWB_AGAINE_%=\n" \
  ".LWB_OKE_%=:\n" \
  "s_waitcnt lgkmcnt(0)\n" \
  "s_barrier\n" \
  "v_cmp_eq_u32 vcc, 0, v5\n" \
  "s_cmp_eq_u64 vcc, 0\n" \
  "s_cbranch_scc0 .LWB_NOE_%=\n" \
  "s_mov_b32 m0, s61\n" \
  "s_lshl_b32 s68, s58, 19\n" \
  "s_add_u32 s68, s68, s48\n" \
  "s_addc_u32 s69, s49, 0\n" \
  "global_load_lds_dwordx4 v0, s[68:69] offset:0 sc1\n" \
  "global_load_lds_dwordx4 v0, s[68:69] offset:1024 sc1\n" \
  "global_load_lds_dwordx4 v0, s[68:69] offset:2048 sc1\n" \
  "global_load_lds_dwordx4 v0, s[68:69] offset:3072 sc1\n" \
  "s_add_u32 m0, s61, 4096\n" \
  "s_add_u32 s70, s68, 4096\n" \
  "s_addc_u32 s71, s69, 0\n" \
  "global_load_lds_dwordx4 v0, s[70:71] offset:0 sc1\n" \
  "global_load_lds_dwordx4 v0, s[70:71] offset:1024 sc1\n" \
  "global_load_lds_dwordx4 v0, s[70:71] offset:2048 sc1\n" \
  "global_load_lds_dwordx4 v0, s[70:71] offset:3072 sc1\n" \
  "s_mov_b32 m0, s62\n" \
  "s_lshl_b32 s72, s58, 18\n" \
  "s_add_u32 s72, s72, s50\n" \
  "s_addc_u32 s73, s51, 0\n" \
  "global_load_lds_dwordx4 v0, s[72:73] offset:0 sc1\n" \
  "global_load_lds_dwordx4 v0, s[72:73] offset:1024 sc1\n" \
  "global_load_lds_dwordx4 v0, s[72:73] offset:2048 sc1\n" \
  "global_load_lds_dwordx4 v0, s[72:73] offset:3072 sc1\n" \
  "s_mov_b32 s67, 1\n" \
  ".LWB_NOE_%=:\n" \
  "s_lshl_b32 s70, s67, 7\n" \
  "s_or_b32 s70, s70, s58\n" \
  "s_lshr_b32 s68, s70, 6\n" \
  "s_add_u32 s68, s68, s70\n" \
  "s_lshl_b32 s68, s68, 12\n" \
  "buffer_load_dword v5, off, s[44:47], s68 sc1\n" \
  "s_mov_b32 s77, 0\n" \
  "s_barrier\n" \
  "s_setprio 0\n" \
  "s_waitcnt vmcnt(0)\n" \
  "v_mov_b32 v12, s65\n" \
  "v_mov_b32 v13, s67\n" \
  "s_mov_b64 exec, 1\n" \
  "ds_add_u32 v12, v13\n" \
  "s_mov_b64 exec, -1\n" \
  "v_cmp_eq_u32 vcc, 0, v5\n" \
  "s_cmp_eq_u64 vcc, 0\n" \
  "s_cbranch_scc0 .LWB_NOF_%=\n" \
  "s_mov_b32 m0, s61\n" \
  "s_lshl_b32 s68, s58, 19\n" \
  "s_add_u32 s68, s68, s48\n" \
  "s_addc_u32 s69, s49, 0\n" \
  "global_load_lds_dwordx4 v0, s[68:69] offset:0 sc1\n" \
  "global_load_lds_dwordx4 v0, s[68:69] offset:1024 sc1\n" \
  "global_load_lds_dwordx4 v0, s[68:69] offset:2048 sc1\n" \
  "global_load_lds_dwordx4 v0, s[68:69] offset:3072 sc1\n" \
  "s_add_u32 m0, s61, 4096\n" \
  "s_add_u32 s70, s68, 4096\n" \
  "s_addc_u32 s71, s69, 0\n" \
  "global_load_lds_dwordx4 v0, s[70:71] offset:0 sc1\n" \
  "global_load_lds_dwordx4 v0, s[70:71] offset:1024 sc1\n" \
  "global_load_lds_dwordx4 v0, s[70:71] offset:2048 sc1\n" \
  "global_load_lds_dwordx4 v0, s[70:71] offset:3072 sc1\n" \
  "s_mov_b32 m0, s62\n" \
  "s_lshl_b32 s72, s58, 18\n" \
  "s_add_u32 s72, s72, s50\n" \
  "s_addc_u32 s73, s51, 0\n" \
  "global_load_lds_dwordx4 v0, s[72:73] offset:0 sc1\n" \
  "global_load_lds_dwordx4 v0, s[72:73] offset:1024 sc1\n" \
  "global_load_lds_dwordx4 v0, s[72:73] offset:2048 sc1\n" \
  "global_load_lds_dwordx4 v0, s[72:73] offset:3072 sc1\n" \
  "s_mov_b32 s67, 1\n" \
  "s_mov_b32 s77, 1\n" \
  ".LWB_NOF_%=:\n" \
  "ds_read_b128 v[48:51], v1 offset:32768\n" \
  "ds_read_b128 v[52:55], v1 offset:33792\n" \
  "ds_read_b128 v[56:59], v1 offset:34816\n" \
  "s_waitcnt lgkmcnt(2)\n" \
  "v_mfma_f32_16x16x32_bf16 v[64:67], v[128:131], v[48:51], 0\n" \
  "ds_read_b128 v[60:63], v1 offset:35840\n" \
  "s_lshl_b32 s75, s57, 19\n" \
  "v_mfma_f32_16x16x32_bf16 v[68:71], v[160:163], v[48:51], 0\n" \
  "s_add_u32 s75, s75, s60\n" \
  "v_mfma_f32_16x16x32_bf16 v[72:75], v[192:195], v[48:51], 0\n" \
  "s_add_u32 s76, s75, 16384\n" \
  "v_mfma_f32_16x16x32_bf16 v[76:79], v[224:227], v[48:51], 0\n" \
  "s_lshl_b32 s70, s67, 7\n" \
  "s_or_b32 s70, s70, s58\n" \
  "s_lshr_b32 s68, s70, 6\n" \
  "s_add_u32 s68, s68, s70\n" \
  "s_lshl_b32 s68, s68, 12\n" \
  "buffer_load_dword v5, off, s[44:47], s68 sc1\n" \
  "s_waitcnt lgkmcnt(2)\n" \
  "v_mfma_f32_16x16x32_bf16 v[64:67], v[132:135], v[52:55], v[64:67]\n" \
  "ds_read_b128 v[48:51], v1 offset:40960\n" \
  "v_mfma_f32_16x16x32_bf16 v[68:71], v[164:167], v[52:55], v[68:71]\n" \
  "v_mfma_f32_16x16x32_bf16 v[72:75], v[196:199], v[52:55], v[72:75]\n" \
  "v_mfma_f32_16x16x32_bf16 v[76:79], v[228:231], v[52:55], v[76:79]\n" \
  "s_waitcnt lgkmcnt(2)\n" \
  "v_mfma_f32_16x16x32_bf16 v[64:67], v[136:139], v[56:59], v[64:67]\n" \
  "ds_read_b128 v[52:55], v1 offset:41984\n" \
  "v_mfma_f32_16x16x32_bf16 v[68:71], v[168:171], v[56:59], v[68:71]\n" \
  "v_mfma_f32_16x16x32_bf16 v[72:75], v[200:203], v[56:59], v[72:75]\n" \
  "v_mfma_f32_16x16x32_bf16 v[76:79], v[232:235], v[56:59], v[76:79]\n" \
  "s_waitcnt lgkmcnt(2)\n" \
  "v_mfma_f32_16x16x32_bf16 v[64:67], v[140:143], v[60:63], v[64:67]\n" \
  "ds_read_b128 v[56:59], v1 offset:43008\n" \
  "v_mfma_f32_16x16x32_bf16 v[68:71], v[172:175], v[60:63], v[68:71]\n" \
  "v_mfma_f32_16x16x32_bf16 v[72:75], v[204:207], v[60:63], v[72:75]\n" \
  "s_cmp_lg_u32 s67, 0\n" \
  "s_cbranch_scc1 .LWB_NOG_%=\n" \
  "s_waitcnt vmcnt(0)\n" \
  "v_cmp_eq_u32 vcc, 0, v5\n" \
  "s_cmp_eq_u64 vcc, 0\n" \
  "s_cbranch_scc0 .LWB_NOG_%=\n" \
  "s_mov_b32 m0, s61\n" \
  "s_lshl_b32 s68, s58, 19\n" \
  "s_add_u32 s68, s68, s48\n" \
  "s_addc_u32 s69, s49, 0\n" \
  "global_load_lds_dwordx4 v0, s[68:69] offset:0 sc1\n" \
  "global_load_lds_dwordx4 v0, s[68:69] offset:1024 sc1\n" \
  "global_load_lds_dwordx4 v0, s[68:69] offset:2048 sc1\n" \
  "global_load_lds_dwordx4 v0, s[68:69] offset:3072 sc1\n" \
  "s_add_u32 m0, s61, 4096\n" \
  "s_add_u32 s70, s68, 4096\n" \
  "s_addc_u32 s71, s69, 0\n" \
  "global_load_lds_dwordx4 v0, s[70:71] offset:0 sc1\n" \
  "global_load_lds_dwordx4 v0, s[70:71] offset:1024 sc1\n" \
  "global_load_lds_dwordx4 v0, s[70:71] offset:2048 sc1\n" \
  "global_load_lds_dwordx4 v0, s[70:71] offset:3072 sc1\n" \
  "s_mov_b32 m0, s62\n" \
  "s_lshl_b32 s72, s58, 18\n" \
  "s_add_u32 s72, s72, s50\n" \
  "s_addc_u32 s73, s51, 0\n" \
  "global_load_lds_dwordx4 v0, s[72:73] offset:0 sc1\n" \
  "global_load_lds_dwordx4 v0, s[72:73] offset:1024 sc1\n" \
  "global_load_lds_dwordx4 v0, s[72:73] offset:2048 sc1\n" \
  "global_load_lds_dwordx4 v0, s[72:73] offset:3072 sc1\n" \
  "s_mov_b32 s67, 1\n" \
  "s_mov_b32 s77, 1\n" \
  ".LWB_NOG_%=:\n" \
  "v_mfma_f32_16x16x32_bf16 v[76:79], v[236:239], v[60:63], v[76:79]\n" \
  "s_waitcnt lgkmcnt(2)\n" \
  "v_mfma_f32_16x16x32_bf16 v[64:67], v[144:147], v[48:51], v[64:67]\n" \
  "ds_read_b128 v[60:63], v1 offset:44032\n" \
  "v_mfma_f32_16x16x32_bf16 v[68:71], v[176:179], v[48:51], v[68:71]\n" \
  "v_mfma_f32_16x16x32_bf16 v[72:75], v[208:211], v[48:51], v[72:75]\n" \
  "v_mfma_f32_16x16x32_bf16 v[76:79], v[240:243], v[48:51], v[76:79]\n" \
  "s_waitcnt lgkmcnt(2)\n" \
  "v_mfma_f32_16x16x32_bf16 v[64:67], v[148:151], v[52:55], v[64:67]\n" \
  "ds_read_b128 v[48:51], v1 offset:36864\n" \
  "v_mfma_f32_16x16x32_bf16 v[68:71], v[180:183], v[52:55], v[68:71]\n" \
  "v_mfma_f32_16x16x32_bf16 v[72:75], v[212:215], v[52:55], v[72:75]\n" \
  "v_mfma_f32_16x16x32_bf16 v[76:79], v[244:247], v[52:55], v[76:79]\n" \
  "s_waitcnt lgkmcnt(2)\n" \
  "v_mfma_f32_16x16x32_bf16 v[64:67], v[152:155], v[56:59], v[64:67]\n" \
  "ds_read_b128 v[52:55], v1 offset:37888\n" \
  "v_mfma_f32_16x16x32_bf16 v[68:71], v[184:187], v[56:59], v[68:71]\n" \
  "v_mfma_f32_16x16x32_bf16 v[72:75], v[216:219], v[56:59], v[72:75]\n" \
  "v_mfma_f32_16x16x32_bf16 v[76:79], v[248:251], v[56:59], v[76:79]\n" \
  "s_waitcnt lgkmcnt(2)\n" \
  "v_mfma_f32_16x16x32_bf16 v[64:67], v[156:159], v[60:63], v[64:67]\n" \
  "ds_read_b128 v[56:59], v1 offset:38912\n" \
  "v_mfma_f32_16x16x32_bf16 v[68:71], v[188:191], v[60:63], v[68:71]\n" \
  "v_mfma_f32_16x16x32_bf16 v[72:75], v[220:223], v[60:63], v[72:75]\n" \
  "v_mfma_f32_16x16x32_bf16 v[76:79], v[252:255], v[60:63], v[76:79]\n" \
  "s_waitcnt lgkmcnt(2)\n" \
  "v_mfma_f32_16x16x32_bf16 v[80:83], v[128:131], v[48:51], 0\n" \
  "ds_read_b128 v[60:63], v1 offset:39936\n" \
  "v_mfma_f32_16x16x32_bf16 v[84:87], v[160:163], v[48:51], 0\n" \
  "v_mfma_f32_16x16x32_bf16 v[88:91], v[192:195], v[48:51], 0\n" \
  "s_waitcnt vmcnt(0)\n" \
  "v_mov_b32 v12, s65\n" \
  "v_mov_b32 v13, s77\n" \
  "s_mov_b64 exec, 1\n" \
  "ds_add_u32 v12, v13\n" \
  "s_mov_b64 exec, -1\n" \
  "v_mfma_f32_16x16x32_bf16 v[92:95], v[224:227], v[48:51], 0\n" \
  "s_lshl_b32 s70, s67, 7\n" \
  "s_or_b32 s70, s70, s58\n" \
  "s_lshr_b32 s68, s70, 6\n" \
  "s_add_u32 s68, s68, s70\n" \
  "s_lshl_b32 s68, s68, 12\n" \
  "buffer_load_dword v5, off, s[44:47], s68 sc1\n" \
  "s_waitcnt lgkmcnt(3)\n" \
  "v_mfma_f32_16x16x32_bf16 v[80:83], v[132:135], v[52:55], v[80:83]\n" \
  "ds_read_b128 v[48:51], v1 offset:45056\n" \
  "v_mfma_f32_16x16x32_bf16 v[84:87], v[164:167], v[52:55], v[84:87]\n" \
  "v_cvt_pk_bf16_f32 v64, v64, v65\n" \
  "v_mfma_f32_16x16x32_bf16 v[88:91], v[196:199], v[52:55], v[88:91]\n" \
  "v_cvt_pk_bf16_f32 v65, v66, v67\n" \
  "v_mfma_f32_16x16x32_bf16 v[92:95], v[228:231], v[52:55], v[92:95]\n" \
  "s_waitcnt lgkmcnt(3)\n" \
  "v_mfma_f32_16x16x32_bf16 v[80:83], v[136:139], v[56:59], v[80:83]\n" \
  "ds_read_b128 v[52:55], v1 offset:46080\n" \
  "v_mfma_f32_16x16x32_bf16 v[84:87], v[168:171], v[56:59], v[84:87]\n" \
  "v_cvt_pk_bf16_f32 v66, v68, v69\n" \
  "v_mfma_f32_16x16x32_bf16 v[88:91], v[200:203], v[56:59], v[88:91]\n" \
  "v_cvt_pk_bf16_f32 v67, v70, v71\n" \
  "v_mfma_f32_16x16x32_bf16 v[92:95], v[232:235], v[56:59], v[92:95]\n" \
  "s_waitcnt lgkmcnt(3)\n" \
  "v_mfma_f32_16x16x32_bf16 v[80:83], v[140:143], v[60:63], v[80:83]\n" \
  "ds_read_b128 v[56:59], v1 offset:47104\n" \
  "v_mfma_f32_16x16x32_bf16 v[84:87], v[172:175], v[60:63], v[84:87]\n" \
  "buffer_store_dwordx4 v[64:67], v2, s[40:43], s75 offen offset:0 sc1\n" \
  "v_mfma_f32_16x16x32_bf16 v[88:91], v[204:207], v[60:63], v[88:91]\n" \
  "v_cvt_pk_bf16_f32 v72, v72, v73\n" \
  "v_mfma_f32_16x16x32_bf16 v[92:95], v[236:239], v[60:63], v[92:95]\n" \
  "s_waitcnt lgkmcnt(2)\n" \
  "v_mfma_f32_16x16x32_bf16 v[80:83], v[144:147], v[48:51], v[80:83]\n" \
  "ds_read_b128 v[60:63], v1 offset:48128\n" \
  "v_mfma_f32_16x16x32_bf16 v[84:87], v[176:179], v[48:51], v[84:87]\n" \
  "v_cvt_pk_bf16_f32 v73, v74, v75\n" \
  "v_mfma_f32_16x16x32_bf16 v[88:91], v[208:211], v[48:51], v[88:91]\n" \
  "v_cvt_pk_bf16_f32 v74, v76, v77\n" \
  "v_mfma_f32_16x16x32_bf16 v[92:95], v[240:243], v[48:51], v[92:95]\n" \
  "s_waitcnt lgkmcnt(2)\n" \
  "v_mfma_f32_16x16x32_bf16 v[80:83], v[148:151], v[52:55], v[80:83]\n" \
  "v_mfma_f32_16x16x32_bf16 v[84:87], v[180:183], v[52:55], v[84:87]\n" \
  "v_cvt_pk_bf16_f32 v75, v78, v79\n" \
  "v_mfma_f32_16x16x32_bf16 v[88:91], v[212:215], v[52:55], v[88:91]\n" \
  "buffer_store_dwordx4 v[72:75], v2, s[40:43], s75 offen offset:128 sc1\n" \
  "v_mfma_f32_16x16x32_bf16 v[92:95], v[244:247], v[52:55], v[92:95]\n" \
  "s_waitcnt lgkmcnt(1)\n" \
  "v_mfma_f32_16x16x32_bf16 v[80:83], v[152:155], v[56:59], v[80:83]\n" \
  "v_mfma_f32_16x16x32_bf16 v[84:87], v[184:187], v[56:59], v[84:87]\n" \
  "v_mfma_f32_16x16x32_bf16 v[88:91], v[216:219], v[56:59], v[88:91]\n" \
  "v_mfma_f32_16x16x32_bf16 v[92:95], v[248:251], v[56:59], v[92:95]\n" \
  "s_waitcnt lgkmcnt(0)\n" \
  "v_mfma_f32_16x16x32_bf16 v[80:83], v[156:159], v[60:63], v[80:83]\n" \
  "v_mfma_f32_16x16x32_bf16 v[84:87], v[188:191], v[60:63], v[84:87]\n" \
  "v_mfma_f32_16x16x32_bf16 v[88:91], v[220:223], v[60:63], v[88:91]\n" \
  "v_mfma_f32_16x16x32_bf16 v[92:95], v[252:255], v[60:63], v[92:95]\n" \
  "s_nop 4\n" \
  "v_cvt_pk_bf16_f32 v80, v80, v81\n" \
  "v_cvt_pk_bf16_f32 v81, v82, v83\n" \
  "v_cvt_pk_bf16_f32 v82, v84, v85\n" \
  "v_cvt_pk_bf16_f32 v83, v86, v87\n" \
  "buffer_store_dwordx4 v[80:83], v2, s[40:43], s76 offen offset:0 sc1\n" \
  "v_cvt_pk_bf16_f32 v88, v88, v89\n" \
  "v_cvt_pk_bf16_f32 v89, v90, v91\n" \
  "v_cvt_pk_bf16_f32 v90, v92, v93\n" \
  "v_cvt_pk_bf16_f32 v91, v94, v95\n" \
  "buffer_store_dwordx4 v[88:91], v2, s[40:43], s76 offen offset:128 sc1\n" \
  "s_mov_b32 s79, s57\n" \
  "s_waitcnt vmcnt(4)\n" \
  "v_cmp_eq_u32 vcc, 0, v5\n" \
  "s_cmp_eq_u64 vcc, 0\n" \
  "s_cbranch_scc0 .LWB_NEXT_%=\n" \
  "s_mov_b32 s57, s58\n" \
  "s_setprio 0\n" \
  "s_branch .LWB_SEEN_%=\n"
#define CONV_ASM \
  "v_mov_b64 v[64:65], 0\n" \
  "v_mov_b64 v[66:67], 0\n" \
  "v_mov_b64 v[68:69], 0\n" \
  "v_mov_b64 v[70:71], 0\n" \
  "v_mov_b64 v[72:73], 0\n" \
  "v_mov_b64 v[74:75], 0\n" \
  "v_mov_b64 v[76:77], 0\n" \
  "v_mov_b64 v[78:79], 0\n" \
  "v_mov_b64 v[80:81], 0\n" \
  "v_mov_b64 v[82:83], 0\n" \
  "v_mov_b64 v[84:85], 0\n" \
  "v_mov_b64 v[86:87], 0\n" \
  "v_mov_b64 v[88:89], 0\n" \
  "v_mov_b64 v[90:91], 0\n" \
  "v_mov_b64 v[92:93], 0\n" \
  "v_mov_b64 v[94:95], 0\n" \
  "v_mov_b64 v[96:97], 0\n" \
  "v_mov_b64 v[98:99], 0\n" \
  "v_mov_b64 v[100:101], 0\n" \
  "v_mov_b64 v[102:103], 0\n" \
  "v_mov_b64 v[104:105], 0\n" \
  "v_mov_b64 v[106:107], 0\n" \
  "v_mov_b64 v[108:109], 0\n" \
  "v_mov_b64 v[110:111], 0\n" \
  "v_mov_b64 v[112:113], 0\n" \
  "v_mov_b64 v[114:115], 0\n" \
  "v_mov_b64 v[116:117], 0\n" \
  "v_mov_b64 v[118:119], 0\n" \
  "v_mov_b64 v[120:121], 0\n" \
  "v_mov_b64 v[122:123], 0\n" \
  "v_mov_b64 v[124:125], 0\n" \
  "v_mov_b64 v[126:127], 0\n" \
  "v_mov_b32 v20, 0\n" \
  "v_mov_b32 v41, 0\n" \
  "v_mov_b32 v254, 0\n" \
  "s_branch .LVA_TAPS_%=\n" \
  ".p2align 6\n" \
  ".LVA_AL_%=:\n" \
  ".LVA_TOP_%=:\n" \
  "s_cmp_lg_u32 s63, 1\n" \
  "s_cbranch_scc1 .LVA_BAR_%=\n" \
  "s_waitcnt vmcnt(0)\n" \
  "v_min3_u32 v56, v42, v43, v41\n" \
  "v_readfirstlane_b32 s78, v254\n" \
  "v_cmp_eq_u32 vcc, 0, v56\n" \
  "s_cbranch_vccz .LVA_SEEN_%=\n" \
  "s_mov_b32 s79, 0\n" \
  "s_cmp_lg_u32 s78, 0\n" \
  "s_cbranch_scc1 .LVA_KNOWN_%=\n" \
  ".LVA_GATE_%=:\n" \
  "s_memrealtime s[74:75]\n" \
  "s_waitcnt lgkmcnt(0)\n" \
  "s_sub_i32 s74, s69, s74\n" \
  "s_cmp_gt_u32 s74, %[gv]\n" \
  "s_cbranch_scc1 .LVA_WHEN_%=\n" \
  "s_sleep 2\n" \
  "s_branch .LVA_GATE_%=\n" \
  ".LVA_NONE_%=:\n" \
  "s_sleep %[sv]\n" \
  ".LVA_WHEN_%=:\n" \
  "buffer_load_dword v254, off, s[44:47], s71 sc1\n" \
  "s_add_u32 s79, s79, 1\n" \
  "s_cmp_gt_u32 s79, 0x40000\n" \
  "s_cbranch_scc1 .LVA_KNOWN_%=\n" \
  "s_waitcnt vmcnt(0)\n" \
  "v_readfirstlane_b32 s78, v254\n" \
  "s_cmp_eq_u32 s78, 0\n" \
  "s_cbranch_scc1 .LVA_NONE_%=\n" \
  ".LVA_KNOWN_%=:\n" \
  "s_memrealtime s[74:75]\n" \
  "s_waitcnt lgkmcnt(0)\n" \
  "s_sub_i32 s74, s78, s74\n" \
  "s_cmp_lt_i32 s74, %[lt]\n" \
  "s_cselect_b32 s70, 0, 0x200000\n" \
  "s_branch .LVA_DUE_%=\n" \
  ".LVA_SOON_%=:\n" \
  "s_cmp_lt_u32 s74, 8\n" \
  "s_cbranch_scc1 .LVA_NOW_%=\n" \
  "s_sleep 1\n" \
  ".LVA_NOW_%=:\n" \
  "s_memrealtime s[74:75]\n" \
  "s_waitcnt lgkmcnt(0)\n" \
  "s_sub_i32 s74, s78, s74\n" \
  ".LVA_DUE_%=:\n" \
  "s_cmp_lt_u32 s74, 1000\n" \
  "s_cbranch_scc1 .LVA_SOON_%=\n" \
  "s_branch .LVA_ASK_%=\n" \
  ".LVA_AGAIN_%=:\n" \
  "s_sleep %[sv]\n" \
  ".LVA_ASK_%=:\n" \
  "buffer_load_dwordx2 v[42:43], v37, s[40:43], s65 offen sc1\n" \
  "buffer_load_dword v41, v38, s[44:47], s66 offen sc1\n" \
  "s_add_u32 s79, s79, 1\n" \
  "s_cmp_gt_u32 s79, 0x40000\n" \
  "s_cbranch_scc1 .LVA_SEEN_%=\n" \
  "s_waitcnt vmcnt(0)\n" \
  "v_min3_u32 v56, v42, v43, v41\n" \
  "v_cmp_eq_u32 vcc, 0, v56\n" \
  "s_cbranch_vccnz .LVA_AGAIN_%=\n" \
  ".LVA_SEEN_%=:\n" \
  "v_and_b32 v62, 0xffff0000, v41\n" \
  "v_lshlrev_b32 v60, 16, v41\n" \
  "v_lshlrev_b32 v56, 16, v42\n" \
  "v_and_b32 v57, 0xffff0000, v42\n" \
  "v_lshlrev_b32 v58, 16, v43\n" \
  "v_and_b32 v59, 0xffff0000, v43\n" \
  "v_rcp_f32 v62, v62\n" \
  "v_pk_add_f32 v[56:57], v[56:57], v[60:61] op_sel_hi:[1,0] neg_lo:[0,1] neg_hi:[0,1]\n" \
  "v_pk_add_f32 v[58:59], v[58:59], v[60:61] op_sel_hi:[1,0] neg_lo:[0,1] neg_hi:[0,1]\n" \
  "v_cvt_pk_bf16_f32 v57, v56, v57\n" \
  "v_cvt_pk_bf16_f32 v59, v58, v59\n" \
  "v_lshlrev_b32 v56, 16, v57\n" \
  "v_and_b32 v57, 0xffff0000, v57\n" \
  "v_lshlrev_b32 v58, 16, v59\n" \
  "v_and_b32 v59, 0xffff0000, v59\n" \
  "v_pk_mul_f32 v[56:57], v[56:57], v[62:63] op_sel_hi:[1,0]\n" \
  "v_pk_mul_f32 v[58:59], v[58:59], v[62:63] op_sel_hi:[1,0]\n" \
  "v_cvt_pk_bf16_f32 v56, v56, v57\n" \
  "v_cvt_pk_bf16_f32 v58, v58, v59\n" \
  "ds_write_b16 v39, v56\n" \
  "ds_write_b16_d16_hi v39, v56 offset:4288\n" \
  "ds_write_b16 v39, v58 offset:8576\n" \
  "ds_write_b16_d16_hi v39, v58 offset:12864\n" \
  "ds_write_b64 v40, v[42:43] offset:512\n" \
  ".LVA_BAR_%=:\n" \
  "s_waitcnt lgkmcnt(0)\n" \
  "s_barrier\n" \
  "ds_read_b128 v[56:59], v0 offset:0\n" \
  "ds_read_u16 v60, v2 offset:0\n" \
  "ds_read_u16 v61, v3 offset:512\n" \
  "ds_read2_b32 v[200:201], v4 offset0:0 offset1:16\n" \
  "ds_read2_b32 v[202:203], v4 offset0:1 offset1:17\n" \
  "ds_read2_b32 v[204:205], v4 offset0:2 offset1:18\n" \
  "ds_read2_b32 v[206:207], v4 offset0:3 offset1:19\n" \
  "ds_read2_b32 v[208:209], v4 offset0:4 offset1:20\n" \
  "s_setprio 3\n" \
  "s_waitcnt lgkmcnt(7)\n" \
  "v_mfma_f32_16x16x32_bf16 v[20:23], v[56:59], v[12:15], v[20:23]\n" \
  "v_mfma_f32_16x16x32_bf16 v[20:23], v[56:59], v[16:19], v[20:23]\n" \
  "s_waitcnt lgkmcnt(5)\n" \
  "v_lshlrev_b32 v60, 16, v60\n" \
  "v_lshlrev_b32 v61, 16, v61\n" \
  "v_mul_f32 v60, s64, v60\n" \
  "v_add_u32 v0, v1, v0\n" \
  "v_add_u32 v2, 64, v2\n" \
  "v_add_u32 v4, 64, v4\n" \
  "s_nop 0\n" \
  "v_cvt_pk_bf16_f32 v62, v20, v60\n" \
  "v_lshlrev_b32 v63, 16, v62\n" \
  "v_and_b32 v62, 0xffff0000, v62\n" \
  "v_add_f32 v62, v62, v63\n" \
  "v_cvt_pk_bf16_f32 v62, v62, v62\n" \
  "v_lshlrev_b32 v62, 16, v62\n" \
  "v_add_f32 v62, v62, v61\n" \
  "v_cvt_pk_bf16_f32 v62, v62, v62\n" \
  "s_mov_b64 exec, s[72:73]\n" \
  "ds_write_b16 v3, v62\n" \
  "s_mov_b64 exec, -1\n" \
  "s_cmp_gt_u32 s63, 1\n" \
  "s_waitcnt lgkmcnt(0)\n" \
  "s_barrier\n" \
  "s_cbranch_scc1 .LVA_REST_%=\n" \
  "s_cmp_eq_u32 s63, 1\n" \
  "s_cbranch_scc1 .LVA_ONE_%=\n" \
  "ds_read_b128 v[56:59], v40 offset:0\n" \
  "s_mov_b32 exec_hi, 0\n" \
  "s_waitcnt lgkmcnt(0)\n" \
  "buffer_store_dwordx4 v[56:59], v7, s[48:51], s67 offen sc1\n" \
  "s_mov_b32 exec_hi, -1\n" \
  "s_add_u32 s67, s67, 0x8000\n" \
  "s_branch .LVA_GO_%=\n" \
  ".LVA_ONE_%=:\n" \
  "s_add_u32 s65, s65, 0x8000\n" \
  "s_add_u32 s66, s66, 2048\n" \
  "s_add_u32 s71, s71, 4\n" \
  "s_max_u32 s76, s65, s70\n" \
  "s_max_u32 s78, s66, s70\n" \
  "buffer_load_dword v254, off, s[44:47], s71 sc1\n" \
  "buffer_load_dwordx2 v[42:43], v37, s[40:43], s76 offen sc1\n" \
  "buffer_load_dword v41, v38, s[44:47], s78 offen sc1\n" \
  "s_memrealtime s[74:75]\n" \
  "v_add_u32 v39, 64, v39\n" \
  "s_waitcnt lgkmcnt(0)\n" \
  "s_add_u32 s69, s74, %[gv]\n" \
  "s_branch .LVA_GO_%=\n" \
  ".LVA_REST_%=:\n" \
  "s_setprio 0\n" \
  ".LVA_GO_%=:\n" \
  "v_alignbit_b32 v246, v202, v200, v6\n" \
  "v_alignbit_b32 v247, v204, v202, v6\n" \
  "v_alignbit_b32 v248, v206, v204, v6\n" \
  "v_alignbit_b32 v249, v208, v206, v6\n" \
  "v_alignbit_b32 v250, v203, v201, v6\n" \
  "v_alignbit_b32 v251, v205, v203, v6\n" \
  "v_alignbit_b32 v252, v207, v205, v6\n" \
  "v_alignbit_b32 v253, v209, v207, v6\n" \
  "ds_read2st64_b32 v[200:201], v28 offset0:17 offset1:16\n" \
  "ds_read2st64_b32 v[202:203], v29 offset0:17 offset1:16\n" \
  "ds_read2st64_b32 v[204:205], v30 offset0:17 offset1:16\n" \
  "ds_read2st64_b32 v[206:207], v31 offset0:17 offset1:16\n" \
  "ds_read2st64_b32 v[208:209], v32 offset0:17 offset1:16\n" \
  "ds_read2st64_b32 v[210:211], v28 offset0:15 offset1:14\n" \
  "ds_read2st64_b32 v[212:213], v29 offset0:15 offset1:14\n" \
  "ds_read2st64_b32 v[214:215], v30 offset0:15 offset1:14\n" \
  "ds_read2st64_b32 v[216:217], v31 offset0:15 offset1:14\n" \
  "ds_read2st64_b32 v[218:219], v32 offset0:15 offset1:14\n" \
  "v_and_b32 v250, v250, v8\n" \
  "v_and_b32 v251, v251, v9\n" \
  "v_and_b32 v252, v252, v10\n" \
  "v_and_b32 v253, v253, v11\n" \
  "s_bitcmp1_b32 s62, 1\n" \
  "s_cbranch_scc1 .LVA_QH_%=\n" \
  "s_bitcmp1_b32 s62, 0\n" \
  "s_cbranch_scc1 .LVA_QB_%=\n" \
  ".LVA_QA_%=:\n" \
  "v_mfma_f32_16x16x32_bf16 v[64:67], v[128:131], v[246:249], v[68:71]\n" \
  "v_mfma_f32_16x16x32_bf16 v[68:71], v[132:135], v[246:249], v[72:75]\n" \
  "v_mfma_f32_16x16x32_bf16 v[72:75], v[136:139], v[246:249], v[76:79]\n" \
  "v_mfma_f32_16x16x32_bf16 v[76:79], v[140:143], v[246:249], v[80:83]\n" \
  "s_cmp_ge_u32 s62, 48\n" \
  "s_cbranch_scc1 .LVA_EA_%=\n" \
  "v_mfma_f32_16x16x32_bf16 v[80:83], v[144:147], v[246:249], v[84:87]\n" \
  "v_mfma_f32_16x16x32_bf16 v[84:87], v[148:151], v[246:249], v[88:91]\n" \
  "v_mfma_f32_16x16x32_bf16 v[88:91], v[152:155], v[246:249], v[92:95]\n" \
  "v_mfma_f32_16x16x32_bf16 v[92:95], v[156:159], v[246:249], v[96:99]\n" \
  "s_cmp_ge_u32 s62, 32\n" \
  "s_cbranch_scc1 .LVA_EA_%=\n" \
  "v_mfma_f32_16x16x32_bf16 v[96:99], v[160:163], v[246:249], v[100:103]\n" \
  "v_mfma_f32_16x16x32_bf16 v[100:103], v[164:167], v[246:249], v[104:107]\n" \
  "v_mfma_f32_16x16x32_bf16 v[104:107], v[168:171], v[246:249], v[108:111]\n" \
  "v_mfma_f32_16x16x32_bf16 v[108:111], v[172:175], v[246:249], v[112:115]\n" \
  "s_cmp_ge_u32 s62, 16\n" \
  "s_cbranch_scc1 .LVA_EA_%=\n" \
  "v_mfma_f32_16x16x32_bf16 v[112:115], v[176:179], v[246:249], v[116:119]\n" \
  "v_mfma_f32_16x16x32_bf16 v[116:119], v[180:183], v[246:249], v[120:123]\n" \
  "v_mfma_f32_16x16x32_bf16 v[120:123], v[184:187], v[246:249], v[124:127]\n" \
  "v_mfma_f32_16x16x32_bf16 v[124:127], v[188:191], v[246:249], 0\n" \
  ".LVA_EA_%=:\n" \
  "s_nop 1\n" \
  "v_mfma_f32_16x16x32_bf16 v[20:23], v[192:195], v[250:253], v[64:67]\n" \
  "s_nop 7\n" \
  "v_permlane32_swap_b32 v22, v23\n" \
  "v_add_f32 v20, v22, v23\n" \
  "s_branch .LVA_PD_%=\n" \
  ".LVA_QB_%=:\n" \
  "v_mfma_f32_16x16x32_bf16 v[64:67], v[128:131], v[246:249], v[64:67]\n" \
  "v_mfma_f32_16x16x32_bf16 v[68:71], v[132:135], v[246:249], v[68:71]\n" \
  "v_mfma_f32_16x16x32_bf16 v[72:75], v[136:139], v[246:249], v[72:75]\n" \
  "v_mfma_f32_16x16x32_bf16 v[76:79], v[140:143], v[246:249], v[76:79]\n" \
  "s_cmp_ge_u32 s62, 48\n" \
  "s_cbranch_scc1 .LVA_EB_%=\n" \
  "v_mfma_f32_16x16x32_bf16 v[80:83], v[144:147], v[246:249], v[80:83]\n" \
  "v_mfma_f32_16x16x32_bf16 v[84:87], v[148:151], v[246:249], v[84:87]\n" \
  "v_mfma_f32_16x16x32_bf16 v[88:91], v[152:155], v[246:249], v[88:91]\n" \
  "v_mfma_f32_16x16x32_bf16 v[92:95], v[156:159], v[246:249], v[92:95]\n" \
  "s_cmp_ge_u32 s62, 32\n" \
  "s_cbranch_scc1 .LVA_EB_%=\n" \
  "v_mfma_f32_16x16x32_bf16 v[96:99], v[160:163], v[246:249], v[96:99]\n" \
  "v_mfma_f32_16x16x32_bf16 v[100:103], v[164:167], v[246:249], v[100:103]\n" \
  "v_mfma_f32_16x16x32_bf16 v[104:107], v[168:171], v[246:249], v[104:107]\n" \
  "v_mfma_f32_16x16x32_bf16 v[108:111], v[172:175], v[246:249], v[108:111]\n" \
  "s_cmp_ge_u32 s62, 16\n" \
  "s_cbranch_scc1 .LVA_EB_%=\n" \
  "v_mfma_f32_16x16x32_bf16 v[112:115], v[176:179], v[246:249], v[112:115]\n" \
  "v_mfma_f32_16x16x32_bf16 v[116:119], v[180:183], v[246:249], v[116:119]\n" \
  "v_mfma_f32_16x16x32_bf16 v[120:123], v[184:187], v[246:249], v[120:123]\n" \
  "v_mfma_f32_16x16x32_bf16 v[124:127], v[188:191], v[246:249], v[124:127]\n" \
  ".LVA_EB_%=:\n" \
  "s_nop 1\n" \
  "v_mfma_f32_16x16x32_bf16 v[20:23], v[192:195], v[250:253], v[64:67]\n" \
  "s_not_b64 s[72:73], s[72:73]\n" \
  "s_nop 6\n" \
  "v_permlane32_swap_b32 v20, v21\n" \
  "v_add_f32 v20, v20, v21\n" \
  "s_branch .LVA_PD_%=\n" \
  ".LVA_QH_%=:\n" \
  "s_bitcmp1_b32 s62, 0\n" \
  "s_cbranch_scc1 .LVA_QD_%=\n" \
  ".LVA_QC_%=:\n" \
  "v_mfma_f32_16x16x32_bf16 v[64:67], v[128:131], v[246:249], v[64:67]\n" \
  "v_mfma_f32_16x16x32_bf16 v[68:71], v[132:135], v[246:249], v[68:71]\n" \
  "v_mfma_f32_16x16x32_bf16 v[72:75], v[136:139], v[246:249], v[72:75]\n" \
  "v_mfma_f32_16x16x32_bf16 v[76:79], v[140:143], v[246:249], v[76:79]\n" \
  "s_cmp_ge_u32 s62, 48\n" \
  "s_cbranch_scc1 .LVA_EC_%=\n" \
  "v_mfma_f32_16x16x32_bf16 v[80:83], v[144:147], v[246:249], v[80:83]\n" \
  "v_mfma_f32_16x16x32_bf16 v[84:87], v[148:151], v[246:249], v[84:87]\n" \
  "v_mfma_f32_16x16x32_bf16 v[88:91], v[152:155], v[246:249], v[88:91]\n" \
  "v_mfma_f32_16x16x32_bf16 v[92:95], v[156:159], v[246:249], v[92:95]\n" \
  "s_cmp_ge_u32 s62, 32\n" \
  "s_cbranch_scc1 .LVA_EC_%=\n" \
  "v_mfma_f32_16x16x32_bf16 v[96:99], v[160:163], v[246:249], v[96:99]\n" \
  "v_mfma_f32_16x16x32_bf16 v[100:103], v[164:167], v[246:249], v[100:103]\n" \
  "v_mfma_f32_16x16x32_bf16 v[104:107], v[168:171], v[246:249], v[104:107]\n" \
  "v_mfma_f32_16x16x32_bf16 v[108:111], v[172:175], v[246:249], v[108:111]\n" \
  "s_cmp_ge_u32 s62, 16\n" \
  "s_cbranch_scc1 .LVA_EC_%=\n" \
  "v_mfma_f32_16x16x32_bf16 v[112:115], v[176:179], v[246:249], v[112:115]\n" \
  "v_mfma_f32_16x16x32_bf16 v[116:119], v[180:183], v[246:249], v[116:119]\n" \
  "v_mfma_f32_16x16x32_bf16 v[120:123], v[184:187], v[246:249], v[120:123]\n" \
  "v_mfma_f32_16x16x32_bf16 v[124:127], v[188:191], v[246:249], v[124:127]\n" \
  ".LVA_EC_%=:\n" \
  "s_nop 1\n" \
  "v_mfma_f32_16x16x32_bf16 v[20:23], v[192:195], v[250:253], v[64:67]\n" \
  "s_nop 7\n" \
  "v_permlane32_swap_b32 v22, v23\n" \
  "v_add_f32 v20, v22, v23\n" \
  "s_branch .LVA_PD_%=\n" \
  ".LVA_QD_%=:\n" \
  "v_mfma_f32_16x16x32_bf16 v[64:67], v[128:131], v[246:249], v[64:67]\n" \
  "v_mfma_f32_16x16x32_bf16 v[68:71], v[132:135], v[246:249], v[68:71]\n" \
  "v_mfma_f32_16x16x32_bf16 v[72:75], v[136:139], v[246:249], v[72:75]\n" \
  "v_mfma_f32_16x16x32_bf16 v[76:79], v[140:143], v[246:249], v[76:79]\n" \
  "s_cmp_ge_u32 s62, 48\n" \
  "s_cbranch_scc1 .LVA_ED_%=\n" \
  "v_mfma_f32_16x16x32_bf16 v[80:83], v[144:147], v[246:249], v[80:83]\n" \
  "v_mfma_f32_16x16x32_bf16 v[84:87], v[148:151], v[246:249], v[84:87]\n" \
  "v_mfma_f32_16x16x32_bf16 v[88:91], v[152:155], v[246:249], v[88:91]\n" \
  "v_mfma_f32_16x16x32_bf16 v[92:95], v[156:159], v[246:249], v[92:95]\n" \
  "s_cmp_ge_u32 s62, 32\n" \
  "s_cbranch_scc1 .LVA_ED_%=\n" \
  "v_mfma_f32_16x16x32_bf16 v[96:99], v[160:163], v[246:249], v[96:99]\n" \
  "v_mfma_f32_16x16x32_bf16 v[100:103], v[164:167], v[246:249], v[100:103]\n" \
  "v_mfma_f32_16x16x32_bf16 v[104:107], v[168:171], v[246:249], v[104:107]\n" \
  "v_mfma_f32_16x16x32_bf16 v[108:111], v[172:175], v[246:249], v[108:111]\n" \
  "s_cmp_ge_u32 s62, 16\n" \
  "s_cbranch_scc1 .LVA_ED_%=\n" \
  "v_mfma_f32_16x16x32_bf16 v[112:115], v[176:179], v[246:249], v[112:115]\n" \
  "v_mfma_f32_16x16x32_bf16 v[116:119], v[180:183], v[246:249], v[116:119]\n" \
  "v_mfma_f32_16x16x32_bf16 v[120:123], v[184:187], v[246:249], v[120:123]\n" \
  "v_mfma_f32_16x16x32_bf16 v[124:127], v[188:191], v[246:249], v[124:127]\n" \
  ".LVA_ED_%=:\n" \
  "s_nop 1\n" \
  "v_mfma_f32_16x16x32_bf16 v[20:23], v[192:195], v[250:253], v[68:71]\n" \
  "s_not_b64 s[72:73], s[72:73]\n" \
  "s_nop 6\n" \
  "v_permlane32_swap_b32 v20, v21\n" \
  "v_add_f32 v20, v20, v21\n" \
  ".LVA_PD_%=:\n" \
  "s_cmp_lg_u32 s63, 0\n" \
  "s_cbranch_scc1 .LVA_NOF_%=\n" \
  "v_lshlrev_b32 v56, 6, v7\n" \
  "s_waitcnt vmcnt(0)\n" \
  "s_mov_b64 exec, 15\n" \
  "global_store_dword v56, v5, s[58:59] sc1\n" \
  "s_mov_b64 exec, -1\n" \
  "s_add_u32 s58, s58, 4096\n" \
  "s_addc_u32 s59, s59, 0\n" \
  ".LVA_NOF_%=:\n" \
  "s_add_u32 s74, s62, 12\n" \
  "s_lshl_b32 s76, s63, 1\n" \
  "s_and_b32 s76, s76, 12\n" \
  "s_xor_b32 s74, s74, s76\n" \
  "s_and_b32 s79, s74, 15\n" \
  "s_sub_u32 s74, s62, 4\n" \
  "s_cmp_lt_u32 s74, 48\n" \
  "s_cselect_b32 s79, s79, 1\n" \
  "s_cmp_lg_u32 s79, 0\n" \
  "s_cbranch_scc1 .LVA_NOS_%=\n" \
  "ds_read_u16 v230, v35 offset:0\n" \
  "ds_read_u16 v231, v35 offset:784\n" \
  "ds_read_u16 v232, v35 offset:1568\n" \
  "ds_read_u16 v233, v35 offset:2352\n" \
  "ds_read_u16 v234, v35 offset:3136\n" \
  "ds_read_u16 v235, v35 offset:3920\n" \
  "ds_read_u16 v236, v35 offset:4704\n" \
  "ds_read_u16 v237, v35 offset:5488\n" \
  "v_lshlrev_b32 v238, 16, v44\n" \
  "v_and_b32 v239, 0xffff0000, v44\n" \
  "v_lshlrev_b32 v240, 16, v45\n" \
  "v_and_b32 v241, 0xffff0000, v45\n" \
  "v_lshlrev_b32 v242, 16, v46\n" \
  "v_and_b32 v243, 0xffff0000, v46\n" \
  "v_lshlrev_b32 v244, 16, v47\n" \
  "v_and_b32 v245, 0xffff0000, v47\n" \
  "v_mov_b64 v[44:45], v[48:49]\n" \
  "v_mov_b64 v[46:47], v[50:51]\n" \
  "v_mov_b64 v[48:49], v[52:53]\n" \
  "v_mov_b64 v[50:51], v[54:55]\n" \
  "s_waitcnt lgkmcnt(0)\n" \
  "v_lshlrev_b32 v230, 16, v230\n" \
  "v_lshlrev_b32 v231, 16, v231\n" \
  "v_lshlrev_b32 v232, 16, v232\n" \
  "v_lshlrev_b32 v233, 16, v233\n" \
  "v_lshlrev_b32 v234, 16, v234\n" \
  "v_lshlrev_b32 v235, 16, v235\n" \
  "v_lshlrev_b32 v236, 16, v236\n" \
  "v_lshlrev_b32 v237, 16, v237\n" \
  "v_pk_add_f32 v[230:231], v[230:231], v[238:239]\n" \
  "v_pk_add_f32 v[232:233], v[232:233], v[240:241]\n" \
  "v_pk_add_f32 v[234:235], v[234:235], v[242:243]\n" \
  "v_pk_add_f32 v[236:237], v[236:237], v[244:245]\n" \
  "v_cvt_pk_bf16_f32 v56, v230, v231\n" \
  "v_cvt_pk_bf16_f32 v57, v232, v233\n" \
  "v_cvt_pk_bf16_f32 v58, v234, v235\n" \
  "v_cvt_pk_bf16_f32 v59, v236, v237\n" \
  "buffer_store_dwordx4 v[56:59], v36, s[52:55], s68 offen sc1\n" \
  "s_add_u32 s68, s68, 0x80000\n" \
  ".LVA_NOS_%=:\n" \
  "s_cmp_ge_u32 s62, 48\n" \
  "s_cbranch_scc1 .LVA_FIN_%=\n" \
  "s_lshr_b32 s78, s62, 2\n" \
  "s_and_b32 s74, s62, 3\n" \
  "ds_read2st64_b32 v[220:221], v28 offset0:13 offset1:12\n" \
  "ds_read2st64_b32 v[222:223], v29 offset0:13 offset1:12\n" \
  "ds_read2st64_b32 v[224:225], v30 offset0:13 offset1:12\n" \
  "ds_read2st64_b32 v[226:227], v31 offset0:13 offset1:12\n" \
  "ds_read2st64_b32 v[228:229], v32 offset0:13 offset1:12\n" \
  "s_waitcnt lgkmcnt(10)\n" \
  "v_alignbit_b32 v230, v202, v200, v6\n" \
  "v_alignbit_b32 v231, v204, v202, v6\n" \
  "v_alignbit_b32 v232, v206, v204, v6\n" \
  "v_alignbit_b32 v233, v208, v206, v6\n" \
  "v_alignbit_b32 v234, v203, v201, v6\n" \
  "v_alignbit_b32 v235, v205, v203, v6\n" \
  "v_alignbit_b32 v236, v207, v205, v6\n" \
  "v_alignbit_b32 v237, v209, v207, v6\n" \
  "s_cbranch_scc1 .LVA_NOZ_%=\n" \
  "v_mfma_f32_16x16x32_bf16 v[196:199], v[24:27], v[230:233], 0\n" \
  ".LVA_NOZ_%=:\n" \
  "s_nop 0\n" \
  "v_mfma_f32_16x16x32_bf16 v[196:199], v[128:131], v[234:237], v[196:199]\n" \
  "ds_read2st64_b32 v[200:201], v28 offset0:11 offset1:10\n" \
  "ds_read2st64_b32 v[202:203], v29 offset0:11 offset1:10\n" \
  "ds_read2st64_b32 v[204:205], v30 offset0:11 offset1:10\n" \
  "ds_read2st64_b32 v[206:207], v31 offset0:11 offset1:10\n" \
  "ds_read2st64_b32 v[208:209], v32 offset0:11 offset1:10\n" \
  "s_waitcnt lgkmcnt(10)\n" \
  "v_alignbit_b32 v238, v212, v210, v6\n" \
  "v_alignbit_b32 v239, v214, v212, v6\n" \
  "v_alignbit_b32 v240, v216, v214, v6\n" \
  "v_alignbit_b32 v241, v218, v216, v6\n" \
  "v_alignbit_b32 v242, v213, v211, v6\n" \
  "v_alignbit_b32 v243, v215, v213, v6\n" \
  "v_alignbit_b32 v244, v217, v215, v6\n" \
  "v_alignbit_b32 v245, v219, v217, v6\n" \
  "v_mfma_f32_16x16x32_bf16 v[196:199], v[132:135], v[238:241], v[196:199]\n" \
  "s_nop 0\n" \
  "v_mfma_f32_16x16x32_bf16 v[196:199], v[136:139], v[242:245], v[196:199]\n" \
  "ds_read2st64_b32 v[210:211], v28 offset0:9 offset1:8\n" \
  "ds_read2st64_b32 v[212:213], v29 offset0:9 offset1:8\n" \
  "ds_read2st64_b32 v[214:215], v30 offset0:9 offset1:8\n" \
  "ds_read2st64_b32 v[216:217], v31 offset0:9 offset1:8\n" \
  "ds_read2st64_b32 v[218:219], v32 offset0:9 offset1:8\n" \
  "s_waitcnt lgkmcnt(10)\n" \
  "v_alignbit_b32 v230, v222, v220, v6\n" \
  "v_alignbit_b32 v231, v224, v222, v6\n" \
  "v_alignbit_b32 v232, v226, v224, v6\n" \
  "v_alignbit_b32 v233, v228, v226, v6\n" \
  "v_alignbit_b32 v234, v223, v221, v6\n" \
  "v_alignbit_b32 v235, v225, v223, v6\n" \
  "v_alignbit_b32 v236, v227, v225, v6\n" \
  "v_alignbit_b32 v237, v229, v227, v6\n" \
  "v_mfma_f32_16x16x32_bf16 v[196:199], v[140:143], v[230:233], v[196:199]\n" \
  "s_nop 0\n" \
  "v_mfma_f32_16x16x32_bf16 v[196:199], v[144:147], v[234:237], v[196:199]\n" \
  "s_cmp_eq_u32 s78, 0\n" \
  "s_cbranch_scc1 .LVA_OUT_%=\n" \
  "ds_read2st64_b32 v[220:221], v28 offset0:7 offset1:6\n" \
  "ds_read2st64_b32 v[222:223], v29 offset0:7 offset1:6\n" \
  "ds_read2st64_b32 v[224:225], v30 offset0:7 offset1:6\n" \
  "ds_read2st64_b32 v[226:227], v31 offset0:7 offset1:6\n" \
  "ds_read2st64_b32 v[228:229], v32 offset0:7 offset1:6\n" \
  "s_waitcnt lgkmcnt(10)\n" \
  "v_alignbit_b32 v238, v202, v200, v6\n" \
  "v_alignbit_b32 v239, v204, v202, v6\n" \
  "v_alignbit_b32 v240, v206, v204, v6\n" \
  "v_alignbit_b32 v241, v208, v206, v6\n" \
  "v_alignbit_b32 v242, v203, v201, v6\n" \
  "v_alignbit_b32 v243, v205, v203, v6\n" \
  "v_alignbit_b32 v244, v207, v205, v6\n" \
  "v_alignbit_b32 v245, v209, v207, v6\n" \
  "v_mfma_f32_16x16x32_bf16 v[196:199], v[148:151], v[238:241], v[196:199]\n" \
  "s_cmp_eq_u32 s78, 1\n" \
  "s_cbranch_scc1 .LVA_OUT_%=\n" \
  "v_mfma_f32_16x16x32_bf16 v[196:199], v[152:155], v[242:245], v[196:199]\n" \
  "s_cmp_eq_u32 s78, 2\n" \
  "s_cbranch_scc1 .LVA_OUT_%=\n" \
  "ds_read2st64_b32 v[200:201], v28 offset0:5 offset1:4\n" \
  "ds_read2st64_b32 v[202:203], v29 offset0:5 offset1:4\n" \
  "ds_read2st64_b32 v[204:205], v30 offset0:5 offset1:4\n" \
  "ds_read2st64_b32 v[206:207], v31 offset0:5 offset1:4\n" \
  "ds_read2st64_b32 v[208:209], v32 offset0:5 offset1:4\n" \
  "s_waitcnt lgkmcnt(10)\n" \
  "v_alignbit_b32 v230, v212, v210, v6\n" \
  "v_alignbit_b32 v231, v214, v212, v6\n" \
  "v_alignbit_b32 v232, v216, v214, v6\n" \
  "v_alignbit_b32 v233, v218, v216, v6\n" \
  "v_alignbit_b32 v234, v213, v211, v6\n" \
  "v_alignbit_b32 v235, v215, v213, v6\n" \
  "v_alignbit_b32 v236, v217, v215, v6\n" \
  "v_alignbit_b32 v237, v219, v217, v6\n" \
  "v_mfma_f32_16x16x32_bf16 v[196:199], v[156:159], v[230:233], v[196:199]\n" \
  "s_cmp_eq_u32 s78, 3\n" \
  "s_cbranch_scc1 .LVA_OUT_%=\n" \
  "v_mfma_f32_16x16x32_bf16 v[196:199], v[160:163], v[234:237], v[196:199]\n" \
  "s_cmp_eq_u32 s78, 4\n" \
  "s_cbranch_scc1 .LVA_OUT_%=\n" \
  "ds_read2st64_b32 v[210:211], v28 offset0:3 offset1:2\n" \
  "ds_read2st64_b32 v[212:213], v29 offset0:3 offset1:2\n" \
  "ds_read2st64_b32 v[214:215], v30 offset0:3 offset1:2\n" \
  "ds_read2st64_b32 v[216:217], v31 offset0:3 offset1:2\n" \
  "ds_read2st64_b32 v[218:219], v32 offset0:3 offset1:2\n" \
  "s_waitcnt lgkmcnt(10)\n" \
  "v_alignbit_b32 v238, v222, v220, v6\n" \
  "v_alignbit_b32 v239, v224, v222, v6\n" \
  "v_alignbit_b32 v240, v226, v224, v6\n" \
  "v_alignbit_b32 v241, v228, v226, v6\n" \
  "v_alignbit_b32 v242, v223, v221, v6\n" \
  "v_alignbit_b32 v243, v225, v223, v6\n" \
  "v_alignbit_b32 v244, v227, v225, v6\n" \
  "v_alignbit_b32 v245, v229, v227, v6\n" \
  "v_mfma_f32_16x16x32_bf16 v[196:199], v[164:167], v[238:241], v[196:199]\n" \
  "s_cmp_eq_u32 s78, 5\n" \
  "s_cbranch_scc1 .LVA_OUT_%=\n" \
  "v_mfma_f32_16x16x32_bf16 v[196:199], v[168:171], v[242:245], v[196:199]\n" \
  "s_cmp_eq_u32 s78, 6\n" \
  "s_cbranch_scc1 .LVA_OUT_%=\n" \
  "ds_read2st64_b32 v[220:221], v28 offset0:1 offset1:0\n" \
  "ds_read2st64_b32 v[222:223], v29 offset0:1 offset1:0\n" \
  "ds_read2st64_b32 v[224:225], v30 offset0:1 offset1:0\n" \
  "ds_read2st64_b32 v[226:227], v31 offset0:1 offset1:0\n" \
  "ds_read2st64_b32 v[228:229], v32 offset0:1 offset1:0\n" \
  "s_waitcnt lgkmcnt(10)\n" \
  "v_alignbit_b32 v230, v202, v200, v6\n" \
  "v_alignbit_b32 v231, v204, v202, v6\n" \
  "v_alignbit_b32 v232, v206, v204, v6\n" \
  "v_alignbit_b32 v233, v208, v206, v6\n" \
  "v_alignbit_b32 v234, v203, v201, v6\n" \
  "v_alignbit_b32 v235, v205, v203, v6\n" \
  "v_alignbit_b32 v236, v207, v205, v6\n" \
  "v_alignbit_b32 v237, v209, v207, v6\n" \
  "v_mfma_f32_16x16x32_bf16 v[196:199], v[172:175], v[230:233], v[196:199]\n" \
  "s_cmp_eq_u32 s78, 7\n" \
  "s_cbranch_scc1 .LVA_OUT_%=\n" \
  "v_mfma_f32_16x16x32_bf16 v[196:199], v[176:179], v[234:237], v[196:199]\n" \
  "s_cmp_eq_u32 s78, 8\n" \
  "s_cbranch_scc1 .LVA_OUT_%=\n" \
  "s_waitcnt lgkmcnt(5)\n" \
  "v_alignbit_b32 v238, v212, v210, v6\n" \
  "v_alignbit_b32 v239, v214, v212, v6\n" \
  "v_alignbit_b32 v240, v216, v214, v6\n" \
  "v_alignbit_b32 v241, v218, v216, v6\n" \
  "v_alignbit_b32 v242, v213, v211, v6\n" \
  "v_alignbit_b32 v243, v215, v213, v6\n" \
  "v_alignbit_b32 v244, v217, v215, v6\n" \
  "v_alignbit_b32 v245, v219, v217, v6\n" \
  "v_mfma_f32_16x16x32_bf16 v[196:199], v[180:183], v[238:241], v[196:199]\n" \
  "s_cmp_eq_u32 s78, 9\n" \
  "s_cbranch_scc1 .LVA_OUT_%=\n" \
  "v_mfma_f32_16x16x32_bf16 v[196:199], v[184:187], v[242:245], v[196:199]\n" \
  "s_cmp_eq_u32 s78, 10\n" \
  "s_cbranch_scc1 .LVA_OUT_%=\n" \
  "s_waitcnt lgkmcnt(0)\n" \
  "v_alignbit_b32 v230, v222, v220, v6\n" \
  "v_alignbit_b32 v231, v224, v222, v6\n" \
  "v_alignbit_b32 v232, v226, v224, v6\n" \
  "v_alignbit_b32 v233, v228, v226, v6\n" \
  "s_nop 1\n" \
  "v_mfma_f32_16x16x32_bf16 v[196:199], v[188:191], v[230:233], v[196:199]\n" \
  ".LVA_OUT_%=:\n" \
  "v_add_u32 v28, 64, v28\n" \
  "v_add_u32 v29, 64, v29\n" \
  "v_add_u32 v30, 64, v30\n" \
  "v_add_u32 v31, 64, v31\n" \
  "v_add_u32 v32, 64, v32\n" \
  "s_cmp_lg_u32 s74, 3\n" \
  "s_cbranch_scc1 .LVA_FIN_%=\n" \
  "ds_read_u16 v56, v33 offset:0\n" \
  "ds_read_u16 v57, v33 offset:64\n" \
  "v_add_u32 v33, 256, v33\n" \
  "v_permlane32_swap_b32 v196, v197\n" \
  "v_permlane32_swap_b32 v198, v199\n" \
  "v_add_f32 v196, v196, v197\n" \
  "v_add_f32 v198, v198, v199\n" \
  "s_waitcnt lgkmcnt(0)\n" \
  "v_lshlrev_b32 v56, 16, v56\n" \
  "v_lshlrev_b32 v57, 16, v57\n" \
  "v_mul_f32 v56, s64, v56\n" \
  "v_mul_f32 v57, s64, v57\n" \
  "v_cvt_pk_bf16_f32 v197, v196, v198\n" \
  "v_cvt_pk_bf16_f32 v57, v56, v57\n" \
  "v_lshlrev_b32 v196, 16, v197\n" \
  "v_and_b32 v197, 0xffff0000, v197\n" \
  "v_lshlrev_b32 v56, 16, v57\n" \
  "v_and_b32 v57, 0xffff0000, v57\n" \
  "v_add_f32 v56, v56, v196\n" \
  "v_add_f32 v57, v57, v197\n" \
  "v_cvt_pk_bf16_f32 v58, v56, v57\n" \
  "ds_write_b16 v34, v58\n" \
  "ds_write_b16_d16_hi v34, v58 offset:64\n" \
  ".LVA_FIN_%=:\n" \
  "s_cmp_lg_u32 s79, 0\n" \
  "s_cbranch_scc1 .LVA_NOG_%=\n" \
  "v_lshlrev_b32 v60, 6, v7\n" \
  "s_waitcnt vmcnt(0)\n" \
  "s_mov_b64 exec, 255\n" \
  "global_store_dword v60, v5, s[60:61] sc1\n" \
  "s_mov_b64 exec, -1\n" \
  "s_add_u32 s60, s60, 0x10000\n" \
  "s_addc_u32 s61, s61, 0\n" \
  ".LVA_NOG_%=:\n" \
  "s_add_u32 s62, s62, 1\n" \
  "s_cmp_eq_u32 s62, 64\n" \
  "s_cbranch_scc1 .LVA_END_%=\n" \
  ".LVA_TAPS_%=:\n" \
  "s_and_b32 s74, s62, 3\n" \
  "s_lshl_b32 s76, s74, 6\n" \
  "v_add_u32 v56, s76, v5\n" \
  "s_cmp_eq_u32 s74, 3\n" \
  "s_cselect_b32 s74, 0, 256\n" \
  "v_add_u32 v57, s74, v56\n" \
  "ds_read_b128 v[128:131], v56 offset:3840\n" \
  "ds_read_b128 v[132:135], v56 offset:3584\n" \
  "ds_read_b128 v[136:139], v56 offset:3328\n" \
  "ds_read_b128 v[140:143], v56 offset:3072\n" \
  "ds_read_b128 v[192:195], v57 offset:3648\n" \
  "s_cmp_ge_u32 s62, 48\n" \
  "s_cbranch_scc1 .LVA_TOP_%=\n" \
  "ds_read_b128 v[144:147], v56 offset:2816\n" \
  "ds_read_b128 v[148:151], v56 offset:2560\n" \
  "ds_read_b128 v[152:155], v56 offset:2304\n" \
  "ds_read_b128 v[156:159], v56 offset:2048\n" \
  "ds_read_b128 v[160:163], v56 offset:1792\n" \
  "ds_read_b128 v[164:167], v56 offset:1536\n" \
  "ds_read_b128 v[168:171], v56 offset:1280\n" \
  "ds_read_b128 v[172:175], v56 offset:1024\n" \
  "ds_read_b128 v[176:179], v56 offset:768\n" \
  "ds_read_b128 v[180:183], v56 offset:512\n" \
  "ds_read_b128 v[184:187], v56 offset:256\n" \
  "ds_read_b128 v[188:191], v56 offset:0\n" \
  "s_branch .LVA_TOP_%=\n" \
  ".LVA_END_%=:\n" \
  "s_endpgm\n"
__device__ __forceinline__ v4u mk_srd(const void* p, const unsigned bytes) {
  const unsigned long long a = (unsigned long long)p;
  return v4u{(unsigned)a, (unsigned)(a >> 32) & 0xffffu, bytes, 0x00020000u};
}
template <bool ATT>
__device__ __forceinline__ void role_chain_t(const MegaArgs& A, char* smem, const int L, const int h, const int rep) {
  __builtin_amdgcn_s_waitcnt(0x0F70);
  u16* swo = reinterpret_cast<u16*>(smem);
  const int w = __builtin_amdgcn_readfirstlane(threadIdx.x >> 6);
  const int wc = (w + ROT) & 7, psw = PSWH;
  char* ws = A.ws;
  unsigned* FL = (unsigned*)(ws + OFF_FL);
  unsigned* TMO = FL + FL_TMO;
#define LANE_IDS()                         \
  const int l = lane_id();                 \
  const int c = l & 15, g = l >> 4;        \
  (void)c; (void)g;
  float* sgm = reinterpret_cast<float*>(smem + C_X);
  float* sgf = sgm + 8;
  unsigned* FQ = FL + FL_FQ + L * 65 * 32 * 32;
  v8u w0[8], w1[8], w2[8];
  PSV;
  {
    LANE_IDS();
    v4u m0r;
    const unsigned sb = lds_base(smem);
    const int q = (l >> 2) & 3, p = l & 3;
    const int f = (q & 1) | ((g & 1) << 1) | ((q >> 1) << 2);
    const unsigned ra = sb + (g >> 1) * 16384 + (4 * (g & 1) + (q & 1) + 8 * (q >> 1)) * 256 + (w ^ f) * 32 + p * 8;
#define WP(k) (WM(k) == 0 ? A.Wv : WM(k) == 1 ? A.Wk : A.Wq)
#define WC(k) (WJ(k) ^ WXC)
#define W_READ(W, X)                                                          \
  {                                                                           \
    const unsigned a_ = (ra ^ (X)) + j * 4096 + i * 32768;                    \
    const v2u x0 = trd(a_), x1 = trd(a_ + 512);                               \
    W[4 * WC(k) + i] = v4u{x0.x, x0.y, x1.x, x1.y};                           \
  }
    v4u u0[16], u1[16], u2[16], zt;
    v2u ze;
    float f2[4] = {};
    if (CSL && L) gate(now10() + CSL);
    PST(0);
    asm volatile("s_nop 4\n\tglobal_load_dwordx4 %0, %1, %2" : "=v"(m0r) : "v"(((2 * (c >> 2) + (c & 1)) * DM + 64 * w + 16 * g + 8 * (((c >> 1) ^ (c >> 3)) & 1)) * 2), "s"(A.mem0) : "memory");
#pragma unroll
    for (int k = 0; k < 12; ++k) w_issue<true>(WP(k >> 2), WC(k >> 2), k >> 2, k & 3, h, w, l, sb);
#pragma unroll
    for (int k = 0; k < 13; ++k) {
      const int j = k & 3;
      if (k == ZC + 1) wait_vm<10>();
      else if (k < 12 || ZC == 10) wait_vm<8>();
      if (CTH && L) __builtin_amdgcn_s_sleep(CTH);
      RAW_BAR();
      if (k == 0) {
        asm volatile("" : "+v"(m0r));
        *reinterpret_cast<v4u*>(smem + C_SM + w * 1024 + l * 16) = m0r;
      }
      if (k == ZC)
        asm volatile("s_nop 4\n\tglobal_load_dwordx4 %0, %2, %3 sc1\n\tglobal_load_dwordx2 %1, %4, %5 sc1"
                     : "=&v"(zt), "=&v"(ze)
                     : "v"(l * 16), "s"(ws + OFF_TAG + (blockIdx.x & 7) * 1024), "v"(0), "s"(ws + OFF_TAG + 8192)
                     : "memory");
#pragma unroll
      for (int i = 0; i < 4; ++i) {
        if (k < 9) w_issue<true>(WP(k + 3), WC(k + 3), (k + 3) & 3, i, h, w, l, sb);
        else {
          const int jo = k - 9, fd = (i & 1) | ((jo & 1) << 1) | ((w & 1) << 2);
          glds16((const char*)A.Wo + (size_t)(h * DH + 64 * (jo >> 1) + 8 * w + 4 * (jo & 1)) * (DM * 2), (unsigned)(i * 1024 + ((l ^ (fd << 1)) << 4)),
                 sb + w * 16384 + jo * 4096 + i * 1024);
        }
        if (k < 12 && WM(k) == (ATT ? 0 : 2)) { W_READ(u1, 0); W_READ(u2, 128); }
        if (k < 12 && WM(k) == 1) W_READ(u0, 0);
        if (ATT && k > 0 && WM(k - 1) == 0) {
#pragma unroll
          for (int e = 0; e < 4; ++e) {
            f2[e & 1] = dot2sq(u1[4 * WC(k - 1) + i][e], f2[e & 1]);
            f2[2 + (e & 1)] = dot2sq(u2[4 * WC(k - 1) + i][e], f2[2 + (e & 1)]);
          }
          asm volatile("" : "+v"(f2[0]), "+v"(f2[1]), "+v"(f2[2]), "+v"(f2[3]));
        }
      }
      if (k == (WORD ? 10 : 4)) {
        const float f = wave_sum_dpp((f2[0] + f2[1]) + (f2[2] + f2[3]));
        if (l == 0) { sgf[w] = f; sgm[16] = 0.f; sgm[32 + w] = 0.f; }
      }
      if (k == (WORD ? 11 : ZC + 2)) {
        asm volatile("" : "+v"(zt), "+v"(ze));
        if (w == 0) {
          if (!zero_seen(zt, ze)) zero_wait(ws);
          const float fs = (sgf[0] + sgf[1]) + (sgf[2] + sgf[3]);
          __builtin_amdgcn_raw_buffer_store_b32(__float_as_uint(fs) | 1u, MKRS(FL + FL_G, L ? 0 : 4 * 512), l * 512 + h * 4, 0, AUX_SC1);
        }
      }
      if (k == 1) PST(1);
      if (k == 3) PST(2);
      if (k == 5) PST(3);
      if (k == 7) PST(4);
      if (k == 9) PST(5);
      if (k == 11) PST(6);
    }
#undef W_READ
#undef WP
#undef WC
#pragma unroll
    for (int t = 0; t < 8; ++t) {
      const v8u a0 = __builtin_shufflevector(u0[2 * t], u0[2 * t + 1], 0, 1, 2, 3, 4, 5, 6, 7), a1 = __builtin_shufflevector(u1[2 * t], u1[2 * t + 1], 0, 1, 2, 3, 4, 5, 6, 7);
      w0[t] = ATT ? a0 : a1;
      w1[t] = ATT ? a1 : a0;
      w2[t] = __builtin_shufflevector(u2[2 * t], u2[2 * t + 1], 0, 1, 2, 3, 4, 5, 6, 7);
    }
    const unsigned rb = sb + (2 * (g >> 1) + (q >> 1)) * 16384 + (4 * (g & 1) + (q & 1)) * 1024 + p * 8;
#pragma unroll
    for (int hf = 0; hf < 2; ++hf) {
      if (hf == 0) WAIT_VM(8); else WAIT_VM(0);
      RAW_BAR();
      v4u fr[8];
#pragma unroll
      for (int i = 0; i < 8; ++i) {
        const unsigned a_ = rb + hf * 8192 + (i & 1) * 65536 + ((4 * wc + ((i >> 1) ^ psw)) ^ f) * 32;
        const v2u r0 = trd(a_), r1 = trd(a_ + 2048);
        fr[i] = v4u{r0.x, r0.y, r1.x, r1.y};
      }
#pragma unroll
      for (int i = 0; i < 8; ++i) asm volatile("" : "+v"(fr[i]));
      RAW_BAR();
#pragma unroll
      for (int i = 0; i < 8; ++i) *reinterpret_cast<v4u*>(swo + w * 8192 + hf * 4096 + i * 512 + l * 8) = fr[i];
    }
  }
  PST(7);
  WSTAMP2();
  const unsigned sb = lds_base(smem);
  const char* gK = ws + OFF_QK + L * SZ_QK + ((size_t)h * 16 + 2 * w) * 1024;
  const char* gV = ws + OFF_VF + L * SZ_O + ((size_t)h * 8 + 2 * w - 8) * 1024;
  float gw2;
  {
    const gu32* gp = (gu32*)(FL + FL_G + REP * 128) + (lane_id() & 31);
    unsigned g0, g1, g2;
    for (unsigned spins = 0;; ++spins) {
      g0 = __hip_atomic_load(gp, RLX_AGENT);
      g1 = __hip_atomic_load(gp + 32, RLX_AGENT);
      g2 = __hip_atomic_load(gp + 64, RLX_AGENT);
      if (__all(g0 != 0u && g1 != 0u && g2 != 0u)) break;
      if (spin_timeout(spins, TMO)) return;
      __builtin_amdgcn_s_sleep(4);
    }
    const float gm = wave_max_dpp(__uint_as_float(g0) * (__uint_as_float(g1) + __uint_as_float(g2)));
    asm volatile("s_nop 0\n\tv_readfirstlane_b32 %0, %1" : "=s"(gw2) : "v"(gm * 1.07f));
  }
  PST(8);
  v8u pv;
  {
    LANE_IDS();
    const unsigned voff = g * 256 + c * 8, wp = sb + w * 16384 + l * 16;
    pv[0] = (((c >> 1) ^ (c >> 3)) & 1) ? 0xEEEE : 0x4444;
    pv[1] = sb + C_SM + l * 16;
    pv[2] = ATT ? sb + ((c & 2) ? C_SK : C_SQ) + ((2 * (c >> 2) + (c & 1)) * QS + 8 * g) * 2 : voff;
    pv[3] = ATT ? l * 8 : wp;
    pv[4] = sb + ((c & 2) ? C_SQ : C_SO) + w * 256 + ((c & 13) + ((g ^ (c >> 2)) & 2)) * 16 + 8 * ((g ^ (c >> 1)) & 1);
    pv[5] = voff;
    pv[6] = wp;
    pv[7] = sb + C_SO + (l >> 5) * 1024 + w * 256 + (l & 31) * 8;
  }
  DRAIN();
  PST(10);
  PSD();
  SST(8);
  const v4u rsA = mk_srd(ws + OFF_ACC + L * SZ_ACC, SZ_ACC);
  const v4u rsF = mk_srd(FQ, 64 * 32 * 32 * 4);
  const v4u rsMK = mk_srd(ws + OFF_MEMK + L * SZ_MEM, SZ_MEM);
  const v4u rsMV = mk_srd(ws + OFF_MEMV + L * SZ_MEM, SZ_MEM);
  const int wr = (w + RR) & 7;
  const char* pAB = ws + OFF_ACC + L * SZ_ACC + (wc - wr) * 4096;
  const char* pGV = ATT ? (const char*)(size_t)(CDS + (XG(h) >> 1) * CDL) : gV;
  unsigned sc[8];
  sc[0] = ATT ? L * 1024 + XG(h) * 256 : h * 128;
  sc[1] = !ATT ? (h * 4 + w - 4) * 512 : (h & 7) == 1 ? L * 1024 + (h >> 3) * 256 + 128 : 1032 - L * 1024 + h * 4096;
  sc[2] = h * 2048 + w * 512;
  sc[3] = NSEG - 1;
  sc[4] = __float_as_uint(gw2);
  sc[5] = w;
  sc[6] = sb;
  const char* dbg = ws;
  const unsigned tg = (now10() / GR) * GR;
  const v4u rsT = mk_srd(FL + FL_TK, 2048);
#define CHAIN_OPS                                                                                                          \
  ::"{v[64:71]}"(w0[0]), "{v[72:79]}"(w0[1]), "{v[80:87]}"(w0[2]), "{v[88:95]}"(w0[3]), "{v[96:103]}"(w0[4]),              \
      "{v[104:111]}"(w0[5]), "{v[112:119]}"(w0[6]), "{v[120:127]}"(w0[7]), "{v[128:135]}"(w1[0]), "{v[136:143]}"(w1[1]),    \
      "{v[144:151]}"(w1[2]), "{v[152:159]}"(w1[3]), "{v[160:167]}"(w1[4]), "{v[168:175]}"(w1[5]), "{v[176:183]}"(w1[6]),    \
      "{v[184:191]}"(w1[7]), "{v[192:199]}"(w2[0]), "{v[200:207]}"(w2[1]), "{v[208:215]}"(w2[2]), "{v[216:223]}"(w2[3]),    \
      "{v[224:231]}"(w2[4]), "{v[232:239]}"(w2[5]), "{v[240:247]}"(w2[6]), "{v[248:255]}"(w2[7]), "{v[0:7]}"(pv),          \
      "{s[40:43]}"(rsA), "{s[44:47]}"(rsF), "{s[48:51]}"(rsMK), "{s[52:55]}"(rsMV), "{s[56:57]}"(pAB), "{s60}"(wr * 4096), "{s61}"(h * 1024 - wr * 4096), \
      "{s[62:63]}"(gK), "{s[64:65]}"(pGV), "{s68}"(sc[0]), "{s69}"(sc[1]), "{s70}"(sc[2]),    \
      "{s71}"(sc[3]), "{s72}"(sc[4]), "{s73}"(sc[5]), "{s74}"(sc[6]), "{s[36:37]}"(dbg),                   \
      "{s34}"(tg), "{s35}"(psw * 1024), [gp] "n"(GP), [gr] "n"(GR), [gm] "n"(GM), \
      "{s[16:19]}"(rsT), "{s20}"(0), "{s22}"(sb + C_X + 128 + 8 * (w & 3)), "{s24}"(L * 1024), [cd] "n"(CD - CDS), \
      "{s25}"(APA - (APN + 1) * APP + L * (APB - APA + (2 * APN + 1) * APP)), [ka] "n"(APL), [an] "n"(APN), [d0] "n"(16 * CD0), [d1] "n"(CD1), [g1] "n"(63 * APP + CG1), [sf] "n"(SLP_1), [rq] "n"(RQM), [pp] "n"(APP) \
      : "memory", "vcc", "scc", "s99", "v63"
  if (ATT) asm volatile("s_mov_b64 exec, -1\ns_nop 4\n" CHAIN_ASM_ATT ".Lcode_end:\n" CHAIN_OPS);
  else asm volatile("s_mov_b64 exec, -1\ns_nop 4\n" CHAIN_ASM_VO CHAIN_OPS);
#undef CHAIN_OPS
#undef LANE_IDS
}

__device__ __forceinline__ void role_chain(const MegaArgs& A, char* smem, const int L, const int h, const int rep) {
  if (__builtin_amdgcn_readfirstlane(threadIdx.x >> 6) < 4) role_chain_t<true>(A, smem, L, h, rep);
  else role_chain_t<false>(A, smem, L, h, rep);
}

constexpr int AW_TK = 0, AW_SO = 114688, AW_M = AW_SO + 16384, AW_P = 24576, AW_MK = AW_P + 2048, AW_CNT = AW_SO + AW_MK + 4096;
__device__ __forceinline__ unsigned umin3(unsigned a, unsigned b, unsigned c) {
  return __builtin_elementwise_min(__builtin_elementwise_min(a, b), c);
}
__device__ __forceinline__ bool all_set(const unsigned r) {
  unsigned long long m;
  asm volatile("v_cmp_eq_u32 %0, 0, %1" : "=s"(m) : "v"(r) : "memory");
  return m == 0ull;
}
__device__ __forceinline__ unsigned long long zero_mask(const unsigned r) {
  unsigned long long m;
  asm volatile("v_cmp_eq_u32 %0, 0, %1" : "=s"(m) : "v"(r) : "memory");
  return m;
}
__device__ __forceinline__ int job_seg(const int n) {
  const int m = n - AWD;
  const int r = (m & 1) ? 64 + (m >> 1) : AWD + (m >> 1);
  return __builtin_amdgcn_readfirstlane(n > 127 ? 128 : (n < AWD || n >= 128 - AWD) ? n : r);
}
template <bool AT>
__device__ __forceinline__ void role_aw_t(const MegaArgs& A, char* smem, const int mode, const int a) {
  __builtin_amdgcn_s_waitcnt(0x0F70);
  const int w = __builtin_amdgcn_readfirstlane(threadIdx.x >> 6);
  const int l = threadIdx.x & 63, c = l & 15, g = l >> 4;
  const int qt = w & 1, hh = (w >> 1) & 1, h = 2 * a + hh;
  char* ws = A.ws;
  unsigned* FL = (unsigned*)(ws + OFF_FL);
  const unsigned sb = lds_base(smem);
  v4u wo[4][8];
  PSV;
  PST(0);
  if (threadIdx.x < 4) ((lu32*)(unsigned long)(sb + AW_CNT))[threadIdx.x] = 0u;
  const unsigned* FQ = FL + FL_FQ + 1024 + h * 32 + 16;
  const v4u rsW = mk_srd(ws + OFF_PW, SZ_PW);
  const v4u rsF = mk_srd(FQ, (129 * 32 - h) * 128 - 64);
  const char* gQ = ws + OFF_QK + ((size_t)h * 16 + 8 * qt) * 1024;
  const char* gV = ws + OFF_VF + ((size_t)h * 8 + 4 * qt) * 1024;
  const char* gM = ws + (qt ? OFF_MEMV : OFF_MEMK) + h * 2048;
  const unsigned mo = sb + AW_M + hh * 4096;
  const unsigned tq0 = __builtin_amdgcn_readfirstlane(sb + AW_TK + (hh * 24 + 8 * qt) * 1024);
  const unsigned tv0 = __builtin_amdgcn_readfirstlane(sb + AW_TK + (hh * 24 + 16 + 4 * qt) * 1024);
  const int gs = job_seg(mode);
  bool got = false;
  {
    const int q = (l >> 2) & 3, p = l & 3, f = q | ((g & 1) << 2);
    const unsigned ra = sb + (4 * g + q) * 256 + (p & 1) * 16;
    unsigned n0 = 0u;
#pragma unroll
    for (int st = 0; st < 4; ++st) {
      if (st == 3 && got) WAIT_VM(12); else if (st == 3) WAIT_VM(0); else WAIT_VM(8);
      RAW_BAR();
#pragma unroll
      for (int k2 = 0; k2 < 2; ++k2) {
        const int ks = 2 * st + k2;
#pragma unroll
        for (int ct = 0; ct < 4; ++ct) {
          const int cp = 2 * w + (ct >> 1);
          const unsigned a_ = ra + (st & 1) * 65536 + (cp >> 2) * 16384 + k2 * 4096 + ((2 * (cp & 3) + (p >> 1)) ^ f) * 32 + (ct & 1) * 8;
          const v2u lo = trd(a_), hi = trd(a_ + 8192);
          wo[ct][ks] = v4u{lo.x, hi.x, lo.y, hi.y};
        }
#pragma unroll
        for (int ct = 0; ct < 4; ++ct) asm volatile("" : "+v"(wo[ct][ks]));
      }
      RAW_BAR();
      SST(4 + st);
      if (st == 0) PST(1); if (st == 1) PST(2); if (st == 2) PST(3); if (st == 3) PST(4);
      if (!AT && st == 1)
        asm volatile("s_nop 4\n\tbuffer_load_dword %0, %1, %2, %3 offen sc1" : "=v"(n0) : "v"(0), "s"(rsF), "s"(gs * 4096) : "memory");
      if (st < 2) wo_issue(A.Wo, a, st + 2, sb);
      if (!AT && st == 2 && all_set(n0)) {
#pragma unroll
        for (int i = 0; i < 8; ++i) glds16c(gQ + (size_t)gs * (NH * 16 * 1024) + i * 1024, l * 16, tq0 + i * 1024);
#pragma unroll
        for (int i = 0; i < 4; ++i) glds16c(gV + (size_t)gs * (NH * 8 * 1024) + i * 1024, l * 16, tv0 + i * 1024);
        got = true;
      }
    }
  }
  if (got) PST(11);
  PSD();
  v8u pv, mk, wr[16];
  {
    const unsigned so = sb + AW_SO + l * 16, ta = sb + AW_TK + hh * 24576 + l * 16;
    const unsigned ix = (c & 2) ? 0xEEEE : 0x4444;
    pv = v8u{AT ? ix : l * 16u, so, (unsigned)(HK3 ? (l >> 3) * 2048 + (l & 7) * 16 : (c >> 1) * 2048 + (c & 1) * 64 + g * 16), AT ? ta : mo + qt * 2048 + l * 16, AT ? ta + qt * 4096 : ix,
             mo + (g * 8 + 2 * (c >> 2) + (c & 1)) * 16, sb + AW_M + 2048 + (g >> 1) * 512 + (c & 3) * 128 + (8 * (g & 1) + (c >> 2)) * 8,
             so + (hh * 2 + qt) * 4096};
#pragma unroll
    for (int i = 0; i < 8; ++i) mk[i] = 16 * (i >> 2) + 4 * g + (i & 3) > 16 * qt + c ? __float_as_uint(-3e38f) : 0u;
#pragma unroll
    for (int i = 0; i < 16; ++i) wr[i] = __builtin_shufflevector(wo[i >> 2][2 * (i & 3)], wo[i >> 2][2 * (i & 3) + 1], 0, 1, 2, 3, 4, 5, 6, 7);
  }
  const char* dbg = ws;
#define AW_OPS                                                                                                               \
  ::"{v[128:135]}"(wr[0]), "{v[136:143]}"(wr[1]), "{v[144:151]}"(wr[2]), "{v[152:159]}"(wr[3]), "{v[160:167]}"(wr[4]),        \
      "{v[168:175]}"(wr[5]), "{v[176:183]}"(wr[6]), "{v[184:191]}"(wr[7]), "{v[192:199]}"(wr[8]), "{v[200:207]}"(wr[9]),      \
      "{v[208:215]}"(wr[10]), "{v[216:223]}"(wr[11]), "{v[224:231]}"(wr[12]), "{v[232:239]}"(wr[13]), "{v[240:247]}"(wr[14]), \
      "{v[248:255]}"(wr[15]), "{v[0:7]}"(pv), "{v[8:15]}"(mk), "{s[40:43]}"(rsW), "{s[44:47]}"(rsF), "{s[48:49]}"(gQ),        \
      "{s[50:51]}"(gV), "{s[52:53]}"(gM), "{s[54:55]}"(FL + FL_FW + (a >> 2) * 32 + (a & 3)), "{s56}"(mode), "{s57}"(gs),      \
      "{s60}"(HK2 ? (a >> 1) * 32768 + w * 256 + (a & 1) * 128 : a * 32768 + w * 256), "{s[92:95]}"(mk_srd(ws, 0)), "{s61}"(AT ? AW_MK + qt * 2048 : tq0), "{s64}"(AT ? 0 : sb + AW_CNT + 8), "{s65}"(sb + AW_CNT + hh * 4), "{s62}"(tv0), "{s63}"(AT ? mo + 2048 - (sb + AW_SO) / 2 : mo + qt * 2048), "{s[84:87]}"(mk_srd(FL + FL_TK + (a >> 2) * 64 + 32, 1028)), "{s[88:89]}"(ws + WS_TOTAL + (28 << 20) + 1024 + qt * 4),            \
      "{s66}"(AT ? AW_P + qt * 1024 + hh * 4 : HK2 ? (a >> 1) * 32768 + (w - 4) * 256 + (a & 1) * 128 : a * 32768 + (w - 4) * 8192), "{s[82:83]}"(w == 4 ? 15ull : 0ull), "{s67}"(__builtin_amdgcn_readfirstlane((int)got)), "{s80}"(0x3e0293ee), "{s[36:37]}"(dbg),        \
      [wd] "n"(AWD), [w2] "n"(128 - 2 * AWD), [w3] "n"(131 - AWD), [dt] "n"(AWDT), [pp] "n"(APP), [mo] "n"(AWMO - 63 * APP), [sm] "n"(SLP_M), [sa] "n"(SLP_A) \
      : "memory", "vcc", "scc", "s81", "v127"
  if (AT) asm volatile("s_mov_b64 exec, -1\ns_nop 4\n" AW_ASM_0 AW_OPS);
  else asm volatile("s_mov_b64 exec, -1\ns_nop 4\n" AW_ASM_1 AW_OPS);
#undef AW_OPS
}
__device__ __forceinline__ void role_aw(const MegaArgs& A, char* smem, const int L, const int a) {
  if (__builtin_amdgcn_readfirstlane(threadIdx.x >> 6) < 4) role_aw_t<true>(A, smem, L, a);
  else role_aw_t<false>(A, smem, L, a);
}

__device__ __forceinline__ void role_red(const MegaArgs& A, char* smem, const int idx) {
  const int w = __builtin_amdgcn_readfirstlane(threadIdx.x >> 6);
  const int l = threadIdx.x & 63;
  const int L = w >> 2, gr = (w >> 1) & 1, r = idx * 2 + (w & 1);
  char* ws = A.ws;
  unsigned* FL = (unsigned*)(ws + OFF_FL);
  unsigned* TMO = FL + FL_TMO;
  const __amdgpu_buffer_rsrc_t rsW = MKRS(ws + OFF_PW, SZ_PW);
  const __amdgpu_buffer_rsrc_t rsX = MKRS(ws + OFF_XA, OFF_X3 - OFF_XA + SZ_X);
  volatile lu32* sm32 = (lu32*)(unsigned long)lds_base(smem);
  volatile lu32* sw = sm32 + (w >> 1) * 32;
  if (threadIdx.x < 128) sm32[threadIdx.x] = 0u;
  __syncthreads();
  unsigned tb = now10() + (L * (APN + 2) + 1 + gr) * GPER;
  const int rn1 = idx < 8 ? 96 + idx : 224 + idx;
  (void)rn1;
  if (L) {
    if (RSL) gate(now10() + RSL);
    const __amdgpu_buffer_rsrc_t rz = MKRS(ws + OFF_X2, SZ_X);
    for (int s = gr; s < NSEG; s += 2) ST128((v4u{0u, 0u, 0u, 0u}), rz, l * 16, (s * SEG + r) * DM * 2);
    DRAIN();
    if (l < 4) set_flag(FL + FL_FZ + (2 * l + (idx >> 3)) * 32 + (idx & 7) * 4 + (w & 3));
    if (l == 0) __hip_atomic_fetch_add((lu32*)(unsigned long)(lds_base(smem) + 480), 1u, __ATOMIC_RELAXED, __HIP_MEMORY_SCOPE_WORKGROUP);
  }
  for (unsigned spins = 0; sm32[120] < 4u && spins < 64u * SPIN_CAP; ++spins) __builtin_amdgcn_s_sleep(2);
  const int c = l & 15, g = l >> 4;
  const int pv = g * 32768 + (c >> 2) * 128 + (c & 3) * 16, xv = (16 * (g >> 1) + c) * 512 + (g & 1) * 8, ov = (g >> 1) * 256 + c * 16 + (g & 1) * 8;
  v4u sel0, sel1;
#pragma unroll
  for (int e = 0; e < 4; ++e) {
    const unsigned one = ((c & 7) >> 1) == e ? 0x3f80u << (16 * (c & 1)) : 0u;
    sel0[e] = c < 8 ? one : 0u;
    sel1[e] = c < 8 ? 0u : one;
  }
  int tries = SPIN_CAP;
  for (int s = gr; s < NSEG; s += 2) {
    const int gs = 64 * L + s;
    const unsigned* fw = FL + FL_FW + (gs * 4 + REP) * 4 * 32;
    const unsigned due = grp_wait<SLP_R, 4>((w & 1) == 0, sw, s + 1, fw, 16, TMO, tb);
    tb = due + (gs >= 125 - AWD || gs < RQ0 ? GATE_RT : 2 * GPER - (L ? GMS : GMR));
    {
      const unsigned t = now10(), e = (int)(due - t) > 0 ? due : t;
      __builtin_amdgcn_raw_buffer_store_b32((e + RDT) | 1u, MKRS(FL + FL_TW, r == 2 * RTW && !L ? 2048 : 0), l * 512, s * 4, AUX_SC1);
    }
#define RTS(k, t) TSR(L ? A.rn : rn1, k, s, (w & 6) * 64 + (t))
    RST(0);
    RTS(0, 0);
    RTS(4, 64);
    const int xo_ = L * (int)(OFF_X3 - OFF_XA) + s * 32768 + r * 16;
    const v2u xa = LD64(rsX, xv, xo_), xb = LD64(rsX, xv + 16384, xo_);
#pragma unroll 1
    for (int i = 0; i < 256; ++i) {
      const int d = (int)(due - now10());
      if (d <= 0) break;
      if (d > 8) __builtin_amdgcn_s_sleep(1);
    }
    if (L) {
      const unsigned g0 = sm32[124];
#pragma unroll 1
      for (int i = 0; i < RGW && g0 < NSEG && sm32[124] == g0; ++i) __builtin_amdgcn_s_sleep(1);
    }
    RST(1);
    f32x4 a0, a1;
    for (;; --tries) {
      v4u p[16];
#pragma unroll
      for (int k = 0; k < 16; ++k) {
        p[k] = LD128(rsW, pv + ((k & 1) + (k < 8 ? 2 : 0)) * 512, (gs * 16 + 2 * (k & 6)) * 32768 + (r >> 1) * 2048 + (r & 1) * 64);
        if ((k & 7) == 7) __builtin_amdgcn_sched_barrier(0);
      }
      if ((w & 5) == 1 && l == 0) __hip_atomic_fetch_max((lu32*)(unsigned long)(lds_base(smem) + 496), s + 1u, __ATOMIC_RELAXED, __HIP_MEMORY_SCOPE_WORKGROUP);
      __builtin_amdgcn_sched_barrier(0);
      a0 = f32x4{};
      a1 = f32x4{};
#pragma unroll
      for (int k = 0; k < 8; ++k) a1 = MFMA16(BF8((k & 1) ? sel1 : sel0), BF8(p[k]), a1);
#pragma unroll
      for (int k = 8; k < 16; ++k) a0 = MFMA16(BF8((k & 1) ? sel1 : sel0), BF8(p[k]), a0);
      if (!__any(__builtin_isunordered(a0[0], a1[0]) | __builtin_isunordered(a0[1], a1[1]) | __builtin_isunordered(a0[2], a1[2]) |
                 __builtin_isunordered(a0[3], a1[3])) || tries <= 0) break;
      asm volatile("s_sleep %0" ::"n"(SLP_P) : "memory");
    }
    RST(2);
    const unsigned nz = L ? 0u : 1u;
    const v4u xr = {xa.x, xa.y, xb.x, xb.y}, ar = {pk2(a0[0], a0[1]), pk2(a0[2], a0[3]), pk2(a1[0], a1[1]), pk2(a1[2], a1[3])};
    v4u xo;
#pragma unroll
    for (int e = 0; e < 4; ++e)
      xo[e] = __builtin_elementwise_max(pk2(lo16(xr[e]) + lo16(ar[e]), hi16(xr[e]) + hi16(ar[e])), nz);
    const __amdgpu_buffer_rsrc_t rsO = MKRS(L ? (char*)A.out : ws + OFF_X2, SZ_X);
    ST64((v2u{xo.x, xo.y}), rsO, ov, (s * SEG + r) * DM * 2);
    ST64((v2u{xo.z, xo.w}), rsO, ov + 512, (s * SEG + r) * DM * 2);
    RTS(1, 0);
    RST(3);
    RTS(5, 64);
    if (L == 0) {
      float sm = 0.f, sq = 0.f;
#pragma unroll
      for (int e = 0; e < 4; ++e) {
        const unsigned x = xo[e];
        sm = __builtin_amdgcn_fdot2_f32_bf16(__builtin_bit_cast(bf16x2_t, x), __builtin_bit_cast(bf16x2_t, 0x3f803f80u), sm, false);
        sq = dot2sq(x, sq);
      }
      float t = swap_add32(sm, sq);
      t += dppr<0x121>(t);
      t += dppr<0x122>(t);
      t += dppr<0x124>(t);
      t += dppr<0x128>(t);
      t = xsum16(t) * (1.0f / DM);
      const float ms = __builtin_bit_cast(float, __builtin_amdgcn_readlane(__builtin_bit_cast(int, t), 32));
      const float var = fmaxf(ms - t * t, 0.f);
      const unsigned st = pk2(t, __builtin_amdgcn_sqrtf(lo16(pk2(lo16(pk2(var, var)) + 1e-5f, 0.f))));
      if (l < 4) __hip_atomic_store((gu32*)(FL + FL_FR + ((s * 4 + l) * 4 + (r >> 3)) * 32 + (r & 7)), st, RLX_AGENT);
    }
    RTS(6, 64);
    RST(4);
    RTS(3, 0);
#undef RTS
  }
  RSD();
  if (idx == 0 && threadIdx.x == 384) {
    gu64* en = (gu64*)(ws + OFF_TAG + 8192);
    __hip_atomic_store(en, __hip_atomic_load(en, RLX_AGENT) + TAG_STEP, RLX_AGENT);
  }
}

constexpr int CUN = 2144;
constexpr int STN = 392;
__device__ __forceinline__ bf16x8 conv_bfrag(const unsigned* up, const unsigned sh) {
  const unsigned d0 = up[0], d1 = up[1], d2 = up[2], d3 = up[3], d4 = up[4];
  const v4u r = {__builtin_amdgcn_alignbit(d1, d0, sh), __builtin_amdgcn_alignbit(d2, d1, sh),
                 __builtin_amdgcn_alignbit(d3, d2, sh), __builtin_amdgcn_alignbit(d4, d3, sh)};
  return BF8(r);
}
__device__ __forceinline__ void role_conv(const MegaArgs& A, char* smem, const int cv) {
  __builtin_amdgcn_s_waitcnt(0x0F70);
  u16* Kr = reinterpret_cast<u16*>(smem);
  u16* Uc = Kr + 8 * 2 * CKN;
  u16* To = Uc + 8 * 2 * CUN;
  u16* Sx = To + 32 * 8;
  u16* St = Sx + 32 * 8;
  const int tid = threadIdx.x;
  const int w = __builtin_amdgcn_readfirstlane(tid >> 6);
  const int l = tid & 63, c = l & 15, g = l >> 4;
  const int d = 8 * cv + w;
  char* ws = A.ws;
  unsigned* FL = (unsigned*)(ws + OFF_FL);
  unsigned* TMO = FL + FL_TMO;
  u16* kr = Kr + w * 2 * CKN;
  float* prm = reinterpret_cast<float*>(kr);
  v4u tx[4];
  LST(16);
  const v2u en = LD64(MKRS(ws + OFF_TAG, 8192 + 8), 0, 8192);
  {
    const unsigned pa = *reinterpret_cast<const unsigned*>(A.logAre + d * NST + 2 * l);
    const unsigned pb = *reinterpret_cast<const unsigned*>(A.Aim + d * NST + 2 * l);
    const unsigned pc = *reinterpret_cast<const unsigned*>(A.Cre + d * NST + 2 * l);
    const unsigned pd = *reinterpret_cast<const unsigned*>(A.Cim + d * NST + 2 * l);
    const float dt = bfr(expf(bf2f(A.logdt[d])));
    if (w < 2) tx[0] = *reinterpret_cast<const v4u*>(A.x0 + (size_t)tid * DM + 8 * cv);
    if (l < 16) reinterpret_cast<unsigned*>(Uc + w * CUN)[l] = 0u;
    if (l < 32) reinterpret_cast<unsigned*>(Uc + w * CUN + 32 + L_SEQ)[l] = 0u;
    if (l < 32) reinterpret_cast<unsigned*>(Uc + w * CUN + 32 + 128)[l] = 0u;
    if (l < 50) {
      reinterpret_cast<v4u*>(kr + 1808)[l] = v4u{0u, 0u, 0u, 0u};
      reinterpret_cast<v4u*>(kr + CKN + 1808)[l] = v4u{0u, 0u, 0u, 0u};
    }
#pragma unroll
    for (int e = 0; e < 2; ++e) {
      const float ar = -bfr(expf(e ? hi16(pa) : lo16(pa))) * dt, ai = (e ? hi16(pb) : lo16(pb)) * dt;
      const float ex = expf(ar);
      float sn, cs;
      sincos1(ai, &sn, &cs);
      prm[2 * l + e] = ar;
      prm[NST + 2 * l + e] = ai;
      prm[2 * NST + 2 * l + e] = (e ? hi16(pc) : lo16(pc)) * dt;
      prm[3 * NST + 2 * l + e] = (e ? hi16(pd) : lo16(pd)) * dt;
      prm[4 * NST + 2 * l + e] = ex * cs;
      prm[5 * NST + 2 * l + e] = ex * sn;
    }
  }
  LST(21);
  const float dsk = bf2f(A.Dskip[d]);
  const u16* ka = kr + (c >> 3) * CKN + 2080 - 16 * (c & 7) + 8 * g;
  const unsigned ush = (c & 1) * 16;
#define KFRAG(D0) (*reinterpret_cast<const bf16x8*>(ka - (D0)))
  const __amdgpu_buffer_rsrc_t rz1 = MKRS(ws + OFF_XA, SZ_X);
  const int t0 = 16 * (4 * (g & 1) + (g >> 1)) + c;
  {
    const u16* uc = Uc + w * CUN;
    const unsigned* ub = reinterpret_cast<const unsigned*>(uc + (c & ~1) + 8 * g + 32);
    const __amdgpu_buffer_rsrc_t rst = MKRS(ws + OFF_GT, L_SEQ * 4);
#define TILE_OUT(dst, av, T)                                                                   \
  {                                                                                            \
    const u16* u_ = Uc + w * CUN + 32 + 128 * (T) + t0;                                        \
    const unsigned uu = pk2(bf2f(u_[0]) * dsk, bf2f(u_[32]) * dsk);                            \
    const unsigned yy = pk2(swap_add32(av[0], av[1]), swap_add32(av[2], av[3]));               \
    const unsigned aa = pk2(lo16(yy) + lo16(uu), hi16(yy) + hi16(uu));                         \
    (dst)[t0] = (u16)aa;                                                                       \
    (dst)[t0 + 32] = (u16)(aa >> 16);                                                          \
  }
#define CHUNK_ST(ck, xr, src, n)                                                               \
  {                                                                                            \
    v4u o;                                                                                     \
    _Pragma("unroll") for (int e = 0; e < 4; ++e)                                              \
      o[e] = pk2(lo16(xr[e]) + bf2f((src)[(2 * e) * (n) + l]), hi16(xr[e]) + bf2f((src)[(2 * e + 1) * (n) + l])); \
    ST128(o, rz1, (l >> 5) * 32768 + (l & 31) * 16, (2 * (ck) * 64 + cv) * 512);               \
  }
#define U_ROW(row, xr_) U_ROW2(row, xr_, __builtin_amdgcn_raw_buffer_load_b32(rst, (row) * 4, 0, AUX_SC1))
#define U_ROW2(row, xr_, stv)                                                                       \
  {                                                                                            \
    const unsigned st_ = (stv);                                                                \
    const float mb_ = lo16(st_), ri_ = 1.0f / hi16(st_);                                       \
    _Pragma("unroll") for (int e = 0; e < 4; ++e) {                                            \
      const unsigned t_ = pk2(lo16(xr_[e]) - mb_, hi16(xr_[e]) - mb_);                         \
      const unsigned q_ = pk2(lo16(t_) * ri_, hi16(t_) * ri_);                                 \
      Uc[(2 * e) * CUN + 32 + (row)] = (u16)q_;                                                \
      Uc[(2 * e + 1) * CUN + 32 + (row)] = (u16)(q_ >> 16);                                    \
    }                                                                                          \
  }
    const unsigned* FS = FL + FL_FS + REP * 32 * 32;
    const int gi = 4 * (g * 8 + (l & 7));
    const int tl = 32 * ((l >> 3) & 1) + 8 * g + (l & 7);
    unsigned khl = 0u;
#pragma unroll 1
    for (int ps = 0; ps < 4; ++ps) {
      float v[32];
      {
        const f32x4 ar = *reinterpret_cast<const f32x4*>(prm + gi), ai = *reinterpret_cast<const f32x4*>(prm + NST + gi);
        const f32x4 cr = *reinterpret_cast<const f32x4*>(prm + 2 * NST + gi), ci = *reinterpret_cast<const f32x4*>(prm + 3 * NST + gi);
        const f32x4 wr = *reinterpret_cast<const f32x4*>(prm + 4 * NST + gi), wi = *reinterpret_cast<const f32x4*>(prm + 5 * NST + gi);
        const float p0 = (float)(64 * ps + 32 * ((l >> 3) & 1));
        float yr0[4], yi0[4];
#pragma unroll
        for (int k = 0; k < 4; ++k) {
          const float e = __expf(ar[k] * p0);
          float sn, cs;
          __sincosf(ai[k] * p0, &sn, &cs);
          const float zr = e * cs, zi = e * sn;
          yr0[k] = cr[k] * zr - ci[k] * zi;
          yi0[k] = cr[k] * zi + ci[k] * zr;
        }
        RUN_START
#pragma unroll
        for (int i = 0; i < 32; ++i) {
          const f32x2_t sm2 = ya + yb;
          v[i] = sm2.x + sm2.y;
          RUN_STEP
        }
      }
#pragma unroll
      for (int i = 0; i < 16; ++i) v[i] = swap_add32(v[i], v[i + 16]);
#pragma unroll
      for (int i = 0; i < 8; ++i) {
        v[i] = swap_add16(v[i], v[i + 8]);
        v[i] += dppf<0xB1>(0.f, v[i]);
        v[i] += dppf<0x4E>(0.f, v[i]);
        v[i] += dppf<0x141>(0.f, v[i]);
      }
      float s0 = v[0], s1 = v[1], s2 = v[2], s3 = v[3], s4 = v[4], s5 = v[5], s6 = v[6], s7 = v[7];
      asm volatile("" : "+v"(s0), "+v"(s1), "+v"(s2), "+v"(s3), "+v"(s4), "+v"(s5), "+v"(s6), "+v"(s7));
      s0 = (l & 4) ? s4 : s0; s1 = (l & 4) ? s5 : s1; s2 = (l & 4) ? s6 : s2; s3 = (l & 4) ? s7 : s3;
      s0 = (l & 2) ? s2 : s0; s1 = (l & 2) ? s3 : s1;
      const float kv = 2.0f * ((l & 1) ? s1 : s0);
      khl = pk2(kv, 0.f);
      khl = pk2(kv, kv - lo16(khl));
      kr[2080 - 64 * ps - tl] = (u16)khl;
      kr[CKN + 2080 - 64 * ps - tl] = (u16)(khl >> 16);
      SST(ps);
      LST(22 + 6 * ps);
      if (ps < 2) {
        if (ps == 0) {
          if (w < 2) {
            const unsigned long long m = (((unsigned long long)en.y << 32) | en.x) + TAG_STEP;
            v4u g0;
            for (unsigned spins = 0; spins < 16u * SPIN_CAP; ++spins) {
              g0 = LD128(MKRS(ws + OFF_G0, 8192), tid * 16, REP * 2048);
              if (__all(((g0.x ^ g0.z) | (g0.y ^ (unsigned)m) | (g0.w ^ (unsigned)(m >> 32))) == 0u)) break;
              asm volatile("s_sleep 2" ::: "memory");
            }
            LST(23);
            U_ROW2(tid, tx[0], g0.x);
          }
          __syncthreads();
          LST(24);
        }
        f32x4 a0t = {};
#pragma unroll
        for (int b = -1; b < 4; ++b) a0t = MFMA16(KFRAG(-32 * b), conv_bfrag(ub + 16 * b, ush), a0t);
        TILE_OUT(St + w * STN + 128 * ps, a0t, 0);
        __syncthreads();
        LST(25 + 6 * ps);
        if (w == ps) {
          CHUNK_ST(w, tx[0], St + 192 * w, STN);
          if (ps == 0) zero_wait(ws);
          LST(26 + 6 * ps);
          DRAIN();
          LST(27 + 6 * ps);
          if (l < 8) set_flag(FL + FL_FC + ((2 * w * 4 + l) * 8 + (cv >> 3)) * 32 + (cv & 7));
        }
        SST(4 + ps);
      }
      if (ps == 2) {
        if (w >= 2) tx[0] = *reinterpret_cast<const v4u*>(A.x0 + (size_t)tid * DM + 8 * cv);
#pragma unroll
        for (int i = 1; i < 4; ++i) tx[i] = *reinterpret_cast<const v4u*>(A.x0 + (size_t)(tid + 512 * i) * DM + 8 * cv);
      }
    }
    {
      unsigned spins = 0;
      while (!__all(__hip_atomic_load((gu32*)FS + (w + 8 * ((l >> 3) & 3)) * 32 + (l & 7), RLX_AGENT) != 0u)) {
        if (spin_timeout(++spins, TMO)) break;
        __builtin_amdgcn_s_sleep(4);
      }
      asm volatile("" ::: "memory");
    }
    if (w >= 2) U_ROW(tid, tx[0]);
#pragma unroll
    for (int i = 1; i < 4; ++i) U_ROW(tid + 512 * i, tx[i]);
    SST(6);
    LST(0);
    const gu32* kg = (gu32*)(FL + FL_KG + cv * 32 + (w >> 2));
    const __amdgpu_buffer_rsrc_t rk = MKRS(ws + OFF_KF + (size_t)d * 2 * CKN * 2, 2 * CKN * 2);
    v4u t[8];
#define K_LOAD(off)                                                                            \
  _Pragma("unroll") for (int i = 0; i < 4; ++i) {                                              \
    t[i] = LD128(rk, (64 * i + l) * 16, off);                                                  \
    t[4 + i] = LD128(rk, (64 * i + l) * 16, (off) + CKN * 2);                                  \
  }
    unsigned kf = __hip_atomic_load(kg, RLX_AGENT);
    RAW_BAR();
    LST(1);
    const bool early = __builtin_amdgcn_readfirstlane(kf) != 0u;
    K_LOAD(early ? 0 : 4 * CKN);
    kf = __hip_atomic_load(kg, RLX_AGENT);
    {
      f32x4 a1t = {};
#pragma unroll
      for (int b = -1; b < 8; ++b) a1t = MFMA16(KFRAG(128 - 32 * b), conv_bfrag(ub + 16 * b, ush), a1t);
      TILE_OUT(St + w * STN + 256, a1t, 1);
    }
    LST(2);
    RAW_BAR();
    if ((w >> 1) == 1) CHUNK_ST(w, tx[0], St + 128 + 64 * w, STN);
    SST(8);
    LST(3);
    if (!early) {
      if (__builtin_amdgcn_readfirstlane(kf) == 0u) (void)wait_n<8, 1>(FL + FL_KG + cv * 32 + (w >> 2), 1, TMO);
      asm volatile("" ::: "memory");
      K_LOAD(0);
    }
#undef K_LOAD
    SST(7);
    LST(4);
    {
#pragma unroll
      for (int i = 0; i < 4; ++i)
        if (64 * i + l < 229) {
          reinterpret_cast<v4u*>(kr)[64 * i + l] = t[i];
          reinterpret_cast<v4u*>(kr + CKN)[64 * i + l] = t[4 + i];
        }
      kr[2080 - 192 - tl] = (u16)khl;
      kr[CKN + 2080 - 192 - tl] = (u16)(khl >> 16);
    }
    unsigned* f1 = FL + FL_FC + (64 * w + (cv >> 3)) * 32 + (cv & 7);
    if ((w >> 1) == 1) {
      DRAIN();
      if (l < 8) set_flag(f1 + l * 256);
    }
    asm volatile("" ::: "memory");
    LST(5);
    {
      f32x4 a2 = {}, a3 = {};
#pragma unroll
      for (int b = -1; b < 16; ++b) {
        const bf16x8 bf = conv_bfrag(ub + 16 * b, ush);
        if (b < 12) a2 = MFMA16(KFRAG(256 - 32 * b), bf, a2);
        a3 = MFMA16(KFRAG(384 - 32 * b), bf, a3);
      }
      TILE_OUT(St + w * STN, a2, 2);
      TILE_OUT(St + w * STN + 128, a3, 3);
    }
    SST(10);
    LST(6);
    RAW_BAR();
    if (w >= 4) {
      CHUNK_ST(w, tx[0], St + 64 * w - 256, STN);
      DRAIN();
      if (l < 8) set_flag(f1 + l * 256);
    }
    if (w == 1) (void)(wait_n<SLP_V, 32>(FL + FL_FZ + REP * 64, 64, TMO));
    SST(9);
    LST(9);
    LSD();
#undef U_ROW
#undef U_ROW2
#undef TILE_OUT
#undef CHUNK_ST
  }
  const unsigned sb = lds_base(smem);
  const unsigned u1 = sb + (16 * CKN + w * CUN) * 2, u2 = u1 + 16 * CUN, to = sb + (16 * CKN + 16 * CUN) * 2, st = to + 1024;
  const unsigned win = ((c & ~1) + 8 * g) * 2, row = 16 * (g >> 1) + c;
  const bool nil = c < 8 && g < 2;
  if (l < 16) reinterpret_cast<unsigned*>(Uc + (8 + w) * CUN)[l] = 0u;
  bf16x8 th, tw;
#pragma unroll
  for (int i = 0; i < 8; ++i) {
    th[i] = (short)kr[2064 - c + 8 * g + i];
    tw[i] = (short)kr[CKN + 2064 - c + 8 * g + i];
  }
  v4u known;
#pragma unroll
  for (int e = 0; e < 4; ++e) known[e] = (c + 8 * g + 2 * e < 32 ? 0xffffu : 0u) | (c + 8 * g + 2 * e < 31 ? 0xffff0000u : 0u);
  const bf16x8 kz = KFRAG(-96);
  const v8u pa = {u2 + (nil ? 0 : c < 8 ? 32 + 16 * g : 64 + 16 * g), nil ? 0u : 64u, u2 + 64 + 2 * row, to + 16 * row + 2 * w, u2 + win,
                  sb + (unsigned)((w * 2 + (c >> 3)) * CKN + 2080 - 16 * (c & 7) + 8 * g) * 2 - 3904, ush, (unsigned)l * 16u};
  const unsigned b1 = u1 + win - 3072;
  const v8u pb = {b1, b1 + 4, b1 + 8, b1 + 12, b1 + 16, u1 + (32 + 512 + t0) * 2, st + (w * STN + t0) * 2, st + (64 * (w & 1) + l) * 2};
  const v4u pc = {(unsigned)((l >> 5) * 32768 + (l & 31) * 16), (unsigned)((l >> 1) * (DM * 2) + (l & 1) * 8), (unsigned)((l >> 4) * 128 + ((l >> 1) & 7) * 4),
                  sb + (16 * CKN + (8 + 4 * (l & 1)) * CUN + 32 + (l >> 1)) * 2};
  const char* dbg = ws;
  asm volatile("s_mov_b64 exec, -1\ns_nop 4\n" CONV_ASM
               ::"{v[0:7]}"(pa), "{v[8:11]}"(known), "{v[12:15]}"(th), "{v[16:19]}"(tw), "{v[24:27]}"(kz), "{v[28:35]}"(pb), "{v[36:39]}"(pc),
               "{v40}"(to + (w ? l * 8 : (l & 31) * 16)), "{v[44:47]}"(tx[1]), "{v[48:51]}"(tx[2]), "{v[52:55]}"(tx[3]),
               "{s[40:43]}"(mk_srd(ws + OFF_X2, SZ_X)), "{s[44:47]}"(mk_srd(FL + FL_FR, 64 * 16 * 128 + 2048)), "{s71}"(64 * 16 * 128 + REP * 512), "{s[48:51]}"(mk_srd(ws + OFF_X3, SZ_X)),
               "{s[52:55]}"(mk_srd(ws + OFF_XA, SZ_X)), "{s[58:59]}"(FL + FL_FC + (64 * 32 + (cv >> 3)) * 32 + (cv & 7)),
               "{s[60:61]}"(FL + FL_FC + (64 * (w + 8) + (cv >> 3)) * 32 + (cv & 7)), "{s62}"(0), "{s63}"(w),
               "{s64}"(__builtin_amdgcn_readfirstlane(__float_as_uint(dsk))), "{s65}"(cv * 16), "{s66}"(REP * 512), "{s67}"(cv * 512),
               "{s68}"((w + 8) * 65536 + cv * 512), "{s69}"(now10()), "{s[72:73]}"(0x0000ffff0000ffffull), "{s[36:37]}"(dbg),
               [lt] "n"(-CVLT), [gv] "n"(GATE_V), [sv] "n"(SLP_V)
               : "memory", "vcc", "scc", "s81", "v255");
#undef KFRAG
}

__device__ __forceinline__ v4u ln_row_packed(const v4u xr) {
  float v[8];
#pragma unroll
  for (int e = 0; e < 4; ++e) { v[2 * e] = lo16(xr[e]); v[2 * e + 1] = hi16(xr[e]); }
  float s = ((v[0] + v[1]) + (v[2] + v[3])) + ((v[4] + v[5]) + (v[6] + v[7]));
  s = wave_sum(s);
  const float mean = s * (1.0f / DM);
  float q = 0.f;
#pragma unroll
  for (int j = 0; j < 8; ++j) { float t = v[j] - mean; q += t * t; }
  q = wave_sum(q);
  const float var = q * (1.0f / DM);
  const float mb = bfr(mean);
  const float sd = bfr(sqrtf(bfr(bfr(var) + 1e-5f)));
  const float ri = 1.0f / sd;
  v4u o;
#pragma unroll
  for (int e = 0; e < 4; ++e) {
    const unsigned t = pk2(v[2 * e] - mb, v[2 * e + 1] - mb);
    const float x0 = lo16(t), x1 = hi16(t);
    float q0 = x0 * ri, q1 = x1 * ri;
    q0 = __builtin_fmaf(__builtin_fmaf(-q0, sd, x0), ri, q0);
    q1 = __builtin_fmaf(__builtin_fmaf(-q1, sd, x1), ri, q1);
    o[e] = pk2(q0, q1);
  }
  return o;
}

__device__ __forceinline__ float bfr1(float x) { return lo16(pk2(x, x)); }
__device__ __forceinline__ void ln4_packed(v4u (&x)[4]) {
  f32x2_t v[16];
#pragma unroll
  for (int e = 0; e < 16; ++e) v[e] = f32x2_t{lo16(x[e >> 2][e & 3]), hi16(x[e >> 2][e & 3])};
  const f32x2_t t = (((v[0] + v[1]) + (v[2] + v[3])) + ((v[4] + v[5]) + (v[6] + v[7]))) +
                    (((v[8] + v[9]) + (v[10] + v[11])) + ((v[12] + v[13]) + (v[14] + v[15])));
  const float mean = red_mod4(t.x + t.y) * (1.0f / DM);
  const float mb = bfr1(mean);
  f32x2_t q0 = {0.f, 0.f}, q1 = {0.f, 0.f};
#pragma unroll
  for (int e = 0; e < 16; e += 2) {
    v[e] -= mb;
    v[e + 1] -= mb;
    q0 += v[e] * v[e];
    q1 += v[e + 1] * v[e + 1];
  }
  q0 += q1;
  const float dm = mean - mb;
  const float var = fmaxf(red_mod4(q0.x + q0.y) * (1.0f / DM) - dm * dm, 0.f);
  const float sd = bfr1(sqrtf(bfr1(bfr1(var) + 1e-5f)));
  const float ri = 1.0f / sd;
#pragma unroll
  for (int e = 0; e < 16; ++e) {
    const unsigned t2 = pk2(v[e].x, v[e].y);
    const f32x2_t q = f32x2_t{lo16(t2), hi16(t2)} * ri;
    x[e >> 2][e & 3] = pk2(q.x, q.y);
  }
}

extern "C" __device__ float mul1(float, float) __asm("llvm.amdgcn.fmul.legacy");
template <bool G0>
__device__ __forceinline__ void role_qkv_t(const MegaArgs& A, char* smem, const int L, const int h) {
  __builtin_amdgcn_s_waitcnt(0x0F70);
  const int w = __builtin_amdgcn_readfirstlane(threadIdx.x >> 6);
  char* ws = A.ws;
  unsigned* FL = (unsigned*)(ws + OFF_FL);
  unsigned* TMO = FL + FL_TMO;
#define LANE_IDS()                         \
  const int l = lane_id();                 \
  const int c = l & 15, g = l >> 4;        \
  (void)c; (void)g;
  unsigned* FC = FL + FL_FC + (L * 64 * 4 + REP) * 8 * 32;
  bf16x8 wa[16], wb[16], wv[16];
  const char* Xb = ws + (L ? OFF_X3 : OFF_XA) + w * 8192;
  const v4u rsF = mk_srd(FC + w * 64, (64 * 32 - REP * 8 - 2 * w) * 128);
  constexpr unsigned Q_R = 131072;
  bool zs, got = false;
  f32x4 cs[3] = {};
  PSV;
  {
    LANE_IDS();
    const unsigned sb = lds_base(smem);
    const int q = (l >> 2) & 3, p = l & 3, f = q | ((g & 1) << 2);
    const unsigned r0 = sb + (g >> 1) * 16384 + (8 * (g & 1) + q) * 256;
    const unsigned ra = r0 + (w ^ f) * 32 + p * 8, rb = r0 + ((2 * (w & 3) + (p >> 1)) ^ f) * 32 + (p & 1) * 16;
    const v4u one = {0x3f803f80u, 0x3f803f80u, 0x3f803f80u, 0x3f803f80u};
    v4u zt;
    v2u ze;
    unsigned n0 = 0u;
#define WP(k) (WM(k) == 0 ? A.Wq : WM(k) == 1 ? A.Wk : A.Wv)
#define W_READ(W, a_)                                    \
  {                                                      \
    const v2u x0 = trd(a_), x1 = trd((a_) + 1024);       \
    W = BF8((v4u{x0.x, x0.y, x1.x, x1.y}));              \
  }
    if (QSL && L) gate(now10() + QSL);
    PST(0);
#pragma unroll
    for (int k = 0; k < 12; ++k) w_issue<false>(WP(k >> 2), WJ(k >> 2), k >> 2, k & 3, h, w, l, sb);
    PST(1);
#pragma unroll
    for (int k = 0; k < 12; ++k) {
      const int j = k & 3;
      if (k == ZK + 1) wait_vm<10>();
      else if (k < 10) wait_vm<8>();
      else if (k == 10) wait_vm<G0 ? 5 : 4>();
      else wait_vm<0>();
      RAW_BAR();
      if (k == ZK)
        asm volatile("s_nop 4\n\tglobal_load_dwordx4 %0, %2, %3 sc1\n\tglobal_load_dwordx2 %1, %4, %5 sc1"
                     : "=&v"(zt), "=&v"(ze)
                     : "v"(l * 16), "s"(ws + OFF_TAG + (blockIdx.x & 7) * 1024), "v"(0), "s"(ws + OFF_TAG + 8192)
                     : "memory");
      if (k == 9) {
        asm volatile("" : "+v"(zt), "+v"(ze));
        zs = zero_seen(zt, ze);
        if (G0) asm volatile("s_nop 4\n\tbuffer_load_dword %0, %1, %2, 0 offen sc1" : "=v"(n0) : "v"((l & 7) * 4 + (l & 8) * 16), "s"(rsF) : "memory");
      }
      if (k == 11 && G0 && zs && all_set(n0)) {
#pragma unroll
        for (int i = 0; i < 8; ++i) glds16c(Xb + i * 1024, l * 16, sb + Q_R + w * 8192 + i * 1024);
        got = true;
      }
#pragma unroll
      for (int i = 0; i < 4; ++i) {
        if (k < 9) w_issue<false>(WP(k + 3), WJ(k + 3), (k + 3) & 3, i, h, w, l, sb);
        if (WM(k) == 2) W_READ(wv[4 * WJ(k) + i], ra + j * 4096 + i * 32768);
        if (WM(k) == (G0 ? 1 : 0)) {
          W_READ(wa[4 * WJ(k) + i], rb + j * 4096 + i * 32768);
          W_READ(wb[4 * WJ(k) + i], rb + 8 + j * 4096 + i * 32768);
        }
        if (k > 0 && WM(k - 1) == 2) cs[2] = MFMA16(BF8(one), wv[4 * WJ(k - 1) + i], cs[2]);
        if (k > 0 && WM(k - 1) == (G0 ? 1 : 0)) {
          cs[0] = MFMA16(BF8(one), wa[4 * WJ(k - 1) + i], cs[0]);
          cs[1] = MFMA16(BF8(one), wb[4 * WJ(k - 1) + i], cs[1]);
        }
        asm volatile("" : "+v"(cs[0]), "+v"(cs[1]), "+v"(cs[2]));
      }
      if (k == 1) PST(2);
      if (k == 3) PST(3);
      if (k == 5) PST(4);
      if (k == 7) PST(5);
      if (k == 9) PST(6);
    }
    if (got) PST(11);
#pragma unroll
    for (int i = 12; i < 16; ++i) cs[2] = MFMA16(BF8(one), wv[i], cs[2]);
    asm volatile("s_nop 7" : "+v"(cs[2]));
    RAW_BAR();
    PST(8);
#undef W_READ
#undef WP
  }
  WSTAMP2();
  constexpr int Q_X = 98304;
  const unsigned sb = lds_base(smem);
  v2u ea, eb, ev;
  {
    LANE_IDS();
    const v4u one = {0x3f803f80u, 0x3f803f80u, 0x3f803f80u, 0x3f803f80u};
    v2u e[3];
#pragma unroll
    for (int i = 0; i < 3; ++i) {
      const float x = g ? 0.f : cs[i][0], x1 = x - bfr1(x);
      e[i] = v2u{pk2(x, x1), pk2(x1 - bfr1(x1), 0.f)};
    }
    ea = e[0]; eb = e[1]; ev = e[2];
    asm volatile("" : "+v"(ea), "+v"(eb), "+v"(ev));
    if (!zs) zero_wait(ws);
    if (l < 32) *reinterpret_cast<v4u*>(smem + Q_X + w * 2048 + (l >> 4) * 16384 + c * 16) = one;
  }
  constexpr int Q_ST = Q_X + 8 * 2048 + 256, Q_CNT = Q_X + 11 * 2048 + 256;
  if (threadIdx.x == 0) *(lu32*)(unsigned long)(sb + Q_CNT) = 0u;
  v8u pv, wr[24];
  v4u pw;
  unsigned sw;
  {
    LANE_IDS();
    const unsigned fa = sb + Q_R + g * 512 + c * 16;
    pv = v8u{ea.x, ea.y, eb.x, eb.y, ev.x, ev.y, (unsigned)l * 16u, fa};
    pw = v4u{sb + Q_ST + c * 4, sb + Q_ST + g * 16,
             G0 ? (l & 7) * 4 + (l & 8) * 16 : (c & 8) ? sb + Q_X + ((w + 1) & 1) * 128 : fa + (w & 3) * 128, (c & 8) ? 0u : Q_R};
    sw = sb + Q_ST + (w & 3) * 32 + (l & 7) * 4;
#pragma unroll
    for (int i = 0; i < 8; ++i) {
      wr[i] = __builtin_shufflevector(__builtin_bit_cast(v4u, wa[2 * i]), __builtin_bit_cast(v4u, wa[2 * i + 1]), 0, 1, 2, 3, 4, 5, 6, 7);
      wr[8 + i] = __builtin_shufflevector(__builtin_bit_cast(v4u, wb[2 * i]), __builtin_bit_cast(v4u, wb[2 * i + 1]), 0, 1, 2, 3, 4, 5, 6, 7);
      wr[16 + i] = __builtin_shufflevector(__builtin_bit_cast(v4u, wv[2 * i]), __builtin_bit_cast(v4u, wv[2 * i + 1]), 0, 1, 2, 3, 4, 5, 6, 7);
    }
  }
  const v4u rsQ = mk_srd(ws + OFF_QK + L * SZ_QK, SZ_QK), rsV = mk_srd(ws + OFF_VF + L * SZ_O, SZ_O);
  const v4u rsM = mk_srd(ws + (G0 ? OFF_MEMK : OFF_MEMV) + L * SZ_MEM, SZ_MEM);
  const unsigned* FQ = FL + FL_FQ + (L * 65 * 32 + h) * 32;
  const char* dbg = ws;
  PST(9);
  __builtin_amdgcn_s_waitcnt(0x0F70);
  RAW_BAR();
  PST(10);
  PSD();
#define QKV_OPS                                                                                                              \
  ::"{v[64:71]}"(wr[0]), "{v[72:79]}"(wr[1]), "{v[80:87]}"(wr[2]), "{v[88:95]}"(wr[3]), "{v[96:103]}"(wr[4]),                \
      "{v[104:111]}"(wr[5]), "{v[112:119]}"(wr[6]), "{v[120:127]}"(wr[7]), "{v[128:135]}"(wr[8]), "{v[136:143]}"(wr[9]),      \
      "{v[144:151]}"(wr[10]), "{v[152:159]}"(wr[11]), "{v[160:167]}"(wr[12]), "{v[168:175]}"(wr[13]), "{v[176:183]}"(wr[14]), \
      "{v[184:191]}"(wr[15]), "{v[192:199]}"(wr[16]), "{v[200:207]}"(wr[17]), "{v[208:215]}"(wr[18]), "{v[216:223]}"(wr[19]), \
      "{v[224:231]}"(wr[20]), "{v[232:239]}"(wr[21]), "{v[240:247]}"(wr[22]), "{v[248:255]}"(wr[23]), "{v[0:7]}"(pv),        \
      "{v[8:11]}"(pw), "{v62}"(sw), "{s[40:43]}"(rsQ), "{s[44:47]}"(rsV), "{s[48:51]}"(rsM), "{s[52:55]}"(rsF),              \
      "{s[56:57]}"(Xb), "{s58}"(sb + Q_R + w * 8192), "{s69}"(__builtin_amdgcn_readfirstlane((int)got)), "{s59}"((h * 16 + (G0 ? 8 : 0) + (w & 3)) * 1024), "{s60}"((h * 8 + w) * 1024), \
      "{s61}"(h * 2048 + (w & 3) * 512), "{s62}"(0), "{s63}"(L ? 0 : QFAST), "{s[64:65]}"(FQ), "{s66}"(NSEG - 1), "{s67}"(0),            \
      "{s[70:71]}"(0xaaaaaaaaaaaaaaaaull), "{s[72:73]}"(0xccccccccccccccccull), "{s78}"(w), "{s80}"(sb + Q_CNT),             \
      "{s[36:37]}"(dbg), [gq] "n"(GATE_Q), [sq] "n"(SLP_Q), [s1] "n"(SLP_Q1 - SLP_Q)                                         \
      : "memory", "vcc", "scc", "s81", "v63"
  if (G0) asm volatile("s_mov_b64 exec, -1\ns_nop 4\n" QKV_ASM_0 QKV_OPS);
  else asm volatile("s_mov_b64 exec, -1\ns_nop 4\n" QKV_ASM_1 QKV_OPS);
#undef QKV_OPS
#undef LANE_IDS
}
__device__ __forceinline__ void role_qkv(const MegaArgs& A, char* smem, const int L, const int h) {
  if (__builtin_amdgcn_readfirstlane(threadIdx.x >> 6) < 4) role_qkv_t<true>(A, smem, L, h);
  else role_qkv_t<false>(A, smem, L, h);
}

constexpr int MEGA_WGS = 256;
constexpr size_t MEGA_LDS = 163840;
static_assert(MEGA_LDS <= 163840 && MEGA_LDS >= 162816 + 256, "lds");
static_assert(AW_SO + 49152 <= MEGA_LDS, "lds");
static_assert(MEGA_LDS >= (size_t)(8 * 2 * CKN + 8 * 2 * CUN + 2 * 32 * 8 + 8 * STN) * 2, "lds");
static_assert(MEGA_LDS >= (size_t)2 * 32 * SMS * 2 && MEGA_LDS >= 131072, "lds");
static_assert(131072 >= (size_t)(40 * NST + 32 * 400) * 4, "lds");
__global__ __launch_bounds__(512, 2) void mega_kernel(const u16* mem0, const u16* Dskip, char* ws, u16* out, const u16* x0,
                                                      const u16* logAre, const u16* Aim, const u16* Cre, const u16* Cim,
                                                      const u16* logdt, const u16* Wq, const u16* Wk, const u16* Wv,
                                                      const u16* Wo) {
  extern __shared__ __attribute__((aligned(16))) char smem[];
  const int b = __builtin_amdgcn_readfirstlane(role_no());
  const MegaArgs A{mem0, Dskip, ws, out, x0, logAre, Aim, Cre, Cim, logdt, Wq, Wk, Wv, Wo, b};
  {
    const int hi = (b >= 32 && b < 96) ? b - 32 : (b >= 200 && b < 248) ? b - 136 : (b >= 96 && b < 104) ? b + 16 : (b >= 248 ? b - 128 : -1);
    if (hi >= 0) start_kgen(A, smem, hi, b < 248 - XI0 + 8 ? b - 248 + XI0 : -1, (b >= 64 && b < 104) || (b >= 232 && b < 240) ? b & 15 : -1);
  }
  if ((EXP_OFF & 1) && b >= 64 && b < 80) return;
  if ((EXP_OFF & 8) && b >= 200 && b < 232) return;
  if ((EXP_OFF & 16) && b >= 32 && b < 64) return;
  if ((EXP_OFF & 32) && b >= 80 && b < 96) return;
  if (b < 64) role_chain(A, smem, b >> 5, b & 31, 0);
  else if (b < 104 || (b >= 232 && b < 240)) role_aw(A, smem, b < 80 ? 0 : b < 96 ? 2 : 1, b & 15);
  else if (b < 168) role_conv(A, smem, b - 104);
  else if (b < 232) role_qkv(A, smem, (b - 168) >> 5, (b - 168) & 31);
  else role_red(A, smem, b - 240);
}

extern "C" void kernel_launch(void* const* d_in, const int* in_sizes, int n_in, void* d_out, int out_size, void* d_ws,
                              size_t ws_size, hipStream_t stream) {
  MegaArgs A;
  A.x0 = (const u16*)d_in[0];
  A.logAre = (const u16*)d_in[1];
  A.Aim = (const u16*)d_in[2];
  A.Cre = (const u16*)d_in[3];
  A.Cim = (const u16*)d_in[4];
  A.logdt = (const u16*)d_in[5];
  A.Dskip = (const u16*)d_in[6];
  A.mem0 = (const u16*)d_in[8];
  A.Wq = (const u16*)d_in[9];
  A.Wk = (const u16*)d_in[10];
  A.Wv = (const u16*)d_in[11];
  A.Wo = (const u16*)d_in[12];
  A.ws = (char*)d_ws;
  A.out = (u16*)d_out;

  (void)hipFuncSetAttribute((const void*)mega_kernel, hipFuncAttributeMaxDynamicSharedMemorySize, (int)MEGA_LDS);
  mega_kernel<<<MEGA_WGS, 512, MEGA_LDS, stream>>>(A.mem0, A.Dskip, A.ws, A.out, A.x0, A.logAre, A.Aim, A.Cre, A.Cim, A.logdt,
                                                   A.Wq, A.Wk, A.Wv, A.Wo);
}
```

```cpp
#include <hip/hip_runtime.h>
#include <hip/hip_bf16.h>
#include <stdint.h>

constexpr int L_SEQ = 2048;
constexpr int DM    = 512;
constexpr int NST   = 128;
constexpr int NH    = 32;
constexpr int DH    = 128;
constexpr int HD    = NH * DH;
constexpr int MM    = 8;
constexpr int SEG   = 32;
constexpr int NSEG  = L_SEQ / SEG;
constexpr int LK    = MM + SEG;

typedef unsigned short u16;
using bf16x8 = __attribute__((ext_vector_type(8))) short;
using f32x4  = __attribute__((ext_vector_type(4))) float;
using f32x16 = __attribute__((ext_vector_type(16))) float;

static __device__ __forceinline__ u16 f2bf(float f) {
  unsigned u = __float_as_uint(f);
  u += 0x7fffu + ((u >> 16) & 1u);
  return (u16)(u >> 16);
}
static __device__ __forceinline__ float bf2f(u16 h) { return __uint_as_float(((unsigned)h) << 16); }
static __device__ __forceinline__ float bfr(float f) { return bf2f(f2bf(f)); }

constexpr size_t SZ_X      = (size_t)L_SEQ * DM * 2;
constexpr size_t OFF_KF    = 0;
constexpr size_t OFF_UT    = OFF_KF + (size_t)DM * L_SEQ * 4;
constexpr size_t OFF_YT    = OFF_UT + SZ_X;
constexpr size_t OFF_XA    = OFF_YT + SZ_X;
constexpr size_t OFF_H     = OFF_XA + SZ_X;
constexpr size_t OFF_X2    = OFF_H + SZ_X;
constexpr size_t OFF_U2    = OFF_X2 + SZ_X;
constexpr size_t OFF_X3    = OFF_U2 + SZ_X;
constexpr size_t SZ_QK     = (size_t)L_SEQ * 2 * HD * 2;
constexpr size_t OFF_QK    = OFF_X3 + SZ_X;
constexpr size_t SZ_O      = (size_t)L_SEQ * HD * 2;
constexpr size_t OFF_VF    = OFF_QK + 2 * SZ_QK;
constexpr size_t SZ_PW     = (size_t)2 * NSEG * 16 * SEG * DM * 2;
constexpr size_t OFF_PW    = OFF_VF + 2 * SZ_O;
constexpr size_t SZ_MEM    = (size_t)NSEG * MM * HD * 2;
constexpr size_t OFF_MEMK  = OFF_PW + SZ_PW;
constexpr size_t OFF_MEMV  = OFF_MEMK + 2 * SZ_MEM;
constexpr size_t OFF_GT    = OFF_MEMV + 2 * SZ_MEM;
constexpr size_t SZ_ACC    = (size_t)2 * 32768;
constexpr size_t OFF_Z     = OFF_GT + (size_t)64 * DM * 4;
constexpr size_t OFF_ACC   = OFF_Z;
constexpr unsigned ACC_LO = 0x88000000u, ACC_HI = 0x87fffff7u;
constexpr size_t OFF_FL    = OFF_ACC + 2 * SZ_ACC;
constexpr int FL_FW  = 0;
constexpr int FL_FR  = FL_FW + 2 * 64 * 16 * 32;
constexpr int FL_TW  = FL_FR + 64 * 16 * 32;
constexpr int FL_FC  = FL_TW + 4 * 128;
constexpr int FL_FQ  = FL_FC + 2 * 64 * 32 * 32;
constexpr int FL_FS  = FL_FQ + 2 * 65 * 32 * 32;
constexpr int FL_FZ  = FL_FS + 4 * 32 * 32;
constexpr int FL_KG  = FL_FZ + 8 * 32;
constexpr int FL_G   = FL_KG + 64 * 32;
constexpr int FL_TK  = FL_G + 4 * 4 * 32;
constexpr int FL_TMO = FL_TK + 2 * 256;
constexpr int FL_S0  = FL_TMO + 64;
constexpr int FL_END = FL_S0 + 2048;
constexpr size_t Z_BYTES   = OFF_FL + (size_t)FL_END * 4 - OFF_Z;
#define TSH 0
constexpr size_t OFF_TAG   = OFF_Z + Z_BYTES + TSH;
constexpr unsigned long long TAG_STEP = 0x9e3779b97f4a7c15ull;
constexpr size_t OFF_G0    = OFF_TAG + 8 * 1024 + 256;
constexpr size_t OFF_X0    = OFF_G0 + 8192;
constexpr size_t WS_TOTAL  = OFF_X0 + 4096;
static_assert(Z_BYTES % 256 == 0 && OFF_Z % 256 == 0 && Z_BYTES <= 2 * 128 * 8192, "zero region");
#define REP ((blockIdx.x >> 1) & 3)
static_assert(WS_TOTAL + (4 << 20) <= (size_t)256 * 1024 * 1024, "workspace too large");

#define CKNP 0
constexpr int CKN = 2208 + CKNP;
static_assert((size_t)DM * 2 * CKN * 2 <= (size_t)DM * L_SEQ * 4 + SZ_X, "K buffer");
__device__ __forceinline__ float pos_of_index(int p) {
  if (p < 256) return (float)p;
  if (p < 384) return (float)(256 + 2 * (p - 256));
  if (p < 512) return (float)(512 + 4 * (p - 384));
  if (p < 640) return (float)(1024 + 8 * (p - 512));
  return 2048.0f;
}
__device__ __forceinline__ int index_of_l(int l) {
  float pf = bfr((float)l);
  int p = (int)pf;
  if (p < 256) return p;
  if (p < 512) return 256 + ((p - 256) >> 1);
  if (p < 1024) return 384 + ((p - 512) >> 2);
  if (p < 2048) return 512 + ((p - 1024) >> 3);
  return 640;
}

template <int CTRL> __device__ __forceinline__ float dppr(float x) {
  return __builtin_bit_cast(float, __builtin_amdgcn_update_dpp(0, __builtin_bit_cast(int, x), CTRL, 0xf, 0xf, false));
}
__device__ __forceinline__ float wave_sum(float v) {
  v += dppr<0x121>(v);
  v += dppr<0x122>(v);
  v += dppr<0x124>(v);
  v += dppr<0x128>(v);
  {
    auto r = __builtin_amdgcn_permlane16_swap(__float_as_uint(v), __float_as_uint(v), false, false);
    v = __uint_as_float(r[0]) + __uint_as_float(r[1]);
  }
  {
    auto r = __builtin_amdgcn_permlane32_swap(__float_as_uint(v), __float_as_uint(v), false, false);
    v = __uint_as_float(r[0]) + __uint_as_float(r[1]);
  }
  return v;
}
__device__ __forceinline__ void ln_row8(float (&v)[8]) {
  float s = 0.f;
#pragma unroll
  for (int j = 0; j < 8; ++j) s += v[j];
  s = wave_sum(s);
  const float mean = s * (1.0f / DM);
  float q = 0.f;
#pragma unroll
  for (int j = 0; j < 8; ++j) { float t = v[j] - mean; q += t * t; }
  q = wave_sum(q);
  const float var = q * (1.0f / DM);
  const float mb = bfr(mean);
  const float sd = bfr(sqrtf(bfr(bfr(var) + 1e-5f)));
#pragma unroll
  for (int j = 0; j < 8; ++j) v[j] = bfr(bfr(v[j] - mb) / sd);
}

#define RLX_AGENT __ATOMIC_RELAXED, __HIP_MEMORY_SCOPE_AGENT
typedef __attribute__((address_space(1))) unsigned gu32;
typedef __attribute__((address_space(3))) unsigned lu32;
typedef unsigned v4u __attribute__((ext_vector_type(4)));
constexpr int AUX_SC1 = 16;
constexpr unsigned SPIN_CAP = 1u << 18;

__device__ __forceinline__ bool wait_flags(const unsigned* flags, int lane, int nfl, unsigned want, unsigned* tmo) {
  for (unsigned spins = 0;; ++spins) {
    unsigned v = want;
    if (lane < nfl) v = __hip_atomic_load((gu32*)flags + lane * 32, RLX_AGENT);
    if (__all(v >= want)) break;
    if (spins > SPIN_CAP) {
      if (lane == 0) __hip_atomic_store((gu32*)tmo, 1u, RLX_AGENT);
      return false;
    }
  }
  __builtin_amdgcn_fence(__ATOMIC_ACQUIRE, "wavefront");
  return true;
}

#define STAMP(i)
#define STAMP_DUMP()
#define WSTAMP(i)
template <int CTRL> __device__ __forceinline__ float dppf(float old, float x) {
  return __builtin_bit_cast(float, __builtin_amdgcn_update_dpp(__builtin_bit_cast(int, old), __builtin_bit_cast(int, x), CTRL, 0xf, 0xf, false));
}
__device__ __forceinline__ float wave_max_dpp(float x) {
  const float ninf = -3e38f;
  x = fmaxf(x, dppf<0x111>(ninf, x));
  x = fmaxf(x, dppf<0x112>(ninf, x));
  x = fmaxf(x, dppf<0x114>(ninf, x));
  x = fmaxf(x, dppf<0x118>(ninf, x));
  x = fmaxf(x, dppf<0x142>(ninf, x));
  x = fmaxf(x, dppf<0x143>(ninf, x));
  return __builtin_bit_cast(float, __builtin_amdgcn_readlane(__builtin_bit_cast(int, x), 63));
}
__device__ __forceinline__ float wave_sum_dpp(float x) {
  x += dppf<0x111>(0.f, x);
  x += dppf<0x112>(0.f, x);
  x += dppf<0x114>(0.f, x);
  x += dppf<0x118>(0.f, x);
  x += dppf<0x142>(0.f, x);
  x += dppf<0x143>(0.f, x);
  return __builtin_bit_cast(float, __builtin_amdgcn_readlane(__builtin_bit_cast(int, x), 63));
}
constexpr int SMS = 528;
constexpr int QS = 144;
constexpr int VS = 80;
constexpr int SOS = 144;
#define VAR_NREP 4
constexpr int NREP = VAR_NREP;
__device__ __forceinline__ float xsum16(float x) {
  auto r = __builtin_amdgcn_permlane16_swap(__float_as_uint(x), __float_as_uint(x), false, false);
  return __uint_as_float(r[0]) + __uint_as_float(r[1]);
}
__device__ __forceinline__ float xsum32(float x) {
  auto r = __builtin_amdgcn_permlane32_swap(__float_as_uint(x), __float_as_uint(x), false, false);
  return __uint_as_float(r[0]) + __uint_as_float(r[1]);
}
__device__ __forceinline__ float xmax16(float x) {
  auto r = __builtin_amdgcn_permlane16_swap(__float_as_uint(x), __float_as_uint(x), false, false);
  return fmaxf(__uint_as_float(r[0]), __uint_as_float(r[1]));
}
__device__ __forceinline__ float xmax32(float x) {
  auto r = __builtin_amdgcn_permlane32_swap(__float_as_uint(x), __float_as_uint(x), false, false);
  return fmaxf(__uint_as_float(r[0]), __uint_as_float(r[1]));
}
__device__ __forceinline__ float red_mod4(float x) {
  x += dppf<0x124>(0.f, x);
  x += dppf<0x128>(0.f, x);
  return xsum32(xsum16(x));
}
typedef __bf16 bf16x2_t __attribute__((ext_vector_type(2)));
typedef float f32x2_t __attribute__((ext_vector_type(2)));
__device__ __forceinline__ unsigned pk2(float a, float b) {
  f32x2_t v = {a, b};
  bf16x2_t r = __builtin_convertvector(v, bf16x2_t);
  return __builtin_bit_cast(unsigned, r);
}
__device__ __forceinline__ float dot2sq(const unsigned x, const float acc) {
  return __builtin_amdgcn_fdot2_f32_bf16(__builtin_bit_cast(bf16x2_t, x), __builtin_bit_cast(bf16x2_t, x), acc, false);
}
__device__ __forceinline__ float lo16(unsigned u) { return __uint_as_float(u << 16); }
__device__ __forceinline__ float hi16(unsigned u) { return __uint_as_float(u & 0xffff0000u); }
__device__ __forceinline__ void scale_pair(float a, float b, float scale, float& oa, float& ob) {
  unsigned u = pk2(a, b);
  unsigned v = pk2(lo16(u) * scale, hi16(u) * scale);
  oa = lo16(v);
  ob = hi16(v);
}

#define XSPLIT 3
__device__ __forceinline__ int role_no() {
  const int b = blockIdx.x, x = b & 7, j = b >> 3;
  if (XSPLIT >= 3) {
    const int y = XSPLIT == 5 ? x & 3 : x >> 1;
    if (XSPLIT >= 6 && (j >> 2) == 6) return (x & 1) ? 104 + 16 * y + j - 16 : 80 + 4 * y + (j & 3);
    if (XSPLIT >= 7 && (j >> 2) == 4) return (x & 1) ? 104 + 16 * y + j - 16 : 64 + 4 * y + (j & 3);
    if (XSPLIT == 5 ? x < 4 : !(x & 1)) {
      if (j >= 16) return 104 + 16 * y + j - 16;
      return XSPLIT == 4 ? 16 * y + j : (j & 8) * 4 + 8 * y + (j & 7);
    }
    if (j < 16) return 168 + (j & 8) * 4 + 8 * y + (j & 7);
    const int i = 4 * y + (j & 3);
    return j < 20 ? 64 + i : j < 24 ? (i < 8 ? 96 + i : 224 + i) : j < 28 ? 80 + i : 240 + i;
  }
  const int L = x >> 2, xx = x & 3;
  if (j < 8) return 32 * L + 8 * xx + j;
  if (j < 12) return 64 + 16 * L + 4 * xx + j - 8;
  if (j < 16) { const int i = 4 * xx + j - 12; return L ? 240 + i : i < 8 ? 96 + i : 224 + i; }
  if (j < 24) return 104 + 8 * x + j - 16;
  return 168 + 32 * L + 8 * xx + j - 24;
}
#define MSTAMP()
#define WSTAMP2()
#define SST(k)
#define TSR(rn, k, ss, t)
#define TS(k, ss, t)
#define RST(k)
#define RSD()
#define LST(k)
#define LSD()
#define PSV
#define PST(k)
#define PSD()
#define PSE()
#define FSD
#define FS(k)
#define FD(ss)
struct MegaArgs {
  const u16* mem0;
  const u16* Dskip;
  char* ws;
  u16* out;
  const u16* x0;
  const u16 *logAre, *Aim, *Cre, *Cim, *logdt;
  const u16 *Wq, *Wk, *Wv, *Wo;
  int rn;
};

__device__ __forceinline__ int lane_id() {
  int l;
  asm volatile("v_mbcnt_lo_u32_b32 %0, -1, 0\n\tv_mbcnt_hi_u32_b32 %0, -1, %0" : "=v"(l));
  return l;
}
__device__ __forceinline__ bool spin_timeout(unsigned spins, unsigned* tmo) {
  if (spins > SPIN_CAP) {
    if (lane_id() == 0) __hip_atomic_store((gu32*)tmo, 1u, RLX_AGENT);
    return true;
  }
  return false;
}
#define PSWH (h & 2)
#define RR 0
#define RQM 63
#define RPM 63
#define CD0 100
#define APN 6
#define APL 300
#define APA 80
#define APB 85
#define APP 204
#define CD 30
#define CDS 30
#define CDL 0
#define XG(h) ((h) >> 3)
#define GP 230
#define GR 50
#define GM 0
#define ROT (4 * (h & 1))
#define CPRIO 1
#define P1PRIO 3
#define QLEAD 2
#define POLL_SLEEP 12
#define SLP_C POLL_SLEEP
#define SLP_A POLL_SLEEP
#define SLP_W POLL_SLEEP
#define SLP_R 4
#define SLP_V 4
#define SLP_Q 4
#define SLP_M 4
#define AWPRIO 2
#define SLP_S 8
#define SPOS 4
#define CVPRE 0
#define QHV 0
#define QCHK 0
#define LEADN 99
#define GATE_V 40
#define GATE_R 380
#define GATE_N 440
#define AWAHEAD 1
#define GATE_M 150
#define GATE_M2 380
#define QPACE 0
#define QPS 6
#define QPL 100
#define GATE_Q 150
#define SLP_Q1 24
#define QPF 3
#define QPV 6
#define QVP 3
#define QP0 2
#define QP1 0
#define QFP 15
#define QUP 7
#define QEP 3
#define AWD 9
#define GPER 200
#define GMA 60
#define GMR 135
#define GMS 0
#define GSTEP 30
#define GATE_RT 130
#define GATE_1 160
#define N1S 20
#define N1E 36
#define PRI_A 3
#define PRI_WA 2
#define PRI_WB 1
#define AWSKIP 3
#define PRI_0 3
#define AWDEF 1
#define SLP_F 2
#define EXP_OFF 0
#define AWSPEC 1
#define CVTHR 48
#define NR_C 8
#define NR_R 8
#define NR_W 8
#define NR_N 2
#define QAHEAD 0
#define NS0 4
#define CVST 16
#define CVNB 9
#define CVFULL 1
#define HK3 0
#define AWDT 120
#define AWMO 0
#define HK2 0
#define RDT 185
#define RQ0 4
#define RGW 40
#define ZK 8
#define ZC 7
#define KBR 8
#define AWE 0
#define AWK 5
#define CRD 40
#define CD2 110
#define CD4 70
#define SLP_2 2
#define CD1 80
#define SLP_1 8
#define CSL 0
#define QSL 0
#define RSL 0
#define CTH 0
#define WORD 0
#define WXC 0
#define PFS 0
#define XI0 8
#define QFAST 58
#define SLP_G 4
#define TCH 0
#define CSD 0
#define QX2 1
#define CET 0
#define CG1 110
#define IPC 20
#define IPQ 20
#define AWDU 180
#define RTW 0
#define CVLT 120
#define SLP_P 2
#define SLP_N POLL_SLEEP
template <int SLP, int WPL>
__device__ __forceinline__ bool wait_n(const unsigned* f, int n, unsigned* tmo) {
  const int l = lane_id();
  for (unsigned spins = 0;; ++spins) {
    unsigned v = 1u;
    if (l < n) v = __hip_atomic_load((gu32*)f + (l / WPL) * 32 + l % WPL, RLX_AGENT);
    if (__all(v != 0u)) break;
    if (spin_timeout(spins, tmo)) return false;
    __builtin_amdgcn_s_sleep(SLP);
  }
  asm volatile("" ::: "memory");
  return true;
}
__device__ __forceinline__ unsigned now10() { return (unsigned)__builtin_amdgcn_s_memrealtime(); }
__device__ __forceinline__ void gate(const unsigned t) {
#pragma unroll 1
  for (int i = 0; i < 64; ++i) {
    const int d = (int)(t - now10());
    if (d <= 0) break;
    if (d > 100) __builtin_amdgcn_s_sleep(32);
    else if (d > 25) __builtin_amdgcn_s_sleep(8);
    else __builtin_amdgcn_s_sleep(2);
  }
}
template <int SLP, int WPL>
__device__ __forceinline__ unsigned grp_wait(const bool leader, volatile lu32* sw, const unsigned val, const unsigned* f, int n,
                                             unsigned* tmo, const unsigned t) {
  unsigned r;
  gate(t);
  if (leader) {
    const int l = lane_id();
    unsigned v;
    for (unsigned spins = 0;; ++spins) {
      v = __hip_atomic_load((gu32*)f + ((l & 15) / WPL) * 32 + l % WPL, RLX_AGENT);
      if (__all(v != 0u) || spin_timeout(spins, tmo)) break;
      __builtin_amdgcn_s_sleep(SLP);
    }
    int d = (int)(v - t);
    d = max(d, __builtin_amdgcn_update_dpp(0, d, 0x121, 0xf, 0xf, false));
    d = max(d, __builtin_amdgcn_update_dpp(0, d, 0x122, 0xf, 0xf, false));
    d = max(d, __builtin_amdgcn_update_dpp(0, d, 0x124, 0xf, 0xf, false));
    d = max(d, __builtin_amdgcn_update_dpp(0, d, 0x128, 0xf, 0xf, false));
    r = t + __builtin_amdgcn_readfirstlane(d);
    sw[1 + (val & 2)] = r;
    *sw = val;
  } else {
    for (unsigned spins = 0; __builtin_amdgcn_readfirstlane(*sw) < val && spins <= 64u * SPIN_CAP; ++spins) __builtin_amdgcn_s_sleep(SLP_F);
    r = __builtin_amdgcn_readfirstlane(sw[1 + (val & 2)]);
  }
  asm volatile("" ::: "memory");
  return r;
}
template <int SLP, int WPL>
__device__ __forceinline__ bool wg_wait(const unsigned* f, int n, unsigned* tmo) {
  if (threadIdx.x < 64) (void)wait_n<SLP, WPL>(f, n, tmo);
  __syncthreads();
  asm volatile("" ::: "memory");
  return true;
}
__device__ __forceinline__ void set_flag(unsigned* f) { __hip_atomic_store((gu32*)f, 1u, RLX_AGENT); }
#define DRAIN() asm volatile("s_waitcnt vmcnt(0)" ::: "memory")
#define MKRS(ptr, bytes) __builtin_amdgcn_make_buffer_rsrc((void*)(ptr), 0, (int)(bytes), 0x00020000)
typedef unsigned v2u __attribute__((ext_vector_type(2)));
#define LD128(rs, voff, soff) __builtin_amdgcn_raw_buffer_load_b128(rs, voff, soff, AUX_SC1)
#define LD64(rs, voff, soff) __builtin_amdgcn_raw_buffer_load_b64(rs, voff, soff, AUX_SC1)
#define ST128(v, rs, voff, soff)                                              \
  do {                                                                        \
    const v4u st_d_ = (v);                                                    \
    __builtin_amdgcn_raw_buffer_store_b128(st_d_, rs, voff, soff, AUX_SC1);   \
    asm volatile("s_nop 1" ::"v"(st_d_) : "memory");                          \
  } while (0)
#define ST64(v, rs, voff, soff) __builtin_amdgcn_raw_buffer_store_b64(v, rs, voff, soff, AUX_SC1)
#define BF8(x) __builtin_bit_cast(bf16x8, x)
#define MFMA16(a, b, c) __builtin_amdgcn_mfma_f32_16x16x32_bf16(a, b, c, 0, 0, 0)
typedef short bf16x4 __attribute__((ext_vector_type(4)));
#define MFMA16K(a, b, c) \
  __builtin_amdgcn_mfma_f32_16x16x16bf16_1k(__builtin_bit_cast(bf16x4, a), __builtin_bit_cast(bf16x4, b), c, 0, 0, 0)
__device__ __forceinline__ float fmax_nn(float a, float b) { return __builtin_elementwise_maximum(a, b); }
typedef __bf16 bfv8 __attribute__((ext_vector_type(8)));
typedef __bf16 bfv16 __attribute__((ext_vector_type(16)));
typedef unsigned v8u __attribute__((ext_vector_type(8)));
#define SMF(a, b, c, idx) \
  __builtin_amdgcn_smfmac_f32_16x16x64_bf16(__builtin_bit_cast(bfv8, a), __builtin_bit_cast(bfv16, b), c, idx, 0, 0)
__device__ __forceinline__ float swap_add32(float a, float b) {
  auto r = __builtin_amdgcn_permlane32_swap(__float_as_uint(a), __float_as_uint(b), false, false);
  return __uint_as_float(r[0]) + __uint_as_float(r[1]);
}
__device__ __forceinline__ float swap_add16(float a, float b) {
  auto r = __builtin_amdgcn_permlane16_swap(__float_as_uint(a), __float_as_uint(b), false, false);
  return __uint_as_float(r[0]) + __uint_as_float(r[1]);
}
__device__ __forceinline__ v4u smf_fix(const v4u d) {
  auto x = __builtin_amdgcn_permlane16_swap(d.x, d.z, false, false);
  auto y = __builtin_amdgcn_permlane16_swap(d.y, d.w, false, false);
  return v4u{x[0], x[1], y[0], y[1]};
}
__device__ __forceinline__ v8u smf_bfrag(const u16* p) {
  const v2u a = *reinterpret_cast<const v2u*>(p), b = *reinterpret_cast<const v2u*>(p + 8);
  const v2u c = *reinterpret_cast<const v2u*>(p + 32), d = *reinterpret_cast<const v2u*>(p + 40);
  return v8u{a.x, b.x, a.y, b.y, c.x, d.x, c.y, d.y};
}

#define VAR_PPA 0
#define VAR_PPB 0
#define VAR_THR 0
#define VAR_THR2 0
#define CV_LEAD 100
#define VAR_EARLY 1
#define VAR_DRAIN 1
#define VAR_D1 0
#define VAR_D2 0
#define VAR_DR2 1
#define VAR_S1 0
#define VAR_S2 0
typedef __attribute__((address_space(1))) unsigned long long gu64;
typedef __attribute__((address_space(1))) char gch;
__device__ __forceinline__ int cvt_rpi(float x) {
  int r;
  asm("v_cvt_rpi_i32_f32 %0, %1" : "=v"(r) : "v"(x));
  return r;
}
typedef short v4s __attribute__((ext_vector_type(4)));
typedef __attribute__((address_space(3))) v4s* lp4s;
#define WNT 0
#define WPOL ""
__device__ __forceinline__ void glds16(const void* sbase, const unsigned voff, const unsigned lds) {
  unsigned keep;
  asm volatile("s_nop 4\n\ts_mov_b32 %0, m0\n\ts_mov_b32 m0, %3\n\ts_nop 0\n\tglobal_load_lds_dwordx4 %1, %2" WPOL "\n\ts_mov_b32 m0, %0"
               : "=&s"(keep)
               : "v"(voff), "s"(sbase), "s"(lds)
               : "memory");
}
__device__ __forceinline__ void glds16c(const void* sbase, const unsigned voff, const unsigned lds) {
  unsigned keep;
  asm volatile("s_nop 4\n\ts_mov_b32 %0, m0\n\ts_mov_b32 m0, %3\n\ts_nop 0\n\tglobal_load_lds_dwordx4 %1, %2 sc1\n\ts_mov_b32 m0, %0"
               : "=&s"(keep)
               : "v"(voff), "s"(sbase), "s"(lds)
               : "memory");
}
__device__ __forceinline__ v2u trd(const unsigned a) {
  return __builtin_bit_cast(v2u, __builtin_amdgcn_ds_read_tr16_b64_v4i16((lp4s)(unsigned long)a));
}
__device__ __forceinline__ unsigned lds_base(char* smem) { return (unsigned)(unsigned long)(__attribute__((address_space(3))) char*)smem; }
#define RAW_BAR()                                           \
  do {                                                      \
    asm volatile("s_waitcnt lgkmcnt(0)" ::: "memory");      \
    __builtin_amdgcn_s_barrier();                           \
    asm volatile("" ::: "memory");                          \
  } while (0)
#define WAIT_VM(n) asm volatile("s_waitcnt vmcnt(" #n ")" ::: "memory")
#define TOURS 7
#define MARK() asm volatile(".Lmark%=:" ::)
template <int ID, int MX = 0, class T = int>
__device__ __forceinline__ void pad(T&& x = 0) {
  if (TOURS && MX) asm volatile("s_branch 1f\n\t.p2alignl 12, 0xbf800000, %2\n.Lpad%1:\n\ts_setpc_b64 s[98:99]\n1:" : "+v"(x) : "n"(ID), "n"(MX));
  else if (TOURS) asm volatile("s_cmp_eq_u32 s0, 0xbe801d62\n\t.set .Lpad%1, . - 4" : "+v"(x) : "n"(ID) : "scc");
}
template <int ID>
__device__ __forceinline__ void hop() { asm volatile("s_call_b64 s[98:99], .Lpad%0" ::"n"(ID) : "s98", "s99"); }
template <int B, int E, int D>
__device__ __forceinline__ void hops() {
  if constexpr (B < E) { hop<B>(); hops<B + D, E, D>(); }
}
template <int B, int E, int D, int I = 0>
__device__ __forceinline__ void tour(const int w) {
  if constexpr ((TOURS & 2) && I < D) {
    if ((w & (D - 1)) == I) hops<B + I, E, D>();
    else tour<B, E, D, I + 1>(w);
  }
}
template <bool SP>
__device__ __forceinline__ void w_issue(const u16* Wm, const int j, const int b, const int i, const int h, const int w, const int l, const unsigned sb) {
  const char* base = (const char*)Wm + (size_t)(128 * j + 16 * w) * (HD * 2) + h * (DH * 2);
  const int r = l >> 4;
  const int f = SP ? ((r & 1) | ((i & 1) << 1) | (((i >> 1) & 1) << 2)) : (r | (((i >> 1) & 1) << 2));
  glds16(base, (unsigned)((4 * i + r) * (HD * 2) + (((l & 15) ^ (f << 1)) << 4)), sb + w * 16384 + b * 4096 + i * 1024);
}
#define WM(k) (WORD ? (k) % 3 : (k) >> 2)
#define WJ(k) (WORD ? (k) / 3 : (k) & 3)
template <int N>
__device__ __forceinline__ void wait_vm() { asm volatile("s_waitcnt vmcnt(%0)" ::"n"(N) : "memory"); }

#define PFW 1
#define IPF 1
#define PN1 1
#define PNH 20
#define PWO 1
__device__ __forceinline__ void pull(const unsigned long long b, const unsigned voff, char* smem) {
  unsigned keep;
  asm volatile("s_nop 4\n\ts_mov_b32 %0, m0\n\ts_mov_b32 m0, %3\n\ts_nop 0\n\tglobal_load_lds_dword %1, %2\n\ts_mov_b32 m0, %0"
               : "=&s"(keep)
               : "v"(voff), "s"(b), "s"(lds_base(smem) + 162816)
               : "memory");
}
__device__ __forceinline__ unsigned long long matrix(const MegaArgs& A, const int k) {
  unsigned long long pv = (unsigned long long)A.Wv, pk = (unsigned long long)A.Wk, pq = (unsigned long long)A.Wq;
  asm volatile("" : "+s"(pv), "+s"(pk), "+s"(pq));
  return k == 0 ? pv : k == 1 ? pk : pq;
}
__device__ __forceinline__ void pull_rows(const MegaArgs& A, char* smem, const int i) {
  const int l = lane_id(), y = (blockIdx.x & 7) >> 1;
  const unsigned long long m = matrix(A, i >> 7);
  if (PWO && i >= 384) pull((unsigned long long)A.Wo + ((8 * y + (i & 7)) * 16 + ((i & 127) >> 3)) * 8192, l * 128, smem);
  else pull(m + (i & 127) * (8 * HD) + y * 2048, (l >> 4) * (HD * 2) + (l & 15) * 128, smem);
}
__device__ __forceinline__ void wo_issue(const u16* Wo, const int a, const int st, const int w, const int i, const unsigned sb) {
  const int l = lane_id(), c = l & 15, g = l >> 4, fd = g | ((i & 1) << 2);
  const char* base = (const char*)Wo + ((size_t)((2 * a + (st >> 2)) * DH + 64 * (w & 1) + 16 * (st & 3)) * DM + 128 * (w >> 1)) * 2;
  glds16(base, (unsigned)((4 * i + g) * (DM * 2) + ((c ^ (fd << 1)) << 4)), sb + (st % 5) * 32768 + w * 4096 + i * 1024);
}
__device__ __forceinline__ bool zero_seen(const v4u t, const v2u e) {
  const unsigned long long m = (((unsigned long long)e.y << 32) | e.x) + TAG_STEP;
  const unsigned lo = (unsigned)m, hi = (unsigned)(m >> 32);
  return __all(((t.x ^ lo) | (t.y ^ hi) | (t.z ^ lo) | (t.w ^ hi)) == 0u);
}
__device__ __forceinline__ void zero_wait(char* ws) {
  const __amdgpu_buffer_rsrc_t rs = MKRS(ws + OFF_TAG, 8192 + 8);
#pragma unroll 1
  for (unsigned spins = 0; spins < 16u * SPIN_CAP; ++spins) {
    const v4u t = LD128(rs, lane_id() * 16, (blockIdx.x & 7) * 1024);
    const v2u e = LD64(rs, 0, 8192);
    if (zero_seen(t, e)) break;
    asm volatile("s_sleep 4" ::: "memory");
  }
  asm volatile("" ::: "memory");
}
__device__ __forceinline__ f32x2_t row_sums(const v4u raw) {
  float v[8];
#pragma unroll
  for (int e = 0; e < 4; ++e) { v[2 * e] = lo16(raw[e]); v[2 * e + 1] = hi16(raw[e]); }
  float s = 0.f;
#pragma unroll
  for (int e = 0; e < 8; ++e) s += v[e];
  const float mean = wave_sum(s) * (1.0f / DM);
  float q = 0.f;
#pragma unroll
  for (int e = 0; e < 8; ++e) { const float t = v[e] - mean; q += t * t; }
  return f32x2_t{mean, wave_sum(q)};
}
__device__ __forceinline__ unsigned row_stats(const f32x2_t s) {
  const float sd = bfr(sqrtf(bfr(bfr(s.y * (1.0f / DM)) + 1e-5f)));
  return (unsigned)f2bf(s.x) | ((unsigned)f2bf(sd) << 16);
}
__device__ __forceinline__ void sincos1(const float x, float* sn, float* cs) {
  const float t = __builtin_amdgcn_sinf(x * 0.07957747155f);
  *sn = __builtin_amdgcn_sinf(x * 0.1591549431f);
  *cs = __builtin_fmaf(-2.0f * t, t, 1.0f);
}
#define RUN_START                                                                                                   \
  const f32x2_t wra = {wr[0], wr[1]}, wia = {wi[0], wi[1]}, wrb = {wr[2], wr[3]}, wib = {wi[2], wi[3]};             \
  const f32x2_t pa = wra + wra, pb = wrb + wrb, qa = wra * wra + wia * wia, qb = wrb * wrb + wib * wib;             \
  f32x2_t ya = {yr0[0], yr0[1]}, yb = {yr0[2], yr0[3]};                                                             \
  f32x2_t za = ya * wra - f32x2_t{yi0[0], yi0[1]} * wia, zb = yb * wrb - f32x2_t{yi0[2], yi0[3]} * wib;
#define RUN_STEP                                                                                                    \
  {                                                                                                                 \
    const f32x2_t na = __builtin_elementwise_fma(pa, za, -(qa * ya)), nb = __builtin_elementwise_fma(pb, zb, -(qb * yb)); \
    ya = za; yb = zb; za = na; zb = nb;                                                                             \
  }
__device__ __forceinline__ unsigned pull_text(const int slot) {
  unsigned long long pc;
  int back, len;
  asm volatile("s_getpc_b64 %0\n0:\n\ts_mov_b32 %1, 0b-.Lcode_at\n\ts_max_i32 %2, 0, .Lcode_end-.Lcode_at" : "=s"(pc), "=s"(back), "=s"(len)::"scc");
  pc -= (long long)back;
  unsigned r;
  asm volatile("s_nop 4\n\tbuffer_load_dword %0, %1, %2, 0 offen" : "=v"(r) : "v"(lane_id() * 64 + slot * 4096),
               "s"(v4u{(unsigned)pc, (unsigned)(pc >> 32) & 0xffffu, (unsigned)len, 0x00020000u}) : "memory");
  return r;
}
__device__ __forceinline__ f32x2_t cmul(const f32x2_t z, const f32x2_t e) {
  return __builtin_elementwise_fma(f32x2_t{z.y, z.x}, f32x2_t{-e.y, e.y}, z * e.x);
}
__device__ __forceinline__ void put_row(v2u* p, f32x2_t (&b)[2], const f32x2_t (&z)[2], const f32x2_t (&m)[2]) {
  unsigned h0, h1, l0, l1, t0, t1;
  f32x2_t n0, n1;
  asm("v_cvt_pk_bf16_f32 %0, %8, %9\n\tv_cvt_pk_bf16_f32 %1, %10, %11\n\tv_dot2_f32_bf16 %2, %0, %16, %8\n\tv_dot2_f32_bf16 %4, %0, %17, %9\n\t"
      "v_dot2_f32_bf16 %3, %1, %16, %10\n\tv_dot2_f32_bf16 %5, %1, %17, %11\n\tv_pk_mul_f32 %6, %12, %14 op_sel_hi:[1,0]\n\tv_pk_mul_f32 %7, %13, %15 op_sel_hi:[1,0]\n\t"
      "v_pk_fma_f32 %6, %12, %14, %6 op_sel:[1,1,0] op_sel_hi:[0,1,1] neg_lo:[0,1,0]\n\tv_cvt_pk_bf16_f32 %2, %2, %4\n\t"
      "v_pk_fma_f32 %7, %13, %15, %7 op_sel:[1,1,0] op_sel_hi:[0,1,1] neg_lo:[0,1,0]\n\tv_cvt_pk_bf16_f32 %3, %3, %5"
      : "=&v"(h0), "=&v"(h1), "=&v"(l0), "=&v"(l1), "=&v"(t0), "=&v"(t1), "=&v"(n0), "=&v"(n1)
      : "v"(b[0].x), "v"(b[0].y), "v"(b[1].x), "v"(b[1].y), "v"(z[0]), "v"(z[1]), "v"(m[0]), "v"(m[1]), "s"(0x0000bf80u), "s"(0xbf800000u));
  p[0] = v2u{h0, h1};
  p[64] = v2u{l0, l1};
  b[0] = n0;
  b[1] = n1;
}
__device__ __forceinline__ void start_kgen(const MegaArgs& A, char* smem, const int hi, const int xi, const int aw) {
  const int tid = threadIdx.x;
  const int w = __builtin_amdgcn_readfirstlane(tid >> 6), l = tid & 63, d = 4 * hi + (w & 3);
  const unsigned t0 = AWE ? now10() : 0u;
  PSV;
  PST(0);
  v4u raw[8], ra, rb;
  unsigned txt = 0u;
  if (IPF) {
    unsigned long long pc;
    int len;
    asm volatile("s_getpc_b64 %0\n.Lcode_at:\n\ts_max_i32 %1, 0, .Lcode_end-.Lcode_at" : "=s"(pc), "=s"(len)::"scc");
    const unsigned slot = ((blockIdx.x >> 3) - 8) + 8 * w;
    if ((blockIdx.x >> 6) == 1) txt = __builtin_amdgcn_raw_buffer_load_b32(MKRS(pc, len), l * 64 + slot * 4096, 0, 0);
  }
  const int r0 = 16 * xi + 2 * w, sx = 2 * ((hi + 96) & 127) + w - 6;
  if (xi >= 0) {
    ra = *reinterpret_cast<const v4u*>(A.x0 + (size_t)r0 * DM + l * 8);
    rb = *reinterpret_cast<const v4u*>(A.x0 + (size_t)(r0 + 1) * DM + l * 8);
  }
  const int pn = w < 4 ? DM * NST * 2 : 0;
  const unsigned p1 = __builtin_amdgcn_raw_buffer_load_b32(MKRS(A.logAre, pn), l * 4, d * NST * 2, 0), p2 = __builtin_amdgcn_raw_buffer_load_b32(MKRS(A.Aim, pn), l * 4, d * NST * 2, 0),
                 p3 = __builtin_amdgcn_raw_buffer_load_b32(MKRS(A.Cre, pn), l * 4, d * NST * 2, 0), p4 = __builtin_amdgcn_raw_buffer_load_b32(MKRS(A.Cim, pn), l * 4, d * NST * 2, 0);
  const u16 p0 = A.logdt[d];
  if (w >= 6) {
#pragma unroll
    for (int i = 0; i < 8; ++i) raw[i] = *reinterpret_cast<const v4u*>(A.x0 + (size_t)(8 * sx + i) * DM + l * 8);
  }
  const v2u en = LD64(MKRS(A.ws + OFF_TAG, 8192 + 8), 0, 8192);
  {
    const __amdgpu_buffer_rsrc_t rz = MKRS(A.ws + OFF_Z, Z_BYTES);
    const unsigned i0 = hi < 16 ? ACC_LO : 0u, i1 = hi < 16 ? ACC_HI : 0u;
    ST128((v4u{i0, i1, i0, i1}), rz, tid * 16, hi * 8192);
    ST128((v4u{0u, 0u, 0u, 0u}), rz, tid * 16, (128 + hi) * 8192);
  }
  if (xi >= 0) {
    const f32x2_t sa = row_sums(ra), sb = row_sums(rb);
    const unsigned st = row_stats((l & 1) ? sb : sa);
    const unsigned long long m = (((unsigned long long)en.y << 32) | en.x) + TAG_STEP;
    ST128((v4u{st, (unsigned)m, st, (unsigned)(m >> 32)}), MKRS(A.ws + OFF_G0, 8192), (l >> 1) * 2048 + (l & 1) * 16, r0 * 16);
    SST(11);
  }
  const int pj = (blockIdx.x >> 3) - 8;
  const bool ps = PFW && w < 6 && pj < PNH;
  if (__builtin_expect(ps, 1)) {
    const bool sl = hi < 32;
    const unsigned voff = sl ? (l >> 2) * (HD * 2) + (l & 2) * 512 + (l & 1) * 128 : (l >> 4) * (HD * 2) + (l & 15) * 128;
#pragma unroll
    for (int e = 0; e < 2; ++e) {
      const int i = sl ? 4 * (pj + 8 * w + 48 * e) : pj + PNH * (w + 6 * e);
      const unsigned long long m = matrix(A, i >> 7);
      if (e < PN1 || __builtin_expect(sl, 1))
        pull(m + (i & 127) * (8 * HD) + (blockIdx.x & 6) * 1024 + (sl ? ((1 - (int)(m >> 8)) & 3) * 256 : 0), voff, smem);
    }
  }
  asm volatile("" ::"v"(txt), "v"(p0), "v"(p1), "v"(p2), "v"(p3), "v"(p4) : "memory");
#define CLEARED() do { if (!ps) WAIT_VM(0); else if (hi < 32) WAIT_VM(2); else wait_vm<PN1>(); __builtin_amdgcn_s_barrier(); } while (0)
  if (w >= 4) {
    tour<208, 220, 4>(w);
    PST(1);
    CLEARED();
    PST(2);
  }
  if (w >= 6) {
    __builtin_amdgcn_s_waitcnt(0x0F70);
    if (w == 7 && l < 8)
      __hip_atomic_store((gu64*)(A.ws + OFF_TAG) + l * 128 + hi, (((unsigned long long)en.y << 32) | en.x) + TAG_STEP, RLX_AGENT);
    __builtin_amdgcn_s_setprio(3);
    PST(8);
    v4u wr[32];
    const u16* wop = A.Wo + (size_t)(hi * DH + 64 * (w - 6)) * DM + l * 8;
    if (hi < 32) {
#pragma unroll
      for (int i = 0; i < 32; ++i) wr[i] = *reinterpret_cast<const v4u*>(wop + i * DM);
    }
    f32x2_t sq = {};
#pragma unroll
    for (int i = 0; i < 8; ++i) {
      if (i == 0) pad<203, 1000>(raw[i]);
      if (i == 3) { asm volatile("" : "+v"(sq), "+v"(raw[i])); pad<206>(sq); }
      if (i == 6) { asm volatile("" : "+v"(sq), "+v"(raw[i])); pad<207, 300>(sq); }
      const f32x2_t t = row_sums(raw[i]);
      if (l == i) sq = t;
    }
    pad<218, 500>(sq);
    const unsigned stw = row_stats(sq);
    if (l < 8) __hip_atomic_store((gu32*)(A.ws + OFF_GT) + 8 * sx + l, stw, RLX_AGENT);
    pad<216>();
    PST(9);
    float m = 0.f;
    if (hi < 32) {
      float c0 = 0.f, c1 = 0.f, c2 = 0.f, c3 = 0.f;
#pragma unroll
      for (int i = 0; i < 64; ++i) {
        const v4u x = wr[i & 31];
        if (i < 32) wr[i] = *reinterpret_cast<const v4u*>(wop + (32 + i) * DM);
        if (i == 20) pad<204, 600>(c0);
        c0 = dot2sq(x.x, c0); c1 = dot2sq(x.y, c1); c2 = dot2sq(x.z, c2); c3 = dot2sq(x.w, c3);
      }
      pad<205>(c0);
      m = wave_max_dpp(fmaxf(fmaxf(c0, c1), fmaxf(c2, c3)));
    }
    zero_wait(A.ws);
    DRAIN();
    PST(10);
    if (l < 4) {
      if (hi < 32) __hip_atomic_store((gu32*)(A.ws + OFF_FL) + FL_G + (l * 4 + w - 5) * 32 + hi, __float_as_uint(m) | 1u, RLX_AGENT);
      set_flag((unsigned*)(A.ws + OFF_FL) + FL_FS + (l * 32 + (sx >> 3)) * 32 + (sx & 7));
    }
    __builtin_amdgcn_s_setprio(0);
  } else if (w >= 4) {
    pad<202>();
    __builtin_amdgcn_s_setprio(3);
    if (PFW && hi >= 32 && pj < PNH)
      for (int i = pj + PNH * (6 * PN1 + w - 4); i < (PWO ? 512 : 384); i += 2 * PNH) pull_rows(A, smem, i);
    __builtin_amdgcn_s_setprio(0);
    PST(8);
    if (aw >= 0) {
      pad<219>();
      if (AWE) gate(t0 + AWE);
#pragma unroll 1
      for (int v = 0; v < 8; ++v)
#pragma unroll
        for (int i = 0; i < 4; ++i) wo_issue(A.Wo, aw, w & 1, v, i, lds_base(smem));
    }
    DRAIN();
  } else {
    pad<201, 300>();
    f32x2_t a[2], ew[3][2];
    const float dt = bfr(expf(bf2f(p0)));
#pragma unroll
    for (int e = 0; e < 2; ++e) {
      const float ar = -bfr(expf(e ? hi16(p1) : lo16(p1))) * dt, ai = (e ? hi16(p2) : lo16(p2)) * dt;
      const float th = ai * 0.15915494f, tl = __builtin_fmaf(ai, 0.15915494f, -th) + ai * 6.4206383e-9f;
      a[e] = f32x2_t{(e ? hi16(p3) : lo16(p3)) * dt, (e ? hi16(p4) : lo16(p4)) * dt} * 2.0f;
#pragma unroll
      for (int k = 0; k < 3; ++k) {
        const float st = k == 0 ? 1.0f : k == 1 ? -8.0f : 128.0f;
        const float ex = __expf(ar * fabsf(st)), t = __builtin_fmaf(tl, st, __builtin_amdgcn_fractf(th * st));
        ew[k][e] = f32x2_t{__builtin_amdgcn_cosf(t), __builtin_amdgcn_sinf(t)} * ex;
      }
    }
    pad<217, 300>(a[0]);
    PST(1);
    const int c = l & 15, g = l >> 4;
    v2u* wr = reinterpret_cast<v2u*>(smem + 65536 + w * 16640) + l;
    const bf16x8* rd = reinterpret_cast<const bf16x8*>(smem + 65536 + w * 16640) + 65 * c + g;
    unsigned* kd = reinterpret_cast<unsigned*>(smem + 139264) + w * 516;
    f32x2_t b[2] = {ew[1][0], ew[1][1]};
    wr[0] = v2u{0x3f80u, 0x3f80u};
    wr[64] = v2u{0u, 0u};
#pragma unroll
    for (int n = 1; n < 16; ++n) {
      if (n < 15) put_row(wr + 130 * n, b, b, ew[1]);
      else put_row(wr + 130 * n, b, a, ew[2]);
      if (n == 8) pad<208, 200>(b[0]);
      if (n == KBR) { PST(7); CLEARED(); PST(2); }
    }
    pad<209, 200>(b[0]);
    bf16x8 tp[16];
#pragma unroll
    for (int i = 0; i < 16; ++i) tp[i] = rd[4 * i];
#pragma unroll
    for (int t = 0; t < 2; ++t) {
#pragma unroll
      for (int n = 0; n < 16; ++n) {
        const int cl = 2 * t + (n >> 3), mk = (8 >> cl) - 1;
        if (!(n & mk)) { a[0] = b[0]; a[1] = b[1]; }
        if (!(n & 7) && (cl == 1 || cl == 2)) { ew[0][0] = cmul(ew[0][0], ew[0][0]); ew[0][1] = cmul(ew[0][1], ew[0][1]); }
        if ((n + 1) & mk) put_row(wr + 130 * n, b, b, ew[0]);
        else put_row(wr + 130 * n, b, a, ew[2]);
        if (t == 0 && n == 6) pad<210, 200>(b[0]);
        if (t == 0 && n == 13) pad<211, 200>(b[0]);
        if (t == 1 && n == 0) pad<212, 200>(b[0]);
        if (t == 1 && n == 9) pad<213, 420>(b[0]);
      }
      bf16x8 x[16];
#pragma unroll
      for (int i = 0; i < 16; ++i) x[i] = rd[4 * i];
      __builtin_amdgcn_sched_barrier(0);
      f32x4 s0 = {}, s1 = {}, s2 = {};
      if (t == 1) pad<214, 200>(s0);
#pragma unroll
      for (int i = 0; i < 8; ++i) {
        s0 = MFMA16(tp[i], x[i], s0);
        s1 = MFMA16(tp[8 + i], x[i], s1);
        s2 = MFMA16(tp[i], x[8 + i], s2);
      }
      const int cl = 2 * t + (c >> 3), m = c & 7;
      const int ix = 128 * cl + ((m >> (3 - cl)) << (7 - cl)) + (m & ((8 >> cl) - 1)) + (g << (5 - cl));
#pragma unroll
      for (int r = 0; r < 4; ++r) {
        const float v = s0[r] + (s1[r] + s2[r]);
        kd[ix + (r << (3 - cl))] = pk2(v, v - lo16(pk2(v, v)));
      }
    }
    const float z = wave_sum_dpp(b[0].x + b[1].x);
    if (l == 0) kd[512] = pk2(z, z - lo16(pk2(z, z)));
    PST(4);
    const __amdgpu_buffer_rsrc_t rs = MKRS(A.ws + OFF_KF + d * (4 * CKN), 1960 * 2), rt = MKRS(A.ws + OFF_KF + d * (4 * CKN) + 2 * CKN, 1960 * 2);
    const unsigned kb = lds_base(smem) + 139264 + w * 2064 - 0x4300 * 4;
    float lag = (float)(2080 - 8 * l);
    pad<215, 800>(lag);
#pragma unroll 1
    for (int i = 0; i < 2; ++i) {
      unsigned k0[8], k1[8];
#pragma unroll
      for (int e = 0; e < 8; ++e) {
        const float f = lag - (float)(512 * (e >> 2) + 2 * (e & 3));
        const unsigned pb = pk2(f, f - 1.0f);
        unsigned a0, a1;
        asm("v_mad_u32_u16 %0, %2, 4, %3\n\tv_mad_u32_u16 %1, %2, 4, %3 op_sel:[1,0,0,0]" : "=&v"(a0), "=v"(a1) : "v"(pb), "s"(kb));
        k0[e] = *(lu32*)(unsigned long)a0;
        k1[e] = *(lu32*)(unsigned long)a1;
      }
#pragma unroll
      for (int q = 0; q < 2; ++q) {
        v4u oh, ol;
#pragma unroll
        for (int e = 0; e < 4; ++e) {
          oh[e] = __builtin_amdgcn_perm(k1[4 * q + e], k0[4 * q + e], 0x05040100u);
          ol[e] = __builtin_amdgcn_perm(k1[4 * q + e], k0[4 * q + e], 0x07060302u);
        }
        ST128(oh, rs, 16 * l + 2048 * i + 1024 * q, 0);
        ST128(ol, rt, 16 * l + 2048 * i + 1024 * q, 0);
      }
      lag -= 1024.0f;
    }
    PST(5);
    zero_wait(A.ws);
    DRAIN();
    if (l == 0) set_flag((unsigned*)(A.ws + OFF_FL) + FL_KG + (hi >> 1) * 32 + (d & 7));
  }
  PST(3);
  RAW_BAR();
  PST(6);
  PSE();
  pad<200, 700>();
  SST(3);
}

constexpr int C_SM = 131072, C_SO = C_SM + 8192, C_KT = C_SO + 2048, C_X = C_KT + 16384, C_SQ = C_X + 1024;
constexpr int C_SK = C_SQ + 2432;
static_assert(C_SK + 8 * QS * 2 <= 163808, "lds");
#define CHAIN_ASM_ATT \
  "s_mov_b32 s66, %[d0]\n" \
  "s_mov_b32 s94, 576\n" \
  "s_mov_b32 s95, 288\n" \
  "s_mov_b32 s84, 0x43400000\n" \
  "s_mov_b32 s85, 0x43400000\n" \
  "s_mov_b32 s92, 0x88000000\n" \
  "s_mov_b32 s93, 0x87fffff7\n" \
  "s_mov_b32 s78, 0x3e0293ee\n" \
  "s_mov_b32 s76, 0x33333333\n" \
  "s_mov_b32 s77, 0x33333333\n" \
  "s_add_u32 s58, s56, s60\n" \
  "s_addc_u32 s59, s57, 0\n" \
  "s_xor_b32 s38, s35, 0\n" \
  "s_add_u32 s26, s58, s38\n" \
  "s_addc_u32 s27, s59, 0\n" \
  "s_xor_b32 s38, s35, 1024\n" \
  "s_add_u32 s28, s58, s38\n" \
  "s_addc_u32 s29, s59, 0\n" \
  "s_xor_b32 s38, s35, 2048\n" \
  "s_add_u32 s30, s58, s38\n" \
  "s_addc_u32 s31, s59, 0\n" \
  "s_xor_b32 s38, s35, 3072\n" \
  "s_add_u32 s32, s58, s38\n" \
  "s_addc_u32 s33, s59, 0\n" \
  "s_lshr_b32 s98, s60, 2\n" \
  "s_add_u32 s98, s98, s74\n" \
  "s_add_u32 s98, s98, 131072\n" \
  "s_lshl_b32 s97, s73, 5\n" \
  "s_add_u32 s97, s97, s74\n" \
  "s_add_u32 s97, s97, 161152\n" \
  "s_lshl_b32 s99, s73, 10\n" \
  "s_mov_b32 s65, 9\n" \
  "v_mbcnt_lo_u32_b32 v48, -1, 0\n" \
  "v_mbcnt_hi_u32_b32 v48, -1, v48\n" \
  "v_and_b32 v49, 15, v48\n" \
  "v_lshl_add_u32 v9, v49, 1, s97\n" \
  "v_lshrrev_b32 v49, 4, v48\n" \
  "v_mad_u32_u24 v9, v49, s94, v9\n" \
  "v_add_u32 v7, s99, v1\n" \
  "v_mov_b64 v[12:13], 0\n" \
  "v_mov_b64 v[14:15], 0\n" \
  "v_mov_b64 v[16:17], 0\n" \
  "v_mov_b64 v[18:19], 0\n" \
  "v_mov_b64 v[20:21], 0\n" \
  "v_mov_b64 v[22:23], 0\n" \
  "s_lshl_b32 s88, s73, 10\n" \
  "v_add_u32 v24, s88, v1\n" \
  "ds_read_b128 v[32:35], v24 offset:0\n" \
  "s_waitcnt lgkmcnt(0)\n" \
  "v_mov_b32 v45, 0\n" \
  "v_dot2c_f32_bf16 v45, v32, v32\n" \
  "v_dot2c_f32_bf16 v45, v33, v33\n" \
  "v_dot2c_f32_bf16 v45, v34, v34\n" \
  "v_dot2c_f32_bf16 v45, v35, v35\n" \
  "s_add_u32 s88, s74, 157760\n" \
  "v_mov_b32 v44, s88\n" \
  "s_nop 0\n" \
  "v_cmpx_lt_f32 vcc, 2.0, v45\n" \
  "ds_max_u32 v44, v45\n" \
  "s_mov_b64 exec, -1\n" \
  "s_branch .LCA_AL_%=\n" \
  ".p2alignl 12, 0xbf800000, 3600\n" \
  ".p2align 6\n" \
  ".Lpad0x6d:\n" \
  "s_setpc_b64 s[98:99]\n" \
  ".p2align 6\n" \
  ".LCA_AL_%=:\n" \
  ".LCA_TOP_%=:\n" \
  "s_waitcnt lgkmcnt(0)\n" \
  "s_barrier\n" \
  "ds_read_b32 v8, v1 offset:26688\n" \
  "s_cmp_lg_u32 s73, s65\n" \
  "s_cbranch_scc1 .LCA_NOTK_%=\n" \
  "s_memrealtime s[38:39]\n" \
  "v_mov_b32 v26, 1\n" \
  "s_mul_i32 s23, s71, %[pp]\n" \
  "s_lshr_b32 s62, s66, 4\n" \
  "s_mov_b64 exec, 1\n" \
  "s_waitcnt lgkmcnt(0)\n" \
  "s_mov_b32 s67, s38\n" \
  "s_add_u32 s38, s38, s62\n" \
  "v_mov_b32 v25, s38\n" \
  "buffer_atomic_umax v25, off, s[16:19], s68\n" \
  "buffer_atomic_add v26, off, s[16:19], s68 offset:64\n" \
  "v_add_u32 v27, s23, v25\n" \
  "buffer_store_dword v27, off, s[16:19], s69 sc1\n" \
  "s_mov_b64 exec, -1\n" \
  "s_branch .LCA_SIDE_%=\n" \
  ".LCA_NOTK_%=:\n" \
  "s_cmp_lg_u32 s73, 0\n" \
  "s_cbranch_scc1 .LCA_NOZ_%=\n" \
  "v_mov_b64 v[26:27], s[92:93]\n" \
  "s_xor_b32 s88, s60, 0x8000\n" \
  "s_add_u32 s88, s88, s61\n" \
  "buffer_store_dwordx2 v[26:27], v5, s[40:43], s88 offen sc1\n" \
  ".LCA_NOZ_%=:\n" \
  "s_nop 7\n" \
  ".LCA_SIDE_%=:\n" \
  "s_waitcnt lgkmcnt(0)\n" \
  "v_readfirstlane_b32 s86, v8\n" \
  "s_mov_b64 vcc, s[76:77]\n" \
  "s_lshr_b32 s86, s86, 23\n" \
  "s_add_i32 s86, s86, s83\n" \
  "s_ashr_i32 s86, s86, 1\n" \
  "s_max_i32 s86, s86, s82\n" \
  "s_min_i32 s86, s86, 100\n" \
  "s_lshl_b32 s86, s86, 23\n" \
  "s_add_i32 s75, s86, 0x3c800000\n" \
  "s_sub_u32 s38, s71, 1\n" \
  "s_cmp_ge_u32 s38, 62\n" \
  "ds_read_b128 v[32:35], v1 offset:0\n" \
  "ds_read_b128 v[36:39], v1 offset:1024\n" \
  "ds_read_b128 v[48:51], v1 offset:2048\n" \
  "ds_read_b128 v[52:55], v1 offset:3072\n" \
  "ds_read_b128 v[40:43], v1 offset:4096\n" \
  "ds_read_b128 v[56:59], v1 offset:5120\n" \
  "ds_read_b128 v[60:63], v1 offset:6144\n" \
  "ds_read_b128 v[44:47], v1 offset:7168\n" \
  "s_setprio 3\n" \
  "s_waitcnt lgkmcnt(7)\n" \
  "v_smfmac_f32_16x16x64_bf16 v[12:15], v[32:35], v[64:71], v0\n" \
  "s_waitcnt lgkmcnt(6)\n" \
  "v_smfmac_f32_16x16x64_bf16 v[12:15], v[36:39], v[72:79], v0\n" \
  "s_waitcnt lgkmcnt(5)\n" \
  "v_smfmac_f32_16x16x64_bf16 v[12:15], v[48:51], v[80:87], v0\n" \
  "s_waitcnt lgkmcnt(4)\n" \
  "v_smfmac_f32_16x16x64_bf16 v[12:15], v[52:55], v[88:95], v0\n" \
  "s_waitcnt lgkmcnt(3)\n" \
  "v_smfmac_f32_16x16x64_bf16 v[12:15], v[40:43], v[96:103], v0\n" \
  "s_waitcnt lgkmcnt(2)\n" \
  "v_smfmac_f32_16x16x64_bf16 v[12:15], v[56:59], v[104:111], v0\n" \
  "s_waitcnt lgkmcnt(1)\n" \
  "v_smfmac_f32_16x16x64_bf16 v[12:15], v[60:63], v[112:119], v0\n" \
  "s_waitcnt lgkmcnt(0)\n" \
  "v_smfmac_f32_16x16x64_bf16 v[12:15], v[44:47], v[120:127], v0\n" \
  "ds_read_b128 v[24:27], v1 offset:10240\n" \
  "ds_read_b128 v[28:31], v1 offset:14336\n" \
  "s_nop 5\n" \
  "v_add_f32 v12, v12, v14\n" \
  "v_add_f32 v13, v13, v15\n" \
  "v_cvt_pk_bf16_f32 v12, v12, v13\n" \
  "ds_write_b16 v9, v12 offset:0\n" \
  "ds_write_b16_d16_hi v9, v12 offset:288\n" \
  "s_setprio 0\n" \
  "s_barrier\n" \
  "v_smfmac_f32_16x16x64_bf16 v[16:19], v[32:35], v[128:135], v0\n" \
  "v_smfmac_f32_16x16x64_bf16 v[20:23], v[32:35], v[192:199], v0\n" \
  "ds_read_b128 v[32:35], v1 offset:11264\n" \
  "v_smfmac_f32_16x16x64_bf16 v[16:19], v[36:39], v[136:143], v0\n" \
  "v_smfmac_f32_16x16x64_bf16 v[20:23], v[36:39], v[200:207], v0\n" \
  "ds_read_b128 v[36:39], v1 offset:15360\n" \
  "v_smfmac_f32_16x16x64_bf16 v[16:19], v[48:51], v[144:151], v0\n" \
  "v_smfmac_f32_16x16x64_bf16 v[20:23], v[48:51], v[208:215], v0\n" \
  "v_smfmac_f32_16x16x64_bf16 v[16:19], v[52:55], v[152:159], v0\n" \
  "v_smfmac_f32_16x16x64_bf16 v[20:23], v[52:55], v[216:223], v0\n" \
  "v_smfmac_f32_16x16x64_bf16 v[16:19], v[40:43], v[160:167], v0\n" \
  "v_smfmac_f32_16x16x64_bf16 v[20:23], v[40:43], v[224:231], v0\n" \
  ".fill ((16 - (. - .LCA_AL_%=)) & 31) >> 2, 4, 0xbf800000\n" \
  "s_waitcnt lgkmcnt(2)\n" \
  "s_barrier\n" \
  "s_setprio 3\n" \
  "ds_read_b128 v[48:51], v2 offset:0\n" \
  "ds_read_b128 v[52:55], v2 offset:64\n" \
  "v_smfmac_f32_16x16x64_bf16 v[16:19], v[56:59], v[168:175], v0\n" \
  "v_smfmac_f32_16x16x64_bf16 v[20:23], v[56:59], v[232:239], v0\n" \
  "ds_read_b128 v[56:59], v2 offset:128\n" \
  "s_cbranch_scc1 .LCA_LAST_%=\n" \
  ".LCA_BACK_%=:\n" \
  "s_waitcnt lgkmcnt(2)\n" \
  "v_mfma_f32_16x16x32_bf16 v[12:15], v[24:27], v[48:51], 0\n" \
  "ds_read_b128 v[24:27], v1 offset:12288\n" \
  "v_mfma_f32_16x16x32_bf16 v[8:11], v[28:31], v[48:51], 0\n" \
  "ds_read_b128 v[28:31], v1 offset:16384\n" \
  "v_mfma_f32_16x16x32_bf16 v[40:43], v[48:51], v[48:51], 0\n" \
  "ds_read_b128 v[48:51], v7 offset:18432\n" \
  "v_smfmac_f32_16x16x64_bf16 v[16:19], v[60:63], v[176:183], v0\n" \
  "v_smfmac_f32_16x16x64_bf16 v[20:23], v[60:63], v[240:247], v0\n" \
  "ds_read_b128 v[60:63], v2 offset:192\n" \
  "s_waitcnt lgkmcnt(5)\n" \
  "v_mfma_f32_16x16x32_bf16 v[12:15], v[32:35], v[52:55], v[12:15]\n" \
  "ds_read_b128 v[32:35], v1 offset:13312\n" \
  "v_mfma_f32_16x16x32_bf16 v[8:11], v[36:39], v[52:55], v[8:11]\n" \
  "ds_read_b128 v[36:39], v1 offset:17408\n" \
  "v_mfma_f32_16x16x32_bf16 v[40:43], v[52:55], v[52:55], v[40:43]\n" \
  "ds_read_b128 v[52:55], v7 offset:22528\n" \
  "s_waitcnt lgkmcnt(7)\n" \
  "v_mfma_f32_16x16x32_bf16 v[40:43], v[56:59], v[56:59], v[40:43]\n" \
  "s_waitcnt lgkmcnt(5)\n" \
  "v_mfma_f32_16x16x32_bf16 v[12:15], v[24:27], v[56:59], v[12:15]\n" \
  "ds_read_b128 v[24:27], v6 offset:2048\n" \
  "v_mfma_f32_16x16x32_bf16 v[8:11], v[28:31], v[56:59], v[8:11]\n" \
  "ds_read_b128 v[28:31], v6 offset:3072\n" \
  "s_waitcnt lgkmcnt(5)\n" \
  "v_mfma_f32_16x16x32_bf16 v[40:43], v[60:63], v[60:63], v[40:43]\n" \
  "ds_read_b128 v[56:59], v6 offset:0\n" \
  "s_waitcnt lgkmcnt(4)\n" \
  "v_mfma_f32_16x16x32_bf16 v[12:15], v[32:35], v[60:63], v[12:15]\n" \
  "ds_read_b128 v[32:35], v6 offset:8192\n" \
  "v_mfma_f32_16x16x32_bf16 v[8:11], v[36:39], v[60:63], v[8:11]\n" \
  "ds_read_b128 v[36:39], v6 offset:9216\n" \
  "v_smfmac_f32_16x16x64_bf16 v[16:19], v[44:47], v[184:191], v0\n" \
  "ds_read_b128 v[60:63], v6 offset:1024\n" \
  "s_waitcnt lgkmcnt(6)\n" \
  "v_smfmac_f32_16x16x64_bf16 v[20:23], v[44:47], v[248:255], v0\n" \
  "v_cndmask_b32_dpp v43, v43, v42, vcc quad_perm:[0,1,0,1] row_mask:0xf bank_mask:0xf\n" \
  "v_mov_b32 v41, 0\n" \
  "v_mov_b32 v42, 0\n" \
  "v_cndmask_b32_dpp v12, v8, v12, vcc quad_perm:[0,1,0,1] row_mask:0xf bank_mask:0xf\n" \
  "v_cndmask_b32_dpp v13, v9, v13, vcc quad_perm:[0,1,0,1] row_mask:0xf bank_mask:0xf\n" \
  "v_cndmask_b32_dpp v14, v10, v14, vcc quad_perm:[0,1,0,1] row_mask:0xf bank_mask:0xf\n" \
  "v_cndmask_b32_dpp v15, v11, v15, vcc quad_perm:[0,1,0,1] row_mask:0xf bank_mask:0xf\n" \
  "v_maximum3_f32 v8, v43, v12, v13\n" \
  "v_maximum3_f32 v8, v8, v14, v15\n" \
  "v_add_f32 v16, v16, v18\n" \
  "v_add_f32 v17, v17, v19\n" \
  "v_max_f32_dpp v8, v8, v8 quad_perm:[2,3,0,1] row_mask:0xf bank_mask:0xf\n" \
  "v_mov_b32 v9, v8\n" \
  "v_add_f32 v20, v20, v22\n" \
  "v_add_f32 v21, v21, v23\n" \
  "v_permlane16_swap_b32 v8, v9\n" \
  "v_max_f32 v8, v8, v9\n" \
  "v_mov_b32 v9, v8\n" \
  "v_cvt_pk_bf16_f32 v16, v16, v17\n" \
  "v_cvt_pk_bf16_f32 v17, v20, v21\n" \
  "v_permlane32_swap_b32 v8, v9\n" \
  "v_max_f32 v8, v8, v9\n" \
  "s_barrier\n" \
  "buffer_load_dword v7, v5, s[16:19], s24 offen sc1\n" \
  "v_mul_f32 v8, s78, v8\n" \
  "v_pk_fma_f32 v[12:13], v[12:13], s[78:79], v[8:9] op_sel_hi:[1,0,0] neg_lo:[0,0,1] neg_hi:[0,0,1]\n" \
  "v_pk_fma_f32 v[14:15], v[14:15], s[78:79], v[8:9] op_sel_hi:[1,0,0] neg_lo:[0,0,1] neg_hi:[0,0,1]\n" \
  "v_fma_f32 v43, v43, s78, -v8\n" \
  "v_exp_f32 v12, v12\n" \
  "v_exp_f32 v13, v13\n" \
  "v_exp_f32 v14, v14\n" \
  "v_exp_f32 v15, v15\n" \
  "v_exp_f32 v43, v43\n" \
  "v_cvt_pk_bf16_f32 v44, v12, v13\n" \
  "v_cvt_pk_bf16_f32 v45, v14, v15\n" \
  "v_pk_add_f32 v[18:19], v[12:13], v[14:15]\n" \
  "v_mov_b32_dpp v46, v44 quad_perm:[2,3,0,1] row_mask:0xf bank_mask:0xf\n" \
  "v_mov_b32_dpp v9, v43 quad_perm:[2,3,0,1] row_mask:0xf bank_mask:0xf\n" \
  "v_mov_b32_dpp v47, v45 quad_perm:[2,3,0,1] row_mask:0xf bank_mask:0xf\n" \
  "v_add_f32 v10, v18, v43\n" \
  "v_cvt_pk_bf16_f32 v40, v43, v9\n" \
  "v_add_f32 v10, v10, v19\n" \
  "v_mfma_f32_16x16x32_bf16 v[12:15], v[48:51], v[44:47], 0\n" \
  "v_cvt_pk_bf16_f32 v43, v43, v9\n" \
  "v_add_f32_dpp v10, v10, v10 quad_perm:[2,3,0,1] row_mask:0xf bank_mask:0xf\n" \
  "v_mfma_f32_16x16x32_bf16 v[20:23], v[52:55], v[44:47], 0\n" \
  "v_mul_f32 v11, s75, v10\n" \
  "v_mul_f32 v10, s75, v10\n" \
  "v_mfma_f32_16x16x16_bf16 v[12:15], v[16:17], v[40:41], v[12:15]\n" \
  "ds_read_b128 v[48:51], v6 offset:10240\n" \
  "v_permlane16_swap_b32 v10, v11\n" \
  "v_add_f32 v10, v10, v11\n" \
  "v_mfma_f32_16x16x16_bf16 v[20:23], v[16:17], v[42:43], v[20:23]\n" \
  "v_mov_b32 v11, v10\n" \
  "ds_read_b128 v[52:55], v6 offset:11264\n" \
  "v_mov_b64 v[18:19], s[84:85]\n" \
  "v_permlane32_swap_b32 v10, v11\n" \
  "v_add_f32 v10, v10, v11\n" \
  "v_rcp_f32 v10, v10\n" \
  "s_mov_b32 s0, 0xbe801d62\n" \
  ".set .Lpad0x6e, . - 4\n" \
  "v_pk_mul_f32 v[12:13], v[12:13], v[10:11] op_sel_hi:[1,0]\n" \
  "v_pk_mul_f32 v[14:15], v[14:15], v[10:11] op_sel_hi:[1,0]\n" \
  "v_pk_mul_f32 v[20:21], v[20:21], v[10:11] op_sel_hi:[1,0]\n" \
  "v_pk_mul_f32 v[22:23], v[22:23], v[10:11] op_sel_hi:[1,0]\n" \
  "v_cvt_pk_bf16_f32 v40, v12, v13\n" \
  "v_cvt_pk_bf16_f32 v41, v14, v15\n" \
  "v_cvt_pk_bf16_f32 v42, v20, v21\n" \
  "ds_write_b64 v4, v[40:41]\n" \
  "v_cvt_pk_bf16_f32 v43, v22, v23\n" \
  "ds_write_b64 v4, v[42:43] offset:1024\n" \
  "v_max_u32 v16, 1, v16\n" \
  "v_max_u32 v17, 1, v17\n" \
  "buffer_store_dwordx2 v[16:17], v3, s[52:55], s70 offen sc1\n" \
  "v_mov_b64 v[12:13], s[84:85]\n" \
  "v_mov_b64 v[14:15], s[84:85]\n" \
  "v_mov_b64 v[16:17], s[84:85]\n" \
  "v_mov_b64 v[20:21], s[84:85]\n" \
  "v_mov_b64 v[22:23], s[84:85]\n" \
  "v_mov_b64 v[8:9], s[84:85]\n" \
  "v_mov_b64 v[10:11], s[84:85]\n" \
  "s_setprio 0\n" \
  "s_waitcnt lgkmcnt(0)\n" \
  "s_barrier\n" \
  "ds_read_b128 v[40:43], v1 offset:8192\n" \
  "ds_read_b128 v[44:47], v1 offset:9216\n" \
  "s_waitcnt lgkmcnt(1)\n" \
  "v_smfmac_f32_16x16x64_bf16 v[12:15], v[40:43], v[56:63], v0\n" \
  "s_waitcnt lgkmcnt(0)\n" \
  "v_smfmac_f32_16x16x64_bf16 v[12:15], v[44:47], v[32:39], v0\n" \
  "ds_read_b128 v[56:59], v6 offset:4096\n" \
  "ds_read_b128 v[60:63], v6 offset:5120\n" \
  "v_smfmac_f32_16x16x64_bf16 v[16:19], v[40:43], v[24:31], v0\n" \
  "ds_read_b128 v[32:35], v6 offset:12288\n" \
  "ds_read_b128 v[36:39], v6 offset:13312\n" \
  "v_smfmac_f32_16x16x64_bf16 v[16:19], v[44:47], v[48:55], v0\n" \
  "ds_read_b128 v[24:27], v6 offset:6144\n" \
  "ds_read_b128 v[28:31], v6 offset:7168\n" \
  "v_add_f32 v12, v12, v14\n" \
  "v_add_f32 v13, v13, v15\n" \
  "global_atomic_add_x2 v5, v[12:13], s[26:27]\n" \
  "ds_read_b128 v[48:51], v6 offset:14336\n" \
  "ds_read_b128 v[52:55], v6 offset:15360\n" \
  "s_waitcnt lgkmcnt(6)\n" \
  "v_add_f32 v16, v16, v18\n" \
  "v_add_f32 v17, v17, v19\n" \
  "global_atomic_add_x2 v5, v[16:17], s[28:29]\n" \
  "v_smfmac_f32_16x16x64_bf16 v[20:23], v[40:43], v[56:63], v0\n" \
  "s_waitcnt lgkmcnt(4)\n" \
  "v_smfmac_f32_16x16x64_bf16 v[20:23], v[44:47], v[32:39], v0\n" \
  "s_waitcnt lgkmcnt(2)\n" \
  "v_smfmac_f32_16x16x64_bf16 v[8:11], v[40:43], v[24:31], v0\n" \
  "s_waitcnt lgkmcnt(0)\n" \
  "v_smfmac_f32_16x16x64_bf16 v[8:11], v[44:47], v[48:55], v0\n" \
  "s_nop 4\n" \
  "v_add_f32 v20, v20, v22\n" \
  "v_add_f32 v21, v21, v23\n" \
  "global_atomic_add_x2 v5, v[20:21], s[30:31]\n" \
  "v_add_f32 v8, v8, v10\n" \
  "v_add_f32 v9, v9, v11\n" \
  "global_atomic_add_x2 v5, v[8:9], s[32:33]\n" \
  "s_memrealtime s[62:63]\n" \
  "v_mbcnt_lo_u32_b32 v48, -1, 0\n" \
  "v_mbcnt_hi_u32_b32 v48, -1, v48\n" \
  "v_lshrrev_b32 v49, 3, v48\n" \
  "v_and_b32 v50, 7, v48\n" \
  "v_lshlrev_b32 v50, 4, v50\n" \
  "v_lshl_or_b32 v44, v49, 8, v50\n" \
  "s_add_u32 s20, s20, 32\n" \
  "s_mov_b32 s21, 0x8000\n" \
  "v_mov_b32 v52, s22\n" \
  "v_mov_b32 v54, s20\n" \
  "s_mul_i32 s88, s71, %[pp]\n" \
  "s_lshl_b32 s89, s71, 10\n" \
  "s_sub_u32 s88, s88, s25\n" \
  "s_mul_i32 s38, s24, %[an]\n" \
  "s_cmp_gt_u32 s89, s38\n" \
  "s_cselect_b32 s89, 0x1000, 0\n" \
  "s_waitcnt vmcnt(5)\n" \
  "v_mov_b32 v46, v7\n" \
  "s_branch .LCA_TKE_%=\n" \
  ".LCA_TKR_%=:\n" \
  "s_cmp_lg_u32 s73, 0\n" \
  "s_cbranch_scc1 .LCA_TKW_%=\n" \
  "s_sub_u32 s21, s21, 1\n" \
  "s_cbranch_scc1 .LCA_TKO_%=\n" \
  "s_and_b32 s38, s21, 7\n" \
  "s_cmp_lg_u32 s38, 7\n" \
  "s_cbranch_scc1 .LCA_TKS_%=\n" \
  "buffer_load_dword v46, v5, s[16:19], s24 offen sc1\n" \
  ".LCA_TKS_%=:\n" \
  "s_sleep 1\n" \
  ".LCA_TKE_%=:\n" \
  "v_readlane_b32 s38, v46, 8\n" \
  "v_readlane_b32 s39, v46, 24\n" \
  "v_readlane_b32 s23, v46, 40\n" \
  "v_readlane_b32 s34, v46, 56\n" \
  "s_add_u32 s38, s38, s39\n" \
  "s_add_u32 s23, s23, s34\n" \
  "s_add_u32 s38, s38, s23\n" \
  "s_cmp_lt_u32 s38, s20\n" \
  "s_cbranch_scc1 .LCA_TKR_%=\n" \
  ".LCA_TKO_%=:\n" \
  "v_readlane_b32 s34, v46, 0\n" \
  "v_readlane_b32 s38, v46, 16\n" \
  "v_readlane_b32 s39, v46, 32\n" \
  "v_readlane_b32 s23, v46, 48\n" \
  "s_max_u32 s34, s34, s38\n" \
  "s_max_u32 s39, s39, s23\n" \
  "s_add_u32 s34, s34, %[cd]\n" \
  "v_readlane_b32 s23, v46, 1\n" \
  "s_max_u32 s34, s34, s39\n" \
  "s_sub_u32 s23, s23, s34\n" \
  "s_sub_u32 s23, s23, s88\n" \
  "s_cmp_lt_u32 s23, s89\n" \
  "s_cselect_b32 s23, s23, 0\n" \
  "s_min_u32 s23, s23, %[ka]\n" \
  "s_add_u32 s34, s34, s23\n" \
  "s_add_u32 s34, s34, s64\n" \
  ".LCA_TKD_%=:\n" \
  "v_mov_b32 v55, s34\n" \
  "ds_write_b64 v52, v[54:55]\n" \
  "s_branch .LCA_WAIU_%=\n" \
  ".Lpad0x6f:\n" \
  "s_setpc_b64 s[98:99]\n" \
  ".LCA_WAIS_%=:\n" \
  "s_cmp_lt_i32 s39, -1000\n" \
  "s_cbranch_scc1 .LCA_WAIE_%=\n" \
  "s_sleep 1\n" \
  ".LCA_WAIU_%=:\n" \
  "s_memrealtime s[38:39]\n" \
  "s_waitcnt lgkmcnt(0)\n" \
  "s_sub_i32 s39, s38, s34\n" \
  "s_cmp_lt_i32 s39, -7\n" \
  "s_cbranch_scc1 .LCA_WAIS_%=\n" \
  "s_cmp_lt_i32 s39, 0\n" \
  "s_cbranch_scc1 .LCA_WAIU_%=\n" \
  ".LCA_WAIE_%=:\n" \
  "buffer_load_dwordx4 v[24:27], v44, s[40:43], s60 offen sc1\n" \
  "buffer_load_dwordx4 v[28:31], v44, s[40:43], s60 offen offset:2048 sc1\n" \
  "s_sub_u32 s62, s62, s67\n" \
  "s_lshl_b32 s62, s62, 4\n" \
  "s_sub_i32 s62, s62, s66\n" \
  "s_min_i32 s62, s62, 128\n" \
  "s_max_i32 s62, s62, -256\n" \
  "s_ashr_i32 s62, s62, 2\n" \
  "s_add_i32 s66, s66, s62\n" \
  "s_add_u32 s70, s70, 0x10000\n" \
  "v_mbcnt_lo_u32_b32 v48, -1, 0\n" \
  "v_mbcnt_hi_u32_b32 v48, -1, v48\n" \
  "v_and_b32 v49, 15, v48\n" \
  "v_lshl_add_u32 v9, v49, 1, s97\n" \
  "v_lshrrev_b32 v49, 4, v48\n" \
  "v_mad_u32_u24 v9, v49, s94, v9\n" \
  "v_add_u32 v7, s99, v1\n" \
  "v_mov_b64 v[12:13], 0\n" \
  "v_mov_b64 v[14:15], 0\n" \
  "v_mov_b64 v[16:17], 0\n" \
  "v_mov_b64 v[18:19], 0\n" \
  "v_mov_b64 v[20:21], 0\n" \
  "v_mov_b64 v[22:23], 0\n" \
  "v_and_b32 v49, 3, v48\n" \
  "v_lshl_add_u32 v40, v49, 2, s98\n" \
  "v_lshrrev_b32 v49, 2, v48\n" \
  "v_bfe_u32 v48, v48, 4, 1\n" \
  "v_xor_b32 v49, v49, v48\n" \
  "v_lshl_add_u32 v40, v49, 5, v40\n" \
  "s_add_i32 s80, s86, 0x35000000\n" \
  "s_sub_u32 s91, 0x4a350000, s86\n" \
  "s_min_u32 s91, s91, 0x4ca00000\n" \
  "s_mov_b32 s89, s60\n" \
  "s_xor_b32 s60, s60, 0x80\n" \
  "s_bitcmp0_b32 s60, 7\n" \
  "s_cselect_b32 s88, 0x8000, 0\n" \
  "s_add_u32 s60, s60, s88\n" \
  "s_and_b32 s60, s60, 0xffff\n" \
  "s_add_u32 s58, s56, s60\n" \
  "s_addc_u32 s59, s57, 0\n" \
  "s_xor_b32 s38, s35, 0\n" \
  "s_add_u32 s26, s58, s38\n" \
  "s_addc_u32 s27, s59, 0\n" \
  "s_xor_b32 s38, s35, 1024\n" \
  "s_add_u32 s28, s58, s38\n" \
  "s_addc_u32 s29, s59, 0\n" \
  "s_xor_b32 s38, s35, 2048\n" \
  "s_add_u32 s30, s58, s38\n" \
  "s_addc_u32 s31, s59, 0\n" \
  "s_xor_b32 s38, s35, 3072\n" \
  "s_add_u32 s32, s58, s38\n" \
  "s_addc_u32 s33, s59, 0\n" \
  "s_sub_u32 s71, s71, 1\n" \
  "s_mov_b32 s87, 0x40000\n" \
  "s_branch .LCA_CHK_%=\n" \
  ".LCA_POLL_%=:\n" \
  "buffer_load_dwordx4 v[24:27], v44, s[40:43], s89 offen sc1\n" \
  "buffer_load_dwordx4 v[28:31], v44, s[40:43], s89 offen offset:2048 sc1\n" \
  ".LCA_CHK_%=:\n" \
  "s_sub_u32 s87, s87, 1\n" \
  "s_cbranch_scc1 .LCA_END_%=\n" \
  "s_waitcnt vmcnt(1)\n" \
  "v_cvt_f32_i32 v32, v24\n" \
  "v_cvt_f32_i32 v33, v25\n" \
  "v_cvt_f32_i32 v34, v26\n" \
  "v_cvt_f32_i32 v35, v27\n" \
  "v_pk_mul_f32 v[24:25], v[32:33], s[80:81] op_sel_hi:[1,0]\n" \
  "v_pk_mul_f32 v[26:27], v[34:35], s[80:81] op_sel_hi:[1,0]\n" \
  "v_max3_f32 v45, |v32|, |v33|, |v34|\n" \
  "v_cvt_pk_bf16_f32 v24, v24, v26\n" \
  "v_cvt_pk_bf16_f32 v25, v25, v27\n" \
  "ds_write2_b32 v40, v24, v25 offset0:0 offset1:4\n" \
  "v_max3_f32 v45, |v45|, |v35|, |v35|\n" \
  "s_waitcnt vmcnt(0)\n" \
  "v_cvt_f32_i32 v32, v28\n" \
  "v_cvt_f32_i32 v33, v29\n" \
  "v_cvt_f32_i32 v34, v30\n" \
  "v_cvt_f32_i32 v35, v31\n" \
  "v_pk_mul_f32 v[28:29], v[32:33], s[80:81] op_sel_hi:[1,0]\n" \
  "v_pk_mul_f32 v[30:31], v[34:35], s[80:81] op_sel_hi:[1,0]\n" \
  "v_max3_f32 v45, |v45|, |v32|, |v33|\n" \
  "v_cvt_pk_bf16_f32 v28, v28, v30\n" \
  "v_cvt_pk_bf16_f32 v29, v29, v31\n" \
  "ds_write2_b32 v40, v28, v29 offset0:128 offset1:132\n" \
  "v_max3_f32 v45, |v45|, |v34|, |v35|\n" \
  "v_cmp_lt_f32 vcc, s91, v45\n" \
  "s_cbranch_vccnz .LCA_BIG_%=\n" \
  ".LCA_SML_%=:\n" \
  "s_branch .LCA_TOP_%=\n" \
  ".Lpad0x6b:\n" \
  "s_setpc_b64 s[98:99]\n" \
  ".LCA_BIG_%=:\n" \
  "v_cmp_lt_f32 vcc, 0x4ca00000, v45\n" \
  "s_cbranch_vccnz .LCA_POLL_%=\n" \
  "v_mul_f32 v45, s80, v45\n" \
  "v_mul_f32 v45, v45, v45\n" \
  "s_add_u32 s88, s74, 157760\n" \
  "v_mov_b32 v44, s88\n" \
  "s_nop 0\n" \
  "v_cmpx_lt_f32 vcc, 2.0, v45\n" \
  "ds_max_u32 v44, v45\n" \
  "s_mov_b64 exec, -1\n" \
  "s_branch .LCA_SML_%=\n" \
  ".LCA_TKW_%=:\n" \
  "s_andn2_b32 s38, s22, 24\n" \
  "v_mov_b32 v53, s38\n" \
  ".LCA_TKX_%=:\n" \
  "s_sleep 1\n" \
  "ds_read_b64 v[46:47], v53\n" \
  "s_sub_u32 s21, s21, 1\n" \
  "s_cbranch_scc1 .LCA_TKD_%=\n" \
  "s_waitcnt lgkmcnt(0)\n" \
  "v_readfirstlane_b32 s38, v46\n" \
  "v_readfirstlane_b32 s34, v47\n" \
  "s_cmp_eq_u32 s38, s20\n" \
  "s_cbranch_scc0 .LCA_TKX_%=\n" \
  "s_branch .LCA_TKD_%=\n" \
  ".LCA_FIRST_%=:\n" \
  "ds_read_b64 v[10:11], v1 offset:26624\n" \
  "ds_read_b128 v[24:27], v1 offset:10240\n" \
  "ds_read_b128 v[28:31], v1 offset:14336\n" \
  "ds_read_b128 v[32:35], v1 offset:11264\n" \
  "ds_read_b128 v[36:39], v1 offset:15360\n" \
  "s_mov_b32 s65, 3\n" \
  "s_waitcnt lgkmcnt(4)\n" \
  "v_readfirstlane_b32 s38, v11\n" \
  "v_add_u32 v9, %[d3], v11\n" \
  "s_cmp_eq_u32 s73, 3\n" \
  "s_cselect_b32 s39, s69, 0x1000\n" \
  "s_mov_b64 exec, 1\n" \
  "buffer_store_dword v9, off, s[16:19], s39 sc1\n" \
  "s_mov_b64 exec, -1\n" \
  "s_sub_u32 s67, s38, 40\n" \
  "v_mul_f32 v11, 0x44870000, v10\n" \
  "v_readfirstlane_b32 s83, v10\n" \
  "v_readfirstlane_b32 s82, v11\n" \
  "s_lshr_b32 s82, s82, 23\n" \
  "s_addk_i32 s82, 0xff83\n" \
  "s_ashr_i32 s82, s82, 1\n" \
  "s_max_i32 s82, s82, -20\n" \
  "s_min_i32 s82, s82, 100\n" \
  "s_lshr_b32 s83, s83, 23\n" \
  "s_sub_i32 s83, s83, 242\n" \
  "v_readfirstlane_b32 s86, v8\n" \
  "s_mov_b64 vcc, s[76:77]\n" \
  "s_lshr_b32 s86, s86, 23\n" \
  "s_add_i32 s86, s86, s83\n" \
  "s_ashr_i32 s86, s86, 1\n" \
  "s_max_i32 s86, s86, s82\n" \
  "s_min_i32 s86, s86, 100\n" \
  "s_lshl_b32 s86, s86, 23\n" \
  "s_add_i32 s75, s86, 0x3c800000\n" \
  "s_waitcnt lgkmcnt(0)\n" \
  "s_branch .LCA_BACK_%=\n" \
  ".LCA_LAST_%=:\n" \
  "s_cmp_lg_u32 s71, 0\n" \
  "s_cbranch_scc1 .LCA_FIRST_%=\n" \
  "v_smfmac_f32_16x16x64_bf16 v[16:19], v[60:63], v[176:183], v0\n" \
  "v_smfmac_f32_16x16x64_bf16 v[20:23], v[60:63], v[240:247], v0\n" \
  "v_smfmac_f32_16x16x64_bf16 v[16:19], v[44:47], v[184:191], v0\n" \
  "v_smfmac_f32_16x16x64_bf16 v[20:23], v[44:47], v[248:255], v0\n" \
  "s_nop 7\n" \
  "v_add_f32 v16, v16, v18\n" \
  "v_add_f32 v17, v17, v19\n" \
  "v_add_f32 v20, v20, v22\n" \
  "v_add_f32 v21, v21, v23\n" \
  "v_cvt_pk_bf16_f32 v16, v16, v17\n" \
  "v_cvt_pk_bf16_f32 v17, v20, v21\n" \
  "s_nop 1\n" \
  "s_nop 0\n" \
  "v_max_u32 v16, 1, v16\n" \
  "v_max_u32 v17, 1, v17\n" \
  "buffer_store_dwordx2 v[16:17], v3, s[52:55], s70 offen sc1\n" \
  "s_waitcnt vmcnt(0)\n" \
  ".LCA_END_%=:\n" \
  "s_endpgm\n"
#define CHAIN_ASM_VO \
  "s_mov_b32 s66, %[d0]\n" \
  "s_mov_b32 s94, 576\n" \
  "s_mov_b32 s95, 288\n" \
  "s_mov_b32 s84, 0x43400000\n" \
  "s_mov_b32 s85, 0x43400000\n" \
  "s_mov_b32 s92, 0x88000000\n" \
  "s_mov_b32 s93, 0x87fffff7\n" \
  "s_mov_b32 s78, 0x3e0293ee\n" \
  "s_mov_b32 s76, 0x33333333\n" \
  "s_mov_b32 s77, 0x33333333\n" \
  "s_add_u32 s58, s56, s60\n" \
  "s_addc_u32 s59, s57, 0\n" \
  "s_xor_b32 s38, s35, 0\n" \
  "s_add_u32 s26, s58, s38\n" \
  "s_addc_u32 s27, s59, 0\n" \
  "s_xor_b32 s38, s35, 1024\n" \
  "s_add_u32 s28, s58, s38\n" \
  "s_addc_u32 s29, s59, 0\n" \
  "s_xor_b32 s38, s35, 2048\n" \
  "s_add_u32 s30, s58, s38\n" \
  "s_addc_u32 s31, s59, 0\n" \
  "s_xor_b32 s38, s35, 3072\n" \
  "s_add_u32 s32, s58, s38\n" \
  "s_addc_u32 s33, s59, 0\n" \
  "s_lshr_b32 s98, s60, 2\n" \
  "s_add_u32 s98, s98, s74\n" \
  "s_add_u32 s98, s98, 131072\n" \
  "s_lshl_b32 s97, s73, 5\n" \
  "s_add_u32 s97, s97, s74\n" \
  "s_add_u32 s97, s97, 158592\n" \
  "s_lshl_b32 s99, s73, 11\n" \
  "s_add_u32 s99, s99, s74\n" \
  "s_add_u32 s99, s99, 133120\n" \
  "v_mbcnt_lo_u32_b32 v48, -1, 0\n" \
  "v_mbcnt_hi_u32_b32 v48, -1, v48\n" \
  "s_sub_u32 s88, s73, 4\n" \
  "v_and_b32 v51, 15, v48\n" \
  "v_lshl_add_u32 v4, v51, 1, s97\n" \
  "v_lshrrev_b32 v49, 4, v48\n" \
  "v_mad_u32_u24 v4, v49, s94, v4\n" \
  "v_lshrrev_b32 v49, 2, v51\n" \
  "v_and_b32 v50, 5, v51\n" \
  "v_and_or_b32 v49, v49, 2, v50\n" \
  "s_lshl_b32 s89, s88, 6\n" \
  "s_add_u32 s89, s89, s74\n" \
  "s_add_u32 s89, s89, 161152\n" \
  "v_and_b32 v50, 48, v48\n" \
  "v_add_u32 v6, s89, v50\n" \
  "v_mad_u32_u24 v6, v49, s95, v6\n" \
  "v_lshlrev_b32 v7, 3, v50\n" \
  "v_lshl_add_u32 v7, v49, 4, v7\n" \
  "v_and_b32 v49, 2, v51\n" \
  "v_lshl_add_u32 v7, v49, 29, v7\n" \
  "v_lshlrev_b32 v10, 4, v48\n" \
  "v_mov_b64 v[12:13], 0\n" \
  "v_mov_b64 v[14:15], 0\n" \
  "v_mov_b64 v[16:17], 0\n" \
  "v_mov_b64 v[18:19], 0\n" \
  "v_mov_b64 v[20:21], 0\n" \
  "v_mov_b64 v[22:23], 0\n" \
  "s_lshl_b32 s88, s73, 10\n" \
  "v_add_u32 v24, s88, v1\n" \
  "ds_read_b128 v[32:35], v24 offset:0\n" \
  "s_waitcnt lgkmcnt(0)\n" \
  "v_mov_b32 v45, 0\n" \
  "v_dot2c_f32_bf16 v45, v32, v32\n" \
  "v_dot2c_f32_bf16 v45, v33, v33\n" \
  "v_dot2c_f32_bf16 v45, v34, v34\n" \
  "v_dot2c_f32_bf16 v45, v35, v35\n" \
  "s_add_u32 s88, s74, 157760\n" \
  "v_mov_b32 v44, s88\n" \
  "s_nop 0\n" \
  "v_cmpx_lt_f32 vcc, 2.0, v45\n" \
  "ds_max_u32 v44, v45\n" \
  "s_mov_b64 exec, -1\n" \
  "v_bfe_u32 v60, v10, 2, 7\n" \
  "s_lshl_b32 s38, s24, 1\n" \
  "s_add_u32 s38, s38, %[so]\n" \
  "global_load_dword v61, v60, s[52:53] offset:0 sc1\n" \
  "global_load_dword v62, v60, s[52:53] offset:128 sc1\n" \
  "global_load_dword v63, v60, s[52:53] offset:256 sc1\n" \
  "v_lshrrev_b32 v56, 8, v10\n" \
  "v_lshl_add_u32 v56, v56, 7, s38\n" \
  "v_and_or_b32 v56, v60, 63, v56\n" \
  "s_cmp_lg_u32 s73, 7\n" \
  "s_cbranch_scc1 2\n" \
  "global_load_dword v57, v56, s[52:53] sc1\n" \
  "s_branch .LCV_AL_%=\n" \
  ".p2alignl 12, 0xbf800000, 3000\n" \
  ".p2align 6\n" \
  ".Lpad0x81:\n" \
  "s_setpc_b64 s[98:99]\n" \
  ".p2align 6\n" \
  ".LCV_AL_%=:\n" \
  ".LCV_TOP_%=:\n" \
  "s_waitcnt lgkmcnt(0)\n" \
  "s_barrier\n" \
  "s_setprio 2\n" \
  "ds_read_b128 v[24:27], v1 offset:0\n" \
  "ds_read_b128 v[28:31], v1 offset:1024\n" \
  "ds_read_b128 v[32:35], v1 offset:2048\n" \
  "ds_read_b128 v[36:39], v1 offset:3072\n" \
  "buffer_load_dword v5, off, s[44:47], s68 sc1\n" \
  "s_cmp_eq_u32 s71, 63\n" \
  "s_waitcnt lgkmcnt(3)\n" \
  "v_smfmac_f32_16x16x64_bf16 v[20:23], v[24:27], v[192:199], v0\n" \
  "ds_read_b128 v[40:43], v1 offset:4096\n" \
  "s_waitcnt lgkmcnt(3)\n" \
  "v_smfmac_f32_16x16x64_bf16 v[20:23], v[28:31], v[200:207], v0\n" \
  "ds_read_b128 v[44:47], v1 offset:5120\n" \
  "s_waitcnt lgkmcnt(3)\n" \
  "v_smfmac_f32_16x16x64_bf16 v[20:23], v[32:35], v[208:215], v0\n" \
  "ds_read_b128 v[48:51], v1 offset:6144\n" \
  "s_waitcnt lgkmcnt(3)\n" \
  "v_smfmac_f32_16x16x64_bf16 v[20:23], v[36:39], v[216:223], v0\n" \
  "ds_read_b128 v[52:55], v1 offset:7168\n" \
  "s_waitcnt lgkmcnt(3)\n" \
  "v_smfmac_f32_16x16x64_bf16 v[20:23], v[40:43], v[224:231], v0\n" \
  "s_waitcnt lgkmcnt(2)\n" \
  "v_smfmac_f32_16x16x64_bf16 v[20:23], v[44:47], v[232:239], v0\n" \
  "s_waitcnt lgkmcnt(1)\n" \
  "v_smfmac_f32_16x16x64_bf16 v[20:23], v[48:51], v[240:247], v0\n" \
  "s_waitcnt lgkmcnt(0)\n" \
  "v_smfmac_f32_16x16x64_bf16 v[20:23], v[52:55], v[248:255], v0\n" \
  "s_nop 0\n" \
  "v_smfmac_f32_16x16x64_bf16 v[12:15], v[24:27], v[64:71], v0\n" \
  "s_nop 0\n" \
  "v_smfmac_f32_16x16x64_bf16 v[12:15], v[28:31], v[72:79], v0\n" \
  "s_nop 0\n" \
  "v_smfmac_f32_16x16x64_bf16 v[12:15], v[32:35], v[80:87], v0\n" \
  "ds_read_b32 v11, v1 offset:26688\n" \
  "v_smfmac_f32_16x16x64_bf16 v[12:15], v[36:39], v[88:95], v0\n" \
  "v_add_f32 v20, v20, v22\n" \
  "v_smfmac_f32_16x16x64_bf16 v[12:15], v[40:43], v[96:103], v0\n" \
  "v_add_f32 v21, v21, v23\n" \
  "v_smfmac_f32_16x16x64_bf16 v[12:15], v[44:47], v[104:111], v0\n" \
  "v_cvt_pk_bf16_f32 v20, v20, v21\n" \
  "v_smfmac_f32_16x16x64_bf16 v[12:15], v[48:51], v[112:119], v0\n" \
  "ds_write_b16 v4, v20 offset:0\n" \
  "v_smfmac_f32_16x16x64_bf16 v[12:15], v[52:55], v[120:127], v0\n" \
  "ds_write_b16_d16_hi v4, v20 offset:288\n" \
  "v_smfmac_f32_16x16x64_bf16 v[16:19], v[24:27], v[128:135], v0\n" \
  "s_nop 1\n" \
  "v_smfmac_f32_16x16x64_bf16 v[16:19], v[28:31], v[136:143], v0\n" \
  "s_nop 1\n" \
  "v_smfmac_f32_16x16x64_bf16 v[16:19], v[32:35], v[144:151], v0\n" \
  "v_add_f32 v12, v12, v14\n" \
  "v_smfmac_f32_16x16x64_bf16 v[16:19], v[36:39], v[152:159], v0\n" \
  "v_add_f32 v13, v13, v15\n" \
  "v_smfmac_f32_16x16x64_bf16 v[16:19], v[40:43], v[160:167], v0\n" \
  "v_cvt_pk_bf16_f32 v12, v12, v13\n" \
  "v_smfmac_f32_16x16x64_bf16 v[16:19], v[44:47], v[168:175], v0\n" \
  "ds_write_b16 v4, v12 offset:128\n" \
  "v_smfmac_f32_16x16x64_bf16 v[16:19], v[48:51], v[176:183], v0\n" \
  "ds_write_b16_d16_hi v4, v12 offset:416\n" \
  "v_smfmac_f32_16x16x64_bf16 v[16:19], v[52:55], v[184:191], v0\n" \
  "s_barrier\n" \
  "s_nop 6\n" \
  "v_add_f32 v16, v16, v18\n" \
  "v_add_f32 v17, v17, v19\n" \
  "v_cvt_pk_bf16_f32 v16, v16, v17\n" \
  "ds_write_b16 v4, v16 offset:2560\n" \
  "ds_write_b16_d16_hi v4, v16 offset:2848\n" \
  "s_cbranch_scc1 .LCV_FIRST_%=\n" \
  ".LCV_BACK_%=:\n" \
  "s_waitcnt lgkmcnt(0)\n" \
  "s_barrier\n" \
  "s_setprio 0\n" \
  "ds_read_b128 v[60:63], v6 offset:0\n" \
  "v_readfirstlane_b32 s86, v11\n" \
  "s_add_u32 s88, s74, 157760\n" \
  "v_mov_b32 v8, s88\n" \
  "v_mov_b32 v9, 0\n" \
  "ds_write_b32 v8, v9\n" \
  "s_lshr_b32 s86, s86, 23\n" \
  "s_add_i32 s86, s86, s83\n" \
  "s_ashr_i32 s86, s86, 1\n" \
  "s_max_i32 s86, s86, s82\n" \
  "s_min_i32 s86, s86, 100\n" \
  "s_lshl_b32 s86, s86, 23\n" \
  "s_cmp_eq_u32 s71, 0\n" \
  "s_waitcnt lgkmcnt(1)\n" \
  "v_max_u32 v60, 1, v60\n" \
  "v_max_u32 v61, 1, v61\n" \
  "v_max_u32 v62, 1, v62\n" \
  "v_max_u32 v63, 1, v63\n" \
  "buffer_store_dwordx4 v[60:63], v7, s[48:51], s69 offen sc1\n" \
  "s_cbranch_scc1 .LCV_LAST_%=\n" \
  "s_waitcnt vmcnt(1)\n" \
  "v_cmp_eq_u32 vcc, 0, v5\n" \
  "s_cmp_eq_u64 vcc, 0\n" \
  "s_cselect_b32 s90, 1, 0\n" \
  "s_cselect_b32 s88, s71, 64\n" \
  "s_cmp_lt_u32 s88, 63\n" \
  "s_cselect_b32 s90, 1, 0\n" \
  "s_barrier\n" \
  "ds_read_b128 v[24:27], v3 offset:0\n" \
  "ds_read_b128 v[28:31], v3 offset:1024\n" \
  "ds_read_b128 v[32:35], v3 offset:2048\n" \
  "ds_read_b128 v[36:39], v3 offset:3072\n" \
  "ds_read_b128 v[40:43], v3 offset:8192\n" \
  "ds_read_b128 v[44:47], v3 offset:9216\n" \
  "ds_read_b128 v[48:51], v3 offset:10240\n" \
  "ds_read_b128 v[52:55], v3 offset:11264\n" \
  "ds_read_b128 v[56:59], v3 offset:4096\n" \
  "ds_read_b128 v[60:63], v3 offset:5120\n" \
  "s_cbranch_scc0 .LCV_NOTOK_%=\n" \
  "s_mov_b32 m0, s99\n" \
  "s_add_u32 s88, s99, 8192\n" \
  "global_load_lds_dwordx4 v10, s[62:63] sc1\n" \
  "global_load_lds_dwordx4 v10, s[62:63] offset:1024 sc1\n" \
  "s_mov_b32 m0, s88\n" \
  "s_nop 0\n" \
  "global_load_lds_dwordx4 v10, s[64:65] sc1\n" \
  "global_load_lds_dwordx4 v10, s[64:65] offset:1024 sc1\n" \
  ".LCV_NOTOK_%=:\n" \
  "v_mov_b64 v[12:13], s[84:85]\n" \
  "v_mov_b64 v[14:15], s[84:85]\n" \
  "v_mov_b64 v[16:17], s[84:85]\n" \
  "v_mov_b64 v[18:19], s[84:85]\n" \
  "v_mov_b64 v[20:21], s[84:85]\n" \
  "v_mov_b64 v[22:23], s[84:85]\n" \
  "s_waitcnt lgkmcnt(0)\n" \
  "s_barrier\n" \
  "ds_read_b128 v[4:7], v1 offset:8192\n" \
  "ds_read_b128 v[8:11], v1 offset:9216\n" \
  "s_waitcnt lgkmcnt(1)\n" \
  "v_smfmac_f32_16x16x64_bf16 v[12:15], v[4:7], v[24:31], v0\n" \
  "s_waitcnt lgkmcnt(0)\n" \
  "v_smfmac_f32_16x16x64_bf16 v[12:15], v[8:11], v[40:47], v0\n" \
  "ds_read_b128 v[24:27], v3 offset:12288\n" \
  "ds_read_b128 v[28:31], v3 offset:13312\n" \
  "v_smfmac_f32_16x16x64_bf16 v[16:19], v[4:7], v[32:39], v0\n" \
  "ds_read_b128 v[32:35], v3 offset:6144\n" \
  "ds_read_b128 v[36:39], v3 offset:7168\n" \
  "v_smfmac_f32_16x16x64_bf16 v[16:19], v[8:11], v[48:55], v0\n" \
  "s_cmp_eq_u32 s90, 1\n" \
  "s_cbranch_scc1 .LCV_NMT_%=\n" \
  "buffer_load_dword v55, off, s[44:47], s68 sc1\n" \
  ".LCV_NMT_%=:\n" \
  "v_add_f32 v12, v12, v14\n" \
  "v_add_f32 v13, v13, v15\n" \
  "global_atomic_add_x2 v2, v[12:13], s[26:27]\n" \
  "v_smfmac_f32_16x16x64_bf16 v[20:23], v[4:7], v[56:63], v0\n" \
  "ds_read_b128 v[56:59], v3 offset:14336\n" \
  "ds_read_b128 v[60:63], v3 offset:15360\n" \
  "v_add_f32 v16, v16, v18\n" \
  "v_add_f32 v17, v17, v19\n" \
  "global_atomic_add_x2 v2, v[16:17], s[28:29]\n" \
  "v_mov_b64 v[40:41], s[84:85]\n" \
  "v_mov_b64 v[42:43], s[84:85]\n" \
  "s_waitcnt lgkmcnt(4)\n" \
  "v_smfmac_f32_16x16x64_bf16 v[20:23], v[8:11], v[24:31], v0\n" \
  "s_waitcnt lgkmcnt(2)\n" \
  "v_smfmac_f32_16x16x64_bf16 v[40:43], v[4:7], v[32:39], v0\n" \
  "s_waitcnt lgkmcnt(0)\n" \
  "v_smfmac_f32_16x16x64_bf16 v[40:43], v[8:11], v[56:63], v0\n" \
  "s_nop 3\n" \
  "v_add_f32 v20, v20, v22\n" \
  "v_add_f32 v21, v21, v23\n" \
  "global_atomic_add_x2 v2, v[20:21], s[30:31]\n" \
  "s_nop 0\n" \
  "v_add_f32 v40, v40, v42\n" \
  "v_add_f32 v41, v41, v43\n" \
  "global_atomic_add_x2 v2, v[40:41], s[32:33]\n" \
  "v_mbcnt_lo_u32_b32 v48, -1, 0\n" \
  "v_mbcnt_hi_u32_b32 v48, -1, v48\n" \
  "s_add_u32 s20, s20, 32\n" \
  "s_cmp_eq_u32 s90, 1\n" \
  "s_cbranch_scc1 .LCV_HAVE_%=\n" \
  "s_mov_b32 s87, 0x40000\n" \
  "v_lshlrev_b32 v10, 4, v48\n" \
  "s_waitcnt vmcnt(4)\n" \
  "v_cmp_eq_u32 vcc, 0, v55\n" \
  "s_cmp_eq_u64 vcc, 0\n" \
  "s_cbranch_scc1 .LCV_GOT_%=\n" \
  ".LCV_LATE_%=:\n" \
  "buffer_load_dword v58, off, s[44:47], s68 sc1\n" \
  "s_sub_u32 s87, s87, 1\n" \
  "s_cbranch_scc1 .LCV_END_%=\n" \
  "s_waitcnt vmcnt(0)\n" \
  "v_cmp_eq_u32 vcc, 0, v58\n" \
  "s_cmp_eq_u64 vcc, 0\n" \
  "s_cbranch_scc1 .LCV_GOT_%=\n" \
  "s_sleep 12\n" \
  "s_branch .LCV_LATE_%=\n" \
  ".Lpad0x82:\n" \
  "s_setpc_b64 s[98:99]\n" \
  ".LCV_GOT_%=:\n" \
  "s_mov_b32 m0, s99\n" \
  "s_add_u32 s88, s99, 8192\n" \
  "global_load_lds_dwordx4 v10, s[62:63] sc1\n" \
  "global_load_lds_dwordx4 v10, s[62:63] offset:1024 sc1\n" \
  "s_mov_b32 m0, s88\n" \
  "s_nop 0\n" \
  "global_load_lds_dwordx4 v10, s[64:65] sc1\n" \
  "global_load_lds_dwordx4 v10, s[64:65] offset:1024 sc1\n" \
  ".LCV_HAVE_%=:\n" \
  "v_lshrrev_b32 v49, 3, v48\n" \
  "v_and_b32 v50, 7, v48\n" \
  "v_lshlrev_b32 v50, 4, v50\n" \
  "v_lshl_or_b32 v44, v49, 8, v50\n" \
  "s_mov_b32 s21, 0x8000\n" \
  "v_mov_b32 v52, s22\n" \
  "v_mov_b32 v54, s20\n" \
  "s_branch .LCV_TKF_%=\n" \
  ".LCV_TKE_%=:\n" \
  "s_sleep 1\n" \
  ".LCV_TKF_%=:\n" \
  "ds_read_b64 v[54:55], v52\n" \
  "s_sub_u32 s21, s21, 1\n" \
  "s_cbranch_scc1 .LCV_TKO_%=\n" \
  "s_waitcnt lgkmcnt(0)\n" \
  "v_readfirstlane_b32 s38, v54\n" \
  "v_readfirstlane_b32 s34, v55\n" \
  "s_cmp_eq_u32 s38, s20\n" \
  "s_cbranch_scc0 .LCV_TKE_%=\n" \
  ".LCV_TKO_%=:\n" \
  "s_branch .LCV_WAIU_%=\n" \
  ".Lpad0x7f:\n" \
  "s_setpc_b64 s[98:99]\n" \
  ".LCV_WAIS_%=:\n" \
  "s_cmp_lt_i32 s39, -1000\n" \
  "s_cbranch_scc1 .LCV_WAIE_%=\n" \
  "s_sleep 1\n" \
  ".LCV_WAIU_%=:\n" \
  "s_memrealtime s[38:39]\n" \
  "s_waitcnt lgkmcnt(0)\n" \
  "s_sub_i32 s39, s38, s34\n" \
  "s_cmp_lt_i32 s39, -7\n" \
  "s_cbranch_scc1 .LCV_WAIS_%=\n" \
  "s_cmp_lt_i32 s39, 0\n" \
  "s_cbranch_scc1 .LCV_WAIU_%=\n" \
  ".LCV_WAIE_%=:\n" \
  "buffer_load_dwordx4 v[24:27], v44, s[40:43], s60 offen sc1\n" \
  "buffer_load_dwordx4 v[28:31], v44, s[40:43], s60 offen offset:2048 sc1\n" \
  "v_mbcnt_lo_u32_b32 v48, -1, 0\n" \
  "v_mbcnt_hi_u32_b32 v48, -1, v48\n" \
  "s_sub_u32 s88, s73, 4\n" \
  "v_and_b32 v51, 15, v48\n" \
  "v_lshl_add_u32 v4, v51, 1, s97\n" \
  "v_lshrrev_b32 v49, 4, v48\n" \
  "v_mad_u32_u24 v4, v49, s94, v4\n" \
  "v_lshrrev_b32 v49, 2, v51\n" \
  "v_and_b32 v50, 5, v51\n" \
  "v_and_or_b32 v49, v49, 2, v50\n" \
  "s_lshl_b32 s89, s88, 6\n" \
  "s_add_u32 s89, s89, s74\n" \
  "s_add_u32 s89, s89, 161152\n" \
  "v_and_b32 v50, 48, v48\n" \
  "v_add_u32 v6, s89, v50\n" \
  "v_mad_u32_u24 v6, v49, s95, v6\n" \
  "v_lshlrev_b32 v7, 3, v50\n" \
  "v_lshl_add_u32 v7, v49, 4, v7\n" \
  "v_and_b32 v49, 2, v51\n" \
  "v_lshl_add_u32 v7, v49, 29, v7\n" \
  "v_lshlrev_b32 v10, 4, v48\n" \
  "v_mov_b64 v[12:13], 0\n" \
  "v_mov_b64 v[14:15], 0\n" \
  "v_mov_b64 v[16:17], 0\n" \
  "v_mov_b64 v[18:19], 0\n" \
  "v_mov_b64 v[20:21], 0\n" \
  "v_mov_b64 v[22:23], 0\n" \
  "v_and_b32 v49, 3, v48\n" \
  "v_lshl_add_u32 v40, v49, 2, s98\n" \
  "v_lshrrev_b32 v49, 2, v48\n" \
  "v_bfe_u32 v48, v48, 4, 1\n" \
  "v_xor_b32 v49, v49, v48\n" \
  "v_lshl_add_u32 v40, v49, 5, v40\n" \
  "s_add_u32 s62, s62, 0x80000\n" \
  "s_addc_u32 s63, s63, 0\n" \
  "s_add_u32 s64, s64, 0x40000\n" \
  "s_addc_u32 s65, s65, 0\n" \
  "s_add_u32 s68, s68, 0x1000\n" \
  "s_add_u32 s69, s69, 0x10000\n" \
  "s_add_i32 s80, s86, 0x35000000\n" \
  "s_sub_u32 s91, 0x4a350000, s86\n" \
  "s_min_u32 s91, s91, 0x4ca00000\n" \
  "s_mov_b32 s89, s60\n" \
  "s_xor_b32 s60, s60, 0x80\n" \
  "s_bitcmp0_b32 s60, 7\n" \
  "s_cselect_b32 s88, 0x8000, 0\n" \
  "s_add_u32 s60, s60, s88\n" \
  "s_and_b32 s60, s60, 0xffff\n" \
  "s_add_u32 s58, s56, s60\n" \
  "s_addc_u32 s59, s57, 0\n" \
  "s_xor_b32 s38, s35, 0\n" \
  "s_add_u32 s26, s58, s38\n" \
  "s_addc_u32 s27, s59, 0\n" \
  "s_xor_b32 s38, s35, 1024\n" \
  "s_add_u32 s28, s58, s38\n" \
  "s_addc_u32 s29, s59, 0\n" \
  "s_xor_b32 s38, s35, 2048\n" \
  "s_add_u32 s30, s58, s38\n" \
  "s_addc_u32 s31, s59, 0\n" \
  "s_xor_b32 s38, s35, 3072\n" \
  "s_add_u32 s32, s58, s38\n" \
  "s_addc_u32 s33, s59, 0\n" \
  "s_sub_u32 s71, s71, 1\n" \
  "s_mov_b32 s87, 0x40000\n" \
  "s_branch .LCV_CHK_%=\n" \
  ".LCV_POLL_%=:\n" \
  "buffer_load_dwordx4 v[24:27], v44, s[40:43], s89 offen sc1\n" \
  "buffer_load_dwordx4 v[28:31], v44, s[40:43], s89 offen offset:2048 sc1\n" \
  ".LCV_CHK_%=:\n" \
  "s_sub_u32 s87, s87, 1\n" \
  "s_cbranch_scc1 .LCV_END_%=\n" \
  "s_waitcnt vmcnt(1)\n" \
  "v_cvt_f32_i32 v32, v24\n" \
  "v_cvt_f32_i32 v33, v25\n" \
  "v_cvt_f32_i32 v34, v26\n" \
  "v_cvt_f32_i32 v35, v27\n" \
  "v_pk_mul_f32 v[24:25], v[32:33], s[80:81] op_sel_hi:[1,0]\n" \
  "v_pk_mul_f32 v[26:27], v[34:35], s[80:81] op_sel_hi:[1,0]\n" \
  "v_max3_f32 v45, |v32|, |v33|, |v34|\n" \
  "v_cvt_pk_bf16_f32 v24, v24, v26\n" \
  "v_cvt_pk_bf16_f32 v25, v25, v27\n" \
  "ds_write2_b32 v40, v24, v25 offset0:0 offset1:4\n" \
  "v_max3_f32 v45, |v45|, |v35|, |v35|\n" \
  "s_waitcnt vmcnt(0)\n" \
  "v_cvt_f32_i32 v32, v28\n" \
  "v_cvt_f32_i32 v33, v29\n" \
  "v_cvt_f32_i32 v34, v30\n" \
  "v_cvt_f32_i32 v35, v31\n" \
  "v_pk_mul_f32 v[28:29], v[32:33], s[80:81] op_sel_hi:[1,0]\n" \
  "v_pk_mul_f32 v[30:31], v[34:35], s[80:81] op_sel_hi:[1,0]\n" \
  "v_max3_f32 v45, |v45|, |v32|, |v33|\n" \
  "v_cvt_pk_bf16_f32 v28, v28, v30\n" \
  "v_cvt_pk_bf16_f32 v29, v29, v31\n" \
  "ds_write2_b32 v40, v28, v29 offset0:128 offset1:132\n" \
  "v_max3_f32 v45, |v45|, |v34|, |v35|\n" \
  "v_cmp_lt_f32 vcc, s91, v45\n" \
  "s_cbranch_vccnz .LCV_BIG_%=\n" \
  ".LCV_SML_%=:\n" \
  "s_branch .LCV_TOP_%=\n" \
  ".Lpad0x83:\n" \
  "s_setpc_b64 s[98:99]\n" \
  ".LCV_BIG_%=:\n" \
  "v_cmp_lt_f32 vcc, 0x4ca00000, v45\n" \
  "s_cbranch_vccnz .LCV_POLL_%=\n" \
  "v_mul_f32 v45, s80, v45\n" \
  "v_mul_f32 v45, v45, v45\n" \
  "s_add_u32 s88, s74, 157760\n" \
  "v_mov_b32 v44, s88\n" \
  "s_nop 0\n" \
  "v_cmpx_lt_f32 vcc, 2.0, v45\n" \
  "ds_max_u32 v44, v45\n" \
  "s_mov_b64 exec, -1\n" \
  "s_branch .LCV_SML_%=\n" \
  ".LCV_FIRST_%=:\n" \
  "s_mov_b32 s87, 0x100000\n" \
  "s_cmp_lg_u32 s73, 7\n" \
  "s_cbranch_scc1 .LCV_FON_%=\n" \
  "global_load_dword v58, v56, s[52:53] sc1\n" \
  "s_sleep %[sf]\n" \
  "global_load_dword v59, v56, s[52:53] sc1\n" \
  ".LCV_FASK_%=:\n" \
  "s_sub_u32 s87, s87, 1\n" \
  "s_cbranch_scc1 .LCV_END_%=\n" \
  "s_waitcnt vmcnt(2)\n" \
  "v_mov_b32 v30, v57\n" \
  "v_cmp_eq_u32 vcc, 0, v57\n" \
  "s_cbranch_vccz .LCV_FSEE_%=\n" \
  "s_cmp_eq_u32 s24, 0\n" \
  "s_cbranch_scc1 1\n" \
  "s_sleep %[sg]\n" \
  "global_load_dword v57, v56, s[52:53] sc1\n" \
  "s_waitcnt vmcnt(2)\n" \
  "v_mov_b32 v30, v58\n" \
  "v_cmp_eq_u32 vcc, 0, v58\n" \
  "s_cbranch_vccz .LCV_FSEE_%=\n" \
  "s_cmp_eq_u32 s24, 0\n" \
  "s_cbranch_scc1 1\n" \
  "s_sleep %[sg]\n" \
  "global_load_dword v58, v56, s[52:53] sc1\n" \
  "s_waitcnt vmcnt(2)\n" \
  "v_mov_b32 v30, v59\n" \
  "v_cmp_eq_u32 vcc, 0, v59\n" \
  "s_cbranch_vccz .LCV_FSEE_%=\n" \
  "s_cmp_eq_u32 s24, 0\n" \
  "s_cbranch_scc1 1\n" \
  "s_sleep %[sg]\n" \
  "global_load_dword v59, v56, s[52:53] sc1\n" \
  "s_branch .LCV_FASK_%=\n" \
  ".LCV_FSEE_%=:\n" \
  "buffer_load_dword v9, off, s[16:19], s24 offset:8 sc1\n" \
  "s_sub_u32 s38, s62, 6144\n" \
  "s_subb_u32 s39, s63, 0\n" \
  "s_add_u32 m0, s99, -6144\n" \
  "s_nop 0\n" \
  "global_load_lds_dwordx4 v10, s[38:39] offset:0 sc1\n" \
  "global_load_lds_dwordx4 v10, s[38:39] offset:1024 sc1\n" \
  "global_load_lds_dwordx4 v10, s[38:39] offset:2048 sc1\n" \
  "global_load_lds_dwordx4 v10, s[38:39] offset:3072 sc1\n" \
  "s_sub_u32 s38, s62, 2048\n" \
  "s_subb_u32 s39, s63, 0\n" \
  "s_add_u32 m0, s99, -2048\n" \
  "s_nop 0\n" \
  "global_load_lds_dwordx4 v10, s[38:39] offset:0 sc1\n" \
  "global_load_lds_dwordx4 v10, s[38:39] offset:1024 sc1\n" \
  "global_load_lds_dwordx4 v10, s[38:39] offset:2048 sc1\n" \
  "global_load_lds_dwordx4 v10, s[38:39] offset:3072 sc1\n" \
  "s_sub_u32 s38, s64, 6144\n" \
  "s_subb_u32 s39, s65, 0\n" \
  "s_add_u32 m0, s99, 2048\n" \
  "s_nop 0\n" \
  "global_load_lds_dwordx4 v10, s[38:39] offset:0 sc1\n" \
  "global_load_lds_dwordx4 v10, s[38:39] offset:1024 sc1\n" \
  "global_load_lds_dwordx4 v10, s[38:39] offset:2048 sc1\n" \
  "global_load_lds_dwordx4 v10, s[38:39] offset:3072 sc1\n" \
  "s_sub_u32 s38, s64, 2048\n" \
  "s_subb_u32 s39, s65, 0\n" \
  "s_add_u32 m0, s99, 6144\n" \
  "s_nop 0\n" \
  "global_load_lds_dwordx4 v10, s[38:39] offset:0 sc1\n" \
  "global_load_lds_dwordx4 v10, s[38:39] offset:1024 sc1\n" \
  "global_load_lds_dwordx4 v10, s[38:39] offset:2048 sc1\n" \
  "global_load_lds_dwordx4 v10, s[38:39] offset:3072 sc1\n" \
  "s_memrealtime vcc\n" \
  "s_nop 1\n" \
  "v_max_u32_dpp v30, v30, v30 row_ror:8 row_mask:0xf bank_mask:0xf\n" \
  "s_nop 1\n" \
  "v_max_u32_dpp v30, v30, v30 row_ror:4 row_mask:0xf bank_mask:0xf\n" \
  "s_nop 1\n" \
  "v_max_u32_dpp v30, v30, v30 row_ror:2 row_mask:0xf bank_mask:0xf\n" \
  "s_nop 1\n" \
  "v_max_u32_dpp v30, v30, v30 row_ror:1 row_mask:0xf bank_mask:0xf\n" \
  "v_mov_b32 v31, v30\n" \
  "s_nop 1\n" \
  "v_permlane16_swap_b32 v30, v31\n" \
  "v_max_u32 v30, v30, v31\n" \
  "v_mov_b32 v31, v30\n" \
  "s_nop 1\n" \
  "v_permlane32_swap_b32 v30, v31\n" \
  "v_max_u32 v30, v30, v31\n" \
  "s_lshr_b32 s88, s68, 10\n" \
  "s_lshl_b32 s88, s88, 8\n" \
  "s_add_u32 s88, s88, s24\n" \
  "s_cmp_eq_u32 s24, 0\n" \
  "v_readfirstlane_b32 s39, v30\n" \
  "v_mov_b32 v31, 1\n" \
  "s_cbranch_scc1 .LCV_FNOW_%=\n" \
  "s_waitcnt vmcnt(16)\n" \
  "v_readfirstlane_b32 s89, v9\n" \
  "s_add_u32 s89, s89, s25\n" \
  "s_sub_u32 s89, s89, %[g1]\n" \
  "s_sub_u32 s38, s89, s39\n" \
  "s_cmp_lt_u32 s38, 800\n" \
  "s_cselect_b32 s39, s89, s39\n" \
  ".LCV_FNOW_%=:\n" \
  "s_add_u32 s89, s39, %[d2]\n" \
  "s_waitcnt lgkmcnt(0)\n" \
  "s_add_u32 s38, vcc_lo, %[d4]\n" \
  "s_max_u32 s89, s89, s38\n" \
  "s_add_u32 s38, s89, %[d1]\n" \
  "v_mov_b32 v30, s38\n" \
  "s_mov_b64 exec, 1\n" \
  "buffer_atomic_umax v30, off, s[16:19], s88\n" \
  "buffer_atomic_add v31, off, s[16:19], s88 offset:64\n" \
  "s_mov_b64 exec, -1\n" \
  ".LCV_FON_%=:\n" \
  "s_add_u32 s62, s62, 0x80000\n" \
  "s_addc_u32 s63, s63, 0\n" \
  "s_add_u32 s64, s64, 0x40000\n" \
  "s_addc_u32 s65, s65, 0\n" \
  "s_add_u32 s68, s68, 0x1000\n" \
  "s_cmp_eq_u32 s73, 7\n" \
  "s_cbranch_scc1 .LCV_FCHK_%=\n" \
  "s_branch .LCV_FBND_%=\n" \
  ".LCV_FMORE_%=:\n" \
  "s_sleep %[sf]\n" \
  "global_load_dword v61, v60, s[52:53] offset:0 sc1\n" \
  "global_load_dword v62, v60, s[52:53] offset:128 sc1\n" \
  "global_load_dword v63, v60, s[52:53] offset:256 sc1\n" \
  "s_sub_u32 s87, s87, 1\n" \
  "s_cbranch_scc1 .LCV_END_%=\n" \
  ".LCV_FBND_%=:\n" \
  "s_waitcnt vmcnt(0)\n" \
  ".LCV_FCHK_%=:\n" \
  "v_min3_u32 v32, v61, v62, v63\n" \
  "v_cmp_eq_u32 vcc, 0, v32\n" \
  "s_cbranch_vccnz .LCV_FMORE_%=\n" \
  "v_add_f32 v24, v62, v63\n" \
  "v_mul_f32 v24, v24, v61\n" \
  "s_nop 1\n" \
  "v_max_f32_dpp v24, v24, v24 row_ror:8 row_mask:0xf bank_mask:0xf\n" \
  "s_nop 1\n" \
  "v_max_f32_dpp v24, v24, v24 row_ror:4 row_mask:0xf bank_mask:0xf\n" \
  "s_nop 1\n" \
  "v_max_f32_dpp v24, v24, v24 row_ror:2 row_mask:0xf bank_mask:0xf\n" \
  "s_nop 1\n" \
  "v_max_f32_dpp v24, v24, v24 row_ror:1 row_mask:0xf bank_mask:0xf\n" \
  "v_mov_b32 v25, v24\n" \
  "s_nop 1\n" \
  "v_permlane16_swap_b32 v24, v25\n" \
  "v_max_f32 v24, v24, v25\n" \
  "v_mul_f32 v24, 0x3f88f5c3, v24\n" \
  "s_nop 0\n" \
  "v_mul_f32 v25, 0x44870000, v24\n" \
  "v_readfirstlane_b32 s83, v24\n" \
  "v_readfirstlane_b32 s82, v25\n" \
  "s_lshr_b32 s82, s82, 23\n" \
  "s_addk_i32 s82, 0xff83\n" \
  "s_ashr_i32 s82, s82, 1\n" \
  "s_max_i32 s82, s82, -20\n" \
  "s_min_i32 s82, s82, 100\n" \
  "s_lshr_b32 s83, s83, 23\n" \
  "s_sub_i32 s83, s83, 242\n" \
  "s_cmp_lg_u32 s73, 7\n" \
  "s_cbranch_scc1 .LCV_BACK_%=\n" \
  "v_mov_b32 v25, s89\n" \
  "s_mov_b64 exec, 1\n" \
  "ds_write_b64 v1, v[24:25] offset:26624\n" \
  "s_mov_b64 exec, -1\n" \
  ".LCV_FWAIT_%=:\n" \
  "s_memrealtime s[38:39]\n" \
  "s_waitcnt lgkmcnt(0)\n" \
  "s_sub_u32 s38, s89, s38\n" \
  "s_cmp_lt_u32 s38, 800\n" \
  "s_cbranch_scc1 .LCV_FWAIT_%=\n" \
  "s_waitcnt vmcnt(2)\n" \
  "s_branch .LCV_BACK_%=\n" \
  ".LCV_LAST_%=:\n" \
  "s_waitcnt vmcnt(0)\n" \
  ".LCV_END_%=:\n" \
  "s_endpgm\n" \
  ".p2alignl 12, 0xbf800000\n" \
  ".Lpad0x64:\n" \
  "s_setpc_b64 s[98:99]\n"
#define QKV_ASM_0 \
  "s_cmp_lg_u32 s69, 0\n" \
  "s_mov_b32 s69, 0\n" \
  "s_cbranch_scc1 .LQA_GOT_%=\n" \
  "s_branch .LQA_POLL_%=\n" \
  "s_branch .LQA_AL_%=\n" \
  ".p2alignl 12, 0xbf800000, 2600\n" \
  ".p2align 6\n" \
  ".Lpad0x95:\n" \
  "s_setpc_b64 s[98:99]\n" \
  ".p2align 6\n" \
  ".LQA_AL_%=:\n" \
  ".LQA_TOP_%=:\n" \
  "s_barrier\n" \
  "ds_read_b128 v[36:39], v7 offset:0\n" \
  "ds_read_b128 v[40:43], v7 offset:2048\n" \
  "ds_read_b128 v[44:47], v7 offset:4096\n" \
  "s_setprio 2\n" \
  "s_waitcnt lgkmcnt(2)\n" \
  "v_mfma_f32_16x16x32_bf16 v[12:15], v[64:67], v[36:39], 0\n" \
  "ds_read_b128 v[48:51], v7 offset:6144\n" \
  "v_mfma_f32_16x16x32_bf16 v[16:19], v[128:131], v[36:39], 0\n" \
  "v_mfma_f32_16x16x32_bf16 v[20:23], v[36:39], v[192:195], 0\n" \
  "s_cmp_lg_u32 s69, 1\n" \
  "s_cbranch_scc1 .LQA_FDN_%=\n" \
  "s_waitcnt vmcnt(0)\n" \
  "v_mov_b32 v52, s80\n" \
  "v_mov_b32 v53, 0x10000\n" \
  "s_mov_b64 exec, 1\n" \
  "ds_add_rtn_u32 v54, v52, v53\n" \
  "s_memrealtime s[0:1]\n" \
  "s_mov_b64 exec, -1\n" \
  "s_add_u32 s67, s67, 4\n" \
  "s_add_u32 s64, s64, 4096\n" \
  "s_addc_u32 s65, s65, 0\n" \
  "v_mov_b32 v55, 0\n" \
  "s_waitcnt lgkmcnt(0)\n" \
  "v_readfirstlane_b32 s74, v54\n" \
  "s_and_b32 s76, s74, 0x30000\n" \
  "s_cmp_lg_u32 s76, 0x30000\n" \
  "s_cbranch_scc1 .LQA_FDN_%=\n" \
  "s_mov_b32 s79, 0x100000\n" \
  "s_branch .LQA_FDT_%=\n" \
  ".LQA_FD_%=:\n" \
  "ds_read_b32 v54, v52\n" \
  "s_sub_u32 s79, s79, 1\n" \
  "s_cbranch_scc1 .LQA_FDE_%=\n" \
  "s_waitcnt lgkmcnt(0)\n" \
  "v_readfirstlane_b32 s74, v54\n" \
  ".LQA_FDT_%=:\n" \
  "s_and_b32 s74, s74, 0xffff\n" \
  "s_cmp_lt_u32 s74, s67\n" \
  "s_cbranch_scc1 .LQA_FD_%=\n" \
  ".LQA_FDE_%=:\n" \
  "v_or_b32 v53, 1, s0\n" \
  "s_cmp_lg_u32 s67, 4\n" \
  "s_cbranch_scc1 .LQA_FDX_%=\n" \
  "v_lshlrev_b32 v54, 5, v6\n" \
  "s_mov_b64 exec, 15\n" \
  "global_store_dword v54, v53, s[84:85] sc1\n" \
  ".LQA_FDX_%=:\n" \
  "s_mov_b64 exec, 1\n" \
  "global_store_dword v55, v53, s[64:65] offset:-4096 sc1\n" \
  "s_mov_b64 exec, -1\n" \
  ".LQA_FDN_%=:\n" \
  "s_waitcnt lgkmcnt(2)\n" \
  "v_mfma_f32_16x16x32_bf16 v[12:15], v[68:71], v[40:43], v[12:15]\n" \
  "ds_read_b128 v[36:39], v7 offset:8192\n" \
  "v_mov_b64 v[24:25], 0\n" \
  "v_lshrrev_b32 v26, 1, v6\n" \
  "buffer_store_dwordx2 v[24:25], v26, s[48:51], s61 offen sc1\n" \
  "v_mfma_f32_16x16x32_bf16 v[16:19], v[132:135], v[40:43], v[16:19]\n" \
  "v_mov_b32 v11, s82\n" \
  "v_mfma_f32_16x16x32_bf16 v[20:23], v[40:43], v[196:199], v[20:23]\n" \
  "s_waitcnt lgkmcnt(2)\n" \
  "v_mfma_f32_16x16x32_bf16 v[12:15], v[72:75], v[44:47], v[12:15]\n" \
  "ds_read_b128 v[40:43], v7 offset:10240\n" \
  "v_mfma_f32_16x16x32_bf16 v[16:19], v[136:139], v[44:47], v[16:19]\n" \
  "v_mfma_f32_16x16x32_bf16 v[20:23], v[44:47], v[200:203], v[20:23]\n" \
  "s_waitcnt lgkmcnt(2)\n" \
  "v_mfma_f32_16x16x32_bf16 v[12:15], v[76:79], v[48:51], v[12:15]\n" \
  "ds_read_b128 v[44:47], v7 offset:12288\n" \
  "v_mfma_f32_16x16x32_bf16 v[16:19], v[140:143], v[48:51], v[16:19]\n" \
  "v_mfma_f32_16x16x32_bf16 v[20:23], v[48:51], v[204:207], v[20:23]\n" \
  "s_waitcnt lgkmcnt(2)\n" \
  "v_mfma_f32_16x16x32_bf16 v[12:15], v[80:83], v[36:39], v[12:15]\n" \
  "ds_read_b128 v[48:51], v7 offset:14336\n" \
  "v_mfma_f32_16x16x32_bf16 v[16:19], v[144:147], v[36:39], v[16:19]\n" \
  "v_mfma_f32_16x16x32_bf16 v[20:23], v[36:39], v[208:211], v[20:23]\n" \
  "s_waitcnt lgkmcnt(2)\n" \
  "v_mfma_f32_16x16x32_bf16 v[12:15], v[84:87], v[40:43], v[12:15]\n" \
  "ds_read_b128 v[36:39], v7 offset:16384\n" \
  "v_mfma_f32_16x16x32_bf16 v[16:19], v[148:151], v[40:43], v[16:19]\n" \
  "v_mfma_f32_16x16x32_bf16 v[20:23], v[40:43], v[212:215], v[20:23]\n" \
  "s_waitcnt lgkmcnt(2)\n" \
  "v_mfma_f32_16x16x32_bf16 v[12:15], v[88:91], v[44:47], v[12:15]\n" \
  "ds_read_b128 v[40:43], v7 offset:18432\n" \
  "v_mfma_f32_16x16x32_bf16 v[16:19], v[152:155], v[44:47], v[16:19]\n" \
  "v_mfma_f32_16x16x32_bf16 v[20:23], v[44:47], v[216:219], v[20:23]\n" \
  "s_waitcnt lgkmcnt(2)\n" \
  "v_mfma_f32_16x16x32_bf16 v[12:15], v[92:95], v[48:51], v[12:15]\n" \
  "ds_read_b128 v[44:47], v7 offset:20480\n" \
  "v_mfma_f32_16x16x32_bf16 v[16:19], v[156:159], v[48:51], v[16:19]\n" \
  "v_mfma_f32_16x16x32_bf16 v[20:23], v[48:51], v[220:223], v[20:23]\n" \
  "s_waitcnt lgkmcnt(2)\n" \
  "v_mfma_f32_16x16x32_bf16 v[12:15], v[96:99], v[36:39], v[12:15]\n" \
  "ds_read_b128 v[48:51], v7 offset:22528\n" \
  "v_mfma_f32_16x16x32_bf16 v[16:19], v[160:163], v[36:39], v[16:19]\n" \
  "v_mfma_f32_16x16x32_bf16 v[20:23], v[36:39], v[224:227], v[20:23]\n" \
  "s_waitcnt lgkmcnt(2)\n" \
  "v_mfma_f32_16x16x32_bf16 v[12:15], v[100:103], v[40:43], v[12:15]\n" \
  "ds_read_b128 v[36:39], v7 offset:24576\n" \
  "v_mfma_f32_16x16x32_bf16 v[16:19], v[164:167], v[40:43], v[16:19]\n" \
  "v_mfma_f32_16x16x32_bf16 v[20:23], v[40:43], v[228:231], v[20:23]\n" \
  "s_waitcnt lgkmcnt(2)\n" \
  "v_mfma_f32_16x16x32_bf16 v[12:15], v[104:107], v[44:47], v[12:15]\n" \
  "ds_read_b128 v[40:43], v7 offset:26624\n" \
  "v_mfma_f32_16x16x32_bf16 v[16:19], v[168:171], v[44:47], v[16:19]\n" \
  "v_mfma_f32_16x16x32_bf16 v[20:23], v[44:47], v[232:235], v[20:23]\n" \
  "s_waitcnt lgkmcnt(2)\n" \
  "v_mfma_f32_16x16x32_bf16 v[12:15], v[108:111], v[48:51], v[12:15]\n" \
  "ds_read_b128 v[44:47], v7 offset:28672\n" \
  "v_mfma_f32_16x16x32_bf16 v[16:19], v[172:175], v[48:51], v[16:19]\n" \
  "v_mfma_f32_16x16x32_bf16 v[20:23], v[48:51], v[236:239], v[20:23]\n" \
  "s_waitcnt lgkmcnt(2)\n" \
  "v_mfma_f32_16x16x32_bf16 v[12:15], v[112:115], v[36:39], v[12:15]\n" \
  "ds_read_b128 v[48:51], v7 offset:30720\n" \
  "v_mfma_f32_16x16x32_bf16 v[16:19], v[176:179], v[36:39], v[16:19]\n" \
  "v_mfma_f32_16x16x32_bf16 v[20:23], v[36:39], v[240:243], v[20:23]\n" \
  "s_waitcnt lgkmcnt(2)\n" \
  "v_mfma_f32_16x16x32_bf16 v[12:15], v[116:119], v[40:43], v[12:15]\n" \
  "ds_read_b128 v[36:39], v7 offset:256\n" \
  "v_mfma_f32_16x16x32_bf16 v[16:19], v[180:183], v[40:43], v[16:19]\n" \
  "v_mfma_f32_16x16x32_bf16 v[20:23], v[40:43], v[244:247], v[20:23]\n" \
  "s_waitcnt lgkmcnt(2)\n" \
  "v_mfma_f32_16x16x32_bf16 v[12:15], v[120:123], v[44:47], v[12:15]\n" \
  "ds_read_b128 v[40:43], v7 offset:2304\n" \
  "v_mfma_f32_16x16x32_bf16 v[16:19], v[184:187], v[44:47], v[16:19]\n" \
  "v_mfma_f32_16x16x32_bf16 v[20:23], v[44:47], v[248:251], v[20:23]\n" \
  "s_waitcnt lgkmcnt(2)\n" \
  "v_mfma_f32_16x16x32_bf16 v[12:15], v[124:127], v[48:51], v[12:15]\n" \
  "ds_read_b128 v[44:47], v7 offset:4352\n" \
  "v_mfma_f32_16x16x32_bf16 v[16:19], v[188:191], v[48:51], v[16:19]\n" \
  "v_mfma_f32_16x16x32_bf16 v[20:23], v[48:51], v[252:255], v[20:23]\n" \
  "s_waitcnt lgkmcnt(2)\n" \
  "v_mfma_f32_16x16x32_bf16 v[24:27], v[64:67], v[36:39], 0\n" \
  "ds_read_b128 v[48:51], v7 offset:6400\n" \
  "v_mfma_f32_16x16x32_bf16 v[28:31], v[128:131], v[36:39], 0\n" \
  "v_mfma_f32_16x16x32_bf16 v[32:35], v[36:39], v[192:195], 0\n" \
  "s_waitcnt lgkmcnt(2)\n" \
  "v_mfma_f32_16x16x32_bf16 v[24:27], v[68:71], v[40:43], v[24:27]\n" \
  "ds_read_b128 v[36:39], v7 offset:8448\n" \
  "v_mfma_f32_16x16x32_bf16 v[28:31], v[132:135], v[40:43], v[28:31]\n" \
  "v_mfma_f32_16x16x32_bf16 v[32:35], v[40:43], v[196:199], v[32:35]\n" \
  "s_waitcnt lgkmcnt(2)\n" \
  "v_mfma_f32_16x16x32_bf16 v[24:27], v[72:75], v[44:47], v[24:27]\n" \
  "ds_read_b128 v[40:43], v7 offset:10496\n" \
  "v_mfma_f32_16x16x32_bf16 v[28:31], v[136:139], v[44:47], v[28:31]\n" \
  "v_mfma_f32_16x16x32_bf16 v[32:35], v[44:47], v[200:203], v[32:35]\n" \
  "s_waitcnt lgkmcnt(2)\n" \
  "v_mfma_f32_16x16x32_bf16 v[24:27], v[76:79], v[48:51], v[24:27]\n" \
  "ds_read_b128 v[44:47], v7 offset:12544\n" \
  "v_mfma_f32_16x16x32_bf16 v[28:31], v[140:143], v[48:51], v[28:31]\n" \
  "v_mfma_f32_16x16x32_bf16 v[32:35], v[48:51], v[204:207], v[32:35]\n" \
  "s_waitcnt lgkmcnt(2)\n" \
  "v_mfma_f32_16x16x32_bf16 v[24:27], v[80:83], v[36:39], v[24:27]\n" \
  "ds_read_b128 v[48:51], v7 offset:14592\n" \
  "v_mfma_f32_16x16x32_bf16 v[28:31], v[144:147], v[36:39], v[28:31]\n" \
  "v_mfma_f32_16x16x32_bf16 v[32:35], v[36:39], v[208:211], v[32:35]\n" \
  "s_barrier\n" \
  "s_mov_b32 s0, 0xbe801d62\n" \
  ".set .Lpad0x96, . - 4\n" \
  "s_waitcnt lgkmcnt(2)\n" \
  "v_mfma_f32_16x16x32_bf16 v[24:27], v[84:87], v[40:43], v[24:27]\n" \
  "ds_read_b128 v[36:39], v7 offset:16640\n" \
  "ds_read_b32 v11, v11 offset:0\n" \
  "v_mfma_f32_16x16x32_bf16 v[28:31], v[148:151], v[40:43], v[28:31]\n" \
  "ds_read2_b32 v[52:53], v8 offset0:0 offset1:0\n" \
  "v_mfma_f32_16x16x32_bf16 v[32:35], v[40:43], v[212:215], v[32:35]\n" \
  "s_waitcnt lgkmcnt(4)\n" \
  "v_mfma_f32_16x16x32_bf16 v[24:27], v[88:91], v[44:47], v[24:27]\n" \
  "ds_read_b128 v[40:43], v7 offset:18688\n" \
  "v_mfma_f32_16x16x32_bf16 v[28:31], v[152:155], v[44:47], v[28:31]\n" \
  "ds_read2_b32 v[54:55], v8 offset0:16 offset1:16\n" \
  "v_mfma_f32_16x16x32_bf16 v[32:35], v[44:47], v[216:219], v[32:35]\n" \
  "s_waitcnt lgkmcnt(5)\n" \
  "v_mfma_f32_16x16x32_bf16 v[24:27], v[92:95], v[48:51], v[24:27]\n" \
  "ds_read_b128 v[44:47], v7 offset:20736\n" \
  "v_mfma_f32_16x16x32_bf16 v[28:31], v[156:159], v[48:51], v[28:31]\n" \
  "ds_read2_b32 v[56:57], v8 offset0:32 offset1:48\n" \
  "v_mfma_f32_16x16x32_bf16 v[32:35], v[48:51], v[220:223], v[32:35]\n" \
  "s_waitcnt lgkmcnt(6)\n" \
  "v_mfma_f32_16x16x32_bf16 v[24:27], v[96:99], v[36:39], v[24:27]\n" \
  "ds_read_b128 v[48:51], v7 offset:22784\n" \
  "v_mfma_f32_16x16x32_bf16 v[28:31], v[160:163], v[36:39], v[28:31]\n" \
  "ds_read_b128 v[58:61], v9 offset:128\n" \
  "v_mfma_f32_16x16x32_bf16 v[32:35], v[36:39], v[224:227], v[32:35]\n" \
  "s_waitcnt lgkmcnt(6)\n" \
  "v_mfma_f32_16x16x16_bf16 v[12:15], v[0:1], v[52:53], v[12:15]\n" \
  "v_mfma_f32_16x16x16_bf16 v[16:19], v[2:3], v[52:53], v[16:19]\n" \
  "v_mfma_f32_16x16x16_bf16 v[20:23], v[52:53], v[4:5], v[20:23]\n" \
  "s_waitcnt lgkmcnt(4)\n" \
  "v_mfma_f32_16x16x16_bf16 v[24:27], v[0:1], v[54:55], v[24:27]\n" \
  "v_mfma_f32_16x16x16_bf16 v[28:31], v[2:3], v[54:55], v[28:31]\n" \
  "v_mfma_f32_16x16x16_bf16 v[32:35], v[54:55], v[4:5], v[32:35]\n" \
  "v_mfma_f32_16x16x32_bf16 v[24:27], v[100:103], v[40:43], v[24:27]\n" \
  "ds_read_b128 v[36:39], v7 offset:24832\n" \
  "v_mfma_f32_16x16x32_bf16 v[28:31], v[164:167], v[40:43], v[28:31]\n" \
  "ds_read_b128 v[52:55], v9 offset:192\n" \
  "v_mfma_f32_16x16x32_bf16 v[32:35], v[40:43], v[228:231], v[32:35]\n" \
  "v_readfirstlane_b32 s69, v11\n" \
  "s_cmp_eq_u32 s69, 1\n" \
  "s_cselect_b32 s76, 0x40000, s62\n" \
  "s_add_u32 s74, s59, 4096\n" \
  "s_waitcnt lgkmcnt(5)\n" \
  "v_mfma_f32_16x16x32_bf16 v[24:27], v[104:107], v[44:47], v[24:27]\n" \
  "ds_read_b128 v[40:43], v7 offset:26880\n" \
  "v_mfma_f32_16x16x32_bf16 v[28:31], v[168:171], v[44:47], v[28:31]\n" \
  "v_mfma_f32_16x16x32_bf16 v[32:35], v[44:47], v[232:235], v[32:35]\n" \
  "buffer_load_dword v11, v10, s[52:55], s76 offen sc1\n" \
  "s_waitcnt lgkmcnt(4)\n" \
  "v_mfma_f32_16x16x32_bf16 v[24:27], v[108:111], v[48:51], v[24:27]\n" \
  "ds_read_b128 v[44:47], v7 offset:28928\n" \
  "v_mfma_f32_16x16x32_bf16 v[28:31], v[172:175], v[48:51], v[28:31]\n" \
  "v_mfma_f32_16x16x32_bf16 v[32:35], v[48:51], v[236:239], v[32:35]\n" \
  "s_waitcnt lgkmcnt(3)\n" \
  "v_mfma_f32_16x16x32_bf16 v[24:27], v[112:115], v[36:39], v[24:27]\n" \
  "ds_read_b128 v[48:51], v7 offset:30976\n" \
  "v_mfma_f32_16x16x32_bf16 v[28:31], v[176:179], v[36:39], v[28:31]\n" \
  "v_mfma_f32_16x16x32_bf16 v[32:35], v[36:39], v[240:243], v[32:35]\n" \
  "s_waitcnt lgkmcnt(2)\n" \
  "v_mfma_f32_16x16x32_bf16 v[24:27], v[116:119], v[40:43], v[24:27]\n" \
  "v_mfma_f32_16x16x32_bf16 v[28:31], v[180:183], v[40:43], v[28:31]\n" \
  "v_mfma_f32_16x16x32_bf16 v[32:35], v[40:43], v[244:247], v[32:35]\n" \
  "s_waitcnt lgkmcnt(1)\n" \
  "v_mfma_f32_16x16x32_bf16 v[24:27], v[120:123], v[44:47], v[24:27]\n" \
  "v_mfma_f32_16x16x32_bf16 v[28:31], v[184:187], v[44:47], v[28:31]\n" \
  "v_mfma_f32_16x16x32_bf16 v[32:35], v[44:47], v[248:251], v[32:35]\n" \
  "s_waitcnt lgkmcnt(0)\n" \
  "v_mfma_f32_16x16x32_bf16 v[24:27], v[124:127], v[48:51], v[24:27]\n" \
  "v_mfma_f32_16x16x32_bf16 v[28:31], v[188:191], v[48:51], v[28:31]\n" \
  "v_mfma_f32_16x16x32_bf16 v[32:35], v[48:51], v[252:255], v[32:35]\n" \
  "s_setprio 2\n" \
  "v_mul_f32 v12, v12, v56\n" \
  "v_mul_f32 v13, v13, v56\n" \
  "v_mul_f32 v14, v14, v56\n" \
  "v_cvt_pk_bf16_f32 v12, v12, v13\n" \
  "v_mul_f32 v15, v15, v56\n" \
  "v_mul_f32 v16, v16, v56\n" \
  "v_cvt_pk_bf16_f32 v13, v14, v15\n" \
  "v_mul_f32 v17, v17, v56\n" \
  "v_mul_f32 v18, v18, v56\n" \
  "v_cvt_pk_bf16_f32 v14, v16, v17\n" \
  "v_mul_f32 v19, v19, v56\n" \
  "v_cvt_pk_bf16_f32 v15, v18, v19\n" \
  "buffer_store_dwordx4 v[12:15], v6, s[40:43], s59 offen offset:0 sc1\n" \
  "v_mul_f32 v24, v24, v57\n" \
  "v_mul_f32 v25, v25, v57\n" \
  "v_mul_f32 v26, v26, v57\n" \
  "v_cvt_pk_bf16_f32 v24, v24, v25\n" \
  "v_mul_f32 v27, v27, v57\n" \
  "v_mul_f32 v28, v28, v57\n" \
  "v_cvt_pk_bf16_f32 v25, v26, v27\n" \
  "v_mul_f32 v29, v29, v57\n" \
  "v_mul_f32 v30, v30, v57\n" \
  "v_cvt_pk_bf16_f32 v26, v28, v29\n" \
  "v_mul_f32 v31, v31, v57\n" \
  "v_cvt_pk_bf16_f32 v27, v30, v31\n" \
  "buffer_store_dwordx4 v[24:27], v6, s[40:43], s74 offen offset:0 sc1\n" \
  "v_mul_f32 v20, v20, v58\n" \
  "v_mul_f32 v21, v21, v59\n" \
  "v_mul_f32 v22, v22, v60\n" \
  "v_mul_f32 v23, v23, v61\n" \
  "v_cvt_pk_bf16_f32 v20, v20, v21\n" \
  "v_cvt_pk_bf16_f32 v21, v22, v23\n" \
  "v_mul_f32 v32, v32, v52\n" \
  "v_mul_f32 v33, v33, v53\n" \
  "v_mul_f32 v34, v34, v54\n" \
  "v_mul_f32 v35, v35, v55\n" \
  "v_cvt_pk_bf16_f32 v22, v32, v33\n" \
  "v_cvt_pk_bf16_f32 v23, v34, v35\n" \
  "buffer_store_dwordx4 v[20:23], v6, s[44:47], s60 offen offset:0 sc1\n" \
  "s_setprio 0\n" \
  "s_add_u32 s59, s59, 0x80000\n" \
  "s_add_u32 s60, s60, 0x40000\n" \
  "s_add_u32 s61, s61, 0x10000\n" \
  "v_xor_b32 v7, 0x20000, v7\n" \
  "s_cmp_eq_u32 s69, 1\n" \
  "s_waitcnt vmcnt(3)\n" \
  "s_cbranch_scc1 .LQA_GO_%=\n" \
  "v_cmp_eq_u32 vcc, 0, v11\n" \
  "s_cmp_eq_u64 vcc, 0\n" \
  "s_cbranch_scc0 .LQA_NOX_%=\n" \
  "s_mov_b32 m0, s58\n" \
  "s_add_u32 s74, s56, 4096\n" \
  "s_addc_u32 s75, s57, 0\n" \
  "s_add_u32 s76, s58, 4096\n" \
  "global_load_lds_dwordx4 v6, s[56:57] offset:0 sc1\n" \
  "global_load_lds_dwordx4 v6, s[56:57] offset:1024 sc1\n" \
  "global_load_lds_dwordx4 v6, s[56:57] offset:2048 sc1\n" \
  "global_load_lds_dwordx4 v6, s[56:57] offset:3072 sc1\n" \
  "s_mov_b32 m0, s76\n" \
  "s_nop 0\n" \
  "global_load_lds_dwordx4 v6, s[74:75] offset:0 sc1\n" \
  "global_load_lds_dwordx4 v6, s[74:75] offset:1024 sc1\n" \
  "global_load_lds_dwordx4 v6, s[74:75] offset:2048 sc1\n" \
  "global_load_lds_dwordx4 v6, s[74:75] offset:3072 sc1\n" \
  "s_mov_b32 s69, 2\n" \
  "s_waitcnt vmcnt(8)\n" \
  "s_branch .LQA_CNT_%=\n" \
  ".Lpad0x97:\n" \
  "s_setpc_b64 s[98:99]\n" \
  ".LQA_NOX_%=:\n" \
  "s_waitcnt vmcnt(0)\n" \
  ".LQA_CNT_%=:\n" \
  "v_mov_b32 v52, s80\n" \
  "v_mov_b32 v53, 0x10000\n" \
  "s_mov_b64 exec, 1\n" \
  "ds_add_rtn_u32 v54, v52, v53\n" \
  "s_memrealtime s[0:1]\n" \
  "s_mov_b64 exec, -1\n" \
  "s_add_u32 s67, s67, 4\n" \
  "s_add_u32 s64, s64, 4096\n" \
  "s_addc_u32 s65, s65, 0\n" \
  "v_mov_b32 v55, 0\n" \
  "s_waitcnt lgkmcnt(0)\n" \
  "v_readfirstlane_b32 s74, v54\n" \
  "s_and_b32 s76, s74, 0x30000\n" \
  "s_cmp_lg_u32 s76, 0x30000\n" \
  "s_cbranch_scc1 .LQA_FWN_%=\n" \
  "s_mov_b32 s79, 0x100000\n" \
  "s_branch .LQA_FWT_%=\n" \
  ".LQA_FW_%=:\n" \
  "ds_read_b32 v54, v52\n" \
  "s_sub_u32 s79, s79, 1\n" \
  "s_cbranch_scc1 .LQA_FWE_%=\n" \
  "s_waitcnt lgkmcnt(0)\n" \
  "v_readfirstlane_b32 s74, v54\n" \
  ".LQA_FWT_%=:\n" \
  "s_and_b32 s74, s74, 0xffff\n" \
  "s_cmp_lt_u32 s74, s67\n" \
  "s_cbranch_scc1 .LQA_FW_%=\n" \
  ".LQA_FWE_%=:\n" \
  "v_or_b32 v53, 1, s0\n" \
  "s_cmp_lg_u32 s67, 4\n" \
  "s_cbranch_scc1 .LQA_FWX_%=\n" \
  "v_lshlrev_b32 v54, 5, v6\n" \
  "s_mov_b64 exec, 15\n" \
  "global_store_dword v54, v53, s[84:85] sc1\n" \
  ".LQA_FWX_%=:\n" \
  "s_mov_b64 exec, 1\n" \
  "global_store_dword v55, v53, s[64:65] offset:-4096 sc1\n" \
  "s_mov_b64 exec, -1\n" \
  "s_waitcnt vmcnt(1)\n" \
  "s_branch .LQA_GO_%=\n" \
  ".LQA_FWN_%=:\n" \
  "s_waitcnt vmcnt(0)\n" \
  ".LQA_GO_%=:\n" \
  "s_sub_u32 s66, s66, 1\n" \
  "s_cbranch_scc1 .LQA_LAST_%=\n" \
  "s_cmp_lg_u32 s69, 0\n" \
  "s_cbranch_scc1 .LQA_NEXT_%=\n" \
  ".LQA_GATE_%=:\n" \
  "s_memrealtime s[74:75]\n" \
  "s_waitcnt lgkmcnt(0)\n" \
  "s_sub_i32 s74, s68, s74\n" \
  "s_cmp_gt_u32 s74, %[gq]\n" \
  "s_cbranch_scc1 .LQA_POLL_%=\n" \
  "s_sleep 3\n" \
  "s_branch .LQA_GATE_%=\n" \
  ".LQA_POLL_%=:\n" \
  "s_mov_b32 s79, 0x40000\n" \
  ".LQA_AGAIN_%=:\n" \
  "buffer_load_dword v11, v10, s[52:55], s62 offen sc1\n" \
  "s_sub_u32 s79, s79, 1\n" \
  "s_cbranch_scc1 .LQA_SEEN_%=\n" \
  "s_waitcnt vmcnt(0)\n" \
  "v_cmp_eq_u32 vcc, 0, v11\n" \
  "s_cmp_eq_u64 vcc, 0\n" \
  "s_cbranch_scc1 .LQA_SEEN_%=\n" \
  "s_sleep %[sq]\n" \
  "s_cmp_ge_u32 s66, s63\n" \
  "s_cbranch_scc1 .LQA_AGAIN_%=\n" \
  "s_sleep %[s1]\n" \
  "s_branch .LQA_AGAIN_%=\n" \
  ".LQA_SEEN_%=:\n" \
  "s_mov_b32 m0, s58\n" \
  "s_add_u32 s74, s56, 4096\n" \
  "s_addc_u32 s75, s57, 0\n" \
  "s_add_u32 s76, s58, 4096\n" \
  "global_load_lds_dwordx4 v6, s[56:57] offset:0 sc1\n" \
  "global_load_lds_dwordx4 v6, s[56:57] offset:1024 sc1\n" \
  "global_load_lds_dwordx4 v6, s[56:57] offset:2048 sc1\n" \
  "global_load_lds_dwordx4 v6, s[56:57] offset:3072 sc1\n" \
  "s_mov_b32 m0, s76\n" \
  "s_nop 0\n" \
  "global_load_lds_dwordx4 v6, s[74:75] offset:0 sc1\n" \
  "global_load_lds_dwordx4 v6, s[74:75] offset:1024 sc1\n" \
  "global_load_lds_dwordx4 v6, s[74:75] offset:2048 sc1\n" \
  "global_load_lds_dwordx4 v6, s[74:75] offset:3072 sc1\n" \
  ".LQA_GOT_%=:\n" \
  "s_memrealtime s[76:77]\n" \
  "s_waitcnt lgkmcnt(0)\n" \
  "s_add_u32 s68, s76, %[gq]\n" \
  "s_waitcnt vmcnt(0)\n" \
  ".LQA_NEXT_%=:\n" \
  "s_add_u32 s56, s56, 0x8000\n" \
  "s_addc_u32 s57, s57, 0\n" \
  "s_xor_b32 s58, s58, 0x20000\n" \
  "s_add_u32 s62, s62, 4096\n" \
  "s_branch .LQA_TOP_%=\n" \
  ".LQA_LAST_%=:\n" \
  "s_barrier\n" \
  "s_endpgm\n" \
  ".p2alignl 12, 0xbf800000\n" \
  ".Lpad0xba:\n" \
  "s_setpc_b64 s[98:99]\n"
#define QKV_ASM_1 \
  "s_barrier\n" \
  "ds_read_b128 v[36:39], v10 offset:0\n" \
  "ds_read_b128 v[40:43], v7 offset:0\n" \
  "ds_read_b128 v[44:47], v10 offset:2048\n" \
  "ds_read_b128 v[48:51], v7 offset:2048\n" \
  "s_branch .LQB_AL_%=\n" \
  ".p2alignl 12, 0xbf800000, 3200\n" \
  ".p2align 6\n" \
  ".Lpad0xa9:\n" \
  "s_setpc_b64 s[98:99]\n" \
  ".p2align 6\n" \
  ".LQB_AL_%=:\n" \
  ".LQB_TOP_%=:\n" \
  "s_setprio 3\n" \
  "s_waitcnt lgkmcnt(3)\n" \
  "v_mfma_f32_16x16x32_bf16 v[56:59], v[36:39], v[36:39], 0\n" \
  "ds_read_b128 v[52:55], v10 offset:4096\n" \
  "s_add_u32 s56, s56, 0x8000\n" \
  "s_addc_u32 s57, s57, 0\n" \
  "s_xor_b32 s58, s58, 0x20000\n" \
  "s_add_u32 s62, s62, 4096\n" \
  "s_waitcnt lgkmcnt(3)\n" \
  "v_mfma_f32_16x16x32_bf16 v[12:15], v[40:43], v[192:195], 0\n" \
  "ds_read_b128 v[36:39], v7 offset:4096\n" \
  "s_waitcnt lgkmcnt(3)\n" \
  "v_mfma_f32_16x16x32_bf16 v[56:59], v[44:47], v[44:47], v[56:59]\n" \
  "ds_read_b128 v[40:43], v10 offset:6144\n" \
  "s_waitcnt lgkmcnt(3)\n" \
  "v_mfma_f32_16x16x32_bf16 v[12:15], v[48:51], v[196:199], v[12:15]\n" \
  "ds_read_b128 v[44:47], v7 offset:6144\n" \
  "s_waitcnt lgkmcnt(3)\n" \
  "v_mfma_f32_16x16x32_bf16 v[56:59], v[52:55], v[52:55], v[56:59]\n" \
  "ds_read_b128 v[48:51], v10 offset:8192\n" \
  "s_waitcnt lgkmcnt(3)\n" \
  "v_mfma_f32_16x16x32_bf16 v[12:15], v[36:39], v[200:203], v[12:15]\n" \
  "ds_read_b128 v[52:55], v7 offset:8192\n" \
  "s_waitcnt lgkmcnt(3)\n" \
  "v_mfma_f32_16x16x32_bf16 v[56:59], v[40:43], v[40:43], v[56:59]\n" \
  "ds_read_b128 v[36:39], v10 offset:10240\n" \
  "s_waitcnt lgkmcnt(3)\n" \
  "v_mfma_f32_16x16x32_bf16 v[12:15], v[44:47], v[204:207], v[12:15]\n" \
  "ds_read_b128 v[40:43], v7 offset:10240\n" \
  "s_waitcnt lgkmcnt(3)\n" \
  "v_mfma_f32_16x16x32_bf16 v[56:59], v[48:51], v[48:51], v[56:59]\n" \
  "ds_read_b128 v[44:47], v10 offset:12288\n" \
  "s_waitcnt lgkmcnt(3)\n" \
  "v_mfma_f32_16x16x32_bf16 v[12:15], v[52:55], v[208:211], v[12:15]\n" \
  "ds_read_b128 v[48:51], v7 offset:12288\n" \
  "s_waitcnt lgkmcnt(3)\n" \
  "v_mfma_f32_16x16x32_bf16 v[56:59], v[36:39], v[36:39], v[56:59]\n" \
  "ds_read_b128 v[52:55], v10 offset:14336\n" \
  "s_waitcnt lgkmcnt(3)\n" \
  "v_mfma_f32_16x16x32_bf16 v[12:15], v[40:43], v[212:215], v[12:15]\n" \
  "ds_read_b128 v[36:39], v7 offset:14336\n" \
  "s_waitcnt lgkmcnt(3)\n" \
  "v_mfma_f32_16x16x32_bf16 v[56:59], v[44:47], v[44:47], v[56:59]\n" \
  "ds_read_b128 v[40:43], v10 offset:16384\n" \
  "s_waitcnt lgkmcnt(3)\n" \
  "v_mfma_f32_16x16x32_bf16 v[12:15], v[48:51], v[216:219], v[12:15]\n" \
  "ds_read_b128 v[44:47], v7 offset:16384\n" \
  "s_waitcnt lgkmcnt(3)\n" \
  "v_mfma_f32_16x16x32_bf16 v[56:59], v[52:55], v[52:55], v[56:59]\n" \
  "ds_read_b128 v[48:51], v10 offset:18432\n" \
  "s_waitcnt lgkmcnt(3)\n" \
  "v_mfma_f32_16x16x32_bf16 v[12:15], v[36:39], v[220:223], v[12:15]\n" \
  "ds_read_b128 v[52:55], v7 offset:18432\n" \
  "s_waitcnt lgkmcnt(3)\n" \
  "v_mfma_f32_16x16x32_bf16 v[56:59], v[40:43], v[40:43], v[56:59]\n" \
  "ds_read_b128 v[36:39], v10 offset:20480\n" \
  "s_waitcnt lgkmcnt(3)\n" \
  "v_mfma_f32_16x16x32_bf16 v[12:15], v[44:47], v[224:227], v[12:15]\n" \
  "ds_read_b128 v[40:43], v7 offset:20480\n" \
  "s_waitcnt lgkmcnt(3)\n" \
  "v_mfma_f32_16x16x32_bf16 v[56:59], v[48:51], v[48:51], v[56:59]\n" \
  "ds_read_b128 v[44:47], v10 offset:22528\n" \
  "s_waitcnt lgkmcnt(3)\n" \
  "v_mfma_f32_16x16x32_bf16 v[12:15], v[52:55], v[228:231], v[12:15]\n" \
  "ds_read_b128 v[48:51], v7 offset:22528\n" \
  "s_waitcnt lgkmcnt(3)\n" \
  "v_mfma_f32_16x16x32_bf16 v[56:59], v[36:39], v[36:39], v[56:59]\n" \
  "ds_read_b128 v[52:55], v10 offset:24576\n" \
  "s_waitcnt lgkmcnt(3)\n" \
  "v_mfma_f32_16x16x32_bf16 v[12:15], v[40:43], v[232:235], v[12:15]\n" \
  "ds_read_b128 v[36:39], v7 offset:24576\n" \
  "s_waitcnt lgkmcnt(3)\n" \
  "v_mfma_f32_16x16x32_bf16 v[56:59], v[44:47], v[44:47], v[56:59]\n" \
  "ds_read_b128 v[40:43], v10 offset:26624\n" \
  "s_waitcnt lgkmcnt(3)\n" \
  "v_mfma_f32_16x16x32_bf16 v[12:15], v[48:51], v[236:239], v[12:15]\n" \
  "ds_read_b128 v[44:47], v7 offset:26624\n" \
  "s_waitcnt lgkmcnt(3)\n" \
  "v_mfma_f32_16x16x32_bf16 v[56:59], v[52:55], v[52:55], v[56:59]\n" \
  "ds_read_b128 v[48:51], v10 offset:28672\n" \
  "s_waitcnt lgkmcnt(3)\n" \
  "v_mfma_f32_16x16x32_bf16 v[12:15], v[36:39], v[240:243], v[12:15]\n" \
  "ds_read_b128 v[52:55], v7 offset:28672\n" \
  "s_waitcnt lgkmcnt(3)\n" \
  "v_mfma_f32_16x16x32_bf16 v[56:59], v[40:43], v[40:43], v[56:59]\n" \
  "ds_read_b128 v[36:39], v10 offset:30720\n" \
  "s_waitcnt lgkmcnt(3)\n" \
  "v_mfma_f32_16x16x32_bf16 v[12:15], v[44:47], v[244:247], v[12:15]\n" \
  "ds_read_b128 v[40:43], v7 offset:30720\n" \
  "s_waitcnt lgkmcnt(3)\n" \
  "v_mfma_f32_16x16x32_bf16 v[56:59], v[48:51], v[48:51], v[56:59]\n" \
  "ds_read_b128 v[44:47], v7 offset:256\n" \
  "s_waitcnt lgkmcnt(3)\n" \
  "v_mfma_f32_16x16x32_bf16 v[12:15], v[52:55], v[248:251], v[12:15]\n" \
  "ds_read_b128 v[48:51], v7 offset:2304\n" \
  "s_waitcnt lgkmcnt(3)\n" \
  "v_mfma_f32_16x16x32_bf16 v[56:59], v[36:39], v[36:39], v[56:59]\n" \
  "ds_read_b128 v[52:55], v7 offset:4352\n" \
  "s_waitcnt lgkmcnt(3)\n" \
  "v_mfma_f32_16x16x32_bf16 v[12:15], v[40:43], v[252:255], v[12:15]\n" \
  "ds_read_b128 v[36:39], v7 offset:6400\n" \
  "s_waitcnt lgkmcnt(3)\n" \
  "v_mfma_f32_16x16x32_bf16 v[16:19], v[44:47], v[192:195], 0\n" \
  "ds_read_b128 v[40:43], v7 offset:8448\n" \
  "s_waitcnt lgkmcnt(3)\n" \
  "v_mfma_f32_16x16x32_bf16 v[16:19], v[48:51], v[196:199], v[16:19]\n" \
  "ds_read_b128 v[44:47], v7 offset:10496\n" \
  "s_waitcnt lgkmcnt(3)\n" \
  "v_mfma_f32_16x16x32_bf16 v[16:19], v[52:55], v[200:203], v[16:19]\n" \
  "ds_read_b128 v[48:51], v7 offset:12544\n" \
  "s_waitcnt lgkmcnt(3)\n" \
  "v_mfma_f32_16x16x32_bf16 v[16:19], v[36:39], v[204:207], v[16:19]\n" \
  "ds_read_b128 v[52:55], v7 offset:14592\n" \
  "s_waitcnt lgkmcnt(3)\n" \
  "v_mfma_f32_16x16x32_bf16 v[16:19], v[40:43], v[208:211], v[16:19]\n" \
  "ds_read_b128 v[36:39], v7 offset:16640\n" \
  "s_waitcnt lgkmcnt(3)\n" \
  "v_mfma_f32_16x16x32_bf16 v[16:19], v[44:47], v[212:215], v[16:19]\n" \
  "ds_read_b128 v[40:43], v7 offset:18688\n" \
  "s_waitcnt lgkmcnt(3)\n" \
  "v_mfma_f32_16x16x32_bf16 v[16:19], v[48:51], v[216:219], v[16:19]\n" \
  "ds_read_b128 v[44:47], v7 offset:20736\n" \
  "s_waitcnt lgkmcnt(3)\n" \
  "v_mfma_f32_16x16x32_bf16 v[16:19], v[52:55], v[220:223], v[16:19]\n" \
  "ds_read_b128 v[48:51], v7 offset:22784\n" \
  "s_waitcnt lgkmcnt(3)\n" \
  "v_mfma_f32_16x16x32_bf16 v[16:19], v[36:39], v[224:227], v[16:19]\n" \
  "ds_read_b128 v[52:55], v7 offset:24832\n" \
  "s_waitcnt lgkmcnt(3)\n" \
  "v_mfma_f32_16x16x32_bf16 v[16:19], v[40:43], v[228:231], v[16:19]\n" \
  "ds_read_b128 v[36:39], v7 offset:26880\n" \
  "s_waitcnt lgkmcnt(3)\n" \
  "v_mfma_f32_16x16x32_bf16 v[16:19], v[44:47], v[232:235], v[16:19]\n" \
  "ds_read_b128 v[40:43], v7 offset:28928\n" \
  "s_waitcnt lgkmcnt(3)\n" \
  "v_mfma_f32_16x16x32_bf16 v[16:19], v[48:51], v[236:239], v[16:19]\n" \
  "ds_read_b128 v[44:47], v7 offset:30976\n" \
  "s_waitcnt lgkmcnt(3)\n" \
  "v_mfma_f32_16x16x32_bf16 v[16:19], v[52:55], v[240:243], v[16:19]\n" \
  "ds_read_b128 v[48:51], v7 offset:0\n" \
  "v_lshrrev_b32 v63, 2, v6\n" \
  "v_and_b32 v63, 0x9c, v63\n" \
  "buffer_load_dword v63, v63, s[52:55], s62 offen sc1\n" \
  "s_waitcnt lgkmcnt(3)\n" \
  "v_mfma_f32_16x16x32_bf16 v[16:19], v[36:39], v[244:247], v[16:19]\n" \
  "ds_read_b128 v[52:55], v7 offset:2048\n" \
  "s_waitcnt lgkmcnt(3)\n" \
  "v_mfma_f32_16x16x32_bf16 v[16:19], v[40:43], v[248:251], v[16:19]\n" \
  "ds_read_b128 v[36:39], v7 offset:4096\n" \
  "s_waitcnt lgkmcnt(3)\n" \
  "v_mfma_f32_16x16x32_bf16 v[16:19], v[44:47], v[252:255], v[16:19]\n" \
  "ds_read_b128 v[40:43], v7 offset:6144\n" \
  "s_setprio 0\n" \
  "s_mov_b32 s0, 0xbe801d62\n" \
  ".set .Lpad0xaa, . - 4\n" \
  "v_cndmask_b32 v56, v56, v57, s[70:71]\n" \
  "v_cndmask_b32 v57, v58, v59, s[70:71]\n" \
  "v_cndmask_b32 v56, v56, v57, s[72:73]\n" \
  "v_mov_b32 v57, v56\n" \
  "v_mov_b32 v58, v56\n" \
  "v_mul_f32 v56, 0x3b000000, v56\n" \
  "v_xor_b32 v10, v11, v10\n" \
  "v_permlane32_swap_b32 v57, v58\n" \
  "v_mul_f32 v57, 0xbb000000, v58\n" \
  "v_fma_f32 v56, -v57, v57, v56\n" \
  "v_max_f32 v56, 0, v56\n" \
  "v_cvt_pk_bf16_f32 v56, v56, v56\n" \
  "v_lshlrev_b32 v56, 16, v56\n" \
  "v_add_f32 v56, 0x3727c5ac, v56\n" \
  "v_cvt_pk_bf16_f32 v56, v56, v56\n" \
  "v_lshlrev_b32 v56, 16, v56\n" \
  "v_sqrt_f32 v56, v56\n" \
  "v_cvt_pk_bf16_f32 v57, v57, v57\n" \
  "v_cvt_pk_bf16_f32 v56, v56, v56\n" \
  "v_lshlrev_b32 v56, 16, v56\n" \
  "v_rcp_f32 v56, v56\n" \
  "s_mov_b64 exec, 0xf0000f\n" \
  "ds_write2_b32 v62, v57, v56 offset1:32\n" \
  "s_mov_b64 exec, -1\n" \
  "s_cmp_eq_u32 s66, 63\n" \
  "s_cbranch_scc1 .LQB_NOE_%=\n" \
  "v_mul_f32 v20, v20, v60\n" \
  "v_mul_f32 v21, v21, v60\n" \
  "v_mul_f32 v22, v22, v60\n" \
  "v_cvt_pk_bf16_f32 v20, v20, v21\n" \
  "v_mul_f32 v23, v23, v60\n" \
  "v_mul_f32 v24, v24, v60\n" \
  "v_cvt_pk_bf16_f32 v21, v22, v23\n" \
  "v_mul_f32 v25, v25, v60\n" \
  "v_mul_f32 v26, v26, v60\n" \
  "v_cvt_pk_bf16_f32 v22, v24, v25\n" \
  "v_mul_f32 v27, v27, v60\n" \
  "v_cvt_pk_bf16_f32 v23, v26, v27\n" \
  "buffer_store_dwordx4 v[20:23], v6, s[40:43], s59 offen offset:0 sc1\n" \
  "s_add_u32 s76, s59, 4096\n" \
  "v_mul_f32 v28, v28, v61\n" \
  "v_mul_f32 v29, v29, v61\n" \
  "v_mul_f32 v30, v30, v61\n" \
  "v_cvt_pk_bf16_f32 v28, v28, v29\n" \
  "v_mul_f32 v31, v31, v61\n" \
  "v_mul_f32 v32, v32, v61\n" \
  "v_cvt_pk_bf16_f32 v29, v30, v31\n" \
  "v_mul_f32 v33, v33, v61\n" \
  "v_mul_f32 v34, v34, v61\n" \
  "v_cvt_pk_bf16_f32 v30, v32, v33\n" \
  "v_mul_f32 v35, v35, v61\n" \
  "v_cvt_pk_bf16_f32 v31, v34, v35\n" \
  "buffer_store_dwordx4 v[28:31], v6, s[40:43], s76 offen offset:0 sc1\n" \
  "s_add_u32 s59, s59, 0x80000\n" \
  "s_waitcnt vmcnt(2)\n" \
  "s_branch .LQB_LOOK_%=\n" \
  ".LQB_NOE_%=:\n" \
  "s_waitcnt vmcnt(0)\n" \
  ".LQB_LOOK_%=:\n" \
  "v_cmp_eq_u32 vcc, 0, v63\n" \
  "v_mov_b32 v58, s82\n" \
  "s_cmp_eq_u64 vcc, 0\n" \
  "s_cselect_b32 s69, 1, 0\n" \
  "v_mov_b32 v59, s69\n" \
  "ds_write_b32 v58, v59\n" \
  "s_cbranch_scc0 .LQB_NOD_%=\n" \
  "s_mov_b32 m0, s58\n" \
  "s_add_u32 s74, s56, 4096\n" \
  "s_addc_u32 s75, s57, 0\n" \
  "s_add_u32 s76, s58, 4096\n" \
  "global_load_lds_dwordx4 v6, s[56:57] offset:0 sc1\n" \
  "global_load_lds_dwordx4 v6, s[56:57] offset:1024 sc1\n" \
  "global_load_lds_dwordx4 v6, s[56:57] offset:2048 sc1\n" \
  "global_load_lds_dwordx4 v6, s[56:57] offset:3072 sc1\n" \
  "s_mov_b32 m0, s76\n" \
  "s_nop 0\n" \
  "global_load_lds_dwordx4 v6, s[74:75] offset:0 sc1\n" \
  "global_load_lds_dwordx4 v6, s[74:75] offset:1024 sc1\n" \
  "global_load_lds_dwordx4 v6, s[74:75] offset:2048 sc1\n" \
  "global_load_lds_dwordx4 v6, s[74:75] offset:3072 sc1\n" \
  ".LQB_NOD_%=:\n" \
  "s_waitcnt lgkmcnt(0)\n" \
  "s_barrier\n" \
  "ds_read2_b32 v[56:57], v8 offset0:0 offset1:0\n" \
  "ds_read2_b32 v[58:59], v8 offset0:16 offset1:16\n" \
  "ds_read_b128 v[28:31], v9 offset:128\n" \
  "ds_read_b128 v[32:35], v9 offset:192\n" \
  "ds_read2_b32 v[60:61], v8 offset0:32 offset1:48\n" \
  "s_setprio 3\n" \
  "s_waitcnt lgkmcnt(3)\n" \
  "v_mfma_f32_16x16x16_bf16 v[12:15], v[56:57], v[4:5], v[12:15]\n" \
  "v_mfma_f32_16x16x16_bf16 v[16:19], v[58:59], v[4:5], v[16:19]\n" \
  "s_setprio 0\n" \
  "s_waitcnt lgkmcnt(0)\n" \
  "s_nop 4\n" \
  "v_mul_f32 v12, v12, v28\n" \
  "v_mul_f32 v13, v13, v29\n" \
  "v_mul_f32 v14, v14, v30\n" \
  "v_mul_f32 v15, v15, v31\n" \
  "v_cvt_pk_bf16_f32 v12, v12, v13\n" \
  "v_cvt_pk_bf16_f32 v13, v14, v15\n" \
  "v_mul_f32 v16, v16, v32\n" \
  "v_mul_f32 v17, v17, v33\n" \
  "v_mul_f32 v18, v18, v34\n" \
  "v_mul_f32 v19, v19, v35\n" \
  "v_cvt_pk_bf16_f32 v14, v16, v17\n" \
  "v_cvt_pk_bf16_f32 v15, v18, v19\n" \
  "buffer_store_dwordx4 v[12:15], v6, s[44:47], s60 offen offset:0 sc1\n" \
  "v_mov_b64 v[16:17], 0\n" \
  "v_lshrrev_b32 v18, 1, v6\n" \
  "buffer_store_dwordx2 v[16:17], v18, s[48:51], s61 offen sc1\n" \
  "s_setprio 0\n" \
  "s_add_u32 s60, s60, 0x40000\n" \
  "s_add_u32 s61, s61, 0x10000\n" \
  "s_mov_b32 s0, 0xbe801d62\n" \
  ".set .Lpad0xab, . - 4\n" \
  "v_mov_b32 v12, s80\n" \
  "v_mov_b32 v13, 1\n" \
  "v_mfma_f32_16x16x16_bf16 v[20:23], v[0:1], v[56:57], 0\n" \
  "v_mfma_f32_16x16x16_bf16 v[24:27], v[2:3], v[56:57], 0\n" \
  "v_mfma_f32_16x16x16_bf16 v[28:31], v[0:1], v[58:59], 0\n" \
  "v_mfma_f32_16x16x16_bf16 v[32:35], v[2:3], v[58:59], 0\n" \
  "v_mfma_f32_16x16x32_bf16 v[20:23], v[64:67], v[48:51], v[20:23]\n" \
  "ds_read_b128 v[44:47], v7 offset:8192\n" \
  "v_mfma_f32_16x16x32_bf16 v[24:27], v[128:131], v[48:51], v[24:27]\n" \
  "v_mfma_f32_16x16x32_bf16 v[20:23], v[68:71], v[52:55], v[20:23]\n" \
  "ds_read_b128 v[48:51], v7 offset:10240\n" \
  "v_mfma_f32_16x16x32_bf16 v[24:27], v[132:135], v[52:55], v[24:27]\n" \
  "v_mfma_f32_16x16x32_bf16 v[20:23], v[72:75], v[36:39], v[20:23]\n" \
  "ds_read_b128 v[52:55], v7 offset:12288\n" \
  "v_mfma_f32_16x16x32_bf16 v[24:27], v[136:139], v[36:39], v[24:27]\n" \
  "v_mfma_f32_16x16x32_bf16 v[20:23], v[76:79], v[40:43], v[20:23]\n" \
  "ds_read_b128 v[36:39], v7 offset:14336\n" \
  "v_mfma_f32_16x16x32_bf16 v[24:27], v[140:143], v[40:43], v[24:27]\n" \
  "s_waitcnt lgkmcnt(3)\n" \
  "v_mfma_f32_16x16x32_bf16 v[20:23], v[80:83], v[44:47], v[20:23]\n" \
  "ds_read_b128 v[40:43], v7 offset:16384\n" \
  "v_mfma_f32_16x16x32_bf16 v[24:27], v[144:147], v[44:47], v[24:27]\n" \
  "s_waitcnt lgkmcnt(3)\n" \
  "v_mfma_f32_16x16x32_bf16 v[20:23], v[84:87], v[48:51], v[20:23]\n" \
  "ds_read_b128 v[44:47], v7 offset:18432\n" \
  "v_mfma_f32_16x16x32_bf16 v[24:27], v[148:151], v[48:51], v[24:27]\n" \
  "s_waitcnt lgkmcnt(3)\n" \
  "v_mfma_f32_16x16x32_bf16 v[20:23], v[88:91], v[52:55], v[20:23]\n" \
  "ds_read_b128 v[48:51], v7 offset:20480\n" \
  "v_mfma_f32_16x16x32_bf16 v[24:27], v[152:155], v[52:55], v[24:27]\n" \
  "s_waitcnt lgkmcnt(3)\n" \
  "v_mfma_f32_16x16x32_bf16 v[20:23], v[92:95], v[36:39], v[20:23]\n" \
  "ds_read_b128 v[52:55], v7 offset:22528\n" \
  "v_mfma_f32_16x16x32_bf16 v[24:27], v[156:159], v[36:39], v[24:27]\n" \
  "s_waitcnt lgkmcnt(3)\n" \
  "v_mfma_f32_16x16x32_bf16 v[20:23], v[96:99], v[40:43], v[20:23]\n" \
  "ds_read_b128 v[36:39], v7 offset:24576\n" \
  "v_mfma_f32_16x16x32_bf16 v[24:27], v[160:163], v[40:43], v[24:27]\n" \
  "s_waitcnt lgkmcnt(3)\n" \
  "v_mfma_f32_16x16x32_bf16 v[20:23], v[100:103], v[44:47], v[20:23]\n" \
  "ds_read_b128 v[40:43], v7 offset:26624\n" \
  "v_mfma_f32_16x16x32_bf16 v[24:27], v[164:167], v[44:47], v[24:27]\n" \
  "s_waitcnt lgkmcnt(3)\n" \
  "v_mfma_f32_16x16x32_bf16 v[20:23], v[104:107], v[48:51], v[20:23]\n" \
  "ds_read_b128 v[44:47], v7 offset:28672\n" \
  "v_mfma_f32_16x16x32_bf16 v[24:27], v[168:171], v[48:51], v[24:27]\n" \
  "s_waitcnt lgkmcnt(3)\n" \
  "v_mfma_f32_16x16x32_bf16 v[20:23], v[108:111], v[52:55], v[20:23]\n" \
  "ds_read_b128 v[48:51], v7 offset:30720\n" \
  "v_mfma_f32_16x16x32_bf16 v[24:27], v[172:175], v[52:55], v[24:27]\n" \
  "s_waitcnt lgkmcnt(3)\n" \
  "v_mfma_f32_16x16x32_bf16 v[20:23], v[112:115], v[36:39], v[20:23]\n" \
  "ds_read_b128 v[52:55], v7 offset:256\n" \
  "v_mfma_f32_16x16x32_bf16 v[24:27], v[176:179], v[36:39], v[24:27]\n" \
  "s_waitcnt lgkmcnt(3)\n" \
  "v_mfma_f32_16x16x32_bf16 v[20:23], v[116:119], v[40:43], v[20:23]\n" \
  "ds_read_b128 v[36:39], v7 offset:2304\n" \
  "v_mfma_f32_16x16x32_bf16 v[24:27], v[180:183], v[40:43], v[24:27]\n" \
  "s_waitcnt lgkmcnt(3)\n" \
  "v_mfma_f32_16x16x32_bf16 v[20:23], v[120:123], v[44:47], v[20:23]\n" \
  "ds_read_b128 v[40:43], v7 offset:4352\n" \
  "v_mfma_f32_16x16x32_bf16 v[24:27], v[184:187], v[44:47], v[24:27]\n" \
  "s_waitcnt lgkmcnt(3)\n" \
  "v_mfma_f32_16x16x32_bf16 v[20:23], v[124:127], v[48:51], v[20:23]\n" \
  "ds_read_b128 v[44:47], v7 offset:6400\n" \
  "v_mfma_f32_16x16x32_bf16 v[24:27], v[188:191], v[48:51], v[24:27]\n" \
  "s_waitcnt lgkmcnt(3)\n" \
  "v_mfma_f32_16x16x32_bf16 v[28:31], v[64:67], v[52:55], v[28:31]\n" \
  "ds_read_b128 v[48:51], v7 offset:8448\n" \
  "s_waitcnt vmcnt(0)\n" \
  "s_mov_b64 exec, 1\n" \
  "ds_add_rtn_u32 v14, v12, v13\n" \
  "s_mov_b64 exec, -1\n" \
  "v_mfma_f32_16x16x32_bf16 v[32:35], v[128:131], v[52:55], v[32:35]\n" \
  "s_waitcnt lgkmcnt(4)\n" \
  "v_mfma_f32_16x16x32_bf16 v[28:31], v[68:71], v[36:39], v[28:31]\n" \
  "ds_read_b128 v[52:55], v7 offset:10496\n" \
  "v_mfma_f32_16x16x32_bf16 v[32:35], v[132:135], v[36:39], v[32:35]\n" \
  "s_waitcnt lgkmcnt(4)\n" \
  "v_mfma_f32_16x16x32_bf16 v[28:31], v[72:75], v[40:43], v[28:31]\n" \
  "ds_read_b128 v[36:39], v7 offset:12544\n" \
  "s_waitcnt lgkmcnt(2)\n" \
  "v_mfma_f32_16x16x32_bf16 v[32:35], v[136:139], v[40:43], v[32:35]\n" \
  "v_readfirstlane_b32 s74, v14\n" \
  "s_and_b32 s74, s74, 3\n" \
  "s_cmp_eq_u32 s74, 3\n" \
  "s_cbranch_scc0 4\n" \
  "s_mov_b64 exec, 1\n" \
  "global_store_dword v16, v13, s[64:65] offset:64 sc1\n" \
  "s_mov_b64 exec, -1\n" \
  "v_mfma_f32_16x16x32_bf16 v[28:31], v[76:79], v[44:47], v[28:31]\n" \
  "ds_read_b128 v[40:43], v7 offset:14592\n" \
  "s_add_u32 s64, s64, 4096\n" \
  "s_addc_u32 s65, s65, 0\n" \
  "v_mfma_f32_16x16x32_bf16 v[32:35], v[140:143], v[44:47], v[32:35]\n" \
  "s_sub_u32 s66, s66, 1\n" \
  "v_mfma_f32_16x16x32_bf16 v[28:31], v[80:83], v[48:51], v[28:31]\n" \
  "ds_read_b128 v[44:47], v7 offset:16640\n" \
  "v_mfma_f32_16x16x32_bf16 v[32:35], v[144:147], v[48:51], v[32:35]\n" \
  "s_waitcnt lgkmcnt(3)\n" \
  "v_mfma_f32_16x16x32_bf16 v[28:31], v[84:87], v[52:55], v[28:31]\n" \
  "ds_read_b128 v[48:51], v7 offset:18688\n" \
  "v_mfma_f32_16x16x32_bf16 v[32:35], v[148:151], v[52:55], v[32:35]\n" \
  "s_waitcnt lgkmcnt(3)\n" \
  "v_mfma_f32_16x16x32_bf16 v[28:31], v[88:91], v[36:39], v[28:31]\n" \
  "ds_read_b128 v[52:55], v7 offset:20736\n" \
  "v_mfma_f32_16x16x32_bf16 v[32:35], v[152:155], v[36:39], v[32:35]\n" \
  "s_waitcnt lgkmcnt(3)\n" \
  "v_mfma_f32_16x16x32_bf16 v[28:31], v[92:95], v[40:43], v[28:31]\n" \
  "ds_read_b128 v[36:39], v7 offset:22784\n" \
  "v_mfma_f32_16x16x32_bf16 v[32:35], v[156:159], v[40:43], v[32:35]\n" \
  "s_waitcnt lgkmcnt(3)\n" \
  "v_mfma_f32_16x16x32_bf16 v[28:31], v[96:99], v[44:47], v[28:31]\n" \
  "ds_read_b128 v[40:43], v7 offset:24832\n" \
  "v_mfma_f32_16x16x32_bf16 v[32:35], v[160:163], v[44:47], v[32:35]\n" \
  "s_waitcnt lgkmcnt(3)\n" \
  "v_mfma_f32_16x16x32_bf16 v[28:31], v[100:103], v[48:51], v[28:31]\n" \
  "ds_read_b128 v[44:47], v7 offset:26880\n" \
  "v_mfma_f32_16x16x32_bf16 v[32:35], v[164:167], v[48:51], v[32:35]\n" \
  "s_waitcnt lgkmcnt(3)\n" \
  "v_mfma_f32_16x16x32_bf16 v[28:31], v[104:107], v[52:55], v[28:31]\n" \
  "ds_read_b128 v[48:51], v7 offset:28928\n" \
  "v_mfma_f32_16x16x32_bf16 v[32:35], v[168:171], v[52:55], v[32:35]\n" \
  "s_waitcnt lgkmcnt(3)\n" \
  "v_mfma_f32_16x16x32_bf16 v[28:31], v[108:111], v[36:39], v[28:31]\n" \
  "ds_read_b128 v[52:55], v7 offset:30976\n" \
  "v_mfma_f32_16x16x32_bf16 v[32:35], v[172:175], v[36:39], v[32:35]\n" \
  "s_waitcnt lgkmcnt(3)\n" \
  "v_mfma_f32_16x16x32_bf16 v[28:31], v[112:115], v[40:43], v[28:31]\n" \
  "v_mfma_f32_16x16x32_bf16 v[32:35], v[176:179], v[40:43], v[32:35]\n" \
  "v_xor_b32 v7, 0x20000, v7\n" \
  "s_waitcnt lgkmcnt(0)\n" \
  "s_barrier\n" \
  "ds_read_b128 v[36:39], v10 offset:0\n" \
  "ds_read_b128 v[40:43], v7 offset:0\n" \
  "v_mfma_f32_16x16x32_bf16 v[28:31], v[116:119], v[44:47], v[28:31]\n" \
  "v_mfma_f32_16x16x32_bf16 v[32:35], v[180:183], v[44:47], v[32:35]\n" \
  "ds_read_b128 v[44:47], v10 offset:2048\n" \
  "v_mfma_f32_16x16x32_bf16 v[28:31], v[120:123], v[48:51], v[28:31]\n" \
  "v_mfma_f32_16x16x32_bf16 v[32:35], v[184:187], v[48:51], v[32:35]\n" \
  "ds_read_b128 v[48:51], v7 offset:2048\n" \
  "v_mfma_f32_16x16x32_bf16 v[28:31], v[124:127], v[52:55], v[28:31]\n" \
  "v_mfma_f32_16x16x32_bf16 v[32:35], v[188:191], v[52:55], v[32:35]\n" \
  "s_cbranch_scc1 .LQB_LAST_%=\n" \
  "s_branch .LQB_TOP_%=\n" \
  ".LQB_LAST_%=:\n" \
  "s_nop 7\n" \
  "v_mul_f32 v20, v20, v60\n" \
  "v_mul_f32 v21, v21, v60\n" \
  "v_mul_f32 v22, v22, v60\n" \
  "v_cvt_pk_bf16_f32 v20, v20, v21\n" \
  "v_mul_f32 v23, v23, v60\n" \
  "v_mul_f32 v24, v24, v60\n" \
  "v_cvt_pk_bf16_f32 v21, v22, v23\n" \
  "v_mul_f32 v25, v25, v60\n" \
  "v_mul_f32 v26, v26, v60\n" \
  "v_cvt_pk_bf16_f32 v22, v24, v25\n" \
  "v_mul_f32 v27, v27, v60\n" \
  "v_cvt_pk_bf16_f32 v23, v26, v27\n" \
  "buffer_store_dwordx4 v[20:23], v6, s[40:43], s59 offen offset:0 sc1\n" \
  "s_add_u32 s76, s59, 4096\n" \
  "v_mul_f32 v28, v28, v61\n" \
  "v_mul_f32 v29, v29, v61\n" \
  "v_mul_f32 v30, v30, v61\n" \
  "v_cvt_pk_bf16_f32 v28, v28, v29\n" \
  "v_mul_f32 v31, v31, v61\n" \
  "v_mul_f32 v32, v32, v61\n" \
  "v_cvt_pk_bf16_f32 v29, v30, v31\n" \
  "v_mul_f32 v33, v33, v61\n" \
  "v_mul_f32 v34, v34, v61\n" \
  "v_cvt_pk_bf16_f32 v30, v32, v33\n" \
  "v_mul_f32 v35, v35, v61\n" \
  "v_cvt_pk_bf16_f32 v31, v34, v35\n" \
  "buffer_store_dwordx4 v[28:31], v6, s[40:43], s76 offen offset:0 sc1\n" \
  "s_add_u32 s59, s59, 0x80000\n" \
  "s_waitcnt vmcnt(0)\n" \
  "s_mov_b64 exec, 1\n" \
  "ds_add_rtn_u32 v14, v12, v13\n" \
  "s_mov_b64 exec, -1\n" \
  "s_waitcnt lgkmcnt(0)\n" \
  "v_readfirstlane_b32 s74, v14\n" \
  "s_and_b32 s74, s74, 3\n" \
  "s_cmp_eq_u32 s74, 3\n" \
  "s_cbranch_scc0 4\n" \
  "s_mov_b64 exec, 1\n" \
  "global_store_dword v16, v13, s[64:65] offset:64 sc1\n" \
  "s_mov_b64 exec, -1\n" \
  "s_endpgm\n" \
  ".p2alignl 12, 0xbf800000\n" \
  ".Lpad0x8c:\n" \
  "s_setpc_b64 s[98:99]\n"
#define AW_ASM_0 \
  "v_add_u32 v17, s61, v1\n" \
  "ds_write_b128 v17, v[8:11]\n" \
  "ds_write_b128 v17, v[12:15] offset:1024\n" \
  ".p2align 6\n" \
  ".LWA_AL_%=:\n" \
  ".LWA_TOP_%=:\n" \
  "v_mov_b32 v16, s65\n" \
  "v_add_u32 v17, s61, v1\n" \
  "s_add_u32 s64, s64, 2\n" \
  "s_mov_b32 s77, 0x1000000\n" \
  "s_branch .LWA_LAND_%=\n" \
  ".LWA_WAIT_%=:\n" \
  "s_setprio 0\n" \
  "s_sleep 1\n" \
  "s_sub_u32 s77, s77, 1\n" \
  "s_cbranch_scc1 .LWA_GO_%=\n" \
  ".LWA_LAND_%=:\n" \
  "ds_read_b32 v18, v16\n" \
  "s_waitcnt lgkmcnt(0)\n" \
  "v_readfirstlane_b32 s68, v18\n" \
  "s_cmp_lt_u32 s68, s64\n" \
  "s_cbranch_scc1 .LWA_WAIT_%=\n" \
  ".LWA_GO_%=:\n" \
  "ds_read_b128 v[64:67], v17 offset:0\n" \
  "ds_read_b128 v[68:71], v17 offset:1024\n" \
  "s_setprio 3\n" \
  "ds_read_b128 v[16:19], v4 offset:0\n" \
  "ds_read_b128 v[32:35], v3 offset:8192\n" \
  "ds_read_b128 v[48:51], v3 offset:12288\n" \
  "ds_read_b128 v[20:23], v4 offset:1024\n" \
  "ds_read_b128 v[36:39], v3 offset:9216\n" \
  "ds_read_b128 v[52:55], v3 offset:13312\n" \
  "ds_read_b128 v[24:27], v4 offset:2048\n" \
  "ds_read_b128 v[40:43], v3 offset:10240\n" \
  "ds_read_b128 v[56:59], v3 offset:14336\n" \
  "ds_read_b128 v[28:31], v4 offset:3072\n" \
  "ds_read_b128 v[44:47], v3 offset:11264\n" \
  "ds_read_b128 v[60:63], v3 offset:15360\n" \
  "s_waitcnt lgkmcnt(10)\n" \
  "v_mfma_f32_16x16x32_bf16 v[64:67], v[32:35], v[16:19], v[64:67]\n" \
  "ds_read_b128 v[72:75], v3 offset:16384\n" \
  "s_waitcnt lgkmcnt(10)\n" \
  "v_mfma_f32_16x16x32_bf16 v[68:71], v[48:51], v[16:19], v[68:71]\n" \
  "ds_read_b128 v[76:79], v3 offset:17408\n" \
  "s_waitcnt lgkmcnt(9)\n" \
  "v_mfma_f32_16x16x32_bf16 v[64:67], v[36:39], v[20:23], v[64:67]\n" \
  "ds_read_b128 v[80:83], v3 offset:18432\n" \
  "s_waitcnt lgkmcnt(9)\n" \
  "v_mfma_f32_16x16x32_bf16 v[68:71], v[52:55], v[20:23], v[68:71]\n" \
  "ds_read_b128 v[84:87], v3 offset:19456\n" \
  "s_waitcnt lgkmcnt(8)\n" \
  "v_mfma_f32_16x16x32_bf16 v[64:67], v[40:43], v[24:27], v[64:67]\n" \
  "ds_read_b128 v[88:91], v3 offset:20480\n" \
  "s_waitcnt lgkmcnt(8)\n" \
  "v_mfma_f32_16x16x32_bf16 v[68:71], v[56:59], v[24:27], v[68:71]\n" \
  "ds_read_b128 v[92:95], v3 offset:21504\n" \
  "s_waitcnt lgkmcnt(7)\n" \
  "v_mfma_f32_16x16x32_bf16 v[64:67], v[44:47], v[28:31], v[64:67]\n" \
  "ds_read_b128 v[96:99], v3 offset:22528\n" \
  "s_waitcnt lgkmcnt(7)\n" \
  "v_mfma_f32_16x16x32_bf16 v[68:71], v[60:63], v[28:31], v[68:71]\n" \
  "ds_read_b128 v[100:103], v3 offset:23552\n" \
  "s_add_u32 s56, s56, 3\n" \
  "s_sub_u32 s68, s56, %[wd]\n" \
  "s_bitcmp1_b32 s68, 0\n" \
  "s_cselect_b32 s58, 64, %[wd]\n" \
  "v_maximum3_f32 v10, v64, v65, v66\n" \
  "s_lshr_b32 s70, s68, 1\n" \
  "s_add_u32 s58, s58, s70\n" \
  "v_maximum3_f32 v10, v10, v67, v68\n" \
  "v_maximum3_f32 v10, v10, v69, v70\n" \
  "v_max_f32 v8, v10, v71\n" \
  "v_mov_b32 v9, v8\n" \
  "s_cmp_lt_u32 s68, %[w2]\n" \
  "s_cselect_b32 s58, s58, s56\n" \
  "v_permlane16_swap_b32 v8, v9\n" \
  "v_max_f32 v8, v8, v9\n" \
  "v_mov_b32 v9, v8\n" \
  "s_min_u32 s58, s58, 128\n" \
  "s_nop 0\n" \
  "v_permlane32_swap_b32 v8, v9\n" \
  "v_max_f32 v8, v8, v9\n" \
  "v_mul_f32 v10, s80, v8\n" \
  "v_pk_fma_f32 v[64:65], v[64:65], s[80:81], v[10:11] op_sel_hi:[1,0,0] neg_lo:[0,0,1] neg_hi:[0,0,1]\n" \
  "v_pk_fma_f32 v[66:67], v[66:67], s[80:81], v[10:11] op_sel_hi:[1,0,0] neg_lo:[0,0,1] neg_hi:[0,0,1]\n" \
  "v_pk_fma_f32 v[68:69], v[68:69], s[80:81], v[10:11] op_sel_hi:[1,0,0] neg_lo:[0,0,1] neg_hi:[0,0,1]\n" \
  "v_pk_fma_f32 v[70:71], v[70:71], s[80:81], v[10:11] op_sel_hi:[1,0,0] neg_lo:[0,0,1] neg_hi:[0,0,1]\n" \
  "v_exp_f32 v64, v64\n" \
  "v_exp_f32 v65, v65\n" \
  "v_exp_f32 v66, v66\n" \
  "v_exp_f32 v67, v67\n" \
  "v_exp_f32 v68, v68\n" \
  "v_exp_f32 v69, v69\n" \
  "v_exp_f32 v70, v70\n" \
  "v_exp_f32 v71, v71\n" \
  "v_cvt_pk_bf16_f32 v104, v64, v65\n" \
  "v_cvt_pk_bf16_f32 v105, v66, v67\n" \
  "v_cvt_pk_bf16_f32 v106, v68, v69\n" \
  "v_cvt_pk_bf16_f32 v107, v70, v71\n" \
  "v_pk_add_f32 v[64:65], v[64:65], v[66:67]\n" \
  "v_pk_add_f32 v[68:69], v[68:69], v[70:71]\n" \
  "s_waitcnt lgkmcnt(0)\n" \
  "v_pk_add_f32 v[64:65], v[64:65], v[68:69]\n" \
  "v_mfma_f32_16x16x32_bf16 v[32:35], v[72:75], v[104:107], 0\n" \
  "v_mfma_f32_16x16x32_bf16 v[48:51], v[88:91], v[104:107], 0\n" \
  "v_mfma_f32_16x16x32_bf16 v[36:39], v[76:79], v[104:107], 0\n" \
  "v_mfma_f32_16x16x32_bf16 v[52:55], v[92:95], v[104:107], 0\n" \
  "v_mfma_f32_16x16x32_bf16 v[40:43], v[80:83], v[104:107], 0\n" \
  "v_mfma_f32_16x16x32_bf16 v[56:59], v[96:99], v[104:107], 0\n" \
  "v_mfma_f32_16x16x32_bf16 v[44:47], v[84:87], v[104:107], 0\n" \
  "v_mfma_f32_16x16x32_bf16 v[60:63], v[100:103], v[104:107], 0\n" \
  "v_add_f32 v11, v64, v65\n" \
  "s_cmp_lt_u32 s56, %[w3]\n" \
  "s_cselect_b32 s68, s57, 0\n" \
  "s_cmp_ge_u32 s68, 64\n" \
  "s_cbranch_scc1 .LWA_EASY_%=\n" \
  "v_cvt_pk_bf16_f32 v108, v32, v33\n" \
  "v_cvt_pk_bf16_f32 v109, v48, v49\n" \
  "v_cvt_pk_bf16_f32 v110, v34, v35\n" \
  "v_cvt_pk_bf16_f32 v111, v50, v51\n" \
  "ds_write_b128 v7, v[108:111] offset:0\n" \
  "v_cvt_pk_bf16_f32 v112, v36, v37\n" \
  "v_cvt_pk_bf16_f32 v113, v52, v53\n" \
  "v_cvt_pk_bf16_f32 v114, v38, v39\n" \
  "v_cvt_pk_bf16_f32 v115, v54, v55\n" \
  "ds_write_b128 v7, v[112:115] offset:1024\n" \
  "v_cvt_pk_bf16_f32 v116, v40, v41\n" \
  "v_cvt_pk_bf16_f32 v117, v56, v57\n" \
  "v_cvt_pk_bf16_f32 v118, v42, v43\n" \
  "v_cvt_pk_bf16_f32 v119, v58, v59\n" \
  "ds_write_b128 v7, v[116:119] offset:2048\n" \
  "v_cvt_pk_bf16_f32 v120, v44, v45\n" \
  "v_cvt_pk_bf16_f32 v121, v60, v61\n" \
  "v_cvt_pk_bf16_f32 v122, v46, v47\n" \
  "v_cvt_pk_bf16_f32 v123, v62, v63\n" \
  "ds_write_b128 v7, v[120:123] offset:3072\n" \
  "s_waitcnt vmcnt(0)\n" \
  "s_waitcnt lgkmcnt(0)\n" \
  "s_barrier\n" \
  "s_setprio 2\n" \
  "ds_read_b128 v[48:51], v1 offset:0\n" \
  "ds_read_b128 v[52:55], v1 offset:1024\n" \
  "ds_read_b128 v[56:59], v1 offset:2048\n" \
  "s_waitcnt lgkmcnt(2)\n" \
  "v_mfma_f32_16x16x32_bf16 v[64:67], v[128:131], v[48:51], 0\n" \
  "ds_read_b128 v[60:63], v1 offset:3072\n" \
  "s_lshl_b32 s75, s57, 19\n" \
  "v_mfma_f32_16x16x32_bf16 v[68:71], v[160:163], v[48:51], 0\n" \
  "s_add_u32 s75, s75, s60\n" \
  "v_mfma_f32_16x16x32_bf16 v[72:75], v[192:195], v[48:51], 0\n" \
  "s_add_u32 s76, s75, 16384\n" \
  "v_mfma_f32_16x16x32_bf16 v[76:79], v[224:227], v[48:51], 0\n" \
  "s_waitcnt lgkmcnt(2)\n" \
  "v_mfma_f32_16x16x32_bf16 v[64:67], v[132:135], v[52:55], v[64:67]\n" \
  "ds_read_b128 v[48:51], v1 offset:8192\n" \
  "v_mfma_f32_16x16x32_bf16 v[68:71], v[164:167], v[52:55], v[68:71]\n" \
  "v_mfma_f32_16x16x32_bf16 v[72:75], v[196:199], v[52:55], v[72:75]\n" \
  "v_mfma_f32_16x16x32_bf16 v[76:79], v[228:231], v[52:55], v[76:79]\n" \
  "s_waitcnt lgkmcnt(2)\n" \
  "v_mfma_f32_16x16x32_bf16 v[64:67], v[136:139], v[56:59], v[64:67]\n" \
  "ds_read_b128 v[52:55], v1 offset:9216\n" \
  "v_mfma_f32_16x16x32_bf16 v[68:71], v[168:171], v[56:59], v[68:71]\n" \
  "v_mfma_f32_16x16x32_bf16 v[72:75], v[200:203], v[56:59], v[72:75]\n" \
  "v_mfma_f32_16x16x32_bf16 v[76:79], v[232:235], v[56:59], v[76:79]\n" \
  "s_waitcnt lgkmcnt(2)\n" \
  "v_mfma_f32_16x16x32_bf16 v[64:67], v[140:143], v[60:63], v[64:67]\n" \
  "ds_read_b128 v[56:59], v1 offset:10240\n" \
  "v_mfma_f32_16x16x32_bf16 v[68:71], v[172:175], v[60:63], v[68:71]\n" \
  "v_mfma_f32_16x16x32_bf16 v[72:75], v[204:207], v[60:63], v[72:75]\n" \
  "v_mfma_f32_16x16x32_bf16 v[76:79], v[236:239], v[60:63], v[76:79]\n" \
  "s_waitcnt lgkmcnt(2)\n" \
  "v_mfma_f32_16x16x32_bf16 v[96:99], v[144:147], v[48:51], 0\n" \
  "ds_read_b128 v[60:63], v1 offset:11264\n" \
  "v_mfma_f32_16x16x32_bf16 v[100:103], v[176:179], v[48:51], 0\n" \
  "v_mfma_f32_16x16x32_bf16 v[104:107], v[208:211], v[48:51], 0\n" \
  "v_mfma_f32_16x16x32_bf16 v[108:111], v[240:243], v[48:51], 0\n" \
  "s_waitcnt lgkmcnt(2)\n" \
  "v_mfma_f32_16x16x32_bf16 v[96:99], v[148:151], v[52:55], v[96:99]\n" \
  "ds_read_b128 v[48:51], v1 offset:4096\n" \
  "v_mfma_f32_16x16x32_bf16 v[100:103], v[180:183], v[52:55], v[100:103]\n" \
  "v_mfma_f32_16x16x32_bf16 v[104:107], v[212:215], v[52:55], v[104:107]\n" \
  "v_mfma_f32_16x16x32_bf16 v[108:111], v[244:247], v[52:55], v[108:111]\n" \
  "s_waitcnt lgkmcnt(2)\n" \
  "v_mfma_f32_16x16x32_bf16 v[96:99], v[152:155], v[56:59], v[96:99]\n" \
  "ds_read_b128 v[52:55], v1 offset:5120\n" \
  "v_mfma_f32_16x16x32_bf16 v[100:103], v[184:187], v[56:59], v[100:103]\n" \
  "v_mfma_f32_16x16x32_bf16 v[104:107], v[216:219], v[56:59], v[104:107]\n" \
  "v_mfma_f32_16x16x32_bf16 v[108:111], v[248:251], v[56:59], v[108:111]\n" \
  "s_waitcnt lgkmcnt(2)\n" \
  "v_mfma_f32_16x16x32_bf16 v[96:99], v[156:159], v[60:63], v[96:99]\n" \
  "ds_read_b128 v[56:59], v1 offset:6144\n" \
  "v_mfma_f32_16x16x32_bf16 v[100:103], v[188:191], v[60:63], v[100:103]\n" \
  "v_mfma_f32_16x16x32_bf16 v[104:107], v[220:223], v[60:63], v[104:107]\n" \
  "v_mfma_f32_16x16x32_bf16 v[108:111], v[252:255], v[60:63], v[108:111]\n" \
  "s_waitcnt lgkmcnt(2)\n" \
  "v_mfma_f32_16x16x32_bf16 v[80:83], v[128:131], v[48:51], 0\n" \
  "ds_read_b128 v[60:63], v1 offset:7168\n" \
  "v_mfma_f32_16x16x32_bf16 v[84:87], v[160:163], v[48:51], 0\n" \
  "v_mfma_f32_16x16x32_bf16 v[88:91], v[192:195], v[48:51], 0\n" \
  "v_mfma_f32_16x16x32_bf16 v[92:95], v[224:227], v[48:51], 0\n" \
  "s_waitcnt lgkmcnt(2)\n" \
  "v_mfma_f32_16x16x32_bf16 v[80:83], v[132:135], v[52:55], v[80:83]\n" \
  "ds_read_b128 v[48:51], v1 offset:12288\n" \
  "v_mfma_f32_16x16x32_bf16 v[84:87], v[164:167], v[52:55], v[84:87]\n" \
  "v_mfma_f32_16x16x32_bf16 v[88:91], v[196:199], v[52:55], v[88:91]\n" \
  "v_mfma_f32_16x16x32_bf16 v[92:95], v[228:231], v[52:55], v[92:95]\n" \
  "s_waitcnt lgkmcnt(2)\n" \
  "v_mfma_f32_16x16x32_bf16 v[80:83], v[136:139], v[56:59], v[80:83]\n" \
  "ds_read_b128 v[52:55], v1 offset:13312\n" \
  "v_mfma_f32_16x16x32_bf16 v[84:87], v[168:171], v[56:59], v[84:87]\n" \
  "v_mfma_f32_16x16x32_bf16 v[88:91], v[200:203], v[56:59], v[88:91]\n" \
  "v_mfma_f32_16x16x32_bf16 v[92:95], v[232:235], v[56:59], v[92:95]\n" \
  "s_waitcnt lgkmcnt(2)\n" \
  "v_mfma_f32_16x16x32_bf16 v[80:83], v[140:143], v[60:63], v[80:83]\n" \
  "ds_read_b128 v[56:59], v1 offset:14336\n" \
  "v_mfma_f32_16x16x32_bf16 v[84:87], v[172:175], v[60:63], v[84:87]\n" \
  "v_mfma_f32_16x16x32_bf16 v[88:91], v[204:207], v[60:63], v[88:91]\n" \
  "v_mfma_f32_16x16x32_bf16 v[92:95], v[236:239], v[60:63], v[92:95]\n" \
  "s_waitcnt lgkmcnt(2)\n" \
  "v_mfma_f32_16x16x32_bf16 v[112:115], v[144:147], v[48:51], 0\n" \
  "ds_read_b128 v[60:63], v1 offset:15360\n" \
  "v_mfma_f32_16x16x32_bf16 v[116:119], v[176:179], v[48:51], 0\n" \
  "v_mfma_f32_16x16x32_bf16 v[120:123], v[208:211], v[48:51], 0\n" \
  "v_mfma_f32_16x16x32_bf16 v[124:127], v[240:243], v[48:51], 0\n" \
  "s_waitcnt lgkmcnt(2)\n" \
  "v_mfma_f32_16x16x32_bf16 v[112:115], v[148:151], v[52:55], v[112:115]\n" \
  "v_mfma_f32_16x16x32_bf16 v[116:119], v[180:183], v[52:55], v[116:119]\n" \
  "v_mfma_f32_16x16x32_bf16 v[120:123], v[212:215], v[52:55], v[120:123]\n" \
  "v_mfma_f32_16x16x32_bf16 v[124:127], v[244:247], v[52:55], v[124:127]\n" \
  "s_waitcnt lgkmcnt(1)\n" \
  "v_mfma_f32_16x16x32_bf16 v[112:115], v[152:155], v[56:59], v[112:115]\n" \
  "v_mfma_f32_16x16x32_bf16 v[116:119], v[184:187], v[56:59], v[116:119]\n" \
  "v_mfma_f32_16x16x32_bf16 v[120:123], v[216:219], v[56:59], v[120:123]\n" \
  "v_mfma_f32_16x16x32_bf16 v[124:127], v[248:251], v[56:59], v[124:127]\n" \
  "s_waitcnt lgkmcnt(0)\n" \
  "v_mfma_f32_16x16x32_bf16 v[112:115], v[156:159], v[60:63], v[112:115]\n" \
  "v_mfma_f32_16x16x32_bf16 v[116:119], v[188:191], v[60:63], v[116:119]\n" \
  "v_mfma_f32_16x16x32_bf16 v[120:123], v[220:223], v[60:63], v[120:123]\n" \
  "v_mfma_f32_16x16x32_bf16 v[124:127], v[252:255], v[60:63], v[124:127]\n" \
  "s_setprio 3\n" \
  "s_barrier\n" \
  "ds_read_b128 v[32:35], v5 offset:0\n" \
  "ds_read_b128 v[36:39], v5 offset:512\n" \
  "ds_read_b128 v[40:43], v5 offset:1024\n" \
  "ds_read_b128 v[44:47], v5 offset:1536\n" \
  "ds_read_b64_tr_b16 v[48:49], v6 offset:0\n" \
  "ds_read_b64_tr_b16 v[50:51], v6 offset:32\n" \
  "ds_read_b64_tr_b16 v[52:53], v6 offset:1024\n" \
  "ds_read_b64_tr_b16 v[54:55], v6 offset:1056\n" \
  "ds_read_b64_tr_b16 v[56:57], v6 offset:4096\n" \
  "ds_read_b64_tr_b16 v[58:59], v6 offset:4128\n" \
  "ds_read_b64_tr_b16 v[60:61], v6 offset:5120\n" \
  "ds_read_b64_tr_b16 v[62:63], v6 offset:5152\n" \
  "s_waitcnt lgkmcnt(11)\n" \
  "v_mfma_f32_16x16x32_bf16 v[12:15], v[32:35], v[16:19], 0\n" \
  "s_waitcnt lgkmcnt(10)\n" \
  "v_mfma_f32_16x16x32_bf16 v[12:15], v[36:39], v[20:23], v[12:15]\n" \
  "s_waitcnt lgkmcnt(9)\n" \
  "v_mfma_f32_16x16x32_bf16 v[12:15], v[40:43], v[24:27], v[12:15]\n" \
  "s_waitcnt lgkmcnt(8)\n" \
  "v_mfma_f32_16x16x32_bf16 v[12:15], v[44:47], v[28:31], v[12:15]\n" \
  "v_mov_b64 v[16:17], 0\n" \
  "v_mov_b64 v[18:19], 0\n" \
  "v_mov_b64 v[20:21], 0\n" \
  "v_mov_b64 v[22:23], 0\n" \
  "v_mov_b64 v[24:25], 0\n" \
  "v_mov_b64 v[26:27], 0\n" \
  "v_mov_b64 v[28:29], 0\n" \
  "v_mov_b64 v[30:31], 0\n" \
  "v_maximum3_f32 v8, v8, v12, v13\n" \
  "v_mov_b32 v9, v8\n" \
  "v_mov_b64 v[32:33], 0\n" \
  "v_mov_b64 v[34:35], 0\n" \
  "v_permlane16_swap_b32 v8, v9\n" \
  "v_max_f32 v8, v8, v9\n" \
  "v_mov_b32 v9, v8\n" \
  "v_mov_b64 v[36:37], 0\n" \
  "v_mov_b64 v[38:39], 0\n" \
  "v_permlane32_swap_b32 v8, v9\n" \
  "v_max_f32 v8, v8, v9\n" \
  "v_mul_f32 v8, s80, v8\n" \
  "v_sub_f32 v14, v10, v8\n" \
  "v_fma_f32 v12, v12, s80, -v8\n" \
  "v_fma_f32 v13, v13, s80, -v8\n" \
  "v_exp_f32 v14, v14\n" \
  "v_exp_f32 v12, v12\n" \
  "v_exp_f32 v13, v13\n" \
  "v_fma_f32 v11, v11, v14, v12\n" \
  "v_add_f32 v11, v11, v13\n" \
  "v_mov_b32 v9, v11\n" \
  "v_mov_b64 v[40:41], 0\n" \
  "v_mov_b64 v[42:43], 0\n" \
  "v_permlane16_swap_b32 v11, v9\n" \
  "v_add_f32 v11, v11, v9\n" \
  "v_mov_b32 v9, v11\n" \
  "v_mov_b64 v[44:45], 0\n" \
  "v_mov_b64 v[46:47], 0\n" \
  "v_permlane32_swap_b32 v11, v9\n" \
  "v_add_f32 v11, v11, v9\n" \
  "v_rcp_f32 v15, v11\n" \
  "v_add_u32 v9, s66, v1\n" \
  "v_mul_f32 v12, v12, v15\n" \
  "v_mul_f32 v13, v13, v15\n" \
  "v_mul_f32 v14, v14, v15\n" \
  "v_cvt_pk_bf16_f32 v12, v12, v13\n" \
  "ds_write2_b32 v9, v12, v14 offset1:2\n" \
  "s_waitcnt lgkmcnt(0)\n" \
  "s_barrier\n" \
  "ds_read_b128 v[8:11], v1 offset:24576\n" \
  "ds_read_b128 v[12:15], v1 offset:25600\n" \
  "s_setprio 0\n" \
  "v_smfmac_f32_16x16x64_bf16 v[16:19], v[48:51], v[128:135], v0\n" \
  "v_smfmac_f32_16x16x64_bf16 v[20:23], v[48:51], v[160:167], v0\n" \
  "v_smfmac_f32_16x16x64_bf16 v[24:27], v[48:51], v[192:199], v0\n" \
  "v_smfmac_f32_16x16x64_bf16 v[28:31], v[48:51], v[224:231], v0\n" \
  "v_smfmac_f32_16x16x64_bf16 v[16:19], v[52:55], v[136:143], v0\n" \
  "v_smfmac_f32_16x16x64_bf16 v[20:23], v[52:55], v[168:175], v0\n" \
  "v_smfmac_f32_16x16x64_bf16 v[24:27], v[52:55], v[200:207], v0\n" \
  "v_smfmac_f32_16x16x64_bf16 v[28:31], v[52:55], v[232:239], v0\n" \
  "v_smfmac_f32_16x16x64_bf16 v[32:35], v[56:59], v[144:151], v0\n" \
  "s_waitcnt lgkmcnt(0)\n" \
  "v_pk_mul_f32 v[64:65], v[64:65], v[10:11] op_sel_hi:[1,0]\n" \
  "v_pk_mul_f32 v[66:67], v[66:67], v[10:11] op_sel_hi:[1,0]\n" \
  "v_smfmac_f32_16x16x64_bf16 v[36:39], v[56:59], v[176:183], v0\n" \
  "v_pk_mul_f32 v[68:69], v[68:69], v[10:11] op_sel_hi:[1,0]\n" \
  "v_pk_mul_f32 v[70:71], v[70:71], v[10:11] op_sel_hi:[1,0]\n" \
  "v_pk_fma_f32 v[64:65], v[96:97], v[10:11], v[64:65] op_sel:[0,1,0]\n" \
  "v_smfmac_f32_16x16x64_bf16 v[40:43], v[56:59], v[208:215], v0\n" \
  "v_pk_fma_f32 v[66:67], v[98:99], v[10:11], v[66:67] op_sel:[0,1,0]\n" \
  "v_pk_fma_f32 v[68:69], v[100:101], v[10:11], v[68:69] op_sel:[0,1,0]\n" \
  "v_pk_fma_f32 v[70:71], v[102:103], v[10:11], v[70:71] op_sel:[0,1,0]\n" \
  "v_smfmac_f32_16x16x64_bf16 v[44:47], v[56:59], v[240:247], v0\n" \
  "v_pk_mul_f32 v[72:73], v[72:73], v[10:11] op_sel_hi:[1,0]\n" \
  "v_pk_mul_f32 v[74:75], v[74:75], v[10:11] op_sel_hi:[1,0]\n" \
  "v_pk_mul_f32 v[76:77], v[76:77], v[10:11] op_sel_hi:[1,0]\n" \
  "v_smfmac_f32_16x16x64_bf16 v[32:35], v[60:63], v[152:159], v0\n" \
  "v_pk_mul_f32 v[78:79], v[78:79], v[10:11] op_sel_hi:[1,0]\n" \
  "v_pk_fma_f32 v[72:73], v[104:105], v[10:11], v[72:73] op_sel:[0,1,0]\n" \
  "v_pk_fma_f32 v[74:75], v[106:107], v[10:11], v[74:75] op_sel:[0,1,0]\n" \
  "v_smfmac_f32_16x16x64_bf16 v[36:39], v[60:63], v[184:191], v0\n" \
  "v_pk_fma_f32 v[76:77], v[108:109], v[10:11], v[76:77] op_sel:[0,1,0]\n" \
  "v_pk_fma_f32 v[78:79], v[110:111], v[10:11], v[78:79] op_sel:[0,1,0]\n" \
  "v_pk_mul_f32 v[80:81], v[80:81], v[14:15] op_sel_hi:[1,0]\n" \
  "v_smfmac_f32_16x16x64_bf16 v[40:43], v[60:63], v[216:223], v0\n" \
  "v_pk_mul_f32 v[82:83], v[82:83], v[14:15] op_sel_hi:[1,0]\n" \
  "v_pk_mul_f32 v[84:85], v[84:85], v[14:15] op_sel_hi:[1,0]\n" \
  "v_pk_mul_f32 v[86:87], v[86:87], v[14:15] op_sel_hi:[1,0]\n" \
  "v_smfmac_f32_16x16x64_bf16 v[44:47], v[60:63], v[248:255], v0\n" \
  "v_pk_fma_f32 v[80:81], v[112:113], v[14:15], v[80:81] op_sel:[0,1,0]\n" \
  "v_pk_fma_f32 v[82:83], v[114:115], v[14:15], v[82:83] op_sel:[0,1,0]\n" \
  "v_pk_fma_f32 v[84:85], v[116:117], v[14:15], v[84:85] op_sel:[0,1,0]\n" \
  "v_pk_fma_f32 v[86:87], v[118:119], v[14:15], v[86:87] op_sel:[0,1,0]\n" \
  "v_pk_mul_f32 v[88:89], v[88:89], v[14:15] op_sel_hi:[1,0]\n" \
  "v_pk_mul_f32 v[90:91], v[90:91], v[14:15] op_sel_hi:[1,0]\n" \
  "v_pk_mul_f32 v[92:93], v[92:93], v[14:15] op_sel_hi:[1,0]\n" \
  "v_pk_mul_f32 v[94:95], v[94:95], v[14:15] op_sel_hi:[1,0]\n" \
  "v_pk_fma_f32 v[88:89], v[120:121], v[14:15], v[88:89] op_sel:[0,1,0]\n" \
  "v_pk_fma_f32 v[90:91], v[122:123], v[14:15], v[90:91] op_sel:[0,1,0]\n" \
  "v_pk_fma_f32 v[92:93], v[124:125], v[14:15], v[92:93] op_sel:[0,1,0]\n" \
  "v_pk_fma_f32 v[94:95], v[126:127], v[14:15], v[94:95] op_sel:[0,1,0]\n" \
  "v_pk_add_f32 v[16:17], v[16:17], v[18:19]\n" \
  "v_pk_add_f32 v[32:33], v[32:33], v[34:35]\n" \
  "v_cvt_pk_bf16_f32 v16, v16, v17\n" \
  "v_cvt_pk_bf16_f32 v17, v32, v33\n" \
  "v_pk_add_f32 v[20:21], v[20:21], v[22:23]\n" \
  "v_pk_add_f32 v[36:37], v[36:37], v[38:39]\n" \
  "v_cvt_pk_bf16_f32 v20, v20, v21\n" \
  "v_cvt_pk_bf16_f32 v21, v36, v37\n" \
  "v_pk_add_f32 v[24:25], v[24:25], v[26:27]\n" \
  "v_pk_add_f32 v[40:41], v[40:41], v[42:43]\n" \
  "v_mfma_f32_16x16x16_bf16 v[64:67], v[16:17], v[8:9], v[64:67]\n" \
  "v_mfma_f32_16x16x16_bf16 v[68:71], v[20:21], v[8:9], v[68:71]\n" \
  "v_cvt_pk_bf16_f32 v24, v24, v25\n" \
  "v_cvt_pk_bf16_f32 v25, v40, v41\n" \
  "v_mfma_f32_16x16x16_bf16 v[80:83], v[16:17], v[12:13], v[80:83]\n" \
  "v_mfma_f32_16x16x16_bf16 v[84:87], v[20:21], v[12:13], v[84:87]\n" \
  "v_pk_add_f32 v[28:29], v[28:29], v[30:31]\n" \
  "v_pk_add_f32 v[44:45], v[44:45], v[46:47]\n" \
  "v_cvt_pk_bf16_f32 v28, v28, v29\n" \
  "v_cvt_pk_bf16_f32 v29, v44, v45\n" \
  "s_nop 1\n" \
  "v_mfma_f32_16x16x16_bf16 v[72:75], v[24:25], v[8:9], v[72:75]\n" \
  "v_mfma_f32_16x16x16_bf16 v[76:79], v[28:29], v[8:9], v[76:79]\n" \
  "v_mfma_f32_16x16x16_bf16 v[88:91], v[24:25], v[12:13], v[88:91]\n" \
  "v_mfma_f32_16x16x16_bf16 v[92:95], v[28:29], v[12:13], v[92:95]\n" \
  "v_cvt_pk_bf16_f32 v64, v64, v65\n" \
  "v_cvt_pk_bf16_f32 v65, v66, v67\n" \
  "v_cvt_pk_bf16_f32 v66, v68, v69\n" \
  "v_cvt_pk_bf16_f32 v67, v70, v71\n" \
  "buffer_store_dwordx4 v[64:67], v2, s[40:43], s75 offen offset:0 sc1\n" \
  "v_cvt_pk_bf16_f32 v80, v80, v81\n" \
  "v_cvt_pk_bf16_f32 v81, v82, v83\n" \
  "v_cvt_pk_bf16_f32 v82, v84, v85\n" \
  "v_cvt_pk_bf16_f32 v83, v86, v87\n" \
  "buffer_store_dwordx4 v[80:83], v2, s[40:43], s76 offen offset:0 sc1\n" \
  "v_cvt_pk_bf16_f32 v72, v72, v73\n" \
  "v_cvt_pk_bf16_f32 v73, v74, v75\n" \
  "v_cvt_pk_bf16_f32 v74, v76, v77\n" \
  "v_cvt_pk_bf16_f32 v75, v78, v79\n" \
  "buffer_store_dwordx4 v[72:75], v2, s[40:43], s75 offen offset:128 sc1\n" \
  "v_cvt_pk_bf16_f32 v88, v88, v89\n" \
  "v_cvt_pk_bf16_f32 v89, v90, v91\n" \
  "v_cvt_pk_bf16_f32 v90, v92, v93\n" \
  "v_cvt_pk_bf16_f32 v91, v94, v95\n" \
  "buffer_store_dwordx4 v[88:91], v2, s[40:43], s76 offen offset:128 sc1\n" \
  ".LWA_NEXT_%=:\n" \
  "v_xor_b32 v3, 0x10000, v3\n" \
  "v_xor_b32 v4, 0x10000, v4\n" \
  "s_mov_b32 s57, s58\n" \
  "s_cmp_eq_u32 s57, 128\n" \
  "s_cbranch_scc1 .LWA_END_%=\n" \
  "s_branch .LWA_TOP_%=\n" \
  ".LWA_END_%=:\n" \
  "s_endpgm\n" \
  ".LWA_EASY_%=:\n" \
  "v_lshrrev_b32 v9, 1, v1\n" \
  "v_mov_b32 v105, 0\n" \
  "v_add_u32 v64, s63, v9\n" \
  "v_mov_b32 v106, 0\n" \
  "s_barrier\n" \
  "s_setprio 3\n" \
  "ds_read_b128 v[72:75], v5 offset:0\n" \
  "ds_read_b128 v[76:79], v5 offset:512\n" \
  "ds_read_b128 v[80:83], v5 offset:1024\n" \
  "ds_read_b128 v[84:87], v5 offset:1536\n" \
  "ds_read_b64 v[88:89], v64 offset:0\n" \
  "ds_read_b64 v[90:91], v64 offset:512\n" \
  "ds_read_b64 v[92:93], v64 offset:1024\n" \
  "ds_read_b64 v[94:95], v64 offset:1536\n" \
  "s_waitcnt lgkmcnt(7)\n" \
  "v_mfma_f32_16x16x32_bf16 v[64:67], v[72:75], v[16:19], 0\n" \
  "s_waitcnt lgkmcnt(6)\n" \
  "v_mfma_f32_16x16x32_bf16 v[64:67], v[76:79], v[20:23], v[64:67]\n" \
  "s_waitcnt lgkmcnt(5)\n" \
  "v_mfma_f32_16x16x32_bf16 v[64:67], v[80:83], v[24:27], v[64:67]\n" \
  "s_waitcnt lgkmcnt(4)\n" \
  "v_mfma_f32_16x16x32_bf16 v[64:67], v[84:87], v[28:31], v[64:67]\n" \
  "s_nop 7\n" \
  "v_maximum3_f32 v8, v8, v64, v65\n" \
  "v_mov_b32 v9, v8\n" \
  "s_nop 1\n" \
  "v_permlane16_swap_b32 v8, v9\n" \
  "v_max_f32 v8, v8, v9\n" \
  "v_mov_b32 v9, v8\n" \
  "s_nop 1\n" \
  "v_permlane32_swap_b32 v8, v9\n" \
  "v_max_f32 v8, v8, v9\n" \
  "v_mul_f32 v8, s80, v8\n" \
  "v_sub_f32 v66, v10, v8\n" \
  "v_fma_f32 v64, v64, s80, -v8\n" \
  "v_fma_f32 v65, v65, s80, -v8\n" \
  "v_exp_f32 v66, v66\n" \
  "v_exp_f32 v64, v64\n" \
  "v_exp_f32 v65, v65\n" \
  "v_fma_f32 v11, v11, v66, v64\n" \
  "v_add_f32 v11, v11, v65\n" \
  "v_mov_b32 v9, v11\n" \
  "s_nop 1\n" \
  "v_permlane16_swap_b32 v11, v9\n" \
  "v_add_f32 v11, v11, v9\n" \
  "v_mov_b32 v9, v11\n" \
  "s_nop 1\n" \
  "v_permlane32_swap_b32 v11, v9\n" \
  "v_add_f32 v11, v11, v9\n" \
  "v_rcp_f32 v68, v11\n" \
  "s_waitcnt lgkmcnt(0)\n" \
  "v_mul_f32 v66, v66, v68\n" \
  "v_mul_f32 v64, v64, v68\n" \
  "v_mul_f32 v65, v65, v68\n" \
  "v_pk_mul_f32 v[32:33], v[32:33], v[66:67] op_sel_hi:[1,0]\n" \
  "v_cvt_pk_bf16_f32 v104, v64, v65\n" \
  "v_pk_mul_f32 v[34:35], v[34:35], v[66:67] op_sel_hi:[1,0]\n" \
  "v_cvt_pk_bf16_f32 v107, v64, v65\n" \
  "v_mul_f32 v48, v48, v66\n" \
  "v_mul_f32 v49, v49, v66\n" \
  "v_mul_f32 v50, v50, v66\n" \
  "v_mul_f32 v51, v51, v66\n" \
  "v_mfma_f32_16x16x16_bf16 v[32:35], v[88:89], v[104:105], v[32:35]\n" \
  "v_mul_f32 v36, v36, v66\n" \
  "v_mul_f32 v37, v37, v66\n" \
  "v_mul_f32 v38, v38, v66\n" \
  "v_mul_f32 v39, v39, v66\n" \
  "v_mfma_f32_16x16x16_bf16 v[48:51], v[88:89], v[106:107], v[48:51]\n" \
  "v_mul_f32 v52, v52, v66\n" \
  "v_mul_f32 v53, v53, v66\n" \
  "v_mul_f32 v54, v54, v66\n" \
  "v_mul_f32 v55, v55, v66\n" \
  "v_mfma_f32_16x16x16_bf16 v[36:39], v[90:91], v[104:105], v[36:39]\n" \
  "v_mul_f32 v40, v40, v66\n" \
  "v_mul_f32 v41, v41, v66\n" \
  "v_mul_f32 v42, v42, v66\n" \
  "v_mul_f32 v43, v43, v66\n" \
  "v_mfma_f32_16x16x16_bf16 v[52:55], v[90:91], v[106:107], v[52:55]\n" \
  "v_mul_f32 v56, v56, v66\n" \
  "v_mul_f32 v57, v57, v66\n" \
  "v_mul_f32 v58, v58, v66\n" \
  "v_mul_f32 v59, v59, v66\n" \
  "v_mfma_f32_16x16x16_bf16 v[40:43], v[92:93], v[104:105], v[40:43]\n" \
  "v_mul_f32 v44, v44, v66\n" \
  "v_mul_f32 v45, v45, v66\n" \
  "v_mul_f32 v46, v46, v66\n" \
  "v_mul_f32 v47, v47, v66\n" \
  "v_mfma_f32_16x16x16_bf16 v[56:59], v[92:93], v[106:107], v[56:59]\n" \
  "v_mul_f32 v60, v60, v66\n" \
  "v_mul_f32 v61, v61, v66\n" \
  "v_mul_f32 v62, v62, v66\n" \
  "v_mul_f32 v63, v63, v66\n" \
  "v_mfma_f32_16x16x16_bf16 v[44:47], v[94:95], v[104:105], v[44:47]\n" \
  "v_cvt_pk_bf16_f32 v108, v32, v33\n" \
  "v_cvt_pk_bf16_f32 v109, v48, v49\n" \
  "v_cvt_pk_bf16_f32 v110, v34, v35\n" \
  "v_cvt_pk_bf16_f32 v111, v50, v51\n" \
  "ds_write_b128 v7, v[108:111] offset:32768\n" \
  "v_mfma_f32_16x16x16_bf16 v[60:63], v[94:95], v[106:107], v[60:63]\n" \
  "v_cvt_pk_bf16_f32 v112, v36, v37\n" \
  "v_cvt_pk_bf16_f32 v113, v52, v53\n" \
  "v_cvt_pk_bf16_f32 v114, v38, v39\n" \
  "v_cvt_pk_bf16_f32 v115, v54, v55\n" \
  "ds_write_b128 v7, v[112:115] offset:33792\n" \
  "v_cvt_pk_bf16_f32 v116, v40, v41\n" \
  "v_cvt_pk_bf16_f32 v117, v56, v57\n" \
  "v_cvt_pk_bf16_f32 v118, v42, v43\n" \
  "v_cvt_pk_bf16_f32 v119, v58, v59\n" \
  "ds_write_b128 v7, v[116:119] offset:34816\n" \
  "v_cvt_pk_bf16_f32 v120, v44, v45\n" \
  "v_cvt_pk_bf16_f32 v121, v60, v61\n" \
  "v_cvt_pk_bf16_f32 v122, v46, v47\n" \
  "v_cvt_pk_bf16_f32 v123, v62, v63\n" \
  "ds_write_b128 v7, v[120:123] offset:35840\n" \
  "s_waitcnt lgkmcnt(0)\n" \
  "s_barrier\n" \
  "s_setprio 2\n" \
  "ds_read_b128 v[48:51], v1 offset:32768\n" \
  "ds_read_b128 v[52:55], v1 offset:33792\n" \
  "ds_read_b128 v[56:59], v1 offset:34816\n" \
  "s_waitcnt lgkmcnt(2)\n" \
  "v_mfma_f32_16x16x32_bf16 v[64:67], v[128:131], v[48:51], 0\n" \
  "ds_read_b128 v[60:63], v1 offset:35840\n" \
  "s_lshl_b32 s75, s57, 19\n" \
  "v_mfma_f32_16x16x32_bf16 v[68:71], v[160:163], v[48:51], 0\n" \
  "s_add_u32 s75, s75, s60\n" \
  "v_mfma_f32_16x16x32_bf16 v[72:75], v[192:195], v[48:51], 0\n" \
  "s_add_u32 s76, s75, 16384\n" \
  "v_mfma_f32_16x16x32_bf16 v[76:79], v[224:227], v[48:51], 0\n" \
  "s_waitcnt lgkmcnt(2)\n" \
  "v_mfma_f32_16x16x32_bf16 v[64:67], v[132:135], v[52:55], v[64:67]\n" \
  "ds_read_b128 v[48:51], v1 offset:40960\n" \
  "v_mfma_f32_16x16x32_bf16 v[68:71], v[164:167], v[52:55], v[68:71]\n" \
  "v_mfma_f32_16x16x32_bf16 v[72:75], v[196:199], v[52:55], v[72:75]\n" \
  "v_mfma_f32_16x16x32_bf16 v[76:79], v[228:231], v[52:55], v[76:79]\n" \
  "s_waitcnt lgkmcnt(2)\n" \
  "v_mfma_f32_16x16x32_bf16 v[64:67], v[136:139], v[56:59], v[64:67]\n" \
  "ds_read_b128 v[52:55], v1 offset:41984\n" \
  "v_mfma_f32_16x16x32_bf16 v[68:71], v[168:171], v[56:59], v[68:71]\n" \
  "v_mfma_f32_16x16x32_bf16 v[72:75], v[200:203], v[56:59], v[72:75]\n" \
  "v_mfma_f32_16x16x32_bf16 v[76:79], v[232:235], v[56:59], v[76:79]\n" \
  "s_waitcnt lgkmcnt(2)\n" \
  "v_mfma_f32_16x16x32_bf16 v[64:67], v[140:143], v[60:63], v[64:67]\n" \
  "ds_read_b128 v[56:59], v1 offset:43008\n" \
  "v_mfma_f32_16x16x32_bf16 v[68:71], v[172:175], v[60:63], v[68:71]\n" \
  "v_mfma_f32_16x16x32_bf16 v[72:75], v[204:207], v[60:63], v[72:75]\n" \
  "v_mfma_f32_16x16x32_bf16 v[76:79], v[236:239], v[60:63], v[76:79]\n" \
  "s_waitcnt lgkmcnt(2)\n" \
  "v_mfma_f32_16x16x32_bf16 v[64:67], v[144:147], v[48:51], v[64:67]\n" \
  "ds_read_b128 v[60:63], v1 offset:44032\n" \
  "v_mfma_f32_16x16x32_bf16 v[68:71], v[176:179], v[48:51], v[68:71]\n" \
  "v_mfma_f32_16x16x32_bf16 v[72:75], v[208:211], v[48:51], v[72:75]\n" \
  "v_mfma_f32_16x16x32_bf16 v[76:79], v[240:243], v[48:51], v[76:79]\n" \
  "s_waitcnt lgkmcnt(2)\n" \
  "v_mfma_f32_16x16x32_bf16 v[64:67], v[148:151], v[52:55], v[64:67]\n" \
  "ds_read_b128 v[48:51], v1 offset:36864\n" \
  "v_mfma_f32_16x16x32_bf16 v[68:71], v[180:183], v[52:55], v[68:71]\n" \
  "v_mfma_f32_16x16x32_bf16 v[72:75], v[212:215], v[52:55], v[72:75]\n" \
  "v_mfma_f32_16x16x32_bf16 v[76:79], v[244:247], v[52:55], v[76:79]\n" \
  "s_waitcnt lgkmcnt(2)\n" \
  "v_mfma_f32_16x16x32_bf16 v[64:67], v[152:155], v[56:59], v[64:67]\n" \
  "ds_read_b128 v[52:55], v1 offset:37888\n" \
  "v_mfma_f32_16x16x32_bf16 v[68:71], v[184:187], v[56:59], v[68:71]\n" \
  "v_mfma_f32_16x16x32_bf16 v[72:75], v[216:219], v[56:59], v[72:75]\n" \
  "v_mfma_f32_16x16x32_bf16 v[76:79], v[248:251], v[56:59], v[76:79]\n" \
  "s_waitcnt lgkmcnt(2)\n" \
  "v_mfma_f32_16x16x32_bf16 v[64:67], v[156:159], v[60:63], v[64:67]\n" \
  "ds_read_b128 v[56:59], v1 offset:38912\n" \
  "v_mfma_f32_16x16x32_bf16 v[68:71], v[188:191], v[60:63], v[68:71]\n" \
  "v_mfma_f32_16x16x32_bf16 v[72:75], v[220:223], v[60:63], v[72:75]\n" \
  "v_mfma_f32_16x16x32_bf16 v[76:79], v[252:255], v[60:63], v[76:79]\n" \
  "s_waitcnt lgkmcnt(2)\n" \
  "v_mfma_f32_16x16x32_bf16 v[80:83], v[128:131], v[48:51], 0\n" \
  "ds_read_b128 v[60:63], v1 offset:39936\n" \
  "v_mfma_f32_16x16x32_bf16 v[84:87], v[160:163], v[48:51], 0\n" \
  "v_mfma_f32_16x16x32_bf16 v[88:91], v[192:195], v[48:51], 0\n" \
  "v_mfma_f32_16x16x32_bf16 v[92:95], v[224:227], v[48:51], 0\n" \
  "s_waitcnt lgkmcnt(2)\n" \
  "v_mfma_f32_16x16x32_bf16 v[80:83], v[132:135], v[52:55], v[80:83]\n" \
  "ds_read_b128 v[48:51], v1 offset:45056\n" \
  "v_mfma_f32_16x16x32_bf16 v[84:87], v[164:167], v[52:55], v[84:87]\n" \
  "v_cvt_pk_bf16_f32 v64, v64, v65\n" \
  "v_mfma_f32_16x16x32_bf16 v[88:91], v[196:199], v[52:55], v[88:91]\n" \
  "v_cvt_pk_bf16_f32 v65, v66, v67\n" \
  "v_mfma_f32_16x16x32_bf16 v[92:95], v[228:231], v[52:55], v[92:95]\n" \
  "s_waitcnt lgkmcnt(2)\n" \
  "v_mfma_f32_16x16x32_bf16 v[80:83], v[136:139], v[56:59], v[80:83]\n" \
  "ds_read_b128 v[52:55], v1 offset:46080\n" \
  "v_mfma_f32_16x16x32_bf16 v[84:87], v[168:171], v[56:59], v[84:87]\n" \
  "v_cvt_pk_bf16_f32 v66, v68, v69\n" \
  "v_mfma_f32_16x16x32_bf16 v[88:91], v[200:203], v[56:59], v[88:91]\n" \
  "v_cvt_pk_bf16_f32 v67, v70, v71\n" \
  "v_mfma_f32_16x16x32_bf16 v[92:95], v[232:235], v[56:59], v[92:95]\n" \
  "s_waitcnt lgkmcnt(2)\n" \
  "v_mfma_f32_16x16x32_bf16 v[80:83], v[140:143], v[60:63], v[80:83]\n" \
  "ds_read_b128 v[56:59], v1 offset:47104\n" \
  "v_mfma_f32_16x16x32_bf16 v[84:87], v[172:175], v[60:63], v[84:87]\n" \
  "buffer_store_dwordx4 v[64:67], v2, s[40:43], s75 offen offset:0 sc1\n" \
  "v_mfma_f32_16x16x32_bf16 v[88:91], v[204:207], v[60:63], v[88:91]\n" \
  "v_cvt_pk_bf16_f32 v72, v72, v73\n" \
  "v_mfma_f32_16x16x32_bf16 v[92:95], v[236:239], v[60:63], v[92:95]\n" \
  "s_waitcnt lgkmcnt(2)\n" \
  "v_mfma_f32_16x16x32_bf16 v[80:83], v[144:147], v[48:51], v[80:83]\n" \
  "ds_read_b128 v[60:63], v1 offset:48128\n" \
  "v_mfma_f32_16x16x32_bf16 v[84:87], v[176:179], v[48:51], v[84:87]\n" \
  "v_cvt_pk_bf16_f32 v73, v74, v75\n" \
  "v_mfma_f32_16x16x32_bf16 v[88:91], v[208:211], v[48:51], v[88:91]\n" \
  "v_cvt_pk_bf16_f32 v74, v76, v77\n" \
  "v_mfma_f32_16x16x32_bf16 v[92:95], v[240:243], v[48:51], v[92:95]\n" \
  "s_waitcnt lgkmcnt(2)\n" \
  "v_mfma_f32_16x16x32_bf16 v[80:83], v[148:151], v[52:55], v[80:83]\n" \
  "v_mfma_f32_16x16x32_bf16 v[84:87], v[180:183], v[52:55], v[84:87]\n" \
  "v_cvt_pk_bf16_f32 v75, v78, v79\n" \
  "v_mfma_f32_16x16x32_bf16 v[88:91], v[212:215], v[52:55], v[88:91]\n" \
  "buffer_store_dwordx4 v[72:75], v2, s[40:43], s75 offen offset:128 sc1\n" \
  "v_mfma_f32_16x16x32_bf16 v[92:95], v[244:247], v[52:55], v[92:95]\n" \
  "s_waitcnt lgkmcnt(1)\n" \
  "v_mfma_f32_16x16x32_bf16 v[80:83], v[152:155], v[56:59], v[80:83]\n" \
  "v_mfma_f32_16x16x32_bf16 v[84:87], v[184:187], v[56:59], v[84:87]\n" \
  "v_mfma_f32_16x16x32_bf16 v[88:91], v[216:219], v[56:59], v[88:91]\n" \
  "v_mfma_f32_16x16x32_bf16 v[92:95], v[248:251], v[56:59], v[92:95]\n" \
  "s_waitcnt lgkmcnt(0)\n" \
  "v_mfma_f32_16x16x32_bf16 v[80:83], v[156:159], v[60:63], v[80:83]\n" \
  "v_mfma_f32_16x16x32_bf16 v[84:87], v[188:191], v[60:63], v[84:87]\n" \
  "v_mfma_f32_16x16x32_bf16 v[88:91], v[220:223], v[60:63], v[88:91]\n" \
  "v_mfma_f32_16x16x32_bf16 v[92:95], v[252:255], v[60:63], v[92:95]\n" \
  "s_nop 4\n" \
  "v_cvt_pk_bf16_f32 v80, v80, v81\n" \
  "v_cvt_pk_bf16_f32 v81, v82, v83\n" \
  "v_cvt_pk_bf16_f32 v82, v84, v85\n" \
  "v_cvt_pk_bf16_f32 v83, v86, v87\n" \
  "buffer_store_dwordx4 v[80:83], v2, s[40:43], s76 offen offset:0 sc1\n" \
  "v_cvt_pk_bf16_f32 v88, v88, v89\n" \
  "v_cvt_pk_bf16_f32 v89, v90, v91\n" \
  "v_cvt_pk_bf16_f32 v90, v92, v93\n" \
  "v_cvt_pk_bf16_f32 v91, v94, v95\n" \
  "buffer_store_dwordx4 v[88:91], v2, s[40:43], s76 offen offset:128 sc1\n" \
  "s_branch .LWA_NEXT_%=\n" \
  ".p2alignl 12, 0xbf800000\n" \
  ".Lpad0x78:\n" \
  "s_setpc_b64 s[98:99]\n"
#define AW_ASM_1 \
  "v_lshlrev_b32 v7, 5, v0\n" \
  "s_mov_b32 s79, -1\n" \
  "s_cmp_eq_u32 s67, 0\n" \
  "s_cbranch_scc1 .LWB_LATE_%=\n" \
  "s_waitcnt vmcnt(0)\n" \
  "v_mov_b32 v12, s65\n" \
  "v_mov_b32 v13, s67\n" \
  "s_mov_b64 exec, 1\n" \
  "ds_add_u32 v12, v13\n" \
  "s_mov_b64 exec, -1\n" \
  ".p2align 6\n" \
  ".LWB_AL_%=:\n" \
  ".LWB_TOP_%=:\n" \
  "s_add_u32 s56, s56, 3\n" \
  "s_mov_b32 s77, 0\n" \
  "s_cmp_lt_u32 s56, %[w3]\n" \
  "s_cselect_b32 s68, s57, 0\n" \
  "s_cmp_ge_u32 s68, 64\n" \
  "s_cbranch_scc0 .LWB_HARD_%=\n" \
  "s_mov_b32 m0, s63\n" \
  "s_lshl_b32 s72, s57, 16\n" \
  "s_add_u32 s72, s72, s52\n" \
  "s_addc_u32 s73, s53, 0\n" \
  "global_load_lds_dwordx4 v0, s[72:73] offset:0 sc1\n" \
  "global_load_lds_dwordx4 v0, s[72:73] offset:1024 sc1\n" \
  ".LWB_HARD_%=:\n" \
  "s_sub_u32 s68, s56, %[wd]\n" \
  "s_bitcmp1_b32 s68, 0\n" \
  "s_cselect_b32 s58, 64, %[wd]\n" \
  "s_lshr_b32 s70, s68, 1\n" \
  "s_add_u32 s58, s58, s70\n" \
  "s_cmp_lt_u32 s68, %[w2]\n" \
  "s_cselect_b32 s58, s58, s56\n" \
  "s_min_u32 s58, s58, 128\n" \
  "s_lshr_b32 s68, s58, 6\n" \
  "s_add_u32 s68, s68, s58\n" \
  "s_lshl_b32 s68, s68, 12\n" \
  "buffer_load_dword v5, off, s[44:47], s68 sc1\n" \
  "s_xor_b32 s61, s61, 0x10000\n" \
  "s_xor_b32 s62, s62, 0x10000\n" \
  "s_mov_b32 s67, 0\n" \
  "s_cmp_lt_u32 s56, %[w3]\n" \
  "s_cselect_b32 s68, s57, 0\n" \
  "s_cmp_ge_u32 s68, 64\n" \
  "s_cbranch_scc1 .LWB_ASKEDE_%=\n" \
  "s_and_b32 s75, s57, 64\n" \
  "s_lshl_b32 s75, s75, 4\n" \
  "buffer_load_dword v14, off, s[84:87], s75 sc1\n" \
  "s_and_b32 s76, s57, 63\n" \
  "s_mul_i32 s76, s76, %[pp]\n" \
  "s_add_u32 s76, s76, %[mo]\n" \
  "s_lshl_b32 s59, s57, 19\n" \
  "s_add_u32 s59, s59, s66\n" \
  "s_add_u32 s78, s59, 4096\n" \
  "v_mov_b64 v[8:9], -1\n" \
  "v_mov_b64 v[10:11], -1\n" \
  "s_barrier\n" \
  "s_setprio 0\n" \
  "buffer_store_dwordx4 v[8:11], v0, s[40:43], s59 offen offset:0 sc1\n" \
  "buffer_store_dwordx4 v[8:11], v0, s[40:43], s59 offen offset:1024 sc1\n" \
  "buffer_store_dwordx4 v[8:11], v0, s[40:43], s59 offen offset:2048 sc1\n" \
  "buffer_store_dwordx4 v[8:11], v0, s[40:43], s59 offen offset:3072 sc1\n" \
  "buffer_store_dwordx4 v[8:11], v0, s[40:43], s78 offen offset:0 sc1\n" \
  "buffer_store_dwordx4 v[8:11], v0, s[40:43], s78 offen offset:1024 sc1\n" \
  "buffer_store_dwordx4 v[8:11], v0, s[40:43], s78 offen offset:2048 sc1\n" \
  "buffer_store_dwordx4 v[8:11], v0, s[40:43], s78 offen offset:3072 sc1\n" \
  "v_mov_b64 v[16:17], 0\n" \
  "v_mov_b64 v[18:19], 0\n" \
  "v_mov_b64 v[20:21], 0\n" \
  "v_mov_b64 v[22:23], 0\n" \
  "v_mov_b64 v[24:25], 0\n" \
  "v_mov_b64 v[26:27], 0\n" \
  "v_mov_b64 v[28:29], 0\n" \
  "v_mov_b64 v[30:31], 0\n" \
  "v_mov_b64 v[32:33], 0\n" \
  "v_mov_b64 v[34:35], 0\n" \
  "v_mov_b64 v[36:37], 0\n" \
  "v_mov_b64 v[38:39], 0\n" \
  "v_mov_b64 v[40:41], 0\n" \
  "v_mov_b64 v[42:43], 0\n" \
  "v_mov_b64 v[44:45], 0\n" \
  "v_mov_b64 v[46:47], 0\n" \
  "s_waitcnt vmcnt(8)\n" \
  "v_readfirstlane_b32 s74, v14\n" \
  "s_add_u32 s74, s74, s76\n" \
  "s_cmp_lt_i32 s79, 0\n" \
  "s_cbranch_scc1 .LWB_NOD_%=\n" \
  "v_mov_b32 v12, s64\n" \
  "v_mov_b32 v13, 1\n" \
  "s_mov_b64 exec, 1\n" \
  "ds_add_rtn_u32 v15, v12, v13\n" \
  "s_mov_b64 exec, -1\n" \
  "s_waitcnt lgkmcnt(0)\n" \
  "v_readfirstlane_b32 s68, v15\n" \
  "s_and_b32 s68, s68, 3\n" \
  "s_cmp_lg_u32 s68, 3\n" \
  "s_cbranch_scc1 .LWB_NOD_%=\n" \
  "s_memrealtime s[68:69]\n" \
  "s_lshl_b32 s72, s79, 11\n" \
  "s_add_u32 s72, s72, s54\n" \
  "s_addc_u32 s73, s55, 0\n" \
  "s_waitcnt lgkmcnt(0)\n" \
  "s_add_u32 s70, s68, 0\n" \
  "s_or_b32 s70, s70, 1\n" \
  "v_mov_b32 v13, s70\n" \
  "s_mov_b64 exec, 15\n" \
  "global_store_dword v7, v13, s[72:73] sc1\n" \
  "s_mov_b64 exec, -1\n" \
  ".LWB_NOD_%=:\n" \
  "s_mov_b32 s79, -1\n" \
  "v_cmp_eq_u32 vcc, 0, v5\n" \
  "s_cmp_eq_u64 vcc, 0\n" \
  "s_cbranch_scc0 .LWB_NOT_%=\n" \
  "s_mov_b32 m0, s61\n" \
  "s_lshl_b32 s68, s58, 19\n" \
  "s_add_u32 s68, s68, s48\n" \
  "s_addc_u32 s69, s49, 0\n" \
  "global_load_lds_dwordx4 v0, s[68:69] offset:0 sc1\n" \
  "global_load_lds_dwordx4 v0, s[68:69] offset:1024 sc1\n" \
  "global_load_lds_dwordx4 v0, s[68:69] offset:2048 sc1\n" \
  "global_load_lds_dwordx4 v0, s[68:69] offset:3072 sc1\n" \
  "s_add_u32 m0, s61, 4096\n" \
  "s_add_u32 s70, s68, 4096\n" \
  "s_addc_u32 s71, s69, 0\n" \
  "global_load_lds_dwordx4 v0, s[70:71] offset:0 sc1\n" \
  "global_load_lds_dwordx4 v0, s[70:71] offset:1024 sc1\n" \
  "global_load_lds_dwordx4 v0, s[70:71] offset:2048 sc1\n" \
  "global_load_lds_dwordx4 v0, s[70:71] offset:3072 sc1\n" \
  "s_mov_b32 m0, s62\n" \
  "s_lshl_b32 s72, s58, 18\n" \
  "s_add_u32 s72, s72, s50\n" \
  "s_addc_u32 s73, s51, 0\n" \
  "global_load_lds_dwordx4 v0, s[72:73] offset:0 sc1\n" \
  "global_load_lds_dwordx4 v0, s[72:73] offset:1024 sc1\n" \
  "global_load_lds_dwordx4 v0, s[72:73] offset:2048 sc1\n" \
  "global_load_lds_dwordx4 v0, s[72:73] offset:3072 sc1\n" \
  "s_mov_b32 s67, 1\n" \
  ".LWB_NOT_%=:\n" \
  "ds_read_b128 v[48:51], v1 offset:0\n" \
  "ds_read_b128 v[52:55], v1 offset:1024\n" \
  "ds_read_b128 v[56:59], v1 offset:2048\n" \
  "s_waitcnt lgkmcnt(2)\n" \
  "v_mfma_f32_16x16x32_bf16 v[64:67], v[128:131], v[48:51], 0\n" \
  "ds_read_b128 v[60:63], v1 offset:3072\n" \
  "v_mfma_f32_16x16x32_bf16 v[68:71], v[160:163], v[48:51], 0\n" \
  "v_mfma_f32_16x16x32_bf16 v[72:75], v[192:195], v[48:51], 0\n" \
  "v_mfma_f32_16x16x32_bf16 v[76:79], v[224:227], v[48:51], 0\n" \
  "s_waitcnt lgkmcnt(2)\n" \
  "v_mfma_f32_16x16x32_bf16 v[64:67], v[132:135], v[52:55], v[64:67]\n" \
  "ds_read_b128 v[48:51], v1 offset:8192\n" \
  "v_mfma_f32_16x16x32_bf16 v[68:71], v[164:167], v[52:55], v[68:71]\n" \
  "v_mfma_f32_16x16x32_bf16 v[72:75], v[196:199], v[52:55], v[72:75]\n" \
  "v_mfma_f32_16x16x32_bf16 v[76:79], v[228:231], v[52:55], v[76:79]\n" \
  "s_waitcnt lgkmcnt(2)\n" \
  "v_mfma_f32_16x16x32_bf16 v[64:67], v[136:139], v[56:59], v[64:67]\n" \
  "ds_read_b128 v[52:55], v1 offset:9216\n" \
  "s_memrealtime s[68:69]\n" \
  "v_mfma_f32_16x16x32_bf16 v[68:71], v[168:171], v[56:59], v[68:71]\n" \
  "v_mfma_f32_16x16x32_bf16 v[72:75], v[200:203], v[56:59], v[72:75]\n" \
  "v_mfma_f32_16x16x32_bf16 v[76:79], v[232:235], v[56:59], v[76:79]\n" \
  "s_waitcnt lgkmcnt(2)\n" \
  "v_mfma_f32_16x16x32_bf16 v[64:67], v[140:143], v[60:63], v[64:67]\n" \
  "ds_read_b128 v[56:59], v1 offset:10240\n" \
  "s_waitcnt lgkmcnt(0)\n" \
  "s_sub_i32 s70, s74, s68\n" \
  "s_cmp_gt_i32 s70, 0\n" \
  "s_cbranch_scc1 .LWB_NOTYET_%=\n" \
  "s_mov_b32 m0, s63\n" \
  "s_lshl_b32 s72, s57, 16\n" \
  "s_add_u32 s72, s72, s52\n" \
  "s_addc_u32 s73, s53, 0\n" \
  "global_load_lds_dwordx4 v0, s[72:73] offset:0 sc1\n" \
  "global_load_lds_dwordx4 v0, s[72:73] offset:1024 sc1\n" \
  "s_mov_b32 s77, 1\n" \
  ".LWB_NOTYET_%=:\n" \
  "s_lshl_b32 s70, s67, 7\n" \
  "s_or_b32 s70, s70, s58\n" \
  "s_lshr_b32 s68, s70, 6\n" \
  "s_add_u32 s68, s68, s70\n" \
  "s_lshl_b32 s68, s68, 12\n" \
  "buffer_load_dword v5, off, s[44:47], s68 sc1\n" \
  "v_mfma_f32_16x16x32_bf16 v[68:71], v[172:175], v[60:63], v[68:71]\n" \
  "v_mfma_f32_16x16x32_bf16 v[72:75], v[204:207], v[60:63], v[72:75]\n" \
  "v_mfma_f32_16x16x32_bf16 v[76:79], v[236:239], v[60:63], v[76:79]\n" \
  "s_waitcnt lgkmcnt(2)\n" \
  "v_mfma_f32_16x16x32_bf16 v[96:99], v[144:147], v[48:51], 0\n" \
  "ds_read_b128 v[60:63], v1 offset:11264\n" \
  "v_mfma_f32_16x16x32_bf16 v[100:103], v[176:179], v[48:51], 0\n" \
  "v_mfma_f32_16x16x32_bf16 v[104:107], v[208:211], v[48:51], 0\n" \
  "v_mfma_f32_16x16x32_bf16 v[108:111], v[240:243], v[48:51], 0\n" \
  "s_waitcnt lgkmcnt(2)\n" \
  "v_mfma_f32_16x16x32_bf16 v[96:99], v[148:151], v[52:55], v[96:99]\n" \
  "ds_read_b128 v[48:51], v1 offset:4096\n" \
  "v_mfma_f32_16x16x32_bf16 v[100:103], v[180:183], v[52:55], v[100:103]\n" \
  "v_mfma_f32_16x16x32_bf16 v[104:107], v[212:215], v[52:55], v[104:107]\n" \
  "v_mfma_f32_16x16x32_bf16 v[108:111], v[244:247], v[52:55], v[108:111]\n" \
  "s_waitcnt lgkmcnt(2)\n" \
  "v_mfma_f32_16x16x32_bf16 v[96:99], v[152:155], v[56:59], v[96:99]\n" \
  "ds_read_b128 v[52:55], v1 offset:5120\n" \
  "v_mfma_f32_16x16x32_bf16 v[100:103], v[184:187], v[56:59], v[100:103]\n" \
  "v_mfma_f32_16x16x32_bf16 v[104:107], v[216:219], v[56:59], v[104:107]\n" \
  "v_mfma_f32_16x16x32_bf16 v[108:111], v[248:251], v[56:59], v[108:111]\n" \
  "s_waitcnt lgkmcnt(2)\n" \
  "v_mfma_f32_16x16x32_bf16 v[96:99], v[156:159], v[60:63], v[96:99]\n" \
  "ds_read_b128 v[56:59], v1 offset:6144\n" \
  "v_mfma_f32_16x16x32_bf16 v[100:103], v[188:191], v[60:63], v[100:103]\n" \
  "v_mfma_f32_16x16x32_bf16 v[104:107], v[220:223], v[60:63], v[104:107]\n" \
  "v_mfma_f32_16x16x32_bf16 v[108:111], v[252:255], v[60:63], v[108:111]\n" \
  "s_waitcnt lgkmcnt(2)\n" \
  "v_mfma_f32_16x16x32_bf16 v[80:83], v[128:131], v[48:51], 0\n" \
  "ds_read_b128 v[60:63], v1 offset:7168\n" \
  "v_mfma_f32_16x16x32_bf16 v[84:87], v[160:163], v[48:51], 0\n" \
  "v_mfma_f32_16x16x32_bf16 v[88:91], v[192:195], v[48:51], 0\n" \
  "v_mfma_f32_16x16x32_bf16 v[92:95], v[224:227], v[48:51], 0\n" \
  "s_waitcnt lgkmcnt(2)\n" \
  "v_mfma_f32_16x16x32_bf16 v[80:83], v[132:135], v[52:55], v[80:83]\n" \
  "ds_read_b128 v[48:51], v1 offset:12288\n" \
  "v_mfma_f32_16x16x32_bf16 v[84:87], v[164:167], v[52:55], v[84:87]\n" \
  "v_mfma_f32_16x16x32_bf16 v[88:91], v[196:199], v[52:55], v[88:91]\n" \
  "v_mfma_f32_16x16x32_bf16 v[92:95], v[228:231], v[52:55], v[92:95]\n" \
  "s_waitcnt lgkmcnt(2)\n" \
  "v_mfma_f32_16x16x32_bf16 v[80:83], v[136:139], v[56:59], v[80:83]\n" \
  "ds_read_b128 v[52:55], v1 offset:13312\n" \
  "v_mfma_f32_16x16x32_bf16 v[84:87], v[168:171], v[56:59], v[84:87]\n" \
  "v_mfma_f32_16x16x32_bf16 v[88:91], v[200:203], v[56:59], v[88:91]\n" \
  "v_mfma_f32_16x16x32_bf16 v[92:95], v[232:235], v[56:59], v[92:95]\n" \
  "s_waitcnt lgkmcnt(2)\n" \
  "v_mfma_f32_16x16x32_bf16 v[80:83], v[140:143], v[60:63], v[80:83]\n" \
  "ds_read_b128 v[56:59], v1 offset:14336\n" \
  "v_mfma_f32_16x16x32_bf16 v[84:87], v[172:175], v[60:63], v[84:87]\n" \
  "v_mfma_f32_16x16x32_bf16 v[88:91], v[204:207], v[60:63], v[88:91]\n" \
  "v_mfma_f32_16x16x32_bf16 v[92:95], v[236:239], v[60:63], v[92:95]\n" \
  "s_waitcnt lgkmcnt(2)\n" \
  "v_mfma_f32_16x16x32_bf16 v[112:115], v[144:147], v[48:51], 0\n" \
  "ds_read_b128 v[60:63], v1 offset:15360\n" \
  "v_mfma_f32_16x16x32_bf16 v[116:119], v[176:179], v[48:51], 0\n" \
  "v_mfma_f32_16x16x32_bf16 v[120:123], v[208:211], v[48:51], 0\n" \
  "v_mfma_f32_16x16x32_bf16 v[124:127], v[240:243], v[48:51], 0\n" \
  "s_waitcnt lgkmcnt(2)\n" \
  "v_mfma_f32_16x16x32_bf16 v[112:115], v[148:151], v[52:55], v[112:115]\n" \
  "v_mfma_f32_16x16x32_bf16 v[116:119], v[180:183], v[52:55], v[116:119]\n" \
  "v_mfma_f32_16x16x32_bf16 v[120:123], v[212:215], v[52:55], v[120:123]\n" \
  "s_waitcnt vmcnt(0)\n" \
  "ds_read_b128 v[8:11], v3 offset:0\n" \
  "ds_read_b128 v[12:15], v3 offset:1024\n" \
  "v_mfma_f32_16x16x32_bf16 v[124:127], v[244:247], v[52:55], v[124:127]\n" \
  "s_waitcnt lgkmcnt(3)\n" \
  "v_mfma_f32_16x16x32_bf16 v[112:115], v[152:155], v[56:59], v[112:115]\n" \
  "v_mfma_f32_16x16x32_bf16 v[116:119], v[184:187], v[56:59], v[116:119]\n" \
  "v_mfma_f32_16x16x32_bf16 v[120:123], v[216:219], v[56:59], v[120:123]\n" \
  "v_mfma_f32_16x16x32_bf16 v[124:127], v[248:251], v[56:59], v[124:127]\n" \
  "s_waitcnt lgkmcnt(2)\n" \
  "v_mfma_f32_16x16x32_bf16 v[112:115], v[156:159], v[60:63], v[112:115]\n" \
  "s_waitcnt lgkmcnt(0)\n" \
  "v_min3_u32 v8, v8, v9, v10\n" \
  "v_mfma_f32_16x16x32_bf16 v[116:119], v[188:191], v[60:63], v[116:119]\n" \
  "v_min3_u32 v12, v12, v13, v14\n" \
  "v_mfma_f32_16x16x32_bf16 v[120:123], v[220:223], v[60:63], v[120:123]\n" \
  "v_min3_u32 v8, v8, v11, v15\n" \
  "v_mfma_f32_16x16x32_bf16 v[124:127], v[252:255], v[60:63], v[124:127]\n" \
  "v_min_u32 v8, v8, v12\n" \
  "v_cmp_eq_u32 vcc, 0, v5\n" \
  "s_cmp_eq_u64 vcc, 0\n" \
  "s_cbranch_scc0 .LWB_NOU_%=\n" \
  "s_mov_b32 m0, s61\n" \
  "s_lshl_b32 s68, s58, 19\n" \
  "s_add_u32 s68, s68, s48\n" \
  "s_addc_u32 s69, s49, 0\n" \
  "global_load_lds_dwordx4 v0, s[68:69] offset:0 sc1\n" \
  "global_load_lds_dwordx4 v0, s[68:69] offset:1024 sc1\n" \
  "global_load_lds_dwordx4 v0, s[68:69] offset:2048 sc1\n" \
  "global_load_lds_dwordx4 v0, s[68:69] offset:3072 sc1\n" \
  "s_add_u32 m0, s61, 4096\n" \
  "s_add_u32 s70, s68, 4096\n" \
  "s_addc_u32 s71, s69, 0\n" \
  "global_load_lds_dwordx4 v0, s[70:71] offset:0 sc1\n" \
  "global_load_lds_dwordx4 v0, s[70:71] offset:1024 sc1\n" \
  "global_load_lds_dwordx4 v0, s[70:71] offset:2048 sc1\n" \
  "global_load_lds_dwordx4 v0, s[70:71] offset:3072 sc1\n" \
  "s_mov_b32 m0, s62\n" \
  "s_lshl_b32 s72, s58, 18\n" \
  "s_add_u32 s72, s72, s50\n" \
  "s_addc_u32 s73, s51, 0\n" \
  "global_load_lds_dwordx4 v0, s[72:73] offset:0 sc1\n" \
  "global_load_lds_dwordx4 v0, s[72:73] offset:1024 sc1\n" \
  "global_load_lds_dwordx4 v0, s[72:73] offset:2048 sc1\n" \
  "global_load_lds_dwordx4 v0, s[72:73] offset:3072 sc1\n" \
  "s_mov_b32 s67, 1\n" \
  ".LWB_NOU_%=:\n" \
  "s_cmp_eq_u32 s77, 1\n" \
  "s_cbranch_scc1 .LWB_HERE_%=\n" \
  "s_memrealtime s[68:69]\n" \
  "s_waitcnt lgkmcnt(0)\n" \
  "s_sub_i32 s70, s74, s68\n" \
  "s_cmp_lt_i32 s70, 50\n" \
  "s_cbranch_scc1 .LWB_SOON_%=\n" \
  ".LWB_GATEA_%=:\n" \
  "s_memrealtime s[68:69]\n" \
  "s_waitcnt lgkmcnt(0)\n" \
  "s_sub_i32 s70, s74, s68\n" \
  "s_sub_u32 s70, s70, 65\n" \
  "s_cmp_gt_u32 s70, 2000\n" \
  "s_cbranch_scc1 .LWB_NEWS_%=\n" \
  "s_sleep 1\n" \
  "s_cmp_lt_u32 s70, 40\n" \
  "s_cbranch_scc1 .LWB_GATEA_%=\n" \
  "s_sleep 8\n" \
  "s_branch .LWB_GATEA_%=\n" \
  ".LWB_HERE_%=:\n" \
  "v_mov_b32 v12, s64\n" \
  "v_mov_b32 v13, 1\n" \
  "s_mov_b64 exec, 1\n" \
  "ds_add_rtn_u32 v15, v12, v13 offset:4\n" \
  "s_mov_b64 exec, -1\n" \
  "s_waitcnt lgkmcnt(0)\n" \
  "v_readfirstlane_b32 s68, v15\n" \
  "s_and_b32 s68, s68, 3\n" \
  "s_cmp_lg_u32 s68, 3\n" \
  "s_cbranch_scc1 .LWB_TEST_%=\n" \
  "s_memrealtime s[68:69]\n" \
  "s_lshl_b32 s72, s57, 11\n" \
  "s_add_u32 s72, s72, s54\n" \
  "s_addc_u32 s73, s55, 0\n" \
  "s_waitcnt lgkmcnt(0)\n" \
  "s_add_u32 s70, s68, %[dt]\n" \
  "s_or_b32 s70, s70, 1\n" \
  "v_mov_b32 v13, s70\n" \
  "s_mov_b64 exec, 15\n" \
  "global_store_dword v7, v13, s[72:73] sc1\n" \
  "s_mov_b64 exec, -1\n" \
  "s_branch .LWB_TEST_%=\n" \
  ".LWB_NEWS_%=:\n" \
  "buffer_load_dword v14, off, s[84:87], s75 sc1\n" \
  "s_waitcnt vmcnt(0)\n" \
  "v_readfirstlane_b32 s74, v14\n" \
  "s_add_u32 s74, s74, s76\n" \
  ".LWB_SOON_%=:\n" \
  "v_mov_b32 v12, s64\n" \
  "v_mov_b32 v13, 1\n" \
  "s_mov_b64 exec, 1\n" \
  "ds_add_rtn_u32 v15, v12, v13 offset:4\n" \
  "s_mov_b64 exec, -1\n" \
  "s_waitcnt lgkmcnt(0)\n" \
  "v_readfirstlane_b32 s68, v15\n" \
  "s_and_b32 s68, s68, 3\n" \
  "s_cmp_lg_u32 s68, 3\n" \
  "s_cbranch_scc1 .LWB_GATEB_%=\n" \
  "s_memrealtime s[68:69]\n" \
  "s_lshl_b32 s72, s57, 11\n" \
  "s_add_u32 s72, s72, s54\n" \
  "s_addc_u32 s73, s55, 0\n" \
  "s_waitcnt lgkmcnt(0)\n" \
  "s_sub_i32 s70, s74, s68\n" \
  "s_cmp_gt_u32 s70, 2000\n" \
  "s_cselect_b32 s70, 0, s70\n" \
  "s_add_u32 s68, s68, s70\n" \
  "s_add_u32 s70, s68, %[du]\n" \
  "s_or_b32 s70, s70, 1\n" \
  "v_mov_b32 v13, s70\n" \
  "s_mov_b64 exec, 15\n" \
  "global_store_dword v7, v13, s[72:73] sc1\n" \
  "s_mov_b64 exec, -1\n" \
  ".LWB_GATEB_%=:\n" \
  "s_memrealtime s[68:69]\n" \
  "s_waitcnt lgkmcnt(0)\n" \
  "s_sub_i32 s70, s74, s68\n" \
  "s_sub_u32 s70, s70, 0\n" \
  "s_cmp_gt_u32 s70, 2000\n" \
  "s_cbranch_scc1 .LWB_ASK_%=\n" \
  "s_sleep 1\n" \
  "s_cmp_lt_u32 s70, 40\n" \
  "s_cbranch_scc1 .LWB_GATEB_%=\n" \
  "s_sleep 8\n" \
  "s_branch .LWB_GATEB_%=\n" \
  ".LWB_AGAIN_%=:\n" \
  "s_sleep %[sm]\n" \
  ".LWB_ASK_%=:\n" \
  "s_mov_b32 m0, s63\n" \
  "s_lshl_b32 s72, s57, 16\n" \
  "s_add_u32 s72, s72, s52\n" \
  "s_addc_u32 s73, s53, 0\n" \
  "global_load_lds_dwordx4 v0, s[72:73] offset:0 sc1\n" \
  "global_load_lds_dwordx4 v0, s[72:73] offset:1024 sc1\n" \
  "s_add_u32 s77, s77, 1\n" \
  ".LWB_ASKED_%=:\n" \
  "s_waitcnt vmcnt(0)\n" \
  "ds_read_b128 v[8:11], v3 offset:0\n" \
  "ds_read_b128 v[12:15], v3 offset:1024\n" \
  "s_cmp_gt_u32 s77, 0x40000\n" \
  "s_cbranch_scc1 .LWB_OK_%=\n" \
  "s_waitcnt lgkmcnt(1)\n" \
  "v_min3_u32 v8, v8, v9, v10\n" \
  "s_waitcnt lgkmcnt(0)\n" \
  "v_min3_u32 v12, v12, v13, v14\n" \
  "v_min3_u32 v8, v8, v11, v15\n" \
  "v_min_u32 v8, v8, v12\n" \
  ".LWB_TEST_%=:\n" \
  "v_cmp_eq_u32 vcc, 0, v8\n" \
  "s_cmp_eq_u64 vcc, 0\n" \
  "s_cbranch_scc0 .LWB_AGAIN_%=\n" \
  ".LWB_OK_%=:\n" \
  "s_waitcnt lgkmcnt(0)\n" \
  "s_barrier\n" \
  "ds_read_b64_tr_b16 v[48:49], v6 offset:0\n" \
  "ds_read_b64_tr_b16 v[50:51], v6 offset:32\n" \
  "ds_read_b64_tr_b16 v[52:53], v6 offset:1024\n" \
  "ds_read_b64_tr_b16 v[54:55], v6 offset:1056\n" \
  "ds_read_b64_tr_b16 v[56:57], v6 offset:4096\n" \
  "ds_read_b64_tr_b16 v[58:59], v6 offset:4128\n" \
  "ds_read_b64_tr_b16 v[60:61], v6 offset:5120\n" \
  "ds_read_b64_tr_b16 v[62:63], v6 offset:5152\n" \
  "s_waitcnt lgkmcnt(6)\n" \
  "v_smfmac_f32_16x16x64_bf16 v[16:19], v[48:51], v[128:135], v4\n" \
  "v_smfmac_f32_16x16x64_bf16 v[20:23], v[48:51], v[160:167], v4\n" \
  "v_smfmac_f32_16x16x64_bf16 v[24:27], v[48:51], v[192:199], v4\n" \
  "v_smfmac_f32_16x16x64_bf16 v[28:31], v[48:51], v[224:231], v4\n" \
  "s_waitcnt lgkmcnt(4)\n" \
  "v_smfmac_f32_16x16x64_bf16 v[16:19], v[52:55], v[136:143], v4\n" \
  "v_smfmac_f32_16x16x64_bf16 v[20:23], v[52:55], v[168:175], v4\n" \
  "v_smfmac_f32_16x16x64_bf16 v[24:27], v[52:55], v[200:207], v4\n" \
  "v_smfmac_f32_16x16x64_bf16 v[28:31], v[52:55], v[232:239], v4\n" \
  "s_waitcnt lgkmcnt(2)\n" \
  "v_smfmac_f32_16x16x64_bf16 v[32:35], v[56:59], v[144:151], v4\n" \
  "v_smfmac_f32_16x16x64_bf16 v[36:39], v[56:59], v[176:183], v4\n" \
  "v_smfmac_f32_16x16x64_bf16 v[40:43], v[56:59], v[208:215], v4\n" \
  "v_smfmac_f32_16x16x64_bf16 v[44:47], v[56:59], v[240:247], v4\n" \
  "s_waitcnt lgkmcnt(0)\n" \
  "v_smfmac_f32_16x16x64_bf16 v[32:35], v[60:63], v[152:159], v4\n" \
  "v_smfmac_f32_16x16x64_bf16 v[36:39], v[60:63], v[184:191], v4\n" \
  "v_smfmac_f32_16x16x64_bf16 v[40:43], v[60:63], v[216:223], v4\n" \
  "v_smfmac_f32_16x16x64_bf16 v[44:47], v[60:63], v[248:255], v4\n" \
  "s_nop 1\n" \
  "s_lshl_b32 s75, s57, 19\n" \
  "s_add_u32 s75, s75, s60\n" \
  "s_add_u32 s76, s75, 16384\n" \
  "v_pk_add_f32 v[16:17], v[16:17], v[18:19]\n" \
  "v_pk_add_f32 v[32:33], v[32:33], v[34:35]\n" \
  "v_cvt_pk_bf16_f32 v16, v16, v17\n" \
  "v_cvt_pk_bf16_f32 v17, v32, v33\n" \
  "v_pk_add_f32 v[20:21], v[20:21], v[22:23]\n" \
  "v_pk_add_f32 v[36:37], v[36:37], v[38:39]\n" \
  "v_cvt_pk_bf16_f32 v20, v20, v21\n" \
  "v_cvt_pk_bf16_f32 v21, v36, v37\n" \
  "v_pk_add_f32 v[24:25], v[24:25], v[26:27]\n" \
  "v_pk_add_f32 v[40:41], v[40:41], v[42:43]\n" \
  "v_cvt_pk_bf16_f32 v24, v24, v25\n" \
  "v_cvt_pk_bf16_f32 v25, v40, v41\n" \
  "v_pk_add_f32 v[28:29], v[28:29], v[30:31]\n" \
  "v_pk_add_f32 v[44:45], v[44:45], v[46:47]\n" \
  "v_cvt_pk_bf16_f32 v28, v28, v29\n" \
  "v_cvt_pk_bf16_f32 v29, v44, v45\n" \
  "s_barrier\n" \
  "ds_read_b128 v[32:35], v1 offset:24576\n" \
  "ds_read_b128 v[36:39], v1 offset:25600\n" \
  "s_setprio 1\n" \
  "s_waitcnt lgkmcnt(0)\n" \
  "v_pk_mul_f32 v[64:65], v[64:65], v[34:35] op_sel_hi:[1,0]\n" \
  "v_pk_mul_f32 v[66:67], v[66:67], v[34:35] op_sel_hi:[1,0]\n" \
  "v_pk_mul_f32 v[68:69], v[68:69], v[34:35] op_sel_hi:[1,0]\n" \
  "v_pk_mul_f32 v[70:71], v[70:71], v[34:35] op_sel_hi:[1,0]\n" \
  "v_pk_fma_f32 v[64:65], v[96:97], v[34:35], v[64:65] op_sel:[0,1,0]\n" \
  "v_pk_fma_f32 v[66:67], v[98:99], v[34:35], v[66:67] op_sel:[0,1,0]\n" \
  "v_pk_fma_f32 v[68:69], v[100:101], v[34:35], v[68:69] op_sel:[0,1,0]\n" \
  "v_pk_fma_f32 v[70:71], v[102:103], v[34:35], v[70:71] op_sel:[0,1,0]\n" \
  "s_nop 1\n" \
  "v_mfma_f32_16x16x16_bf16 v[64:67], v[16:17], v[32:33], v[64:67]\n" \
  "v_mfma_f32_16x16x16_bf16 v[68:71], v[20:21], v[32:33], v[68:71]\n" \
  "v_pk_mul_f32 v[72:73], v[72:73], v[34:35] op_sel_hi:[1,0]\n" \
  "v_pk_mul_f32 v[74:75], v[74:75], v[34:35] op_sel_hi:[1,0]\n" \
  "v_pk_mul_f32 v[76:77], v[76:77], v[34:35] op_sel_hi:[1,0]\n" \
  "v_pk_mul_f32 v[78:79], v[78:79], v[34:35] op_sel_hi:[1,0]\n" \
  "v_pk_fma_f32 v[72:73], v[104:105], v[34:35], v[72:73] op_sel:[0,1,0]\n" \
  "v_pk_fma_f32 v[74:75], v[106:107], v[34:35], v[74:75] op_sel:[0,1,0]\n" \
  "v_pk_fma_f32 v[76:77], v[108:109], v[34:35], v[76:77] op_sel:[0,1,0]\n" \
  "v_pk_fma_f32 v[78:79], v[110:111], v[34:35], v[78:79] op_sel:[0,1,0]\n" \
  "s_nop 1\n" \
  "v_mfma_f32_16x16x16_bf16 v[72:75], v[24:25], v[32:33], v[72:75]\n" \
  "v_mfma_f32_16x16x16_bf16 v[76:79], v[28:29], v[32:33], v[76:79]\n" \
  "v_cvt_pk_bf16_f32 v64, v64, v65\n" \
  "v_cvt_pk_bf16_f32 v65, v66, v67\n" \
  "v_cvt_pk_bf16_f32 v66, v68, v69\n" \
  "v_cvt_pk_bf16_f32 v67, v70, v71\n" \
  "buffer_store_dwordx4 v[64:67], v2, s[40:43], s75 offen offset:0 sc1\n" \
  "v_pk_mul_f32 v[80:81], v[80:81], v[38:39] op_sel_hi:[1,0]\n" \
  "v_pk_mul_f32 v[82:83], v[82:83], v[38:39] op_sel_hi:[1,0]\n" \
  "v_pk_mul_f32 v[84:85], v[84:85], v[38:39] op_sel_hi:[1,0]\n" \
  "v_pk_mul_f32 v[86:87], v[86:87], v[38:39] op_sel_hi:[1,0]\n" \
  "v_pk_fma_f32 v[80:81], v[112:113], v[38:39], v[80:81] op_sel:[0,1,0]\n" \
  "v_pk_fma_f32 v[82:83], v[114:115], v[38:39], v[82:83] op_sel:[0,1,0]\n" \
  "v_pk_fma_f32 v[84:85], v[116:117], v[38:39], v[84:85] op_sel:[0,1,0]\n" \
  "v_pk_fma_f32 v[86:87], v[118:119], v[38:39], v[86:87] op_sel:[0,1,0]\n" \
  "s_nop 1\n" \
  "v_mfma_f32_16x16x16_bf16 v[80:83], v[16:17], v[36:37], v[80:83]\n" \
  "v_mfma_f32_16x16x16_bf16 v[84:87], v[20:21], v[36:37], v[84:87]\n" \
  "v_cvt_pk_bf16_f32 v72, v72, v73\n" \
  "v_cvt_pk_bf16_f32 v73, v74, v75\n" \
  "v_cvt_pk_bf16_f32 v74, v76, v77\n" \
  "v_cvt_pk_bf16_f32 v75, v78, v79\n" \
  "buffer_store_dwordx4 v[72:75], v2, s[40:43], s75 offen offset:128 sc1\n" \
  "v_pk_mul_f32 v[88:89], v[88:89], v[38:39] op_sel_hi:[1,0]\n" \
  "v_pk_mul_f32 v[90:91], v[90:91], v[38:39] op_sel_hi:[1,0]\n" \
  "v_pk_mul_f32 v[92:93], v[92:93], v[38:39] op_sel_hi:[1,0]\n" \
  "v_pk_mul_f32 v[94:95], v[94:95], v[38:39] op_sel_hi:[1,0]\n" \
  "v_pk_fma_f32 v[88:89], v[120:121], v[38:39], v[88:89] op_sel:[0,1,0]\n" \
  "v_pk_fma_f32 v[90:91], v[122:123], v[38:39], v[90:91] op_sel:[0,1,0]\n" \
  "v_pk_fma_f32 v[92:93], v[124:125], v[38:39], v[92:93] op_sel:[0,1,0]\n" \
  "v_pk_fma_f32 v[94:95], v[126:127], v[38:39], v[94:95] op_sel:[0,1,0]\n" \
  "s_nop 1\n" \
  "v_mfma_f32_16x16x16_bf16 v[88:91], v[24:25], v[36:37], v[88:91]\n" \
  "v_mfma_f32_16x16x16_bf16 v[92:95], v[28:29], v[36:37], v[92:95]\n" \
  "v_cvt_pk_bf16_f32 v80, v80, v81\n" \
  "v_cvt_pk_bf16_f32 v81, v82, v83\n" \
  "v_cvt_pk_bf16_f32 v82, v84, v85\n" \
  "v_cvt_pk_bf16_f32 v83, v86, v87\n" \
  "buffer_store_dwordx4 v[80:83], v2, s[40:43], s76 offen offset:0 sc1\n" \
  "s_nop 2\n" \
  "v_cvt_pk_bf16_f32 v88, v88, v89\n" \
  "v_cvt_pk_bf16_f32 v89, v90, v91\n" \
  "v_cvt_pk_bf16_f32 v90, v92, v93\n" \
  "v_cvt_pk_bf16_f32 v91, v94, v95\n" \
  "buffer_store_dwordx4 v[88:91], v2, s[40:43], s76 offen offset:128 sc1\n" \
  "s_waitcnt vmcnt(4)\n" \
  "v_mov_b32 v12, s65\n" \
  "v_mov_b32 v13, s67\n" \
  "s_mov_b64 exec, 1\n" \
  "ds_add_u32 v12, v13\n" \
  "s_mov_b64 exec, -1\n" \
  ".LWB_NEXT_%=:\n" \
  "s_setprio 0\n" \
  "s_mov_b32 s57, s58\n" \
  "s_cmp_eq_u32 s67, 0\n" \
  "s_cbranch_scc1 .LWB_MISS_%=\n" \
  "s_branch .LWB_TOP_%=\n" \
  ".LWB_MISS_%=:\n" \
  "s_cmp_eq_u32 s57, 128\n" \
  "s_cbranch_scc1 .LWB_END_%=\n" \
  ".LWB_LATE_%=:\n" \
  "s_mov_b32 s77, 0x40000\n" \
  ".LWB_POLL_%=:\n" \
  "s_lshr_b32 s68, s57, 6\n" \
  "s_add_u32 s68, s68, s57\n" \
  "s_lshl_b32 s68, s68, 12\n" \
  "buffer_load_dword v5, off, s[44:47], s68 sc1\n" \
  "s_sub_u32 s77, s77, 1\n" \
  "s_cbranch_scc1 .LWB_SEEN_%=\n" \
  "s_waitcnt vmcnt(0)\n" \
  "v_cmp_eq_u32 vcc, 0, v5\n" \
  "s_cmp_eq_u64 vcc, 0\n" \
  "s_cbranch_scc1 .LWB_SEEN_%=\n" \
  "s_sleep %[sa]\n" \
  "s_branch .LWB_POLL_%=\n" \
  ".LWB_SEEN_%=:\n" \
  "s_mov_b32 m0, s61\n" \
  "s_lshl_b32 s68, s57, 19\n" \
  "s_add_u32 s68, s68, s48\n" \
  "s_addc_u32 s69, s49, 0\n" \
  "global_load_lds_dwordx4 v0, s[68:69] offset:0 sc1\n" \
  "global_load_lds_dwordx4 v0, s[68:69] offset:1024 sc1\n" \
  "global_load_lds_dwordx4 v0, s[68:69] offset:2048 sc1\n" \
  "global_load_lds_dwordx4 v0, s[68:69] offset:3072 sc1\n" \
  "s_add_u32 m0, s61, 4096\n" \
  "s_add_u32 s70, s68, 4096\n" \
  "s_addc_u32 s71, s69, 0\n" \
  "global_load_lds_dwordx4 v0, s[70:71] offset:0 sc1\n" \
  "global_load_lds_dwordx4 v0, s[70:71] offset:1024 sc1\n" \
  "global_load_lds_dwordx4 v0, s[70:71] offset:2048 sc1\n" \
  "global_load_lds_dwordx4 v0, s[70:71] offset:3072 sc1\n" \
  "s_mov_b32 m0, s62\n" \
  "s_lshl_b32 s72, s57, 18\n" \
  "s_add_u32 s72, s72, s50\n" \
  "s_addc_u32 s73, s51, 0\n" \
  "global_load_lds_dwordx4 v0, s[72:73] offset:0 sc1\n" \
  "global_load_lds_dwordx4 v0, s[72:73] offset:1024 sc1\n" \
  "global_load_lds_dwordx4 v0, s[72:73] offset:2048 sc1\n" \
  "global_load_lds_dwordx4 v0, s[72:73] offset:3072 sc1\n" \
  "s_mov_b32 s67, 1\n" \
  "s_waitcnt vmcnt(0)\n" \
  "v_mov_b32 v12, s65\n" \
  "v_mov_b32 v13, s67\n" \
  "s_mov_b64 exec, 1\n" \
  "ds_add_u32 v12, v13\n" \
  "s_mov_b64 exec, -1\n" \
  "s_branch .LWB_TOP_%=\n" \
  ".LWB_END_%=:\n" \
  "s_endpgm\n" \
  ".LWB_AGAINE_%=:\n" \
  "s_sleep %[sm]\n" \
  ".LWB_ASKE_%=:\n" \
  "s_mov_b32 m0, s63\n" \
  "s_lshl_b32 s72, s57, 16\n" \
  "s_add_u32 s72, s72, s52\n" \
  "s_addc_u32 s73, s53, 0\n" \
  "global_load_lds_dwordx4 v0, s[72:73] offset:0 sc1\n" \
  "global_load_lds_dwordx4 v0, s[72:73] offset:1024 sc1\n" \
  "s_add_u32 s77, s77, 1\n" \
  ".LWB_ASKEDE_%=:\n" \
  "s_waitcnt vmcnt(0)\n" \
  "ds_read_b128 v[8:11], v3 offset:0\n" \
  "ds_read_b128 v[12:15], v3 offset:1024\n" \
  "s_cmp_gt_u32 s77, 0x40000\n" \
  "s_cbranch_scc1 .LWB_OKE_%=\n" \
  "s_waitcnt lgkmcnt(1)\n" \
  "v_min3_u32 v8, v8, v9, v10\n" \
  "s_waitcnt lgkmcnt(0)\n" \
  "v_min3_u32 v12, v12, v13, v14\n" \
  "v_min3_u32 v8, v8, v11, v15\n" \
  "v_min_u32 v8, v8, v12\n" \
  ".LWB_TESTE_%=:\n" \
  "v_cmp_eq_u32 vcc, 0, v8\n" \
  "s_cmp_eq_u64 vcc, 0\n" \
  "s_cbranch_scc0 .LWB_AGAINE_%=\n" \
  ".LWB_OKE_%=:\n" \
  "s_waitcnt lgkmcnt(0)\n" \
  "s_barrier\n" \
  "v_cmp_eq_u32 vcc, 0, v5\n" \
  "s_cmp_eq_u64 vcc, 0\n" \
  "s_cbranch_scc0 .LWB_NOE_%=\n" \
  "s_mov_b32 m0, s61\n" \
  "s_lshl_b32 s68, s58, 19\n" \
  "s_add_u32 s68, s68, s48\n" \
  "s_addc_u32 s69, s49, 0\n" \
  "global_load_lds_dwordx4 v0, s[68:69] offset:0 sc1\n" \
  "global_load_lds_dwordx4 v0, s[68:69] offset:1024 sc1\n" \
  "global_load_lds_dwordx4 v0, s[68:69] offset:2048 sc1\n" \
  "global_load_lds_dwordx4 v0, s[68:69] offset:3072 sc1\n" \
  "s_add_u32 m0, s61, 4096\n" \
  "s_add_u32 s70, s68, 4096\n" \
  "s_addc_u32 s71, s69, 0\n" \
  "global_load_lds_dwordx4 v0, s[70:71] offset:0 sc1\n" \
  "global_load_lds_dwordx4 v0, s[70:71] offset:1024 sc1\n" \
  "global_load_lds_dwordx4 v0, s[70:71] offset:2048 sc1\n" \
  "global_load_lds_dwordx4 v0, s[70:71] offset:3072 sc1\n" \
  "s_mov_b32 m0, s62\n" \
  "s_lshl_b32 s72, s58, 18\n" \
  "s_add_u32 s72, s72, s50\n" \
  "s_addc_u32 s73, s51, 0\n" \
  "global_load_lds_dwordx4 v0, s[72:73] offset:0 sc1\n" \
  "global_load_lds_dwordx4 v0, s[72:73] offset:1024 sc1\n" \
  "global_load_lds_dwordx4 v0, s[72:73] offset:2048 sc1\n" \
  "global_load_lds_dwordx4 v0, s[72:73] offset:3072 sc1\n" \
  "s_mov_b32 s67, 1\n" \
  ".LWB_NOE_%=:\n" \
  "s_lshl_b32 s70, s67, 7\n" \
  "s_or_b32 s70, s70, s58\n" \
  "s_lshr_b32 s68, s70, 6\n" \
  "s_add_u32 s68, s68, s70\n" \
  "s_lshl_b32 s68, s68, 12\n" \
  "buffer_load_dword v5, off, s[44:47], s68 sc1\n" \
  "s_mov_b32 s77, 0\n" \
  "s_barrier\n" \
  "s_setprio 0\n" \
  "s_waitcnt vmcnt(0)\n" \
  "v_mov_b32 v12, s65\n" \
  "v_mov_b32 v13, s67\n" \
  "s_mov_b64 exec, 1\n" \
  "ds_add_u32 v12, v13\n" \
  "s_mov_b64 exec, -1\n" \
  "v_cmp_eq_u32 vcc, 0, v5\n" \
  "s_cmp_eq_u64 vcc, 0\n" \
  "s_cbranch_scc0 .LWB_NOF_%=\n" \
  "s_mov_b32 m0, s61\n" \
  "s_lshl_b32 s68, s58, 19\n" \
  "s_add_u32 s68, s68, s48\n" \
  "s_addc_u32 s69, s49, 0\n" \
  "global_load_lds_dwordx4 v0, s[68:69] offset:0 sc1\n" \
  "global_load_lds_dwordx4 v0, s[68:69] offset:1024 sc1\n" \
  "global_load_lds_dwordx4 v0, s[68:69] offset:2048 sc1\n" \
  "global_load_lds_dwordx4 v0, s[68:69] offset:3072 sc1\n" \
  "s_add_u32 m0, s61, 4096\n" \
  "s_add_u32 s70, s68, 4096\n" \
  "s_addc_u32 s71, s69, 0\n" \
  "global_load_lds_dwordx4 v0, s[70:71] offset:0 sc1\n" \
  "global_load_lds_dwordx4 v0, s[70:71] offset:1024 sc1\n" \
  "global_load_lds_dwordx4 v0, s[70:71] offset:2048 sc1\n" \
  "global_load_lds_dwordx4 v0, s[70:71] offset:3072 sc1\n" \
  "s_mov_b32 m0, s62\n" \
  "s_lshl_b32 s72, s58, 18\n" \
  "s_add_u32 s72, s72, s50\n" \
  "s_addc_u32 s73, s51, 0\n" \
  "global_load_lds_dwordx4 v0, s[72:73] offset:0 sc1\n" \
  "global_load_lds_dwordx4 v0, s[72:73] offset:1024 sc1\n" \
  "global_load_lds_dwordx4 v0, s[72:73] offset:2048 sc1\n" \
  "global_load_lds_dwordx4 v0, s[72:73] offset:3072 sc1\n" \
  "s_mov_b32 s67, 1\n" \
  "s_mov_b32 s77, 1\n" \
  ".LWB_NOF_%=:\n" \
  "ds_read_b128 v[48:51], v1 offset:32768\n" \
  "ds_read_b128 v[52:55], v1 offset:33792\n" \
  "ds_read_b128 v[56:59], v1 offset:34816\n" \
  "s_waitcnt lgkmcnt(2)\n" \
  "v_mfma_f32_16x16x32_bf16 v[64:67], v[128:131], v[48:51], 0\n" \
  "ds_read_b128 v[60:63], v1 offset:35840\n" \
  "s_lshl_b32 s75, s57, 19\n" \
  "v_mfma_f32_16x16x32_bf16 v[68:71], v[160:163], v[48:51], 0\n" \
  "s_add_u32 s75, s75, s60\n" \
  "v_mfma_f32_16x16x32_bf16 v[72:75], v[192:195], v[48:51], 0\n" \
  "s_add_u32 s76, s75, 16384\n" \
  "v_mfma_f32_16x16x32_bf16 v[76:79], v[224:227], v[48:51], 0\n" \
  "s_lshl_b32 s70, s67, 7\n" \
  "s_or_b32 s70, s70, s58\n" \
  "s_lshr_b32 s68, s70, 6\n" \
  "s_add_u32 s68, s68, s70\n" \
  "s_lshl_b32 s68, s68, 12\n" \
  "buffer_load_dword v5, off, s[44:47], s68 sc1\n" \
  "s_waitcnt lgkmcnt(2)\n" \
  "v_mfma_f32_16x16x32_bf16 v[64:67], v[132:135], v[52:55], v[64:67]\n" \
  "ds_read_b128 v[48:51], v1 offset:40960\n" \
  "v_mfma_f32_16x16x32_bf16 v[68:71], v[164:167], v[52:55], v[68:71]\n" \
  "v_mfma_f32_16x16x32_bf16 v[72:75], v[196:199], v[52:55], v[72:75]\n" \
  "v_mfma_f32_16x16x32_bf16 v[76:79], v[228:231], v[52:55], v[76:79]\n" \
  "s_waitcnt lgkmcnt(2)\n" \
  "v_mfma_f32_16x16x32_bf16 v[64:67], v[136:139], v[56:59], v[64:67]\n" \
  "ds_read_b128 v[52:55], v1 offset:41984\n" \
  "v_mfma_f32_16x16x32_bf16 v[68:71], v[168:171], v[56:59], v[68:71]\n" \
  "v_mfma_f32_16x16x32_bf16 v[72:75], v[200:203], v[56:59], v[72:75]\n" \
  "v_mfma_f32_16x16x32_bf16 v[76:79], v[232:235], v[56:59], v[76:79]\n" \
  "s_waitcnt lgkmcnt(2)\n" \
  "v_mfma_f32_16x16x32_bf16 v[64:67], v[140:143], v[60:63], v[64:67]\n" \
  "ds_read_b128 v[56:59], v1 offset:43008\n" \
  "v_mfma_f32_16x16x32_bf16 v[68:71], v[172:175], v[60:63], v[68:71]\n" \
  "v_mfma_f32_16x16x32_bf16 v[72:75], v[204:207], v[60:63], v[72:75]\n" \
  "s_cmp_lg_u32 s67, 0\n" \
  "s_cbranch_scc1 .LWB_NOG_%=\n" \
  "s_waitcnt vmcnt(0)\n" \
  "v_cmp_eq_u32 vcc, 0, v5\n" \
  "s_cmp_eq_u64 vcc, 0\n" \
  "s_cbranch_scc0 .LWB_NOG_%=\n" \
  "s_mov_b32 m0, s61\n" \
  "s_lshl_b32 s68, s58, 19\n" \
  "s_add_u32 s68, s68, s48\n" \
  "s_addc_u32 s69, s49, 0\n" \
  "global_load_lds_dwordx4 v0, s[68:69] offset:0 sc1\n" \
  "global_load_lds_dwordx4 v0, s[68:69] offset:1024 sc1\n" \
  "global_load_lds_dwordx4 v0, s[68:69] offset:2048 sc1\n" \
  "global_load_lds_dwordx4 v0, s[68:69] offset:3072 sc1\n" \
  "s_add_u32 m0, s61, 4096\n" \
  "s_add_u32 s70, s68, 4096\n" \
  "s_addc_u32 s71, s69, 0\n" \
  "global_load_lds_dwordx4 v0, s[70:71] offset:0 sc1\n" \
  "global_load_lds_dwordx4 v0, s[70:71] offset:1024 sc1\n" \
  "global_load_lds_dwordx4 v0, s[70:71] offset:2048 sc1\n" \
  "global_load_lds_dwordx4 v0, s[70:71] offset:3072 sc1\n" \
  "s_mov_b32 m0, s62\n" \
  "s_lshl_b32 s72, s58, 18\n" \
  "s_add_u32 s72, s72, s50\n" \
  "s_addc_u32 s73, s51, 0\n" \
  "global_load_lds_dwordx4 v0, s[72:73] offset:0 sc1\n" \
  "global_load_lds_dwordx4 v0, s[72:73] offset:1024 sc1\n" \
  "global_load_lds_dwordx4 v0, s[72:73] offset:2048 sc1\n" \
  "global_load_lds_dwordx4 v0, s[72:73] offset:3072 sc1\n" \
  "s_mov_b32 s67, 1\n" \
  "s_mov_b32 s77, 1\n" \
  ".LWB_NOG_%=:\n" \
  "v_mfma_f32_16x16x32_bf16 v[76:79], v[236:239], v[60:63], v[76:79]\n" \
  "s_waitcnt lgkmcnt(2)\n" \
  "v_mfma_f32_16x16x32_bf16 v[64:67], v[144:147], v[48:51], v[64:67]\n" \
  "ds_read_b128 v[60:63], v1 offset:44032\n" \
  "v_mfma_f32_16x16x32_bf16 v[68:71], v[176:179], v[48:51], v[68:71]\n" \
  "v_mfma_f32_16x16x32_bf16 v[72:75], v[208:211], v[48:51], v[72:75]\n" \
  "v_mfma_f32_16x16x32_bf16 v[76:79], v[240:243], v[48:51], v[76:79]\n" \
  "s_waitcnt lgkmcnt(2)\n" \
  "v_mfma_f32_16x16x32_bf16 v[64:67], v[148:151], v[52:55], v[64:67]\n" \
  "ds_read_b128 v[48:51], v1 offset:36864\n" \
  "v_mfma_f32_16x16x32_bf16 v[68:71], v[180:183], v[52:55], v[68:71]\n" \
  "v_mfma_f32_16x16x32_bf16 v[72:75], v[212:215], v[52:55], v[72:75]\n" \
  "v_mfma_f32_16x16x32_bf16 v[76:79], v[244:247], v[52:55], v[76:79]\n" \
  "s_waitcnt lgkmcnt(2)\n" \
  "v_mfma_f32_16x16x32_bf16 v[64:67], v[152:155], v[56:59], v[64:67]\n" \
  "ds_read_b128 v[52:55], v1 offset:37888\n" \
  "v_mfma_f32_16x16x32_bf16 v[68:71], v[184:187], v[56:59], v[68:71]\n" \
  "v_mfma_f32_16x16x32_bf16 v[72:75], v[216:219], v[56:59], v[72:75]\n" \
  "v_mfma_f32_16x16x32_bf16 v[76:79], v[248:251], v[56:59], v[76:79]\n" \
  "s_waitcnt lgkmcnt(2)\n" \
  "v_mfma_f32_16x16x32_bf16 v[64:67], v[156:159], v[60:63], v[64:67]\n" \
  "ds_read_b128 v[56:59], v1 offset:38912\n" \
  "v_mfma_f32_16x16x32_bf16 v[68:71], v[188:191], v[60:63], v[68:71]\n" \
  "v_mfma_f32_16x16x32_bf16 v[72:75], v[220:223], v[60:63], v[72:75]\n" \
  "v_mfma_f32_16x16x32_bf16 v[76:79], v[252:255], v[60:63], v[76:79]\n" \
  "s_waitcnt lgkmcnt(2)\n" \
  "v_mfma_f32_16x16x32_bf16 v[80:83], v[128:131], v[48:51], 0\n" \
  "ds_read_b128 v[60:63], v1 offset:39936\n" \
  "v_mfma_f32_16x16x32_bf16 v[84:87], v[160:163], v[48:51], 0\n" \
  "v_mfma_f32_16x16x32_bf16 v[88:91], v[192:195], v[48:51], 0\n" \
  "s_waitcnt vmcnt(0)\n" \
  "v_mov_b32 v12, s65\n" \
  "v_mov_b32 v13, s77\n" \
  "s_mov_b64 exec, 1\n" \
  "ds_add_u32 v12, v13\n" \
  "s_mov_b64 exec, -1\n" \
  "v_mfma_f32_16x16x32_bf16 v[92:95], v[224:227], v[48:51], 0\n" \
  "s_lshl_b32 s70, s67, 7\n" \
  "s_or_b32 s70, s70, s58\n" \
  "s_lshr_b32 s68, s70, 6\n" \
  "s_add_u32 s68, s68, s70\n" \
  "s_lshl_b32 s68, s68, 12\n" \
  "buffer_load_dword v5, off, s[44:47], s68 sc1\n" \
  "s_waitcnt lgkmcnt(3)\n" \
  "v_mfma_f32_16x16x32_bf16 v[80:83], v[132:135], v[52:55], v[80:83]\n" \
  "ds_read_b128 v[48:51], v1 offset:45056\n" \
  "v_mfma_f32_16x16x32_bf16 v[84:87], v[164:167], v[52:55], v[84:87]\n" \
  "v_cvt_pk_bf16_f32 v64, v64, v65\n" \
  "v_mfma_f32_16x16x32_bf16 v[88:91], v[196:199], v[52:55], v[88:91]\n" \
  "v_cvt_pk_bf16_f32 v65, v66, v67\n" \
  "v_mfma_f32_16x16x32_bf16 v[92:95], v[228:231], v[52:55], v[92:95]\n" \
  "s_waitcnt lgkmcnt(3)\n" \
  "v_mfma_f32_16x16x32_bf16 v[80:83], v[136:139], v[56:59], v[80:83]\n" \
  "ds_read_b128 v[52:55], v1 offset:46080\n" \
  "v_mfma_f32_16x16x32_bf16 v[84:87], v[168:171], v[56:59], v[84:87]\n" \
  "v_cvt_pk_bf16_f32 v66, v68, v69\n" \
  "v_mfma_f32_16x16x32_bf16 v[88:91], v[200:203], v[56:59], v[88:91]\n" \
  "v_cvt_pk_bf16_f32 v67, v70, v71\n" \
  "v_mfma_f32_16x16x32_bf16 v[92:95], v[232:235], v[56:59], v[92:95]\n" \
  "s_waitcnt lgkmcnt(3)\n" \
  "v_mfma_f32_16x16x32_bf16 v[80:83], v[140:143], v[60:63], v[80:83]\n" \
  "ds_read_b128 v[56:59], v1 offset:47104\n" \
  "v_mfma_f32_16x16x32_bf16 v[84:87], v[172:175], v[60:63], v[84:87]\n" \
  "buffer_store_dwordx4 v[64:67], v2, s[40:43], s75 offen offset:0 sc1\n" \
  "v_mfma_f32_16x16x32_bf16 v[88:91], v[204:207], v[60:63], v[88:91]\n" \
  "v_cvt_pk_bf16_f32 v72, v72, v73\n" \
  "v_mfma_f32_16x16x32_bf16 v[92:95], v[236:239], v[60:63], v[92:95]\n" \
  "s_waitcnt lgkmcnt(2)\n" \
  "v_mfma_f32_16x16x32_bf16 v[80:83], v[144:147], v[48:51], v[80:83]\n" \
  "ds_read_b128 v[60:63], v1 offset:48128\n" \
  "v_mfma_f32_16x16x32_bf16 v[84:87], v[176:179], v[48:51], v[84:87]\n" \
  "v_cvt_pk_bf16_f32 v73, v74, v75\n" \
  "v_mfma_f32_16x16x32_bf16 v[88:91], v[208:211], v[48:51], v[88:91]\n" \
  "v_cvt_pk_bf16_f32 v74, v76, v77\n" \
  "v_mfma_f32_16x16x32_bf16 v[92:95], v[240:243], v[48:51], v[92:95]\n" \
  "s_waitcnt lgkmcnt(2)\n" \
  "v_mfma_f32_16x16x32_bf16 v[80:83], v[148:151], v[52:55], v[80:83]\n" \
  "v_mfma_f32_16x16x32_bf16 v[84:87], v[180:183], v[52:55], v[84:87]\n" \
  "v_cvt_pk_bf16_f32 v75, v78, v79\n" \
  "v_mfma_f32_16x16x32_bf16 v[88:91], v[212:215], v[52:55], v[88:91]\n" \
  "buffer_store_dwordx4 v[72:75], v2, s[40:43], s75 offen offset:128 sc1\n" \
  "v_mfma_f32_16x16x32_bf16 v[92:95], v[244:247], v[52:55], v[92:95]\n" \
  "s_waitcnt lgkmcnt(1)\n" \
  "v_mfma_f32_16x16x32_bf16 v[80:83], v[152:155], v[56:59], v[80:83]\n" \
  "v_mfma_f32_16x16x32_bf16 v[84:87], v[184:187], v[56:59], v[84:87]\n" \
  "v_mfma_f32_16x16x32_bf16 v[88:91], v[216:219], v[56:59], v[88:91]\n" \
  "v_mfma_f32_16x16x32_bf16 v[92:95], v[248:251], v[56:59], v[92:95]\n" \
  "s_waitcnt lgkmcnt(0)\n" \
  "v_mfma_f32_16x16x32_bf16 v[80:83], v[156:159], v[60:63], v[80:83]\n" \
  "v_mfma_f32_16x16x32_bf16 v[84:87], v[188:191], v[60:63], v[84:87]\n" \
  "v_mfma_f32_16x16x32_bf16 v[88:91], v[220:223], v[60:63], v[88:91]\n" \
  "v_mfma_f32_16x16x32_bf16 v[92:95], v[252:255], v[60:63], v[92:95]\n" \
  "s_nop 4\n" \
  "v_cvt_pk_bf16_f32 v80, v80, v81\n" \
  "v_cvt_pk_bf16_f32 v81, v82, v83\n" \
  "v_cvt_pk_bf16_f32 v82, v84, v85\n" \
  "v_cvt_pk_bf16_f32 v83, v86, v87\n" \
  "buffer_store_dwordx4 v[80:83], v2, s[40:43], s76 offen offset:0 sc1\n" \
  "v_cvt_pk_bf16_f32 v88, v88, v89\n" \
  "v_cvt_pk_bf16_f32 v89, v90, v91\n" \
  "v_cvt_pk_bf16_f32 v90, v92, v93\n" \
  "v_cvt_pk_bf16_f32 v91, v94, v95\n" \
  "buffer_store_dwordx4 v[88:91], v2, s[40:43], s76 offen offset:128 sc1\n" \
  "s_mov_b32 s79, s57\n" \
  "s_waitcnt vmcnt(4)\n" \
  "v_cmp_eq_u32 vcc, 0, v5\n" \
  "s_cmp_eq_u64 vcc, 0\n" \
  "s_cbranch_scc0 .LWB_NEXT_%=\n" \
  "s_mov_b32 s57, s58\n" \
  "s_setprio 0\n" \
  "s_branch .LWB_SEEN_%=\n"
#define CONV_ASM \
  "v_mov_b64 v[64:65], 0\n" \
  "v_mov_b64 v[66:67], 0\n" \
  "v_mov_b64 v[68:69], 0\n" \
  "v_mov_b64 v[70:71], 0\n" \
  "v_mov_b64 v[72:73], 0\n" \
  "v_mov_b64 v[74:75], 0\n" \
  "v_mov_b64 v[76:77], 0\n" \
  "v_mov_b64 v[78:79], 0\n" \
  "v_mov_b64 v[80:81], 0\n" \
  "v_mov_b64 v[82:83], 0\n" \
  "v_mov_b64 v[84:85], 0\n" \
  "v_mov_b64 v[86:87], 0\n" \
  "v_mov_b64 v[88:89], 0\n" \
  "v_mov_b64 v[90:91], 0\n" \
  "v_mov_b64 v[92:93], 0\n" \
  "v_mov_b64 v[94:95], 0\n" \
  "v_mov_b64 v[96:97], 0\n" \
  "v_mov_b64 v[98:99], 0\n" \
  "v_mov_b64 v[100:101], 0\n" \
  "v_mov_b64 v[102:103], 0\n" \
  "v_mov_b64 v[104:105], 0\n" \
  "v_mov_b64 v[106:107], 0\n" \
  "v_mov_b64 v[108:109], 0\n" \
  "v_mov_b64 v[110:111], 0\n" \
  "v_mov_b64 v[112:113], 0\n" \
  "v_mov_b64 v[114:115], 0\n" \
  "v_mov_b64 v[116:117], 0\n" \
  "v_mov_b64 v[118:119], 0\n" \
  "v_mov_b64 v[120:121], 0\n" \
  "v_mov_b64 v[122:123], 0\n" \
  "v_mov_b64 v[124:125], 0\n" \
  "v_mov_b64 v[126:127], 0\n" \
  "v_mov_b32 v20, 0\n" \
  "v_mov_b32 v41, 0\n" \
  "v_mov_b32 v254, 0\n" \
  "s_branch .LVA_TAPS_%=\n" \
  "s_branch .LVA_AL_%=\n" \
  ".p2alignl 12, 0xbf800000, 3700\n" \
  ".p2align 6\n" \
  ".Lpad0xb0:\n" \
  "s_setpc_b64 s[98:99]\n" \
  ".p2align 6\n" \
  ".LVA_AL_%=:\n" \
  ".LVA_TOP_%=:\n" \
  "s_cmp_lg_u32 s63, 1\n" \
  "s_cbranch_scc1 .LVA_BAR_%=\n" \
  "s_waitcnt vmcnt(0)\n" \
  "v_min3_u32 v56, v42, v43, v41\n" \
  "v_readfirstlane_b32 s78, v254\n" \
  "v_cmp_eq_u32 vcc, 0, v56\n" \
  "s_cbranch_vccz .LVA_SEEN_%=\n" \
  "s_mov_b32 s79, 0\n" \
  "s_cmp_lg_u32 s78, 0\n" \
  "s_cbranch_scc1 .LVA_KNOWN_%=\n" \
  ".LVA_GATE_%=:\n" \
  "s_memrealtime s[74:75]\n" \
  "s_waitcnt lgkmcnt(0)\n" \
  "s_sub_i32 s74, s69, s74\n" \
  "s_cmp_gt_u32 s74, %[gv]\n" \
  "s_cbranch_scc1 .LVA_WHEN_%=\n" \
  "s_sleep 2\n" \
  "s_branch .LVA_GATE_%=\n" \
  ".LVA_NONE_%=:\n" \
  "s_sleep %[sv]\n" \
  ".LVA_WHEN_%=:\n" \
  "buffer_load_dword v254, off, s[44:47], s71 sc1\n" \
  "s_add_u32 s79, s79, 1\n" \
  "s_cmp_gt_u32 s79, 0x40000\n" \
  "s_cbranch_scc1 .LVA_KNOWN_%=\n" \
  "s_waitcnt vmcnt(0)\n" \
  "v_readfirstlane_b32 s78, v254\n" \
  "s_cmp_eq_u32 s78, 0\n" \
  "s_cbranch_scc1 .LVA_NONE_%=\n" \
  ".LVA_KNOWN_%=:\n" \
  "s_memrealtime s[74:75]\n" \
  "s_waitcnt lgkmcnt(0)\n" \
  "s_sub_i32 s74, s78, s74\n" \
  "s_cmp_lt_i32 s74, %[lt]\n" \
  "s_cselect_b32 s70, 0, 0x200000\n" \
  "s_branch .LVA_DUE_%=\n" \
  ".LVA_SOON_%=:\n" \
  "s_cmp_lt_u32 s74, 8\n" \
  "s_cbranch_scc1 .LVA_NOW_%=\n" \
  "s_sleep 1\n" \
  ".LVA_NOW_%=:\n" \
  "s_memrealtime s[74:75]\n" \
  "s_waitcnt lgkmcnt(0)\n" \
  "s_sub_i32 s74, s78, s74\n" \
  ".LVA_DUE_%=:\n" \
  "s_cmp_lt_u32 s74, 1000\n" \
  "s_cbranch_scc1 .LVA_SOON_%=\n" \
  "s_branch .LVA_ASK_%=\n" \
  ".LVA_AGAIN_%=:\n" \
  "s_sleep %[sv]\n" \
  ".LVA_ASK_%=:\n" \
  "buffer_load_dwordx2 v[42:43], v37, s[40:43], s65 offen sc1\n" \
  "buffer_load_dword v41, v38, s[44:47], s66 offen sc1\n" \
  "s_add_u32 s79, s79, 1\n" \
  "s_cmp_gt_u32 s79, 0x40000\n" \
  "s_cbranch_scc1 .LVA_SEEN_%=\n" \
  "s_waitcnt vmcnt(0)\n" \
  "v_min3_u32 v56, v42, v43, v41\n" \
  "v_cmp_eq_u32 vcc, 0, v56\n" \
  "s_cbranch_vccnz .LVA_AGAIN_%=\n" \
  ".LVA_SEEN_%=:\n" \
  "v_and_b32 v62, 0xffff0000, v41\n" \
  "v_lshlrev_b32 v60, 16, v41\n" \
  "v_lshlrev_b32 v56, 16, v42\n" \
  "v_and_b32 v57, 0xffff0000, v42\n" \
  "v_lshlrev_b32 v58, 16, v43\n" \
  "v_and_b32 v59, 0xffff0000, v43\n" \
  "v_rcp_f32 v62, v62\n" \
  "v_pk_add_f32 v[56:57], v[56:57], v[60:61] op_sel_hi:[1,0] neg_lo:[0,1] neg_hi:[0,1]\n" \
  "v_pk_add_f32 v[58:59], v[58:59], v[60:61] op_sel_hi:[1,0] neg_lo:[0,1] neg_hi:[0,1]\n" \
  "v_cvt_pk_bf16_f32 v57, v56, v57\n" \
  "v_cvt_pk_bf16_f32 v59, v58, v59\n" \
  "v_lshlrev_b32 v56, 16, v57\n" \
  "v_and_b32 v57, 0xffff0000, v57\n" \
  "v_lshlrev_b32 v58, 16, v59\n" \
  "v_and_b32 v59, 0xffff0000, v59\n" \
  "v_pk_mul_f32 v[56:57], v[56:57], v[62:63] op_sel_hi:[1,0]\n" \
  "v_pk_mul_f32 v[58:59], v[58:59], v[62:63] op_sel_hi:[1,0]\n" \
  "v_cvt_pk_bf16_f32 v56, v56, v57\n" \
  "v_cvt_pk_bf16_f32 v58, v58, v59\n" \
  "ds_write_b16 v39, v56\n" \
  "ds_write_b16_d16_hi v39, v56 offset:4288\n" \
  "ds_write_b16 v39, v58 offset:8576\n" \
  "ds_write_b16_d16_hi v39, v58 offset:12864\n" \
  "ds_write_b64 v40, v[42:43] offset:512\n" \
  ".LVA_BAR_%=:\n" \
  "s_waitcnt lgkmcnt(0)\n" \
  "s_barrier\n" \
  "ds_read_b128 v[56:59], v0 offset:0\n" \
  "ds_read_u16 v60, v2 offset:0\n" \
  "ds_read_u16 v61, v3 offset:512\n" \
  "ds_read2_b32 v[200:201], v4 offset0:0 offset1:16\n" \
  "ds_read2_b32 v[202:203], v4 offset0:1 offset1:17\n" \
  "ds_read2_b32 v[204:205], v4 offset0:2 offset1:18\n" \
  "ds_read2_b32 v[206:207], v4 offset0:3 offset1:19\n" \
  "ds_read2_b32 v[208:209], v4 offset0:4 offset1:20\n" \
  "s_setprio 3\n" \
  "s_waitcnt lgkmcnt(7)\n" \
  "v_mfma_f32_16x16x32_bf16 v[20:23], v[56:59], v[12:15], v[20:23]\n" \
  "v_mfma_f32_16x16x32_bf16 v[20:23], v[56:59], v[16:19], v[20:23]\n" \
  "s_waitcnt lgkmcnt(5)\n" \
  "v_lshlrev_b32 v60, 16, v60\n" \
  "v_lshlrev_b32 v61, 16, v61\n" \
  "v_mul_f32 v60, s64, v60\n" \
  "v_add_u32 v0, v1, v0\n" \
  "v_add_u32 v2, 64, v2\n" \
  "v_add_u32 v4, 64, v4\n" \
  "s_nop 0\n" \
  "v_cvt_pk_bf16_f32 v62, v20, v60\n" \
  "v_lshlrev_b32 v63, 16, v62\n" \
  "v_and_b32 v62, 0xffff0000, v62\n" \
  "v_add_f32 v62, v62, v63\n" \
  "v_cvt_pk_bf16_f32 v62, v62, v62\n" \
  "v_lshlrev_b32 v62, 16, v62\n" \
  "v_add_f32 v62, v62, v61\n" \
  "v_cvt_pk_bf16_f32 v62, v62, v62\n" \
  "s_mov_b64 exec, s[72:73]\n" \
  "ds_write_b16 v3, v62\n" \
  "s_mov_b64 exec, -1\n" \
  "s_cmp_gt_u32 s63, 1\n" \
  "s_waitcnt lgkmcnt(0)\n" \
  "s_barrier\n" \
  "s_cbranch_scc1 .LVA_REST_%=\n" \
  "s_cmp_eq_u32 s63, 1\n" \
  "s_cbranch_scc1 .LVA_ONE_%=\n" \
  "ds_read_b128 v[56:59], v40 offset:0\n" \
  "s_mov_b32 exec_hi, 0\n" \
  "s_waitcnt lgkmcnt(0)\n" \
  "buffer_store_dwordx4 v[56:59], v7, s[48:51], s67 offen sc1\n" \
  "s_mov_b32 exec_hi, -1\n" \
  "s_add_u32 s67, s67, 0x8000\n" \
  "s_branch .LVA_GO_%=\n" \
  ".LVA_ONE_%=:\n" \
  "s_add_u32 s65, s65, 0x8000\n" \
  "s_add_u32 s66, s66, 2048\n" \
  "s_add_u32 s71, s71, 4\n" \
  "s_max_u32 s76, s65, s70\n" \
  "s_max_u32 s78, s66, s70\n" \
  "buffer_load_dword v254, off, s[44:47], s71 sc1\n" \
  "buffer_load_dwordx2 v[42:43], v37, s[40:43], s76 offen sc1\n" \
  "buffer_load_dword v41, v38, s[44:47], s78 offen sc1\n" \
  "s_memrealtime s[74:75]\n" \
  "v_add_u32 v39, 64, v39\n" \
  "s_waitcnt lgkmcnt(0)\n" \
  "s_add_u32 s69, s74, %[gv]\n" \
  "s_branch .LVA_GO_%=\n" \
  ".LVA_REST_%=:\n" \
  "s_setprio 0\n" \
  ".LVA_GO_%=:\n" \
  "v_alignbit_b32 v246, v202, v200, v6\n" \
  "v_alignbit_b32 v247, v204, v202, v6\n" \
  "v_alignbit_b32 v248, v206, v204, v6\n" \
  "v_alignbit_b32 v249, v208, v206, v6\n" \
  "v_alignbit_b32 v250, v203, v201, v6\n" \
  "v_alignbit_b32 v251, v205, v203, v6\n" \
  "v_alignbit_b32 v252, v207, v205, v6\n" \
  "v_alignbit_b32 v253, v209, v207, v6\n" \
  "ds_read2st64_b32 v[200:201], v28 offset0:17 offset1:16\n" \
  "ds_read2st64_b32 v[202:203], v29 offset0:17 offset1:16\n" \
  "ds_read2st64_b32 v[204:205], v30 offset0:17 offset1:16\n" \
  "ds_read2st64_b32 v[206:207], v31 offset0:17 offset1:16\n" \
  "ds_read2st64_b32 v[208:209], v32 offset0:17 offset1:16\n" \
  "ds_read2st64_b32 v[210:211], v28 offset0:15 offset1:14\n" \
  "ds_read2st64_b32 v[212:213], v29 offset0:15 offset1:14\n" \
  "ds_read2st64_b32 v[214:215], v30 offset0:15 offset1:14\n" \
  "ds_read2st64_b32 v[216:217], v31 offset0:15 offset1:14\n" \
  "ds_read2st64_b32 v[218:219], v32 offset0:15 offset1:14\n" \
  "v_and_b32 v250, v250, v8\n" \
  "v_and_b32 v251, v251, v9\n" \
  "v_and_b32 v252, v252, v10\n" \
  "v_and_b32 v253, v253, v11\n" \
  "s_bitcmp1_b32 s62, 1\n" \
  "s_cbranch_scc1 .LVA_QH_%=\n" \
  "s_bitcmp1_b32 s62, 0\n" \
  "s_cbranch_scc1 .LVA_QB_%=\n" \
  ".LVA_QA_%=:\n" \
  "v_mfma_f32_16x16x32_bf16 v[64:67], v[128:131], v[246:249], v[68:71]\n" \
  "v_mfma_f32_16x16x32_bf16 v[68:71], v[132:135], v[246:249], v[72:75]\n" \
  "v_mfma_f32_16x16x32_bf16 v[72:75], v[136:139], v[246:249], v[76:79]\n" \
  "v_mfma_f32_16x16x32_bf16 v[76:79], v[140:143], v[246:249], v[80:83]\n" \
  "s_cmp_ge_u32 s62, 48\n" \
  "s_cbranch_scc1 .LVA_EA_%=\n" \
  "v_mfma_f32_16x16x32_bf16 v[80:83], v[144:147], v[246:249], v[84:87]\n" \
  "v_mfma_f32_16x16x32_bf16 v[84:87], v[148:151], v[246:249], v[88:91]\n" \
  "v_mfma_f32_16x16x32_bf16 v[88:91], v[152:155], v[246:249], v[92:95]\n" \
  "v_mfma_f32_16x16x32_bf16 v[92:95], v[156:159], v[246:249], v[96:99]\n" \
  "s_cmp_ge_u32 s62, 32\n" \
  "s_cbranch_scc1 .LVA_EA_%=\n" \
  "v_mfma_f32_16x16x32_bf16 v[96:99], v[160:163], v[246:249], v[100:103]\n" \
  "v_mfma_f32_16x16x32_bf16 v[100:103], v[164:167], v[246:249], v[104:107]\n" \
  "v_mfma_f32_16x16x32_bf16 v[104:107], v[168:171], v[246:249], v[108:111]\n" \
  "v_mfma_f32_16x16x32_bf16 v[108:111], v[172:175], v[246:249], v[112:115]\n" \
  "s_cmp_ge_u32 s62, 16\n" \
  "s_cbranch_scc1 .LVA_EA_%=\n" \
  "v_mfma_f32_16x16x32_bf16 v[112:115], v[176:179], v[246:249], v[116:119]\n" \
  "v_mfma_f32_16x16x32_bf16 v[116:119], v[180:183], v[246:249], v[120:123]\n" \
  "v_mfma_f32_16x16x32_bf16 v[120:123], v[184:187], v[246:249], v[124:127]\n" \
  "v_mfma_f32_16x16x32_bf16 v[124:127], v[188:191], v[246:249], 0\n" \
  ".LVA_EA_%=:\n" \
  "s_nop 1\n" \
  "v_mfma_f32_16x16x32_bf16 v[20:23], v[192:195], v[250:253], v[64:67]\n" \
  "s_nop 7\n" \
  "v_permlane32_swap_b32 v22, v23\n" \
  "v_add_f32 v20, v22, v23\n" \
  "s_branch .LVA_PD_%=\n" \
  ".Lpad0xb1:\n" \
  "s_setpc_b64 s[98:99]\n" \
  ".LVA_QB_%=:\n" \
  "v_mfma_f32_16x16x32_bf16 v[64:67], v[128:131], v[246:249], v[64:67]\n" \
  "v_mfma_f32_16x16x32_bf16 v[68:71], v[132:135], v[246:249], v[68:71]\n" \
  "v_mfma_f32_16x16x32_bf16 v[72:75], v[136:139], v[246:249], v[72:75]\n" \
  "v_mfma_f32_16x16x32_bf16 v[76:79], v[140:143], v[246:249], v[76:79]\n" \
  "s_cmp_ge_u32 s62, 48\n" \
  "s_cbranch_scc1 .LVA_EB_%=\n" \
  "v_mfma_f32_16x16x32_bf16 v[80:83], v[144:147], v[246:249], v[80:83]\n" \
  "v_mfma_f32_16x16x32_bf16 v[84:87], v[148:151], v[246:249], v[84:87]\n" \
  "v_mfma_f32_16x16x32_bf16 v[88:91], v[152:155], v[246:249], v[88:91]\n" \
  "v_mfma_f32_16x16x32_bf16 v[92:95], v[156:159], v[246:249], v[92:95]\n" \
  "s_cmp_ge_u32 s62, 32\n" \
  "s_cbranch_scc1 .LVA_EB_%=\n" \
  "v_mfma_f32_16x16x32_bf16 v[96:99], v[160:163], v[246:249], v[96:99]\n" \
  "v_mfma_f32_16x16x32_bf16 v[100:103], v[164:167], v[246:249], v[100:103]\n" \
  "v_mfma_f32_16x16x32_bf16 v[104:107], v[168:171], v[246:249], v[104:107]\n" \
  "v_mfma_f32_16x16x32_bf16 v[108:111], v[172:175], v[246:249], v[108:111]\n" \
  "s_cmp_ge_u32 s62, 16\n" \
  "s_cbranch_scc1 .LVA_EB_%=\n" \
  "v_mfma_f32_16x16x32_bf16 v[112:115], v[176:179], v[246:249], v[112:115]\n" \
  "v_mfma_f32_16x16x32_bf16 v[116:119], v[180:183], v[246:249], v[116:119]\n" \
  "v_mfma_f32_16x16x32_bf16 v[120:123], v[184:187], v[246:249], v[120:123]\n" \
  "v_mfma_f32_16x16x32_bf16 v[124:127], v[188:191], v[246:249], v[124:127]\n" \
  ".LVA_EB_%=:\n" \
  "s_nop 1\n" \
  "v_mfma_f32_16x16x32_bf16 v[20:23], v[192:195], v[250:253], v[64:67]\n" \
  "s_not_b64 s[72:73], s[72:73]\n" \
  "s_nop 6\n" \
  "v_permlane32_swap_b32 v20, v21\n" \
  "v_add_f32 v20, v20, v21\n" \
  "s_branch .LVA_PD_%=\n" \
  ".LVA_QH_%=:\n" \
  "s_bitcmp1_b32 s62, 0\n" \
  "s_cbranch_scc1 .LVA_QD_%=\n" \
  ".LVA_QC_%=:\n" \
  "v_mfma_f32_16x16x32_bf16 v[64:67], v[128:131], v[246:249], v[64:67]\n" \
  "v_mfma_f32_16x16x32_bf16 v[68:71], v[132:135], v[246:249], v[68:71]\n" \
  "v_mfma_f32_16x16x32_bf16 v[72:75], v[136:139], v[246:249], v[72:75]\n" \
  "v_mfma_f32_16x16x32_bf16 v[76:79], v[140:143], v[246:249], v[76:79]\n" \
  "s_cmp_ge_u32 s62, 48\n" \
  "s_cbranch_scc1 .LVA_EC_%=\n" \
  "v_mfma_f32_16x16x32_bf16 v[80:83], v[144:147], v[246:249], v[80:83]\n" \
  "v_mfma_f32_16x16x32_bf16 v[84:87], v[148:151], v[246:249], v[84:87]\n" \
  "v_mfma_f32_16x16x32_bf16 v[88:91], v[152:155], v[246:249], v[88:91]\n" \
  "v_mfma_f32_16x16x32_bf16 v[92:95], v[156:159], v[246:249], v[92:95]\n" \
  "s_cmp_ge_u32 s62, 32\n" \
  "s_cbranch_scc1 .LVA_EC_%=\n" \
  "v_mfma_f32_16x16x32_bf16 v[96:99], v[160:163], v[246:249], v[96:99]\n" \
  "v_mfma_f32_16x16x32_bf16 v[100:103], v[164:167], v[246:249], v[100:103]\n" \
  "v_mfma_f32_16x16x32_bf16 v[104:107], v[168:171], v[246:249], v[104:107]\n" \
  "v_mfma_f32_16x16x32_bf16 v[108:111], v[172:175], v[246:249], v[108:111]\n" \
  "s_cmp_ge_u32 s62, 16\n" \
  "s_cbranch_scc1 .LVA_EC_%=\n" \
  "v_mfma_f32_16x16x32_bf16 v[112:115], v[176:179], v[246:249], v[112:115]\n" \
  "v_mfma_f32_16x16x32_bf16 v[116:119], v[180:183], v[246:249], v[116:119]\n" \
  "v_mfma_f32_16x16x32_bf16 v[120:123], v[184:187], v[246:249], v[120:123]\n" \
  "v_mfma_f32_16x16x32_bf16 v[124:127], v[188:191], v[246:249], v[124:127]\n" \
  ".LVA_EC_%=:\n" \
  "s_nop 1\n" \
  "v_mfma_f32_16x16x32_bf16 v[20:23], v[192:195], v[250:253], v[64:67]\n" \
  "s_nop 7\n" \
  "v_permlane32_swap_b32 v22, v23\n" \
  "v_add_f32 v20, v22, v23\n" \
  "s_branch .LVA_PD_%=\n" \
  ".LVA_QD_%=:\n" \
  "v_mfma_f32_16x16x32_bf16 v[64:67], v[128:131], v[246:249], v[64:67]\n" \
  "v_mfma_f32_16x16x32_bf16 v[68:71], v[132:135], v[246:249], v[68:71]\n" \
  "v_mfma_f32_16x16x32_bf16 v[72:75], v[136:139], v[246:249], v[72:75]\n" \
  "v_mfma_f32_16x16x32_bf16 v[76:79], v[140:143], v[246:249], v[76:79]\n" \
  "s_cmp_ge_u32 s62, 48\n" \
  "s_cbranch_scc1 .LVA_ED_%=\n" \
  "v_mfma_f32_16x16x32_bf16 v[80:83], v[144:147], v[246:249], v[80:83]\n" \
  "v_mfma_f32_16x16x32_bf16 v[84:87], v[148:151], v[246:249], v[84:87]\n" \
  "v_mfma_f32_16x16x32_bf16 v[88:91], v[152:155], v[246:249], v[88:91]\n" \
  "v_mfma_f32_16x16x32_bf16 v[92:95], v[156:159], v[246:249], v[92:95]\n" \
  "s_cmp_ge_u32 s62, 32\n" \
  "s_cbranch_scc1 .LVA_ED_%=\n" \
  "v_mfma_f32_16x16x32_bf16 v[96:99], v[160:163], v[246:249], v[96:99]\n" \
  "v_mfma_f32_16x16x32_bf16 v[100:103], v[164:167], v[246:249], v[100:103]\n" \
  "v_mfma_f32_16x16x32_bf16 v[104:107], v[168:171], v[246:249], v[104:107]\n" \
  "v_mfma_f32_16x16x32_bf16 v[108:111], v[172:175], v[246:249], v[108:111]\n" \
  "s_cmp_ge_u32 s62, 16\n" \
  "s_cbranch_scc1 .LVA_ED_%=\n" \
  "v_mfma_f32_16x16x32_bf16 v[112:115], v[176:179], v[246:249], v[112:115]\n" \
  "v_mfma_f32_16x16x32_bf16 v[116:119], v[180:183], v[246:249], v[116:119]\n" \
  "v_mfma_f32_16x16x32_bf16 v[120:123], v[184:187], v[246:249], v[120:123]\n" \
  "v_mfma_f32_16x16x32_bf16 v[124:127], v[188:191], v[246:249], v[124:127]\n" \
  ".LVA_ED_%=:\n" \
  "s_nop 1\n" \
  "v_mfma_f32_16x16x32_bf16 v[20:23], v[192:195], v[250:253], v[68:71]\n" \
  "s_not_b64 s[72:73], s[72:73]\n" \
  "s_nop 6\n" \
  "v_permlane32_swap_b32 v20, v21\n" \
  "v_add_f32 v20, v20, v21\n" \
  ".LVA_PD_%=:\n" \
  "s_cmp_lg_u32 s63, 0\n" \
  "s_cbranch_scc1 .LVA_NOF_%=\n" \
  "v_lshlrev_b32 v56, 6, v7\n" \
  "s_waitcnt vmcnt(0)\n" \
  "s_mov_b64 exec, 15\n" \
  "global_store_dword v56, v5, s[58:59] sc1\n" \
  "s_mov_b64 exec, -1\n" \
  "s_add_u32 s58, s58, 4096\n" \
  "s_addc_u32 s59, s59, 0\n" \
  ".LVA_NOF_%=:\n" \
  "s_add_u32 s74, s62, 12\n" \
  "s_lshl_b32 s76, s63, 1\n" \
  "s_and_b32 s76, s76, 12\n" \
  "s_xor_b32 s74, s74, s76\n" \
  "s_and_b32 s79, s74, 15\n" \
  "s_sub_u32 s74, s62, 4\n" \
  "s_cmp_lt_u32 s74, 48\n" \
  "s_cselect_b32 s79, s79, 1\n" \
  "s_cmp_lg_u32 s79, 0\n" \
  "s_cbranch_scc1 .LVA_NOS_%=\n" \
  "ds_read_u16 v230, v35 offset:0\n" \
  "ds_read_u16 v231, v35 offset:784\n" \
  "ds_read_u16 v232, v35 offset:1568\n" \
  "ds_read_u16 v233, v35 offset:2352\n" \
  "ds_read_u16 v234, v35 offset:3136\n" \
  "ds_read_u16 v235, v35 offset:3920\n" \
  "ds_read_u16 v236, v35 offset:4704\n" \
  "ds_read_u16 v237, v35 offset:5488\n" \
  "v_lshlrev_b32 v238, 16, v44\n" \
  "v_and_b32 v239, 0xffff0000, v44\n" \
  "v_lshlrev_b32 v240, 16, v45\n" \
  "v_and_b32 v241, 0xffff0000, v45\n" \
  "v_lshlrev_b32 v242, 16, v46\n" \
  "v_and_b32 v243, 0xffff0000, v46\n" \
  "v_lshlrev_b32 v244, 16, v47\n" \
  "v_and_b32 v245, 0xffff0000, v47\n" \
  "v_mov_b64 v[44:45], v[48:49]\n" \
  "v_mov_b64 v[46:47], v[50:51]\n" \
  "v_mov_b64 v[48:49], v[52:53]\n" \
  "v_mov_b64 v[50:51], v[54:55]\n" \
  "s_waitcnt lgkmcnt(0)\n" \
  "v_lshlrev_b32 v230, 16, v230\n" \
  "v_lshlrev_b32 v231, 16, v231\n" \
  "v_lshlrev_b32 v232, 16, v232\n" \
  "v_lshlrev_b32 v233, 16, v233\n" \
  "v_lshlrev_b32 v234, 16, v234\n" \
  "v_lshlrev_b32 v235, 16, v235\n" \
  "v_lshlrev_b32 v236, 16, v236\n" \
  "v_lshlrev_b32 v237, 16, v237\n" \
  "v_pk_add_f32 v[230:231], v[230:231], v[238:239]\n" \
  "v_pk_add_f32 v[232:233], v[232:233], v[240:241]\n" \
  "v_pk_add_f32 v[234:235], v[234:235], v[242:243]\n" \
  "v_pk_add_f32 v[236:237], v[236:237], v[244:245]\n" \
  "v_cvt_pk_bf16_f32 v56, v230, v231\n" \
  "v_cvt_pk_bf16_f32 v57, v232, v233\n" \
  "v_cvt_pk_bf16_f32 v58, v234, v235\n" \
  "v_cvt_pk_bf16_f32 v59, v236, v237\n" \
  "buffer_store_dwordx4 v[56:59], v36, s[52:55], s68 offen sc1\n" \
  "s_add_u32 s68, s68, 0x80000\n" \
  "s_mov_b32 s0, 0xbe801d62\n" \
  ".set .Lpad0xb2, . - 4\n" \
  ".LVA_NOS_%=:\n" \
  "s_cmp_ge_u32 s62, 48\n" \
  "s_cbranch_scc1 .LVA_FIN_%=\n" \
  "s_lshr_b32 s78, s62, 2\n" \
  "s_and_b32 s74, s62, 3\n" \
  "ds_read2st64_b32 v[220:221], v28 offset0:13 offset1:12\n" \
  "ds_read2st64_b32 v[222:223], v29 offset0:13 offset1:12\n" \
  "ds_read2st64_b32 v[224:225], v30 offset0:13 offset1:12\n" \
  "ds_read2st64_b32 v[226:227], v31 offset0:13 offset1:12\n" \
  "ds_read2st64_b32 v[228:229], v32 offset0:13 offset1:12\n" \
  "s_waitcnt lgkmcnt(10)\n" \
  "v_alignbit_b32 v230, v202, v200, v6\n" \
  "v_alignbit_b32 v231, v204, v202, v6\n" \
  "v_alignbit_b32 v232, v206, v204, v6\n" \
  "v_alignbit_b32 v233, v208, v206, v6\n" \
  "v_alignbit_b32 v234, v203, v201, v6\n" \
  "v_alignbit_b32 v235, v205, v203, v6\n" \
  "v_alignbit_b32 v236, v207, v205, v6\n" \
  "v_alignbit_b32 v237, v209, v207, v6\n" \
  "s_cbranch_scc1 .LVA_NOZ_%=\n" \
  "v_mfma_f32_16x16x32_bf16 v[196:199], v[24:27], v[230:233], 0\n" \
  ".LVA_NOZ_%=:\n" \
  "s_nop 0\n" \
  "v_mfma_f32_16x16x32_bf16 v[196:199], v[128:131], v[234:237], v[196:199]\n" \
  "ds_read2st64_b32 v[200:201], v28 offset0:11 offset1:10\n" \
  "ds_read2st64_b32 v[202:203], v29 offset0:11 offset1:10\n" \
  "ds_read2st64_b32 v[204:205], v30 offset0:11 offset1:10\n" \
  "ds_read2st64_b32 v[206:207], v31 offset0:11 offset1:10\n" \
  "ds_read2st64_b32 v[208:209], v32 offset0:11 offset1:10\n" \
  "s_waitcnt lgkmcnt(10)\n" \
  "v_alignbit_b32 v238, v212, v210, v6\n" \
  "v_alignbit_b32 v239, v214, v212, v6\n" \
  "v_alignbit_b32 v240, v216, v214, v6\n" \
  "v_alignbit_b32 v241, v218, v216, v6\n" \
  "v_alignbit_b32 v242, v213, v211, v6\n" \
  "v_alignbit_b32 v243, v215, v213, v6\n" \
  "v_alignbit_b32 v244, v217, v215, v6\n" \
  "v_alignbit_b32 v245, v219, v217, v6\n" \
  "v_mfma_f32_16x16x32_bf16 v[196:199], v[132:135], v[238:241], v[196:199]\n" \
  "s_nop 0\n" \
  "v_mfma_f32_16x16x32_bf16 v[196:199], v[136:139], v[242:245], v[196:199]\n" \
  "ds_read2st64_b32 v[210:211], v28 offset0:9 offset1:8\n" \
  "ds_read2st64_b32 v[212:213], v29 offset0:9 offset1:8\n" \
  "ds_read2st64_b32 v[214:215], v30 offset0:9 offset1:8\n" \
  "ds_read2st64_b32 v[216:217], v31 offset0:9 offset1:8\n" \
  "ds_read2st64_b32 v[218:219], v32 offset0:9 offset1:8\n" \
  "s_waitcnt lgkmcnt(10)\n" \
  "v_alignbit_b32 v230, v222, v220, v6\n" \
  "v_alignbit_b32 v231, v224, v222, v6\n" \
  "v_alignbit_b32 v232, v226, v224, v6\n" \
  "v_alignbit_b32 v233, v228, v226, v6\n" \
  "v_alignbit_b32 v234, v223, v221, v6\n" \
  "v_alignbit_b32 v235, v225, v223, v6\n" \
  "v_alignbit_b32 v236, v227, v225, v6\n" \
  "v_alignbit_b32 v237, v229, v227, v6\n" \
  "v_mfma_f32_16x16x32_bf16 v[196:199], v[140:143], v[230:233], v[196:199]\n" \
  "s_nop 0\n" \
  "v_mfma_f32_16x16x32_bf16 v[196:199], v[144:147], v[234:237], v[196:199]\n" \
  "s_cmp_eq_u32 s78, 0\n" \
  "s_cbranch_scc1 .LVA_OUT_%=\n" \
  "ds_read2st64_b32 v[220:221], v28 offset0:7 offset1:6\n" \
  "ds_read2st64_b32 v[222:223], v29 offset0:7 offset1:6\n" \
  "ds_read2st64_b32 v[224:225], v30 offset0:7 offset1:6\n" \
  "ds_read2st64_b32 v[226:227], v31 offset0:7 offset1:6\n" \
  "ds_read2st64_b32 v[228:229], v32 offset0:7 offset1:6\n" \
  "s_waitcnt lgkmcnt(10)\n" \
  "v_alignbit_b32 v238, v202, v200, v6\n" \
  "v_alignbit_b32 v239, v204, v202, v6\n" \
  "v_alignbit_b32 v240, v206, v204, v6\n" \
  "v_alignbit_b32 v241, v208, v206, v6\n" \
  "v_alignbit_b32 v242, v203, v201, v6\n" \
  "v_alignbit_b32 v243, v205, v203, v6\n" \
  "v_alignbit_b32 v244, v207, v205, v6\n" \
  "v_alignbit_b32 v245, v209, v207, v6\n" \
  "v_mfma_f32_16x16x32_bf16 v[196:199], v[148:151], v[238:241], v[196:199]\n" \
  "s_cmp_eq_u32 s78, 1\n" \
  "s_cbranch_scc1 .LVA_OUT_%=\n" \
  "v_mfma_f32_16x16x32_bf16 v[196:199], v[152:155], v[242:245], v[196:199]\n" \
  "s_cmp_eq_u32 s78, 2\n" \
  "s_cbranch_scc1 .LVA_OUT_%=\n" \
  "ds_read2st64_b32 v[200:201], v28 offset0:5 offset1:4\n" \
  "ds_read2st64_b32 v[202:203], v29 offset0:5 offset1:4\n" \
  "ds_read2st64_b32 v[204:205], v30 offset0:5 offset1:4\n" \
  "ds_read2st64_b32 v[206:207], v31 offset0:5 offset1:4\n" \
  "ds_read2st64_b32 v[208:209], v32 offset0:5 offset1:4\n" \
  "s_waitcnt lgkmcnt(10)\n" \
  "v_alignbit_b32 v230, v212, v210, v6\n" \
  "v_alignbit_b32 v231, v214, v212, v6\n" \
  "v_alignbit_b32 v232, v216, v214, v6\n" \
  "v_alignbit_b32 v233, v218, v216, v6\n" \
  "v_alignbit_b32 v234, v213, v211, v6\n" \
  "v_alignbit_b32 v235, v215, v213, v6\n" \
  "v_alignbit_b32 v236, v217, v215, v6\n" \
  "v_alignbit_b32 v237, v219, v217, v6\n" \
  "v_mfma_f32_16x16x32_bf16 v[196:199], v[156:159], v[230:233], v[196:199]\n" \
  "s_cmp_eq_u32 s78, 3\n" \
  "s_cbranch_scc1 .LVA_OUT_%=\n" \
  "v_mfma_f32_16x16x32_bf16 v[196:199], v[160:163], v[234:237], v[196:199]\n" \
  "s_cmp_eq_u32 s78, 4\n" \
  "s_cbranch_scc1 .LVA_OUT_%=\n" \
  "ds_read2st64_b32 v[210:211], v28 offset0:3 offset1:2\n" \
  "ds_read2st64_b32 v[212:213], v29 offset0:3 offset1:2\n" \
  "ds_read2st64_b32 v[214:215], v30 offset0:3 offset1:2\n" \
  "ds_read2st64_b32 v[216:217], v31 offset0:3 offset1:2\n" \
  "ds_read2st64_b32 v[218:219], v32 offset0:3 offset1:2\n" \
  "s_waitcnt lgkmcnt(10)\n" \
  "v_alignbit_b32 v238, v222, v220, v6\n" \
  "v_alignbit_b32 v239, v224, v222, v6\n" \
  "v_alignbit_b32 v240, v226, v224, v6\n" \
  "v_alignbit_b32 v241, v228, v226, v6\n" \
  "v_alignbit_b32 v242, v223, v221, v6\n" \
  "v_alignbit_b32 v243, v225, v223, v6\n" \
  "v_alignbit_b32 v244, v227, v225, v6\n" \
  "v_alignbit_b32 v245, v229, v227, v6\n" \
  "v_mfma_f32_16x16x32_bf16 v[196:199], v[164:167], v[238:241], v[196:199]\n" \
  "s_cmp_eq_u32 s78, 5\n" \
  "s_cbranch_scc1 .LVA_OUT_%=\n" \
  "v_mfma_f32_16x16x32_bf16 v[196:199], v[168:171], v[242:245], v[196:199]\n" \
  "s_cmp_eq_u32 s78, 6\n" \
  "s_cbranch_scc1 .LVA_OUT_%=\n" \
  "ds_read2st64_b32 v[220:221], v28 offset0:1 offset1:0\n" \
  "ds_read2st64_b32 v[222:223], v29 offset0:1 offset1:0\n" \
  "ds_read2st64_b32 v[224:225], v30 offset0:1 offset1:0\n" \
  "ds_read2st64_b32 v[226:227], v31 offset0:1 offset1:0\n" \
  "ds_read2st64_b32 v[228:229], v32 offset0:1 offset1:0\n" \
  "s_waitcnt lgkmcnt(10)\n" \
  "v_alignbit_b32 v230, v202, v200, v6\n" \
  "v_alignbit_b32 v231, v204, v202, v6\n" \
  "v_alignbit_b32 v232, v206, v204, v6\n" \
  "v_alignbit_b32 v233, v208, v206, v6\n" \
  "v_alignbit_b32 v234, v203, v201, v6\n" \
  "v_alignbit_b32 v235, v205, v203, v6\n" \
  "v_alignbit_b32 v236, v207, v205, v6\n" \
  "v_alignbit_b32 v237, v209, v207, v6\n" \
  "v_mfma_f32_16x16x32_bf16 v[196:199], v[172:175], v[230:233], v[196:199]\n" \
  "s_cmp_eq_u32 s78, 7\n" \
  "s_cbranch_scc1 .LVA_OUT_%=\n" \
  "v_mfma_f32_16x16x32_bf16 v[196:199], v[176:179], v[234:237], v[196:199]\n" \
  "s_cmp_eq_u32 s78, 8\n" \
  "s_cbranch_scc1 .LVA_OUT_%=\n" \
  "s_waitcnt lgkmcnt(5)\n" \
  "v_alignbit_b32 v238, v212, v210, v6\n" \
  "v_alignbit_b32 v239, v214, v212, v6\n" \
  "v_alignbit_b32 v240, v216, v214, v6\n" \
  "v_alignbit_b32 v241, v218, v216, v6\n" \
  "v_alignbit_b32 v242, v213, v211, v6\n" \
  "v_alignbit_b32 v243, v215, v213, v6\n" \
  "v_alignbit_b32 v244, v217, v215, v6\n" \
  "v_alignbit_b32 v245, v219, v217, v6\n" \
  "v_mfma_f32_16x16x32_bf16 v[196:199], v[180:183], v[238:241], v[196:199]\n" \
  "s_cmp_eq_u32 s78, 9\n" \
  "s_cbranch_scc1 .LVA_OUT_%=\n" \
  "v_mfma_f32_16x16x32_bf16 v[196:199], v[184:187], v[242:245], v[196:199]\n" \
  "s_cmp_eq_u32 s78, 10\n" \
  "s_cbranch_scc1 .LVA_OUT_%=\n" \
  "s_waitcnt lgkmcnt(0)\n" \
  "v_alignbit_b32 v230, v222, v220, v6\n" \
  "v_alignbit_b32 v231, v224, v222, v6\n" \
  "v_alignbit_b32 v232, v226, v224, v6\n" \
  "v_alignbit_b32 v233, v228, v226, v6\n" \
  "s_nop 1\n" \
  "v_mfma_f32_16x16x32_bf16 v[196:199], v[188:191], v[230:233], v[196:199]\n" \
  ".LVA_OUT_%=:\n" \
  "s_mov_b32 s0, 0xbe801d62\n" \
  ".set .Lpad0xb3, . - 4\n" \
  "v_add_u32 v28, 64, v28\n" \
  "v_add_u32 v29, 64, v29\n" \
  "v_add_u32 v30, 64, v30\n" \
  "v_add_u32 v31, 64, v31\n" \
  "v_add_u32 v32, 64, v32\n" \
  "s_cmp_lg_u32 s74, 3\n" \
  "s_cbranch_scc1 .LVA_FIN_%=\n" \
  "ds_read_u16 v56, v33 offset:0\n" \
  "ds_read_u16 v57, v33 offset:64\n" \
  "v_add_u32 v33, 256, v33\n" \
  "v_permlane32_swap_b32 v196, v197\n" \
  "v_permlane32_swap_b32 v198, v199\n" \
  "v_add_f32 v196, v196, v197\n" \
  "v_add_f32 v198, v198, v199\n" \
  "s_waitcnt lgkmcnt(0)\n" \
  "v_lshlrev_b32 v56, 16, v56\n" \
  "v_lshlrev_b32 v57, 16, v57\n" \
  "v_mul_f32 v56, s64, v56\n" \
  "v_mul_f32 v57, s64, v57\n" \
  "v_cvt_pk_bf16_f32 v197, v196, v198\n" \
  "v_cvt_pk_bf16_f32 v57, v56, v57\n" \
  "v_lshlrev_b32 v196, 16, v197\n" \
  "v_and_b32 v197, 0xffff0000, v197\n" \
  "v_lshlrev_b32 v56, 16, v57\n" \
  "v_and_b32 v57, 0xffff0000, v57\n" \
  "v_add_f32 v56, v56, v196\n" \
  "v_add_f32 v57, v57, v197\n" \
  "v_cvt_pk_bf16_f32 v58, v56, v57\n" \
  "ds_write_b16 v34, v58\n" \
  "ds_write_b16_d16_hi v34, v58 offset:64\n" \
  ".LVA_FIN_%=:\n" \
  "s_cmp_lg_u32 s79, 0\n" \
  "s_cbranch_scc1 .LVA_NOG_%=\n" \
  "v_lshlrev_b32 v60, 6, v7\n" \
  "s_waitcnt vmcnt(0)\n" \
  "s_mov_b64 exec, 255\n" \
  "global_store_dword v60, v5, s[60:61] sc1\n" \
  "s_mov_b64 exec, -1\n" \
  "s_add_u32 s60, s60, 0x10000\n" \
  "s_addc_u32 s61, s61, 0\n" \
  ".LVA_NOG_%=:\n" \
  "s_add_u32 s62, s62, 1\n" \
  "s_cmp_eq_u32 s62, 64\n" \
  "s_cbranch_scc1 .LVA_END_%=\n" \
  ".LVA_TAPS_%=:\n" \
  "s_and_b32 s74, s62, 3\n" \
  "s_lshl_b32 s76, s74, 6\n" \
  "v_add_u32 v56, s76, v5\n" \
  "s_cmp_eq_u32 s74, 3\n" \
  "s_cselect_b32 s74, 0, 256\n" \
  "v_add_u32 v57, s74, v56\n" \
  "ds_read_b128 v[128:131], v56 offset:3840\n" \
  "ds_read_b128 v[132:135], v56 offset:3584\n" \
  "ds_read_b128 v[136:139], v56 offset:3328\n" \
  "ds_read_b128 v[140:143], v56 offset:3072\n" \
  "ds_read_b128 v[192:195], v57 offset:3648\n" \
  "s_cmp_ge_u32 s62, 48\n" \
  "s_cbranch_scc1 .LVA_TOP_%=\n" \
  "ds_read_b128 v[144:147], v56 offset:2816\n" \
  "ds_read_b128 v[148:151], v56 offset:2560\n" \
  "ds_read_b128 v[152:155], v56 offset:2304\n" \
  "ds_read_b128 v[156:159], v56 offset:2048\n" \
  "ds_read_b128 v[160:163], v56 offset:1792\n" \
  "ds_read_b128 v[164:167], v56 offset:1536\n" \
  "ds_read_b128 v[168:171], v56 offset:1280\n" \
  "ds_read_b128 v[172:175], v56 offset:1024\n" \
  "ds_read_b128 v[176:179], v56 offset:768\n" \
  "ds_read_b128 v[180:183], v56 offset:512\n" \
  "ds_read_b128 v[184:187], v56 offset:256\n" \
  "ds_read_b128 v[188:191], v56 offset:0\n" \
  "s_branch .LVA_TOP_%=\n" \
  ".LVA_END_%=:\n" \
  "s_endpgm\n"
__device__ __forceinline__ v4u mk_srd(const void* p, const unsigned bytes) {
  const unsigned long long a = (unsigned long long)p;
  return v4u{(unsigned)a, (unsigned)(a >> 32) & 0xffffu, bytes, 0x00020000u};
}
template <bool ATT>
__device__ __forceinline__ void role_chain_t(const MegaArgs& A, char* smem, const int L, const int h, const int rep) {
  __builtin_amdgcn_s_waitcnt(0x0F70);
  MARK();
  u16* swo = reinterpret_cast<u16*>(smem);
  const int w = __builtin_amdgcn_readfirstlane(threadIdx.x >> 6);
  const int wc = (w + ROT) & 7, psw = PSWH;
  char* ws = A.ws;
  unsigned* FL = (unsigned*)(ws + OFF_FL);
  unsigned* TMO = FL + FL_TMO;
#define LANE_IDS()                         \
  const int l = lane_id();                 \
  const int c = l & 15, g = l >> 4;        \
  (void)c; (void)g;
  float* sgm = reinterpret_cast<float*>(smem + C_X);
  float* sgf = sgm + 8;
  unsigned* FQ = FL + FL_FQ + L * 65 * 32 * 32;
  v8u w0[8], w1[8], w2[8];
  PSV;
  {
    LANE_IDS();
    v4u m0r;
    const unsigned sb = lds_base(smem);
    const int q = (l >> 2) & 3, p = l & 3;
    const int f = (q & 1) | ((g & 1) << 1) | ((q >> 1) << 2);
    const unsigned ra = sb + (g >> 1) * 16384 + (4 * (g & 1) + (q & 1) + 8 * (q >> 1)) * 256 + (w ^ f) * 32 + p * 8;
#define WP(k) (WM(k) == 0 ? A.Wv : WM(k) == 1 ? A.Wk : A.Wq)
#define WC(k) (WJ(k) ^ WXC)
#define W_READ(W, X)                                                          \
  {                                                                           \
    const unsigned a_ = (ra ^ (X)) + j * 4096 + i * 32768;                    \
    const v2u x0 = trd(a_), x1 = trd(a_ + 512);                               \
    W[4 * WC(k) + i] = v4u{x0.x, x0.y, x1.x, x1.y};                           \
  }
    v4u u0[16], u1[16], u2[16], zt;
    v2u ze;
    float f2[4] = {};
    unsigned txt = 0u;
    if (CSL && L) gate(now10() + CSL);
    PST(0);
    asm volatile("s_nop 4\n\tglobal_load_dwordx4 %0, %1, %2" : "=v"(m0r) : "v"(((2 * (c >> 2) + (c & 1)) * DM + 64 * w + 16 * g + 8 * (((c >> 1) ^ (c >> 3)) & 1)) * 2), "s"(A.mem0) : "memory");
#pragma unroll
    for (int k = 0; k < 12; ++k) w_issue<true>(WP(k >> 2), WC(k >> 2), k >> 2, k & 3, h, w, l, sb);
    constexpr int PB = ATT ? 100 : 120;
    tour<PB + 3, PB + 12, 4>(w);
#pragma unroll
    for (int k = 0; k < 13; ++k) {
      const int j = k & 3;
      if (k == (ATT ? 2 : 4)) pad<PB + 3>(txt);
      if (k == (ATT ? 4 : 7)) pad<PB + 4>(txt);
      if (k == (ATT ? 6 : 10)) pad<PB + 1, 600>(txt);
      if (k == (ATT ? 10 : 9)) pad<PB + 5>(txt);
      if (k == ZC + 1) wait_vm<10>();
      else if (k < 12 || ZC == 10) wait_vm<8>();
      if (CTH && L) __builtin_amdgcn_s_sleep(CTH);
      RAW_BAR();
      if (k == 0) {
        asm volatile("" : "+v"(m0r));
        *reinterpret_cast<v4u*>(smem + C_SM + w * 1024 + l * 16) = m0r;
      }
      if (k == IPC) txt = pull_text((h & 7) + 8 * w);
      if (k == IPC + 2) asm volatile("" ::"v"(txt));
      if (k == ZC)
        asm volatile("s_nop 4\n\tglobal_load_dwordx4 %0, %2, %3 sc1\n\tglobal_load_dwordx2 %1, %4, %5 sc1"
                     : "=&v"(zt), "=&v"(ze)
                     : "v"(l * 16), "s"(ws + OFF_TAG + (blockIdx.x & 7) * 1024), "v"(0), "s"(ws + OFF_TAG + 8192)
                     : "memory");
#pragma unroll
      for (int i = 0; i < 4; ++i) {
        if (k < 9) w_issue<true>(WP(k + 3), WC(k + 3), (k + 3) & 3, i, h, w, l, sb);
        else {
          const int jo = k - 9, fd = (i & 1) | ((jo & 1) << 1) | ((w & 1) << 2);
          glds16((const char*)A.Wo + (size_t)(h * DH + 64 * (jo >> 1) + 8 * w + 4 * (jo & 1)) * (DM * 2), (unsigned)(i * 1024 + ((l ^ (fd << 1)) << 4)),
                 sb + w * 16384 + jo * 4096 + i * 1024);
        }
        if (k < 12 && WM(k) == (ATT ? 0 : 2)) { W_READ(u1, 0); W_READ(u2, 128); }
        if (k < 12 && WM(k) == 1) W_READ(u0, 0);
        if (ATT && k > 0 && WM(k - 1) == 0) {
#pragma unroll
          for (int e = 0; e < 4; ++e) {
            f2[e & 1] = dot2sq(u1[4 * WC(k - 1) + i][e], f2[e & 1]);
            f2[2 + (e & 1)] = dot2sq(u2[4 * WC(k - 1) + i][e], f2[2 + (e & 1)]);
          }
          asm volatile("" : "+v"(f2[0]), "+v"(f2[1]), "+v"(f2[2]), "+v"(f2[3]));
        }
      }
      if (k == (WORD ? 10 : 4)) {
        const float f = wave_sum_dpp((f2[0] + f2[1]) + (f2[2] + f2[3]));
        if (l == 0) { sgf[w] = f; sgm[16] = 0.f; sgm[32 + w] = 1.f; }
      }
      if (k == (WORD ? 11 : ZC + 2)) {
        asm volatile("" : "+v"(zt), "+v"(ze));
        if (w == 0) {
          if (__builtin_expect(!zero_seen(zt, ze), 0)) zero_wait(ws);
          const float fs = (sgf[0] + sgf[1]) + (sgf[2] + sgf[3]);
          __builtin_amdgcn_raw_buffer_store_b32(__float_as_uint(fs) | 1u, MKRS(FL + FL_G, L ? 0 : 4 * 512), l * 512 + h * 4, 0, AUX_SC1);
        }
      }
      if (k == 1) PST(1);
      if (k == 3) PST(2);
      if (k == 5) PST(3);
      if (k == 7) PST(4);
      if (k == 9) PST(5);
      if (k == 11) PST(6);
    }
#undef W_READ
#undef WP
#undef WC
#pragma unroll
    for (int t = 0; t < 8; ++t) {
      const v8u a0 = __builtin_shufflevector(u0[2 * t], u0[2 * t + 1], 0, 1, 2, 3, 4, 5, 6, 7), a1 = __builtin_shufflevector(u1[2 * t], u1[2 * t + 1], 0, 1, 2, 3, 4, 5, 6, 7);
      w0[t] = ATT ? a0 : a1;
      w1[t] = ATT ? a1 : a0;
      w2[t] = __builtin_shufflevector(u2[2 * t], u2[2 * t + 1], 0, 1, 2, 3, 4, 5, 6, 7);
    }
    const unsigned rb = sb + (2 * (g >> 1) + (q >> 1)) * 16384 + (4 * (g & 1) + (q & 1)) * 1024 + p * 8;
#pragma unroll
    for (int hf = 0; hf < 2; ++hf) {
      if (hf == 0) WAIT_VM(8); else WAIT_VM(0);
      if (hf == 1) pad<PB + 6>();
      RAW_BAR();
      if (hf == 1 && w == 0) __builtin_amdgcn_raw_buffer_store_b32((now10() + CRD) | 1u, MKRS(FL + FL_S0 + L * 512, 4 * 512), l * 512 + (h >> 3) * 128 + 32 + (h & 7) * 4, 0, AUX_SC1);
      v4u fr[8];
#pragma unroll
      for (int i = 0; i < 8; ++i) {
        const unsigned a_ = rb + hf * 8192 + (i & 1) * 65536 + ((4 * wc + ((i >> 1) ^ psw)) ^ f) * 32;
        const v2u r0 = trd(a_), r1 = trd(a_ + 2048);
        fr[i] = v4u{r0.x, r0.y, r1.x, r1.y};
      }
#pragma unroll
      for (int i = 0; i < 8; ++i) asm volatile("" : "+v"(fr[i]));
      RAW_BAR();
#pragma unroll
      for (int i = 0; i < 8; ++i) *reinterpret_cast<v4u*>(swo + w * 8192 + hf * 4096 + i * 512 + l * 8) = fr[i];
    }
  }
  PST(7);
  WSTAMP2();
  const unsigned sb = lds_base(smem);
  const char* gK = ws + OFF_QK + L * SZ_QK + ((size_t)h * 16 + 2 * w) * 1024;
  const char* gV = ws + OFF_VF + L * SZ_O + ((size_t)h * 8 + 2 * w - 8) * 1024;
  PST(8);
  pad<ATT ? 108 : 128>();
  v8u pv;
  {
    LANE_IDS();
    const unsigned voff = g * 256 + c * 8, wp = sb + w * 16384 + l * 16;
    pv[0] = (((c >> 1) ^ (c >> 3)) & 1) ? 0xEEEE : 0x4444;
    pv[1] = sb + C_SM + l * 16;
    pv[2] = ATT ? sb + ((c & 2) ? C_SK : C_SQ) + ((2 * (c >> 2) + (c & 1)) * QS + 8 * g) * 2 : voff;
    pv[3] = ATT ? l * 8 : wp;
    pv[4] = sb + ((c & 2) ? C_SQ : C_SO) + w * 256 + ((c & 13) + ((g ^ (c >> 2)) & 2)) * 16 + 8 * ((g ^ (c >> 1)) & 1);
    pv[5] = voff;
    pv[6] = wp;
    pv[7] = sb + C_SO + (l >> 5) * 1024 + w * 256 + (l & 31) * 8;
  }
  PST(10);
  PSD();
  SST(8);
  const v4u rsA = mk_srd(ws + OFF_ACC + L * SZ_ACC, SZ_ACC);
  const v4u rsF = mk_srd(FQ, 64 * 32 * 32 * 4);
  const v4u rsMK = mk_srd(ws + OFF_MEMK + L * SZ_MEM, SZ_MEM);
  const v4u rsMV = mk_srd(ATT ? ws + OFF_MEMV + L * SZ_MEM : (char*)(FL + FL_G + REP * 128), SZ_MEM);
  const int wr = (w + RR) & 7;
  const char* pAB = ws + OFF_ACC + L * SZ_ACC + (wc - wr) * 4096;
  const char* pGV = ATT ? (const char*)(size_t)(CDS + (XG(h) >> 1) * CDL) : gV;
  unsigned sc[8];
  sc[0] = ATT ? L * 1024 + XG(h) * 256 : h * 128;
  sc[1] = !ATT ? (h * 4 + w - 4) * 512 : (h & 7) == 1 ? L * 1024 + (h >> 3) * 256 + 128 : 1032 - L * 1024 + h * 4096;
  sc[2] = h * 2048 + w * 512;
  sc[3] = NSEG - 1;
  sc[5] = w;
  sc[6] = sb;
  const char* dbg = ws;
  const unsigned tg = (now10() / GR) * GR;
  const v4u rsT = mk_srd(FL + FL_TK, 2048);
#define CHAIN_OPS                                                                                                          \
  ::"{v[64:71]}"(w0[0]), "{v[72:79]}"(w0[1]), "{v[80:87]}"(w0[2]), "{v[88:95]}"(w0[3]), "{v[96:103]}"(w0[4]),              \
      "{v[104:111]}"(w0[5]), "{v[112:119]}"(w0[6]), "{v[120:127]}"(w0[7]), "{v[128:135]}"(w1[0]), "{v[136:143]}"(w1[1]),    \
      "{v[144:151]}"(w1[2]), "{v[152:159]}"(w1[3]), "{v[160:167]}"(w1[4]), "{v[168:175]}"(w1[5]), "{v[176:183]}"(w1[6]),    \
      "{v[184:191]}"(w1[7]), "{v[192:199]}"(w2[0]), "{v[200:207]}"(w2[1]), "{v[208:215]}"(w2[2]), "{v[216:223]}"(w2[3]),    \
      "{v[224:231]}"(w2[4]), "{v[232:239]}"(w2[5]), "{v[240:247]}"(w2[6]), "{v[248:255]}"(w2[7]), "{v[0:7]}"(pv),          \
      "{s[40:43]}"(rsA), "{s[44:47]}"(rsF), "{s[48:51]}"(rsMK), "{s[52:55]}"(rsMV), "{s[56:57]}"(pAB), "{s60}"(wr * 4096), "{s61}"(h * 1024 - wr * 4096), \
      "{s[62:63]}"(gK), "{s[64:65]}"(pGV), "{s68}"(sc[0]), "{s69}"(sc[1]), "{s70}"(sc[2]),    \
      "{s71}"(sc[3]), "{s73}"(sc[5]), "{s74}"(sc[6]), "{s[36:37]}"(dbg),                   \
      "{s34}"(tg), "{s35}"(psw * 1024), [gp] "n"(GP), [gr] "n"(GR), [gm] "n"(GM), \
      "{s[16:19]}"(rsT), "{s20}"(0), "{s22}"(sb + C_X + 128 + 8 * (w & 3)), "{s24}"(L * 1024), [cd] "n"(CD - CDS), \
      "{s25}"(APA - (APN + 1) * APP + L * (APB - APA + (2 * APN + 1) * APP)), [ka] "n"(APL), [an] "n"(APN), [d0] "n"(16 * CD0), [d1] "n"(CD1), [d2] "n"(CD2), [d3] "n"(CD1 + 63 * APP), [d4] "n"(CD4), [so] "n"((FL_S0 - FL_G) * 4), [sg] "n"(SLP_2), [g1] "n"(63 * APP + CG1 + CD2), [sf] "n"(SLP_1), [rq] "n"(RQM), [pp] "n"(APP) \
      : "memory", "vcc", "scc", "s99", "v63"
  if (ATT) asm volatile("s_mov_b64 exec, -1\ns_nop 4\n" CHAIN_ASM_ATT ".Lcode_end:\n" CHAIN_OPS);
  else asm volatile("s_mov_b64 exec, -1\ns_nop 4\n" CHAIN_ASM_VO CHAIN_OPS);
#undef CHAIN_OPS
#undef LANE_IDS
}

__device__ __forceinline__ void role_chain(const MegaArgs& A, char* smem, const int L, const int h, const int rep) {
  if (__builtin_amdgcn_readfirstlane(threadIdx.x >> 6) < 4) role_chain_t<true>(A, smem, L, h, rep);
  else role_chain_t<false>(A, smem, L, h, rep);
}

constexpr int AW_TK = 0, AW_SO = 114688, AW_M = AW_SO + 16384, AW_P = 24576, AW_MK = AW_P + 2048, AW_CNT = AW_SO + AW_MK + 4096;
__device__ __forceinline__ unsigned umin3(unsigned a, unsigned b, unsigned c) {
  return __builtin_elementwise_min(__builtin_elementwise_min(a, b), c);
}
__device__ __forceinline__ bool all_set(const unsigned r) {
  unsigned long long m;
  asm volatile("v_cmp_eq_u32 %0, 0, %1" : "=s"(m) : "v"(r) : "memory");
  return m == 0ull;
}
__device__ __forceinline__ unsigned long long zero_mask(const unsigned r) {
  unsigned long long m;
  asm volatile("v_cmp_eq_u32 %0, 0, %1" : "=s"(m) : "v"(r) : "memory");
  return m;
}
__device__ __forceinline__ int job_seg(const int n) {
  const int m = n - AWD;
  const int r = (m & 1) ? 64 + (m >> 1) : AWD + (m >> 1);
  return __builtin_amdgcn_readfirstlane(n > 127 ? 128 : (n < AWD || n >= 128 - AWD) ? n : r);
}
template <bool AT>
__device__ __forceinline__ void role_aw_t(const MegaArgs& A, char* smem, const int mode, const int a) {
  __builtin_amdgcn_s_waitcnt(0x0F70);
  MARK();
  const int w = __builtin_amdgcn_readfirstlane(threadIdx.x >> 6);
  const int l = threadIdx.x & 63, c = l & 15, g = l >> 4;
  const int qt = w & 1, hh = (w >> 1) & 1, h = 2 * a + hh;
  char* ws = A.ws;
  unsigned* FL = (unsigned*)(ws + OFF_FL);
  const unsigned sb = lds_base(smem);
  v4u wo[4][8];
  PSV;
  PST(0);
  const unsigned* FQ = FL + FL_FQ + 1024 + h * 32 + 16;
  const v4u rsW = mk_srd(ws + OFF_PW, SZ_PW);
  const v4u rsF = mk_srd(FQ, (129 * 32 - h) * 128 - 64);
  const char* gQ = ws + OFF_QK + ((size_t)h * 16 + 8 * qt) * 1024;
  const char* gV = ws + OFF_VF + ((size_t)h * 8 + 4 * qt) * 1024;
  const char* gM = ws + (qt ? OFF_MEMV : OFF_MEMK) + h * 2048;
  const unsigned mo = sb + AW_M + hh * 4096;
  const unsigned tq0 = __builtin_amdgcn_readfirstlane(sb + AW_TK + (hh * 24 + 8 * qt) * 1024);
  const unsigned tv0 = __builtin_amdgcn_readfirstlane(sb + AW_TK + (hh * 24 + 16 + 4 * qt) * 1024);
  const int gs = job_seg(mode);
  bool got = false;
  {
    const int q = (l >> 2) & 3, p = l & 3, f = q | ((g & 1) << 2);
    const unsigned ra = sb + (4 * g + q) * 256 + (p & 1) * 16;
    unsigned n0 = 0u;
#pragma unroll
    for (int i = 0; i < 8; ++i) wo_issue(A.Wo, a, 2 + (i >> 2), w, i & 3, sb);
#pragma unroll
    for (int ks = 0; ks < 8; ++ks) {
      if (ks >= 2 && ks <= 4) WAIT_VM(12);
      if (ks == 5) wait_vm<8 + (!AT && AWK < 5)>();
      if (ks == 6) wait_vm<4 + (!AT && AWK < 6)>();
      if (ks == 7) WAIT_VM(0);
      if (ks) RAW_BAR();
      if (!AT && ks == AWK)
        asm volatile("s_nop 4\n\tbuffer_load_dword %0, %1, %2, %3 offen sc1" : "=v"(n0) : "v"(0), "s"(rsF), "s"(gs * 4096) : "memory");
      if (ks == 5 && threadIdx.x < 4) ((lu32*)(unsigned long)(sb + AW_CNT))[threadIdx.x] = 0u;
      if (!AT && ks == 7 && __builtin_expect(all_set(n0), 1)) {
#pragma unroll
        for (int i = 0; i < 8; ++i) glds16c(gQ + (size_t)gs * (NH * 16 * 1024) + i * 1024, l * 16, tq0 + i * 1024);
#pragma unroll
        for (int i = 0; i < 4; ++i) glds16c(gV + (size_t)gs * (NH * 8 * 1024) + i * 1024, l * 16, tv0 + i * 1024);
        got = true;
      }
#pragma unroll
      for (int ct = 0; ct < 4; ++ct) {
        if (ks < 4) wo_issue(A.Wo, a, ks + 4, w, ct, sb);
        const int cp = 2 * w + (ct >> 1);
        const unsigned a_ = ra + (ks % 5) * 32768 + (cp >> 2) * 8192 + ((2 * (cp & 3) + (p >> 1)) ^ f) * 32 + (ct & 1) * 8;
        const v2u lo = trd(a_), hi = trd(a_ + 4096);
        wo[ct][ks] = v4u{lo.x, hi.x, lo.y, hi.y};
      }
#pragma unroll
      for (int ct = 0; ct < 4; ++ct) asm volatile("" : "+v"(wo[ct][ks]));
      if (ks == 1) PST(1); if (ks == 3) PST(2); if (ks == 5) PST(3); if (ks == 7) PST(4);
    }
    RAW_BAR();
  }
  if (got) PST(11);
  PSD();
  v8u pv, mk, wr[16];
  {
    const unsigned so = sb + AW_SO + l * 16, ta = sb + AW_TK + hh * 24576 + l * 16;
    const unsigned ix = (c & 2) ? 0xEEEE : 0x4444;
    pv = v8u{AT ? ix : l * 16u, so, (unsigned)(HK3 ? (l >> 3) * 2048 + (l & 7) * 16 : (c >> 1) * 2048 + (c & 1) * 64 + g * 16), AT ? ta : mo + qt * 2048 + l * 16, AT ? ta + qt * 4096 : ix,
             mo + (g * 8 + 2 * (c >> 2) + (c & 1)) * 16, sb + AW_M + 2048 + (g >> 1) * 512 + (c & 3) * 128 + (8 * (g & 1) + (c >> 2)) * 8,
             so + (hh * 2 + qt) * 4096};
#pragma unroll
    for (int i = 0; i < 8; ++i) mk[i] = 16 * (i >> 2) + 4 * g + (i & 3) > 16 * qt + c ? __float_as_uint(-3e38f) : 0u;
#pragma unroll
    for (int i = 0; i < 16; ++i) wr[i] = __builtin_shufflevector(wo[i >> 2][2 * (i & 3)], wo[i >> 2][2 * (i & 3) + 1], 0, 1, 2, 3, 4, 5, 6, 7);
  }
  const char* dbg = ws;
#define AW_OPS                                                                                                               \
  ::"{v[128:135]}"(wr[0]), "{v[136:143]}"(wr[1]), "{v[144:151]}"(wr[2]), "{v[152:159]}"(wr[3]), "{v[160:167]}"(wr[4]),        \
      "{v[168:175]}"(wr[5]), "{v[176:183]}"(wr[6]), "{v[184:191]}"(wr[7]), "{v[192:199]}"(wr[8]), "{v[200:207]}"(wr[9]),      \
      "{v[208:215]}"(wr[10]), "{v[216:223]}"(wr[11]), "{v[224:231]}"(wr[12]), "{v[232:239]}"(wr[13]), "{v[240:247]}"(wr[14]), \
      "{v[248:255]}"(wr[15]), "{v[0:7]}"(pv), "{v[8:15]}"(mk), "{s[40:43]}"(rsW), "{s[44:47]}"(rsF), "{s[48:49]}"(gQ),        \
      "{s[50:51]}"(gV), "{s[52:53]}"(gM), "{s[54:55]}"(FL + FL_FW + (a >> 2) * 32 + (a & 3)), "{s56}"(mode), "{s57}"(gs),      \
      "{s60}"(HK2 ? (a >> 1) * 32768 + w * 256 + (a & 1) * 128 : a * 32768 + w * 256), "{s[92:95]}"(mk_srd(ws, 0)), "{s61}"(AT ? AW_MK + qt * 2048 : tq0), "{s64}"(AT ? 0 : sb + AW_CNT + 8), "{s65}"(sb + AW_CNT + hh * 4), "{s62}"(tv0), "{s63}"(AT ? mo + 2048 - (sb + AW_SO) / 2 : mo + qt * 2048), "{s[84:87]}"(mk_srd(FL + FL_TK + (a >> 2) * 64 + 32, 1028)), "{s[88:89]}"(ws + WS_TOTAL + (28 << 20) + 1024 + qt * 4),            \
      "{s66}"(AT ? AW_P + qt * 1024 + hh * 4 : HK2 ? (a >> 1) * 32768 + (w - 4) * 256 + (a & 1) * 128 : a * 32768 + (w - 4) * 8192), "{s[82:83]}"(w == 4 ? 15ull : 0ull), "{s67}"(__builtin_amdgcn_readfirstlane((int)got)), "{s80}"(0x3e0293ee), "{s[36:37]}"(dbg),        \
      [wd] "n"(AWD), [w2] "n"(128 - 2 * AWD), [w3] "n"(131 - AWD), [dt] "n"(AWDT), [du] "n"(AWDU), [pp] "n"(APP), [mo] "n"(AWMO - 63 * APP), [sm] "n"(SLP_M), [sa] "n"(SLP_A) \
      : "memory", "vcc", "scc", "s81", "v127"
  if (AT) asm volatile("s_mov_b64 exec, -1\ns_nop 4\n" AW_ASM_0 AW_OPS);
  else asm volatile("s_mov_b64 exec, -1\ns_nop 4\n" AW_ASM_1 AW_OPS);
#undef AW_OPS
}
__device__ __forceinline__ void role_aw(const MegaArgs& A, char* smem, const int L, const int a) {
  if (__builtin_amdgcn_readfirstlane(threadIdx.x >> 6) < 4) role_aw_t<true>(A, smem, L, a);
  else role_aw_t<false>(A, smem, L, a);
}

__device__ __forceinline__ void role_red(const MegaArgs& A, char* smem, const int idx) {
  const int w = __builtin_amdgcn_readfirstlane(threadIdx.x >> 6);
  const int l = threadIdx.x & 63;
  const int L = w >> 2, gr = (w >> 1) & 1, r = idx * 2 + (w & 1);
  char* ws = A.ws;
  unsigned* FL = (unsigned*)(ws + OFF_FL);
  unsigned* TMO = FL + FL_TMO;
  const __amdgpu_buffer_rsrc_t rsW = MKRS(ws + OFF_PW, SZ_PW);
  const __amdgpu_buffer_rsrc_t rsX = MKRS(ws + OFF_XA, OFF_X3 - OFF_XA + SZ_X);
  volatile lu32* sm32 = (lu32*)(unsigned long)lds_base(smem);
  volatile lu32* sw = sm32 + (w >> 1) * 32;
  if (threadIdx.x < 128) sm32[threadIdx.x] = 0u;
  __syncthreads();
  unsigned tb = now10() + (L * (APN + 2) + 1 + gr) * GPER;
  const int rn1 = idx < 8 ? 96 + idx : 224 + idx;
  (void)rn1;
  if (L) {
    if (RSL) gate(now10() + RSL);
    const __amdgpu_buffer_rsrc_t rz = MKRS(ws + OFF_X2, SZ_X);
    for (int s = gr; s < NSEG; s += 2) ST128((v4u{0u, 0u, 0u, 0u}), rz, l * 16, (s * SEG + r) * DM * 2);
    DRAIN();
    if (l < 4) set_flag(FL + FL_FZ + (2 * l + (idx >> 3)) * 32 + (idx & 7) * 4 + (w & 3));
    if (l == 0) __hip_atomic_fetch_add((lu32*)(unsigned long)(lds_base(smem) + 480), 1u, __ATOMIC_RELAXED, __HIP_MEMORY_SCOPE_WORKGROUP);
  }
  for (unsigned spins = 0; sm32[120] < 4u && spins < 64u * SPIN_CAP; ++spins) __builtin_amdgcn_s_sleep(2);
  const int c = l & 15, g = l >> 4;
  const int pv = g * 32768 + (c >> 2) * 128 + (c & 3) * 16, xv = (16 * (g >> 1) + c) * 512 + (g & 1) * 8, ov = (g >> 1) * 256 + c * 16 + (g & 1) * 8;
  v4u sel0, sel1;
#pragma unroll
  for (int e = 0; e < 4; ++e) {
    const unsigned one = ((c & 7) >> 1) == e ? 0x3f80u << (16 * (c & 1)) : 0u;
    sel0[e] = c < 8 ? one : 0u;
    sel1[e] = c < 8 ? 0u : one;
  }
  int tries = SPIN_CAP;
  for (int s = gr; s < NSEG; s += 2) {
    const int gs = 64 * L + s;
    const unsigned* fw = FL + FL_FW + (gs * 4 + REP) * 4 * 32;
    const unsigned due = grp_wait<SLP_R, 4>((w & 1) == 0, sw, s + 1, fw, 16, TMO, tb);
    tb = due + (gs >= 125 - AWD || gs < RQ0 ? GATE_RT : 2 * GPER - (L ? GMS : GMR));
    {
      const unsigned t = now10(), e = (int)(due - t) > 0 ? due : t;
      __builtin_amdgcn_raw_buffer_store_b32((e + RDT) | 1u, MKRS(FL + FL_TW, r == 2 * RTW && !L ? 2048 : 0), l * 512, s * 4, AUX_SC1);
    }
#define RTS(k, t) TSR(L ? A.rn : rn1, k, s, (w & 6) * 64 + (t))
    RST(0);
    RTS(0, 0);
    RTS(4, 64);
    const int xo_ = L * (int)(OFF_X3 - OFF_XA) + s * 32768 + r * 16;
    const v2u xa = LD64(rsX, xv, xo_), xb = LD64(rsX, xv + 16384, xo_);
#pragma unroll 1
    for (int i = 0; i < 256; ++i) {
      const int d = (int)(due - now10());
      if (d <= 0) break;
      if (d > 8) __builtin_amdgcn_s_sleep(1);
    }
    if (L) {
      const unsigned g0 = sm32[124];
#pragma unroll 1
      for (int i = 0; i < RGW && g0 < NSEG && sm32[124] == g0; ++i) __builtin_amdgcn_s_sleep(1);
    }
    RST(1);
    f32x4 a0, a1;
    for (;; --tries) {
      v4u p[16];
#pragma unroll
      for (int k = 0; k < 16; ++k) {
        p[k] = LD128(rsW, pv + ((k & 1) + (k < 8 ? 2 : 0)) * 512, (gs * 16 + 2 * (k & 6)) * 32768 + (r >> 1) * 2048 + (r & 1) * 64);
        if ((k & 7) == 7) __builtin_amdgcn_sched_barrier(0);
      }
      if ((w & 5) == 1 && l == 0) __hip_atomic_fetch_max((lu32*)(unsigned long)(lds_base(smem) + 496), s + 1u, __ATOMIC_RELAXED, __HIP_MEMORY_SCOPE_WORKGROUP);
      __builtin_amdgcn_sched_barrier(0);
      a0 = f32x4{};
      a1 = f32x4{};
#pragma unroll
      for (int k = 0; k < 8; ++k) a1 = MFMA16(BF8((k & 1) ? sel1 : sel0), BF8(p[k]), a1);
#pragma unroll
      for (int k = 8; k < 16; ++k) a0 = MFMA16(BF8((k & 1) ? sel1 : sel0), BF8(p[k]), a0);
      if (!__any(__builtin_isunordered(a0[0], a1[0]) | __builtin_isunordered(a0[1], a1[1]) | __builtin_isunordered(a0[2], a1[2]) |
                 __builtin_isunordered(a0[3], a1[3])) || tries <= 0) break;
      asm volatile("s_sleep %0" ::"n"(SLP_P) : "memory");
    }
    RST(2);
    const unsigned nz = L ? 0u : 1u;
    const v4u xr = {xa.x, xa.y, xb.x, xb.y}, ar = {pk2(a0[0], a0[1]), pk2(a0[2], a0[3]), pk2(a1[0], a1[1]), pk2(a1[2], a1[3])};
    v4u xo;
#pragma unroll
    for (int e = 0; e < 4; ++e)
      xo[e] = __builtin_elementwise_max(pk2(lo16(xr[e]) + lo16(ar[e]), hi16(xr[e]) + hi16(ar[e])), nz);
    const __amdgpu_buffer_rsrc_t rsO = MKRS(L ? (char*)A.out : ws + OFF_X2, SZ_X);
    ST64((v2u{xo.x, xo.y}), rsO, ov, (s * SEG + r) * DM * 2);
    ST64((v2u{xo.z, xo.w}), rsO, ov + 512, (s * SEG + r) * DM * 2);
    RTS(1, 0);
    RST(3);
    RTS(5, 64);
    if (L == 0) {
      float sm = 0.f, sq = 0.f;
#pragma unroll
      for (int e = 0; e < 4; ++e) {
        const unsigned x = xo[e];
        sm = __builtin_amdgcn_fdot2_f32_bf16(__builtin_bit_cast(bf16x2_t, x), __builtin_bit_cast(bf16x2_t, 0x3f803f80u), sm, false);
        sq = dot2sq(x, sq);
      }
      float t = swap_add32(sm, sq);
      t += dppr<0x121>(t);
      t += dppr<0x122>(t);
      t += dppr<0x124>(t);
      t += dppr<0x128>(t);
      t = xsum16(t) * (1.0f / DM);
      const float ms = __builtin_bit_cast(float, __builtin_amdgcn_readlane(__builtin_bit_cast(int, t), 32));
      const float var = fmaxf(ms - t * t, 0.f);
      const unsigned st = pk2(t, __builtin_amdgcn_sqrtf(lo16(pk2(lo16(pk2(var, var)) + 1e-5f, 0.f))));
      if (l < 4) __hip_atomic_store((gu32*)(FL + FL_FR + ((s * 4 + l) * 4 + (r >> 3)) * 32 + (r & 7)), st, RLX_AGENT);
    }
    RTS(6, 64);
    RST(4);
    RTS(3, 0);
#undef RTS
  }
  RSD();
  if (idx == 0 && threadIdx.x == 384) {
    gu64* en = (gu64*)(ws + OFF_TAG + 8192);
    __hip_atomic_store(en, __hip_atomic_load(en, RLX_AGENT) + TAG_STEP, RLX_AGENT);
  }
  if (TOURS) asm volatile("s_endpgm\n\t.p2alignl 12, 0xbf800000\n.Lpad0xa0:\n\ts_setpc_b64 s[98:99]" ::: "memory");
}

constexpr int CUN = 2144;
constexpr int STN = 392;
__device__ __forceinline__ bf16x8 conv_bfrag(const unsigned* up, const unsigned sh) {
  const unsigned d0 = up[0], d1 = up[1], d2 = up[2], d3 = up[3], d4 = up[4];
  const v4u r = {__builtin_amdgcn_alignbit(d1, d0, sh), __builtin_amdgcn_alignbit(d2, d1, sh),
                 __builtin_amdgcn_alignbit(d3, d2, sh), __builtin_amdgcn_alignbit(d4, d3, sh)};
  return BF8(r);
}
__device__ __forceinline__ void role_conv(const MegaArgs& A, char* smem, const int cv) {
  __builtin_amdgcn_s_waitcnt(0x0F70);
  MARK();
  u16* Kr = reinterpret_cast<u16*>(smem);
  u16* Uc = Kr + 8 * 2 * CKN;
  u16* To = Uc + 8 * 2 * CUN;
  u16* Sx = To + 32 * 8;
  u16* St = Sx + 32 * 8;
  const int tid = threadIdx.x;
  const int w = __builtin_amdgcn_readfirstlane(tid >> 6);
  const int l = tid & 63, c = l & 15, g = l >> 4;
  const int d = 8 * cv + w;
  char* ws = A.ws;
  unsigned* FL = (unsigned*)(ws + OFF_FL);
  unsigned* TMO = FL + FL_TMO;
  u16* kr = Kr + w * 2 * CKN;
  float* prm = reinterpret_cast<float*>(kr);
  v4u tx[4];
  LST(16);
  const v2u en = LD64(MKRS(ws + OFF_TAG, 8192 + 8), 0, 8192);
  {
    const unsigned pa = *reinterpret_cast<const unsigned*>(A.logAre + d * NST + 2 * l);
    const unsigned pb = *reinterpret_cast<const unsigned*>(A.Aim + d * NST + 2 * l);
    const unsigned pc = *reinterpret_cast<const unsigned*>(A.Cre + d * NST + 2 * l);
    const unsigned pd = *reinterpret_cast<const unsigned*>(A.Cim + d * NST + 2 * l);
    const float dt = bfr(expf(bf2f(A.logdt[d])));
    tx[0] = __builtin_amdgcn_raw_buffer_load_b128(MKRS(A.x0 + 8 * cv, 128 * DM * 2), tid * (DM * 2), 0, 0);
    tour<193, 200, 8>(w);
    reinterpret_cast<unsigned*>(Uc + w * CUN + ((l & 32) ? 32 + 512 : 0))[(l & 32) ? l & 31 : c] = 0u;
    reinterpret_cast<unsigned*>(Uc + w * CUN + 32 + ((l & 32) ? L_SEQ : 128))[l & 31] = 0u;
    if (l < 50) {
      reinterpret_cast<v4u*>(kr + 1808)[l] = v4u{0u, 0u, 0u, 0u};
      reinterpret_cast<v4u*>(kr + CKN + 1808)[l] = v4u{0u, 0u, 0u, 0u};
    }
#pragma unroll
    for (int e = 0; e < 2; ++e) {
      const float ar = -bfr(expf(e ? hi16(pa) : lo16(pa))) * dt, ai = (e ? hi16(pb) : lo16(pb)) * dt;
      const float ex = expf(ar);
      float sn, cs;
      sincos1(ai, &sn, &cs);
      if (e == 1) pad<187, 1050>(sn);
      prm[2 * l + e] = ar;
      prm[NST + 2 * l + e] = ai;
      prm[2 * NST + 2 * l + e] = (e ? hi16(pc) : lo16(pc)) * dt;
      prm[3 * NST + 2 * l + e] = (e ? hi16(pd) : lo16(pd)) * dt;
      prm[4 * NST + 2 * l + e] = ex * cs;
      prm[5 * NST + 2 * l + e] = ex * sn;
    }
  }
  LST(21);
  const float dsk = bf2f(A.Dskip[d]);
  const u16* ka = kr + (c >> 3) * CKN + 2080 - 16 * (c & 7) + 8 * g;
  const unsigned ush = (c & 1) * 16;
#define KFRAG(D0) (*reinterpret_cast<const bf16x8*>(ka - (D0)))
  const __amdgpu_buffer_rsrc_t rz1 = MKRS(ws + OFF_XA, SZ_X);
  const int t0 = 16 * (4 * (g & 1) + (g >> 1)) + c;
  {
    const u16* uc = Uc + w * CUN;
    const unsigned* ub = reinterpret_cast<const unsigned*>(uc + (c & ~1) + 8 * g + 32);
    const __amdgpu_buffer_rsrc_t rst = MKRS(ws + OFF_GT, L_SEQ * 4);
#define TILE_OUT(dst, av, T)                                                                   \
  {                                                                                            \
    const u16* u_ = Uc + w * CUN + 32 + 128 * (T) + t0;                                        \
    const unsigned uu = pk2(bf2f(u_[0]) * dsk, bf2f(u_[32]) * dsk);                            \
    const unsigned yy = pk2(swap_add32(av[0], av[1]), swap_add32(av[2], av[3]));               \
    const unsigned aa = pk2(lo16(yy) + lo16(uu), hi16(yy) + hi16(uu));                         \
    (dst)[t0] = (u16)aa;                                                                       \
    (dst)[t0 + 32] = (u16)(aa >> 16);                                                          \
  }
#define CHUNK_ST(ck, xr, src, n)                                                               \
  {                                                                                            \
    v4u o;                                                                                     \
    _Pragma("unroll") for (int e = 0; e < 4; ++e)                                              \
      o[e] = pk2(lo16(xr[e]) + bf2f((src)[(2 * e) * (n) + l]), hi16(xr[e]) + bf2f((src)[(2 * e + 1) * (n) + l])); \
    ST128(o, rz1, (l >> 5) * 32768 + (l & 31) * 16, (2 * (ck) * 64 + cv) * 512);               \
  }
#define U_ROW(row, xr_) U_ROW2(row, xr_, __builtin_amdgcn_raw_buffer_load_b32(rst, (row) * 4, 0, AUX_SC1))
#define U_ROW2(row, xr_, stv)                                                                       \
  {                                                                                            \
    const unsigned st_ = (stv);                                                                \
    const float mb_ = lo16(st_), ri_ = 1.0f / hi16(st_);                                       \
    _Pragma("unroll") for (int e = 0; e < 4; ++e) {                                            \
      const unsigned t_ = pk2(lo16(xr_[e]) - mb_, hi16(xr_[e]) - mb_);                         \
      const unsigned q_ = pk2(lo16(t_) * ri_, hi16(t_) * ri_);                                 \
      Uc[(2 * e) * CUN + 32 + (row)] = (u16)q_;                                                \
      Uc[(2 * e + 1) * CUN + 32 + (row)] = (u16)(q_ >> 16);                                    \
    }                                                                                          \
  }
    const unsigned* FS = FL + FL_FS + REP * 32 * 32;
    const int gi = 4 * (g * 8 + (l & 7));
    const int tl = 32 * ((l >> 3) & 1) + 8 * g + (l & 7);
    unsigned khl = 0u, fs = 0u, st0 = 0u;
    const int fo = (w + 8 * ((l >> 3) & 3)) * 32 + (l & 7);
#pragma unroll 1
    for (int ps = 0; ps < 2; ++ps) {
      float v[32];
      {
        const f32x4 ar = *reinterpret_cast<const f32x4*>(prm + gi), ai = *reinterpret_cast<const f32x4*>(prm + NST + gi);
        const f32x4 cr = *reinterpret_cast<const f32x4*>(prm + 2 * NST + gi), ci = *reinterpret_cast<const f32x4*>(prm + 3 * NST + gi);
        const f32x4 wr = *reinterpret_cast<const f32x4*>(prm + 4 * NST + gi), wi = *reinterpret_cast<const f32x4*>(prm + 5 * NST + gi);
        float p0 = (float)(64 * ps + 32 * ((l >> 3) & 1));
        pad<188, 1050>(p0);
        float yr0[4], yi0[4];
#pragma unroll
        for (int k = 0; k < 4; ++k) {
          const float e = __expf(ar[k] * p0);
          float sn, cs;
          __sincosf(ai[k] * p0, &sn, &cs);
          const float zr = e * cs, zi = e * sn;
          yr0[k] = cr[k] * zr - ci[k] * zi;
          yi0[k] = cr[k] * zi + ci[k] * zr;
        }
        RUN_START
#pragma unroll
        for (int i = 0; i < 32; ++i) {
          const f32x2_t sm2 = ya + yb;
          v[i] = sm2.x + sm2.y;
          if (i == 18) pad<189, 1000>(za);
          RUN_STEP
        }
      }
#pragma unroll
      for (int i = 0; i < 16; ++i) v[i] = swap_add32(v[i], v[i + 16]);
#pragma unroll
      for (int i = 0; i < 8; ++i) {
        if (i == 6) pad<190, 1000>(v[i]);
        v[i] = swap_add16(v[i], v[i + 8]);
        v[i] += dppf<0xB1>(0.f, v[i]);
        v[i] += dppf<0x4E>(0.f, v[i]);
        v[i] += dppf<0x141>(0.f, v[i]);
      }
      float s0 = v[0], s1 = v[1], s2 = v[2], s3 = v[3], s4 = v[4], s5 = v[5], s6 = v[6], s7 = v[7];
      asm volatile("" : "+v"(s0), "+v"(s1), "+v"(s2), "+v"(s3), "+v"(s4), "+v"(s5), "+v"(s6), "+v"(s7));
      s0 = (l & 4) ? s4 : s0; s1 = (l & 4) ? s5 : s1; s2 = (l & 4) ? s6 : s2; s3 = (l & 4) ? s7 : s3;
      s0 = (l & 2) ? s2 : s0; s1 = (l & 2) ? s3 : s1;
      const float kv = 2.0f * ((l & 1) ? s1 : s0);
      khl = pk2(kv, 0.f);
      khl = pk2(kv, kv - lo16(khl));
      kr[2080 - 64 * ps - tl] = (u16)khl;
      kr[CKN + 2080 - 64 * ps - tl] = (u16)(khl >> 16);
      SST(ps);
      LST(22 + 6 * ps);
      {
        if (ps == 0) {
          if (w < 2) {
            const unsigned long long m = (((unsigned long long)en.y << 32) | en.x) + TAG_STEP;
            v4u g0;
#pragma unroll 1
            for (unsigned spins = 0; spins < 16u * SPIN_CAP; ++spins) {
              g0 = LD128(MKRS(ws + OFF_G0, 8192), tid * 16, REP * 2048);
              if (__all(((g0.x ^ g0.z) | (g0.y ^ (unsigned)m) | (g0.w ^ (unsigned)(m >> 32))) == 0u)) break;
              asm volatile("s_sleep 2" ::: "memory");
            }
            LST(23);
            U_ROW2(tid, tx[0], g0.x);
          }
          __syncthreads();
          LST(24);
        }
        f32x4 a0t = {};
        pad<191, 1050>(a0t);
#pragma unroll
        for (int b = -1; b < 2; ++b) a0t = MFMA16(KFRAG(-32 * b), conv_bfrag(ub + 16 * b, ush), a0t);
        if (ps) {
#pragma unroll
          for (int b = 2; b < 4; ++b) a0t = MFMA16(KFRAG(-32 * b), conv_bfrag(ub + 16 * b, ush), a0t);
        }
        TILE_OUT(St + w * STN + 128 * ps, a0t, 0);
        __syncthreads();
        LST(25 + 6 * ps);
        if (w == ps) {
          CHUNK_ST(w, tx[0], St + 192 * w, STN);
          LST(26 + 6 * ps);
          pad<192, 900>();
          DRAIN();
          LST(27 + 6 * ps);
          if (ps == 0) {
            if (l < 4) __hip_atomic_store((gu64*)(ws + OFF_X0) + l * 128 + (cv >> 3) * 16 + (cv & 7), (((unsigned long long)en.y << 32) | en.x) + TAG_STEP, RLX_AGENT);
            zero_wait(ws);
          }
          if (l < 8) set_flag(FL + FL_FC + ((2 * w * 4 + l) * 8 + (cv >> 3)) * 32 + (cv & 7));
        }
        SST(4 + ps);
        if (ps == 0 && w >= 2) tx[0] = *reinterpret_cast<const v4u*>(A.x0 + (size_t)tid * DM + 8 * cv);
      }
#define FS_WAIT(o)                                                                             \
  {                                                                                            \
    _Pragma("unroll 1") for (unsigned spins = 0; !__all(fs != 0u) && !spin_timeout(spins, TMO); ++spins) { \
      __builtin_amdgcn_s_sleep(4);                                                             \
      fs = __hip_atomic_load((gu32*)FS + (o), RLX_AGENT);                                      \
    }                                                                                          \
    asm volatile("" ::: "memory");                                                             \
  }
    }
    pad<193, 950>();
    const gu32* kg = (gu32*)(FL + FL_KG + cv * 32 + w);
    fs = __hip_atomic_load((gu32*)FS + w * 32 + (l & 7), RLX_AGENT);
    unsigned kf = __hip_atomic_load(kg, RLX_AGENT);
    FS_WAIT(w * 32 + (l & 7));
    st0 = __builtin_amdgcn_raw_buffer_load_b32(rst, tid * 4, 0, AUX_SC1);
    SST(6);
    LST(0);
    const __amdgpu_buffer_rsrc_t rk = MKRS(ws + OFF_KF + (size_t)d * 2 * CKN * 2, 2 * CKN * 2);
    v4u t[8];
    if (__builtin_amdgcn_readfirstlane(kf) == 0u) (void)wait_n<8, 1>(FL + FL_KG + cv * 32 + w, 1, TMO);
    asm volatile("" ::: "memory");
    LST(1);
#pragma unroll
    for (int i = 0; i < 4; ++i) {
      t[i] = LD128(rk, (64 * i + l) * 16, 0);
      t[4 + i] = LD128(rk, (64 * i + l) * 16, CKN * 2);
    }
    pad<195, 800>();
    if (w >= 2) U_ROW2(tid, tx[0], st0);
    unsigned* f1 = FL + FL_FC + (64 * w + (cv >> 3)) * 32 + (cv & 7);
    SST(7);
    LST(4);
    {
#pragma unroll
      for (int i = 0; i < 4; ++i)
        if (64 * i + l < 245) {
          reinterpret_cast<v4u*>(kr)[64 * i + l] = t[i];
          reinterpret_cast<v4u*>(kr + CKN)[64 * i + l] = t[4 + i];
        }
      kr[2080 - 64 - tl] = (u16)khl;
      kr[CKN + 2080 - 64 - tl] = (u16)(khl >> 16);
      if (l < 33) kr[l] = kr[CKN + l] = 0;
    }
    pad<196, 1050>();
    RAW_BAR();
    LST(5);
#pragma unroll
    for (int i = 1; i < 4; ++i) tx[i] = *reinterpret_cast<const v4u*>(A.x0 + (size_t)(tid + 512 * i) * DM + 8 * cv);
    fs = __hip_atomic_load((gu32*)FS + fo, RLX_AGENT);
    {
      f32x4 a1 = {};
#pragma unroll
      for (int b = -1; b < 8; ++b) a1 = MFMA16(KFRAG(128 - 32 * b), conv_bfrag(ub + 16 * b, ush), a1);
      pad<197, 700>(a1);
      TILE_OUT(St + w * STN, a1, 1);
    }
    LST(2);
    RAW_BAR();
    if ((w >> 1) == 1) {
      CHUNK_ST(w, tx[0], St + 64 * w - 128, STN);
      DRAIN();
      if (l < 8) set_flag(f1 + l * 256);
    }
    LST(3);
    {
      f32x4 a2 = {}, a3 = {};
#pragma unroll
      for (int b = -1; b < 16; ++b) {
        const bf16x8 bf = conv_bfrag(ub + 16 * b, ush);
        if (b < 12) a2 = MFMA16(KFRAG(256 - 32 * b), bf, a2);
        a3 = MFMA16(KFRAG(384 - 32 * b), bf, a3);
        if (b == 4) pad<198, 700>(a3);
      }
      pad<199, 1100>(a3);
      TILE_OUT(St + w * STN + 128, a2, 2);
      TILE_OUT(St + w * STN + 256, a3, 3);
    }
    SST(10);
    LST(6);
    RAW_BAR();
    if (w >= 4) {
      CHUNK_ST(w, tx[0], St + 64 * w - 128, STN);
      DRAIN();
      if (l < 8) set_flag(f1 + l * 256);
    }
    FS_WAIT(fo);
#undef FS_WAIT
#pragma unroll
    for (int i = 1; i < 4; ++i) {
      if (i == 2) pad<194, 1100>(tx[i]);
      U_ROW(tid + 512 * i, tx[i]);
    }
    if (w < 4) tour<176, 180, 4>(w);
    if (w == 1) (void)(wait_n<SLP_V, 32>(FL + FL_FZ + REP * 64, 64, TMO));
    SST(9);
    LST(9);
    LSD();
#undef U_ROW
#undef U_ROW2
#undef TILE_OUT
#undef CHUNK_ST
  }
  const unsigned sb = lds_base(smem);
  const unsigned u1 = sb + (16 * CKN + w * CUN) * 2, u2 = u1 + 16 * CUN, to = sb + (16 * CKN + 16 * CUN) * 2, st = to + 1024;
  const unsigned win = ((c & ~1) + 8 * g) * 2, row = 16 * (g >> 1) + c;
  const bool nil = c < 8 && g < 2;
  if (l < 16) reinterpret_cast<unsigned*>(Uc + (8 + w) * CUN)[l] = 0u;
  bf16x8 th, tw;
#pragma unroll
  for (int i = 0; i < 8; ++i) {
    th[i] = (short)kr[2064 - c + 8 * g + i];
    tw[i] = (short)kr[CKN + 2064 - c + 8 * g + i];
  }
  v4u known;
#pragma unroll
  for (int e = 0; e < 4; ++e) known[e] = (c + 8 * g + 2 * e < 32 ? 0xffffu : 0u) | (c + 8 * g + 2 * e < 31 ? 0xffff0000u : 0u);
  const bf16x8 kz = KFRAG(-96);
  const v8u pa = {u2 + (nil ? 0 : c < 8 ? 32 + 16 * g : 64 + 16 * g), nil ? 0u : 64u, u2 + 64 + 2 * row, to + 16 * row + 2 * w, u2 + win,
                  sb + (unsigned)((w * 2 + (c >> 3)) * CKN + 2080 - 16 * (c & 7) + 8 * g) * 2 - 3904, ush, (unsigned)l * 16u};
  const unsigned b1 = u1 + win - 3072;
  const v8u pb = {b1, b1 + 4, b1 + 8, b1 + 12, b1 + 16, u1 + (32 + 512 + t0) * 2, st + (w * STN + t0) * 2, st + (64 * (w & 1) + l) * 2};
  const v4u pc = {(unsigned)((l >> 5) * 32768 + (l & 31) * 16), (unsigned)((l >> 1) * (DM * 2) + (l & 1) * 8), (unsigned)((l >> 4) * 128 + ((l >> 1) & 7) * 4),
                  sb + (16 * CKN + (8 + 4 * (l & 1)) * CUN + 32 + (l >> 1)) * 2};
  const char* dbg = ws;
  asm volatile("s_mov_b64 exec, -1\ns_nop 4\n" CONV_ASM
               ::"{v[0:7]}"(pa), "{v[8:11]}"(known), "{v[12:15]}"(th), "{v[16:19]}"(tw), "{v[24:27]}"(kz), "{v[28:35]}"(pb), "{v[36:39]}"(pc),
               "{v40}"(to + (w ? l * 8 : (l & 31) * 16)), "{v[44:47]}"(tx[1]), "{v[48:51]}"(tx[2]), "{v[52:55]}"(tx[3]),
               "{s[40:43]}"(mk_srd(ws + OFF_X2, SZ_X)), "{s[44:47]}"(mk_srd(FL + FL_FR, 64 * 16 * 128 + 2048)), "{s71}"(64 * 16 * 128 + REP * 512), "{s[48:51]}"(mk_srd(ws + OFF_X3, SZ_X)),
               "{s[52:55]}"(mk_srd(ws + OFF_XA, SZ_X)), "{s[58:59]}"(FL + FL_FC + (64 * 32 + (cv >> 3)) * 32 + (cv & 7)),
               "{s[60:61]}"(FL + FL_FC + (64 * (w + 8) + (cv >> 3)) * 32 + (cv & 7)), "{s62}"(0), "{s63}"(w),
               "{s64}"(__builtin_amdgcn_readfirstlane(__float_as_uint(dsk))), "{s65}"(cv * 16), "{s66}"(REP * 512), "{s67}"(cv * 512),
               "{s68}"((w + 8) * 65536 + cv * 512), "{s69}"(now10()), "{s[72:73]}"(0x0000ffff0000ffffull), "{s[36:37]}"(dbg),
               [lt] "n"(-CVLT), [gv] "n"(GATE_V), [sv] "n"(SLP_V)
               : "memory", "vcc", "scc", "s81", "v255");
#undef KFRAG
}

__device__ __forceinline__ v4u ln_row_packed(const v4u xr) {
  float v[8];
#pragma unroll
  for (int e = 0; e < 4; ++e) { v[2 * e] = lo16(xr[e]); v[2 * e + 1] = hi16(xr[e]); }
  float s = ((v[0] + v[1]) + (v[2] + v[3])) + ((v[4] + v[5]) + (v[6] + v[7]));
  s = wave_sum(s);
  const float mean = s * (1.0f / DM);
  float q = 0.f;
#pragma unroll
  for (int j = 0; j < 8; ++j) { float t = v[j] - mean; q += t * t; }
  q = wave_sum(q);
  const float var = q * (1.0f / DM);
  const float mb = bfr(mean);
  const float sd = bfr(sqrtf(bfr(bfr(var) + 1e-5f)));
  const float ri = 1.0f / sd;
  v4u o;
#pragma unroll
  for (int e = 0; e < 4; ++e) {
    const unsigned t = pk2(v[2 * e] - mb, v[2 * e + 1] - mb);
    const float x0 = lo16(t), x1 = hi16(t);
    float q0 = x0 * ri, q1 = x1 * ri;
    q0 = __builtin_fmaf(__builtin_fmaf(-q0, sd, x0), ri, q0);
    q1 = __builtin_fmaf(__builtin_fmaf(-q1, sd, x1), ri, q1);
    o[e] = pk2(q0, q1);
  }
  return o;
}

__device__ __forceinline__ float bfr1(float x) { return lo16(pk2(x, x)); }
__device__ __forceinline__ void ln4_packed(v4u (&x)[4]) {
  f32x2_t v[16];
#pragma unroll
  for (int e = 0; e < 16; ++e) v[e] = f32x2_t{lo16(x[e >> 2][e & 3]), hi16(x[e >> 2][e & 3])};
  const f32x2_t t = (((v[0] + v[1]) + (v[2] + v[3])) + ((v[4] + v[5]) + (v[6] + v[7]))) +
                    (((v[8] + v[9]) + (v[10] + v[11])) + ((v[12] + v[13]) + (v[14] + v[15])));
  const float mean = red_mod4(t.x + t.y) * (1.0f / DM);
  const float mb = bfr1(mean);
  f32x2_t q0 = {0.f, 0.f}, q1 = {0.f, 0.f};
#pragma unroll
  for (int e = 0; e < 16; e += 2) {
    v[e] -= mb;
    v[e + 1] -= mb;
    q0 += v[e] * v[e];
    q1 += v[e + 1] * v[e + 1];
  }
  q0 += q1;
  const float dm = mean - mb;
  const float var = fmaxf(red_mod4(q0.x + q0.y) * (1.0f / DM) - dm * dm, 0.f);
  const float sd = bfr1(sqrtf(bfr1(bfr1(var) + 1e-5f)));
  const float ri = 1.0f / sd;
#pragma unroll
  for (int e = 0; e < 16; ++e) {
    const unsigned t2 = pk2(v[e].x, v[e].y);
    const f32x2_t q = f32x2_t{lo16(t2), hi16(t2)} * ri;
    x[e >> 2][e & 3] = pk2(q.x, q.y);
  }
}

extern "C" __device__ float mul1(float, float) __asm("llvm.amdgcn.fmul.legacy");
template <bool G0>
__device__ __forceinline__ void role_qkv_t(const MegaArgs& A, char* smem, const int L, const int h) {
  __builtin_amdgcn_s_waitcnt(0x0F70);
  MARK();
  const int w = __builtin_amdgcn_readfirstlane(threadIdx.x >> 6);
  char* ws = A.ws;
  unsigned* FL = (unsigned*)(ws + OFF_FL);
  unsigned* TMO = FL + FL_TMO;
#define LANE_IDS()                         \
  const int l = lane_id();                 \
  const int c = l & 15, g = l >> 4;        \
  (void)c; (void)g;
  unsigned* FC = FL + FL_FC + (L * 64 * 4 + REP) * 8 * 32;
  bf16x8 wa[16], wb[16], wv[16];
  const char* Xb = ws + (L ? OFF_X3 : OFF_XA) + (w & 3) * 8192;
  const v4u rsF = mk_srd(FC + (w & 3) * 64, (64 * 32 - REP * 8 - 2 * (w & 3)) * 128);
  constexpr unsigned Q_R = 131072;
  bool zs, got = false;
  v2u n0 = {};
  f32x4 cs[3] = {};
  PSV;
  {
    LANE_IDS();
    const unsigned sb = lds_base(smem);
    const int q = (l >> 2) & 3, p = l & 3, f = q | ((g & 1) << 2);
    const unsigned r0 = sb + (g >> 1) * 16384 + (8 * (g & 1) + q) * 256;
    const unsigned ra = r0 + (w ^ f) * 32 + p * 8, rb = r0 + ((2 * (w & 3) + (p >> 1)) ^ f) * 32 + (p & 1) * 16;
    const v4u one = {0x3f803f80u, 0x3f803f80u, 0x3f803f80u, 0x3f803f80u};
    v4u zt;
    v2u ze;
    unsigned txt = 0u;
#define WP(k) (WM(k) == 0 ? A.Wv : WM(k) == 1 ? A.Wk : A.Wq)
#define W_READ(W, a_)                                    \
  {                                                      \
    const v2u x0 = trd(a_), x1 = trd((a_) + 1024);       \
    W = BF8((v4u{x0.x, x0.y, x1.x, x1.y}));              \
  }
    if (QSL && L) gate(now10() + QSL);
    PST(0);
#pragma unroll
    for (int k = 0; k < 12; ++k) w_issue<false>(WP(k >> 2), WJ(k >> 2), k >> 2, k & 3, h, w, l, sb);
    PST(1);
    constexpr int PB = G0 ? 140 : 160;
    tour<PB + 5, PB + 12, 4>(w);
#pragma unroll
    for (int k = 0; k < 12; ++k) {
      const int j = k & 3;
      if (k == (G0 ? 3 : 3)) pad<PB + 5>(cs[2]);
      if (k == (G0 ? 5 : 7)) pad<PB + 6>(cs[2]);
      if (k == (G0 ? 11 : 9)) pad<PB + 7>(cs[2]);
      if (k == (G0 ? 7 : 11)) pad<PB + 1, 700>(cs[2]);
      if (k == ZK + 1) wait_vm<10>();
      else if (k < 10) wait_vm<8>();
      else if (k == 10) wait_vm<G0 && ZK < 8 ? 5 : 4>();
      else wait_vm<G0 ? 1 : 0>();
      RAW_BAR();
      if (k == IPQ) txt = pull_text((h & 7) + 8 * w);
      if (k == IPQ + 2) asm volatile("" ::"v"(txt));
      if (k == ZK)
        asm volatile("s_nop 4\n\tglobal_load_dwordx4 %0, %2, %3 sc1\n\tglobal_load_dwordx2 %1, %4, %5 sc1"
                     : "=&v"(zt), "=&v"(ze)
                     : "v"(l * 16), "s"(ws + OFF_TAG + (blockIdx.x & 7) * 1024), "v"(0), "s"(ws + OFF_TAG + 8192)
                     : "memory");
#define X_LOOK() asm volatile("s_nop 4\n\tglobal_load_dwordx2 %0, %1, %2 sc1" : "=v"(n0) : "v"((l & 7) * 8 + (l & 8) * 16), "s"(ws + OFF_X0 + REP * 1024 + w * 256) : "memory")
#define X_SEEN() zero_seen(v4u{n0.x, n0.y, n0.x, n0.y}, ze)
      if (k == ZK + 2) {
        asm volatile("" : "+v"(zt), "+v"(ze));
        zs = zero_seen(zt, ze);
        if (G0) X_LOOK();
      }
#define X_ASK()                                                                                                \
  {                                                                                                            \
    asm volatile("" : "+v"(n0));                                                                               \
    got = !L && X_SEEN();                                                                                      \
    if (__builtin_expect(got, 1)) {                                                                            \
      _Pragma("unroll 1") for (int i = 0; i < 8192; i += 1024) glds16c(Xb + i, l * 16, sb + Q_R + w * 8192 + i); \
    }                                                                                                          \
  }
      if (k == 11 && G0) {
        WAIT_VM(0);
        X_ASK();
        X_LOOK();
      }
#pragma unroll
      for (int i = 0; i < 4; ++i) {
        if (k < 9) w_issue<false>(WP(k + 3), WJ(k + 3), (k + 3) & 3, i, h, w, l, sb);
        if (WM(k) == 0) W_READ(wv[4 * WJ(k) + i], ra + j * 4096 + i * 32768);
        if (WM(k) == (G0 ? 1 : 2)) {
          W_READ(wa[4 * WJ(k) + i], rb + j * 4096 + i * 32768);
          W_READ(wb[4 * WJ(k) + i], rb + 8 + j * 4096 + i * 32768);
        }
        if (k > 0 && WM(k - 1) == 0) cs[2] = MFMA16(BF8(one), wv[4 * WJ(k - 1) + i], cs[2]);
        if (k > 0 && WM(k - 1) == (G0 ? 1 : 2)) {
          cs[0] = MFMA16(BF8(one), wa[4 * WJ(k - 1) + i], cs[0]);
          cs[1] = MFMA16(BF8(one), wb[4 * WJ(k - 1) + i], cs[1]);
        }
        asm volatile("" : "+v"(cs[0]), "+v"(cs[1]), "+v"(cs[2]));
      }
      if (k == 1) PST(2);
      if (k == 3) PST(3);
      if (k == 5) PST(4);
      if (k == 7) PST(5);
      if (k == 9) PST(6);
    }
    if (got) PST(11);
#pragma unroll
    for (int i = 12; i < 16 && !G0; ++i) {
      cs[0] = MFMA16(BF8(one), wa[i], cs[0]);
      cs[1] = MFMA16(BF8(one), wb[i], cs[1]);
    }
    asm volatile("s_nop 7" : "+v"(cs[0]), "+v"(cs[1]));
    RAW_BAR();
    PST(8);
    if (G0 && __builtin_expect(!got, 1)) {
      WAIT_VM(0);
      X_ASK();
    }
    if (got) PST(12);
#undef X_LOOK
#undef X_SEEN
#undef X_ASK
#undef W_READ
#undef WP
  }
  WSTAMP2();
  constexpr int Q_X = 98304;
  const unsigned sb = lds_base(smem);
  v2u ea, eb, ev;
  {
    LANE_IDS();
    v2u e[3];
#pragma unroll
    for (int i = 0; i < 3; ++i) {
      const float x = g ? 0.f : cs[i][0], x1 = x - bfr1(x);
      e[i] = v2u{pk2(x, x1), pk2(x1 - bfr1(x1), 0.f)};
    }
    ea = e[0]; eb = e[1]; ev = e[2];
    asm volatile("" : "+v"(ea), "+v"(eb), "+v"(ev));
    if (__builtin_expect(!zs, 0)) zero_wait(ws);
    *reinterpret_cast<v2u*>(smem + Q_X + w * 2048 + (l >> 5) * 16384 + (l & 31) * 8) = v2u{0x3f803f80u, 0x3f803f80u};
  }
  pad<G0 ? 148 : 168>();
  constexpr int Q_ST = Q_X + 8 * 2048 + 256, Q_CNT = Q_X + 11 * 2048 + 256;
  *(lu32*)(unsigned long)(sb + Q_CNT) = 0u;
  v8u pv, wr[24];
  v4u pw;
  unsigned sw;
  {
    LANE_IDS();
    const unsigned fa = sb + Q_R + g * 512 + c * 16;
    pv = v8u{ea.x, ea.y, eb.x, eb.y, ev.x, ev.y, (unsigned)l * 16u, fa};
    pw = v4u{sb + Q_ST + c * 4, sb + Q_ST + g * 16,
             G0 ? (l & 7) * 4 + (l & 8) * 16 : (c & 8) ? sb + Q_X + ((w + 1) & 1) * 128 : fa + (w & 3) * 128, (c & 8) ? 0u : Q_R};
    sw = sb + Q_ST + (w & 3) * 32 + (l & 7) * 4;
#pragma unroll
    for (int i = 0; i < 8; ++i) {
      wr[i] = __builtin_shufflevector(__builtin_bit_cast(v4u, wa[2 * i]), __builtin_bit_cast(v4u, wa[2 * i + 1]), 0, 1, 2, 3, 4, 5, 6, 7);
      wr[8 + i] = __builtin_shufflevector(__builtin_bit_cast(v4u, wb[2 * i]), __builtin_bit_cast(v4u, wb[2 * i + 1]), 0, 1, 2, 3, 4, 5, 6, 7);
      wr[16 + i] = __builtin_shufflevector(__builtin_bit_cast(v4u, wv[2 * i]), __builtin_bit_cast(v4u, wv[2 * i + 1]), 0, 1, 2, 3, 4, 5, 6, 7);
    }
  }
  const v4u rsQ = mk_srd(ws + OFF_QK + L * SZ_QK, SZ_QK), rsV = mk_srd(ws + OFF_VF + L * SZ_O, SZ_O);
  const v4u rsM = mk_srd(ws + (G0 ? OFF_MEMK : OFF_MEMV) + L * SZ_MEM, SZ_MEM);
  const unsigned* FQ = FL + FL_FQ + (L * 65 * 32 + h) * 32;
  const char* dbg = ws;
  PST(9);
  __builtin_amdgcn_s_waitcnt(0x0F70);
  asm volatile("" ::"v"(n0));
  RAW_BAR();
  PST(10);
  PSD();
#define QKV_OPS                                                                                                              \
  ::"{v[64:71]}"(wr[0]), "{v[72:79]}"(wr[1]), "{v[80:87]}"(wr[2]), "{v[88:95]}"(wr[3]), "{v[96:103]}"(wr[4]),                \
      "{v[104:111]}"(wr[5]), "{v[112:119]}"(wr[6]), "{v[120:127]}"(wr[7]), "{v[128:135]}"(wr[8]), "{v[136:143]}"(wr[9]),      \
      "{v[144:151]}"(wr[10]), "{v[152:159]}"(wr[11]), "{v[160:167]}"(wr[12]), "{v[168:175]}"(wr[13]), "{v[176:183]}"(wr[14]), \
      "{v[184:191]}"(wr[15]), "{v[192:199]}"(wr[16]), "{v[200:207]}"(wr[17]), "{v[208:215]}"(wr[18]), "{v[216:223]}"(wr[19]), \
      "{v[224:231]}"(wr[20]), "{v[232:239]}"(wr[21]), "{v[240:247]}"(wr[22]), "{v[248:255]}"(wr[23]), "{v[0:7]}"(pv),        \
      "{v[8:11]}"(pw), "{v62}"(sw), "{s[40:43]}"(rsQ), "{s[44:47]}"(rsV), "{s[48:51]}"(rsM), "{s[52:55]}"(rsF),              \
      "{s[56:57]}"(Xb), "{s58}"(sb + Q_R + (w & 3) * 8192), "{s82}"(sb + Q_CNT + 16 + (w & 3) * 4), "{s69}"(__builtin_amdgcn_readfirstlane((int)got)), "{s59}"((h * 16 + (G0 ? 8 : 0) + (w & 3)) * 1024), "{s60}"((h * 8 + w) * 1024), \
      "{s61}"(h * 2048 + (w & 3) * 512), "{s62}"(0), "{s63}"(L ? 0 : QFAST), "{s[64:65]}"(FQ), "{s66}"(NSEG - 1), "{s67}"(0),            \
      "{s[70:71]}"(0xaaaaaaaaaaaaaaaaull), "{s[72:73]}"(0xccccccccccccccccull), "{s78}"(w), "{s80}"(sb + Q_CNT), "{s[84:85]}"(FL + FL_S0 + L * 512 + (h >> 3) * 32 + (h & 7)),             \
      "{s[36:37]}"(dbg), [gq] "n"(GATE_Q), [sq] "n"(SLP_Q), [s1] "n"(SLP_Q1 - SLP_Q)                                         \
      : "memory", "vcc", "scc", "s81", "v63"
  if (G0) asm volatile("s_mov_b64 exec, -1\ns_nop 4\n" QKV_ASM_0 QKV_OPS);
  else asm volatile("s_mov_b64 exec, -1\ns_nop 4\n" QKV_ASM_1 QKV_OPS);
#undef QKV_OPS
#undef LANE_IDS
}
__device__ __forceinline__ void role_qkv(const MegaArgs& A, char* smem, const int L, const int h) {
  if (__builtin_amdgcn_readfirstlane(threadIdx.x >> 6) < 4) role_qkv_t<true>(A, smem, L, h);
  else role_qkv_t<false>(A, smem, L, h);
}

constexpr int MEGA_WGS = 256;
constexpr size_t MEGA_LDS = 163840;
static_assert(MEGA_LDS <= 163840 && MEGA_LDS >= 162816 + 256, "lds");
static_assert(AW_SO + 49152 <= MEGA_LDS, "lds");
static_assert(MEGA_LDS >= (size_t)(8 * 2 * CKN + 8 * 2 * CUN + 2 * 32 * 8 + 8 * STN) * 2, "lds");
static_assert(MEGA_LDS >= (size_t)2 * 32 * SMS * 2 && MEGA_LDS >= 131072, "lds");
static_assert(131072 >= (size_t)(40 * NST + 32 * 400) * 4, "lds");
__global__ __launch_bounds__(512, 2) void mega_kernel(const u16* mem0, const u16* Dskip, char* ws, u16* out, const u16* x0,
                                                      const u16* logAre, const u16* Aim, const u16* Cre, const u16* Cim,
                                                      const u16* logdt, const u16* Wq, const u16* Wk, const u16* Wv,
                                                      const u16* Wo) {
  extern __shared__ __attribute__((aligned(16))) char smem[];
  if (TOURS & 4) {
    const unsigned cls = (0xfdac >> (((blockIdx.x & 1) * 4 + (blockIdx.x >> 6)) * 2)) & 3;
#define TO(a, b, c, d, e, f, g, h) "s_branch .Lpad" #a "\n\ts_branch .Lpad" #b "\n\ts_branch .Lpad" #c "\n\ts_branch .Lpad" #d "\n\ts_branch .Lpad" #e \
                                   "\n\ts_branch .Lpad" #f "\n\ts_branch .Lpad" #g "\n\ts_branch .Lpad" #h "\n\t"
    asm volatile("s_load_dword s94, %1, 0\n\ts_load_dword s95, %1, 64\n\ts_getpc_b64 s[96:97]\n\ts_add_u32 s96, s96, %0\n\ts_addc_u32 s97, s97, 0\n\ts_swappc_b64 s[98:99], s[96:97]\n\ts_branch 2f\n\t"
                 TO(0xc8, 0x78, 0x64, 0x79, 0x65, 0x81, 0x6d, 0x6e) TO(0xc8, 0xa0, 0x8c, 0xa1, 0x8d, 0xa9, 0x95, 0x97)
                 TO(0xc8, 0xba, 0xbb, 0xbc, 0xbd, 0xbe, 0xbf, 0xc0) TO(0xc9, 0xca, 0xcb, 0xcc, 0xcd, 0xce, 0xcf, 0xc8) "2:\n\ts_waitcnt lgkmcnt(0)"
                 ::"s"(cls * 32 + (__builtin_amdgcn_readfirstlane(threadIdx.x) >> 4) + 16), "s"(__builtin_amdgcn_kernarg_segment_ptr()) : "s94", "s95", "s96", "s97", "s98", "s99", "scc");
#undef TO
  }
  const int b = __builtin_amdgcn_readfirstlane(role_no());
  const MegaArgs A{mem0, Dskip, ws, out, x0, logAre, Aim, Cre, Cim, logdt, Wq, Wk, Wv, Wo, b};
  {
    const int hi = (b >= 32 && b < 96) ? b - 32 : (b >= 200 && b < 248) ? b - 136 : (b >= 96 && b < 104) ? b + 16 : (b >= 248 ? b - 128 : -1);
    if (hi >= 0) start_kgen(A, smem, hi, b < 248 - XI0 + 8 ? b - 248 + XI0 : -1, (b >= 64 && b < 104) || (b >= 232 && b < 240) ? b & 15 : -1);
  }
  if ((EXP_OFF & 1) && b >= 64 && b < 80) return;
  if ((EXP_OFF & 8) && b >= 200 && b < 232) return;
  if ((EXP_OFF & 16) && b >= 32 && b < 64) return;
  if ((EXP_OFF & 32) && b >= 80 && b < 96) return;
  if (b < 64) role_chain(A, smem, b >> 5, b & 31, 0);
  else if (b < 104 || (b >= 232 && b < 240)) role_aw(A, smem, b < 80 ? 0 : b < 96 ? 2 : 1, b & 15);
  else if (b < 168) role_conv(A, smem, b - 104);
  else if (b < 232) role_qkv(A, smem, (b - 168) >> 5, (b - 168) & 31);
  else role_red(A, smem, b - 240);
}

extern "C" void kernel_launch(void* const* d_in, const int* in_sizes, int n_in, void* d_out, int out_size, void* d_ws,
                              size_t ws_size, hipStream_t stream) {
  MegaArgs A;
  A.x0 = (const u16*)d_in[0];
  A.logAre = (const u16*)d_in[1];
  A.Aim = (const u16*)d_in[2];
  A.Cre = (const u16*)d_in[3];
  A.Cim = (const u16*)d_in[4];
  A.logdt = (const u16*)d_in[5];
  A.Dskip = (const u16*)d_in[6];
  A.mem0 = (const u16*)d_in[8];
  A.Wq = (const u16*)d_in[9];
  A.Wk = (const u16*)d_in[10];
  A.Wv = (const u16*)d_in[11];
  A.Wo = (const u16*)d_in[12];
  A.ws = (char*)d_ws;
  A.out = (u16*)d_out;

  (void)hipFuncSetAttribute((const void*)mega_kernel, hipFuncAttributeMaxDynamicSharedMemorySize, (int)MEGA_LDS);
  mega_kernel<<<MEGA_WGS, 512, MEGA_LDS, stream>>>(A.mem0, A.Dskip, A.ws, A.out, A.x0, A.logAre, A.Aim, A.Cre, A.Cim, A.logdt,
                                                   A.Wq, A.Wk, A.Wv, A.Wo);
}
```

```cpp
#include <hip/hip_runtime.h>
#include <hip/hip_bf16.h>
#include <stdint.h>

constexpr int L_SEQ = 2048;
constexpr int DM    = 512;
constexpr int NST   = 128;
constexpr int NH    = 32;
constexpr int DH    = 128;
constexpr int HD    = NH * DH;
constexpr int MM    = 8;
constexpr int SEG   = 32;
constexpr int NSEG  = L_SEQ / SEG;
constexpr int LK    = MM + SEG;

typedef unsigned short u16;
using bf16x8 = __attribute__((ext_vector_type(8))) short;
using f32x4  = __attribute__((ext_vector_type(4))) float;
using f32x16 = __attribute__((ext_vector_type(16))) float;

static __device__ __forceinline__ u16 f2bf(float f) {
  unsigned u = __float_as_uint(f);
  u += 0x7fffu + ((u >> 16) & 1u);
  return (u16)(u >> 16);
}
static __device__ __forceinline__ float bf2f(u16 h) { return __uint_as_float(((unsigned)h) << 16); }
static __device__ __forceinline__ float bfr(float f) { return bf2f(f2bf(f)); }

constexpr size_t SZ_X      = (size_t)L_SEQ * DM * 2;
constexpr size_t OFF_KF    = 0;
constexpr size_t OFF_UT    = OFF_KF + (size_t)DM * L_SEQ * 4;
constexpr size_t OFF_YT    = OFF_UT + SZ_X;
constexpr size_t OFF_XA    = OFF_YT + SZ_X;
constexpr size_t OFF_H     = OFF_XA + SZ_X;
constexpr size_t OFF_X2    = OFF_H + SZ_X;
constexpr size_t OFF_U2    = OFF_X2 + SZ_X;
constexpr size_t OFF_X3    = OFF_U2 + SZ_X;
constexpr size_t SZ_QK     = (size_t)L_SEQ * 2 * HD * 2;
constexpr size_t OFF_QK    = OFF_X3 + SZ_X;
constexpr size_t SZ_O      = (size_t)L_SEQ * HD * 2;
constexpr size_t OFF_VF    = OFF_QK + 2 * SZ_QK;
constexpr size_t SZ_PW     = (size_t)2 * NSEG * 16 * SEG * DM * 2;
constexpr size_t OFF_PW    = OFF_VF + 2 * SZ_O;
constexpr size_t SZ_MEM    = (size_t)NSEG * MM * HD * 2;
constexpr size_t OFF_MEMK  = OFF_PW + SZ_PW;
constexpr size_t OFF_MEMV  = OFF_MEMK + 2 * SZ_MEM;
constexpr size_t OFF_GT    = OFF_MEMV + 2 * SZ_MEM;
constexpr size_t SZ_ACC    = (size_t)2 * 32768;
constexpr size_t OFF_Z     = OFF_GT + (size_t)64 * DM * 4;
constexpr size_t OFF_ACC   = OFF_Z;
constexpr unsigned ACC_LO = 0x88000000u, ACC_HI = 0x87fffff7u;
constexpr size_t OFF_FL    = OFF_ACC + 2 * SZ_ACC;
constexpr int FL_FW  = 0;
constexpr int FL_FR  = FL_FW + 2 * 64 * 16 * 32;
constexpr int FL_TW  = FL_FR + 64 * 16 * 32;
constexpr int FL_FC  = FL_TW + 4 * 128;
constexpr int FL_FQ  = FL_FC + 2 * 64 * 32 * 32;
constexpr int FL_FS  = FL_FQ + 2 * 65 * 32 * 32;
constexpr int FL_FZ  = FL_FS + 4 * 32 * 32;
constexpr int FL_KG  = FL_FZ + 8 * 32;
constexpr int FL_G   = FL_KG + 64 * 32;
constexpr int FL_TK  = FL_G + 4 * 4 * 32;
constexpr int FL_TMO = FL_TK + 2 * 256;
constexpr int FL_S0  = FL_TMO + 64;
constexpr int FL_END = FL_S0 + 2048;
constexpr size_t Z_BYTES   = OFF_FL + (size_t)FL_END * 4 - OFF_Z;
#define TSH 0
constexpr size_t OFF_TAG   = OFF_Z + Z_BYTES + TSH;
constexpr unsigned long long TAG_STEP = 0x9e3779b97f4a7c15ull;
constexpr size_t OFF_G0    = OFF_TAG + 8 * 1024 + 256;
constexpr size_t OFF_X0    = OFF_G0 + 8192;
constexpr size_t WS_TOTAL  = OFF_X0 + 4096;
static_assert(Z_BYTES % 256 == 0 && OFF_Z % 256 == 0 && Z_BYTES <= 2 * 128 * 8192, "zero region");
#define REP ((blockIdx.x >> 1) & 3)
static_assert(WS_TOTAL + (4 << 20) <= (size_t)256 * 1024 * 1024, "workspace too large");

#define CKNP 96
constexpr int CKN = 2208 + CKNP;
static_assert((size_t)DM * 2 * CKN * 2 <= (size_t)DM * L_SEQ * 4 + SZ_X, "K buffer");
__device__ __forceinline__ float pos_of_index(int p) {
  if (p < 256) return (float)p;
  if (p < 384) return (float)(256 + 2 * (p - 256));
  if (p < 512) return (float)(512 + 4 * (p - 384));
  if (p < 640) return (float)(1024 + 8 * (p - 512));
  return 2048.0f;
}
__device__ __forceinline__ int index_of_l(int l) {
  float pf = bfr((float)l);
  int p = (int)pf;
  if (p < 256) return p;
  if (p < 512) return 256 + ((p - 256) >> 1);
  if (p < 1024) return 384 + ((p - 512) >> 2);
  if (p < 2048) return 512 + ((p - 1024) >> 3);
  return 640;
}

template <int CTRL> __device__ __forceinline__ float dppr(float x) {
  return __builtin_bit_cast(float, __builtin_amdgcn_update_dpp(0, __builtin_bit_cast(int, x), CTRL, 0xf, 0xf, false));
}
__device__ __forceinline__ float wave_sum(float v) {
  v += dppr<0x121>(v);
  v += dppr<0x122>(v);
  v += dppr<0x124>(v);
  v += dppr<0x128>(v);
  {
    auto r = __builtin_amdgcn_permlane16_swap(__float_as_uint(v), __float_as_uint(v), false, false);
    v = __uint_as_float(r[0]) + __uint_as_float(r[1]);
  }
  {
    auto r = __builtin_amdgcn_permlane32_swap(__float_as_uint(v), __float_as_uint(v), false, false);
    v = __uint_as_float(r[0]) + __uint_as_float(r[1]);
  }
  return v;
}
__device__ __forceinline__ void ln_row8(float (&v)[8]) {
  float s = 0.f;
#pragma unroll
  for (int j = 0; j < 8; ++j) s += v[j];
  s = wave_sum(s);
  const float mean = s * (1.0f / DM);
  float q = 0.f;
#pragma unroll
  for (int j = 0; j < 8; ++j) { float t = v[j] - mean; q += t * t; }
  q = wave_sum(q);
  const float var = q * (1.0f / DM);
  const float mb = bfr(mean);
  const float sd = bfr(sqrtf(bfr(bfr(var) + 1e-5f)));
#pragma unroll
  for (int j = 0; j < 8; ++j) v[j] = bfr(bfr(v[j] - mb) / sd);
}

#define RLX_AGENT __ATOMIC_RELAXED, __HIP_MEMORY_SCOPE_AGENT
typedef __attribute__((address_space(1))) unsigned gu32;
typedef __attribute__((address_space(3))) unsigned lu32;
typedef unsigned v4u __attribute__((ext_vector_type(4)));
constexpr int AUX_SC1 = 16;
constexpr unsigned SPIN_CAP = 1u << 18;

__device__ __forceinline__ bool wait_flags(const unsigned* flags, int lane, int nfl, unsigned want, unsigned* tmo) {
  for (unsigned spins = 0;; ++spins) {
    unsigned v = want;
    if (lane < nfl) v = __hip_atomic_load((gu32*)flags + lane * 32, RLX_AGENT);
    if (__all(v >= want)) break;
    if (spins > SPIN_CAP) {
      if (lane == 0) __hip_atomic_store((gu32*)tmo, 1u, RLX_AGENT);
      return false;
    }
  }
  __builtin_amdgcn_fence(__ATOMIC_ACQUIRE, "wavefront");
  return true;
}

#define STAMP(i)
#define STAMP_DUMP()
#define WSTAMP(i)
template <int CTRL> __device__ __forceinline__ float dppf(float old, float x) {
  return __builtin_bit_cast(float, __builtin_amdgcn_update_dpp(__builtin_bit_cast(int, old), __builtin_bit_cast(int, x), CTRL, 0xf, 0xf, false));
}
__device__ __forceinline__ float wave_max_dpp(float x) {
  const float ninf = -3e38f;
  x = fmaxf(x, dppf<0x111>(ninf, x));
  x = fmaxf(x, dppf<0x112>(ninf, x));
  x = fmaxf(x, dppf<0x114>(ninf, x));
  x = fmaxf(x, dppf<0x118>(ninf, x));
  x = fmaxf(x, dppf<0x142>(ninf, x));
  x = fmaxf(x, dppf<0x143>(ninf, x));
  return __builtin_bit_cast(float, __builtin_amdgcn_readlane(__builtin_bit_cast(int, x), 63));
}
__device__ __forceinline__ float wave_sum_dpp(float x) {
  x += dppf<0x111>(0.f, x);
  x += dppf<0x112>(0.f, x);
  x += dppf<0x114>(0.f, x);
  x += dppf<0x118>(0.f, x);
  x += dppf<0x142>(0.f, x);
  x += dppf<0x143>(0.f, x);
  return __builtin_bit_cast(float, __builtin_amdgcn_readlane(__builtin_bit_cast(int, x), 63));
}
constexpr int SMS = 528;
constexpr int QS = 144;
constexpr int VS = 80;
constexpr int SOS = 144;
#define VAR_NREP 4
constexpr int NREP = VAR_NREP;
__device__ __forceinline__ float xsum16(float x) {
  auto r = __builtin_amdgcn_permlane16_swap(__float_as_uint(x), __float_as_uint(x), false, false);
  return __uint_as_float(r[0]) + __uint_as_float(r[1]);
}
__device__ __forceinline__ float xsum32(float x) {
  auto r = __builtin_amdgcn_permlane32_swap(__float_as_uint(x), __float_as_uint(x), false, false);
  return __uint_as_float(r[0]) + __uint_as_float(r[1]);
}
__device__ __forceinline__ float xmax16(float x) {
  auto r = __builtin_amdgcn_permlane16_swap(__float_as_uint(x), __float_as_uint(x), false, false);
  return fmaxf(__uint_as_float(r[0]), __uint_as_float(r[1]));
}
__device__ __forceinline__ float xmax32(float x) {
  auto r = __builtin_amdgcn_permlane32_swap(__float_as_uint(x), __float_as_uint(x), false, false);
  return fmaxf(__uint_as_float(r[0]), __uint_as_float(r[1]));
}
__device__ __forceinline__ float red_mod4(float x) {
  x += dppf<0x124>(0.f, x);
  x += dppf<0x128>(0.f, x);
  return xsum32(xsum16(x));
}
typedef __bf16 bf16x2_t __attribute__((ext_vector_type(2)));
typedef float f32x2_t __attribute__((ext_vector_type(2)));
__device__ __forceinline__ unsigned pk2(float a, float b) {
  f32x2_t v = {a, b};
  bf16x2_t r = __builtin_convertvector(v, bf16x2_t);
  return __builtin_bit_cast(unsigned, r);
}
__device__ __forceinline__ float dot2sq(const unsigned x, const float acc) {
  return __builtin_amdgcn_fdot2_f32_bf16(__builtin_bit_cast(bf16x2_t, x), __builtin_bit_cast(bf16x2_t, x), acc, false);
}
__device__ __forceinline__ float lo16(unsigned u) { return __uint_as_float(u << 16); }
__device__ __forceinline__ float hi16(unsigned u) { return __uint_as_float(u & 0xffff0000u); }
__device__ __forceinline__ void scale_pair(float a, float b, float scale, float& oa, float& ob) {
  unsigned u = pk2(a, b);
  unsigned v = pk2(lo16(u) * scale, hi16(u) * scale);
  oa = lo16(v);
  ob = hi16(v);
}

#define XSPLIT 3
__device__ __forceinline__ int role_no() {
  const int b = blockIdx.x, x = b & 7, j = b >> 3;
  if (XSPLIT >= 3) {
    const int y = XSPLIT == 5 ? x & 3 : x >> 1;
    if (XSPLIT >= 6 && (j >> 2) == 6) return (x & 1) ? 104 + 16 * y + j - 16 : 80 + 4 * y + (j & 3);
    if (XSPLIT >= 7 && (j >> 2) == 4) return (x & 1) ? 104 + 16 * y + j - 16 : 64 + 4 * y + (j & 3);
    if (XSPLIT == 5 ? x < 4 : !(x & 1)) {
      if (j >= 16) return 104 + 16 * y + j - 16;
      return XSPLIT == 4 ? 16 * y + j : (j & 8) * 4 + 8 * y + (j & 7);
    }
    if (j < 16) return 168 + (j & 8) * 4 + 8 * y + (j & 7);
    const int i = 4 * y + (j & 3);
    return j < 20 ? 64 + i : j < 24 ? (i < 8 ? 96 + i : 224 + i) : j < 28 ? 80 + i : 240 + i;
  }
  const int L = x >> 2, xx = x & 3;
  if (j < 8) return 32 * L + 8 * xx + j;
  if (j < 12) return 64 + 16 * L + 4 * xx + j - 8;
  if (j < 16) { const int i = 4 * xx + j - 12; return L ? 240 + i : i < 8 ? 96 + i : 224 + i; }
  if (j < 24) return 104 + 8 * x + j - 16;
  return 168 + 32 * L + 8 * xx + j - 24;
}
#define MSTAMP()
#define WSTAMP2()
#define SST(k)
#define TSR(rn, k, ss, t)
#define TS(k, ss, t)
#define RST(k)
#define RSD()
#define LST(k)
#define LSC(k)
#define LSD()
#define PSV
#define PST(k)
#define PSD()
#define PSE()
#define FSD
#define FS(k)
#define FD(ss)
struct MegaArgs {
  const u16* mem0;
  const u16* Dskip;
  char* ws;
  u16* out;
  const u16* x0;
  const u16 *logAre, *Aim, *Cre, *Cim, *logdt;
  const u16 *Wq, *Wk, *Wv, *Wo;
  int rn;
};

__device__ __forceinline__ int lane_id() {
  int l;
  asm volatile("v_mbcnt_lo_u32_b32 %0, -1, 0\n\tv_mbcnt_hi_u32_b32 %0, -1, %0" : "=v"(l));
  return l;
}
__device__ __forceinline__ bool spin_timeout(unsigned spins, unsigned* tmo) {
  if (spins > SPIN_CAP) {
    if (lane_id() == 0) __hip_atomic_store((gu32*)tmo, 1u, RLX_AGENT);
    return true;
  }
  return false;
}
#define PSWH (h & 2)
#define RR 0
#define RQM 63
#define RPM 63
#define CD0 100
#define APN 6
#define APL 300
#define APA 90
#define APB 105
#define APP 200
#define CD 30
#define CDS 30
#define CDL 0
#define XG(h) ((h) >> 3)
#define GP 230
#define GR 50
#define GM 0
#define ROT (4 * (h & 1))
#define CPRIO 1
#define P1PRIO 3
#define QLEAD 2
#define POLL_SLEEP 12
#define SLP_C POLL_SLEEP
#define SLP_A POLL_SLEEP
#define SLP_W POLL_SLEEP
#define SLP_R 4
#define SLP_V 4
#define SLP_Q 4
#define SLP_M 4
#define AWPRIO 2
#define SLP_S 8
#define SPOS 4
#define CVPRE 0
#define QHV 0
#define QCHK 0
#define LEADN 99
#define GATE_V 40
#define GATE_R 380
#define GATE_N 440
#define AWAHEAD 1
#define GATE_M 150
#define GATE_M2 380
#define QPACE 0
#define QPS 6
#define QPL 100
#define GATE_Q 150
#define SLP_Q1 24
#define QPF 3
#define QPV 6
#define QVP 3
#define QP0 2
#define QP1 0
#define QFP 15
#define QUP 7
#define QEP 3
#define AWD 9
#define GPER 200
#define GMA 60
#define GMR 135
#define GMS 0
#define GSTEP 30
#define GATE_RT 130
#define GATE_1 160
#define N1S 20
#define N1E 36
#define PRI_A 3
#define PRI_WA 2
#define PRI_WB 1
#define AWSKIP 3
#define PRI_0 3
#define AWDEF 1
#define SLP_F 2
#define EXP_OFF 0
#define AWSPEC 1
#define CVTHR 48
#define NR_C 8
#define NR_R 8
#define NR_W 8
#define NR_N 2
#define QAHEAD 0
#define NS0 4
#define CVST 16
#define CVNB 9
#define CVFULL 1
#define HK3 0
#define AWDT 120
#define AWMO 0
#define HK2 0
#define RDT 185
#define RQ0 4
#define RGW 40
#define ZK 8
#define ZC 7
#define KBR 8
#define AWE 0
#define AWK 5
#define CRD 40
#define CD2 110
#define AWLM 45
#define AWLD 50
#define TXQ 3
#define QN0 6
#define CD4 70
#define SLP_2 2
#define CD1 80
#define SLP_1 8
#define CSL 0
#define QSL 0
#define RSL 0
#define CTH 0
#define WORD 0
#define WXC 0
#define PFS 0
#define XI0 8
#define QFAST 58
#define SLP_G 4
#define TCH 0
#define CSD 0
#define QX2 1
#define CET 0
#define CG1 110
#define IPC 20
#define IPQ 20
#define AWDU 180
#define RTW 0
#define CVLT 120
#define SLP_P 2
#define SLP_N POLL_SLEEP
template <int SLP, int WPL>
__device__ __forceinline__ bool wait_n(const unsigned* f, int n, unsigned* tmo) {
  const int l = lane_id();
  for (unsigned spins = 0;; ++spins) {
    unsigned v = 1u;
    if (l < n) v = __hip_atomic_load((gu32*)f + (l / WPL) * 32 + l % WPL, RLX_AGENT);
    if (__all(v != 0u)) break;
    if (spin_timeout(spins, tmo)) return false;
    __builtin_amdgcn_s_sleep(SLP);
  }
  asm volatile("" ::: "memory");
  return true;
}
__device__ __forceinline__ unsigned now10() { return (unsigned)__builtin_amdgcn_s_memrealtime(); }
__device__ __forceinline__ void gate(const unsigned t) {
#pragma unroll 1
  for (int i = 0; i < 64; ++i) {
    const int d = (int)(t - now10());
    if (d <= 0) break;
    if (d > 100) __builtin_amdgcn_s_sleep(32);
    else if (d > 25) __builtin_amdgcn_s_sleep(8);
    else __builtin_amdgcn_s_sleep(2);
  }
}
template <int SLP, int WPL>
__device__ __forceinline__ unsigned grp_wait(const bool leader, volatile lu32* sw, const unsigned val, const unsigned* f, int n,
                                             unsigned* tmo, const unsigned t) {
  unsigned r;
  gate(t);
  if (leader) {
    const int l = lane_id();
    unsigned v;
    for (unsigned spins = 0;; ++spins) {
      v = __hip_atomic_load((gu32*)f + ((l & 15) / WPL) * 32 + l % WPL, RLX_AGENT);
      if (__all(v != 0u) || spin_timeout(spins, tmo)) break;
      __builtin_amdgcn_s_sleep(SLP);
    }
    int d = (int)(v - t);
    d = max(d, __builtin_amdgcn_update_dpp(0, d, 0x121, 0xf, 0xf, false));
    d = max(d, __builtin_amdgcn_update_dpp(0, d, 0x122, 0xf, 0xf, false));
    d = max(d, __builtin_amdgcn_update_dpp(0, d, 0x124, 0xf, 0xf, false));
    d = max(d, __builtin_amdgcn_update_dpp(0, d, 0x128, 0xf, 0xf, false));
    r = t + __builtin_amdgcn_readfirstlane(d);
    sw[1 + (val & 2)] = r;
    *sw = val;
  } else {
    for (unsigned spins = 0; __builtin_amdgcn_readfirstlane(*sw) < val && spins <= 64u * SPIN_CAP; ++spins) __builtin_amdgcn_s_sleep(SLP_F);
    r = __builtin_amdgcn_readfirstlane(sw[1 + (val & 2)]);
  }
  asm volatile("" ::: "memory");
  return r;
}
template <int SLP, int WPL>
__device__ __forceinline__ bool wg_wait(const unsigned* f, int n, unsigned* tmo) {
  if (threadIdx.x < 64) (void)wait_n<SLP, WPL>(f, n, tmo);
  __syncthreads();
  asm volatile("" ::: "memory");
  return true;
}
__device__ __forceinline__ void set_flag(unsigned* f) { __hip_atomic_store((gu32*)f, 1u, RLX_AGENT); }
#define DRAIN() asm volatile("s_waitcnt vmcnt(0)" ::: "memory")
#define MKRS(ptr, bytes) __builtin_amdgcn_make_buffer_rsrc((void*)(ptr), 0, (int)(bytes), 0x00020000)
typedef unsigned v2u __attribute__((ext_vector_type(2)));
#define LD128(rs, voff, soff) __builtin_amdgcn_raw_buffer_load_b128(rs, voff, soff, AUX_SC1)
#define LD64(rs, voff, soff) __builtin_amdgcn_raw_buffer_load_b64(rs, voff, soff, AUX_SC1)
#define ST128(v, rs, voff, soff)                                              \
  do {                                                                        \
    const v4u st_d_ = (v);                                                    \
    __builtin_amdgcn_raw_buffer_store_b128(st_d_, rs, voff, soff, AUX_SC1);   \
    asm volatile("s_nop 1" ::"v"(st_d_) : "memory");                          \
  } while (0)
#define ST64(v, rs, voff, soff) __builtin_amdgcn_raw_buffer_store_b64(v, rs, voff, soff, AUX_SC1)
#define BF8(x) __builtin_bit_cast(bf16x8, x)
#define MFMA16(a, b, c) __builtin_amdgcn_mfma_f32_16x16x32_bf16(a, b, c, 0, 0, 0)
typedef short bf16x4 __attribute__((ext_vector_type(4)));
#define MFMA16K(a, b, c) \
  __builtin_amdgcn_mfma_f32_16x16x16bf16_1k(__builtin_bit_cast(bf16x4, a), __builtin_bit_cast(bf16x4, b), c, 0, 0, 0)
__device__ __forceinline__ float fmax_nn(float a, float b) { return __builtin_elementwise_maximum(a, b); }
typedef __bf16 bfv8 __attribute__((ext_vector_type(8)));
typedef __bf16 bfv16 __attribute__((ext_vector_type(16)));
typedef unsigned v8u __attribute__((ext_vector_type(8)));
#define SMF(a, b, c, idx) \
  __builtin_amdgcn_smfmac_f32_16x16x64_bf16(__builtin_bit_cast(bfv8, a), __builtin_bit_cast(bfv16, b), c, idx, 0, 0)
__device__ __forceinline__ float swap_add32(float a, float b) {
  auto r = __builtin_amdgcn_permlane32_swap(__float_as_uint(a), __float_as_uint(b), false, false);
  return __uint_as_float(r[0]) + __uint_as_float(r[1]);
}
__device__ __forceinline__ float swap_add16(float a, float b) {
  auto r = __builtin_amdgcn_permlane16_swap(__float_as_uint(a), __float_as_uint(b), false, false);
  return __uint_as_float(r[0]) + __uint_as_float(r[1]);
}
__device__ __forceinline__ v4u smf_fix(const v4u d) {
  auto x = __builtin_amdgcn_permlane16_swap(d.x, d.z, false, false);
  auto y = __builtin_amdgcn_permlane16_swap(d.y, d.w, false, false);
  return v4u{x[0], x[1], y[0], y[1]};
}
__device__ __forceinline__ v8u smf_bfrag(const u16* p) {
  const v2u a = *reinterpret_cast<const v2u*>(p), b = *reinterpret_cast<const v2u*>(p + 8);
  const v2u c = *reinterpret_cast<const v2u*>(p + 32), d = *reinterpret_cast<const v2u*>(p + 40);
  return v8u{a.x, b.x, a.y, b.y, c.x, d.x, c.y, d.y};
}

#define VAR_PPA 0
#define VAR_PPB 0
#define VAR_THR 0
#define VAR_THR2 0
#define CV_LEAD 100
#define VAR_EARLY 1
#define VAR_DRAIN 1
#define VAR_D1 0
#define VAR_D2 0
#define VAR_DR2 1
#define VAR_S1 0
#define VAR_S2 0
typedef __attribute__((address_space(1))) unsigned long long gu64;
typedef __attribute__((address_space(1))) char gch;
__device__ __forceinline__ int cvt_rpi(float x) {
  int r;
  asm("v_cvt_rpi_i32_f32 %0, %1" : "=v"(r) : "v"(x));
  return r;
}
typedef short v4s __attribute__((ext_vector_type(4)));
typedef __attribute__((address_space(3))) v4s* lp4s;
#define WNT 0
#define WPOL ""
__device__ __forceinline__ void glds16(const void* sbase, const unsigned voff, const unsigned lds) {
  unsigned keep;
  asm volatile("s_nop 4\n\ts_mov_b32 %0, m0\n\ts_mov_b32 m0, %3\n\ts_nop 0\n\tglobal_load_lds_dwordx4 %1, %2" WPOL "\n\ts_mov_b32 m0, %0"
               : "=&s"(keep)
               : "v"(voff), "s"(sbase), "s"(lds)
               : "memory");
}
__device__ __forceinline__ void glds16c(const void* sbase, const unsigned voff, const unsigned lds) {
  unsigned keep;
  asm volatile("s_nop 4\n\ts_mov_b32 %0, m0\n\ts_mov_b32 m0, %3\n\ts_nop 0\n\tglobal_load_lds_dwordx4 %1, %2 sc1\n\ts_mov_b32 m0, %0"
               : "=&s"(keep)
               : "v"(voff), "s"(sbase), "s"(lds)
               : "memory");
}
__device__ __forceinline__ v2u trd(const unsigned a) {
  return __builtin_bit_cast(v2u, __builtin_amdgcn_ds_read_tr16_b64_v4i16((lp4s)(unsigned long)a));
}
__device__ __forceinline__ unsigned lds_base(char* smem) { return (unsigned)(unsigned long)(__attribute__((address_space(3))) char*)smem; }
#define RAW_BAR()                                           \
  do {                                                      \
    asm volatile("s_waitcnt lgkmcnt(0)" ::: "memory");      \
    __builtin_amdgcn_s_barrier();                           \
    asm volatile("" ::: "memory");                          \
  } while (0)
#define WAIT_VM(n) asm volatile("s_waitcnt vmcnt(" #n ")" ::: "memory")
#define TOURS 7
#define MARK() asm volatile(".Lmark%=:" ::)
template <int ID, int MX = 0, class T = int>
__device__ __forceinline__ void pad(T&& x = 0) {
  if (TOURS && MX) asm volatile("s_branch 1f\n\t.p2alignl 12, 0xbf800000, %2\n.Lpad%1:\n\ts_setpc_b64 s[98:99]\n1:" : "+v"(x) : "n"(ID), "n"(MX));
  else if (TOURS) asm volatile("s_cmp_eq_u32 s0, 0xbe801d62\n\t.set .Lpad%1, . - 4" : "+v"(x) : "n"(ID) : "scc");
}
template <int ID>
__device__ __forceinline__ void hop() { asm volatile("s_call_b64 s[98:99], .Lpad%0" ::"n"(ID) : "s98", "s99"); }
template <int B, int E, int D>
__device__ __forceinline__ void hops() {
  if constexpr (B < E) { hop<B>(); hops<B + D, E, D>(); }
}
template <int B, int E, int D, int I = 0>
__device__ __forceinline__ void tour(const int w) {
  if constexpr ((TOURS & 2) && I < D) {
    if ((w & (D - 1)) == I) hops<B + I, E, D>();
    else tour<B, E, D, I + 1>(w);
  }
}
template <bool SP>
__device__ __forceinline__ void w_issue(const u16* Wm, const int j, const int b, const int i, const int h, const int w, const int l, const unsigned sb) {
  const char* base = (const char*)Wm + (size_t)(128 * j + 16 * w) * (HD * 2) + h * (DH * 2);
  const int r = l >> 4;
  const int f = SP ? ((r & 1) | ((i & 1) << 1) | (((i >> 1) & 1) << 2)) : (r | (((i >> 1) & 1) << 2));
  glds16(base, (unsigned)((4 * i + r) * (HD * 2) + (((l & 15) ^ (f << 1)) << 4)), sb + w * 16384 + b * 4096 + i * 1024);
}
#define WM(k) (WORD ? (k) % 3 : (k) >> 2)
#define WJ(k) (WORD ? (k) / 3 : (k) & 3)
template <int N>
__device__ __forceinline__ void wait_vm() { asm volatile("s_waitcnt vmcnt(%0)" ::"n"(N) : "memory"); }

#define PFW 1
#define IPF 1
#define PN1 1
#define PNH 20
#define PWO 1
__device__ __forceinline__ void pull(const unsigned long long b, const unsigned voff, char* smem) {
  unsigned keep;
  asm volatile("s_nop 4\n\ts_mov_b32 %0, m0\n\ts_mov_b32 m0, %3\n\ts_nop 0\n\tglobal_load_lds_dword %1, %2\n\ts_mov_b32 m0, %0"
               : "=&s"(keep)
               : "v"(voff), "s"(b), "s"(lds_base(smem) + 162816)
               : "memory");
}
__device__ __forceinline__ unsigned long long matrix(const MegaArgs& A, const int k) {
  unsigned long long pv = (unsigned long long)A.Wv, pk = (unsigned long long)A.Wk, pq = (unsigned long long)A.Wq;
  asm volatile("" : "+s"(pv), "+s"(pk), "+s"(pq));
  return k == 0 ? pv : k == 1 ? pk : pq;
}
__device__ __forceinline__ void pull_rows(const MegaArgs& A, char* smem, const int i) {
  const int l = lane_id(), y = (blockIdx.x & 7) >> 1;
  const unsigned long long m = matrix(A, i >> 7);
  if (PWO && i >= 384) pull((unsigned long long)A.Wo + ((8 * y + (i & 7)) * 16 + ((i & 127) >> 3)) * 8192, l * 128, smem);
  else pull(m + (i & 127) * (8 * HD) + y * 2048, (l >> 4) * (HD * 2) + (l & 15) * 128, smem);
}
__device__ __forceinline__ void wo_issue(const u16* Wo, const int a, const int st, const int w, const int i, const unsigned sb) {
  const int l = lane_id(), c = l & 15, g = l >> 4, fd = g | ((i & 1) << 2);
  const char* base = (const char*)Wo + ((size_t)((2 * a + (st >> 2)) * DH + 64 * (w & 1) + 16 * (st & 3)) * DM + 128 * (w >> 1)) * 2;
  glds16(base, (unsigned)((4 * i + g) * (DM * 2) + ((c ^ (fd << 1)) << 4)), sb + (st % 5) * 32768 + w * 4096 + i * 1024);
}
__device__ __forceinline__ bool zero_seen(const v4u t, const v2u e) {
  const unsigned long long m = (((unsigned long long)e.y << 32) | e.x) + TAG_STEP;
  const unsigned lo = (unsigned)m, hi = (unsigned)(m >> 32);
  return __all(((t.x ^ lo) | (t.y ^ hi) | (t.z ^ lo) | (t.w ^ hi)) == 0u);
}
__device__ __forceinline__ void zero_wait(char* ws) {
  const __amdgpu_buffer_rsrc_t rs = MKRS(ws + OFF_TAG, 8192 + 8);
#pragma unroll 1
  for (unsigned spins = 0; spins < 16u * SPIN_CAP; ++spins) {
    const v4u t = LD128(rs, lane_id() * 16, (blockIdx.x & 7) * 1024);
    const v2u e = LD64(rs, 0, 8192);
    if (zero_seen(t, e)) break;
    asm volatile("s_sleep 4" ::: "memory");
  }
  asm volatile("" ::: "memory");
}
__device__ __forceinline__ f32x2_t row_sums(const v4u raw) {
  float v[8];
#pragma unroll
  for (int e = 0; e < 4; ++e) { v[2 * e] = lo16(raw[e]); v[2 * e + 1] = hi16(raw[e]); }
  float s = 0.f;
#pragma unroll
  for (int e = 0; e < 8; ++e) s += v[e];
  const float mean = wave_sum(s) * (1.0f / DM);
  float q = 0.f;
#pragma unroll
  for (int e = 0; e < 8; ++e) { const float t = v[e] - mean; q += t * t; }
  return f32x2_t{mean, wave_sum(q)};
}
__device__ __forceinline__ unsigned row_stats(const f32x2_t s) {
  const float sd = bfr(sqrtf(bfr(bfr(s.y * (1.0f / DM)) + 1e-5f)));
  return (unsigned)f2bf(s.x) | ((unsigned)f2bf(sd) << 16);
}
__device__ __forceinline__ void sincos1(const float x, float* sn, float* cs) {
  const float t = __builtin_amdgcn_sinf(x * 0.07957747155f);
  *sn = __builtin_amdgcn_sinf(x * 0.1591549431f);
  *cs = __builtin_fmaf(-2.0f * t, t, 1.0f);
}
#define RUN_START                                                                                                   \
  const f32x2_t wra = {wr[0], wr[1]}, wia = {wi[0], wi[1]}, wrb = {wr[2], wr[3]}, wib = {wi[2], wi[3]};             \
  const f32x2_t pa = wra + wra, pb = wrb + wrb, qa = wra * wra + wia * wia, qb = wrb * wrb + wib * wib;             \
  f32x2_t ya = {yr0[0], yr0[1]}, yb = {yr0[2], yr0[3]};                                                             \
  f32x2_t za = ya * wra - f32x2_t{yi0[0], yi0[1]} * wia, zb = yb * wrb - f32x2_t{yi0[2], yi0[3]} * wib;
#define RUN_STEP                                                                                                    \
  {                                                                                                                 \
    const f32x2_t na = __builtin_elementwise_fma(pa, za, -(qa * ya)), nb = __builtin_elementwise_fma(pb, zb, -(qb * yb)); \
    ya = za; yb = zb; za = na; zb = nb;                                                                             \
  }
__device__ __forceinline__ unsigned pull_text(const int slot) {
  unsigned long long pc;
  int back, len;
  asm volatile("s_getpc_b64 %0\n0:\n\ts_mov_b32 %1, 0b-.Lcode_at\n\ts_max_i32 %2, 0, .Lcode_end-.Lcode_at" : "=s"(pc), "=s"(back), "=s"(len)::"scc");
  pc -= (long long)back;
  unsigned r;
  asm volatile("s_nop 4\n\tbuffer_load_dword %0, %1, %2, 0 offen" : "=v"(r) : "v"(lane_id() * 64 + slot * 4096),
               "s"(v4u{(unsigned)pc, (unsigned)(pc >> 32) & 0xffffu, (unsigned)len, 0x00020000u}) : "memory");
  return r;
}
__device__ __forceinline__ f32x2_t cmul(const f32x2_t z, const f32x2_t e) {
  return __builtin_elementwise_fma(f32x2_t{z.y, z.x}, f32x2_t{-e.y, e.y}, z * e.x);
}
__device__ __forceinline__ void put_row(v2u* p, f32x2_t (&b)[2], const f32x2_t (&z)[2], const f32x2_t (&m)[2]) {
  unsigned h0, h1, l0, l1, t0, t1;
  f32x2_t n0, n1;
  asm("v_cvt_pk_bf16_f32 %0, %8, %9\n\tv_cvt_pk_bf16_f32 %1, %10, %11\n\tv_dot2_f32_bf16 %2, %0, %16, %8\n\tv_dot2_f32_bf16 %4, %0, %17, %9\n\t"
      "v_dot2_f32_bf16 %3, %1, %16, %10\n\tv_dot2_f32_bf16 %5, %1, %17, %11\n\tv_pk_mul_f32 %6, %12, %14 op_sel_hi:[1,0]\n\tv_pk_mul_f32 %7, %13, %15 op_sel_hi:[1,0]\n\t"
      "v_pk_fma_f32 %6, %12, %14, %6 op_sel:[1,1,0] op_sel_hi:[0,1,1] neg_lo:[0,1,0]\n\tv_cvt_pk_bf16_f32 %2, %2, %4\n\t"
      "v_pk_fma_f32 %7, %13, %15, %7 op_sel:[1,1,0] op_sel_hi:[0,1,1] neg_lo:[0,1,0]\n\tv_cvt_pk_bf16_f32 %3, %3, %5"
      : "=&v"(h0), "=&v"(h1), "=&v"(l0), "=&v"(l1), "=&v"(t0), "=&v"(t1), "=&v"(n0), "=&v"(n1)
      : "v"(b[0].x), "v"(b[0].y), "v"(b[1].x), "v"(b[1].y), "v"(z[0]), "v"(z[1]), "v"(m[0]), "v"(m[1]), "s"(0x0000bf80u), "s"(0xbf800000u));
  p[0] = v2u{h0, h1};
  p[64] = v2u{l0, l1};
  b[0] = n0;
  b[1] = n1;
}
__device__ __forceinline__ void start_kgen(const MegaArgs& A, char* smem, const int hi, const int xi, const int aw) {
  const int tid = threadIdx.x;
  const int w = __builtin_amdgcn_readfirstlane(tid >> 6), l = tid & 63, d = 4 * hi + (w & 3);
  const unsigned t0 = AWE ? now10() : 0u;
  PSV;
  PST(0);
  v4u raw[8], ra, rb;
  unsigned txt = 0u;
  if (IPF) {
    unsigned long long pc;
    int len;
    asm volatile("s_getpc_b64 %0\n.Lcode_at:\n\ts_max_i32 %1, 0, .Lcode_end-.Lcode_at" : "=s"(pc), "=s"(len)::"scc");
    const unsigned slot = ((blockIdx.x >> 3) - 8) + 8 * w;
    if ((blockIdx.x >> 6) == 1) txt = __builtin_amdgcn_raw_buffer_load_b32(MKRS(pc, len), l * 64 + slot * 4096, 0, 0);
  }
  const int r0 = 16 * xi + 2 * w, sx = 2 * ((hi + 96) & 127) + w - 6;
  if (xi >= 0) {
    ra = *reinterpret_cast<const v4u*>(A.x0 + (size_t)r0 * DM + l * 8);
    rb = *reinterpret_cast<const v4u*>(A.x0 + (size_t)(r0 + 1) * DM + l * 8);
  }
  const int pn = w < 4 ? DM * NST * 2 : 0;
  const unsigned p1 = __builtin_amdgcn_raw_buffer_load_b32(MKRS(A.logAre, pn), l * 4, d * NST * 2, 0), p2 = __builtin_amdgcn_raw_buffer_load_b32(MKRS(A.Aim, pn), l * 4, d * NST * 2, 0),
                 p3 = __builtin_amdgcn_raw_buffer_load_b32(MKRS(A.Cre, pn), l * 4, d * NST * 2, 0), p4 = __builtin_amdgcn_raw_buffer_load_b32(MKRS(A.Cim, pn), l * 4, d * NST * 2, 0);
  const u16 p0 = A.logdt[d];
  if (w >= 6) {
#pragma unroll
    for (int i = 0; i < 8; ++i) raw[i] = *reinterpret_cast<const v4u*>(A.x0 + (size_t)(8 * sx + i) * DM + l * 8);
  }
  const v2u en = LD64(MKRS(A.ws + OFF_TAG, 8192 + 8), 0, 8192);
  {
    const __amdgpu_buffer_rsrc_t rz = MKRS(A.ws + OFF_Z, Z_BYTES);
    const unsigned i0 = hi < 16 ? ACC_LO : 0u, i1 = hi < 16 ? ACC_HI : 0u;
    ST128((v4u{i0, i1, i0, i1}), rz, tid * 16, hi * 8192);
    ST128((v4u{0u, 0u, 0u, 0u}), rz, tid * 16, (128 + hi) * 8192);
  }
  if (xi >= 0) {
    const f32x2_t sa = row_sums(ra), sb = row_sums(rb);
    const unsigned st = row_stats((l & 1) ? sb : sa);
    const unsigned long long m = (((unsigned long long)en.y << 32) | en.x) + TAG_STEP;
    ST128((v4u{st, (unsigned)m, st, (unsigned)(m >> 32)}), MKRS(A.ws + OFF_G0, 8192), (l >> 1) * 2048 + (l & 1) * 16, r0 * 16);
    SST(11);
  }
  const int pj = (blockIdx.x >> 3) - 8;
  const bool ps = PFW && w < 6 && pj < PNH;
  if (__builtin_expect(ps, 1)) {
    const bool sl = hi < 32;
    const unsigned voff = sl ? (l >> 2) * (HD * 2) + (l & 2) * 512 + (l & 1) * 128 : (l >> 4) * (HD * 2) + (l & 15) * 128;
#pragma unroll
    for (int e = 0; e < 2; ++e) {
      const int i = sl ? 4 * (pj + 8 * w + 48 * e) : pj + PNH * (w + 6 * e);
      const unsigned long long m = matrix(A, i >> 7);
      if (e < PN1 || __builtin_expect(sl, 1))
        pull(m + (i & 127) * (8 * HD) + (blockIdx.x & 6) * 1024 + (sl ? ((1 - (int)(m >> 8)) & 3) * 256 : 0), voff, smem);
    }
  }
  asm volatile("" ::"v"(txt), "v"(p0), "v"(p1), "v"(p2), "v"(p3), "v"(p4) : "memory");
#define CLEARED() do { if (!ps) WAIT_VM(0); else if (hi < 32) WAIT_VM(2); else wait_vm<PN1>(); __builtin_amdgcn_s_barrier(); } while (0)
  if (w >= 4) {
    tour<208, 220, 4>(w);
    PST(1);
    CLEARED();
    PST(2);
  }
  if (w >= 6) {
    __builtin_amdgcn_s_waitcnt(0x0F70);
    if (w == 7 && l < 8)
      __hip_atomic_store((gu64*)(A.ws + OFF_TAG) + l * 128 + hi, (((unsigned long long)en.y << 32) | en.x) + TAG_STEP, RLX_AGENT);
    __builtin_amdgcn_s_setprio(3);
    PST(8);
    v4u wr[32];
    const u16* wop = A.Wo + (size_t)(hi * DH + 64 * (w - 6)) * DM + l * 8;
    if (hi < 32) {
#pragma unroll
      for (int i = 0; i < 32; ++i) wr[i] = *reinterpret_cast<const v4u*>(wop + i * DM);
    }
    f32x2_t sq = {};
#pragma unroll
    for (int i = 0; i < 8; ++i) {
      if (i == 0) pad<203, 1000>(raw[i]);
      if (i == 3) { asm volatile("" : "+v"(sq), "+v"(raw[i])); pad<206>(sq); }
      if (i == 6) { asm volatile("" : "+v"(sq), "+v"(raw[i])); pad<207, 300>(sq); }
      const f32x2_t t = row_sums(raw[i]);
      if (l == i) sq = t;
    }
    pad<218, 500>(sq);
    const unsigned stw = row_stats(sq);
    if (l < 8) __hip_atomic_store((gu32*)(A.ws + OFF_GT) + 8 * sx + l, stw, RLX_AGENT);
    pad<216>();
    PST(9);
    float m = 0.f;
    if (hi < 32) {
      float c0 = 0.f, c1 = 0.f, c2 = 0.f, c3 = 0.f;
#pragma unroll
      for (int i = 0; i < 64; ++i) {
        const v4u x = wr[i & 31];
        if (i < 32) wr[i] = *reinterpret_cast<const v4u*>(wop + (32 + i) * DM);
        if (i == 20) pad<204, 600>(c0);
        c0 = dot2sq(x.x, c0); c1 = dot2sq(x.y, c1); c2 = dot2sq(x.z, c2); c3 = dot2sq(x.w, c3);
      }
      pad<205>(c0);
      m = wave_max_dpp(fmaxf(fmaxf(c0, c1), fmaxf(c2, c3)));
    }
    zero_wait(A.ws);
    DRAIN();
    PST(10);
    if (l < 4) {
      if (hi < 32) __hip_atomic_store((gu32*)(A.ws + OFF_FL) + FL_G + (l * 4 + w - 5) * 32 + hi, __float_as_uint(m) | 1u, RLX_AGENT);
      set_flag((unsigned*)(A.ws + OFF_FL) + FL_FS + (l * 32 + (sx >> 3)) * 32 + (sx & 7));
    }
    __builtin_amdgcn_s_setprio(0);
  } else if (w >= 4) {
    pad<202>();
    __builtin_amdgcn_s_setprio(3);
    if (PFW && hi >= 32 && pj < PNH)
      for (int i = pj + PNH * (6 * PN1 + w - 4); i < (PWO ? 512 : 384); i += 2 * PNH) pull_rows(A, smem, i);
    __builtin_amdgcn_s_setprio(0);
    PST(8);
    if (aw >= 0) {
      pad<219>();
      if (AWE) gate(t0 + AWE);
#pragma unroll 1
      for (int v = 0; v < 8; ++v)
#pragma unroll
        for (int i = 0; i < 4; ++i) wo_issue(A.Wo, aw, w & 1, v, i, lds_base(smem));
    }
    DRAIN();
  } else {
    pad<201, 300>();
    f32x2_t a[2], ew[3][2];
    const float dt = bfr(expf(bf2f(p0)));
#pragma unroll
    for (int e = 0; e < 2; ++e) {
      const float ar = -bfr(expf(e ? hi16(p1) : lo16(p1))) * dt, ai = (e ? hi16(p2) : lo16(p2)) * dt;
      const float th = ai * 0.15915494f, tl = __builtin_fmaf(ai, 0.15915494f, -th) + ai * 6.4206383e-9f;
      a[e] = f32x2_t{(e ? hi16(p3) : lo16(p3)) * dt, (e ? hi16(p4) : lo16(p4)) * dt} * 2.0f;
#pragma unroll
      for (int k = 0; k < 3; ++k) {
        const float st = k == 0 ? 1.0f : k == 1 ? -8.0f : 128.0f;
        const float ex = __expf(ar * fabsf(st)), t = __builtin_fmaf(tl, st, __builtin_amdgcn_fractf(th * st));
        ew[k][e] = f32x2_t{__builtin_amdgcn_cosf(t), __builtin_amdgcn_sinf(t)} * ex;
      }
    }
    pad<217, 300>(a[0]);
    PST(1);
    const int c = l & 15, g = l >> 4;
    v2u* wr = reinterpret_cast<v2u*>(smem + 65536 + w * 16640) + l;
    const bf16x8* rd = reinterpret_cast<const bf16x8*>(smem + 65536 + w * 16640) + 65 * c + g;
    unsigned* kd = reinterpret_cast<unsigned*>(smem + 139264) + w * 516;
    f32x2_t b[2] = {ew[1][0], ew[1][1]};
    wr[0] = v2u{0x3f80u, 0x3f80u};
    wr[64] = v2u{0u, 0u};
#pragma unroll
    for (int n = 1; n < 16; ++n) {
      if (n < 15) put_row(wr + 130 * n, b, b, ew[1]);
      else put_row(wr + 130 * n, b, a, ew[2]);
      if (n == 8) pad<208, 200>(b[0]);
      if (n == KBR) { PST(7); CLEARED(); PST(2); }
    }
    pad<209, 200>(b[0]);
    bf16x8 tp[16];
#pragma unroll
    for (int i = 0; i < 16; ++i) tp[i] = rd[4 * i];
#pragma unroll
    for (int t = 0; t < 2; ++t) {
#pragma unroll
      for (int n = 0; n < 16; ++n) {
        const int cl = 2 * t + (n >> 3), mk = (8 >> cl) - 1;
        if (!(n & mk)) { a[0] = b[0]; a[1] = b[1]; }
        if (!(n & 7) && (cl == 1 || cl == 2)) { ew[0][0] = cmul(ew[0][0], ew[0][0]); ew[0][1] = cmul(ew[0][1], ew[0][1]); }
        if ((n + 1) & mk) put_row(wr + 130 * n, b, b, ew[0]);
        else put_row(wr + 130 * n, b, a, ew[2]);
        if (t == 0 && n == 6) pad<210, 200>(b[0]);
        if (t == 0 && n == 13) pad<211, 200>(b[0]);
        if (t == 1 && n == 0) pad<212, 200>(b[0]);
        if (t == 1 && n == 9) pad<213, 420>(b[0]);
      }
      bf16x8 x[16];
#pragma unroll
      for (int i = 0; i < 16; ++i) x[i] = rd[4 * i];
      __builtin_amdgcn_sched_barrier(0);
      f32x4 s0 = {}, s1 = {}, s2 = {};
      if (t == 1) pad<214, 200>(s0);
#pragma unroll
      for (int i = 0; i < 8; ++i) {
        s0 = MFMA16(tp[i], x[i], s0);
        s1 = MFMA16(tp[8 + i], x[i], s1);
        s2 = MFMA16(tp[i], x[8 + i], s2);
      }
      const int cl = 2 * t + (c >> 3), m = c & 7;
      const int ix = 128 * cl + ((m >> (3 - cl)) << (7 - cl)) + (m & ((8 >> cl) - 1)) + (g << (5 - cl));
#pragma unroll
      for (int r = 0; r < 4; ++r) {
        const float v = s0[r] + (s1[r] + s2[r]);
        kd[ix + (r << (3 - cl))] = pk2(v, v - lo16(pk2(v, v)));
      }
    }
    const float z = wave_sum_dpp(b[0].x + b[1].x);
    if (l == 0) kd[512] = pk2(z, z - lo16(pk2(z, z)));
    PST(4);
    const __amdgpu_buffer_rsrc_t rs = MKRS(A.ws + OFF_KF + d * (4 * CKN), 1960 * 2), rt = MKRS(A.ws + OFF_KF + d * (4 * CKN) + 2 * CKN, 1960 * 2);
    const unsigned kb = lds_base(smem) + 139264 + w * 2064 - 0x4300 * 4;
    float lag = (float)(2080 - 8 * l);
    pad<215, 800>(lag);
#pragma unroll 1
    for (int i = 0; i < 2; ++i) {
      unsigned k0[8], k1[8];
#pragma unroll
      for (int e = 0; e < 8; ++e) {
        const float f = lag - (float)(512 * (e >> 2) + 2 * (e & 3));
        const unsigned pb = pk2(f, f - 1.0f);
        unsigned a0, a1;
        asm("v_mad_u32_u16 %0, %2, 4, %3\n\tv_mad_u32_u16 %1, %2, 4, %3 op_sel:[1,0,0,0]" : "=&v"(a0), "=v"(a1) : "v"(pb), "s"(kb));
        k0[e] = *(lu32*)(unsigned long)a0;
        k1[e] = *(lu32*)(unsigned long)a1;
      }
#pragma unroll
      for (int q = 0; q < 2; ++q) {
        v4u oh, ol;
#pragma unroll
        for (int e = 0; e < 4; ++e) {
          oh[e] = __builtin_amdgcn_perm(k1[4 * q + e], k0[4 * q + e], 0x05040100u);
          ol[e] = __builtin_amdgcn_perm(k1[4 * q + e], k0[4 * q + e], 0x07060302u);
        }
        ST128(oh, rs, 16 * l + 2048 * i + 1024 * q, 0);
        ST128(ol, rt, 16 * l + 2048 * i + 1024 * q, 0);
      }
      lag -= 1024.0f;
    }
    PST(5);
    zero_wait(A.ws);
    DRAIN();
    if (l == 0) set_flag((unsigned*)(A.ws + OFF_FL) + FL_KG + (hi >> 1) * 32 + (d & 7));
  }
  PST(3);
  RAW_BAR();
  PST(6);
  PSE();
  pad<200, 700>();
  SST(3);
}

constexpr int C_SM = 131072, C_SO = C_SM + 8192, C_KT = C_SO + 2048, C_X = C_KT + 16384, C_SQ = C_X + 1024;
constexpr int C_SK = C_SQ + 2432;
static_assert(C_SK + 8 * QS * 2 <= 163808, "lds");
#define CHAIN_ASM_ATT \
  "s_mov_b32 s66, %[d0]\n" \
  "s_mov_b32 s94, 576\n" \
  "s_mov_b32 s95, 288\n" \
  "s_mov_b32 s84, 0x43400000\n" \
  "s_mov_b32 s85, 0x43400000\n" \
  "s_mov_b32 s92, 0x88000000\n" \
  "s_mov_b32 s93, 0x87fffff7\n" \
  "s_mov_b32 s78, 0x3e0293ee\n" \
  "s_mov_b32 s76, 0x33333333\n" \
  "s_mov_b32 s77, 0x33333333\n" \
  "s_add_u32 s58, s56, s60\n" \
  "s_addc_u32 s59, s57, 0\n" \
  "s_xor_b32 s38, s35, 0\n" \
  "s_add_u32 s26, s58, s38\n" \
  "s_addc_u32 s27, s59, 0\n" \
  "s_xor_b32 s38, s35, 1024\n" \
  "s_add_u32 s28, s58, s38\n" \
  "s_addc_u32 s29, s59, 0\n" \
  "s_xor_b32 s38, s35, 2048\n" \
  "s_add_u32 s30, s58, s38\n" \
  "s_addc_u32 s31, s59, 0\n" \
  "s_xor_b32 s38, s35, 3072\n" \
  "s_add_u32 s32, s58, s38\n" \
  "s_addc_u32 s33, s59, 0\n" \
  "s_lshr_b32 s98, s60, 2\n" \
  "s_add_u32 s98, s98, s74\n" \
  "s_add_u32 s98, s98, 131072\n" \
  "s_lshl_b32 s97, s73, 5\n" \
  "s_add_u32 s97, s97, s74\n" \
  "s_add_u32 s97, s97, 161152\n" \
  "s_lshl_b32 s99, s73, 10\n" \
  "s_mov_b32 s65, 9\n" \
  "v_mbcnt_lo_u32_b32 v48, -1, 0\n" \
  "v_mbcnt_hi_u32_b32 v48, -1, v48\n" \
  "v_and_b32 v49, 15, v48\n" \
  "v_lshl_add_u32 v9, v49, 1, s97\n" \
  "v_lshrrev_b32 v49, 4, v48\n" \
  "v_mad_u32_u24 v9, v49, s94, v9\n" \
  "v_add_u32 v7, s99, v1\n" \
  "v_mov_b64 v[12:13], 0\n" \
  "v_mov_b64 v[14:15], 0\n" \
  "v_mov_b64 v[16:17], 0\n" \
  "v_mov_b64 v[18:19], 0\n" \
  "v_mov_b64 v[20:21], 0\n" \
  "v_mov_b64 v[22:23], 0\n" \
  "s_lshl_b32 s88, s73, 10\n" \
  "v_add_u32 v24, s88, v1\n" \
  "ds_read_b128 v[32:35], v24 offset:0\n" \
  "s_waitcnt lgkmcnt(0)\n" \
  "v_mov_b32 v45, 0\n" \
  "v_dot2c_f32_bf16 v45, v32, v32\n" \
  "v_dot2c_f32_bf16 v45, v33, v33\n" \
  "v_dot2c_f32_bf16 v45, v34, v34\n" \
  "v_dot2c_f32_bf16 v45, v35, v35\n" \
  "s_add_u32 s88, s74, 157760\n" \
  "v_mov_b32 v44, s88\n" \
  "s_nop 0\n" \
  "v_cmpx_lt_f32 vcc, 2.0, v45\n" \
  "ds_max_u32 v44, v45\n" \
  "s_mov_b64 exec, -1\n" \
  "s_branch .LCA_AL_%=\n" \
  ".p2alignl 12, 0xbf800000, 3600\n" \
  ".p2align 6\n" \
  ".Lpad0x6d:\n" \
  "s_setpc_b64 s[98:99]\n" \
  ".p2align 6\n" \
  ".LCA_AL_%=:\n" \
  ".LCA_TOP_%=:\n" \
  "s_waitcnt lgkmcnt(0)\n" \
  "s_barrier\n" \
  "ds_read_b32 v8, v1 offset:26688\n" \
  "s_cmp_lg_u32 s73, s65\n" \
  "s_cbranch_scc1 .LCA_NOTK_%=\n" \
  "s_memrealtime s[38:39]\n" \
  "v_mov_b32 v26, 1\n" \
  "s_mul_i32 s23, s71, %[pp]\n" \
  "s_lshr_b32 s62, s66, 4\n" \
  "s_mov_b64 exec, 1\n" \
  "s_waitcnt lgkmcnt(0)\n" \
  "s_mov_b32 s67, s38\n" \
  "s_add_u32 s38, s38, s62\n" \
  "v_mov_b32 v25, s38\n" \
  "buffer_atomic_umax v25, off, s[16:19], s68\n" \
  "buffer_atomic_add v26, off, s[16:19], s68 offset:64\n" \
  "v_add_u32 v27, s23, v25\n" \
  "buffer_store_dword v27, off, s[16:19], s69 sc1\n" \
  "s_mov_b64 exec, -1\n" \
  "s_branch .LCA_SIDE_%=\n" \
  ".LCA_NOTK_%=:\n" \
  "s_cmp_lg_u32 s73, 0\n" \
  "s_cbranch_scc1 .LCA_NOZ_%=\n" \
  "v_mov_b64 v[26:27], s[92:93]\n" \
  "s_xor_b32 s88, s60, 0x8000\n" \
  "s_add_u32 s88, s88, s61\n" \
  "buffer_store_dwordx2 v[26:27], v5, s[40:43], s88 offen sc1\n" \
  ".LCA_NOZ_%=:\n" \
  "s_nop 7\n" \
  ".LCA_SIDE_%=:\n" \
  "s_waitcnt lgkmcnt(0)\n" \
  "v_readfirstlane_b32 s86, v8\n" \
  "s_mov_b64 vcc, s[76:77]\n" \
  "s_lshr_b32 s86, s86, 23\n" \
  "s_add_i32 s86, s86, s83\n" \
  "s_ashr_i32 s86, s86, 1\n" \
  "s_max_i32 s86, s86, s82\n" \
  "s_min_i32 s86, s86, 100\n" \
  "s_lshl_b32 s86, s86, 23\n" \
  "s_add_i32 s75, s86, 0x3c800000\n" \
  "s_sub_u32 s38, s71, 1\n" \
  "s_cmp_ge_u32 s38, 62\n" \
  "ds_read_b128 v[32:35], v1 offset:0\n" \
  "ds_read_b128 v[36:39], v1 offset:1024\n" \
  "ds_read_b128 v[48:51], v1 offset:2048\n" \
  "ds_read_b128 v[52:55], v1 offset:3072\n" \
  "ds_read_b128 v[40:43], v1 offset:4096\n" \
  "ds_read_b128 v[56:59], v1 offset:5120\n" \
  "ds_read_b128 v[60:63], v1 offset:6144\n" \
  "ds_read_b128 v[44:47], v1 offset:7168\n" \
  "s_setprio 3\n" \
  "s_waitcnt lgkmcnt(7)\n" \
  "v_smfmac_f32_16x16x64_bf16 v[12:15], v[32:35], v[64:71], v0\n" \
  "s_waitcnt lgkmcnt(6)\n" \
  "v_smfmac_f32_16x16x64_bf16 v[12:15], v[36:39], v[72:79], v0\n" \
  "s_waitcnt lgkmcnt(5)\n" \
  "v_smfmac_f32_16x16x64_bf16 v[12:15], v[48:51], v[80:87], v0\n" \
  "s_waitcnt lgkmcnt(4)\n" \
  "v_smfmac_f32_16x16x64_bf16 v[12:15], v[52:55], v[88:95], v0\n" \
  "s_waitcnt lgkmcnt(3)\n" \
  "v_smfmac_f32_16x16x64_bf16 v[12:15], v[40:43], v[96:103], v0\n" \
  "s_waitcnt lgkmcnt(2)\n" \
  "v_smfmac_f32_16x16x64_bf16 v[12:15], v[56:59], v[104:111], v0\n" \
  "s_waitcnt lgkmcnt(1)\n" \
  "v_smfmac_f32_16x16x64_bf16 v[12:15], v[60:63], v[112:119], v0\n" \
  "s_waitcnt lgkmcnt(0)\n" \
  "v_smfmac_f32_16x16x64_bf16 v[12:15], v[44:47], v[120:127], v0\n" \
  "ds_read_b128 v[24:27], v1 offset:10240\n" \
  "ds_read_b128 v[28:31], v1 offset:14336\n" \
  "s_nop 5\n" \
  "v_add_f32 v12, v12, v14\n" \
  "v_add_f32 v13, v13, v15\n" \
  "v_cvt_pk_bf16_f32 v12, v12, v13\n" \
  "ds_write_b16 v9, v12 offset:0\n" \
  "ds_write_b16_d16_hi v9, v12 offset:288\n" \
  "s_setprio 0\n" \
  "s_barrier\n" \
  "v_smfmac_f32_16x16x64_bf16 v[16:19], v[32:35], v[128:135], v0\n" \
  "v_smfmac_f32_16x16x64_bf16 v[20:23], v[32:35], v[192:199], v0\n" \
  "ds_read_b128 v[32:35], v1 offset:11264\n" \
  "v_smfmac_f32_16x16x64_bf16 v[16:19], v[36:39], v[136:143], v0\n" \
  "v_smfmac_f32_16x16x64_bf16 v[20:23], v[36:39], v[200:207], v0\n" \
  "ds_read_b128 v[36:39], v1 offset:15360\n" \
  "v_smfmac_f32_16x16x64_bf16 v[16:19], v[48:51], v[144:151], v0\n" \
  "v_smfmac_f32_16x16x64_bf16 v[20:23], v[48:51], v[208:215], v0\n" \
  "v_smfmac_f32_16x16x64_bf16 v[16:19], v[52:55], v[152:159], v0\n" \
  "v_smfmac_f32_16x16x64_bf16 v[20:23], v[52:55], v[216:223], v0\n" \
  "v_smfmac_f32_16x16x64_bf16 v[16:19], v[40:43], v[160:167], v0\n" \
  "v_smfmac_f32_16x16x64_bf16 v[20:23], v[40:43], v[224:231], v0\n" \
  ".fill ((16 - (. - .LCA_AL_%=)) & 31) >> 2, 4, 0xbf800000\n" \
  "s_waitcnt lgkmcnt(2)\n" \
  "s_barrier\n" \
  "s_setprio 3\n" \
  "ds_read_b128 v[48:51], v2 offset:0\n" \
  "ds_read_b128 v[52:55], v2 offset:64\n" \
  "v_smfmac_f32_16x16x64_bf16 v[16:19], v[56:59], v[168:175], v0\n" \
  "v_smfmac_f32_16x16x64_bf16 v[20:23], v[56:59], v[232:239], v0\n" \
  "ds_read_b128 v[56:59], v2 offset:128\n" \
  "s_cbranch_scc1 .LCA_LAST_%=\n" \
  ".LCA_BACK_%=:\n" \
  "s_waitcnt lgkmcnt(2)\n" \
  "v_mfma_f32_16x16x32_bf16 v[12:15], v[24:27], v[48:51], 0\n" \
  "ds_read_b128 v[24:27], v1 offset:12288\n" \
  "v_mfma_f32_16x16x32_bf16 v[8:11], v[28:31], v[48:51], 0\n" \
  "ds_read_b128 v[28:31], v1 offset:16384\n" \
  "v_mfma_f32_16x16x32_bf16 v[40:43], v[48:51], v[48:51], 0\n" \
  "ds_read_b128 v[48:51], v7 offset:18432\n" \
  "v_smfmac_f32_16x16x64_bf16 v[16:19], v[60:63], v[176:183], v0\n" \
  "v_smfmac_f32_16x16x64_bf16 v[20:23], v[60:63], v[240:247], v0\n" \
  "ds_read_b128 v[60:63], v2 offset:192\n" \
  "s_waitcnt lgkmcnt(5)\n" \
  "v_mfma_f32_16x16x32_bf16 v[12:15], v[32:35], v[52:55], v[12:15]\n" \
  "ds_read_b128 v[32:35], v1 offset:13312\n" \
  "v_mfma_f32_16x16x32_bf16 v[8:11], v[36:39], v[52:55], v[8:11]\n" \
  "ds_read_b128 v[36:39], v1 offset:17408\n" \
  "v_mfma_f32_16x16x32_bf16 v[40:43], v[52:55], v[52:55], v[40:43]\n" \
  "ds_read_b128 v[52:55], v7 offset:22528\n" \
  "s_waitcnt lgkmcnt(7)\n" \
  "v_mfma_f32_16x16x32_bf16 v[40:43], v[56:59], v[56:59], v[40:43]\n" \
  "s_waitcnt lgkmcnt(5)\n" \
  "v_mfma_f32_16x16x32_bf16 v[12:15], v[24:27], v[56:59], v[12:15]\n" \
  "ds_read_b128 v[24:27], v6 offset:2048\n" \
  "v_mfma_f32_16x16x32_bf16 v[8:11], v[28:31], v[56:59], v[8:11]\n" \
  "ds_read_b128 v[28:31], v6 offset:3072\n" \
  "s_waitcnt lgkmcnt(5)\n" \
  "v_mfma_f32_16x16x32_bf16 v[40:43], v[60:63], v[60:63], v[40:43]\n" \
  "ds_read_b128 v[56:59], v6 offset:0\n" \
  "s_waitcnt lgkmcnt(4)\n" \
  "v_mfma_f32_16x16x32_bf16 v[12:15], v[32:35], v[60:63], v[12:15]\n" \
  "ds_read_b128 v[32:35], v6 offset:8192\n" \
  "v_mfma_f32_16x16x32_bf16 v[8:11], v[36:39], v[60:63], v[8:11]\n" \
  "ds_read_b128 v[36:39], v6 offset:9216\n" \
  "v_smfmac_f32_16x16x64_bf16 v[16:19], v[44:47], v[184:191], v0\n" \
  "ds_read_b128 v[60:63], v6 offset:1024\n" \
  "s_waitcnt lgkmcnt(6)\n" \
  "v_smfmac_f32_16x16x64_bf16 v[20:23], v[44:47], v[248:255], v0\n" \
  "v_cndmask_b32_dpp v43, v43, v42, vcc quad_perm:[0,1,0,1] row_mask:0xf bank_mask:0xf\n" \
  "v_mov_b32 v41, 0\n" \
  "v_mov_b32 v42, 0\n" \
  "v_cndmask_b32_dpp v12, v8, v12, vcc quad_perm:[0,1,0,1] row_mask:0xf bank_mask:0xf\n" \
  "v_cndmask_b32_dpp v13, v9, v13, vcc quad_perm:[0,1,0,1] row_mask:0xf bank_mask:0xf\n" \
  "v_cndmask_b32_dpp v14, v10, v14, vcc quad_perm:[0,1,0,1] row_mask:0xf bank_mask:0xf\n" \
  "v_cndmask_b32_dpp v15, v11, v15, vcc quad_perm:[0,1,0,1] row_mask:0xf bank_mask:0xf\n" \
  "v_maximum3_f32 v8, v43, v12, v13\n" \
  "v_maximum3_f32 v8, v8, v14, v15\n" \
  "v_add_f32 v16, v16, v18\n" \
  "v_add_f32 v17, v17, v19\n" \
  "v_max_f32_dpp v8, v8, v8 quad_perm:[2,3,0,1] row_mask:0xf bank_mask:0xf\n" \
  "v_mov_b32 v9, v8\n" \
  "v_add_f32 v20, v20, v22\n" \
  "v_add_f32 v21, v21, v23\n" \
  "v_permlane16_swap_b32 v8, v9\n" \
  "v_max_f32 v8, v8, v9\n" \
  "v_mov_b32 v9, v8\n" \
  "v_cvt_pk_bf16_f32 v16, v16, v17\n" \
  "v_cvt_pk_bf16_f32 v17, v20, v21\n" \
  "v_permlane32_swap_b32 v8, v9\n" \
  "v_max_f32 v8, v8, v9\n" \
  "s_barrier\n" \
  "buffer_load_dword v7, v5, s[16:19], s24 offen sc1\n" \
  "v_mul_f32 v8, s78, v8\n" \
  "v_pk_fma_f32 v[12:13], v[12:13], s[78:79], v[8:9] op_sel_hi:[1,0,0] neg_lo:[0,0,1] neg_hi:[0,0,1]\n" \
  "v_pk_fma_f32 v[14:15], v[14:15], s[78:79], v[8:9] op_sel_hi:[1,0,0] neg_lo:[0,0,1] neg_hi:[0,0,1]\n" \
  "v_fma_f32 v43, v43, s78, -v8\n" \
  "v_exp_f32 v12, v12\n" \
  "v_exp_f32 v13, v13\n" \
  "v_exp_f32 v14, v14\n" \
  "v_exp_f32 v15, v15\n" \
  "v_exp_f32 v43, v43\n" \
  "v_cvt_pk_bf16_f32 v44, v12, v13\n" \
  "v_cvt_pk_bf16_f32 v45, v14, v15\n" \
  "v_pk_add_f32 v[18:19], v[12:13], v[14:15]\n" \
  "v_mov_b32_dpp v46, v44 quad_perm:[2,3,0,1] row_mask:0xf bank_mask:0xf\n" \
  "v_mov_b32_dpp v9, v43 quad_perm:[2,3,0,1] row_mask:0xf bank_mask:0xf\n" \
  "v_mov_b32_dpp v47, v45 quad_perm:[2,3,0,1] row_mask:0xf bank_mask:0xf\n" \
  "v_add_f32 v10, v18, v43\n" \
  "v_cvt_pk_bf16_f32 v40, v43, v9\n" \
  "v_add_f32 v10, v10, v19\n" \
  "v_mfma_f32_16x16x32_bf16 v[12:15], v[48:51], v[44:47], 0\n" \
  "v_cvt_pk_bf16_f32 v43, v43, v9\n" \
  "v_add_f32_dpp v10, v10, v10 quad_perm:[2,3,0,1] row_mask:0xf bank_mask:0xf\n" \
  "v_mfma_f32_16x16x32_bf16 v[20:23], v[52:55], v[44:47], 0\n" \
  "v_mul_f32 v11, s75, v10\n" \
  "v_mul_f32 v10, s75, v10\n" \
  "v_mfma_f32_16x16x16_bf16 v[12:15], v[16:17], v[40:41], v[12:15]\n" \
  "ds_read_b128 v[48:51], v6 offset:10240\n" \
  "v_permlane16_swap_b32 v10, v11\n" \
  "v_add_f32 v10, v10, v11\n" \
  "v_mfma_f32_16x16x16_bf16 v[20:23], v[16:17], v[42:43], v[20:23]\n" \
  "v_mov_b32 v11, v10\n" \
  "ds_read_b128 v[52:55], v6 offset:11264\n" \
  "v_mov_b64 v[18:19], s[84:85]\n" \
  "v_permlane32_swap_b32 v10, v11\n" \
  "v_add_f32 v10, v10, v11\n" \
  "v_rcp_f32 v10, v10\n" \
  "s_mov_b32 s0, 0xbe801d62\n" \
  ".set .Lpad0x6e, . - 4\n" \
  "v_pk_mul_f32 v[12:13], v[12:13], v[10:11] op_sel_hi:[1,0]\n" \
  "v_pk_mul_f32 v[14:15], v[14:15], v[10:11] op_sel_hi:[1,0]\n" \
  "v_pk_mul_f32 v[20:21], v[20:21], v[10:11] op_sel_hi:[1,0]\n" \
  "v_pk_mul_f32 v[22:23], v[22:23], v[10:11] op_sel_hi:[1,0]\n" \
  "v_cvt_pk_bf16_f32 v40, v12, v13\n" \
  "v_cvt_pk_bf16_f32 v41, v14, v15\n" \
  "v_cvt_pk_bf16_f32 v42, v20, v21\n" \
  "ds_write_b64 v4, v[40:41]\n" \
  "v_cvt_pk_bf16_f32 v43, v22, v23\n" \
  "ds_write_b64 v4, v[42:43] offset:1024\n" \
  "v_max_u32 v16, 1, v16\n" \
  "v_max_u32 v17, 1, v17\n" \
  "buffer_store_dwordx2 v[16:17], v3, s[52:55], s70 offen sc1\n" \
  "v_mov_b64 v[12:13], s[84:85]\n" \
  "v_mov_b64 v[14:15], s[84:85]\n" \
  "v_mov_b64 v[16:17], s[84:85]\n" \
  "v_mov_b64 v[20:21], s[84:85]\n" \
  "v_mov_b64 v[22:23], s[84:85]\n" \
  "v_mov_b64 v[8:9], s[84:85]\n" \
  "v_mov_b64 v[10:11], s[84:85]\n" \
  "s_setprio 0\n" \
  "s_waitcnt lgkmcnt(0)\n" \
  "s_barrier\n" \
  "ds_read_b128 v[40:43], v1 offset:8192\n" \
  "ds_read_b128 v[44:47], v1 offset:9216\n" \
  "s_waitcnt lgkmcnt(1)\n" \
  "v_smfmac_f32_16x16x64_bf16 v[12:15], v[40:43], v[56:63], v0\n" \
  "s_waitcnt lgkmcnt(0)\n" \
  "v_smfmac_f32_16x16x64_bf16 v[12:15], v[44:47], v[32:39], v0\n" \
  "ds_read_b128 v[56:59], v6 offset:4096\n" \
  "ds_read_b128 v[60:63], v6 offset:5120\n" \
  "v_smfmac_f32_16x16x64_bf16 v[16:19], v[40:43], v[24:31], v0\n" \
  "ds_read_b128 v[32:35], v6 offset:12288\n" \
  "ds_read_b128 v[36:39], v6 offset:13312\n" \
  "v_smfmac_f32_16x16x64_bf16 v[16:19], v[44:47], v[48:55], v0\n" \
  "ds_read_b128 v[24:27], v6 offset:6144\n" \
  "ds_read_b128 v[28:31], v6 offset:7168\n" \
  "v_add_f32 v12, v12, v14\n" \
  "v_add_f32 v13, v13, v15\n" \
  "global_atomic_add_x2 v5, v[12:13], s[26:27]\n" \
  "ds_read_b128 v[48:51], v6 offset:14336\n" \
  "ds_read_b128 v[52:55], v6 offset:15360\n" \
  "s_waitcnt lgkmcnt(6)\n" \
  "v_add_f32 v16, v16, v18\n" \
  "v_add_f32 v17, v17, v19\n" \
  "global_atomic_add_x2 v5, v[16:17], s[28:29]\n" \
  "v_smfmac_f32_16x16x64_bf16 v[20:23], v[40:43], v[56:63], v0\n" \
  "s_waitcnt lgkmcnt(4)\n" \
  "v_smfmac_f32_16x16x64_bf16 v[20:23], v[44:47], v[32:39], v0\n" \
  "s_waitcnt lgkmcnt(2)\n" \
  "v_smfmac_f32_16x16x64_bf16 v[8:11], v[40:43], v[24:31], v0\n" \
  "s_waitcnt lgkmcnt(0)\n" \
  "v_smfmac_f32_16x16x64_bf16 v[8:11], v[44:47], v[48:55], v0\n" \
  "s_nop 4\n" \
  "v_add_f32 v20, v20, v22\n" \
  "v_add_f32 v21, v21, v23\n" \
  "global_atomic_add_x2 v5, v[20:21], s[30:31]\n" \
  "v_add_f32 v8, v8, v10\n" \
  "v_add_f32 v9, v9, v11\n" \
  "global_atomic_add_x2 v5, v[8:9], s[32:33]\n" \
  "s_memrealtime s[62:63]\n" \
  "v_mbcnt_lo_u32_b32 v48, -1, 0\n" \
  "v_mbcnt_hi_u32_b32 v48, -1, v48\n" \
  "v_lshrrev_b32 v49, 3, v48\n" \
  "v_and_b32 v50, 7, v48\n" \
  "v_lshlrev_b32 v50, 4, v50\n" \
  "v_lshl_or_b32 v44, v49, 8, v50\n" \
  "s_add_u32 s20, s20, 32\n" \
  "s_mov_b32 s21, 0x8000\n" \
  "v_mov_b32 v52, s22\n" \
  "v_mov_b32 v54, s20\n" \
  "s_mul_i32 s88, s71, %[pp]\n" \
  "s_lshl_b32 s89, s71, 10\n" \
  "s_sub_u32 s88, s88, s25\n" \
  "s_mul_i32 s38, s24, %[an]\n" \
  "s_cmp_gt_u32 s89, s38\n" \
  "s_cselect_b32 s89, 0x1000, 0\n" \
  "s_waitcnt vmcnt(5)\n" \
  "v_mov_b32 v46, v7\n" \
  "s_branch .LCA_TKE_%=\n" \
  ".LCA_TKR_%=:\n" \
  "s_cmp_lg_u32 s73, 0\n" \
  "s_cbranch_scc1 .LCA_TKW_%=\n" \
  "s_sub_u32 s21, s21, 1\n" \
  "s_cbranch_scc1 .LCA_TKO_%=\n" \
  "s_and_b32 s38, s21, 7\n" \
  "s_cmp_lg_u32 s38, 7\n" \
  "s_cbranch_scc1 .LCA_TKS_%=\n" \
  "buffer_load_dword v46, v5, s[16:19], s24 offen sc1\n" \
  ".LCA_TKS_%=:\n" \
  "s_sleep 1\n" \
  ".LCA_TKE_%=:\n" \
  "v_readlane_b32 s38, v46, 8\n" \
  "v_readlane_b32 s39, v46, 24\n" \
  "v_readlane_b32 s23, v46, 40\n" \
  "v_readlane_b32 s34, v46, 56\n" \
  "s_add_u32 s38, s38, s39\n" \
  "s_add_u32 s23, s23, s34\n" \
  "s_add_u32 s38, s38, s23\n" \
  "s_cmp_lt_u32 s38, s20\n" \
  "s_cbranch_scc1 .LCA_TKR_%=\n" \
  ".LCA_TKO_%=:\n" \
  "v_readlane_b32 s34, v46, 0\n" \
  "v_readlane_b32 s38, v46, 16\n" \
  "v_readlane_b32 s39, v46, 32\n" \
  "v_readlane_b32 s23, v46, 48\n" \
  "s_max_u32 s34, s34, s38\n" \
  "s_max_u32 s39, s39, s23\n" \
  "s_add_u32 s34, s34, %[cd]\n" \
  "v_readlane_b32 s23, v46, 1\n" \
  "s_max_u32 s34, s34, s39\n" \
  "s_sub_u32 s23, s23, s34\n" \
  "s_sub_u32 s23, s23, s88\n" \
  "s_cmp_lt_u32 s23, s89\n" \
  "s_cselect_b32 s23, s23, 0\n" \
  "s_min_u32 s23, s23, %[ka]\n" \
  "s_add_u32 s34, s34, s23\n" \
  "s_add_u32 s34, s34, s64\n" \
  ".LCA_TKD_%=:\n" \
  "v_mov_b32 v55, s34\n" \
  "ds_write_b64 v52, v[54:55]\n" \
  "s_branch .LCA_WAIU_%=\n" \
  ".Lpad0x6f:\n" \
  "s_setpc_b64 s[98:99]\n" \
  ".LCA_WAIS_%=:\n" \
  "s_cmp_lt_i32 s39, -1000\n" \
  "s_cbranch_scc1 .LCA_WAIE_%=\n" \
  "s_sleep 1\n" \
  ".LCA_WAIU_%=:\n" \
  "s_memrealtime s[38:39]\n" \
  "s_waitcnt lgkmcnt(0)\n" \
  "s_sub_i32 s39, s38, s34\n" \
  "s_cmp_lt_i32 s39, -7\n" \
  "s_cbranch_scc1 .LCA_WAIS_%=\n" \
  "s_cmp_lt_i32 s39, 0\n" \
  "s_cbranch_scc1 .LCA_WAIU_%=\n" \
  ".LCA_WAIE_%=:\n" \
  "buffer_load_dwordx4 v[24:27], v44, s[40:43], s60 offen sc1\n" \
  "buffer_load_dwordx4 v[28:31], v44, s[40:43], s60 offen offset:2048 sc1\n" \
  "s_sub_u32 s62, s62, s67\n" \
  "s_lshl_b32 s62, s62, 4\n" \
  "s_sub_i32 s62, s62, s66\n" \
  "s_min_i32 s62, s62, 128\n" \
  "s_max_i32 s62, s62, -256\n" \
  "s_ashr_i32 s62, s62, 2\n" \
  "s_add_i32 s66, s66, s62\n" \
  "s_add_u32 s70, s70, 0x10000\n" \
  "v_mbcnt_lo_u32_b32 v48, -1, 0\n" \
  "v_mbcnt_hi_u32_b32 v48, -1, v48\n" \
  "v_and_b32 v49, 15, v48\n" \
  "v_lshl_add_u32 v9, v49, 1, s97\n" \
  "v_lshrrev_b32 v49, 4, v48\n" \
  "v_mad_u32_u24 v9, v49, s94, v9\n" \
  "v_add_u32 v7, s99, v1\n" \
  "v_mov_b64 v[12:13], 0\n" \
  "v_mov_b64 v[14:15], 0\n" \
  "v_mov_b64 v[16:17], 0\n" \
  "v_mov_b64 v[18:19], 0\n" \
  "v_mov_b64 v[20:21], 0\n" \
  "v_mov_b64 v[22:23], 0\n" \
  "v_and_b32 v49, 3, v48\n" \
  "v_lshl_add_u32 v40, v49, 2, s98\n" \
  "v_lshrrev_b32 v49, 2, v48\n" \
  "v_bfe_u32 v48, v48, 4, 1\n" \
  "v_xor_b32 v49, v49, v48\n" \
  "v_lshl_add_u32 v40, v49, 5, v40\n" \
  "s_add_i32 s80, s86, 0x35000000\n" \
  "s_sub_u32 s91, 0x4a350000, s86\n" \
  "s_min_u32 s91, s91, 0x4ca00000\n" \
  "s_mov_b32 s89, s60\n" \
  "s_xor_b32 s60, s60, 0x80\n" \
  "s_bitcmp0_b32 s60, 7\n" \
  "s_cselect_b32 s88, 0x8000, 0\n" \
  "s_add_u32 s60, s60, s88\n" \
  "s_and_b32 s60, s60, 0xffff\n" \
  "s_add_u32 s58, s56, s60\n" \
  "s_addc_u32 s59, s57, 0\n" \
  "s_xor_b32 s38, s35, 0\n" \
  "s_add_u32 s26, s58, s38\n" \
  "s_addc_u32 s27, s59, 0\n" \
  "s_xor_b32 s38, s35, 1024\n" \
  "s_add_u32 s28, s58, s38\n" \
  "s_addc_u32 s29, s59, 0\n" \
  "s_xor_b32 s38, s35, 2048\n" \
  "s_add_u32 s30, s58, s38\n" \
  "s_addc_u32 s31, s59, 0\n" \
  "s_xor_b32 s38, s35, 3072\n" \
  "s_add_u32 s32, s58, s38\n" \
  "s_addc_u32 s33, s59, 0\n" \
  "s_sub_u32 s71, s71, 1\n" \
  "s_mov_b32 s87, 0x40000\n" \
  "s_branch .LCA_CHK_%=\n" \
  ".LCA_POLL_%=:\n" \
  "buffer_load_dwordx4 v[24:27], v44, s[40:43], s89 offen sc1\n" \
  "buffer_load_dwordx4 v[28:31], v44, s[40:43], s89 offen offset:2048 sc1\n" \
  ".LCA_CHK_%=:\n" \
  "s_sub_u32 s87, s87, 1\n" \
  "s_cbranch_scc1 .LCA_END_%=\n" \
  "s_waitcnt vmcnt(1)\n" \
  "v_cvt_f32_i32 v32, v24\n" \
  "v_cvt_f32_i32 v33, v25\n" \
  "v_cvt_f32_i32 v34, v26\n" \
  "v_cvt_f32_i32 v35, v27\n" \
  "v_pk_mul_f32 v[24:25], v[32:33], s[80:81] op_sel_hi:[1,0]\n" \
  "v_pk_mul_f32 v[26:27], v[34:35], s[80:81] op_sel_hi:[1,0]\n" \
  "v_max3_f32 v45, |v32|, |v33|, |v34|\n" \
  "v_cvt_pk_bf16_f32 v24, v24, v26\n" \
  "v_cvt_pk_bf16_f32 v25, v25, v27\n" \
  "ds_write2_b32 v40, v24, v25 offset0:0 offset1:4\n" \
  "v_max3_f32 v45, |v45|, |v35|, |v35|\n" \
  "s_waitcnt vmcnt(0)\n" \
  "v_cvt_f32_i32 v32, v28\n" \
  "v_cvt_f32_i32 v33, v29\n" \
  "v_cvt_f32_i32 v34, v30\n" \
  "v_cvt_f32_i32 v35, v31\n" \
  "v_pk_mul_f32 v[28:29], v[32:33], s[80:81] op_sel_hi:[1,0]\n" \
  "v_pk_mul_f32 v[30:31], v[34:35], s[80:81] op_sel_hi:[1,0]\n" \
  "v_max3_f32 v45, |v45|, |v32|, |v33|\n" \
  "v_cvt_pk_bf16_f32 v28, v28, v30\n" \
  "v_cvt_pk_bf16_f32 v29, v29, v31\n" \
  "ds_write2_b32 v40, v28, v29 offset0:128 offset1:132\n" \
  "v_max3_f32 v45, |v45|, |v34|, |v35|\n" \
  "v_cmp_lt_f32 vcc, s91, v45\n" \
  "s_cbranch_vccnz .LCA_BIG_%=\n" \
  ".LCA_SML_%=:\n" \
  "s_branch .LCA_TOP_%=\n" \
  ".Lpad0x6b:\n" \
  "s_setpc_b64 s[98:99]\n" \
  ".LCA_BIG_%=:\n" \
  "v_cmp_lt_f32 vcc, 0x4ca00000, v45\n" \
  "s_cbranch_vccnz .LCA_POLL_%=\n" \
  "v_mul_f32 v45, s80, v45\n" \
  "v_mul_f32 v45, v45, v45\n" \
  "s_add_u32 s88, s74, 157760\n" \
  "v_mov_b32 v44, s88\n" \
  "s_nop 0\n" \
  "v_cmpx_lt_f32 vcc, 2.0, v45\n" \
  "ds_max_u32 v44, v45\n" \
  "s_mov_b64 exec, -1\n" \
  "s_branch .LCA_SML_%=\n" \
  ".LCA_TKW_%=:\n" \
  "s_andn2_b32 s38, s22, 24\n" \
  "v_mov_b32 v53, s38\n" \
  ".LCA_TKX_%=:\n" \
  "s_sleep 1\n" \
  "ds_read_b64 v[46:47], v53\n" \
  "s_sub_u32 s21, s21, 1\n" \
  "s_cbranch_scc1 .LCA_TKD_%=\n" \
  "s_waitcnt lgkmcnt(0)\n" \
  "v_readfirstlane_b32 s38, v46\n" \
  "v_readfirstlane_b32 s34, v47\n" \
  "s_cmp_eq_u32 s38, s20\n" \
  "s_cbranch_scc0 .LCA_TKX_%=\n" \
  "s_branch .LCA_TKD_%=\n" \
  ".LCA_FIRST_%=:\n" \
  "ds_read_b64 v[10:11], v1 offset:26624\n" \
  "ds_read_b128 v[24:27], v1 offset:10240\n" \
  "ds_read_b128 v[28:31], v1 offset:14336\n" \
  "ds_read_b128 v[32:35], v1 offset:11264\n" \
  "ds_read_b128 v[36:39], v1 offset:15360\n" \
  "s_mov_b32 s65, 3\n" \
  "s_waitcnt lgkmcnt(4)\n" \
  "v_readfirstlane_b32 s38, v11\n" \
  "v_add_u32 v9, %[d3], v11\n" \
  "s_cmp_eq_u32 s73, 3\n" \
  "s_cselect_b32 s39, s69, 0x1000\n" \
  "s_mov_b64 exec, 1\n" \
  "buffer_store_dword v9, off, s[16:19], s39 sc1\n" \
  "s_mov_b64 exec, -1\n" \
  "s_sub_u32 s67, s38, 40\n" \
  "v_mul_f32 v11, 0x44870000, v10\n" \
  "v_readfirstlane_b32 s83, v10\n" \
  "v_readfirstlane_b32 s82, v11\n" \
  "s_lshr_b32 s82, s82, 23\n" \
  "s_addk_i32 s82, 0xff83\n" \
  "s_ashr_i32 s82, s82, 1\n" \
  "s_max_i32 s82, s82, -20\n" \
  "s_min_i32 s82, s82, 100\n" \
  "s_lshr_b32 s83, s83, 23\n" \
  "s_sub_i32 s83, s83, 242\n" \
  "v_readfirstlane_b32 s86, v8\n" \
  "s_mov_b64 vcc, s[76:77]\n" \
  "s_lshr_b32 s86, s86, 23\n" \
  "s_add_i32 s86, s86, s83\n" \
  "s_ashr_i32 s86, s86, 1\n" \
  "s_max_i32 s86, s86, s82\n" \
  "s_min_i32 s86, s86, 100\n" \
  "s_lshl_b32 s86, s86, 23\n" \
  "s_add_i32 s75, s86, 0x3c800000\n" \
  "s_waitcnt lgkmcnt(0)\n" \
  "s_branch .LCA_BACK_%=\n" \
  ".LCA_LAST_%=:\n" \
  "s_cmp_lg_u32 s71, 0\n" \
  "s_cbranch_scc1 .LCA_FIRST_%=\n" \
  "v_smfmac_f32_16x16x64_bf16 v[16:19], v[60:63], v[176:183], v0\n" \
  "v_smfmac_f32_16x16x64_bf16 v[20:23], v[60:63], v[240:247], v0\n" \
  "v_smfmac_f32_16x16x64_bf16 v[16:19], v[44:47], v[184:191], v0\n" \
  "v_smfmac_f32_16x16x64_bf16 v[20:23], v[44:47], v[248:255], v0\n" \
  "s_nop 7\n" \
  "v_add_f32 v16, v16, v18\n" \
  "v_add_f32 v17, v17, v19\n" \
  "v_add_f32 v20, v20, v22\n" \
  "v_add_f32 v21, v21, v23\n" \
  "v_cvt_pk_bf16_f32 v16, v16, v17\n" \
  "v_cvt_pk_bf16_f32 v17, v20, v21\n" \
  "s_nop 1\n" \
  "s_nop 0\n" \
  "v_max_u32 v16, 1, v16\n" \
  "v_max_u32 v17, 1, v17\n" \
  "buffer_store_dwordx2 v[16:17], v3, s[52:55], s70 offen sc1\n" \
  "s_waitcnt vmcnt(0)\n" \
  ".LCA_END_%=:\n" \
  "s_endpgm\n"
#define CHAIN_ASM_VO \
  "s_mov_b32 s66, %[d0]\n" \
  "s_mov_b32 s94, 576\n" \
  "s_mov_b32 s95, 288\n" \
  "s_mov_b32 s84, 0x43400000\n" \
  "s_mov_b32 s85, 0x43400000\n" \
  "s_mov_b32 s92, 0x88000000\n" \
  "s_mov_b32 s93, 0x87fffff7\n" \
  "s_mov_b32 s78, 0x3e0293ee\n" \
  "s_mov_b32 s76, 0x33333333\n" \
  "s_mov_b32 s77, 0x33333333\n" \
  "s_add_u32 s58, s56, s60\n" \
  "s_addc_u32 s59, s57, 0\n" \
  "s_xor_b32 s38, s35, 0\n" \
  "s_add_u32 s26, s58, s38\n" \
  "s_addc_u32 s27, s59, 0\n" \
  "s_xor_b32 s38, s35, 1024\n" \
  "s_add_u32 s28, s58, s38\n" \
  "s_addc_u32 s29, s59, 0\n" \
  "s_xor_b32 s38, s35, 2048\n" \
  "s_add_u32 s30, s58, s38\n" \
  "s_addc_u32 s31, s59, 0\n" \
  "s_xor_b32 s38, s35, 3072\n" \
  "s_add_u32 s32, s58, s38\n" \
  "s_addc_u32 s33, s59, 0\n" \
  "s_lshr_b32 s98, s60, 2\n" \
  "s_add_u32 s98, s98, s74\n" \
  "s_add_u32 s98, s98, 131072\n" \
  "s_lshl_b32 s97, s73, 5\n" \
  "s_add_u32 s97, s97, s74\n" \
  "s_add_u32 s97, s97, 158592\n" \
  "s_lshl_b32 s99, s73, 11\n" \
  "s_add_u32 s99, s99, s74\n" \
  "s_add_u32 s99, s99, 133120\n" \
  "v_mbcnt_lo_u32_b32 v48, -1, 0\n" \
  "v_mbcnt_hi_u32_b32 v48, -1, v48\n" \
  "s_sub_u32 s88, s73, 4\n" \
  "v_and_b32 v51, 15, v48\n" \
  "v_lshl_add_u32 v4, v51, 1, s97\n" \
  "v_lshrrev_b32 v49, 4, v48\n" \
  "v_mad_u32_u24 v4, v49, s94, v4\n" \
  "v_lshrrev_b32 v49, 2, v51\n" \
  "v_and_b32 v50, 5, v51\n" \
  "v_and_or_b32 v49, v49, 2, v50\n" \
  "s_lshl_b32 s89, s88, 6\n" \
  "s_add_u32 s89, s89, s74\n" \
  "s_add_u32 s89, s89, 161152\n" \
  "v_and_b32 v50, 48, v48\n" \
  "v_add_u32 v6, s89, v50\n" \
  "v_mad_u32_u24 v6, v49, s95, v6\n" \
  "v_lshlrev_b32 v7, 3, v50\n" \
  "v_lshl_add_u32 v7, v49, 4, v7\n" \
  "v_and_b32 v49, 2, v51\n" \
  "v_lshl_add_u32 v7, v49, 29, v7\n" \
  "v_lshlrev_b32 v10, 4, v48\n" \
  "v_mov_b64 v[12:13], 0\n" \
  "v_mov_b64 v[14:15], 0\n" \
  "v_mov_b64 v[16:17], 0\n" \
  "v_mov_b64 v[18:19], 0\n" \
  "v_mov_b64 v[20:21], 0\n" \
  "v_mov_b64 v[22:23], 0\n" \
  "s_lshl_b32 s88, s73, 10\n" \
  "v_add_u32 v24, s88, v1\n" \
  "ds_read_b128 v[32:35], v24 offset:0\n" \
  "s_waitcnt lgkmcnt(0)\n" \
  "v_mov_b32 v45, 0\n" \
  "v_dot2c_f32_bf16 v45, v32, v32\n" \
  "v_dot2c_f32_bf16 v45, v33, v33\n" \
  "v_dot2c_f32_bf16 v45, v34, v34\n" \
  "v_dot2c_f32_bf16 v45, v35, v35\n" \
  "s_add_u32 s88, s74, 157760\n" \
  "v_mov_b32 v44, s88\n" \
  "s_nop 0\n" \
  "v_cmpx_lt_f32 vcc, 2.0, v45\n" \
  "ds_max_u32 v44, v45\n" \
  "s_mov_b64 exec, -1\n" \
  "v_bfe_u32 v60, v10, 2, 7\n" \
  "s_lshl_b32 s38, s24, 1\n" \
  "s_add_u32 s38, s38, %[so]\n" \
  "global_load_dword v61, v60, s[52:53] offset:0 sc1\n" \
  "global_load_dword v62, v60, s[52:53] offset:128 sc1\n" \
  "global_load_dword v63, v60, s[52:53] offset:256 sc1\n" \
  "v_lshrrev_b32 v56, 8, v10\n" \
  "v_lshl_add_u32 v56, v56, 7, s38\n" \
  "v_and_or_b32 v56, v60, 63, v56\n" \
  "s_cmp_lg_u32 s73, 7\n" \
  "s_cbranch_scc1 2\n" \
  "global_load_dword v57, v56, s[52:53] sc1\n" \
  "s_branch .LCV_AL_%=\n" \
  ".p2alignl 12, 0xbf800000, 3000\n" \
  ".p2align 6\n" \
  ".Lpad0x81:\n" \
  "s_setpc_b64 s[98:99]\n" \
  ".p2align 6\n" \
  ".LCV_AL_%=:\n" \
  ".LCV_TOP_%=:\n" \
  "s_waitcnt lgkmcnt(0)\n" \
  "s_barrier\n" \
  "s_setprio 2\n" \
  "ds_read_b128 v[24:27], v1 offset:0\n" \
  "ds_read_b128 v[28:31], v1 offset:1024\n" \
  "ds_read_b128 v[32:35], v1 offset:2048\n" \
  "ds_read_b128 v[36:39], v1 offset:3072\n" \
  "buffer_load_dword v5, off, s[44:47], s68 sc1\n" \
  "s_cmp_eq_u32 s71, 63\n" \
  "s_waitcnt lgkmcnt(3)\n" \
  "v_smfmac_f32_16x16x64_bf16 v[20:23], v[24:27], v[192:199], v0\n" \
  "ds_read_b128 v[40:43], v1 offset:4096\n" \
  "s_waitcnt lgkmcnt(3)\n" \
  "v_smfmac_f32_16x16x64_bf16 v[20:23], v[28:31], v[200:207], v0\n" \
  "ds_read_b128 v[44:47], v1 offset:5120\n" \
  "s_waitcnt lgkmcnt(3)\n" \
  "v_smfmac_f32_16x16x64_bf16 v[20:23], v[32:35], v[208:215], v0\n" \
  "ds_read_b128 v[48:51], v1 offset:6144\n" \
  "s_waitcnt lgkmcnt(3)\n" \
  "v_smfmac_f32_16x16x64_bf16 v[20:23], v[36:39], v[216:223], v0\n" \
  "ds_read_b128 v[52:55], v1 offset:7168\n" \
  "s_waitcnt lgkmcnt(3)\n" \
  "v_smfmac_f32_16x16x64_bf16 v[20:23], v[40:43], v[224:231], v0\n" \
  "s_waitcnt lgkmcnt(2)\n" \
  "v_smfmac_f32_16x16x64_bf16 v[20:23], v[44:47], v[232:239], v0\n" \
  "s_waitcnt lgkmcnt(1)\n" \
  "v_smfmac_f32_16x16x64_bf16 v[20:23], v[48:51], v[240:247], v0\n" \
  "s_waitcnt lgkmcnt(0)\n" \
  "v_smfmac_f32_16x16x64_bf16 v[20:23], v[52:55], v[248:255], v0\n" \
  "s_nop 0\n" \
  "v_smfmac_f32_16x16x64_bf16 v[12:15], v[24:27], v[64:71], v0\n" \
  "s_nop 0\n" \
  "v_smfmac_f32_16x16x64_bf16 v[12:15], v[28:31], v[72:79], v0\n" \
  "s_nop 0\n" \
  "v_smfmac_f32_16x16x64_bf16 v[12:15], v[32:35], v[80:87], v0\n" \
  "ds_read_b32 v11, v1 offset:26688\n" \
  "v_smfmac_f32_16x16x64_bf16 v[12:15], v[36:39], v[88:95], v0\n" \
  "v_add_f32 v20, v20, v22\n" \
  "v_smfmac_f32_16x16x64_bf16 v[12:15], v[40:43], v[96:103], v0\n" \
  "v_add_f32 v21, v21, v23\n" \
  "v_smfmac_f32_16x16x64_bf16 v[12:15], v[44:47], v[104:111], v0\n" \
  "v_cvt_pk_bf16_f32 v20, v20, v21\n" \
  "v_smfmac_f32_16x16x64_bf16 v[12:15], v[48:51], v[112:119], v0\n" \
  "ds_write_b16 v4, v20 offset:0\n" \
  "v_smfmac_f32_16x16x64_bf16 v[12:15], v[52:55], v[120:127], v0\n" \
  "ds_write_b16_d16_hi v4, v20 offset:288\n" \
  "v_smfmac_f32_16x16x64_bf16 v[16:19], v[24:27], v[128:135], v0\n" \
  "s_nop 1\n" \
  "v_smfmac_f32_16x16x64_bf16 v[16:19], v[28:31], v[136:143], v0\n" \
  "s_nop 1\n" \
  "v_smfmac_f32_16x16x64_bf16 v[16:19], v[32:35], v[144:151], v0\n" \
  "v_add_f32 v12, v12, v14\n" \
  "v_smfmac_f32_16x16x64_bf16 v[16:19], v[36:39], v[152:159], v0\n" \
  "v_add_f32 v13, v13, v15\n" \
  "v_smfmac_f32_16x16x64_bf16 v[16:19], v[40:43], v[160:167], v0\n" \
  "v_cvt_pk_bf16_f32 v12, v12, v13\n" \
  "v_smfmac_f32_16x16x64_bf16 v[16:19], v[44:47], v[168:175], v0\n" \
  "ds_write_b16 v4, v12 offset:128\n" \
  "v_smfmac_f32_16x16x64_bf16 v[16:19], v[48:51], v[176:183], v0\n" \
  "ds_write_b16_d16_hi v4, v12 offset:416\n" \
  "v_smfmac_f32_16x16x64_bf16 v[16:19], v[52:55], v[184:191], v0\n" \
  "s_barrier\n" \
  "s_nop 6\n" \
  "v_add_f32 v16, v16, v18\n" \
  "v_add_f32 v17, v17, v19\n" \
  "v_cvt_pk_bf16_f32 v16, v16, v17\n" \
  "ds_write_b16 v4, v16 offset:2560\n" \
  "ds_write_b16_d16_hi v4, v16 offset:2848\n" \
  "s_cbranch_scc1 .LCV_FIRST_%=\n" \
  ".LCV_BACK_%=:\n" \
  "s_waitcnt lgkmcnt(0)\n" \
  "s_barrier\n" \
  "s_setprio 0\n" \
  "ds_read_b128 v[60:63], v6 offset:0\n" \
  "v_readfirstlane_b32 s86, v11\n" \
  "s_add_u32 s88, s74, 157760\n" \
  "v_mov_b32 v8, s88\n" \
  "v_mov_b32 v9, 0\n" \
  "ds_write_b32 v8, v9\n" \
  "s_lshr_b32 s86, s86, 23\n" \
  "s_add_i32 s86, s86, s83\n" \
  "s_ashr_i32 s86, s86, 1\n" \
  "s_max_i32 s86, s86, s82\n" \
  "s_min_i32 s86, s86, 100\n" \
  "s_lshl_b32 s86, s86, 23\n" \
  "s_cmp_eq_u32 s71, 0\n" \
  "s_waitcnt lgkmcnt(1)\n" \
  "v_max_u32 v60, 1, v60\n" \
  "v_max_u32 v61, 1, v61\n" \
  "v_max_u32 v62, 1, v62\n" \
  "v_max_u32 v63, 1, v63\n" \
  "buffer_store_dwordx4 v[60:63], v7, s[48:51], s69 offen sc1\n" \
  "s_cbranch_scc1 .LCV_LAST_%=\n" \
  "s_waitcnt vmcnt(1)\n" \
  "v_cmp_eq_u32 vcc, 0, v5\n" \
  "s_cmp_eq_u64 vcc, 0\n" \
  "s_cselect_b32 s90, 1, 0\n" \
  "s_cselect_b32 s88, s71, 64\n" \
  "s_cmp_lt_u32 s88, 63\n" \
  "s_cselect_b32 s90, 1, 0\n" \
  "s_barrier\n" \
  "ds_read_b128 v[24:27], v3 offset:0\n" \
  "ds_read_b128 v[28:31], v3 offset:1024\n" \
  "ds_read_b128 v[32:35], v3 offset:2048\n" \
  "ds_read_b128 v[36:39], v3 offset:3072\n" \
  "ds_read_b128 v[40:43], v3 offset:8192\n" \
  "ds_read_b128 v[44:47], v3 offset:9216\n" \
  "ds_read_b128 v[48:51], v3 offset:10240\n" \
  "ds_read_b128 v[52:55], v3 offset:11264\n" \
  "ds_read_b128 v[56:59], v3 offset:4096\n" \
  "ds_read_b128 v[60:63], v3 offset:5120\n" \
  "s_cbranch_scc0 .LCV_NOTOK_%=\n" \
  "s_mov_b32 m0, s99\n" \
  "s_add_u32 s88, s99, 8192\n" \
  "global_load_lds_dwordx4 v10, s[62:63] sc1\n" \
  "global_load_lds_dwordx4 v10, s[62:63] offset:1024 sc1\n" \
  "s_mov_b32 m0, s88\n" \
  "s_nop 0\n" \
  "global_load_lds_dwordx4 v10, s[64:65] sc1\n" \
  "global_load_lds_dwordx4 v10, s[64:65] offset:1024 sc1\n" \
  ".LCV_NOTOK_%=:\n" \
  "v_mov_b64 v[12:13], s[84:85]\n" \
  "v_mov_b64 v[14:15], s[84:85]\n" \
  "v_mov_b64 v[16:17], s[84:85]\n" \
  "v_mov_b64 v[18:19], s[84:85]\n" \
  "v_mov_b64 v[20:21], s[84:85]\n" \
  "v_mov_b64 v[22:23], s[84:85]\n" \
  "s_cmp_eq_u32 s90, 1\n" \
  "s_cbranch_scc1 .LCV_NMT_%=\n" \
  "s_add_u32 m0, s22, 64\n" \
  "s_mov_b64 exec, 1\n" \
  "buffer_load_dword off, s[44:47], s68 sc1 lds\n" \
  "s_mov_b64 exec, -1\n" \
  ".LCV_NMT_%=:\n" \
  "s_waitcnt lgkmcnt(0)\n" \
  "s_barrier\n" \
  "ds_read_b128 v[4:7], v1 offset:8192\n" \
  "ds_read_b128 v[8:11], v1 offset:9216\n" \
  "s_waitcnt lgkmcnt(1)\n" \
  "v_smfmac_f32_16x16x64_bf16 v[12:15], v[4:7], v[24:31], v0\n" \
  "s_waitcnt lgkmcnt(0)\n" \
  "v_smfmac_f32_16x16x64_bf16 v[12:15], v[8:11], v[40:47], v0\n" \
  "ds_read_b128 v[24:27], v3 offset:12288\n" \
  "ds_read_b128 v[28:31], v3 offset:13312\n" \
  "v_smfmac_f32_16x16x64_bf16 v[16:19], v[4:7], v[32:39], v0\n" \
  "ds_read_b128 v[32:35], v3 offset:6144\n" \
  "ds_read_b128 v[36:39], v3 offset:7168\n" \
  "v_smfmac_f32_16x16x64_bf16 v[16:19], v[8:11], v[48:55], v0\n" \
  "s_nop 1\n" \
  "v_add_f32 v12, v12, v14\n" \
  "v_add_f32 v13, v13, v15\n" \
  "global_atomic_add_x2 v2, v[12:13], s[26:27]\n" \
  "v_smfmac_f32_16x16x64_bf16 v[20:23], v[4:7], v[56:63], v0\n" \
  "ds_read_b128 v[56:59], v3 offset:14336\n" \
  "ds_read_b128 v[60:63], v3 offset:15360\n" \
  "v_add_f32 v16, v16, v18\n" \
  "v_add_f32 v17, v17, v19\n" \
  "global_atomic_add_x2 v2, v[16:17], s[28:29]\n" \
  "v_mov_b64 v[40:41], s[84:85]\n" \
  "v_mov_b64 v[42:43], s[84:85]\n" \
  "s_waitcnt lgkmcnt(4)\n" \
  "v_smfmac_f32_16x16x64_bf16 v[20:23], v[8:11], v[24:31], v0\n" \
  "s_waitcnt lgkmcnt(2)\n" \
  "v_smfmac_f32_16x16x64_bf16 v[40:43], v[4:7], v[32:39], v0\n" \
  "s_waitcnt lgkmcnt(0)\n" \
  "v_smfmac_f32_16x16x64_bf16 v[40:43], v[8:11], v[56:63], v0\n" \
  "s_nop 3\n" \
  "v_add_f32 v20, v20, v22\n" \
  "v_add_f32 v21, v21, v23\n" \
  "global_atomic_add_x2 v2, v[20:21], s[30:31]\n" \
  "s_nop 0\n" \
  "v_add_f32 v40, v40, v42\n" \
  "v_add_f32 v41, v41, v43\n" \
  "global_atomic_add_x2 v2, v[40:41], s[32:33]\n" \
  "v_mbcnt_lo_u32_b32 v48, -1, 0\n" \
  "v_mbcnt_hi_u32_b32 v48, -1, v48\n" \
  "s_add_u32 s20, s20, 32\n" \
  "s_cmp_eq_u32 s90, 1\n" \
  "s_cbranch_scc1 .LCV_HAVE_%=\n" \
  "s_mov_b32 s87, 0x40000\n" \
  "v_lshlrev_b32 v10, 4, v48\n" \
  "s_waitcnt vmcnt(4)\n" \
  "v_mov_b32 v55, s22\n" \
  "s_nop 0\n" \
  "ds_read_b32 v55, v55 offset:64\n" \
  "s_waitcnt lgkmcnt(0)\n" \
  "v_cmp_eq_u32 vcc, 0, v55\n" \
  "s_cmp_eq_u64 vcc, 0\n" \
  "s_cbranch_scc1 .LCV_GOT_%=\n" \
  ".LCV_LATE_%=:\n" \
  "buffer_load_dword v58, off, s[44:47], s68 sc1\n" \
  "s_sub_u32 s87, s87, 1\n" \
  "s_cbranch_scc1 .LCV_END_%=\n" \
  "s_waitcnt vmcnt(0)\n" \
  "v_cmp_eq_u32 vcc, 0, v58\n" \
  "s_cmp_eq_u64 vcc, 0\n" \
  "s_cbranch_scc1 .LCV_GOT_%=\n" \
  "s_sleep 12\n" \
  "s_branch .LCV_LATE_%=\n" \
  ".Lpad0x82:\n" \
  "s_setpc_b64 s[98:99]\n" \
  ".LCV_GOT_%=:\n" \
  "s_mov_b32 m0, s99\n" \
  "s_add_u32 s88, s99, 8192\n" \
  "global_load_lds_dwordx4 v10, s[62:63] sc1\n" \
  "global_load_lds_dwordx4 v10, s[62:63] offset:1024 sc1\n" \
  "s_mov_b32 m0, s88\n" \
  "s_nop 0\n" \
  "global_load_lds_dwordx4 v10, s[64:65] sc1\n" \
  "global_load_lds_dwordx4 v10, s[64:65] offset:1024 sc1\n" \
  ".LCV_HAVE_%=:\n" \
  "v_lshrrev_b32 v49, 3, v48\n" \
  "v_and_b32 v50, 7, v48\n" \
  "v_lshlrev_b32 v50, 4, v50\n" \
  "v_lshl_or_b32 v44, v49, 8, v50\n" \
  "s_mov_b32 s21, 0x8000\n" \
  "v_mov_b32 v52, s22\n" \
  "v_mov_b32 v54, s20\n" \
  "s_branch .LCV_TKF_%=\n" \
  ".LCV_TKE_%=:\n" \
  "s_sleep 1\n" \
  ".LCV_TKF_%=:\n" \
  "ds_read_b64 v[54:55], v52\n" \
  "s_sub_u32 s21, s21, 1\n" \
  "s_cbranch_scc1 .LCV_TKO_%=\n" \
  "s_waitcnt lgkmcnt(0)\n" \
  "v_readfirstlane_b32 s38, v54\n" \
  "v_readfirstlane_b32 s34, v55\n" \
  "s_cmp_eq_u32 s38, s20\n" \
  "s_cbranch_scc0 .LCV_TKE_%=\n" \
  ".LCV_TKO_%=:\n" \
  "s_branch .LCV_WAIU_%=\n" \
  ".Lpad0x7f:\n" \
  "s_setpc_b64 s[98:99]\n" \
  ".LCV_WAIS_%=:\n" \
  "s_cmp_lt_i32 s39, -1000\n" \
  "s_cbranch_scc1 .LCV_WAIE_%=\n" \
  "s_sleep 1\n" \
  ".LCV_WAIU_%=:\n" \
  "s_memrealtime s[38:39]\n" \
  "s_waitcnt lgkmcnt(0)\n" \
  "s_sub_i32 s39, s38, s34\n" \
  "s_cmp_lt_i32 s39, -7\n" \
  "s_cbranch_scc1 .LCV_WAIS_%=\n" \
  "s_cmp_lt_i32 s39, 0\n" \
  "s_cbranch_scc1 .LCV_WAIU_%=\n" \
  ".LCV_WAIE_%=:\n" \
  "buffer_load_dwordx4 v[24:27], v44, s[40:43], s60 offen sc1\n" \
  "buffer_load_dwordx4 v[28:31], v44, s[40:43], s60 offen offset:2048 sc1\n" \
  "v_mbcnt_lo_u32_b32 v48, -1, 0\n" \
  "v_mbcnt_hi_u32_b32 v48, -1, v48\n" \
  "s_sub_u32 s88, s73, 4\n" \
  "v_and_b32 v51, 15, v48\n" \
  "v_lshl_add_u32 v4, v51, 1, s97\n" \
  "v_lshrrev_b32 v49, 4, v48\n" \
  "v_mad_u32_u24 v4, v49, s94, v4\n" \
  "v_lshrrev_b32 v49, 2, v51\n" \
  "v_and_b32 v50, 5, v51\n" \
  "v_and_or_b32 v49, v49, 2, v50\n" \
  "s_lshl_b32 s89, s88, 6\n" \
  "s_add_u32 s89, s89, s74\n" \
  "s_add_u32 s89, s89, 161152\n" \
  "v_and_b32 v50, 48, v48\n" \
  "v_add_u32 v6, s89, v50\n" \
  "v_mad_u32_u24 v6, v49, s95, v6\n" \
  "v_lshlrev_b32 v7, 3, v50\n" \
  "v_lshl_add_u32 v7, v49, 4, v7\n" \
  "v_and_b32 v49, 2, v51\n" \
  "v_lshl_add_u32 v7, v49, 29, v7\n" \
  "v_lshlrev_b32 v10, 4, v48\n" \
  "v_mov_b64 v[12:13], 0\n" \
  "v_mov_b64 v[14:15], 0\n" \
  "v_mov_b64 v[16:17], 0\n" \
  "v_mov_b64 v[18:19], 0\n" \
  "v_mov_b64 v[20:21], 0\n" \
  "v_mov_b64 v[22:23], 0\n" \
  "v_and_b32 v49, 3, v48\n" \
  "v_lshl_add_u32 v40, v49, 2, s98\n" \
  "v_lshrrev_b32 v49, 2, v48\n" \
  "v_bfe_u32 v48, v48, 4, 1\n" \
  "v_xor_b32 v49, v49, v48\n" \
  "v_lshl_add_u32 v40, v49, 5, v40\n" \
  "s_add_u32 s62, s62, 0x80000\n" \
  "s_addc_u32 s63, s63, 0\n" \
  "s_add_u32 s64, s64, 0x40000\n" \
  "s_addc_u32 s65, s65, 0\n" \
  "s_add_u32 s68, s68, 0x1000\n" \
  "s_add_u32 s69, s69, 0x10000\n" \
  "s_add_i32 s80, s86, 0x35000000\n" \
  "s_sub_u32 s91, 0x4a350000, s86\n" \
  "s_min_u32 s91, s91, 0x4ca00000\n" \
  "s_mov_b32 s89, s60\n" \
  "s_xor_b32 s60, s60, 0x80\n" \
  "s_bitcmp0_b32 s60, 7\n" \
  "s_cselect_b32 s88, 0x8000, 0\n" \
  "s_add_u32 s60, s60, s88\n" \
  "s_and_b32 s60, s60, 0xffff\n" \
  "s_add_u32 s58, s56, s60\n" \
  "s_addc_u32 s59, s57, 0\n" \
  "s_xor_b32 s38, s35, 0\n" \
  "s_add_u32 s26, s58, s38\n" \
  "s_addc_u32 s27, s59, 0\n" \
  "s_xor_b32 s38, s35, 1024\n" \
  "s_add_u32 s28, s58, s38\n" \
  "s_addc_u32 s29, s59, 0\n" \
  "s_xor_b32 s38, s35, 2048\n" \
  "s_add_u32 s30, s58, s38\n" \
  "s_addc_u32 s31, s59, 0\n" \
  "s_xor_b32 s38, s35, 3072\n" \
  "s_add_u32 s32, s58, s38\n" \
  "s_addc_u32 s33, s59, 0\n" \
  "s_sub_u32 s71, s71, 1\n" \
  "s_mov_b32 s87, 0x40000\n" \
  "s_branch .LCV_CHK_%=\n" \
  ".LCV_POLL_%=:\n" \
  "buffer_load_dwordx4 v[24:27], v44, s[40:43], s89 offen sc1\n" \
  "buffer_load_dwordx4 v[28:31], v44, s[40:43], s89 offen offset:2048 sc1\n" \
  ".LCV_CHK_%=:\n" \
  "s_sub_u32 s87, s87, 1\n" \
  "s_cbranch_scc1 .LCV_END_%=\n" \
  "s_waitcnt vmcnt(1)\n" \
  "v_cvt_f32_i32 v32, v24\n" \
  "v_cvt_f32_i32 v33, v25\n" \
  "v_cvt_f32_i32 v34, v26\n" \
  "v_cvt_f32_i32 v35, v27\n" \
  "v_pk_mul_f32 v[24:25], v[32:33], s[80:81] op_sel_hi:[1,0]\n" \
  "v_pk_mul_f32 v[26:27], v[34:35], s[80:81] op_sel_hi:[1,0]\n" \
  "v_max3_f32 v45, |v32|, |v33|, |v34|\n" \
  "v_cvt_pk_bf16_f32 v24, v24, v26\n" \
  "v_cvt_pk_bf16_f32 v25, v25, v27\n" \
  "ds_write2_b32 v40, v24, v25 offset0:0 offset1:4\n" \
  "v_max3_f32 v45, |v45|, |v35|, |v35|\n" \
  "s_waitcnt vmcnt(0)\n" \
  "v_cvt_f32_i32 v32, v28\n" \
  "v_cvt_f32_i32 v33, v29\n" \
  "v_cvt_f32_i32 v34, v30\n" \
  "v_cvt_f32_i32 v35, v31\n" \
  "v_pk_mul_f32 v[28:29], v[32:33], s[80:81] op_sel_hi:[1,0]\n" \
  "v_pk_mul_f32 v[30:31], v[34:35], s[80:81] op_sel_hi:[1,0]\n" \
  "v_max3_f32 v45, |v45|, |v32|, |v33|\n" \
  "v_cvt_pk_bf16_f32 v28, v28, v30\n" \
  "v_cvt_pk_bf16_f32 v29, v29, v31\n" \
  "ds_write2_b32 v40, v28, v29 offset0:128 offset1:132\n" \
  "v_max3_f32 v45, |v45|, |v34|, |v35|\n" \
  "v_cmp_lt_f32 vcc, s91, v45\n" \
  "s_cbranch_vccnz .LCV_BIG_%=\n" \
  ".LCV_SML_%=:\n" \
  "s_branch .LCV_TOP_%=\n" \
  ".Lpad0x83:\n" \
  "s_setpc_b64 s[98:99]\n" \
  ".LCV_BIG_%=:\n" \
  "v_cmp_lt_f32 vcc, 0x4ca00000, v45\n" \
  "s_cbranch_vccnz .LCV_POLL_%=\n" \
  "v_mul_f32 v45, s80, v45\n" \
  "v_mul_f32 v45, v45, v45\n" \
  "s_add_u32 s88, s74, 157760\n" \
  "v_mov_b32 v44, s88\n" \
  "s_nop 0\n" \
  "v_cmpx_lt_f32 vcc, 2.0, v45\n" \
  "ds_max_u32 v44, v45\n" \
  "s_mov_b64 exec, -1\n" \
  "s_branch .LCV_SML_%=\n" \
  ".LCV_FIRST_%=:\n" \
  "s_mov_b32 s87, 0x100000\n" \
  "s_cmp_lg_u32 s73, 7\n" \
  "s_cbranch_scc1 .LCV_FON_%=\n" \
  "global_load_dword v58, v56, s[52:53] sc1\n" \
  "s_sleep %[sf]\n" \
  "global_load_dword v59, v56, s[52:53] sc1\n" \
  ".LCV_FASK_%=:\n" \
  "s_sub_u32 s87, s87, 1\n" \
  "s_cbranch_scc1 .LCV_END_%=\n" \
  "s_waitcnt vmcnt(2)\n" \
  "v_mov_b32 v30, v57\n" \
  "v_cmp_eq_u32 vcc, 0, v57\n" \
  "s_cbranch_vccz .LCV_FSEE_%=\n" \
  "s_cmp_eq_u32 s24, 0\n" \
  "s_cbranch_scc1 1\n" \
  "s_sleep %[sg]\n" \
  "global_load_dword v57, v56, s[52:53] sc1\n" \
  "s_waitcnt vmcnt(2)\n" \
  "v_mov_b32 v30, v58\n" \
  "v_cmp_eq_u32 vcc, 0, v58\n" \
  "s_cbranch_vccz .LCV_FSEE_%=\n" \
  "s_cmp_eq_u32 s24, 0\n" \
  "s_cbranch_scc1 1\n" \
  "s_sleep %[sg]\n" \
  "global_load_dword v58, v56, s[52:53] sc1\n" \
  "s_waitcnt vmcnt(2)\n" \
  "v_mov_b32 v30, v59\n" \
  "v_cmp_eq_u32 vcc, 0, v59\n" \
  "s_cbranch_vccz .LCV_FSEE_%=\n" \
  "s_cmp_eq_u32 s24, 0\n" \
  "s_cbranch_scc1 1\n" \
  "s_sleep %[sg]\n" \
  "global_load_dword v59, v56, s[52:53] sc1\n" \
  "s_branch .LCV_FASK_%=\n" \
  ".LCV_FSEE_%=:\n" \
  "buffer_load_dword v9, off, s[16:19], s24 offset:8 sc1\n" \
  "s_sub_u32 s38, s62, 6144\n" \
  "s_subb_u32 s39, s63, 0\n" \
  "s_add_u32 m0, s99, -6144\n" \
  "s_nop 0\n" \
  "global_load_lds_dwordx4 v10, s[38:39] offset:0 sc1\n" \
  "global_load_lds_dwordx4 v10, s[38:39] offset:1024 sc1\n" \
  "global_load_lds_dwordx4 v10, s[38:39] offset:2048 sc1\n" \
  "global_load_lds_dwordx4 v10, s[38:39] offset:3072 sc1\n" \
  "s_sub_u32 s38, s62, 2048\n" \
  "s_subb_u32 s39, s63, 0\n" \
  "s_add_u32 m0, s99, -2048\n" \
  "s_nop 0\n" \
  "global_load_lds_dwordx4 v10, s[38:39] offset:0 sc1\n" \
  "global_load_lds_dwordx4 v10, s[38:39] offset:1024 sc1\n" \
  "global_load_lds_dwordx4 v10, s[38:39] offset:2048 sc1\n" \
  "global_load_lds_dwordx4 v10, s[38:39] offset:3072 sc1\n" \
  "s_sub_u32 s38, s64, 6144\n" \
  "s_subb_u32 s39, s65, 0\n" \
  "s_add_u32 m0, s99, 2048\n" \
  "s_nop 0\n" \
  "global_load_lds_dwordx4 v10, s[38:39] offset:0 sc1\n" \
  "global_load_lds_dwordx4 v10, s[38:39] offset:1024 sc1\n" \
  "global_load_lds_dwordx4 v10, s[38:39] offset:2048 sc1\n" \
  "global_load_lds_dwordx4 v10, s[38:39] offset:3072 sc1\n" \
  "s_sub_u32 s38, s64, 2048\n" \
  "s_subb_u32 s39, s65, 0\n" \
  "s_add_u32 m0, s99, 6144\n" \
  "s_nop 0\n" \
  "global_load_lds_dwordx4 v10, s[38:39] offset:0 sc1\n" \
  "global_load_lds_dwordx4 v10, s[38:39] offset:1024 sc1\n" \
  "global_load_lds_dwordx4 v10, s[38:39] offset:2048 sc1\n" \
  "global_load_lds_dwordx4 v10, s[38:39] offset:3072 sc1\n" \
  "s_memrealtime vcc\n" \
  "s_nop 1\n" \
  "v_max_u32_dpp v30, v30, v30 row_ror:8 row_mask:0xf bank_mask:0xf\n" \
  "s_nop 1\n" \
  "v_max_u32_dpp v30, v30, v30 row_ror:4 row_mask:0xf bank_mask:0xf\n" \
  "s_nop 1\n" \
  "v_max_u32_dpp v30, v30, v30 row_ror:2 row_mask:0xf bank_mask:0xf\n" \
  "s_nop 1\n" \
  "v_max_u32_dpp v30, v30, v30 row_ror:1 row_mask:0xf bank_mask:0xf\n" \
  "v_mov_b32 v31, v30\n" \
  "s_nop 1\n" \
  "v_permlane16_swap_b32 v30, v31\n" \
  "v_max_u32 v30, v30, v31\n" \
  "v_mov_b32 v31, v30\n" \
  "s_nop 1\n" \
  "v_permlane32_swap_b32 v30, v31\n" \
  "v_max_u32 v30, v30, v31\n" \
  "s_lshr_b32 s88, s68, 10\n" \
  "s_lshl_b32 s88, s88, 8\n" \
  "s_add_u32 s88, s88, s24\n" \
  "s_cmp_eq_u32 s24, 0\n" \
  "v_readfirstlane_b32 s39, v30\n" \
  "v_mov_b32 v31, 1\n" \
  "s_cbranch_scc1 .LCV_FNOW_%=\n" \
  "s_waitcnt vmcnt(16)\n" \
  "v_readfirstlane_b32 s89, v9\n" \
  "s_add_u32 s89, s89, s25\n" \
  "s_sub_u32 s89, s89, %[g1]\n" \
  "s_sub_u32 s38, s89, s39\n" \
  "s_cmp_lt_u32 s38, 800\n" \
  "s_cselect_b32 s39, s89, s39\n" \
  ".LCV_FNOW_%=:\n" \
  "s_add_u32 s89, s39, %[d2]\n" \
  "s_waitcnt lgkmcnt(0)\n" \
  "s_add_u32 s38, vcc_lo, %[d4]\n" \
  "s_max_u32 s89, s89, s38\n" \
  "s_add_u32 s38, s89, %[d1]\n" \
  "v_mov_b32 v30, s38\n" \
  "s_mov_b64 exec, 1\n" \
  "buffer_atomic_umax v30, off, s[16:19], s88\n" \
  "buffer_atomic_add v31, off, s[16:19], s88 offset:64\n" \
  "s_mov_b64 exec, -1\n" \
  ".LCV_FON_%=:\n" \
  "s_add_u32 s62, s62, 0x80000\n" \
  "s_addc_u32 s63, s63, 0\n" \
  "s_add_u32 s64, s64, 0x40000\n" \
  "s_addc_u32 s65, s65, 0\n" \
  "s_add_u32 s68, s68, 0x1000\n" \
  "s_cmp_eq_u32 s73, 7\n" \
  "s_cbranch_scc1 .LCV_FCHK_%=\n" \
  "s_branch .LCV_FBND_%=\n" \
  ".LCV_FMORE_%=:\n" \
  "s_sleep %[sf]\n" \
  "global_load_dword v61, v60, s[52:53] offset:0 sc1\n" \
  "global_load_dword v62, v60, s[52:53] offset:128 sc1\n" \
  "global_load_dword v63, v60, s[52:53] offset:256 sc1\n" \
  "s_sub_u32 s87, s87, 1\n" \
  "s_cbranch_scc1 .LCV_END_%=\n" \
  ".LCV_FBND_%=:\n" \
  "s_waitcnt vmcnt(0)\n" \
  ".LCV_FCHK_%=:\n" \
  "v_min3_u32 v32, v61, v62, v63\n" \
  "v_cmp_eq_u32 vcc, 0, v32\n" \
  "s_cbranch_vccnz .LCV_FMORE_%=\n" \
  "v_add_f32 v24, v62, v63\n" \
  "v_mul_f32 v24, v24, v61\n" \
  "s_nop 1\n" \
  "v_max_f32_dpp v24, v24, v24 row_ror:8 row_mask:0xf bank_mask:0xf\n" \
  "s_nop 1\n" \
  "v_max_f32_dpp v24, v24, v24 row_ror:4 row_mask:0xf bank_mask:0xf\n" \
  "s_nop 1\n" \
  "v_max_f32_dpp v24, v24, v24 row_ror:2 row_mask:0xf bank_mask:0xf\n" \
  "s_nop 1\n" \
  "v_max_f32_dpp v24, v24, v24 row_ror:1 row_mask:0xf bank_mask:0xf\n" \
  "v_mov_b32 v25, v24\n" \
  "s_nop 1\n" \
  "v_permlane16_swap_b32 v24, v25\n" \
  "v_max_f32 v24, v24, v25\n" \
  "v_mul_f32 v24, 0x3f88f5c3, v24\n" \
  "s_nop 0\n" \
  "v_mul_f32 v25, 0x44870000, v24\n" \
  "v_readfirstlane_b32 s83, v24\n" \
  "v_readfirstlane_b32 s82, v25\n" \
  "s_lshr_b32 s82, s82, 23\n" \
  "s_addk_i32 s82, 0xff83\n" \
  "s_ashr_i32 s82, s82, 1\n" \
  "s_max_i32 s82, s82, -20\n" \
  "s_min_i32 s82, s82, 100\n" \
  "s_lshr_b32 s83, s83, 23\n" \
  "s_sub_i32 s83, s83, 242\n" \
  "s_cmp_lg_u32 s73, 7\n" \
  "s_cbranch_scc1 .LCV_BACK_%=\n" \
  "v_mov_b32 v25, s89\n" \
  "s_mov_b64 exec, 1\n" \
  "ds_write_b64 v1, v[24:25] offset:26624\n" \
  "s_mov_b64 exec, -1\n" \
  ".LCV_FWAIT_%=:\n" \
  "s_memrealtime s[38:39]\n" \
  "s_waitcnt lgkmcnt(0)\n" \
  "s_sub_u32 s38, s89, s38\n" \
  "s_cmp_lt_u32 s38, 800\n" \
  "s_cbranch_scc1 .LCV_FWAIT_%=\n" \
  "s_waitcnt vmcnt(2)\n" \
  "s_branch .LCV_BACK_%=\n" \
  ".LCV_LAST_%=:\n" \
  "s_waitcnt vmcnt(0)\n" \
  ".LCV_END_%=:\n" \
  "s_endpgm\n" \
  ".p2alignl 12, 0xbf800000\n" \
  ".Lpad0x64:\n" \
  "s_setpc_b64 s[98:99]\n"
#define QKV_ASM_0 \
  "s_cmp_lg_u32 s69, 0\n" \
  "s_mov_b32 s69, 0\n" \
  "s_cbranch_scc1 .LQA_GOT_%=\n" \
  "s_branch .LQA_POLL_%=\n" \
  "s_branch .LQA_AL_%=\n" \
  ".p2alignl 12, 0xbf800000, 2600\n" \
  ".p2align 6\n" \
  ".Lpad0x95:\n" \
  "s_setpc_b64 s[98:99]\n" \
  ".p2align 6\n" \
  ".LQA_AL_%=:\n" \
  ".LQA_TOP_%=:\n" \
  "s_barrier\n" \
  "ds_read_b128 v[36:39], v7 offset:0\n" \
  "ds_read_b128 v[40:43], v7 offset:2048\n" \
  "ds_read_b128 v[44:47], v7 offset:4096\n" \
  "s_setprio 2\n" \
  "s_waitcnt lgkmcnt(2)\n" \
  "v_mfma_f32_16x16x32_bf16 v[12:15], v[64:67], v[36:39], 0\n" \
  "ds_read_b128 v[48:51], v7 offset:6144\n" \
  "v_mfma_f32_16x16x32_bf16 v[16:19], v[128:131], v[36:39], 0\n" \
  "v_mfma_f32_16x16x32_bf16 v[20:23], v[36:39], v[192:195], 0\n" \
  "s_cmp_lg_u32 s69, 1\n" \
  "s_cbranch_scc1 .LQA_FDN_%=\n" \
  "s_waitcnt vmcnt(0)\n" \
  "v_mov_b32 v52, s80\n" \
  "v_mov_b32 v53, 0x10000\n" \
  "s_mov_b64 exec, 1\n" \
  "ds_add_rtn_u32 v54, v52, v53\n" \
  "s_memrealtime s[0:1]\n" \
  "s_mov_b64 exec, -1\n" \
  "s_add_u32 s67, s67, 4\n" \
  "s_add_u32 s64, s64, 4096\n" \
  "s_addc_u32 s65, s65, 0\n" \
  "v_mov_b32 v55, 0\n" \
  "s_waitcnt lgkmcnt(0)\n" \
  "v_readfirstlane_b32 s74, v54\n" \
  "s_and_b32 s76, s74, 0x30000\n" \
  "s_cmp_lg_u32 s76, 0x30000\n" \
  "s_cbranch_scc1 .LQA_FDN_%=\n" \
  "s_mov_b32 s79, 0x100000\n" \
  "s_branch .LQA_FDT_%=\n" \
  ".LQA_FD_%=:\n" \
  "ds_read_b32 v54, v52\n" \
  "s_sub_u32 s79, s79, 1\n" \
  "s_cbranch_scc1 .LQA_FDE_%=\n" \
  "s_waitcnt lgkmcnt(0)\n" \
  "v_readfirstlane_b32 s74, v54\n" \
  ".LQA_FDT_%=:\n" \
  "s_and_b32 s74, s74, 0xffff\n" \
  "s_cmp_lt_u32 s74, s67\n" \
  "s_cbranch_scc1 .LQA_FD_%=\n" \
  ".LQA_FDE_%=:\n" \
  "v_or_b32 v53, 1, s0\n" \
  "s_cmp_lg_u32 s67, s83\n" \
  "s_cbranch_scc1 .LQA_FDX_%=\n" \
  "v_lshlrev_b32 v54, 5, v6\n" \
  "s_mov_b64 exec, 15\n" \
  "global_store_dword v54, v53, s[84:85] sc1\n" \
  ".LQA_FDX_%=:\n" \
  "s_mov_b64 exec, 1\n" \
  "global_store_dword v55, v53, s[64:65] offset:-4096 sc1\n" \
  "s_mov_b64 exec, -1\n" \
  ".LQA_FDN_%=:\n" \
  "s_waitcnt lgkmcnt(2)\n" \
  "v_mfma_f32_16x16x32_bf16 v[12:15], v[68:71], v[40:43], v[12:15]\n" \
  "ds_read_b128 v[36:39], v7 offset:8192\n" \
  "v_mov_b64 v[24:25], 0\n" \
  "v_lshrrev_b32 v26, 1, v6\n" \
  "buffer_store_dwordx2 v[24:25], v26, s[48:51], s61 offen sc1\n" \
  "v_mfma_f32_16x16x32_bf16 v[16:19], v[132:135], v[40:43], v[16:19]\n" \
  "v_mov_b32 v11, s82\n" \
  "v_mfma_f32_16x16x32_bf16 v[20:23], v[40:43], v[196:199], v[20:23]\n" \
  "s_waitcnt lgkmcnt(2)\n" \
  "v_mfma_f32_16x16x32_bf16 v[12:15], v[72:75], v[44:47], v[12:15]\n" \
  "ds_read_b128 v[40:43], v7 offset:10240\n" \
  "v_mfma_f32_16x16x32_bf16 v[16:19], v[136:139], v[44:47], v[16:19]\n" \
  "v_mfma_f32_16x16x32_bf16 v[20:23], v[44:47], v[200:203], v[20:23]\n" \
  "s_waitcnt lgkmcnt(2)\n" \
  "v_mfma_f32_16x16x32_bf16 v[12:15], v[76:79], v[48:51], v[12:15]\n" \
  "ds_read_b128 v[44:47], v7 offset:12288\n" \
  "v_mfma_f32_16x16x32_bf16 v[16:19], v[140:143], v[48:51], v[16:19]\n" \
  "v_mfma_f32_16x16x32_bf16 v[20:23], v[48:51], v[204:207], v[20:23]\n" \
  "s_waitcnt lgkmcnt(2)\n" \
  "v_mfma_f32_16x16x32_bf16 v[12:15], v[80:83], v[36:39], v[12:15]\n" \
  "ds_read_b128 v[48:51], v7 offset:14336\n" \
  "v_mfma_f32_16x16x32_bf16 v[16:19], v[144:147], v[36:39], v[16:19]\n" \
  "v_mfma_f32_16x16x32_bf16 v[20:23], v[36:39], v[208:211], v[20:23]\n" \
  "s_waitcnt lgkmcnt(2)\n" \
  "v_mfma_f32_16x16x32_bf16 v[12:15], v[84:87], v[40:43], v[12:15]\n" \
  "ds_read_b128 v[36:39], v7 offset:16384\n" \
  "v_mfma_f32_16x16x32_bf16 v[16:19], v[148:151], v[40:43], v[16:19]\n" \
  "v_mfma_f32_16x16x32_bf16 v[20:23], v[40:43], v[212:215], v[20:23]\n" \
  "s_waitcnt lgkmcnt(2)\n" \
  "v_mfma_f32_16x16x32_bf16 v[12:15], v[88:91], v[44:47], v[12:15]\n" \
  "ds_read_b128 v[40:43], v7 offset:18432\n" \
  "v_mfma_f32_16x16x32_bf16 v[16:19], v[152:155], v[44:47], v[16:19]\n" \
  "v_mfma_f32_16x16x32_bf16 v[20:23], v[44:47], v[216:219], v[20:23]\n" \
  "s_waitcnt lgkmcnt(2)\n" \
  "v_mfma_f32_16x16x32_bf16 v[12:15], v[92:95], v[48:51], v[12:15]\n" \
  "ds_read_b128 v[44:47], v7 offset:20480\n" \
  "v_mfma_f32_16x16x32_bf16 v[16:19], v[156:159], v[48:51], v[16:19]\n" \
  "v_mfma_f32_16x16x32_bf16 v[20:23], v[48:51], v[220:223], v[20:23]\n" \
  "s_waitcnt lgkmcnt(2)\n" \
  "v_mfma_f32_16x16x32_bf16 v[12:15], v[96:99], v[36:39], v[12:15]\n" \
  "ds_read_b128 v[48:51], v7 offset:22528\n" \
  "v_mfma_f32_16x16x32_bf16 v[16:19], v[160:163], v[36:39], v[16:19]\n" \
  "v_mfma_f32_16x16x32_bf16 v[20:23], v[36:39], v[224:227], v[20:23]\n" \
  "s_waitcnt lgkmcnt(2)\n" \
  "v_mfma_f32_16x16x32_bf16 v[12:15], v[100:103], v[40:43], v[12:15]\n" \
  "ds_read_b128 v[36:39], v7 offset:24576\n" \
  "v_mfma_f32_16x16x32_bf16 v[16:19], v[164:167], v[40:43], v[16:19]\n" \
  "v_mfma_f32_16x16x32_bf16 v[20:23], v[40:43], v[228:231], v[20:23]\n" \
  "s_waitcnt lgkmcnt(2)\n" \
  "v_mfma_f32_16x16x32_bf16 v[12:15], v[104:107], v[44:47], v[12:15]\n" \
  "ds_read_b128 v[40:43], v7 offset:26624\n" \
  "v_mfma_f32_16x16x32_bf16 v[16:19], v[168:171], v[44:47], v[16:19]\n" \
  "v_mfma_f32_16x16x32_bf16 v[20:23], v[44:47], v[232:235], v[20:23]\n" \
  "s_waitcnt lgkmcnt(2)\n" \
  "v_mfma_f32_16x16x32_bf16 v[12:15], v[108:111], v[48:51], v[12:15]\n" \
  "ds_read_b128 v[44:47], v7 offset:28672\n" \
  "v_mfma_f32_16x16x32_bf16 v[16:19], v[172:175], v[48:51], v[16:19]\n" \
  "v_mfma_f32_16x16x32_bf16 v[20:23], v[48:51], v[236:239], v[20:23]\n" \
  "s_waitcnt lgkmcnt(2)\n" \
  "v_mfma_f32_16x16x32_bf16 v[12:15], v[112:115], v[36:39], v[12:15]\n" \
  "ds_read_b128 v[48:51], v7 offset:30720\n" \
  "v_mfma_f32_16x16x32_bf16 v[16:19], v[176:179], v[36:39], v[16:19]\n" \
  "v_mfma_f32_16x16x32_bf16 v[20:23], v[36:39], v[240:243], v[20:23]\n" \
  "s_waitcnt lgkmcnt(2)\n" \
  "v_mfma_f32_16x16x32_bf16 v[12:15], v[116:119], v[40:43], v[12:15]\n" \
  "ds_read_b128 v[36:39], v7 offset:256\n" \
  "v_mfma_f32_16x16x32_bf16 v[16:19], v[180:183], v[40:43], v[16:19]\n" \
  "v_mfma_f32_16x16x32_bf16 v[20:23], v[40:43], v[244:247], v[20:23]\n" \
  "s_waitcnt lgkmcnt(2)\n" \
  "v_mfma_f32_16x16x32_bf16 v[12:15], v[120:123], v[44:47], v[12:15]\n" \
  "ds_read_b128 v[40:43], v7 offset:2304\n" \
  "v_mfma_f32_16x16x32_bf16 v[16:19], v[184:187], v[44:47], v[16:19]\n" \
  "v_mfma_f32_16x16x32_bf16 v[20:23], v[44:47], v[248:251], v[20:23]\n" \
  "s_waitcnt lgkmcnt(2)\n" \
  "v_mfma_f32_16x16x32_bf16 v[12:15], v[124:127], v[48:51], v[12:15]\n" \
  "ds_read_b128 v[44:47], v7 offset:4352\n" \
  "v_mfma_f32_16x16x32_bf16 v[16:19], v[188:191], v[48:51], v[16:19]\n" \
  "v_mfma_f32_16x16x32_bf16 v[20:23], v[48:51], v[252:255], v[20:23]\n" \
  "s_waitcnt lgkmcnt(2)\n" \
  "v_mfma_f32_16x16x32_bf16 v[24:27], v[64:67], v[36:39], 0\n" \
  "ds_read_b128 v[48:51], v7 offset:6400\n" \
  "v_mfma_f32_16x16x32_bf16 v[28:31], v[128:131], v[36:39], 0\n" \
  "v_mfma_f32_16x16x32_bf16 v[32:35], v[36:39], v[192:195], 0\n" \
  "s_waitcnt lgkmcnt(2)\n" \
  "v_mfma_f32_16x16x32_bf16 v[24:27], v[68:71], v[40:43], v[24:27]\n" \
  "ds_read_b128 v[36:39], v7 offset:8448\n" \
  "v_mfma_f32_16x16x32_bf16 v[28:31], v[132:135], v[40:43], v[28:31]\n" \
  "v_mfma_f32_16x16x32_bf16 v[32:35], v[40:43], v[196:199], v[32:35]\n" \
  "s_waitcnt lgkmcnt(2)\n" \
  "v_mfma_f32_16x16x32_bf16 v[24:27], v[72:75], v[44:47], v[24:27]\n" \
  "ds_read_b128 v[40:43], v7 offset:10496\n" \
  "v_mfma_f32_16x16x32_bf16 v[28:31], v[136:139], v[44:47], v[28:31]\n" \
  "v_mfma_f32_16x16x32_bf16 v[32:35], v[44:47], v[200:203], v[32:35]\n" \
  "s_waitcnt lgkmcnt(2)\n" \
  "v_mfma_f32_16x16x32_bf16 v[24:27], v[76:79], v[48:51], v[24:27]\n" \
  "ds_read_b128 v[44:47], v7 offset:12544\n" \
  "v_mfma_f32_16x16x32_bf16 v[28:31], v[140:143], v[48:51], v[28:31]\n" \
  "v_mfma_f32_16x16x32_bf16 v[32:35], v[48:51], v[204:207], v[32:35]\n" \
  "s_waitcnt lgkmcnt(2)\n" \
  "v_mfma_f32_16x16x32_bf16 v[24:27], v[80:83], v[36:39], v[24:27]\n" \
  "ds_read_b128 v[48:51], v7 offset:14592\n" \
  "v_mfma_f32_16x16x32_bf16 v[28:31], v[144:147], v[36:39], v[28:31]\n" \
  "v_mfma_f32_16x16x32_bf16 v[32:35], v[36:39], v[208:211], v[32:35]\n" \
  "s_barrier\n" \
  "s_mov_b32 s0, 0xbe801d62\n" \
  ".set .Lpad0x96, . - 4\n" \
  "s_waitcnt lgkmcnt(2)\n" \
  "v_mfma_f32_16x16x32_bf16 v[24:27], v[84:87], v[40:43], v[24:27]\n" \
  "ds_read_b128 v[36:39], v7 offset:16640\n" \
  "ds_read_b32 v11, v11 offset:0\n" \
  "v_mfma_f32_16x16x32_bf16 v[28:31], v[148:151], v[40:43], v[28:31]\n" \
  "ds_read2_b32 v[52:53], v8 offset0:0 offset1:0\n" \
  "v_mfma_f32_16x16x32_bf16 v[32:35], v[40:43], v[212:215], v[32:35]\n" \
  "s_waitcnt lgkmcnt(4)\n" \
  "v_mfma_f32_16x16x32_bf16 v[24:27], v[88:91], v[44:47], v[24:27]\n" \
  "ds_read_b128 v[40:43], v7 offset:18688\n" \
  "v_mfma_f32_16x16x32_bf16 v[28:31], v[152:155], v[44:47], v[28:31]\n" \
  "ds_read2_b32 v[54:55], v8 offset0:16 offset1:16\n" \
  "v_mfma_f32_16x16x32_bf16 v[32:35], v[44:47], v[216:219], v[32:35]\n" \
  "s_waitcnt lgkmcnt(3)\n" \
  "v_mfma_f32_16x16x32_bf16 v[24:27], v[92:95], v[48:51], v[24:27]\n" \
  "ds_read_b128 v[44:47], v7 offset:20736\n" \
  "v_readfirstlane_b32 s69, v11\n" \
  "s_cmp_eq_u32 s69, 1\n" \
  "s_cselect_b32 s76, 0x100000, s62\n" \
  "v_mfma_f32_16x16x32_bf16 v[28:31], v[156:159], v[48:51], v[28:31]\n" \
  "buffer_load_dword v11, v10, s[52:55], s76 offen sc1\n" \
  "v_mfma_f32_16x16x32_bf16 v[32:35], v[48:51], v[220:223], v[32:35]\n" \
  "ds_read2_b32 v[56:57], v8 offset0:32 offset1:48\n" \
  "v_mfma_f32_16x16x32_bf16 v[24:27], v[96:99], v[36:39], v[24:27]\n" \
  "ds_read_b128 v[48:51], v7 offset:22784\n" \
  "v_mfma_f32_16x16x32_bf16 v[28:31], v[160:163], v[36:39], v[28:31]\n" \
  "v_mfma_f32_16x16x32_bf16 v[32:35], v[36:39], v[224:227], v[32:35]\n" \
  "ds_read_b128 v[58:61], v9 offset:128\n" \
  "s_waitcnt lgkmcnt(6)\n" \
  "v_mfma_f32_16x16x16_bf16 v[12:15], v[0:1], v[52:53], v[12:15]\n" \
  "v_mfma_f32_16x16x16_bf16 v[16:19], v[2:3], v[52:53], v[16:19]\n" \
  "v_mfma_f32_16x16x16_bf16 v[20:23], v[52:53], v[4:5], v[20:23]\n" \
  "s_waitcnt lgkmcnt(4)\n" \
  "v_mfma_f32_16x16x16_bf16 v[24:27], v[0:1], v[54:55], v[24:27]\n" \
  "v_mfma_f32_16x16x16_bf16 v[28:31], v[2:3], v[54:55], v[28:31]\n" \
  "v_mfma_f32_16x16x16_bf16 v[32:35], v[54:55], v[4:5], v[32:35]\n" \
  "v_mfma_f32_16x16x32_bf16 v[24:27], v[100:103], v[40:43], v[24:27]\n" \
  "ds_read_b128 v[36:39], v7 offset:24832\n" \
  "v_mfma_f32_16x16x32_bf16 v[28:31], v[164:167], v[40:43], v[28:31]\n" \
  "v_mfma_f32_16x16x32_bf16 v[32:35], v[40:43], v[228:231], v[32:35]\n" \
  "ds_read_b128 v[52:55], v9 offset:192\n" \
  "s_waitcnt lgkmcnt(5)\n" \
  "v_mfma_f32_16x16x32_bf16 v[24:27], v[104:107], v[44:47], v[24:27]\n" \
  "ds_read_b128 v[40:43], v7 offset:26880\n" \
  "v_mfma_f32_16x16x32_bf16 v[28:31], v[168:171], v[44:47], v[28:31]\n" \
  "v_mfma_f32_16x16x32_bf16 v[32:35], v[44:47], v[232:235], v[32:35]\n" \
  "s_waitcnt lgkmcnt(4)\n" \
  "v_mfma_f32_16x16x32_bf16 v[24:27], v[108:111], v[48:51], v[24:27]\n" \
  "ds_read_b128 v[44:47], v7 offset:28928\n" \
  "v_mfma_f32_16x16x32_bf16 v[28:31], v[172:175], v[48:51], v[28:31]\n" \
  "v_mfma_f32_16x16x32_bf16 v[32:35], v[48:51], v[236:239], v[32:35]\n" \
  "s_waitcnt lgkmcnt(3)\n" \
  "v_mfma_f32_16x16x32_bf16 v[24:27], v[112:115], v[36:39], v[24:27]\n" \
  "ds_read_b128 v[48:51], v7 offset:30976\n" \
  "v_mfma_f32_16x16x32_bf16 v[28:31], v[176:179], v[36:39], v[28:31]\n" \
  "v_mfma_f32_16x16x32_bf16 v[32:35], v[36:39], v[240:243], v[32:35]\n" \
  "s_waitcnt lgkmcnt(2)\n" \
  "v_mfma_f32_16x16x32_bf16 v[24:27], v[116:119], v[40:43], v[24:27]\n" \
  "v_mfma_f32_16x16x32_bf16 v[28:31], v[180:183], v[40:43], v[28:31]\n" \
  "v_mfma_f32_16x16x32_bf16 v[32:35], v[40:43], v[244:247], v[32:35]\n" \
  "s_waitcnt lgkmcnt(1)\n" \
  "v_mfma_f32_16x16x32_bf16 v[24:27], v[120:123], v[44:47], v[24:27]\n" \
  "v_mfma_f32_16x16x32_bf16 v[28:31], v[184:187], v[44:47], v[28:31]\n" \
  "v_mfma_f32_16x16x32_bf16 v[32:35], v[44:47], v[248:251], v[32:35]\n" \
  "s_waitcnt lgkmcnt(0)\n" \
  "v_mfma_f32_16x16x32_bf16 v[24:27], v[124:127], v[48:51], v[24:27]\n" \
  "v_mfma_f32_16x16x32_bf16 v[28:31], v[188:191], v[48:51], v[28:31]\n" \
  "v_mfma_f32_16x16x32_bf16 v[32:35], v[48:51], v[252:255], v[32:35]\n" \
  "s_setprio 2\n" \
  "v_mul_f32 v12, v12, v56\n" \
  "v_mul_f32 v13, v13, v56\n" \
  "v_mul_f32 v14, v14, v56\n" \
  "v_cvt_pk_bf16_f32 v12, v12, v13\n" \
  "v_mul_f32 v15, v15, v56\n" \
  "v_mul_f32 v16, v16, v56\n" \
  "v_cvt_pk_bf16_f32 v13, v14, v15\n" \
  "v_mul_f32 v17, v17, v56\n" \
  "v_mul_f32 v18, v18, v56\n" \
  "v_cvt_pk_bf16_f32 v14, v16, v17\n" \
  "v_mul_f32 v19, v19, v56\n" \
  "v_cvt_pk_bf16_f32 v15, v18, v19\n" \
  "buffer_store_dwordx4 v[12:15], v6, s[40:43], s59 offen offset:0 sc1\n" \
  "s_cmp_eq_u32 s69, 1\n" \
  "s_cbranch_scc1 .LQA_EPI_%=\n" \
  "s_waitcnt vmcnt(1)\n" \
  "v_cmp_eq_u32 vcc, 0, v11\n" \
  "s_cmp_eq_u64 vcc, 0\n" \
  "s_cbranch_scc0 .LQA_MISS_%=\n" \
  "s_mov_b32 m0, s58\n" \
  "s_add_u32 s74, s56, 4096\n" \
  "s_addc_u32 s75, s57, 0\n" \
  "s_add_u32 s76, s58, 4096\n" \
  "global_load_lds_dwordx4 v6, s[56:57] offset:0 sc1\n" \
  "global_load_lds_dwordx4 v6, s[56:57] offset:1024 sc1\n" \
  "global_load_lds_dwordx4 v6, s[56:57] offset:2048 sc1\n" \
  "global_load_lds_dwordx4 v6, s[56:57] offset:3072 sc1\n" \
  "s_mov_b32 m0, s76\n" \
  "s_nop 0\n" \
  "global_load_lds_dwordx4 v6, s[74:75] offset:0 sc1\n" \
  "global_load_lds_dwordx4 v6, s[74:75] offset:1024 sc1\n" \
  "global_load_lds_dwordx4 v6, s[74:75] offset:2048 sc1\n" \
  "global_load_lds_dwordx4 v6, s[74:75] offset:3072 sc1\n" \
  "s_mov_b32 s69, 3\n" \
  ".LQA_EPI_%=:\n" \
  "v_mul_f32 v24, v24, v57\n" \
  "v_mul_f32 v25, v25, v57\n" \
  "v_mul_f32 v26, v26, v57\n" \
  "v_cvt_pk_bf16_f32 v24, v24, v25\n" \
  "v_mul_f32 v27, v27, v57\n" \
  "v_mul_f32 v28, v28, v57\n" \
  "v_cvt_pk_bf16_f32 v25, v26, v27\n" \
  "v_mul_f32 v29, v29, v57\n" \
  "v_mul_f32 v30, v30, v57\n" \
  "v_cvt_pk_bf16_f32 v26, v28, v29\n" \
  "v_mul_f32 v31, v31, v57\n" \
  "v_cvt_pk_bf16_f32 v27, v30, v31\n" \
  "s_add_u32 s74, s59, 4096\n" \
  "buffer_store_dwordx4 v[24:27], v6, s[40:43], s74 offen offset:0 sc1\n" \
  "v_mul_f32 v20, v20, v58\n" \
  "v_mul_f32 v21, v21, v59\n" \
  "v_mul_f32 v22, v22, v60\n" \
  "v_mul_f32 v23, v23, v61\n" \
  "v_cvt_pk_bf16_f32 v20, v20, v21\n" \
  "v_cvt_pk_bf16_f32 v21, v22, v23\n" \
  "v_mul_f32 v32, v32, v52\n" \
  "v_mul_f32 v33, v33, v53\n" \
  "v_mul_f32 v34, v34, v54\n" \
  "v_mul_f32 v35, v35, v55\n" \
  "v_cvt_pk_bf16_f32 v22, v32, v33\n" \
  "v_cvt_pk_bf16_f32 v23, v34, v35\n" \
  "buffer_store_dwordx4 v[20:23], v6, s[44:47], s60 offen offset:0 sc1\n" \
  "s_setprio 0\n" \
  "s_add_u32 s59, s59, 0x80000\n" \
  "s_add_u32 s60, s60, 0x40000\n" \
  "s_add_u32 s61, s61, 0x10000\n" \
  "v_xor_b32 v7, 0x20000, v7\n" \
  "s_cmp_eq_u32 s69, 1\n" \
  "s_waitcnt vmcnt(3)\n" \
  "s_cbranch_scc1 .LQA_GO_%=\n" \
  "s_lshr_b32 s69, s69, 1\n" \
  "s_waitcnt vmcnt(2)\n" \
  "s_cbranch_scc1 .LQA_GO_%=\n" \
  "v_cmp_eq_u32 vcc, 0, v11\n" \
  "s_cmp_eq_u64 vcc, 0\n" \
  "s_cbranch_scc0 .LQA_NOX_%=\n" \
  "s_mov_b32 m0, s58\n" \
  "s_add_u32 s74, s56, 4096\n" \
  "s_addc_u32 s75, s57, 0\n" \
  "s_add_u32 s76, s58, 4096\n" \
  "global_load_lds_dwordx4 v6, s[56:57] offset:0 sc1\n" \
  "global_load_lds_dwordx4 v6, s[56:57] offset:1024 sc1\n" \
  "global_load_lds_dwordx4 v6, s[56:57] offset:2048 sc1\n" \
  "global_load_lds_dwordx4 v6, s[56:57] offset:3072 sc1\n" \
  "s_mov_b32 m0, s76\n" \
  "s_nop 0\n" \
  "global_load_lds_dwordx4 v6, s[74:75] offset:0 sc1\n" \
  "global_load_lds_dwordx4 v6, s[74:75] offset:1024 sc1\n" \
  "global_load_lds_dwordx4 v6, s[74:75] offset:2048 sc1\n" \
  "global_load_lds_dwordx4 v6, s[74:75] offset:3072 sc1\n" \
  "s_mov_b32 s69, 2\n" \
  "s_waitcnt vmcnt(8)\n" \
  "s_branch .LQA_CNT_%=\n" \
  ".Lpad0x97:\n" \
  "s_setpc_b64 s[98:99]\n" \
  ".LQA_MISS_%=:\n" \
  "buffer_load_dword v11, v10, s[52:55], s62 offen sc1\n" \
  "s_branch .LQA_EPI_%=\n" \
  ".LQA_NOX_%=:\n" \
  "s_waitcnt vmcnt(0)\n" \
  ".LQA_CNT_%=:\n" \
  "v_mov_b32 v52, s80\n" \
  "v_mov_b32 v53, 0x10000\n" \
  "s_mov_b64 exec, 1\n" \
  "ds_add_rtn_u32 v54, v52, v53\n" \
  "s_memrealtime s[0:1]\n" \
  "s_mov_b64 exec, -1\n" \
  "s_add_u32 s67, s67, 4\n" \
  "s_add_u32 s64, s64, 4096\n" \
  "s_addc_u32 s65, s65, 0\n" \
  "v_mov_b32 v55, 0\n" \
  "s_waitcnt lgkmcnt(0)\n" \
  "v_readfirstlane_b32 s74, v54\n" \
  "s_and_b32 s76, s74, 0x30000\n" \
  "s_cmp_lg_u32 s76, 0x30000\n" \
  "s_cbranch_scc1 .LQA_FWN_%=\n" \
  "s_mov_b32 s79, 0x100000\n" \
  "s_branch .LQA_FWT_%=\n" \
  ".LQA_FW_%=:\n" \
  "ds_read_b32 v54, v52\n" \
  "s_sub_u32 s79, s79, 1\n" \
  "s_cbranch_scc1 .LQA_FWE_%=\n" \
  "s_waitcnt lgkmcnt(0)\n" \
  "v_readfirstlane_b32 s74, v54\n" \
  ".LQA_FWT_%=:\n" \
  "s_and_b32 s74, s74, 0xffff\n" \
  "s_cmp_lt_u32 s74, s67\n" \
  "s_cbranch_scc1 .LQA_FW_%=\n" \
  ".LQA_FWE_%=:\n" \
  "v_or_b32 v53, 1, s0\n" \
  "s_cmp_lg_u32 s67, s83\n" \
  "s_cbranch_scc1 .LQA_FWX_%=\n" \
  "v_lshlrev_b32 v54, 5, v6\n" \
  "s_mov_b64 exec, 15\n" \
  "global_store_dword v54, v53, s[84:85] sc1\n" \
  ".LQA_FWX_%=:\n" \
  "s_mov_b64 exec, 1\n" \
  "global_store_dword v55, v53, s[64:65] offset:-4096 sc1\n" \
  "s_mov_b64 exec, -1\n" \
  "s_waitcnt vmcnt(1)\n" \
  "s_branch .LQA_GO_%=\n" \
  ".LQA_FWN_%=:\n" \
  "s_waitcnt vmcnt(0)\n" \
  ".LQA_GO_%=:\n" \
  "s_sub_u32 s66, s66, 1\n" \
  "s_cbranch_scc1 .LQA_LAST_%=\n" \
  "s_cmp_lg_u32 s69, 0\n" \
  "s_cbranch_scc1 .LQA_NEXT_%=\n" \
  ".LQA_GATE_%=:\n" \
  "s_memrealtime s[74:75]\n" \
  "s_waitcnt lgkmcnt(0)\n" \
  "s_sub_i32 s74, s68, s74\n" \
  "s_cmp_gt_u32 s74, %[gq]\n" \
  "s_cbranch_scc1 .LQA_POLL_%=\n" \
  "s_sleep 3\n" \
  "s_branch .LQA_GATE_%=\n" \
  ".LQA_POLL_%=:\n" \
  "s_mov_b32 s79, 0x40000\n" \
  ".LQA_AGAIN_%=:\n" \
  "buffer_load_dword v11, v10, s[52:55], s62 offen sc1\n" \
  "s_sub_u32 s79, s79, 1\n" \
  "s_cbranch_scc1 .LQA_SEEN_%=\n" \
  "s_waitcnt vmcnt(0)\n" \
  "v_cmp_eq_u32 vcc, 0, v11\n" \
  "s_cmp_eq_u64 vcc, 0\n" \
  "s_cbranch_scc1 .LQA_SEEN_%=\n" \
  "s_sleep %[sq]\n" \
  "s_cmp_ge_u32 s66, s63\n" \
  "s_cbranch_scc1 .LQA_AGAIN_%=\n" \
  "s_sleep %[s1]\n" \
  "s_branch .LQA_AGAIN_%=\n" \
  ".LQA_SEEN_%=:\n" \
  "s_mov_b32 m0, s58\n" \
  "s_add_u32 s74, s56, 4096\n" \
  "s_addc_u32 s75, s57, 0\n" \
  "s_add_u32 s76, s58, 4096\n" \
  "global_load_lds_dwordx4 v6, s[56:57] offset:0 sc1\n" \
  "global_load_lds_dwordx4 v6, s[56:57] offset:1024 sc1\n" \
  "global_load_lds_dwordx4 v6, s[56:57] offset:2048 sc1\n" \
  "global_load_lds_dwordx4 v6, s[56:57] offset:3072 sc1\n" \
  "s_mov_b32 m0, s76\n" \
  "s_nop 0\n" \
  "global_load_lds_dwordx4 v6, s[74:75] offset:0 sc1\n" \
  "global_load_lds_dwordx4 v6, s[74:75] offset:1024 sc1\n" \
  "global_load_lds_dwordx4 v6, s[74:75] offset:2048 sc1\n" \
  "global_load_lds_dwordx4 v6, s[74:75] offset:3072 sc1\n" \
  ".LQA_GOT_%=:\n" \
  "s_memrealtime s[76:77]\n" \
  "s_waitcnt lgkmcnt(0)\n" \
  "s_add_u32 s68, s76, %[gq]\n" \
  "s_waitcnt vmcnt(0)\n" \
  ".LQA_NEXT_%=:\n" \
  "s_add_u32 s56, s56, 0x8000\n" \
  "s_addc_u32 s57, s57, 0\n" \
  "s_xor_b32 s58, s58, 0x20000\n" \
  "s_add_u32 s62, s62, 4096\n" \
  "s_branch .LQA_TOP_%=\n" \
  ".LQA_LAST_%=:\n" \
  "s_barrier\n" \
  "s_cmp_eq_u32 s86, 0\n" \
  "s_cbranch_scc1 .LQA_END_%=\n" \
  "s_mov_b32 s86, 0\n" \
  "s_mov_b32 s66, 63\n" \
  "s_mov_b32 s62, 0x40000\n" \
  "s_add_u32 s54, s54, (128 - %[n0]) * 4096\n" \
  "s_add_u32 s59, s59, (64 - %[n0]) * 0x80000\n" \
  "s_add_u32 s60, s60, (64 - %[n0]) * 0x40000\n" \
  "s_add_u32 s61, s61, (64 - %[n0]) * 0x10000\n" \
  "s_add_u32 s64, s64, (65 - %[n0]) * 4096\n" \
  "s_addc_u32 s65, s65, 0\n" \
  "s_add_u32 s56, s56, %[x3]\n" \
  "s_addc_u32 s57, s57, 0\n" \
  "s_add_u32 s84, s84, 2048\n" \
  "s_addc_u32 s85, s85, 0\n" \
  "s_add_u32 s67, s67, 4\n" \
  "s_add_u32 s83, s67, 4\n" \
  "s_branch .LQA_POLL_%=\n" \
  ".LQA_END_%=:\n" \
  "s_endpgm\n" \
  ".p2alignl 12, 0xbf800000\n" \
  ".Lpad0xba:\n" \
  "s_setpc_b64 s[98:99]\n"
#define QKV_ASM_1 \
  ".LQB_GO_%=:\n" \
  "s_barrier\n" \
  "ds_read_b128 v[36:39], v10 offset:0\n" \
  "ds_read_b128 v[40:43], v7 offset:0\n" \
  "ds_read_b128 v[44:47], v10 offset:2048\n" \
  "ds_read_b128 v[48:51], v7 offset:2048\n" \
  "s_branch .LQB_AL_%=\n" \
  ".p2alignl 12, 0xbf800000, 3200\n" \
  ".p2align 6\n" \
  ".Lpad0xa9:\n" \
  "s_setpc_b64 s[98:99]\n" \
  ".p2align 6\n" \
  ".LQB_AL_%=:\n" \
  ".LQB_TOP_%=:\n" \
  "s_setprio 3\n" \
  "s_waitcnt lgkmcnt(3)\n" \
  "v_mfma_f32_16x16x32_bf16 v[56:59], v[36:39], v[36:39], 0\n" \
  "ds_read_b128 v[52:55], v10 offset:4096\n" \
  "s_add_u32 s56, s56, 0x8000\n" \
  "s_addc_u32 s57, s57, 0\n" \
  "s_xor_b32 s58, s58, 0x20000\n" \
  "s_add_u32 s62, s62, 4096\n" \
  "s_waitcnt lgkmcnt(3)\n" \
  "v_mfma_f32_16x16x32_bf16 v[12:15], v[40:43], v[192:195], 0\n" \
  "ds_read_b128 v[36:39], v7 offset:4096\n" \
  "s_waitcnt lgkmcnt(3)\n" \
  "v_mfma_f32_16x16x32_bf16 v[56:59], v[44:47], v[44:47], v[56:59]\n" \
  "ds_read_b128 v[40:43], v10 offset:6144\n" \
  "s_waitcnt lgkmcnt(3)\n" \
  "v_mfma_f32_16x16x32_bf16 v[12:15], v[48:51], v[196:199], v[12:15]\n" \
  "ds_read_b128 v[44:47], v7 offset:6144\n" \
  "s_waitcnt lgkmcnt(3)\n" \
  "v_mfma_f32_16x16x32_bf16 v[56:59], v[52:55], v[52:55], v[56:59]\n" \
  "ds_read_b128 v[48:51], v10 offset:8192\n" \
  "s_waitcnt lgkmcnt(3)\n" \
  "v_mfma_f32_16x16x32_bf16 v[12:15], v[36:39], v[200:203], v[12:15]\n" \
  "ds_read_b128 v[52:55], v7 offset:8192\n" \
  "s_waitcnt lgkmcnt(3)\n" \
  "v_mfma_f32_16x16x32_bf16 v[56:59], v[40:43], v[40:43], v[56:59]\n" \
  "ds_read_b128 v[36:39], v10 offset:10240\n" \
  "s_waitcnt lgkmcnt(3)\n" \
  "v_mfma_f32_16x16x32_bf16 v[12:15], v[44:47], v[204:207], v[12:15]\n" \
  "ds_read_b128 v[40:43], v7 offset:10240\n" \
  "s_waitcnt lgkmcnt(3)\n" \
  "v_mfma_f32_16x16x32_bf16 v[56:59], v[48:51], v[48:51], v[56:59]\n" \
  "ds_read_b128 v[44:47], v10 offset:12288\n" \
  "s_waitcnt lgkmcnt(3)\n" \
  "v_mfma_f32_16x16x32_bf16 v[12:15], v[52:55], v[208:211], v[12:15]\n" \
  "ds_read_b128 v[48:51], v7 offset:12288\n" \
  "s_waitcnt lgkmcnt(3)\n" \
  "v_mfma_f32_16x16x32_bf16 v[56:59], v[36:39], v[36:39], v[56:59]\n" \
  "ds_read_b128 v[52:55], v10 offset:14336\n" \
  "s_waitcnt lgkmcnt(3)\n" \
  "v_mfma_f32_16x16x32_bf16 v[12:15], v[40:43], v[212:215], v[12:15]\n" \
  "ds_read_b128 v[36:39], v7 offset:14336\n" \
  "s_waitcnt lgkmcnt(3)\n" \
  "v_mfma_f32_16x16x32_bf16 v[56:59], v[44:47], v[44:47], v[56:59]\n" \
  "ds_read_b128 v[40:43], v10 offset:16384\n" \
  "s_waitcnt lgkmcnt(3)\n" \
  "v_mfma_f32_16x16x32_bf16 v[12:15], v[48:51], v[216:219], v[12:15]\n" \
  "ds_read_b128 v[44:47], v7 offset:16384\n" \
  "s_waitcnt lgkmcnt(3)\n" \
  "v_mfma_f32_16x16x32_bf16 v[56:59], v[52:55], v[52:55], v[56:59]\n" \
  "ds_read_b128 v[48:51], v10 offset:18432\n" \
  "s_waitcnt lgkmcnt(3)\n" \
  "v_mfma_f32_16x16x32_bf16 v[12:15], v[36:39], v[220:223], v[12:15]\n" \
  "ds_read_b128 v[52:55], v7 offset:18432\n" \
  "s_waitcnt lgkmcnt(3)\n" \
  "v_mfma_f32_16x16x32_bf16 v[56:59], v[40:43], v[40:43], v[56:59]\n" \
  "ds_read_b128 v[36:39], v10 offset:20480\n" \
  "s_waitcnt lgkmcnt(3)\n" \
  "v_mfma_f32_16x16x32_bf16 v[12:15], v[44:47], v[224:227], v[12:15]\n" \
  "ds_read_b128 v[40:43], v7 offset:20480\n" \
  "s_waitcnt lgkmcnt(3)\n" \
  "v_mfma_f32_16x16x32_bf16 v[56:59], v[48:51], v[48:51], v[56:59]\n" \
  "ds_read_b128 v[44:47], v10 offset:22528\n" \
  "s_waitcnt lgkmcnt(3)\n" \
  "v_mfma_f32_16x16x32_bf16 v[12:15], v[52:55], v[228:231], v[12:15]\n" \
  "ds_read_b128 v[48:51], v7 offset:22528\n" \
  "s_waitcnt lgkmcnt(3)\n" \
  "v_mfma_f32_16x16x32_bf16 v[56:59], v[36:39], v[36:39], v[56:59]\n" \
  "ds_read_b128 v[52:55], v10 offset:24576\n" \
  "s_waitcnt lgkmcnt(3)\n" \
  "v_mfma_f32_16x16x32_bf16 v[12:15], v[40:43], v[232:235], v[12:15]\n" \
  "ds_read_b128 v[36:39], v7 offset:24576\n" \
  "s_waitcnt lgkmcnt(3)\n" \
  "v_mfma_f32_16x16x32_bf16 v[56:59], v[44:47], v[44:47], v[56:59]\n" \
  "ds_read_b128 v[40:43], v10 offset:26624\n" \
  "s_waitcnt lgkmcnt(3)\n" \
  "v_mfma_f32_16x16x32_bf16 v[12:15], v[48:51], v[236:239], v[12:15]\n" \
  "ds_read_b128 v[44:47], v7 offset:26624\n" \
  "s_waitcnt lgkmcnt(3)\n" \
  "v_mfma_f32_16x16x32_bf16 v[56:59], v[52:55], v[52:55], v[56:59]\n" \
  "ds_read_b128 v[48:51], v10 offset:28672\n" \
  "s_waitcnt lgkmcnt(3)\n" \
  "v_mfma_f32_16x16x32_bf16 v[12:15], v[36:39], v[240:243], v[12:15]\n" \
  "ds_read_b128 v[52:55], v7 offset:28672\n" \
  "s_waitcnt lgkmcnt(3)\n" \
  "v_mfma_f32_16x16x32_bf16 v[56:59], v[40:43], v[40:43], v[56:59]\n" \
  "ds_read_b128 v[36:39], v10 offset:30720\n" \
  "s_waitcnt lgkmcnt(3)\n" \
  "v_mfma_f32_16x16x32_bf16 v[12:15], v[44:47], v[244:247], v[12:15]\n" \
  "ds_read_b128 v[40:43], v7 offset:30720\n" \
  "s_waitcnt lgkmcnt(3)\n" \
  "v_mfma_f32_16x16x32_bf16 v[56:59], v[48:51], v[48:51], v[56:59]\n" \
  "ds_read_b128 v[44:47], v7 offset:256\n" \
  "s_waitcnt lgkmcnt(3)\n" \
  "v_mfma_f32_16x16x32_bf16 v[12:15], v[52:55], v[248:251], v[12:15]\n" \
  "ds_read_b128 v[48:51], v7 offset:2304\n" \
  "s_waitcnt lgkmcnt(3)\n" \
  "v_mfma_f32_16x16x32_bf16 v[56:59], v[36:39], v[36:39], v[56:59]\n" \
  "ds_read_b128 v[52:55], v7 offset:4352\n" \
  "s_waitcnt lgkmcnt(3)\n" \
  "v_mfma_f32_16x16x32_bf16 v[12:15], v[40:43], v[252:255], v[12:15]\n" \
  "ds_read_b128 v[36:39], v7 offset:6400\n" \
  "s_waitcnt lgkmcnt(3)\n" \
  "v_mfma_f32_16x16x32_bf16 v[16:19], v[44:47], v[192:195], 0\n" \
  "ds_read_b128 v[40:43], v7 offset:8448\n" \
  "s_waitcnt lgkmcnt(3)\n" \
  "v_mfma_f32_16x16x32_bf16 v[16:19], v[48:51], v[196:199], v[16:19]\n" \
  "ds_read_b128 v[44:47], v7 offset:10496\n" \
  "s_waitcnt lgkmcnt(3)\n" \
  "v_mfma_f32_16x16x32_bf16 v[16:19], v[52:55], v[200:203], v[16:19]\n" \
  "ds_read_b128 v[48:51], v7 offset:12544\n" \
  "s_waitcnt lgkmcnt(3)\n" \
  "v_mfma_f32_16x16x32_bf16 v[16:19], v[36:39], v[204:207], v[16:19]\n" \
  "ds_read_b128 v[52:55], v7 offset:14592\n" \
  "s_waitcnt lgkmcnt(3)\n" \
  "v_mfma_f32_16x16x32_bf16 v[16:19], v[40:43], v[208:211], v[16:19]\n" \
  "ds_read_b128 v[36:39], v7 offset:16640\n" \
  "s_waitcnt lgkmcnt(3)\n" \
  "v_mfma_f32_16x16x32_bf16 v[16:19], v[44:47], v[212:215], v[16:19]\n" \
  "ds_read_b128 v[40:43], v7 offset:18688\n" \
  "s_waitcnt lgkmcnt(3)\n" \
  "v_mfma_f32_16x16x32_bf16 v[16:19], v[48:51], v[216:219], v[16:19]\n" \
  "ds_read_b128 v[44:47], v7 offset:20736\n" \
  "s_waitcnt lgkmcnt(3)\n" \
  "v_mfma_f32_16x16x32_bf16 v[16:19], v[52:55], v[220:223], v[16:19]\n" \
  "ds_read_b128 v[48:51], v7 offset:22784\n" \
  "s_waitcnt lgkmcnt(3)\n" \
  "v_mfma_f32_16x16x32_bf16 v[16:19], v[36:39], v[224:227], v[16:19]\n" \
  "ds_read_b128 v[52:55], v7 offset:24832\n" \
  "s_waitcnt lgkmcnt(3)\n" \
  "v_mfma_f32_16x16x32_bf16 v[16:19], v[40:43], v[228:231], v[16:19]\n" \
  "ds_read_b128 v[36:39], v7 offset:26880\n" \
  "s_waitcnt lgkmcnt(3)\n" \
  "v_mfma_f32_16x16x32_bf16 v[16:19], v[44:47], v[232:235], v[16:19]\n" \
  "ds_read_b128 v[40:43], v7 offset:28928\n" \
  "s_waitcnt lgkmcnt(3)\n" \
  "v_mfma_f32_16x16x32_bf16 v[16:19], v[48:51], v[236:239], v[16:19]\n" \
  "ds_read_b128 v[44:47], v7 offset:30976\n" \
  "s_waitcnt lgkmcnt(3)\n" \
  "v_mfma_f32_16x16x32_bf16 v[16:19], v[52:55], v[240:243], v[16:19]\n" \
  "ds_read_b128 v[48:51], v7 offset:0\n" \
  "v_lshrrev_b32 v63, 2, v6\n" \
  "v_and_b32 v63, 0x9c, v63\n" \
  "buffer_load_dword v63, v63, s[52:55], s62 offen sc1\n" \
  "s_waitcnt lgkmcnt(3)\n" \
  "v_mfma_f32_16x16x32_bf16 v[16:19], v[36:39], v[244:247], v[16:19]\n" \
  "ds_read_b128 v[52:55], v7 offset:2048\n" \
  "s_waitcnt lgkmcnt(3)\n" \
  "v_mfma_f32_16x16x32_bf16 v[16:19], v[40:43], v[248:251], v[16:19]\n" \
  "ds_read_b128 v[36:39], v7 offset:4096\n" \
  "s_waitcnt lgkmcnt(3)\n" \
  "v_mfma_f32_16x16x32_bf16 v[16:19], v[44:47], v[252:255], v[16:19]\n" \
  "ds_read_b128 v[40:43], v7 offset:6144\n" \
  "s_setprio 0\n" \
  "s_mov_b32 s0, 0xbe801d62\n" \
  ".set .Lpad0xaa, . - 4\n" \
  "v_cndmask_b32 v56, v56, v57, s[70:71]\n" \
  "v_cndmask_b32 v57, v58, v59, s[70:71]\n" \
  "v_cndmask_b32 v56, v56, v57, s[72:73]\n" \
  "v_mov_b32 v57, v56\n" \
  "v_mov_b32 v58, v56\n" \
  "v_mul_f32 v56, 0x3b000000, v56\n" \
  "v_xor_b32 v10, v11, v10\n" \
  "v_permlane32_swap_b32 v57, v58\n" \
  "v_mul_f32 v57, 0xbb000000, v58\n" \
  "v_fma_f32 v56, -v57, v57, v56\n" \
  "v_max_f32 v56, 0, v56\n" \
  "v_cvt_pk_bf16_f32 v56, v56, v56\n" \
  "v_lshlrev_b32 v56, 16, v56\n" \
  "v_add_f32 v56, 0x3727c5ac, v56\n" \
  "v_cvt_pk_bf16_f32 v56, v56, v56\n" \
  "v_lshlrev_b32 v56, 16, v56\n" \
  "v_sqrt_f32 v56, v56\n" \
  "v_cvt_pk_bf16_f32 v57, v57, v57\n" \
  "v_cvt_pk_bf16_f32 v56, v56, v56\n" \
  "v_lshlrev_b32 v56, 16, v56\n" \
  "v_rcp_f32 v56, v56\n" \
  "s_mov_b64 exec, 0xf0000f\n" \
  "ds_write2_b32 v62, v57, v56 offset1:32\n" \
  "s_mov_b64 exec, -1\n" \
  "s_cmp_eq_u32 s66, s83\n" \
  "s_cbranch_scc1 .LQB_NOE_%=\n" \
  "v_mul_f32 v20, v20, v60\n" \
  "v_mul_f32 v21, v21, v60\n" \
  "v_mul_f32 v22, v22, v60\n" \
  "v_cvt_pk_bf16_f32 v20, v20, v21\n" \
  "v_mul_f32 v23, v23, v60\n" \
  "v_mul_f32 v24, v24, v60\n" \
  "v_cvt_pk_bf16_f32 v21, v22, v23\n" \
  "v_mul_f32 v25, v25, v60\n" \
  "v_mul_f32 v26, v26, v60\n" \
  "v_cvt_pk_bf16_f32 v22, v24, v25\n" \
  "v_mul_f32 v27, v27, v60\n" \
  "v_cvt_pk_bf16_f32 v23, v26, v27\n" \
  "buffer_store_dwordx4 v[20:23], v6, s[40:43], s59 offen offset:0 sc1\n" \
  "s_add_u32 s76, s59, 4096\n" \
  "v_mul_f32 v28, v28, v61\n" \
  "v_mul_f32 v29, v29, v61\n" \
  "v_mul_f32 v30, v30, v61\n" \
  "v_cvt_pk_bf16_f32 v28, v28, v29\n" \
  "v_mul_f32 v31, v31, v61\n" \
  "v_mul_f32 v32, v32, v61\n" \
  "v_cvt_pk_bf16_f32 v29, v30, v31\n" \
  "v_mul_f32 v33, v33, v61\n" \
  "v_mul_f32 v34, v34, v61\n" \
  "v_cvt_pk_bf16_f32 v30, v32, v33\n" \
  "v_mul_f32 v35, v35, v61\n" \
  "v_cvt_pk_bf16_f32 v31, v34, v35\n" \
  "buffer_store_dwordx4 v[28:31], v6, s[40:43], s76 offen offset:0 sc1\n" \
  "s_add_u32 s59, s59, 0x80000\n" \
  "s_waitcnt vmcnt(2)\n" \
  "s_branch .LQB_LOOK_%=\n" \
  ".LQB_NOE_%=:\n" \
  "s_waitcnt vmcnt(0)\n" \
  ".LQB_LOOK_%=:\n" \
  "v_cmp_eq_u32 vcc, 0, v63\n" \
  "v_mov_b32 v58, s82\n" \
  "s_cmp_eq_u64 vcc, 0\n" \
  "s_cselect_b32 s69, 1, 0\n" \
  "v_mov_b32 v59, s69\n" \
  "ds_write_b32 v58, v59\n" \
  "s_cbranch_scc0 .LQB_NOD_%=\n" \
  "s_mov_b32 m0, s58\n" \
  "s_add_u32 s74, s56, 4096\n" \
  "s_addc_u32 s75, s57, 0\n" \
  "s_add_u32 s76, s58, 4096\n" \
  "global_load_lds_dwordx4 v6, s[56:57] offset:0 sc1\n" \
  "global_load_lds_dwordx4 v6, s[56:57] offset:1024 sc1\n" \
  "global_load_lds_dwordx4 v6, s[56:57] offset:2048 sc1\n" \
  "global_load_lds_dwordx4 v6, s[56:57] offset:3072 sc1\n" \
  "s_mov_b32 m0, s76\n" \
  "s_nop 0\n" \
  "global_load_lds_dwordx4 v6, s[74:75] offset:0 sc1\n" \
  "global_load_lds_dwordx4 v6, s[74:75] offset:1024 sc1\n" \
  "global_load_lds_dwordx4 v6, s[74:75] offset:2048 sc1\n" \
  "global_load_lds_dwordx4 v6, s[74:75] offset:3072 sc1\n" \
  ".LQB_NOD_%=:\n" \
  "s_waitcnt lgkmcnt(0)\n" \
  "s_barrier\n" \
  "ds_read2_b32 v[56:57], v8 offset0:0 offset1:0\n" \
  "ds_read2_b32 v[58:59], v8 offset0:16 offset1:16\n" \
  "ds_read_b128 v[28:31], v9 offset:128\n" \
  "ds_read_b128 v[32:35], v9 offset:192\n" \
  "ds_read2_b32 v[60:61], v8 offset0:32 offset1:48\n" \
  "s_setprio 3\n" \
  "s_waitcnt lgkmcnt(3)\n" \
  "v_mfma_f32_16x16x16_bf16 v[12:15], v[56:57], v[4:5], v[12:15]\n" \
  "v_mfma_f32_16x16x16_bf16 v[16:19], v[58:59], v[4:5], v[16:19]\n" \
  "s_setprio 0\n" \
  "s_waitcnt lgkmcnt(0)\n" \
  "s_nop 4\n" \
  "v_mul_f32 v12, v12, v28\n" \
  "v_mul_f32 v13, v13, v29\n" \
  "v_mul_f32 v14, v14, v30\n" \
  "v_mul_f32 v15, v15, v31\n" \
  "v_cvt_pk_bf16_f32 v12, v12, v13\n" \
  "v_cvt_pk_bf16_f32 v13, v14, v15\n" \
  "v_mul_f32 v16, v16, v32\n" \
  "v_mul_f32 v17, v17, v33\n" \
  "v_mul_f32 v18, v18, v34\n" \
  "v_mul_f32 v19, v19, v35\n" \
  "v_cvt_pk_bf16_f32 v14, v16, v17\n" \
  "v_cvt_pk_bf16_f32 v15, v18, v19\n" \
  "buffer_store_dwordx4 v[12:15], v6, s[44:47], s60 offen offset:0 sc1\n" \
  "v_mov_b64 v[16:17], 0\n" \
  "v_lshrrev_b32 v18, 1, v6\n" \
  "buffer_store_dwordx2 v[16:17], v18, s[48:51], s61 offen sc1\n" \
  "s_setprio 0\n" \
  "s_add_u32 s60, s60, 0x40000\n" \
  "s_add_u32 s61, s61, 0x10000\n" \
  "s_mov_b32 s0, 0xbe801d62\n" \
  ".set .Lpad0xab, . - 4\n" \
  "v_mov_b32 v12, s80\n" \
  "v_mov_b32 v13, 1\n" \
  "v_mfma_f32_16x16x16_bf16 v[20:23], v[0:1], v[56:57], 0\n" \
  "v_mfma_f32_16x16x16_bf16 v[24:27], v[2:3], v[56:57], 0\n" \
  "v_mfma_f32_16x16x16_bf16 v[28:31], v[0:1], v[58:59], 0\n" \
  "v_mfma_f32_16x16x16_bf16 v[32:35], v[2:3], v[58:59], 0\n" \
  "v_mfma_f32_16x16x32_bf16 v[20:23], v[64:67], v[48:51], v[20:23]\n" \
  "ds_read_b128 v[44:47], v7 offset:8192\n" \
  "v_mfma_f32_16x16x32_bf16 v[24:27], v[128:131], v[48:51], v[24:27]\n" \
  "v_mfma_f32_16x16x32_bf16 v[20:23], v[68:71], v[52:55], v[20:23]\n" \
  "ds_read_b128 v[48:51], v7 offset:10240\n" \
  "v_mfma_f32_16x16x32_bf16 v[24:27], v[132:135], v[52:55], v[24:27]\n" \
  "v_mfma_f32_16x16x32_bf16 v[20:23], v[72:75], v[36:39], v[20:23]\n" \
  "ds_read_b128 v[52:55], v7 offset:12288\n" \
  "v_mfma_f32_16x16x32_bf16 v[24:27], v[136:139], v[36:39], v[24:27]\n" \
  "v_mfma_f32_16x16x32_bf16 v[20:23], v[76:79], v[40:43], v[20:23]\n" \
  "ds_read_b128 v[36:39], v7 offset:14336\n" \
  "v_mfma_f32_16x16x32_bf16 v[24:27], v[140:143], v[40:43], v[24:27]\n" \
  "s_waitcnt lgkmcnt(3)\n" \
  "v_mfma_f32_16x16x32_bf16 v[20:23], v[80:83], v[44:47], v[20:23]\n" \
  "ds_read_b128 v[40:43], v7 offset:16384\n" \
  "v_mfma_f32_16x16x32_bf16 v[24:27], v[144:147], v[44:47], v[24:27]\n" \
  "s_waitcnt lgkmcnt(3)\n" \
  "v_mfma_f32_16x16x32_bf16 v[20:23], v[84:87], v[48:51], v[20:23]\n" \
  "ds_read_b128 v[44:47], v7 offset:18432\n" \
  "v_mfma_f32_16x16x32_bf16 v[24:27], v[148:151], v[48:51], v[24:27]\n" \
  "s_waitcnt lgkmcnt(3)\n" \
  "v_mfma_f32_16x16x32_bf16 v[20:23], v[88:91], v[52:55], v[20:23]\n" \
  "ds_read_b128 v[48:51], v7 offset:20480\n" \
  "v_mfma_f32_16x16x32_bf16 v[24:27], v[152:155], v[52:55], v[24:27]\n" \
  "s_waitcnt lgkmcnt(3)\n" \
  "v_mfma_f32_16x16x32_bf16 v[20:23], v[92:95], v[36:39], v[20:23]\n" \
  "ds_read_b128 v[52:55], v7 offset:22528\n" \
  "v_mfma_f32_16x16x32_bf16 v[24:27], v[156:159], v[36:39], v[24:27]\n" \
  "s_waitcnt lgkmcnt(3)\n" \
  "v_mfma_f32_16x16x32_bf16 v[20:23], v[96:99], v[40:43], v[20:23]\n" \
  "ds_read_b128 v[36:39], v7 offset:24576\n" \
  "v_mfma_f32_16x16x32_bf16 v[24:27], v[160:163], v[40:43], v[24:27]\n" \
  "s_waitcnt lgkmcnt(3)\n" \
  "v_mfma_f32_16x16x32_bf16 v[20:23], v[100:103], v[44:47], v[20:23]\n" \
  "ds_read_b128 v[40:43], v7 offset:26624\n" \
  "v_mfma_f32_16x16x32_bf16 v[24:27], v[164:167], v[44:47], v[24:27]\n" \
  "s_waitcnt lgkmcnt(3)\n" \
  "v_mfma_f32_16x16x32_bf16 v[20:23], v[104:107], v[48:51], v[20:23]\n" \
  "ds_read_b128 v[44:47], v7 offset:28672\n" \
  "v_mfma_f32_16x16x32_bf16 v[24:27], v[168:171], v[48:51], v[24:27]\n" \
  "s_waitcnt lgkmcnt(3)\n" \
  "v_mfma_f32_16x16x32_bf16 v[20:23], v[108:111], v[52:55], v[20:23]\n" \
  "ds_read_b128 v[48:51], v7 offset:30720\n" \
  "v_mfma_f32_16x16x32_bf16 v[24:27], v[172:175], v[52:55], v[24:27]\n" \
  "s_waitcnt lgkmcnt(3)\n" \
  "v_mfma_f32_16x16x32_bf16 v[20:23], v[112:115], v[36:39], v[20:23]\n" \
  "ds_read_b128 v[52:55], v7 offset:256\n" \
  "v_mfma_f32_16x16x32_bf16 v[24:27], v[176:179], v[36:39], v[24:27]\n" \
  "s_waitcnt lgkmcnt(3)\n" \
  "v_mfma_f32_16x16x32_bf16 v[20:23], v[116:119], v[40:43], v[20:23]\n" \
  "ds_read_b128 v[36:39], v7 offset:2304\n" \
  "v_mfma_f32_16x16x32_bf16 v[24:27], v[180:183], v[40:43], v[24:27]\n" \
  "s_waitcnt lgkmcnt(3)\n" \
  "v_mfma_f32_16x16x32_bf16 v[20:23], v[120:123], v[44:47], v[20:23]\n" \
  "ds_read_b128 v[40:43], v7 offset:4352\n" \
  "v_mfma_f32_16x16x32_bf16 v[24:27], v[184:187], v[44:47], v[24:27]\n" \
  "s_waitcnt lgkmcnt(3)\n" \
  "v_mfma_f32_16x16x32_bf16 v[20:23], v[124:127], v[48:51], v[20:23]\n" \
  "ds_read_b128 v[44:47], v7 offset:6400\n" \
  "v_mfma_f32_16x16x32_bf16 v[24:27], v[188:191], v[48:51], v[24:27]\n" \
  "s_waitcnt lgkmcnt(3)\n" \
  "v_mfma_f32_16x16x32_bf16 v[28:31], v[64:67], v[52:55], v[28:31]\n" \
  "ds_read_b128 v[48:51], v7 offset:8448\n" \
  "s_waitcnt vmcnt(0)\n" \
  "s_mov_b64 exec, 1\n" \
  "ds_add_rtn_u32 v14, v12, v13\n" \
  "s_mov_b64 exec, -1\n" \
  "v_mfma_f32_16x16x32_bf16 v[32:35], v[128:131], v[52:55], v[32:35]\n" \
  "s_waitcnt lgkmcnt(4)\n" \
  "v_mfma_f32_16x16x32_bf16 v[28:31], v[68:71], v[36:39], v[28:31]\n" \
  "ds_read_b128 v[52:55], v7 offset:10496\n" \
  "v_mfma_f32_16x16x32_bf16 v[32:35], v[132:135], v[36:39], v[32:35]\n" \
  "s_waitcnt lgkmcnt(4)\n" \
  "v_mfma_f32_16x16x32_bf16 v[28:31], v[72:75], v[40:43], v[28:31]\n" \
  "ds_read_b128 v[36:39], v7 offset:12544\n" \
  "s_waitcnt lgkmcnt(2)\n" \
  "v_mfma_f32_16x16x32_bf16 v[32:35], v[136:139], v[40:43], v[32:35]\n" \
  "v_readfirstlane_b32 s74, v14\n" \
  "s_cmp_eq_u32 s66, s83\n" \
  "s_cselect_b32 s74, 0, s74\n" \
  "s_and_b32 s74, s74, 3\n" \
  "s_cmp_eq_u32 s74, 3\n" \
  "s_cbranch_scc0 4\n" \
  "s_mov_b64 exec, 1\n" \
  "global_store_dword v16, v13, s[64:65] offset:64 sc1\n" \
  "s_mov_b64 exec, -1\n" \
  "v_mfma_f32_16x16x32_bf16 v[28:31], v[76:79], v[44:47], v[28:31]\n" \
  "ds_read_b128 v[40:43], v7 offset:14592\n" \
  "s_add_u32 s64, s64, 4096\n" \
  "s_addc_u32 s65, s65, 0\n" \
  "v_mfma_f32_16x16x32_bf16 v[32:35], v[140:143], v[44:47], v[32:35]\n" \
  "s_sub_u32 s66, s66, 1\n" \
  "v_mfma_f32_16x16x32_bf16 v[28:31], v[80:83], v[48:51], v[28:31]\n" \
  "ds_read_b128 v[44:47], v7 offset:16640\n" \
  "v_mfma_f32_16x16x32_bf16 v[32:35], v[144:147], v[48:51], v[32:35]\n" \
  "s_waitcnt lgkmcnt(3)\n" \
  "v_mfma_f32_16x16x32_bf16 v[28:31], v[84:87], v[52:55], v[28:31]\n" \
  "ds_read_b128 v[48:51], v7 offset:18688\n" \
  "v_mfma_f32_16x16x32_bf16 v[32:35], v[148:151], v[52:55], v[32:35]\n" \
  "s_waitcnt lgkmcnt(3)\n" \
  "v_mfma_f32_16x16x32_bf16 v[28:31], v[88:91], v[36:39], v[28:31]\n" \
  "ds_read_b128 v[52:55], v7 offset:20736\n" \
  "v_mfma_f32_16x16x32_bf16 v[32:35], v[152:155], v[36:39], v[32:35]\n" \
  "s_waitcnt lgkmcnt(3)\n" \
  "v_mfma_f32_16x16x32_bf16 v[28:31], v[92:95], v[40:43], v[28:31]\n" \
  "ds_read_b128 v[36:39], v7 offset:22784\n" \
  "v_mfma_f32_16x16x32_bf16 v[32:35], v[156:159], v[40:43], v[32:35]\n" \
  "s_waitcnt lgkmcnt(3)\n" \
  "v_mfma_f32_16x16x32_bf16 v[28:31], v[96:99], v[44:47], v[28:31]\n" \
  "ds_read_b128 v[40:43], v7 offset:24832\n" \
  "v_mfma_f32_16x16x32_bf16 v[32:35], v[160:163], v[44:47], v[32:35]\n" \
  "s_waitcnt lgkmcnt(3)\n" \
  "v_mfma_f32_16x16x32_bf16 v[28:31], v[100:103], v[48:51], v[28:31]\n" \
  "ds_read_b128 v[44:47], v7 offset:26880\n" \
  "v_mfma_f32_16x16x32_bf16 v[32:35], v[164:167], v[48:51], v[32:35]\n" \
  "s_waitcnt lgkmcnt(3)\n" \
  "v_mfma_f32_16x16x32_bf16 v[28:31], v[104:107], v[52:55], v[28:31]\n" \
  "ds_read_b128 v[48:51], v7 offset:28928\n" \
  "v_mfma_f32_16x16x32_bf16 v[32:35], v[168:171], v[52:55], v[32:35]\n" \
  "s_waitcnt lgkmcnt(3)\n" \
  "v_mfma_f32_16x16x32_bf16 v[28:31], v[108:111], v[36:39], v[28:31]\n" \
  "ds_read_b128 v[52:55], v7 offset:30976\n" \
  "v_mfma_f32_16x16x32_bf16 v[32:35], v[172:175], v[36:39], v[32:35]\n" \
  "s_waitcnt lgkmcnt(3)\n" \
  "v_mfma_f32_16x16x32_bf16 v[28:31], v[112:115], v[40:43], v[28:31]\n" \
  "v_mfma_f32_16x16x32_bf16 v[32:35], v[176:179], v[40:43], v[32:35]\n" \
  "v_xor_b32 v7, 0x20000, v7\n" \
  "s_waitcnt lgkmcnt(0)\n" \
  "s_barrier\n" \
  "ds_read_b128 v[36:39], v10 offset:0\n" \
  "ds_read_b128 v[40:43], v7 offset:0\n" \
  "v_mfma_f32_16x16x32_bf16 v[28:31], v[116:119], v[44:47], v[28:31]\n" \
  "v_mfma_f32_16x16x32_bf16 v[32:35], v[180:183], v[44:47], v[32:35]\n" \
  "ds_read_b128 v[44:47], v10 offset:2048\n" \
  "v_mfma_f32_16x16x32_bf16 v[28:31], v[120:123], v[48:51], v[28:31]\n" \
  "v_mfma_f32_16x16x32_bf16 v[32:35], v[184:187], v[48:51], v[32:35]\n" \
  "ds_read_b128 v[48:51], v7 offset:2048\n" \
  "v_mfma_f32_16x16x32_bf16 v[28:31], v[124:127], v[52:55], v[28:31]\n" \
  "v_mfma_f32_16x16x32_bf16 v[32:35], v[188:191], v[52:55], v[32:35]\n" \
  "s_cbranch_scc1 .LQB_LAST_%=\n" \
  "s_branch .LQB_TOP_%=\n" \
  ".LQB_LAST_%=:\n" \
  "s_nop 7\n" \
  "v_mul_f32 v20, v20, v60\n" \
  "v_mul_f32 v21, v21, v60\n" \
  "v_mul_f32 v22, v22, v60\n" \
  "v_cvt_pk_bf16_f32 v20, v20, v21\n" \
  "v_mul_f32 v23, v23, v60\n" \
  "v_mul_f32 v24, v24, v60\n" \
  "v_cvt_pk_bf16_f32 v21, v22, v23\n" \
  "v_mul_f32 v25, v25, v60\n" \
  "v_mul_f32 v26, v26, v60\n" \
  "v_cvt_pk_bf16_f32 v22, v24, v25\n" \
  "v_mul_f32 v27, v27, v60\n" \
  "v_cvt_pk_bf16_f32 v23, v26, v27\n" \
  "buffer_store_dwordx4 v[20:23], v6, s[40:43], s59 offen offset:0 sc1\n" \
  "s_add_u32 s76, s59, 4096\n" \
  "v_mul_f32 v28, v28, v61\n" \
  "v_mul_f32 v29, v29, v61\n" \
  "v_mul_f32 v30, v30, v61\n" \
  "v_cvt_pk_bf16_f32 v28, v28, v29\n" \
  "v_mul_f32 v31, v31, v61\n" \
  "v_mul_f32 v32, v32, v61\n" \
  "v_cvt_pk_bf16_f32 v29, v30, v31\n" \
  "v_mul_f32 v33, v33, v61\n" \
  "v_mul_f32 v34, v34, v61\n" \
  "v_cvt_pk_bf16_f32 v30, v32, v33\n" \
  "v_mul_f32 v35, v35, v61\n" \
  "v_cvt_pk_bf16_f32 v31, v34, v35\n" \
  "buffer_store_dwordx4 v[28:31], v6, s[40:43], s76 offen offset:0 sc1\n" \
  "s_add_u32 s59, s59, 0x80000\n" \
  "s_waitcnt vmcnt(0)\n" \
  "s_mov_b64 exec, 1\n" \
  "ds_add_rtn_u32 v14, v12, v13\n" \
  "s_mov_b64 exec, -1\n" \
  "s_waitcnt lgkmcnt(0)\n" \
  "v_readfirstlane_b32 s74, v14\n" \
  "s_cmp_eq_u32 s66, s83\n" \
  "s_cselect_b32 s74, 0, s74\n" \
  "s_and_b32 s74, s74, 3\n" \
  "s_cmp_eq_u32 s74, 3\n" \
  "s_cbranch_scc0 4\n" \
  "s_mov_b64 exec, 1\n" \
  "global_store_dword v16, v13, s[64:65] offset:64 sc1\n" \
  "s_mov_b64 exec, -1\n" \
  "s_cmp_eq_u32 s86, 0\n" \
  "s_cbranch_scc1 .LQB_END_%=\n" \
  "s_mov_b32 s86, 0\n" \
  "s_mov_b32 s66, 63\n" \
  "s_mov_b32 s62, 0x40000\n" \
  "s_add_u32 s54, s54, (128 - %[n0]) * 4096\n" \
  "s_add_u32 s59, s59, (64 - %[n0]) * 0x80000\n" \
  "s_add_u32 s60, s60, (64 - %[n0]) * 0x40000\n" \
  "s_add_u32 s61, s61, (64 - %[n0]) * 0x10000\n" \
  "s_add_u32 s64, s64, (65 - %[n0]) * 4096\n" \
  "s_addc_u32 s65, s65, 0\n" \
  "s_add_u32 s56, s56, %[x3]\n" \
  "s_addc_u32 s57, s57, 0\n" \
  "s_add_u32 s84, s84, 2048\n" \
  "s_addc_u32 s85, s85, 0\n" \
  "s_mov_b32 s83, 63\n" \
  "s_branch .LQB_GO_%=\n" \
  ".LQB_END_%=:\n" \
  "s_endpgm\n" \
  ".p2alignl 12, 0xbf800000\n" \
  ".Lpad0x8c:\n" \
  "s_setpc_b64 s[98:99]\n"
#define AW_ASM_0 \
  "v_add_u32 v17, s61, v1\n" \
  "ds_write_b128 v17, v[8:11]\n" \
  "ds_write_b128 v17, v[12:15] offset:1024\n" \
  ".p2align 6\n" \
  ".LWA_AL_%=:\n" \
  ".LWA_TOP_%=:\n" \
  "v_mov_b32 v16, s65\n" \
  "v_add_u32 v17, s61, v1\n" \
  "s_add_u32 s64, s64, 2\n" \
  "s_mov_b32 s77, 0x1000000\n" \
  "s_branch .LWA_LAND_%=\n" \
  ".LWA_WAIT_%=:\n" \
  "s_setprio 0\n" \
  "s_sleep 1\n" \
  "s_sub_u32 s77, s77, 1\n" \
  "s_cbranch_scc1 .LWA_GO_%=\n" \
  ".LWA_LAND_%=:\n" \
  "ds_read_b32 v18, v16\n" \
  "s_waitcnt lgkmcnt(0)\n" \
  "v_readfirstlane_b32 s68, v18\n" \
  "s_cmp_lt_u32 s68, s64\n" \
  "s_cbranch_scc1 .LWA_WAIT_%=\n" \
  ".LWA_GO_%=:\n" \
  "ds_read_b128 v[64:67], v17 offset:0\n" \
  "ds_read_b128 v[68:71], v17 offset:1024\n" \
  "s_setprio 3\n" \
  "ds_read_b128 v[16:19], v4 offset:0\n" \
  "ds_read_b128 v[32:35], v3 offset:8192\n" \
  "ds_read_b128 v[48:51], v3 offset:12288\n" \
  "ds_read_b128 v[20:23], v4 offset:1024\n" \
  "ds_read_b128 v[36:39], v3 offset:9216\n" \
  "ds_read_b128 v[52:55], v3 offset:13312\n" \
  "ds_read_b128 v[24:27], v4 offset:2048\n" \
  "ds_read_b128 v[40:43], v3 offset:10240\n" \
  "ds_read_b128 v[56:59], v3 offset:14336\n" \
  "ds_read_b128 v[28:31], v4 offset:3072\n" \
  "ds_read_b128 v[44:47], v3 offset:11264\n" \
  "ds_read_b128 v[60:63], v3 offset:15360\n" \
  "s_waitcnt lgkmcnt(10)\n" \
  "v_mfma_f32_16x16x32_bf16 v[64:67], v[32:35], v[16:19], v[64:67]\n" \
  "ds_read_b128 v[72:75], v3 offset:16384\n" \
  "s_waitcnt lgkmcnt(10)\n" \
  "v_mfma_f32_16x16x32_bf16 v[68:71], v[48:51], v[16:19], v[68:71]\n" \
  "ds_read_b128 v[76:79], v3 offset:17408\n" \
  "s_waitcnt lgkmcnt(9)\n" \
  "v_mfma_f32_16x16x32_bf16 v[64:67], v[36:39], v[20:23], v[64:67]\n" \
  "ds_read_b128 v[80:83], v3 offset:18432\n" \
  "s_waitcnt lgkmcnt(9)\n" \
  "v_mfma_f32_16x16x32_bf16 v[68:71], v[52:55], v[20:23], v[68:71]\n" \
  "ds_read_b128 v[84:87], v3 offset:19456\n" \
  "s_waitcnt lgkmcnt(8)\n" \
  "v_mfma_f32_16x16x32_bf16 v[64:67], v[40:43], v[24:27], v[64:67]\n" \
  "ds_read_b128 v[88:91], v3 offset:20480\n" \
  "s_waitcnt lgkmcnt(8)\n" \
  "v_mfma_f32_16x16x32_bf16 v[68:71], v[56:59], v[24:27], v[68:71]\n" \
  "ds_read_b128 v[92:95], v3 offset:21504\n" \
  "s_waitcnt lgkmcnt(7)\n" \
  "v_mfma_f32_16x16x32_bf16 v[64:67], v[44:47], v[28:31], v[64:67]\n" \
  "ds_read_b128 v[96:99], v3 offset:22528\n" \
  "s_waitcnt lgkmcnt(7)\n" \
  "v_mfma_f32_16x16x32_bf16 v[68:71], v[60:63], v[28:31], v[68:71]\n" \
  "ds_read_b128 v[100:103], v3 offset:23552\n" \
  "s_add_u32 s56, s56, 3\n" \
  "s_sub_u32 s68, s56, %[wd]\n" \
  "s_bitcmp1_b32 s68, 0\n" \
  "s_cselect_b32 s58, 64, %[wd]\n" \
  "v_maximum3_f32 v10, v64, v65, v66\n" \
  "s_lshr_b32 s70, s68, 1\n" \
  "s_add_u32 s58, s58, s70\n" \
  "v_maximum3_f32 v10, v10, v67, v68\n" \
  "v_maximum3_f32 v10, v10, v69, v70\n" \
  "v_max_f32 v8, v10, v71\n" \
  "v_mov_b32 v9, v8\n" \
  "s_cmp_lt_u32 s68, %[w2]\n" \
  "s_cselect_b32 s58, s58, s56\n" \
  "v_permlane16_swap_b32 v8, v9\n" \
  "v_max_f32 v8, v8, v9\n" \
  "v_mov_b32 v9, v8\n" \
  "s_min_u32 s58, s58, 128\n" \
  "s_nop 0\n" \
  "v_permlane32_swap_b32 v8, v9\n" \
  "v_max_f32 v8, v8, v9\n" \
  "v_mul_f32 v10, s80, v8\n" \
  "v_pk_fma_f32 v[64:65], v[64:65], s[80:81], v[10:11] op_sel_hi:[1,0,0] neg_lo:[0,0,1] neg_hi:[0,0,1]\n" \
  "v_pk_fma_f32 v[66:67], v[66:67], s[80:81], v[10:11] op_sel_hi:[1,0,0] neg_lo:[0,0,1] neg_hi:[0,0,1]\n" \
  "v_pk_fma_f32 v[68:69], v[68:69], s[80:81], v[10:11] op_sel_hi:[1,0,0] neg_lo:[0,0,1] neg_hi:[0,0,1]\n" \
  "v_pk_fma_f32 v[70:71], v[70:71], s[80:81], v[10:11] op_sel_hi:[1,0,0] neg_lo:[0,0,1] neg_hi:[0,0,1]\n" \
  "v_exp_f32 v64, v64\n" \
  "v_exp_f32 v65, v65\n" \
  "v_exp_f32 v66, v66\n" \
  "v_exp_f32 v67, v67\n" \
  "v_exp_f32 v68, v68\n" \
  "v_exp_f32 v69, v69\n" \
  "v_exp_f32 v70, v70\n" \
  "v_exp_f32 v71, v71\n" \
  "v_cvt_pk_bf16_f32 v104, v64, v65\n" \
  "v_cvt_pk_bf16_f32 v105, v66, v67\n" \
  "v_cvt_pk_bf16_f32 v106, v68, v69\n" \
  "v_cvt_pk_bf16_f32 v107, v70, v71\n" \
  "v_pk_add_f32 v[64:65], v[64:65], v[66:67]\n" \
  "v_pk_add_f32 v[68:69], v[68:69], v[70:71]\n" \
  "s_waitcnt lgkmcnt(0)\n" \
  "v_pk_add_f32 v[64:65], v[64:65], v[68:69]\n" \
  "v_mfma_f32_16x16x32_bf16 v[32:35], v[72:75], v[104:107], 0\n" \
  "v_mfma_f32_16x16x32_bf16 v[48:51], v[88:91], v[104:107], 0\n" \
  "v_mfma_f32_16x16x32_bf16 v[36:39], v[76:79], v[104:107], 0\n" \
  "v_mfma_f32_16x16x32_bf16 v[52:55], v[92:95], v[104:107], 0\n" \
  "v_mfma_f32_16x16x32_bf16 v[40:43], v[80:83], v[104:107], 0\n" \
  "v_mfma_f32_16x16x32_bf16 v[56:59], v[96:99], v[104:107], 0\n" \
  "v_mfma_f32_16x16x32_bf16 v[44:47], v[84:87], v[104:107], 0\n" \
  "v_mfma_f32_16x16x32_bf16 v[60:63], v[100:103], v[104:107], 0\n" \
  "v_add_f32 v11, v64, v65\n" \
  "s_cmp_lt_u32 s56, %[w3]\n" \
  "s_cselect_b32 s68, s57, 0\n" \
  "s_cmp_ge_u32 s68, 64\n" \
  "s_cbranch_scc1 .LWA_EASY_%=\n" \
  "v_cvt_pk_bf16_f32 v108, v32, v33\n" \
  "v_cvt_pk_bf16_f32 v109, v48, v49\n" \
  "v_cvt_pk_bf16_f32 v110, v34, v35\n" \
  "v_cvt_pk_bf16_f32 v111, v50, v51\n" \
  "ds_write_b128 v7, v[108:111] offset:0\n" \
  "v_cvt_pk_bf16_f32 v112, v36, v37\n" \
  "v_cvt_pk_bf16_f32 v113, v52, v53\n" \
  "v_cvt_pk_bf16_f32 v114, v38, v39\n" \
  "v_cvt_pk_bf16_f32 v115, v54, v55\n" \
  "ds_write_b128 v7, v[112:115] offset:1024\n" \
  "v_cvt_pk_bf16_f32 v116, v40, v41\n" \
  "v_cvt_pk_bf16_f32 v117, v56, v57\n" \
  "v_cvt_pk_bf16_f32 v118, v42, v43\n" \
  "v_cvt_pk_bf16_f32 v119, v58, v59\n" \
  "ds_write_b128 v7, v[116:119] offset:2048\n" \
  "v_cvt_pk_bf16_f32 v120, v44, v45\n" \
  "v_cvt_pk_bf16_f32 v121, v60, v61\n" \
  "v_cvt_pk_bf16_f32 v122, v46, v47\n" \
  "v_cvt_pk_bf16_f32 v123, v62, v63\n" \
  "ds_write_b128 v7, v[120:123] offset:3072\n" \
  "s_waitcnt vmcnt(0)\n" \
  "s_waitcnt lgkmcnt(0)\n" \
  "s_barrier\n" \
  "s_setprio 2\n" \
  "ds_read_b128 v[48:51], v1 offset:0\n" \
  "ds_read_b128 v[52:55], v1 offset:1024\n" \
  "ds_read_b128 v[56:59], v1 offset:2048\n" \
  "s_waitcnt lgkmcnt(2)\n" \
  "v_mfma_f32_16x16x32_bf16 v[64:67], v[128:131], v[48:51], 0\n" \
  "ds_read_b128 v[60:63], v1 offset:3072\n" \
  "s_lshl_b32 s75, s57, 19\n" \
  "v_mfma_f32_16x16x32_bf16 v[68:71], v[160:163], v[48:51], 0\n" \
  "s_add_u32 s75, s75, s60\n" \
  "v_mfma_f32_16x16x32_bf16 v[72:75], v[192:195], v[48:51], 0\n" \
  "s_add_u32 s76, s75, 16384\n" \
  "v_mfma_f32_16x16x32_bf16 v[76:79], v[224:227], v[48:51], 0\n" \
  "s_waitcnt lgkmcnt(2)\n" \
  "v_mfma_f32_16x16x32_bf16 v[64:67], v[132:135], v[52:55], v[64:67]\n" \
  "ds_read_b128 v[48:51], v1 offset:8192\n" \
  "v_mfma_f32_16x16x32_bf16 v[68:71], v[164:167], v[52:55], v[68:71]\n" \
  "v_mfma_f32_16x16x32_bf16 v[72:75], v[196:199], v[52:55], v[72:75]\n" \
  "v_mfma_f32_16x16x32_bf16 v[76:79], v[228:231], v[52:55], v[76:79]\n" \
  "s_waitcnt lgkmcnt(2)\n" \
  "v_mfma_f32_16x16x32_bf16 v[64:67], v[136:139], v[56:59], v[64:67]\n" \
  "ds_read_b128 v[52:55], v1 offset:9216\n" \
  "v_mfma_f32_16x16x32_bf16 v[68:71], v[168:171], v[56:59], v[68:71]\n" \
  "v_mfma_f32_16x16x32_bf16 v[72:75], v[200:203], v[56:59], v[72:75]\n" \
  "v_mfma_f32_16x16x32_bf16 v[76:79], v[232:235], v[56:59], v[76:79]\n" \
  "s_waitcnt lgkmcnt(2)\n" \
  "v_mfma_f32_16x16x32_bf16 v[64:67], v[140:143], v[60:63], v[64:67]\n" \
  "ds_read_b128 v[56:59], v1 offset:10240\n" \
  "v_mfma_f32_16x16x32_bf16 v[68:71], v[172:175], v[60:63], v[68:71]\n" \
  "v_mfma_f32_16x16x32_bf16 v[72:75], v[204:207], v[60:63], v[72:75]\n" \
  "v_mfma_f32_16x16x32_bf16 v[76:79], v[236:239], v[60:63], v[76:79]\n" \
  "s_waitcnt lgkmcnt(2)\n" \
  "v_mfma_f32_16x16x32_bf16 v[96:99], v[144:147], v[48:51], 0\n" \
  "ds_read_b128 v[60:63], v1 offset:11264\n" \
  "v_mfma_f32_16x16x32_bf16 v[100:103], v[176:179], v[48:51], 0\n" \
  "v_mfma_f32_16x16x32_bf16 v[104:107], v[208:211], v[48:51], 0\n" \
  "v_mfma_f32_16x16x32_bf16 v[108:111], v[240:243], v[48:51], 0\n" \
  "s_waitcnt lgkmcnt(2)\n" \
  "v_mfma_f32_16x16x32_bf16 v[96:99], v[148:151], v[52:55], v[96:99]\n" \
  "ds_read_b128 v[48:51], v1 offset:4096\n" \
  "v_mfma_f32_16x16x32_bf16 v[100:103], v[180:183], v[52:55], v[100:103]\n" \
  "v_mfma_f32_16x16x32_bf16 v[104:107], v[212:215], v[52:55], v[104:107]\n" \
  "v_mfma_f32_16x16x32_bf16 v[108:111], v[244:247], v[52:55], v[108:111]\n" \
  "s_waitcnt lgkmcnt(2)\n" \
  "v_mfma_f32_16x16x32_bf16 v[96:99], v[152:155], v[56:59], v[96:99]\n" \
  "ds_read_b128 v[52:55], v1 offset:5120\n" \
  "v_mfma_f32_16x16x32_bf16 v[100:103], v[184:187], v[56:59], v[100:103]\n" \
  "v_mfma_f32_16x16x32_bf16 v[104:107], v[216:219], v[56:59], v[104:107]\n" \
  "v_mfma_f32_16x16x32_bf16 v[108:111], v[248:251], v[56:59], v[108:111]\n" \
  "s_waitcnt lgkmcnt(2)\n" \
  "v_mfma_f32_16x16x32_bf16 v[96:99], v[156:159], v[60:63], v[96:99]\n" \
  "ds_read_b128 v[56:59], v1 offset:6144\n" \
  "v_mfma_f32_16x16x32_bf16 v[100:103], v[188:191], v[60:63], v[100:103]\n" \
  "v_mfma_f32_16x16x32_bf16 v[104:107], v[220:223], v[60:63], v[104:107]\n" \
  "v_mfma_f32_16x16x32_bf16 v[108:111], v[252:255], v[60:63], v[108:111]\n" \
  "s_waitcnt lgkmcnt(2)\n" \
  "v_mfma_f32_16x16x32_bf16 v[80:83], v[128:131], v[48:51], 0\n" \
  "ds_read_b128 v[60:63], v1 offset:7168\n" \
  "v_mfma_f32_16x16x32_bf16 v[84:87], v[160:163], v[48:51], 0\n" \
  "v_mfma_f32_16x16x32_bf16 v[88:91], v[192:195], v[48:51], 0\n" \
  "v_mfma_f32_16x16x32_bf16 v[92:95], v[224:227], v[48:51], 0\n" \
  "s_waitcnt lgkmcnt(2)\n" \
  "v_mfma_f32_16x16x32_bf16 v[80:83], v[132:135], v[52:55], v[80:83]\n" \
  "ds_read_b128 v[48:51], v1 offset:12288\n" \
  "v_mfma_f32_16x16x32_bf16 v[84:87], v[164:167], v[52:55], v[84:87]\n" \
  "v_mfma_f32_16x16x32_bf16 v[88:91], v[196:199], v[52:55], v[88:91]\n" \
  "v_mfma_f32_16x16x32_bf16 v[92:95], v[228:231], v[52:55], v[92:95]\n" \
  "s_waitcnt lgkmcnt(2)\n" \
  "v_mfma_f32_16x16x32_bf16 v[80:83], v[136:139], v[56:59], v[80:83]\n" \
  "ds_read_b128 v[52:55], v1 offset:13312\n" \
  "v_mfma_f32_16x16x32_bf16 v[84:87], v[168:171], v[56:59], v[84:87]\n" \
  "v_mfma_f32_16x16x32_bf16 v[88:91], v[200:203], v[56:59], v[88:91]\n" \
  "v_mfma_f32_16x16x32_bf16 v[92:95], v[232:235], v[56:59], v[92:95]\n" \
  "s_waitcnt lgkmcnt(2)\n" \
  "v_mfma_f32_16x16x32_bf16 v[80:83], v[140:143], v[60:63], v[80:83]\n" \
  "ds_read_b128 v[56:59], v1 offset:14336\n" \
  "v_mfma_f32_16x16x32_bf16 v[84:87], v[172:175], v[60:63], v[84:87]\n" \
  "v_mfma_f32_16x16x32_bf16 v[88:91], v[204:207], v[60:63], v[88:91]\n" \
  "v_mfma_f32_16x16x32_bf16 v[92:95], v[236:239], v[60:63], v[92:95]\n" \
  "s_waitcnt lgkmcnt(2)\n" \
  "v_mfma_f32_16x16x32_bf16 v[112:115], v[144:147], v[48:51], 0\n" \
  "ds_read_b128 v[60:63], v1 offset:15360\n" \
  "v_mfma_f32_16x16x32_bf16 v[116:119], v[176:179], v[48:51], 0\n" \
  "v_mfma_f32_16x16x32_bf16 v[120:123], v[208:211], v[48:51], 0\n" \
  "v_mfma_f32_16x16x32_bf16 v[124:127], v[240:243], v[48:51], 0\n" \
  "s_waitcnt lgkmcnt(2)\n" \
  "v_mfma_f32_16x16x32_bf16 v[112:115], v[148:151], v[52:55], v[112:115]\n" \
  "v_mfma_f32_16x16x32_bf16 v[116:119], v[180:183], v[52:55], v[116:119]\n" \
  "v_mfma_f32_16x16x32_bf16 v[120:123], v[212:215], v[52:55], v[120:123]\n" \
  "v_mfma_f32_16x16x32_bf16 v[124:127], v[244:247], v[52:55], v[124:127]\n" \
  "s_waitcnt lgkmcnt(1)\n" \
  "v_mfma_f32_16x16x32_bf16 v[112:115], v[152:155], v[56:59], v[112:115]\n" \
  "v_mfma_f32_16x16x32_bf16 v[116:119], v[184:187], v[56:59], v[116:119]\n" \
  "v_mfma_f32_16x16x32_bf16 v[120:123], v[216:219], v[56:59], v[120:123]\n" \
  "v_mfma_f32_16x16x32_bf16 v[124:127], v[248:251], v[56:59], v[124:127]\n" \
  "s_waitcnt lgkmcnt(0)\n" \
  "v_mfma_f32_16x16x32_bf16 v[112:115], v[156:159], v[60:63], v[112:115]\n" \
  "v_mfma_f32_16x16x32_bf16 v[116:119], v[188:191], v[60:63], v[116:119]\n" \
  "v_mfma_f32_16x16x32_bf16 v[120:123], v[220:223], v[60:63], v[120:123]\n" \
  "v_mfma_f32_16x16x32_bf16 v[124:127], v[252:255], v[60:63], v[124:127]\n" \
  "s_setprio 3\n" \
  "s_barrier\n" \
  "ds_read_b128 v[32:35], v5 offset:0\n" \
  "ds_read_b128 v[36:39], v5 offset:512\n" \
  "ds_read_b128 v[40:43], v5 offset:1024\n" \
  "ds_read_b128 v[44:47], v5 offset:1536\n" \
  "ds_read_b64_tr_b16 v[48:49], v6 offset:0\n" \
  "ds_read_b64_tr_b16 v[50:51], v6 offset:32\n" \
  "ds_read_b64_tr_b16 v[52:53], v6 offset:1024\n" \
  "ds_read_b64_tr_b16 v[54:55], v6 offset:1056\n" \
  "ds_read_b64_tr_b16 v[56:57], v6 offset:4096\n" \
  "ds_read_b64_tr_b16 v[58:59], v6 offset:4128\n" \
  "ds_read_b64_tr_b16 v[60:61], v6 offset:5120\n" \
  "ds_read_b64_tr_b16 v[62:63], v6 offset:5152\n" \
  "s_waitcnt lgkmcnt(11)\n" \
  "v_mfma_f32_16x16x32_bf16 v[12:15], v[32:35], v[16:19], 0\n" \
  "s_waitcnt lgkmcnt(10)\n" \
  "v_mfma_f32_16x16x32_bf16 v[12:15], v[36:39], v[20:23], v[12:15]\n" \
  "s_waitcnt lgkmcnt(9)\n" \
  "v_mfma_f32_16x16x32_bf16 v[12:15], v[40:43], v[24:27], v[12:15]\n" \
  "s_waitcnt lgkmcnt(8)\n" \
  "v_mfma_f32_16x16x32_bf16 v[12:15], v[44:47], v[28:31], v[12:15]\n" \
  "v_mov_b64 v[16:17], 0\n" \
  "v_mov_b64 v[18:19], 0\n" \
  "v_mov_b64 v[20:21], 0\n" \
  "v_mov_b64 v[22:23], 0\n" \
  "v_mov_b64 v[24:25], 0\n" \
  "v_mov_b64 v[26:27], 0\n" \
  "v_mov_b64 v[28:29], 0\n" \
  "v_mov_b64 v[30:31], 0\n" \
  "v_maximum3_f32 v8, v8, v12, v13\n" \
  "v_mov_b32 v9, v8\n" \
  "v_mov_b64 v[32:33], 0\n" \
  "v_mov_b64 v[34:35], 0\n" \
  "v_permlane16_swap_b32 v8, v9\n" \
  "v_max_f32 v8, v8, v9\n" \
  "v_mov_b32 v9, v8\n" \
  "v_mov_b64 v[36:37], 0\n" \
  "v_mov_b64 v[38:39], 0\n" \
  "v_permlane32_swap_b32 v8, v9\n" \
  "v_max_f32 v8, v8, v9\n" \
  "v_mul_f32 v8, s80, v8\n" \
  "v_sub_f32 v14, v10, v8\n" \
  "v_fma_f32 v12, v12, s80, -v8\n" \
  "v_fma_f32 v13, v13, s80, -v8\n" \
  "v_exp_f32 v14, v14\n" \
  "v_exp_f32 v12, v12\n" \
  "v_exp_f32 v13, v13\n" \
  "v_fma_f32 v11, v11, v14, v12\n" \
  "v_add_f32 v11, v11, v13\n" \
  "v_mov_b32 v9, v11\n" \
  "v_mov_b64 v[40:41], 0\n" \
  "v_mov_b64 v[42:43], 0\n" \
  "v_permlane16_swap_b32 v11, v9\n" \
  "v_add_f32 v11, v11, v9\n" \
  "v_mov_b32 v9, v11\n" \
  "v_mov_b64 v[44:45], 0\n" \
  "v_mov_b64 v[46:47], 0\n" \
  "v_permlane32_swap_b32 v11, v9\n" \
  "v_add_f32 v11, v11, v9\n" \
  "v_rcp_f32 v15, v11\n" \
  "v_add_u32 v9, s66, v1\n" \
  "v_mul_f32 v12, v12, v15\n" \
  "v_mul_f32 v13, v13, v15\n" \
  "v_mul_f32 v14, v14, v15\n" \
  "v_cvt_pk_bf16_f32 v12, v12, v13\n" \
  "ds_write2_b32 v9, v12, v14 offset1:2\n" \
  "s_waitcnt lgkmcnt(0)\n" \
  "s_barrier\n" \
  "ds_read_b128 v[8:11], v1 offset:24576\n" \
  "ds_read_b128 v[12:15], v1 offset:25600\n" \
  "s_setprio 0\n" \
  "v_smfmac_f32_16x16x64_bf16 v[16:19], v[48:51], v[128:135], v0\n" \
  "v_smfmac_f32_16x16x64_bf16 v[20:23], v[48:51], v[160:167], v0\n" \
  "v_smfmac_f32_16x16x64_bf16 v[24:27], v[48:51], v[192:199], v0\n" \
  "v_smfmac_f32_16x16x64_bf16 v[28:31], v[48:51], v[224:231], v0\n" \
  "v_smfmac_f32_16x16x64_bf16 v[16:19], v[52:55], v[136:143], v0\n" \
  "v_smfmac_f32_16x16x64_bf16 v[20:23], v[52:55], v[168:175], v0\n" \
  "v_smfmac_f32_16x16x64_bf16 v[24:27], v[52:55], v[200:207], v0\n" \
  "v_smfmac_f32_16x16x64_bf16 v[28:31], v[52:55], v[232:239], v0\n" \
  "v_smfmac_f32_16x16x64_bf16 v[32:35], v[56:59], v[144:151], v0\n" \
  "s_waitcnt lgkmcnt(0)\n" \
  "v_pk_mul_f32 v[64:65], v[64:65], v[10:11] op_sel_hi:[1,0]\n" \
  "v_pk_mul_f32 v[66:67], v[66:67], v[10:11] op_sel_hi:[1,0]\n" \
  "v_smfmac_f32_16x16x64_bf16 v[36:39], v[56:59], v[176:183], v0\n" \
  "v_pk_mul_f32 v[68:69], v[68:69], v[10:11] op_sel_hi:[1,0]\n" \
  "v_pk_mul_f32 v[70:71], v[70:71], v[10:11] op_sel_hi:[1,0]\n" \
  "v_pk_fma_f32 v[64:65], v[96:97], v[10:11], v[64:65] op_sel:[0,1,0]\n" \
  "v_smfmac_f32_16x16x64_bf16 v[40:43], v[56:59], v[208:215], v0\n" \
  "v_pk_fma_f32 v[66:67], v[98:99], v[10:11], v[66:67] op_sel:[0,1,0]\n" \
  "v_pk_fma_f32 v[68:69], v[100:101], v[10:11], v[68:69] op_sel:[0,1,0]\n" \
  "v_pk_fma_f32 v[70:71], v[102:103], v[10:11], v[70:71] op_sel:[0,1,0]\n" \
  "v_smfmac_f32_16x16x64_bf16 v[44:47], v[56:59], v[240:247], v0\n" \
  "v_pk_mul_f32 v[72:73], v[72:73], v[10:11] op_sel_hi:[1,0]\n" \
  "v_pk_mul_f32 v[74:75], v[74:75], v[10:11] op_sel_hi:[1,0]\n" \
  "v_pk_mul_f32 v[76:77], v[76:77], v[10:11] op_sel_hi:[1,0]\n" \
  "v_smfmac_f32_16x16x64_bf16 v[32:35], v[60:63], v[152:159], v0\n" \
  "v_pk_mul_f32 v[78:79], v[78:79], v[10:11] op_sel_hi:[1,0]\n" \
  "v_pk_fma_f32 v[72:73], v[104:105], v[10:11], v[72:73] op_sel:[0,1,0]\n" \
  "v_pk_fma_f32 v[74:75], v[106:107], v[10:11], v[74:75] op_sel:[0,1,0]\n" \
  "v_smfmac_f32_16x16x64_bf16 v[36:39], v[60:63], v[184:191], v0\n" \
  "v_pk_fma_f32 v[76:77], v[108:109], v[10:11], v[76:77] op_sel:[0,1,0]\n" \
  "v_pk_fma_f32 v[78:79], v[110:111], v[10:11], v[78:79] op_sel:[0,1,0]\n" \
  "v_pk_mul_f32 v[80:81], v[80:81], v[14:15] op_sel_hi:[1,0]\n" \
  "v_smfmac_f32_16x16x64_bf16 v[40:43], v[60:63], v[216:223], v0\n" \
  "v_pk_mul_f32 v[82:83], v[82:83], v[14:15] op_sel_hi:[1,0]\n" \
  "v_pk_mul_f32 v[84:85], v[84:85], v[14:15] op_sel_hi:[1,0]\n" \
  "v_pk_mul_f32 v[86:87], v[86:87], v[14:15] op_sel_hi:[1,0]\n" \
  "v_smfmac_f32_16x16x64_bf16 v[44:47], v[60:63], v[248:255], v0\n" \
  "v_pk_fma_f32 v[80:81], v[112:113], v[14:15], v[80:81] op_sel:[0,1,0]\n" \
  "v_pk_fma_f32 v[82:83], v[114:115], v[14:15], v[82:83] op_sel:[0,1,0]\n" \
  "v_pk_fma_f32 v[84:85], v[116:117], v[14:15], v[84:85] op_sel:[0,1,0]\n" \
  "v_pk_fma_f32 v[86:87], v[118:119], v[14:15], v[86:87] op_sel:[0,1,0]\n" \
  "v_pk_mul_f32 v[88:89], v[88:89], v[14:15] op_sel_hi:[1,0]\n" \
  "v_pk_mul_f32 v[90:91], v[90:91], v[14:15] op_sel_hi:[1,0]\n" \
  "v_pk_mul_f32 v[92:93], v[92:93], v[14:15] op_sel_hi:[1,0]\n" \
  "v_pk_mul_f32 v[94:95], v[94:95], v[14:15] op_sel_hi:[1,0]\n" \
  "v_pk_fma_f32 v[88:89], v[120:121], v[14:15], v[88:89] op_sel:[0,1,0]\n" \
  "v_pk_fma_f32 v[90:91], v[122:123], v[14:15], v[90:91] op_sel:[0,1,0]\n" \
  "v_pk_fma_f32 v[92:93], v[124:125], v[14:15], v[92:93] op_sel:[0,1,0]\n" \
  "v_pk_fma_f32 v[94:95], v[126:127], v[14:15], v[94:95] op_sel:[0,1,0]\n" \
  "v_pk_add_f32 v[16:17], v[16:17], v[18:19]\n" \
  "v_pk_add_f32 v[32:33], v[32:33], v[34:35]\n" \
  "v_cvt_pk_bf16_f32 v16, v16, v17\n" \
  "v_cvt_pk_bf16_f32 v17, v32, v33\n" \
  "v_pk_add_f32 v[20:21], v[20:21], v[22:23]\n" \
  "v_pk_add_f32 v[36:37], v[36:37], v[38:39]\n" \
  "v_cvt_pk_bf16_f32 v20, v20, v21\n" \
  "v_cvt_pk_bf16_f32 v21, v36, v37\n" \
  "v_pk_add_f32 v[24:25], v[24:25], v[26:27]\n" \
  "v_pk_add_f32 v[40:41], v[40:41], v[42:43]\n" \
  "v_mfma_f32_16x16x16_bf16 v[64:67], v[16:17], v[8:9], v[64:67]\n" \
  "v_mfma_f32_16x16x16_bf16 v[68:71], v[20:21], v[8:9], v[68:71]\n" \
  "v_cvt_pk_bf16_f32 v24, v24, v25\n" \
  "v_cvt_pk_bf16_f32 v25, v40, v41\n" \
  "v_mfma_f32_16x16x16_bf16 v[80:83], v[16:17], v[12:13], v[80:83]\n" \
  "v_mfma_f32_16x16x16_bf16 v[84:87], v[20:21], v[12:13], v[84:87]\n" \
  "v_pk_add_f32 v[28:29], v[28:29], v[30:31]\n" \
  "v_pk_add_f32 v[44:45], v[44:45], v[46:47]\n" \
  "v_cvt_pk_bf16_f32 v28, v28, v29\n" \
  "v_cvt_pk_bf16_f32 v29, v44, v45\n" \
  "s_nop 1\n" \
  "v_mfma_f32_16x16x16_bf16 v[72:75], v[24:25], v[8:9], v[72:75]\n" \
  "v_mfma_f32_16x16x16_bf16 v[76:79], v[28:29], v[8:9], v[76:79]\n" \
  "v_mfma_f32_16x16x16_bf16 v[88:91], v[24:25], v[12:13], v[88:91]\n" \
  "v_mfma_f32_16x16x16_bf16 v[92:95], v[28:29], v[12:13], v[92:95]\n" \
  "v_cvt_pk_bf16_f32 v64, v64, v65\n" \
  "v_cvt_pk_bf16_f32 v65, v66, v67\n" \
  "v_cvt_pk_bf16_f32 v66, v68, v69\n" \
  "v_cvt_pk_bf16_f32 v67, v70, v71\n" \
  "buffer_store_dwordx4 v[64:67], v2, s[40:43], s75 offen offset:0 sc1\n" \
  "v_cvt_pk_bf16_f32 v80, v80, v81\n" \
  "v_cvt_pk_bf16_f32 v81, v82, v83\n" \
  "v_cvt_pk_bf16_f32 v82, v84, v85\n" \
  "v_cvt_pk_bf16_f32 v83, v86, v87\n" \
  "buffer_store_dwordx4 v[80:83], v2, s[40:43], s76 offen offset:0 sc1\n" \
  "v_cvt_pk_bf16_f32 v72, v72, v73\n" \
  "v_cvt_pk_bf16_f32 v73, v74, v75\n" \
  "v_cvt_pk_bf16_f32 v74, v76, v77\n" \
  "v_cvt_pk_bf16_f32 v75, v78, v79\n" \
  "buffer_store_dwordx4 v[72:75], v2, s[40:43], s75 offen offset:128 sc1\n" \
  "v_cvt_pk_bf16_f32 v88, v88, v89\n" \
  "v_cvt_pk_bf16_f32 v89, v90, v91\n" \
  "v_cvt_pk_bf16_f32 v90, v92, v93\n" \
  "v_cvt_pk_bf16_f32 v91, v94, v95\n" \
  "buffer_store_dwordx4 v[88:91], v2, s[40:43], s76 offen offset:128 sc1\n" \
  ".LWA_NEXT_%=:\n" \
  "v_xor_b32 v3, 0x10000, v3\n" \
  "v_xor_b32 v4, 0x10000, v4\n" \
  "s_mov_b32 s57, s58\n" \
  "s_cmp_eq_u32 s57, 128\n" \
  "s_cbranch_scc1 .LWA_END_%=\n" \
  "s_branch .LWA_TOP_%=\n" \
  ".LWA_END_%=:\n" \
  "s_endpgm\n" \
  ".LWA_EASY_%=:\n" \
  "v_lshrrev_b32 v9, 1, v1\n" \
  "v_mov_b32 v105, 0\n" \
  "v_add_u32 v64, s63, v9\n" \
  "v_mov_b32 v106, 0\n" \
  "s_barrier\n" \
  "s_setprio 3\n" \
  "ds_read_b128 v[72:75], v5 offset:0\n" \
  "ds_read_b128 v[76:79], v5 offset:512\n" \
  "ds_read_b128 v[80:83], v5 offset:1024\n" \
  "ds_read_b128 v[84:87], v5 offset:1536\n" \
  "ds_read_b64 v[88:89], v64 offset:0\n" \
  "ds_read_b64 v[90:91], v64 offset:512\n" \
  "ds_read_b64 v[92:93], v64 offset:1024\n" \
  "ds_read_b64 v[94:95], v64 offset:1536\n" \
  "s_waitcnt lgkmcnt(7)\n" \
  "v_mfma_f32_16x16x32_bf16 v[64:67], v[72:75], v[16:19], 0\n" \
  "s_waitcnt lgkmcnt(6)\n" \
  "v_mfma_f32_16x16x32_bf16 v[64:67], v[76:79], v[20:23], v[64:67]\n" \
  "s_waitcnt lgkmcnt(5)\n" \
  "v_mfma_f32_16x16x32_bf16 v[64:67], v[80:83], v[24:27], v[64:67]\n" \
  "s_waitcnt lgkmcnt(4)\n" \
  "v_mfma_f32_16x16x32_bf16 v[64:67], v[84:87], v[28:31], v[64:67]\n" \
  "s_nop 7\n" \
  "v_maximum3_f32 v8, v8, v64, v65\n" \
  "v_mov_b32 v9, v8\n" \
  "s_nop 1\n" \
  "v_permlane16_swap_b32 v8, v9\n" \
  "v_max_f32 v8, v8, v9\n" \
  "v_mov_b32 v9, v8\n" \
  "s_nop 1\n" \
  "v_permlane32_swap_b32 v8, v9\n" \
  "v_max_f32 v8, v8, v9\n" \
  "v_mul_f32 v8, s80, v8\n" \
  "v_sub_f32 v66, v10, v8\n" \
  "v_fma_f32 v64, v64, s80, -v8\n" \
  "v_fma_f32 v65, v65, s80, -v8\n" \
  "v_exp_f32 v66, v66\n" \
  "v_exp_f32 v64, v64\n" \
  "v_exp_f32 v65, v65\n" \
  "v_fma_f32 v11, v11, v66, v64\n" \
  "v_add_f32 v11, v11, v65\n" \
  "v_mov_b32 v9, v11\n" \
  "s_nop 1\n" \
  "v_permlane16_swap_b32 v11, v9\n" \
  "v_add_f32 v11, v11, v9\n" \
  "v_mov_b32 v9, v11\n" \
  "s_nop 1\n" \
  "v_permlane32_swap_b32 v11, v9\n" \
  "v_add_f32 v11, v11, v9\n" \
  "v_rcp_f32 v68, v11\n" \
  "s_waitcnt lgkmcnt(0)\n" \
  "v_mul_f32 v66, v66, v68\n" \
  "v_mul_f32 v64, v64, v68\n" \
  "v_mul_f32 v65, v65, v68\n" \
  "v_pk_mul_f32 v[32:33], v[32:33], v[66:67] op_sel_hi:[1,0]\n" \
  "v_cvt_pk_bf16_f32 v104, v64, v65\n" \
  "v_pk_mul_f32 v[34:35], v[34:35], v[66:67] op_sel_hi:[1,0]\n" \
  "v_cvt_pk_bf16_f32 v107, v64, v65\n" \
  "v_mul_f32 v48, v48, v66\n" \
  "v_mul_f32 v49, v49, v66\n" \
  "v_mul_f32 v50, v50, v66\n" \
  "v_mul_f32 v51, v51, v66\n" \
  "v_mfma_f32_16x16x16_bf16 v[32:35], v[88:89], v[104:105], v[32:35]\n" \
  "v_mul_f32 v36, v36, v66\n" \
  "v_mul_f32 v37, v37, v66\n" \
  "v_mul_f32 v38, v38, v66\n" \
  "v_mul_f32 v39, v39, v66\n" \
  "v_mfma_f32_16x16x16_bf16 v[48:51], v[88:89], v[106:107], v[48:51]\n" \
  "v_mul_f32 v52, v52, v66\n" \
  "v_mul_f32 v53, v53, v66\n" \
  "v_mul_f32 v54, v54, v66\n" \
  "v_mul_f32 v55, v55, v66\n" \
  "v_mfma_f32_16x16x16_bf16 v[36:39], v[90:91], v[104:105], v[36:39]\n" \
  "v_mul_f32 v40, v40, v66\n" \
  "v_mul_f32 v41, v41, v66\n" \
  "v_mul_f32 v42, v42, v66\n" \
  "v_mul_f32 v43, v43, v66\n" \
  "v_mfma_f32_16x16x16_bf16 v[52:55], v[90:91], v[106:107], v[52:55]\n" \
  "v_mul_f32 v56, v56, v66\n" \
  "v_mul_f32 v57, v57, v66\n" \
  "v_mul_f32 v58, v58, v66\n" \
  "v_mul_f32 v59, v59, v66\n" \
  "v_mfma_f32_16x16x16_bf16 v[40:43], v[92:93], v[104:105], v[40:43]\n" \
  "v_mul_f32 v44, v44, v66\n" \
  "v_mul_f32 v45, v45, v66\n" \
  "v_mul_f32 v46, v46, v66\n" \
  "v_mul_f32 v47, v47, v66\n" \
  "v_mfma_f32_16x16x16_bf16 v[56:59], v[92:93], v[106:107], v[56:59]\n" \
  "v_mul_f32 v60, v60, v66\n" \
  "v_mul_f32 v61, v61, v66\n" \
  "v_mul_f32 v62, v62, v66\n" \
  "v_mul_f32 v63, v63, v66\n" \
  "v_mfma_f32_16x16x16_bf16 v[44:47], v[94:95], v[104:105], v[44:47]\n" \
  "v_cvt_pk_bf16_f32 v108, v32, v33\n" \
  "v_cvt_pk_bf16_f32 v109, v48, v49\n" \
  "v_cvt_pk_bf16_f32 v110, v34, v35\n" \
  "v_cvt_pk_bf16_f32 v111, v50, v51\n" \
  "ds_write_b128 v7, v[108:111] offset:32768\n" \
  "v_mfma_f32_16x16x16_bf16 v[60:63], v[94:95], v[106:107], v[60:63]\n" \
  "v_cvt_pk_bf16_f32 v112, v36, v37\n" \
  "v_cvt_pk_bf16_f32 v113, v52, v53\n" \
  "v_cvt_pk_bf16_f32 v114, v38, v39\n" \
  "v_cvt_pk_bf16_f32 v115, v54, v55\n" \
  "ds_write_b128 v7, v[112:115] offset:33792\n" \
  "v_cvt_pk_bf16_f32 v116, v40, v41\n" \
  "v_cvt_pk_bf16_f32 v117, v56, v57\n" \
  "v_cvt_pk_bf16_f32 v118, v42, v43\n" \
  "v_cvt_pk_bf16_f32 v119, v58, v59\n" \
  "ds_write_b128 v7, v[116:119] offset:34816\n" \
  "v_cvt_pk_bf16_f32 v120, v44, v45\n" \
  "v_cvt_pk_bf16_f32 v121, v60, v61\n" \
  "v_cvt_pk_bf16_f32 v122, v46, v47\n" \
  "v_cvt_pk_bf16_f32 v123, v62, v63\n" \
  "ds_write_b128 v7, v[120:123] offset:35840\n" \
  "s_waitcnt lgkmcnt(0)\n" \
  "s_barrier\n" \
  "s_setprio 2\n" \
  "ds_read_b128 v[48:51], v1 offset:32768\n" \
  "ds_read_b128 v[52:55], v1 offset:33792\n" \
  "ds_read_b128 v[56:59], v1 offset:34816\n" \
  "s_waitcnt lgkmcnt(2)\n" \
  "v_mfma_f32_16x16x32_bf16 v[64:67], v[128:131], v[48:51], 0\n" \
  "ds_read_b128 v[60:63], v1 offset:35840\n" \
  "s_lshl_b32 s75, s57, 19\n" \
  "v_mfma_f32_16x16x32_bf16 v[68:71], v[160:163], v[48:51], 0\n" \
  "s_add_u32 s75, s75, s60\n" \
  "v_mfma_f32_16x16x32_bf16 v[72:75], v[192:195], v[48:51], 0\n" \
  "s_add_u32 s76, s75, 16384\n" \
  "v_mfma_f32_16x16x32_bf16 v[76:79], v[224:227], v[48:51], 0\n" \
  "s_waitcnt lgkmcnt(2)\n" \
  "v_mfma_f32_16x16x32_bf16 v[64:67], v[132:135], v[52:55], v[64:67]\n" \
  "ds_read_b128 v[48:51], v1 offset:40960\n" \
  "v_mfma_f32_16x16x32_bf16 v[68:71], v[164:167], v[52:55], v[68:71]\n" \
  "v_mfma_f32_16x16x32_bf16 v[72:75], v[196:199], v[52:55], v[72:75]\n" \
  "v_mfma_f32_16x16x32_bf16 v[76:79], v[228:231], v[52:55], v[76:79]\n" \
  "s_waitcnt lgkmcnt(2)\n" \
  "v_mfma_f32_16x16x32_bf16 v[64:67], v[136:139], v[56:59], v[64:67]\n" \
  "ds_read_b128 v[52:55], v1 offset:41984\n" \
  "v_mfma_f32_16x16x32_bf16 v[68:71], v[168:171], v[56:59], v[68:71]\n" \
  "v_mfma_f32_16x16x32_bf16 v[72:75], v[200:203], v[56:59], v[72:75]\n" \
  "v_mfma_f32_16x16x32_bf16 v[76:79], v[232:235], v[56:59], v[76:79]\n" \
  "s_waitcnt lgkmcnt(2)\n" \
  "v_mfma_f32_16x16x32_bf16 v[64:67], v[140:143], v[60:63], v[64:67]\n" \
  "ds_read_b128 v[56:59], v1 offset:43008\n" \
  "v_mfma_f32_16x16x32_bf16 v[68:71], v[172:175], v[60:63], v[68:71]\n" \
  "v_mfma_f32_16x16x32_bf16 v[72:75], v[204:207], v[60:63], v[72:75]\n" \
  "v_mfma_f32_16x16x32_bf16 v[76:79], v[236:239], v[60:63], v[76:79]\n" \
  "s_waitcnt lgkmcnt(2)\n" \
  "v_mfma_f32_16x16x32_bf16 v[64:67], v[144:147], v[48:51], v[64:67]\n" \
  "ds_read_b128 v[60:63], v1 offset:44032\n" \
  "v_mfma_f32_16x16x32_bf16 v[68:71], v[176:179], v[48:51], v[68:71]\n" \
  "v_mfma_f32_16x16x32_bf16 v[72:75], v[208:211], v[48:51], v[72:75]\n" \
  "v_mfma_f32_16x16x32_bf16 v[76:79], v[240:243], v[48:51], v[76:79]\n" \
  "s_waitcnt lgkmcnt(2)\n" \
  "v_mfma_f32_16x16x32_bf16 v[64:67], v[148:151], v[52:55], v[64:67]\n" \
  "ds_read_b128 v[48:51], v1 offset:36864\n" \
  "v_mfma_f32_16x16x32_bf16 v[68:71], v[180:183], v[52:55], v[68:71]\n" \
  "v_mfma_f32_16x16x32_bf16 v[72:75], v[212:215], v[52:55], v[72:75]\n" \
  "v_mfma_f32_16x16x32_bf16 v[76:79], v[244:247], v[52:55], v[76:79]\n" \
  "s_waitcnt lgkmcnt(2)\n" \
  "v_mfma_f32_16x16x32_bf16 v[64:67], v[152:155], v[56:59], v[64:67]\n" \
  "ds_read_b128 v[52:55], v1 offset:37888\n" \
  "v_mfma_f32_16x16x32_bf16 v[68:71], v[184:187], v[56:59], v[68:71]\n" \
  "v_mfma_f32_16x16x32_bf16 v[72:75], v[216:219], v[56:59], v[72:75]\n" \
  "v_mfma_f32_16x16x32_bf16 v[76:79], v[248:251], v[56:59], v[76:79]\n" \
  "s_waitcnt lgkmcnt(2)\n" \
  "v_mfma_f32_16x16x32_bf16 v[64:67], v[156:159], v[60:63], v[64:67]\n" \
  "ds_read_b128 v[56:59], v1 offset:38912\n" \
  "v_mfma_f32_16x16x32_bf16 v[68:71], v[188:191], v[60:63], v[68:71]\n" \
  "v_mfma_f32_16x16x32_bf16 v[72:75], v[220:223], v[60:63], v[72:75]\n" \
  "v_mfma_f32_16x16x32_bf16 v[76:79], v[252:255], v[60:63], v[76:79]\n" \
  "s_waitcnt lgkmcnt(2)\n" \
  "v_mfma_f32_16x16x32_bf16 v[80:83], v[128:131], v[48:51], 0\n" \
  "ds_read_b128 v[60:63], v1 offset:39936\n" \
  "v_mfma_f32_16x16x32_bf16 v[84:87], v[160:163], v[48:51], 0\n" \
  "v_mfma_f32_16x16x32_bf16 v[88:91], v[192:195], v[48:51], 0\n" \
  "v_mfma_f32_16x16x32_bf16 v[92:95], v[224:227], v[48:51], 0\n" \
  "s_waitcnt lgkmcnt(2)\n" \
  "v_mfma_f32_16x16x32_bf16 v[80:83], v[132:135], v[52:55], v[80:83]\n" \
  "ds_read_b128 v[48:51], v1 offset:45056\n" \
  "v_mfma_f32_16x16x32_bf16 v[84:87], v[164:167], v[52:55], v[84:87]\n" \
  "v_cvt_pk_bf16_f32 v64, v64, v65\n" \
  "v_mfma_f32_16x16x32_bf16 v[88:91], v[196:199], v[52:55], v[88:91]\n" \
  "v_cvt_pk_bf16_f32 v65, v66, v67\n" \
  "v_mfma_f32_16x16x32_bf16 v[92:95], v[228:231], v[52:55], v[92:95]\n" \
  "s_waitcnt lgkmcnt(2)\n" \
  "v_mfma_f32_16x16x32_bf16 v[80:83], v[136:139], v[56:59], v[80:83]\n" \
  "ds_read_b128 v[52:55], v1 offset:46080\n" \
  "v_mfma_f32_16x16x32_bf16 v[84:87], v[168:171], v[56:59], v[84:87]\n" \
  "v_cvt_pk_bf16_f32 v66, v68, v69\n" \
  "v_mfma_f32_16x16x32_bf16 v[88:91], v[200:203], v[56:59], v[88:91]\n" \
  "v_cvt_pk_bf16_f32 v67, v70, v71\n" \
  "v_mfma_f32_16x16x32_bf16 v[92:95], v[232:235], v[56:59], v[92:95]\n" \
  "s_waitcnt lgkmcnt(2)\n" \
  "v_mfma_f32_16x16x32_bf16 v[80:83], v[140:143], v[60:63], v[80:83]\n" \
  "ds_read_b128 v[56:59], v1 offset:47104\n" \
  "v_mfma_f32_16x16x32_bf16 v[84:87], v[172:175], v[60:63], v[84:87]\n" \
  "buffer_store_dwordx4 v[64:67], v2, s[40:43], s75 offen offset:0 sc1\n" \
  "v_mfma_f32_16x16x32_bf16 v[88:91], v[204:207], v[60:63], v[88:91]\n" \
  "v_cvt_pk_bf16_f32 v72, v72, v73\n" \
  "v_mfma_f32_16x16x32_bf16 v[92:95], v[236:239], v[60:63], v[92:95]\n" \
  "s_waitcnt lgkmcnt(2)\n" \
  "v_mfma_f32_16x16x32_bf16 v[80:83], v[144:147], v[48:51], v[80:83]\n" \
  "ds_read_b128 v[60:63], v1 offset:48128\n" \
  "v_mfma_f32_16x16x32_bf16 v[84:87], v[176:179], v[48:51], v[84:87]\n" \
  "v_cvt_pk_bf16_f32 v73, v74, v75\n" \
  "v_mfma_f32_16x16x32_bf16 v[88:91], v[208:211], v[48:51], v[88:91]\n" \
  "v_cvt_pk_bf16_f32 v74, v76, v77\n" \
  "v_mfma_f32_16x16x32_bf16 v[92:95], v[240:243], v[48:51], v[92:95]\n" \
  "s_waitcnt lgkmcnt(2)\n" \
  "v_mfma_f32_16x16x32_bf16 v[80:83], v[148:151], v[52:55], v[80:83]\n" \
  "v_mfma_f32_16x16x32_bf16 v[84:87], v[180:183], v[52:55], v[84:87]\n" \
  "v_cvt_pk_bf16_f32 v75, v78, v79\n" \
  "v_mfma_f32_16x16x32_bf16 v[88:91], v[212:215], v[52:55], v[88:91]\n" \
  "buffer_store_dwordx4 v[72:75], v2, s[40:43], s75 offen offset:128 sc1\n" \
  "v_mfma_f32_16x16x32_bf16 v[92:95], v[244:247], v[52:55], v[92:95]\n" \
  "s_waitcnt lgkmcnt(1)\n" \
  "v_mfma_f32_16x16x32_bf16 v[80:83], v[152:155], v[56:59], v[80:83]\n" \
  "v_mfma_f32_16x16x32_bf16 v[84:87], v[184:187], v[56:59], v[84:87]\n" \
  "v_mfma_f32_16x16x32_bf16 v[88:91], v[216:219], v[56:59], v[88:91]\n" \
  "v_mfma_f32_16x16x32_bf16 v[92:95], v[248:251], v[56:59], v[92:95]\n" \
  "s_waitcnt lgkmcnt(0)\n" \
  "v_mfma_f32_16x16x32_bf16 v[80:83], v[156:159], v[60:63], v[80:83]\n" \
  "v_mfma_f32_16x16x32_bf16 v[84:87], v[188:191], v[60:63], v[84:87]\n" \
  "v_mfma_f32_16x16x32_bf16 v[88:91], v[220:223], v[60:63], v[88:91]\n" \
  "v_mfma_f32_16x16x32_bf16 v[92:95], v[252:255], v[60:63], v[92:95]\n" \
  "s_nop 4\n" \
  "v_cvt_pk_bf16_f32 v80, v80, v81\n" \
  "v_cvt_pk_bf16_f32 v81, v82, v83\n" \
  "v_cvt_pk_bf16_f32 v82, v84, v85\n" \
  "v_cvt_pk_bf16_f32 v83, v86, v87\n" \
  "buffer_store_dwordx4 v[80:83], v2, s[40:43], s76 offen offset:0 sc1\n" \
  "v_cvt_pk_bf16_f32 v88, v88, v89\n" \
  "v_cvt_pk_bf16_f32 v89, v90, v91\n" \
  "v_cvt_pk_bf16_f32 v90, v92, v93\n" \
  "v_cvt_pk_bf16_f32 v91, v94, v95\n" \
  "buffer_store_dwordx4 v[88:91], v2, s[40:43], s76 offen offset:128 sc1\n" \
  "s_branch .LWA_NEXT_%=\n" \
  ".p2alignl 12, 0xbf800000\n" \
  ".Lpad0x78:\n" \
  "s_setpc_b64 s[98:99]\n"
#define AW_ASM_1 \
  "v_lshlrev_b32 v7, 5, v0\n" \
  "s_mov_b32 s79, -1\n" \
  "s_cmp_eq_u32 s67, 0\n" \
  "s_cbranch_scc1 .LWB_LATE_%=\n" \
  "s_waitcnt vmcnt(0)\n" \
  "v_mov_b32 v12, s65\n" \
  "v_mov_b32 v13, s67\n" \
  "s_mov_b64 exec, 1\n" \
  "ds_add_u32 v12, v13\n" \
  "s_mov_b64 exec, -1\n" \
  ".p2align 6\n" \
  ".LWB_AL_%=:\n" \
  ".LWB_TOP_%=:\n" \
  "s_add_u32 s56, s56, 3\n" \
  "s_mov_b32 s77, 0\n" \
  "s_cmp_lt_u32 s56, %[w3]\n" \
  "s_cselect_b32 s68, s57, 0\n" \
  "s_cmp_ge_u32 s68, 64\n" \
  "s_cbranch_scc0 .LWB_HARD_%=\n" \
  "s_mov_b32 m0, s63\n" \
  "s_lshl_b32 s72, s57, 16\n" \
  "s_add_u32 s72, s72, s52\n" \
  "s_addc_u32 s73, s53, 0\n" \
  "global_load_lds_dwordx4 v0, s[72:73] offset:0 sc1\n" \
  "global_load_lds_dwordx4 v0, s[72:73] offset:1024 sc1\n" \
  ".LWB_HARD_%=:\n" \
  "s_sub_u32 s68, s56, %[wd]\n" \
  "s_bitcmp1_b32 s68, 0\n" \
  "s_cselect_b32 s58, 64, %[wd]\n" \
  "s_lshr_b32 s70, s68, 1\n" \
  "s_add_u32 s58, s58, s70\n" \
  "s_cmp_lt_u32 s68, %[w2]\n" \
  "s_cselect_b32 s58, s58, s56\n" \
  "s_min_u32 s58, s58, 128\n" \
  "s_lshr_b32 s68, s58, 6\n" \
  "s_add_u32 s68, s68, s58\n" \
  "s_lshl_b32 s68, s68, 12\n" \
  "buffer_load_dword v5, off, s[44:47], s68 sc1\n" \
  "s_xor_b32 s61, s61, 0x10000\n" \
  "s_xor_b32 s62, s62, 0x10000\n" \
  "s_mov_b32 s67, 0\n" \
  "s_cmp_lt_u32 s56, %[w3]\n" \
  "s_cselect_b32 s68, s57, 0\n" \
  "s_cmp_ge_u32 s68, 64\n" \
  "s_cbranch_scc1 .LWB_ASKEDE_%=\n" \
  "s_and_b32 s75, s57, 64\n" \
  "s_lshl_b32 s75, s75, 4\n" \
  "buffer_load_dword v14, off, s[84:87], s75 sc1\n" \
  "s_and_b32 s76, s57, 63\n" \
  "s_cmp_eq_u32 s76, 63\n" \
  "s_mul_i32 s76, s76, %[pp]\n" \
  "s_cselect_b32 s76, %[lm], s76\n" \
  "s_add_u32 s76, s76, %[mo]\n" \
  "s_lshl_b32 s59, s57, 19\n" \
  "s_add_u32 s59, s59, s66\n" \
  "s_add_u32 s78, s59, 4096\n" \
  "v_mov_b64 v[8:9], -1\n" \
  "v_mov_b64 v[10:11], -1\n" \
  "s_barrier\n" \
  "s_setprio 0\n" \
  "buffer_store_dwordx4 v[8:11], v0, s[40:43], s59 offen offset:0 sc1\n" \
  "buffer_store_dwordx4 v[8:11], v0, s[40:43], s59 offen offset:1024 sc1\n" \
  "buffer_store_dwordx4 v[8:11], v0, s[40:43], s59 offen offset:2048 sc1\n" \
  "buffer_store_dwordx4 v[8:11], v0, s[40:43], s59 offen offset:3072 sc1\n" \
  "buffer_store_dwordx4 v[8:11], v0, s[40:43], s78 offen offset:0 sc1\n" \
  "buffer_store_dwordx4 v[8:11], v0, s[40:43], s78 offen offset:1024 sc1\n" \
  "buffer_store_dwordx4 v[8:11], v0, s[40:43], s78 offen offset:2048 sc1\n" \
  "buffer_store_dwordx4 v[8:11], v0, s[40:43], s78 offen offset:3072 sc1\n" \
  "v_mov_b64 v[16:17], 0\n" \
  "v_mov_b64 v[18:19], 0\n" \
  "v_mov_b64 v[20:21], 0\n" \
  "v_mov_b64 v[22:23], 0\n" \
  "v_mov_b64 v[24:25], 0\n" \
  "v_mov_b64 v[26:27], 0\n" \
  "v_mov_b64 v[28:29], 0\n" \
  "v_mov_b64 v[30:31], 0\n" \
  "v_mov_b64 v[32:33], 0\n" \
  "v_mov_b64 v[34:35], 0\n" \
  "v_mov_b64 v[36:37], 0\n" \
  "v_mov_b64 v[38:39], 0\n" \
  "v_mov_b64 v[40:41], 0\n" \
  "v_mov_b64 v[42:43], 0\n" \
  "v_mov_b64 v[44:45], 0\n" \
  "v_mov_b64 v[46:47], 0\n" \
  "s_waitcnt vmcnt(8)\n" \
  "v_readfirstlane_b32 s74, v14\n" \
  "s_add_u32 s74, s74, s76\n" \
  "s_cmp_lt_i32 s79, 0\n" \
  "s_cbranch_scc1 .LWB_NOD_%=\n" \
  "v_mov_b32 v12, s64\n" \
  "v_mov_b32 v13, 1\n" \
  "s_mov_b64 exec, 1\n" \
  "ds_add_rtn_u32 v15, v12, v13\n" \
  "s_mov_b64 exec, -1\n" \
  "s_waitcnt lgkmcnt(0)\n" \
  "v_readfirstlane_b32 s68, v15\n" \
  "s_and_b32 s68, s68, 3\n" \
  "s_cmp_lg_u32 s68, 3\n" \
  "s_cbranch_scc1 .LWB_NOD_%=\n" \
  "s_memrealtime s[68:69]\n" \
  "s_lshl_b32 s72, s79, 11\n" \
  "s_add_u32 s72, s72, s54\n" \
  "s_addc_u32 s73, s55, 0\n" \
  "s_waitcnt lgkmcnt(0)\n" \
  "s_add_u32 s70, s68, 0\n" \
  "s_or_b32 s70, s70, 1\n" \
  "v_mov_b32 v13, s70\n" \
  "s_mov_b64 exec, 15\n" \
  "global_store_dword v7, v13, s[72:73] sc1\n" \
  "s_mov_b64 exec, -1\n" \
  ".LWB_NOD_%=:\n" \
  "s_mov_b32 s79, -1\n" \
  "v_cmp_eq_u32 vcc, 0, v5\n" \
  "s_cmp_eq_u64 vcc, 0\n" \
  "s_cbranch_scc0 .LWB_NOT_%=\n" \
  "s_mov_b32 m0, s61\n" \
  "s_lshl_b32 s68, s58, 19\n" \
  "s_add_u32 s68, s68, s48\n" \
  "s_addc_u32 s69, s49, 0\n" \
  "global_load_lds_dwordx4 v0, s[68:69] offset:0 sc1\n" \
  "global_load_lds_dwordx4 v0, s[68:69] offset:1024 sc1\n" \
  "global_load_lds_dwordx4 v0, s[68:69] offset:2048 sc1\n" \
  "global_load_lds_dwordx4 v0, s[68:69] offset:3072 sc1\n" \
  "s_add_u32 m0, s61, 4096\n" \
  "s_add_u32 s70, s68, 4096\n" \
  "s_addc_u32 s71, s69, 0\n" \
  "global_load_lds_dwordx4 v0, s[70:71] offset:0 sc1\n" \
  "global_load_lds_dwordx4 v0, s[70:71] offset:1024 sc1\n" \
  "global_load_lds_dwordx4 v0, s[70:71] offset:2048 sc1\n" \
  "global_load_lds_dwordx4 v0, s[70:71] offset:3072 sc1\n" \
  "s_mov_b32 m0, s62\n" \
  "s_lshl_b32 s72, s58, 18\n" \
  "s_add_u32 s72, s72, s50\n" \
  "s_addc_u32 s73, s51, 0\n" \
  "global_load_lds_dwordx4 v0, s[72:73] offset:0 sc1\n" \
  "global_load_lds_dwordx4 v0, s[72:73] offset:1024 sc1\n" \
  "global_load_lds_dwordx4 v0, s[72:73] offset:2048 sc1\n" \
  "global_load_lds_dwordx4 v0, s[72:73] offset:3072 sc1\n" \
  "s_mov_b32 s67, 1\n" \
  ".LWB_NOT_%=:\n" \
  "ds_read_b128 v[48:51], v1 offset:0\n" \
  "ds_read_b128 v[52:55], v1 offset:1024\n" \
  "ds_read_b128 v[56:59], v1 offset:2048\n" \
  "s_waitcnt lgkmcnt(2)\n" \
  "v_mfma_f32_16x16x32_bf16 v[64:67], v[128:131], v[48:51], 0\n" \
  "ds_read_b128 v[60:63], v1 offset:3072\n" \
  "v_mfma_f32_16x16x32_bf16 v[68:71], v[160:163], v[48:51], 0\n" \
  "v_mfma_f32_16x16x32_bf16 v[72:75], v[192:195], v[48:51], 0\n" \
  "v_mfma_f32_16x16x32_bf16 v[76:79], v[224:227], v[48:51], 0\n" \
  "s_waitcnt lgkmcnt(2)\n" \
  "v_mfma_f32_16x16x32_bf16 v[64:67], v[132:135], v[52:55], v[64:67]\n" \
  "ds_read_b128 v[48:51], v1 offset:8192\n" \
  "v_mfma_f32_16x16x32_bf16 v[68:71], v[164:167], v[52:55], v[68:71]\n" \
  "v_mfma_f32_16x16x32_bf16 v[72:75], v[196:199], v[52:55], v[72:75]\n" \
  "v_mfma_f32_16x16x32_bf16 v[76:79], v[228:231], v[52:55], v[76:79]\n" \
  "s_waitcnt lgkmcnt(2)\n" \
  "v_mfma_f32_16x16x32_bf16 v[64:67], v[136:139], v[56:59], v[64:67]\n" \
  "ds_read_b128 v[52:55], v1 offset:9216\n" \
  "s_memrealtime s[68:69]\n" \
  "v_mfma_f32_16x16x32_bf16 v[68:71], v[168:171], v[56:59], v[68:71]\n" \
  "v_mfma_f32_16x16x32_bf16 v[72:75], v[200:203], v[56:59], v[72:75]\n" \
  "v_mfma_f32_16x16x32_bf16 v[76:79], v[232:235], v[56:59], v[76:79]\n" \
  "s_waitcnt lgkmcnt(2)\n" \
  "v_mfma_f32_16x16x32_bf16 v[64:67], v[140:143], v[60:63], v[64:67]\n" \
  "ds_read_b128 v[56:59], v1 offset:10240\n" \
  "s_waitcnt lgkmcnt(0)\n" \
  "s_sub_i32 s70, s74, s68\n" \
  "s_cmp_gt_i32 s70, 0\n" \
  "s_cbranch_scc1 .LWB_NOTYET_%=\n" \
  "s_mov_b32 m0, s63\n" \
  "s_lshl_b32 s72, s57, 16\n" \
  "s_add_u32 s72, s72, s52\n" \
  "s_addc_u32 s73, s53, 0\n" \
  "global_load_lds_dwordx4 v0, s[72:73] offset:0 sc1\n" \
  "global_load_lds_dwordx4 v0, s[72:73] offset:1024 sc1\n" \
  "s_mov_b32 s77, 1\n" \
  ".LWB_NOTYET_%=:\n" \
  "s_lshl_b32 s70, s67, 7\n" \
  "s_or_b32 s70, s70, s58\n" \
  "s_lshr_b32 s68, s70, 6\n" \
  "s_add_u32 s68, s68, s70\n" \
  "s_lshl_b32 s68, s68, 12\n" \
  "buffer_load_dword v5, off, s[44:47], s68 sc1\n" \
  "v_mfma_f32_16x16x32_bf16 v[68:71], v[172:175], v[60:63], v[68:71]\n" \
  "v_mfma_f32_16x16x32_bf16 v[72:75], v[204:207], v[60:63], v[72:75]\n" \
  "v_mfma_f32_16x16x32_bf16 v[76:79], v[236:239], v[60:63], v[76:79]\n" \
  "s_waitcnt lgkmcnt(2)\n" \
  "v_mfma_f32_16x16x32_bf16 v[96:99], v[144:147], v[48:51], 0\n" \
  "ds_read_b128 v[60:63], v1 offset:11264\n" \
  "v_mfma_f32_16x16x32_bf16 v[100:103], v[176:179], v[48:51], 0\n" \
  "v_mfma_f32_16x16x32_bf16 v[104:107], v[208:211], v[48:51], 0\n" \
  "v_mfma_f32_16x16x32_bf16 v[108:111], v[240:243], v[48:51], 0\n" \
  "s_waitcnt lgkmcnt(2)\n" \
  "v_mfma_f32_16x16x32_bf16 v[96:99], v[148:151], v[52:55], v[96:99]\n" \
  "ds_read_b128 v[48:51], v1 offset:4096\n" \
  "v_mfma_f32_16x16x32_bf16 v[100:103], v[180:183], v[52:55], v[100:103]\n" \
  "v_mfma_f32_16x16x32_bf16 v[104:107], v[212:215], v[52:55], v[104:107]\n" \
  "v_mfma_f32_16x16x32_bf16 v[108:111], v[244:247], v[52:55], v[108:111]\n" \
  "s_waitcnt lgkmcnt(2)\n" \
  "v_mfma_f32_16x16x32_bf16 v[96:99], v[152:155], v[56:59], v[96:99]\n" \
  "ds_read_b128 v[52:55], v1 offset:5120\n" \
  "v_mfma_f32_16x16x32_bf16 v[100:103], v[184:187], v[56:59], v[100:103]\n" \
  "v_mfma_f32_16x16x32_bf16 v[104:107], v[216:219], v[56:59], v[104:107]\n" \
  "v_mfma_f32_16x16x32_bf16 v[108:111], v[248:251], v[56:59], v[108:111]\n" \
  "s_waitcnt lgkmcnt(2)\n" \
  "v_mfma_f32_16x16x32_bf16 v[96:99], v[156:159], v[60:63], v[96:99]\n" \
  "ds_read_b128 v[56:59], v1 offset:6144\n" \
  "v_mfma_f32_16x16x32_bf16 v[100:103], v[188:191], v[60:63], v[100:103]\n" \
  "v_mfma_f32_16x16x32_bf16 v[104:107], v[220:223], v[60:63], v[104:107]\n" \
  "v_mfma_f32_16x16x32_bf16 v[108:111], v[252:255], v[60:63], v[108:111]\n" \
  "s_waitcnt lgkmcnt(2)\n" \
  "v_mfma_f32_16x16x32_bf16 v[80:83], v[128:131], v[48:51], 0\n" \
  "ds_read_b128 v[60:63], v1 offset:7168\n" \
  "v_mfma_f32_16x16x32_bf16 v[84:87], v[160:163], v[48:51], 0\n" \
  "v_mfma_f32_16x16x32_bf16 v[88:91], v[192:195], v[48:51], 0\n" \
  "v_mfma_f32_16x16x32_bf16 v[92:95], v[224:227], v[48:51], 0\n" \
  "s_waitcnt lgkmcnt(2)\n" \
  "v_mfma_f32_16x16x32_bf16 v[80:83], v[132:135], v[52:55], v[80:83]\n" \
  "ds_read_b128 v[48:51], v1 offset:12288\n" \
  "v_mfma_f32_16x16x32_bf16 v[84:87], v[164:167], v[52:55], v[84:87]\n" \
  "v_mfma_f32_16x16x32_bf16 v[88:91], v[196:199], v[52:55], v[88:91]\n" \
  "v_mfma_f32_16x16x32_bf16 v[92:95], v[228:231], v[52:55], v[92:95]\n" \
  "s_waitcnt lgkmcnt(2)\n" \
  "v_mfma_f32_16x16x32_bf16 v[80:83], v[136:139], v[56:59], v[80:83]\n" \
  "ds_read_b128 v[52:55], v1 offset:13312\n" \
  "v_mfma_f32_16x16x32_bf16 v[84:87], v[168:171], v[56:59], v[84:87]\n" \
  "v_mfma_f32_16x16x32_bf16 v[88:91], v[200:203], v[56:59], v[88:91]\n" \
  "v_mfma_f32_16x16x32_bf16 v[92:95], v[232:235], v[56:59], v[92:95]\n" \
  "s_waitcnt lgkmcnt(2)\n" \
  "v_mfma_f32_16x16x32_bf16 v[80:83], v[140:143], v[60:63], v[80:83]\n" \
  "ds_read_b128 v[56:59], v1 offset:14336\n" \
  "v_mfma_f32_16x16x32_bf16 v[84:87], v[172:175], v[60:63], v[84:87]\n" \
  "v_mfma_f32_16x16x32_bf16 v[88:91], v[204:207], v[60:63], v[88:91]\n" \
  "v_mfma_f32_16x16x32_bf16 v[92:95], v[236:239], v[60:63], v[92:95]\n" \
  "s_waitcnt lgkmcnt(2)\n" \
  "v_mfma_f32_16x16x32_bf16 v[112:115], v[144:147], v[48:51], 0\n" \
  "ds_read_b128 v[60:63], v1 offset:15360\n" \
  "v_mfma_f32_16x16x32_bf16 v[116:119], v[176:179], v[48:51], 0\n" \
  "v_mfma_f32_16x16x32_bf16 v[120:123], v[208:211], v[48:51], 0\n" \
  "v_mfma_f32_16x16x32_bf16 v[124:127], v[240:243], v[48:51], 0\n" \
  "s_waitcnt lgkmcnt(2)\n" \
  "v_mfma_f32_16x16x32_bf16 v[112:115], v[148:151], v[52:55], v[112:115]\n" \
  "v_mfma_f32_16x16x32_bf16 v[116:119], v[180:183], v[52:55], v[116:119]\n" \
  "v_mfma_f32_16x16x32_bf16 v[120:123], v[212:215], v[52:55], v[120:123]\n" \
  "s_waitcnt vmcnt(0)\n" \
  "ds_read_b128 v[8:11], v3 offset:0\n" \
  "ds_read_b128 v[12:15], v3 offset:1024\n" \
  "v_mfma_f32_16x16x32_bf16 v[124:127], v[244:247], v[52:55], v[124:127]\n" \
  "s_waitcnt lgkmcnt(3)\n" \
  "v_mfma_f32_16x16x32_bf16 v[112:115], v[152:155], v[56:59], v[112:115]\n" \
  "v_mfma_f32_16x16x32_bf16 v[116:119], v[184:187], v[56:59], v[116:119]\n" \
  "v_mfma_f32_16x16x32_bf16 v[120:123], v[216:219], v[56:59], v[120:123]\n" \
  "v_mfma_f32_16x16x32_bf16 v[124:127], v[248:251], v[56:59], v[124:127]\n" \
  "s_waitcnt lgkmcnt(2)\n" \
  "v_mfma_f32_16x16x32_bf16 v[112:115], v[156:159], v[60:63], v[112:115]\n" \
  "s_waitcnt lgkmcnt(0)\n" \
  "v_min3_u32 v8, v8, v9, v10\n" \
  "v_mfma_f32_16x16x32_bf16 v[116:119], v[188:191], v[60:63], v[116:119]\n" \
  "v_min3_u32 v12, v12, v13, v14\n" \
  "v_mfma_f32_16x16x32_bf16 v[120:123], v[220:223], v[60:63], v[120:123]\n" \
  "v_min3_u32 v8, v8, v11, v15\n" \
  "v_mfma_f32_16x16x32_bf16 v[124:127], v[252:255], v[60:63], v[124:127]\n" \
  "v_min_u32 v8, v8, v12\n" \
  "v_cmp_eq_u32 vcc, 0, v5\n" \
  "s_cmp_eq_u64 vcc, 0\n" \
  "s_cbranch_scc0 .LWB_NOU_%=\n" \
  "s_mov_b32 m0, s61\n" \
  "s_lshl_b32 s68, s58, 19\n" \
  "s_add_u32 s68, s68, s48\n" \
  "s_addc_u32 s69, s49, 0\n" \
  "global_load_lds_dwordx4 v0, s[68:69] offset:0 sc1\n" \
  "global_load_lds_dwordx4 v0, s[68:69] offset:1024 sc1\n" \
  "global_load_lds_dwordx4 v0, s[68:69] offset:2048 sc1\n" \
  "global_load_lds_dwordx4 v0, s[68:69] offset:3072 sc1\n" \
  "s_add_u32 m0, s61, 4096\n" \
  "s_add_u32 s70, s68, 4096\n" \
  "s_addc_u32 s71, s69, 0\n" \
  "global_load_lds_dwordx4 v0, s[70:71] offset:0 sc1\n" \
  "global_load_lds_dwordx4 v0, s[70:71] offset:1024 sc1\n" \
  "global_load_lds_dwordx4 v0, s[70:71] offset:2048 sc1\n" \
  "global_load_lds_dwordx4 v0, s[70:71] offset:3072 sc1\n" \
  "s_mov_b32 m0, s62\n" \
  "s_lshl_b32 s72, s58, 18\n" \
  "s_add_u32 s72, s72, s50\n" \
  "s_addc_u32 s73, s51, 0\n" \
  "global_load_lds_dwordx4 v0, s[72:73] offset:0 sc1\n" \
  "global_load_lds_dwordx4 v0, s[72:73] offset:1024 sc1\n" \
  "global_load_lds_dwordx4 v0, s[72:73] offset:2048 sc1\n" \
  "global_load_lds_dwordx4 v0, s[72:73] offset:3072 sc1\n" \
  "s_mov_b32 s67, 1\n" \
  ".LWB_NOU_%=:\n" \
  "s_cmp_eq_u32 s77, 1\n" \
  "s_cbranch_scc1 .LWB_HERE_%=\n" \
  "s_memrealtime s[68:69]\n" \
  "s_waitcnt lgkmcnt(0)\n" \
  "s_sub_i32 s70, s74, s68\n" \
  "s_cmp_lt_i32 s70, 50\n" \
  "s_cbranch_scc1 .LWB_SOON_%=\n" \
  ".LWB_GATEA_%=:\n" \
  "s_memrealtime s[68:69]\n" \
  "s_waitcnt lgkmcnt(0)\n" \
  "s_sub_i32 s70, s74, s68\n" \
  "s_sub_u32 s70, s70, 65\n" \
  "s_cmp_gt_u32 s70, 2000\n" \
  "s_cbranch_scc1 .LWB_NEWS_%=\n" \
  "s_sleep 1\n" \
  "s_cmp_lt_u32 s70, 40\n" \
  "s_cbranch_scc1 .LWB_GATEA_%=\n" \
  "s_sleep 8\n" \
  "s_branch .LWB_GATEA_%=\n" \
  ".LWB_HERE_%=:\n" \
  "v_mov_b32 v12, s64\n" \
  "v_mov_b32 v13, 1\n" \
  "s_mov_b64 exec, 1\n" \
  "ds_add_rtn_u32 v15, v12, v13 offset:4\n" \
  "s_mov_b64 exec, -1\n" \
  "s_waitcnt lgkmcnt(0)\n" \
  "v_readfirstlane_b32 s68, v15\n" \
  "s_and_b32 s68, s68, 3\n" \
  "s_cmp_lg_u32 s68, 3\n" \
  "s_cbranch_scc1 .LWB_TEST_%=\n" \
  "s_memrealtime s[68:69]\n" \
  "s_lshl_b32 s72, s57, 11\n" \
  "s_add_u32 s72, s72, s54\n" \
  "s_addc_u32 s73, s55, 0\n" \
  "s_waitcnt lgkmcnt(0)\n" \
  "s_cmp_eq_u32 s57, 127\n" \
  "s_cselect_b32 s70, %[ld], 0\n" \
  "s_sub_u32 s68, s68, s70\n" \
  "s_add_u32 s70, s68, %[dt]\n" \
  "s_or_b32 s70, s70, 1\n" \
  "v_mov_b32 v13, s70\n" \
  "s_mov_b64 exec, 15\n" \
  "global_store_dword v7, v13, s[72:73] sc1\n" \
  "s_mov_b64 exec, -1\n" \
  "s_branch .LWB_TEST_%=\n" \
  ".LWB_NEWS_%=:\n" \
  "buffer_load_dword v14, off, s[84:87], s75 sc1\n" \
  "s_waitcnt vmcnt(0)\n" \
  "v_readfirstlane_b32 s74, v14\n" \
  "s_add_u32 s74, s74, s76\n" \
  ".LWB_SOON_%=:\n" \
  "v_mov_b32 v12, s64\n" \
  "v_mov_b32 v13, 1\n" \
  "s_mov_b64 exec, 1\n" \
  "ds_add_rtn_u32 v15, v12, v13 offset:4\n" \
  "s_mov_b64 exec, -1\n" \
  "s_waitcnt lgkmcnt(0)\n" \
  "v_readfirstlane_b32 s68, v15\n" \
  "s_and_b32 s68, s68, 3\n" \
  "s_cmp_lg_u32 s68, 3\n" \
  "s_cbranch_scc1 .LWB_GATEB_%=\n" \
  "s_memrealtime s[68:69]\n" \
  "s_lshl_b32 s72, s57, 11\n" \
  "s_add_u32 s72, s72, s54\n" \
  "s_addc_u32 s73, s55, 0\n" \
  "s_waitcnt lgkmcnt(0)\n" \
  "s_sub_i32 s70, s74, s68\n" \
  "s_cmp_gt_u32 s70, 2000\n" \
  "s_cselect_b32 s70, 0, s70\n" \
  "s_add_u32 s68, s68, s70\n" \
  "s_cmp_eq_u32 s57, 127\n" \
  "s_cselect_b32 s70, %[ld], 0\n" \
  "s_sub_u32 s68, s68, s70\n" \
  "s_add_u32 s70, s68, %[du]\n" \
  "s_or_b32 s70, s70, 1\n" \
  "v_mov_b32 v13, s70\n" \
  "s_mov_b64 exec, 15\n" \
  "global_store_dword v7, v13, s[72:73] sc1\n" \
  "s_mov_b64 exec, -1\n" \
  ".LWB_GATEB_%=:\n" \
  "s_memrealtime s[68:69]\n" \
  "s_waitcnt lgkmcnt(0)\n" \
  "s_sub_i32 s70, s74, s68\n" \
  "s_sub_u32 s70, s70, 0\n" \
  "s_cmp_gt_u32 s70, 2000\n" \
  "s_cbranch_scc1 .LWB_ASK_%=\n" \
  "s_sleep 1\n" \
  "s_cmp_lt_u32 s70, 40\n" \
  "s_cbranch_scc1 .LWB_GATEB_%=\n" \
  "s_sleep 8\n" \
  "s_branch .LWB_GATEB_%=\n" \
  ".LWB_AGAIN_%=:\n" \
  "s_sleep %[sm]\n" \
  ".LWB_ASK_%=:\n" \
  "s_mov_b32 m0, s63\n" \
  "s_lshl_b32 s72, s57, 16\n" \
  "s_add_u32 s72, s72, s52\n" \
  "s_addc_u32 s73, s53, 0\n" \
  "global_load_lds_dwordx4 v0, s[72:73] offset:0 sc1\n" \
  "global_load_lds_dwordx4 v0, s[72:73] offset:1024 sc1\n" \
  "s_add_u32 s77, s77, 1\n" \
  ".LWB_ASKED_%=:\n" \
  "s_waitcnt vmcnt(0)\n" \
  "ds_read_b128 v[8:11], v3 offset:0\n" \
  "ds_read_b128 v[12:15], v3 offset:1024\n" \
  "s_cmp_gt_u32 s77, 0x40000\n" \
  "s_cbranch_scc1 .LWB_OK_%=\n" \
  "s_waitcnt lgkmcnt(1)\n" \
  "v_min3_u32 v8, v8, v9, v10\n" \
  "s_waitcnt lgkmcnt(0)\n" \
  "v_min3_u32 v12, v12, v13, v14\n" \
  "v_min3_u32 v8, v8, v11, v15\n" \
  "v_min_u32 v8, v8, v12\n" \
  ".LWB_TEST_%=:\n" \
  "v_cmp_eq_u32 vcc, 0, v8\n" \
  "s_cmp_eq_u64 vcc, 0\n" \
  "s_cbranch_scc0 .LWB_AGAIN_%=\n" \
  ".LWB_OK_%=:\n" \
  "s_waitcnt lgkmcnt(0)\n" \
  "s_barrier\n" \
  "ds_read_b64_tr_b16 v[48:49], v6 offset:0\n" \
  "ds_read_b64_tr_b16 v[50:51], v6 offset:32\n" \
  "ds_read_b64_tr_b16 v[52:53], v6 offset:1024\n" \
  "ds_read_b64_tr_b16 v[54:55], v6 offset:1056\n" \
  "ds_read_b64_tr_b16 v[56:57], v6 offset:4096\n" \
  "ds_read_b64_tr_b16 v[58:59], v6 offset:4128\n" \
  "ds_read_b64_tr_b16 v[60:61], v6 offset:5120\n" \
  "ds_read_b64_tr_b16 v[62:63], v6 offset:5152\n" \
  "s_waitcnt lgkmcnt(6)\n" \
  "v_smfmac_f32_16x16x64_bf16 v[16:19], v[48:51], v[128:135], v4\n" \
  "v_smfmac_f32_16x16x64_bf16 v[20:23], v[48:51], v[160:167], v4\n" \
  "v_smfmac_f32_16x16x64_bf16 v[24:27], v[48:51], v[192:199], v4\n" \
  "v_smfmac_f32_16x16x64_bf16 v[28:31], v[48:51], v[224:231], v4\n" \
  "s_waitcnt lgkmcnt(4)\n" \
  "v_smfmac_f32_16x16x64_bf16 v[16:19], v[52:55], v[136:143], v4\n" \
  "v_smfmac_f32_16x16x64_bf16 v[20:23], v[52:55], v[168:175], v4\n" \
  "v_smfmac_f32_16x16x64_bf16 v[24:27], v[52:55], v[200:207], v4\n" \
  "v_smfmac_f32_16x16x64_bf16 v[28:31], v[52:55], v[232:239], v4\n" \
  "s_waitcnt lgkmcnt(2)\n" \
  "v_smfmac_f32_16x16x64_bf16 v[32:35], v[56:59], v[144:151], v4\n" \
  "v_smfmac_f32_16x16x64_bf16 v[36:39], v[56:59], v[176:183], v4\n" \
  "v_smfmac_f32_16x16x64_bf16 v[40:43], v[56:59], v[208:215], v4\n" \
  "v_smfmac_f32_16x16x64_bf16 v[44:47], v[56:59], v[240:247], v4\n" \
  "s_waitcnt lgkmcnt(0)\n" \
  "v_smfmac_f32_16x16x64_bf16 v[32:35], v[60:63], v[152:159], v4\n" \
  "v_smfmac_f32_16x16x64_bf16 v[36:39], v[60:63], v[184:191], v4\n" \
  "v_smfmac_f32_16x16x64_bf16 v[40:43], v[60:63], v[216:223], v4\n" \
  "v_smfmac_f32_16x16x64_bf16 v[44:47], v[60:63], v[248:255], v4\n" \
  "s_nop 1\n" \
  "s_lshl_b32 s75, s57, 19\n" \
  "s_add_u32 s75, s75, s60\n" \
  "s_add_u32 s76, s75, 16384\n" \
  "v_pk_add_f32 v[16:17], v[16:17], v[18:19]\n" \
  "v_pk_add_f32 v[32:33], v[32:33], v[34:35]\n" \
  "v_cvt_pk_bf16_f32 v16, v16, v17\n" \
  "v_cvt_pk_bf16_f32 v17, v32, v33\n" \
  "v_pk_add_f32 v[20:21], v[20:21], v[22:23]\n" \
  "v_pk_add_f32 v[36:37], v[36:37], v[38:39]\n" \
  "v_cvt_pk_bf16_f32 v20, v20, v21\n" \
  "v_cvt_pk_bf16_f32 v21, v36, v37\n" \
  "v_pk_add_f32 v[24:25], v[24:25], v[26:27]\n" \
  "v_pk_add_f32 v[40:41], v[40:41], v[42:43]\n" \
  "v_cvt_pk_bf16_f32 v24, v24, v25\n" \
  "v_cvt_pk_bf16_f32 v25, v40, v41\n" \
  "v_pk_add_f32 v[28:29], v[28:29], v[30:31]\n" \
  "v_pk_add_f32 v[44:45], v[44:45], v[46:47]\n" \
  "v_cvt_pk_bf16_f32 v28, v28, v29\n" \
  "v_cvt_pk_bf16_f32 v29, v44, v45\n" \
  "s_barrier\n" \
  "ds_read_b128 v[32:35], v1 offset:24576\n" \
  "ds_read_b128 v[36:39], v1 offset:25600\n" \
  "s_setprio 1\n" \
  "s_waitcnt lgkmcnt(0)\n" \
  "v_pk_mul_f32 v[64:65], v[64:65], v[34:35] op_sel_hi:[1,0]\n" \
  "v_pk_mul_f32 v[66:67], v[66:67], v[34:35] op_sel_hi:[1,0]\n" \
  "v_pk_mul_f32 v[68:69], v[68:69], v[34:35] op_sel_hi:[1,0]\n" \
  "v_pk_mul_f32 v[70:71], v[70:71], v[34:35] op_sel_hi:[1,0]\n" \
  "v_pk_fma_f32 v[64:65], v[96:97], v[34:35], v[64:65] op_sel:[0,1,0]\n" \
  "v_pk_fma_f32 v[66:67], v[98:99], v[34:35], v[66:67] op_sel:[0,1,0]\n" \
  "v_pk_fma_f32 v[68:69], v[100:101], v[34:35], v[68:69] op_sel:[0,1,0]\n" \
  "v_pk_fma_f32 v[70:71], v[102:103], v[34:35], v[70:71] op_sel:[0,1,0]\n" \
  "s_nop 1\n" \
  "v_mfma_f32_16x16x16_bf16 v[64:67], v[16:17], v[32:33], v[64:67]\n" \
  "v_mfma_f32_16x16x16_bf16 v[68:71], v[20:21], v[32:33], v[68:71]\n" \
  "v_pk_mul_f32 v[72:73], v[72:73], v[34:35] op_sel_hi:[1,0]\n" \
  "v_pk_mul_f32 v[74:75], v[74:75], v[34:35] op_sel_hi:[1,0]\n" \
  "v_pk_mul_f32 v[76:77], v[76:77], v[34:35] op_sel_hi:[1,0]\n" \
  "v_pk_mul_f32 v[78:79], v[78:79], v[34:35] op_sel_hi:[1,0]\n" \
  "v_pk_fma_f32 v[72:73], v[104:105], v[34:35], v[72:73] op_sel:[0,1,0]\n" \
  "v_pk_fma_f32 v[74:75], v[106:107], v[34:35], v[74:75] op_sel:[0,1,0]\n" \
  "v_pk_fma_f32 v[76:77], v[108:109], v[34:35], v[76:77] op_sel:[0,1,0]\n" \
  "v_pk_fma_f32 v[78:79], v[110:111], v[34:35], v[78:79] op_sel:[0,1,0]\n" \
  "s_nop 1\n" \
  "v_mfma_f32_16x16x16_bf16 v[72:75], v[24:25], v[32:33], v[72:75]\n" \
  "v_mfma_f32_16x16x16_bf16 v[76:79], v[28:29], v[32:33], v[76:79]\n" \
  "v_cvt_pk_bf16_f32 v64, v64, v65\n" \
  "v_cvt_pk_bf16_f32 v65, v66, v67\n" \
  "v_cvt_pk_bf16_f32 v66, v68, v69\n" \
  "v_cvt_pk_bf16_f32 v67, v70, v71\n" \
  "buffer_store_dwordx4 v[64:67], v2, s[40:43], s75 offen offset:0 sc1\n" \
  "v_pk_mul_f32 v[80:81], v[80:81], v[38:39] op_sel_hi:[1,0]\n" \
  "v_pk_mul_f32 v[82:83], v[82:83], v[38:39] op_sel_hi:[1,0]\n" \
  "v_pk_mul_f32 v[84:85], v[84:85], v[38:39] op_sel_hi:[1,0]\n" \
  "v_pk_mul_f32 v[86:87], v[86:87], v[38:39] op_sel_hi:[1,0]\n" \
  "v_pk_fma_f32 v[80:81], v[112:113], v[38:39], v[80:81] op_sel:[0,1,0]\n" \
  "v_pk_fma_f32 v[82:83], v[114:115], v[38:39], v[82:83] op_sel:[0,1,0]\n" \
  "v_pk_fma_f32 v[84:85], v[116:117], v[38:39], v[84:85] op_sel:[0,1,0]\n" \
  "v_pk_fma_f32 v[86:87], v[118:119], v[38:39], v[86:87] op_sel:[0,1,0]\n" \
  "s_nop 1\n" \
  "v_mfma_f32_16x16x16_bf16 v[80:83], v[16:17], v[36:37], v[80:83]\n" \
  "v_mfma_f32_16x16x16_bf16 v[84:87], v[20:21], v[36:37], v[84:87]\n" \
  "v_cvt_pk_bf16_f32 v72, v72, v73\n" \
  "v_cvt_pk_bf16_f32 v73, v74, v75\n" \
  "v_cvt_pk_bf16_f32 v74, v76, v77\n" \
  "v_cvt_pk_bf16_f32 v75, v78, v79\n" \
  "buffer_store_dwordx4 v[72:75], v2, s[40:43], s75 offen offset:128 sc1\n" \
  "v_pk_mul_f32 v[88:89], v[88:89], v[38:39] op_sel_hi:[1,0]\n" \
  "v_pk_mul_f32 v[90:91], v[90:91], v[38:39] op_sel_hi:[1,0]\n" \
  "v_pk_mul_f32 v[92:93], v[92:93], v[38:39] op_sel_hi:[1,0]\n" \
  "v_pk_mul_f32 v[94:95], v[94:95], v[38:39] op_sel_hi:[1,0]\n" \
  "v_pk_fma_f32 v[88:89], v[120:121], v[38:39], v[88:89] op_sel:[0,1,0]\n" \
  "v_pk_fma_f32 v[90:91], v[122:123], v[38:39], v[90:91] op_sel:[0,1,0]\n" \
  "v_pk_fma_f32 v[92:93], v[124:125], v[38:39], v[92:93] op_sel:[0,1,0]\n" \
  "v_pk_fma_f32 v[94:95], v[126:127], v[38:39], v[94:95] op_sel:[0,1,0]\n" \
  "s_nop 1\n" \
  "v_mfma_f32_16x16x16_bf16 v[88:91], v[24:25], v[36:37], v[88:91]\n" \
  "v_mfma_f32_16x16x16_bf16 v[92:95], v[28:29], v[36:37], v[92:95]\n" \
  "v_cvt_pk_bf16_f32 v80, v80, v81\n" \
  "v_cvt_pk_bf16_f32 v81, v82, v83\n" \
  "v_cvt_pk_bf16_f32 v82, v84, v85\n" \
  "v_cvt_pk_bf16_f32 v83, v86, v87\n" \
  "buffer_store_dwordx4 v[80:83], v2, s[40:43], s76 offen offset:0 sc1\n" \
  "s_nop 2\n" \
  "v_cvt_pk_bf16_f32 v88, v88, v89\n" \
  "v_cvt_pk_bf16_f32 v89, v90, v91\n" \
  "v_cvt_pk_bf16_f32 v90, v92, v93\n" \
  "v_cvt_pk_bf16_f32 v91, v94, v95\n" \
  "buffer_store_dwordx4 v[88:91], v2, s[40:43], s76 offen offset:128 sc1\n" \
  "s_waitcnt vmcnt(4)\n" \
  "v_mov_b32 v12, s65\n" \
  "v_mov_b32 v13, s67\n" \
  "s_mov_b64 exec, 1\n" \
  "ds_add_u32 v12, v13\n" \
  "s_mov_b64 exec, -1\n" \
  ".LWB_NEXT_%=:\n" \
  "s_setprio 0\n" \
  "s_mov_b32 s57, s58\n" \
  "s_cmp_eq_u32 s67, 0\n" \
  "s_cbranch_scc1 .LWB_MISS_%=\n" \
  "s_branch .LWB_TOP_%=\n" \
  ".LWB_MISS_%=:\n" \
  "s_cmp_eq_u32 s57, 128\n" \
  "s_cbranch_scc1 .LWB_END_%=\n" \
  ".LWB_LATE_%=:\n" \
  "s_mov_b32 s77, 0x40000\n" \
  ".LWB_POLL_%=:\n" \
  "s_lshr_b32 s68, s57, 6\n" \
  "s_add_u32 s68, s68, s57\n" \
  "s_lshl_b32 s68, s68, 12\n" \
  "buffer_load_dword v5, off, s[44:47], s68 sc1\n" \
  "s_sub_u32 s77, s77, 1\n" \
  "s_cbranch_scc1 .LWB_SEEN_%=\n" \
  "s_waitcnt vmcnt(0)\n" \
  "v_cmp_eq_u32 vcc, 0, v5\n" \
  "s_cmp_eq_u64 vcc, 0\n" \
  "s_cbranch_scc1 .LWB_SEEN_%=\n" \
  "s_sleep %[sa]\n" \
  "s_branch .LWB_POLL_%=\n" \
  ".LWB_SEEN_%=:\n" \
  "s_mov_b32 m0, s61\n" \
  "s_lshl_b32 s68, s57, 19\n" \
  "s_add_u32 s68, s68, s48\n" \
  "s_addc_u32 s69, s49, 0\n" \
  "global_load_lds_dwordx4 v0, s[68:69] offset:0 sc1\n" \
  "global_load_lds_dwordx4 v0, s[68:69] offset:1024 sc1\n" \
  "global_load_lds_dwordx4 v0, s[68:69] offset:2048 sc1\n" \
  "global_load_lds_dwordx4 v0, s[68:69] offset:3072 sc1\n" \
  "s_add_u32 m0, s61, 4096\n" \
  "s_add_u32 s70, s68, 4096\n" \
  "s_addc_u32 s71, s69, 0\n" \
  "global_load_lds_dwordx4 v0, s[70:71] offset:0 sc1\n" \
  "global_load_lds_dwordx4 v0, s[70:71] offset:1024 sc1\n" \
  "global_load_lds_dwordx4 v0, s[70:71] offset:2048 sc1\n" \
  "global_load_lds_dwordx4 v0, s[70:71] offset:3072 sc1\n" \
  "s_mov_b32 m0, s62\n" \
  "s_lshl_b32 s72, s57, 18\n" \
  "s_add_u32 s72, s72, s50\n" \
  "s_addc_u32 s73, s51, 0\n" \
  "global_load_lds_dwordx4 v0, s[72:73] offset:0 sc1\n" \
  "global_load_lds_dwordx4 v0, s[72:73] offset:1024 sc1\n" \
  "global_load_lds_dwordx4 v0, s[72:73] offset:2048 sc1\n" \
  "global_load_lds_dwordx4 v0, s[72:73] offset:3072 sc1\n" \
  "s_mov_b32 s67, 1\n" \
  "s_waitcnt vmcnt(0)\n" \
  "v_mov_b32 v12, s65\n" \
  "v_mov_b32 v13, s67\n" \
  "s_mov_b64 exec, 1\n" \
  "ds_add_u32 v12, v13\n" \
  "s_mov_b64 exec, -1\n" \
  "s_branch .LWB_TOP_%=\n" \
  ".LWB_END_%=:\n" \
  "s_endpgm\n" \
  ".LWB_AGAINE_%=:\n" \
  "s_sleep %[sm]\n" \
  ".LWB_ASKE_%=:\n" \
  "s_mov_b32 m0, s63\n" \
  "s_lshl_b32 s72, s57, 16\n" \
  "s_add_u32 s72, s72, s52\n" \
  "s_addc_u32 s73, s53, 0\n" \
  "global_load_lds_dwordx4 v0, s[72:73] offset:0 sc1\n" \
  "global_load_lds_dwordx4 v0, s[72:73] offset:1024 sc1\n" \
  "s_add_u32 s77, s77, 1\n" \
  ".LWB_ASKEDE_%=:\n" \
  "s_waitcnt vmcnt(0)\n" \
  "ds_read_b128 v[8:11], v3 offset:0\n" \
  "ds_read_b128 v[12:15], v3 offset:1024\n" \
  "s_cmp_gt_u32 s77, 0x40000\n" \
  "s_cbranch_scc1 .LWB_OKE_%=\n" \
  "s_waitcnt lgkmcnt(1)\n" \
  "v_min3_u32 v8, v8, v9, v10\n" \
  "s_waitcnt lgkmcnt(0)\n" \
  "v_min3_u32 v12, v12, v13, v14\n" \
  "v_min3_u32 v8, v8, v11, v15\n" \
  "v_min_u32 v8, v8, v12\n" \
  ".LWB_TESTE_%=:\n" \
  "v_cmp_eq_u32 vcc, 0, v8\n" \
  "s_cmp_eq_u64 vcc, 0\n" \
  "s_cbranch_scc0 .LWB_AGAINE_%=\n" \
  ".LWB_OKE_%=:\n" \
  "s_waitcnt lgkmcnt(0)\n" \
  "s_barrier\n" \
  "v_cmp_eq_u32 vcc, 0, v5\n" \
  "s_cmp_eq_u64 vcc, 0\n" \
  "s_cbranch_scc0 .LWB_NOE_%=\n" \
  "s_mov_b32 m0, s61\n" \
  "s_lshl_b32 s68, s58, 19\n" \
  "s_add_u32 s68, s68, s48\n" \
  "s_addc_u32 s69, s49, 0\n" \
  "global_load_lds_dwordx4 v0, s[68:69] offset:0 sc1\n" \
  "global_load_lds_dwordx4 v0, s[68:69] offset:1024 sc1\n" \
  "global_load_lds_dwordx4 v0, s[68:69] offset:2048 sc1\n" \
  "global_load_lds_dwordx4 v0, s[68:69] offset:3072 sc1\n" \
  "s_add_u32 m0, s61, 4096\n" \
  "s_add_u32 s70, s68, 4096\n" \
  "s_addc_u32 s71, s69, 0\n" \
  "global_load_lds_dwordx4 v0, s[70:71] offset:0 sc1\n" \
  "global_load_lds_dwordx4 v0, s[70:71] offset:1024 sc1\n" \
  "global_load_lds_dwordx4 v0, s[70:71] offset:2048 sc1\n" \
  "global_load_lds_dwordx4 v0, s[70:71] offset:3072 sc1\n" \
  "s_mov_b32 m0, s62\n" \
  "s_lshl_b32 s72, s58, 18\n" \
  "s_add_u32 s72, s72, s50\n" \
  "s_addc_u32 s73, s51, 0\n" \
  "global_load_lds_dwordx4 v0, s[72:73] offset:0 sc1\n" \
  "global_load_lds_dwordx4 v0, s[72:73] offset:1024 sc1\n" \
  "global_load_lds_dwordx4 v0, s[72:73] offset:2048 sc1\n" \
  "global_load_lds_dwordx4 v0, s[72:73] offset:3072 sc1\n" \
  "s_mov_b32 s67, 1\n" \
  ".LWB_NOE_%=:\n" \
  "s_lshl_b32 s70, s67, 7\n" \
  "s_or_b32 s70, s70, s58\n" \
  "s_lshr_b32 s68, s70, 6\n" \
  "s_add_u32 s68, s68, s70\n" \
  "s_lshl_b32 s68, s68, 12\n" \
  "buffer_load_dword v5, off, s[44:47], s68 sc1\n" \
  "s_mov_b32 s77, 0\n" \
  "s_barrier\n" \
  "s_setprio 0\n" \
  "s_waitcnt vmcnt(0)\n" \
  "v_mov_b32 v12, s65\n" \
  "v_mov_b32 v13, s67\n" \
  "s_mov_b64 exec, 1\n" \
  "ds_add_u32 v12, v13\n" \
  "s_mov_b64 exec, -1\n" \
  "v_cmp_eq_u32 vcc, 0, v5\n" \
  "s_cmp_eq_u64 vcc, 0\n" \
  "s_cbranch_scc0 .LWB_NOF_%=\n" \
  "s_mov_b32 m0, s61\n" \
  "s_lshl_b32 s68, s58, 19\n" \
  "s_add_u32 s68, s68, s48\n" \
  "s_addc_u32 s69, s49, 0\n" \
  "global_load_lds_dwordx4 v0, s[68:69] offset:0 sc1\n" \
  "global_load_lds_dwordx4 v0, s[68:69] offset:1024 sc1\n" \
  "global_load_lds_dwordx4 v0, s[68:69] offset:2048 sc1\n" \
  "global_load_lds_dwordx4 v0, s[68:69] offset:3072 sc1\n" \
  "s_add_u32 m0, s61, 4096\n" \
  "s_add_u32 s70, s68, 4096\n" \
  "s_addc_u32 s71, s69, 0\n" \
  "global_load_lds_dwordx4 v0, s[70:71] offset:0 sc1\n" \
  "global_load_lds_dwordx4 v0, s[70:71] offset:1024 sc1\n" \
  "global_load_lds_dwordx4 v0, s[70:71] offset:2048 sc1\n" \
  "global_load_lds_dwordx4 v0, s[70:71] offset:3072 sc1\n" \
  "s_mov_b32 m0, s62\n" \
  "s_lshl_b32 s72, s58, 18\n" \
  "s_add_u32 s72, s72, s50\n" \
  "s_addc_u32 s73, s51, 0\n" \
  "global_load_lds_dwordx4 v0, s[72:73] offset:0 sc1\n" \
  "global_load_lds_dwordx4 v0, s[72:73] offset:1024 sc1\n" \
  "global_load_lds_dwordx4 v0, s[72:73] offset:2048 sc1\n" \
  "global_load_lds_dwordx4 v0, s[72:73] offset:3072 sc1\n" \
  "s_mov_b32 s67, 1\n" \
  "s_mov_b32 s77, 1\n" \
  ".LWB_NOF_%=:\n" \
  "ds_read_b128 v[48:51], v1 offset:32768\n" \
  "ds_read_b128 v[52:55], v1 offset:33792\n" \
  "ds_read_b128 v[56:59], v1 offset:34816\n" \
  "s_waitcnt lgkmcnt(2)\n" \
  "v_mfma_f32_16x16x32_bf16 v[64:67], v[128:131], v[48:51], 0\n" \
  "ds_read_b128 v[60:63], v1 offset:35840\n" \
  "s_lshl_b32 s75, s57, 19\n" \
  "v_mfma_f32_16x16x32_bf16 v[68:71], v[160:163], v[48:51], 0\n" \
  "s_add_u32 s75, s75, s60\n" \
  "v_mfma_f32_16x16x32_bf16 v[72:75], v[192:195], v[48:51], 0\n" \
  "s_add_u32 s76, s75, 16384\n" \
  "v_mfma_f32_16x16x32_bf16 v[76:79], v[224:227], v[48:51], 0\n" \
  "s_lshl_b32 s70, s67, 7\n" \
  "s_or_b32 s70, s70, s58\n" \
  "s_lshr_b32 s68, s70, 6\n" \
  "s_add_u32 s68, s68, s70\n" \
  "s_lshl_b32 s68, s68, 12\n" \
  "buffer_load_dword v5, off, s[44:47], s68 sc1\n" \
  "s_waitcnt lgkmcnt(2)\n" \
  "v_mfma_f32_16x16x32_bf16 v[64:67], v[132:135], v[52:55], v[64:67]\n" \
  "ds_read_b128 v[48:51], v1 offset:40960\n" \
  "v_mfma_f32_16x16x32_bf16 v[68:71], v[164:167], v[52:55], v[68:71]\n" \
  "v_mfma_f32_16x16x32_bf16 v[72:75], v[196:199], v[52:55], v[72:75]\n" \
  "v_mfma_f32_16x16x32_bf16 v[76:79], v[228:231], v[52:55], v[76:79]\n" \
  "s_waitcnt lgkmcnt(2)\n" \
  "v_mfma_f32_16x16x32_bf16 v[64:67], v[136:139], v[56:59], v[64:67]\n" \
  "ds_read_b128 v[52:55], v1 offset:41984\n" \
  "v_mfma_f32_16x16x32_bf16 v[68:71], v[168:171], v[56:59], v[68:71]\n" \
  "v_mfma_f32_16x16x32_bf16 v[72:75], v[200:203], v[56:59], v[72:75]\n" \
  "v_mfma_f32_16x16x32_bf16 v[76:79], v[232:235], v[56:59], v[76:79]\n" \
  "s_waitcnt lgkmcnt(2)\n" \
  "v_mfma_f32_16x16x32_bf16 v[64:67], v[140:143], v[60:63], v[64:67]\n" \
  "ds_read_b128 v[56:59], v1 offset:43008\n" \
  "v_mfma_f32_16x16x32_bf16 v[68:71], v[172:175], v[60:63], v[68:71]\n" \
  "v_mfma_f32_16x16x32_bf16 v[72:75], v[204:207], v[60:63], v[72:75]\n" \
  "s_cmp_lg_u32 s67, 0\n" \
  "s_cbranch_scc1 .LWB_NOG_%=\n" \
  "s_waitcnt vmcnt(0)\n" \
  "v_cmp_eq_u32 vcc, 0, v5\n" \
  "s_cmp_eq_u64 vcc, 0\n" \
  "s_cbranch_scc0 .LWB_NOG_%=\n" \
  "s_mov_b32 m0, s61\n" \
  "s_lshl_b32 s68, s58, 19\n" \
  "s_add_u32 s68, s68, s48\n" \
  "s_addc_u32 s69, s49, 0\n" \
  "global_load_lds_dwordx4 v0, s[68:69] offset:0 sc1\n" \
  "global_load_lds_dwordx4 v0, s[68:69] offset:1024 sc1\n" \
  "global_load_lds_dwordx4 v0, s[68:69] offset:2048 sc1\n" \
  "global_load_lds_dwordx4 v0, s[68:69] offset:3072 sc1\n" \
  "s_add_u32 m0, s61, 4096\n" \
  "s_add_u32 s70, s68, 4096\n" \
  "s_addc_u32 s71, s69, 0\n" \
  "global_load_lds_dwordx4 v0, s[70:71] offset:0 sc1\n" \
  "global_load_lds_dwordx4 v0, s[70:71] offset:1024 sc1\n" \
  "global_load_lds_dwordx4 v0, s[70:71] offset:2048 sc1\n" \
  "global_load_lds_dwordx4 v0, s[70:71] offset:3072 sc1\n" \
  "s_mov_b32 m0, s62\n" \
  "s_lshl_b32 s72, s58, 18\n" \
  "s_add_u32 s72, s72, s50\n" \
  "s_addc_u32 s73, s51, 0\n" \
  "global_load_lds_dwordx4 v0, s[72:73] offset:0 sc1\n" \
  "global_load_lds_dwordx4 v0, s[72:73] offset:1024 sc1\n" \
  "global_load_lds_dwordx4 v0, s[72:73] offset:2048 sc1\n" \
  "global_load_lds_dwordx4 v0, s[72:73] offset:3072 sc1\n" \
  "s_mov_b32 s67, 1\n" \
  "s_mov_b32 s77, 1\n" \
  ".LWB_NOG_%=:\n" \
  "v_mfma_f32_16x16x32_bf16 v[76:79], v[236:239], v[60:63], v[76:79]\n" \
  "s_waitcnt lgkmcnt(2)\n" \
  "v_mfma_f32_16x16x32_bf16 v[64:67], v[144:147], v[48:51], v[64:67]\n" \
  "ds_read_b128 v[60:63], v1 offset:44032\n" \
  "v_mfma_f32_16x16x32_bf16 v[68:71], v[176:179], v[48:51], v[68:71]\n" \
  "v_mfma_f32_16x16x32_bf16 v[72:75], v[208:211], v[48:51], v[72:75]\n" \
  "v_mfma_f32_16x16x32_bf16 v[76:79], v[240:243], v[48:51], v[76:79]\n" \
  "s_waitcnt lgkmcnt(2)\n" \
  "v_mfma_f32_16x16x32_bf16 v[64:67], v[148:151], v[52:55], v[64:67]\n" \
  "ds_read_b128 v[48:51], v1 offset:36864\n" \
  "v_mfma_f32_16x16x32_bf16 v[68:71], v[180:183], v[52:55], v[68:71]\n" \
  "v_mfma_f32_16x16x32_bf16 v[72:75], v[212:215], v[52:55], v[72:75]\n" \
  "v_mfma_f32_16x16x32_bf16 v[76:79], v[244:247], v[52:55], v[76:79]\n" \
  "s_waitcnt lgkmcnt(2)\n" \
  "v_mfma_f32_16x16x32_bf16 v[64:67], v[152:155], v[56:59], v[64:67]\n" \
  "ds_read_b128 v[52:55], v1 offset:37888\n" \
  "v_mfma_f32_16x16x32_bf16 v[68:71], v[184:187], v[56:59], v[68:71]\n" \
  "v_mfma_f32_16x16x32_bf16 v[72:75], v[216:219], v[56:59], v[72:75]\n" \
  "v_mfma_f32_16x16x32_bf16 v[76:79], v[248:251], v[56:59], v[76:79]\n" \
  "s_waitcnt lgkmcnt(2)\n" \
  "v_mfma_f32_16x16x32_bf16 v[64:67], v[156:159], v[60:63], v[64:67]\n" \
  "ds_read_b128 v[56:59], v1 offset:38912\n" \
  "v_mfma_f32_16x16x32_bf16 v[68:71], v[188:191], v[60:63], v[68:71]\n" \
  "v_mfma_f32_16x16x32_bf16 v[72:75], v[220:223], v[60:63], v[72:75]\n" \
  "v_mfma_f32_16x16x32_bf16 v[76:79], v[252:255], v[60:63], v[76:79]\n" \
  "s_waitcnt lgkmcnt(2)\n" \
  "v_mfma_f32_16x16x32_bf16 v[80:83], v[128:131], v[48:51], 0\n" \
  "ds_read_b128 v[60:63], v1 offset:39936\n" \
  "v_mfma_f32_16x16x32_bf16 v[84:87], v[160:163], v[48:51], 0\n" \
  "v_mfma_f32_16x16x32_bf16 v[88:91], v[192:195], v[48:51], 0\n" \
  "s_waitcnt vmcnt(0)\n" \
  "v_mov_b32 v12, s65\n" \
  "v_mov_b32 v13, s77\n" \
  "s_mov_b64 exec, 1\n" \
  "ds_add_u32 v12, v13\n" \
  "s_mov_b64 exec, -1\n" \
  "v_mfma_f32_16x16x32_bf16 v[92:95], v[224:227], v[48:51], 0\n" \
  "s_lshl_b32 s70, s67, 7\n" \
  "s_or_b32 s70, s70, s58\n" \
  "s_lshr_b32 s68, s70, 6\n" \
  "s_add_u32 s68, s68, s70\n" \
  "s_lshl_b32 s68, s68, 12\n" \
  "buffer_load_dword v5, off, s[44:47], s68 sc1\n" \
  "s_waitcnt lgkmcnt(3)\n" \
  "v_mfma_f32_16x16x32_bf16 v[80:83], v[132:135], v[52:55], v[80:83]\n" \
  "ds_read_b128 v[48:51], v1 offset:45056\n" \
  "v_mfma_f32_16x16x32_bf16 v[84:87], v[164:167], v[52:55], v[84:87]\n" \
  "v_cvt_pk_bf16_f32 v64, v64, v65\n" \
  "v_mfma_f32_16x16x32_bf16 v[88:91], v[196:199], v[52:55], v[88:91]\n" \
  "v_cvt_pk_bf16_f32 v65, v66, v67\n" \
  "v_mfma_f32_16x16x32_bf16 v[92:95], v[228:231], v[52:55], v[92:95]\n" \
  "s_waitcnt lgkmcnt(3)\n" \
  "v_mfma_f32_16x16x32_bf16 v[80:83], v[136:139], v[56:59], v[80:83]\n" \
  "ds_read_b128 v[52:55], v1 offset:46080\n" \
  "v_mfma_f32_16x16x32_bf16 v[84:87], v[168:171], v[56:59], v[84:87]\n" \
  "v_cvt_pk_bf16_f32 v66, v68, v69\n" \
  "v_mfma_f32_16x16x32_bf16 v[88:91], v[200:203], v[56:59], v[88:91]\n" \
  "v_cvt_pk_bf16_f32 v67, v70, v71\n" \
  "v_mfma_f32_16x16x32_bf16 v[92:95], v[232:235], v[56:59], v[92:95]\n" \
  "s_waitcnt lgkmcnt(3)\n" \
  "v_mfma_f32_16x16x32_bf16 v[80:83], v[140:143], v[60:63], v[80:83]\n" \
  "ds_read_b128 v[56:59], v1 offset:47104\n" \
  "v_mfma_f32_16x16x32_bf16 v[84:87], v[172:175], v[60:63], v[84:87]\n" \
  "buffer_store_dwordx4 v[64:67], v2, s[40:43], s75 offen offset:0 sc1\n" \
  "v_mfma_f32_16x16x32_bf16 v[88:91], v[204:207], v[60:63], v[88:91]\n" \
  "v_cvt_pk_bf16_f32 v72, v72, v73\n" \
  "v_mfma_f32_16x16x32_bf16 v[92:95], v[236:239], v[60:63], v[92:95]\n" \
  "s_waitcnt lgkmcnt(2)\n" \
  "v_mfma_f32_16x16x32_bf16 v[80:83], v[144:147], v[48:51], v[80:83]\n" \
  "ds_read_b128 v[60:63], v1 offset:48128\n" \
  "v_mfma_f32_16x16x32_bf16 v[84:87], v[176:179], v[48:51], v[84:87]\n" \
  "v_cvt_pk_bf16_f32 v73, v74, v75\n" \
  "v_mfma_f32_16x16x32_bf16 v[88:91], v[208:211], v[48:51], v[88:91]\n" \
  "v_cvt_pk_bf16_f32 v74, v76, v77\n" \
  "v_mfma_f32_16x16x32_bf16 v[92:95], v[240:243], v[48:51], v[92:95]\n" \
  "s_waitcnt lgkmcnt(2)\n" \
  "v_mfma_f32_16x16x32_bf16 v[80:83], v[148:151], v[52:55], v[80:83]\n" \
  "v_mfma_f32_16x16x32_bf16 v[84:87], v[180:183], v[52:55], v[84:87]\n" \
  "v_cvt_pk_bf16_f32 v75, v78, v79\n" \
  "v_mfma_f32_16x16x32_bf16 v[88:91], v[212:215], v[52:55], v[88:91]\n" \
  "buffer_store_dwordx4 v[72:75], v2, s[40:43], s75 offen offset:128 sc1\n" \
  "v_mfma_f32_16x16x32_bf16 v[92:95], v[244:247], v[52:55], v[92:95]\n" \
  "s_waitcnt lgkmcnt(1)\n" \
  "v_mfma_f32_16x16x32_bf16 v[80:83], v[152:155], v[56:59], v[80:83]\n" \
  "v_mfma_f32_16x16x32_bf16 v[84:87], v[184:187], v[56:59], v[84:87]\n" \
  "v_mfma_f32_16x16x32_bf16 v[88:91], v[216:219], v[56:59], v[88:91]\n" \
  "v_mfma_f32_16x16x32_bf16 v[92:95], v[248:251], v[56:59], v[92:95]\n" \
  "s_waitcnt lgkmcnt(0)\n" \
  "v_mfma_f32_16x16x32_bf16 v[80:83], v[156:159], v[60:63], v[80:83]\n" \
  "v_mfma_f32_16x16x32_bf16 v[84:87], v[188:191], v[60:63], v[84:87]\n" \
  "v_mfma_f32_16x16x32_bf16 v[88:91], v[220:223], v[60:63], v[88:91]\n" \
  "v_mfma_f32_16x16x32_bf16 v[92:95], v[252:255], v[60:63], v[92:95]\n" \
  "s_nop 4\n" \
  "v_cvt_pk_bf16_f32 v80, v80, v81\n" \
  "v_cvt_pk_bf16_f32 v81, v82, v83\n" \
  "v_cvt_pk_bf16_f32 v82, v84, v85\n" \
  "v_cvt_pk_bf16_f32 v83, v86, v87\n" \
  "buffer_store_dwordx4 v[80:83], v2, s[40:43], s76 offen offset:0 sc1\n" \
  "v_cvt_pk_bf16_f32 v88, v88, v89\n" \
  "v_cvt_pk_bf16_f32 v89, v90, v91\n" \
  "v_cvt_pk_bf16_f32 v90, v92, v93\n" \
  "v_cvt_pk_bf16_f32 v91, v94, v95\n" \
  "buffer_store_dwordx4 v[88:91], v2, s[40:43], s76 offen offset:128 sc1\n" \
  "s_mov_b32 s79, s57\n" \
  "s_waitcnt vmcnt(4)\n" \
  "v_cmp_eq_u32 vcc, 0, v5\n" \
  "s_cmp_eq_u64 vcc, 0\n" \
  "s_cbranch_scc0 .LWB_NEXT_%=\n" \
  "s_mov_b32 s57, s58\n" \
  "s_setprio 0\n" \
  "s_branch .LWB_SEEN_%=\n"
#define CONV_ASM \
  "v_mov_b64 v[64:65], 0\n" \
  "v_mov_b64 v[66:67], 0\n" \
  "v_mov_b64 v[68:69], 0\n" \
  "v_mov_b64 v[70:71], 0\n" \
  "v_mov_b64 v[72:73], 0\n" \
  "v_mov_b64 v[74:75], 0\n" \
  "v_mov_b64 v[76:77], 0\n" \
  "v_mov_b64 v[78:79], 0\n" \
  "v_mov_b64 v[80:81], 0\n" \
  "v_mov_b64 v[82:83], 0\n" \
  "v_mov_b64 v[84:85], 0\n" \
  "v_mov_b64 v[86:87], 0\n" \
  "v_mov_b64 v[88:89], 0\n" \
  "v_mov_b64 v[90:91], 0\n" \
  "v_mov_b64 v[92:93], 0\n" \
  "v_mov_b64 v[94:95], 0\n" \
  "v_mov_b64 v[96:97], 0\n" \
  "v_mov_b64 v[98:99], 0\n" \
  "v_mov_b64 v[100:101], 0\n" \
  "v_mov_b64 v[102:103], 0\n" \
  "v_mov_b64 v[104:105], 0\n" \
  "v_mov_b64 v[106:107], 0\n" \
  "v_mov_b64 v[108:109], 0\n" \
  "v_mov_b64 v[110:111], 0\n" \
  "v_mov_b64 v[112:113], 0\n" \
  "v_mov_b64 v[114:115], 0\n" \
  "v_mov_b64 v[116:117], 0\n" \
  "v_mov_b64 v[118:119], 0\n" \
  "v_mov_b64 v[120:121], 0\n" \
  "v_mov_b64 v[122:123], 0\n" \
  "v_mov_b64 v[124:125], 0\n" \
  "v_mov_b64 v[126:127], 0\n" \
  "v_mov_b32 v20, 0\n" \
  "v_mov_b32 v41, 0\n" \
  "v_mov_b32 v29, 0\n" \
  "s_branch .LVA_TAPS_%=\n" \
  "s_branch .LVA_AL_%=\n" \
  ".p2alignl 12, 0xbf800000, 3700\n" \
  ".p2align 6\n" \
  ".Lpad0xb0:\n" \
  "s_setpc_b64 s[98:99]\n" \
  ".p2align 6\n" \
  ".LVA_AL_%=:\n" \
  ".LVA_TOP_%=:\n" \
  "s_cmp_lg_u32 s63, 1\n" \
  "s_cbranch_scc1 .LVA_BAR_%=\n" \
  "s_waitcnt vmcnt(0)\n" \
  "v_min3_u32 v56, v42, v43, v41\n" \
  "v_readfirstlane_b32 s78, v29\n" \
  "v_cmp_eq_u32 vcc, 0, v56\n" \
  "s_cbranch_vccz .LVA_SEEN_%=\n" \
  "s_mov_b32 s79, 0\n" \
  "s_cmp_lg_u32 s78, 0\n" \
  "s_cbranch_scc1 .LVA_KNOWN_%=\n" \
  ".LVA_GATE_%=:\n" \
  "s_memrealtime s[74:75]\n" \
  "s_waitcnt lgkmcnt(0)\n" \
  "s_sub_i32 s74, s69, s74\n" \
  "s_cmp_gt_u32 s74, %[gv]\n" \
  "s_cbranch_scc1 .LVA_WHEN_%=\n" \
  "s_sleep 2\n" \
  "s_branch .LVA_GATE_%=\n" \
  ".LVA_NONE_%=:\n" \
  "s_sleep %[sv]\n" \
  ".LVA_WHEN_%=:\n" \
  "buffer_load_dword v29, off, s[44:47], s71 sc1\n" \
  "s_add_u32 s79, s79, 1\n" \
  "s_cmp_gt_u32 s79, 0x40000\n" \
  "s_cbranch_scc1 .LVA_KNOWN_%=\n" \
  "s_waitcnt vmcnt(0)\n" \
  "v_readfirstlane_b32 s78, v29\n" \
  "s_cmp_eq_u32 s78, 0\n" \
  "s_cbranch_scc1 .LVA_NONE_%=\n" \
  ".LVA_KNOWN_%=:\n" \
  "s_memrealtime s[74:75]\n" \
  "s_waitcnt lgkmcnt(0)\n" \
  "s_sub_i32 s74, s78, s74\n" \
  "s_cmp_lt_i32 s74, %[lt]\n" \
  "s_cselect_b32 s70, 0, 0x200000\n" \
  "s_branch .LVA_DUE_%=\n" \
  ".LVA_SOON_%=:\n" \
  "s_cmp_lt_u32 s74, 8\n" \
  "s_cbranch_scc1 .LVA_NOW_%=\n" \
  "s_sleep 1\n" \
  ".LVA_NOW_%=:\n" \
  "s_memrealtime s[74:75]\n" \
  "s_waitcnt lgkmcnt(0)\n" \
  "s_sub_i32 s74, s78, s74\n" \
  ".LVA_DUE_%=:\n" \
  "s_cmp_lt_u32 s74, 1000\n" \
  "s_cbranch_scc1 .LVA_SOON_%=\n" \
  "s_branch .LVA_ASK_%=\n" \
  ".LVA_AGAIN_%=:\n" \
  "s_sleep %[sv]\n" \
  ".LVA_ASK_%=:\n" \
  "buffer_load_dwordx2 v[42:43], v37, s[40:43], s65 offen sc1\n" \
  "buffer_load_dword v41, v38, s[44:47], s66 offen sc1\n" \
  "s_add_u32 s79, s79, 1\n" \
  "s_cmp_gt_u32 s79, 0x40000\n" \
  "s_cbranch_scc1 .LVA_SEEN_%=\n" \
  "s_waitcnt vmcnt(0)\n" \
  "v_min3_u32 v56, v42, v43, v41\n" \
  "v_cmp_eq_u32 vcc, 0, v56\n" \
  "s_cbranch_vccnz .LVA_AGAIN_%=\n" \
  ".LVA_SEEN_%=:\n" \
  "v_and_b32 v62, 0xffff0000, v41\n" \
  "v_lshlrev_b32 v60, 16, v41\n" \
  "v_lshlrev_b32 v56, 16, v42\n" \
  "v_and_b32 v57, 0xffff0000, v42\n" \
  "v_lshlrev_b32 v58, 16, v43\n" \
  "v_and_b32 v59, 0xffff0000, v43\n" \
  "v_rcp_f32 v62, v62\n" \
  "v_pk_add_f32 v[56:57], v[56:57], v[60:61] op_sel_hi:[1,0] neg_lo:[0,1] neg_hi:[0,1]\n" \
  "v_pk_add_f32 v[58:59], v[58:59], v[60:61] op_sel_hi:[1,0] neg_lo:[0,1] neg_hi:[0,1]\n" \
  "v_cvt_pk_bf16_f32 v57, v56, v57\n" \
  "v_cvt_pk_bf16_f32 v59, v58, v59\n" \
  "v_lshlrev_b32 v56, 16, v57\n" \
  "v_and_b32 v57, 0xffff0000, v57\n" \
  "v_lshlrev_b32 v58, 16, v59\n" \
  "v_and_b32 v59, 0xffff0000, v59\n" \
  "v_pk_mul_f32 v[56:57], v[56:57], v[62:63] op_sel_hi:[1,0]\n" \
  "v_pk_mul_f32 v[58:59], v[58:59], v[62:63] op_sel_hi:[1,0]\n" \
  "v_cvt_pk_bf16_f32 v56, v56, v57\n" \
  "v_cvt_pk_bf16_f32 v58, v58, v59\n" \
  "ds_write_b16 v39, v56\n" \
  "ds_write_b16_d16_hi v39, v56 offset:4288\n" \
  "ds_write_b16 v39, v58 offset:8576\n" \
  "ds_write_b16_d16_hi v39, v58 offset:12864\n" \
  "ds_write_b64 v40, v[42:43] offset:512\n" \
  ".LVA_BAR_%=:\n" \
  "s_waitcnt lgkmcnt(0)\n" \
  "s_barrier\n" \
  "ds_read_b128 v[56:59], v0 offset:0\n" \
  "ds_read_u16 v60, v2 offset:0\n" \
  "ds_read_u16 v61, v3 offset:512\n" \
  "ds_read2_b32 v[244:245], v4 offset0:0 offset1:1\n" \
  "ds_read2_b32 v[246:247], v4 offset0:2 offset1:3\n" \
  "ds_read_b32 v248, v4 offset:16\n" \
  "ds_read2_b32 v[250:251], v4 offset0:16 offset1:17\n" \
  "ds_read2_b32 v[252:253], v4 offset0:18 offset1:19\n" \
  "ds_read_b32 v254, v4 offset:80\n" \
  "s_setprio 3\n" \
  "s_waitcnt lgkmcnt(8)\n" \
  "v_mfma_f32_16x16x32_bf16 v[20:23], v[56:59], v[12:15], v[20:23]\n" \
  "v_mfma_f32_16x16x32_bf16 v[20:23], v[56:59], v[16:19], v[20:23]\n" \
  "s_waitcnt lgkmcnt(6)\n" \
  "v_lshlrev_b32 v60, 16, v60\n" \
  "v_lshlrev_b32 v61, 16, v61\n" \
  "v_mul_f32 v60, s64, v60\n" \
  "v_add_u32 v0, v1, v0\n" \
  "v_add_u32 v2, 64, v2\n" \
  "v_add_u32 v4, 64, v4\n" \
  "s_nop 0\n" \
  "v_cvt_pk_bf16_f32 v62, v20, v60\n" \
  "v_lshlrev_b32 v63, 16, v62\n" \
  "v_and_b32 v62, 0xffff0000, v62\n" \
  "v_add_f32 v62, v62, v63\n" \
  "v_cvt_pk_bf16_f32 v62, v62, v62\n" \
  "v_lshlrev_b32 v62, 16, v62\n" \
  "v_add_f32 v62, v62, v61\n" \
  "v_cvt_pk_bf16_f32 v62, v62, v62\n" \
  "s_mov_b64 exec, s[72:73]\n" \
  "ds_write_b16 v3, v62\n" \
  "s_mov_b64 exec, -1\n" \
  "s_cmp_gt_u32 s63, 1\n" \
  "s_waitcnt lgkmcnt(0)\n" \
  "s_barrier\n" \
  "s_cbranch_scc1 .LVA_REST_%=\n" \
  "s_cmp_eq_u32 s63, 1\n" \
  "s_cbranch_scc1 .LVA_ONE_%=\n" \
  "ds_read_b128 v[56:59], v40 offset:0\n" \
  "s_mov_b32 exec_hi, 0\n" \
  "s_waitcnt lgkmcnt(0)\n" \
  "buffer_store_dwordx4 v[56:59], v7, s[48:51], s67 offen sc1\n" \
  "s_mov_b32 exec_hi, -1\n" \
  "s_add_u32 s67, s67, 0x8000\n" \
  "s_branch .LVA_GO_%=\n" \
  ".LVA_ONE_%=:\n" \
  "s_add_u32 s65, s65, 0x8000\n" \
  "s_add_u32 s66, s66, 2048\n" \
  "s_add_u32 s71, s71, 4\n" \
  "s_max_u32 s76, s65, s70\n" \
  "s_max_u32 s78, s66, s70\n" \
  "buffer_load_dword v29, off, s[44:47], s71 sc1\n" \
  "buffer_load_dwordx2 v[42:43], v37, s[40:43], s76 offen sc1\n" \
  "buffer_load_dword v41, v38, s[44:47], s78 offen sc1\n" \
  "s_memrealtime s[74:75]\n" \
  "v_add_u32 v39, 64, v39\n" \
  "s_waitcnt lgkmcnt(0)\n" \
  "s_add_u32 s69, s74, %[gv]\n" \
  "s_branch .LVA_GO_%=\n" \
  ".LVA_REST_%=:\n" \
  "s_setprio 0\n" \
  ".LVA_GO_%=:\n" \
  "ds_read2_b32 v[58:59], v28 offset0:0 offset1:1\n" \
  "ds_read2_b32 v[60:61], v28 offset0:2 offset1:3\n" \
  "ds_read_b32 v62, v28 offset:16\n" \
  "v_alignbit_b32 v244, v245, v244, v6\n" \
  "v_alignbit_b32 v245, v246, v245, v6\n" \
  "v_alignbit_b32 v246, v247, v246, v6\n" \
  "v_alignbit_b32 v247, v248, v247, v6\n" \
  "v_alignbit_b32 v250, v251, v250, v6\n" \
  "v_alignbit_b32 v251, v252, v251, v6\n" \
  "v_alignbit_b32 v252, v253, v252, v6\n" \
  "v_alignbit_b32 v253, v254, v253, v6\n" \
  "v_and_b32 v250, v250, v8\n" \
  "v_and_b32 v251, v251, v9\n" \
  "v_and_b32 v252, v252, v10\n" \
  "v_and_b32 v253, v253, v11\n" \
  "s_bitcmp1_b32 s62, 1\n" \
  "s_cbranch_scc1 .LVA_QH_%=\n" \
  "s_bitcmp1_b32 s62, 0\n" \
  "s_cbranch_scc1 .LVA_QB_%=\n" \
  ".LVA_QA_%=:\n" \
  "v_mfma_f32_16x16x32_bf16 v[64:67], v[128:131], v[244:247], v[68:71]\n" \
  "v_mfma_f32_16x16x32_bf16 v[68:71], v[132:135], v[244:247], v[72:75]\n" \
  "v_mfma_f32_16x16x32_bf16 v[72:75], v[136:139], v[244:247], v[76:79]\n" \
  "v_mfma_f32_16x16x32_bf16 v[76:79], v[140:143], v[244:247], v[80:83]\n" \
  "v_mfma_f32_16x16x32_bf16 v[20:23], v[192:195], v[250:253], v[64:67]\n" \
  "s_nop 7\n" \
  "v_permlane32_swap_b32 v22, v23\n" \
  "v_add_f32 v20, v22, v23\n" \
  "s_cmp_lg_u32 s62, 0\n" \
  "s_cbranch_scc1 .LVA_MOVE_%=\n" \
  "s_branch .LVA_FAR_%=\n" \
  ".Lpad0xb1:\n" \
  "s_setpc_b64 s[98:99]\n" \
  ".LVA_QB_%=:\n" \
  "v_mfma_f32_16x16x32_bf16 v[64:67], v[128:131], v[244:247], v[64:67]\n" \
  "v_mfma_f32_16x16x32_bf16 v[68:71], v[132:135], v[244:247], v[68:71]\n" \
  "v_mfma_f32_16x16x32_bf16 v[72:75], v[136:139], v[244:247], v[72:75]\n" \
  "v_mfma_f32_16x16x32_bf16 v[76:79], v[140:143], v[244:247], v[76:79]\n" \
  "v_mfma_f32_16x16x32_bf16 v[20:23], v[192:195], v[250:253], v[64:67]\n" \
  "s_not_b64 s[72:73], s[72:73]\n" \
  "s_nop 6\n" \
  "v_permlane32_swap_b32 v20, v21\n" \
  "v_add_f32 v20, v20, v21\n" \
  "s_branch .LVA_FAR_%=\n" \
  ".LVA_QH_%=:\n" \
  "s_bitcmp1_b32 s62, 0\n" \
  "s_cbranch_scc1 .LVA_QD_%=\n" \
  ".LVA_QC_%=:\n" \
  "v_mfma_f32_16x16x32_bf16 v[64:67], v[128:131], v[244:247], v[64:67]\n" \
  "v_mfma_f32_16x16x32_bf16 v[68:71], v[132:135], v[244:247], v[68:71]\n" \
  "v_mfma_f32_16x16x32_bf16 v[72:75], v[136:139], v[244:247], v[72:75]\n" \
  "v_mfma_f32_16x16x32_bf16 v[76:79], v[140:143], v[244:247], v[76:79]\n" \
  "v_mfma_f32_16x16x32_bf16 v[20:23], v[192:195], v[250:253], v[64:67]\n" \
  "s_nop 7\n" \
  "v_permlane32_swap_b32 v22, v23\n" \
  "v_add_f32 v20, v22, v23\n" \
  "s_branch .LVA_FAR_%=\n" \
  ".LVA_QD_%=:\n" \
  "v_mfma_f32_16x16x32_bf16 v[64:67], v[128:131], v[244:247], v[64:67]\n" \
  "v_mfma_f32_16x16x32_bf16 v[68:71], v[132:135], v[244:247], v[68:71]\n" \
  "v_mfma_f32_16x16x32_bf16 v[72:75], v[136:139], v[244:247], v[72:75]\n" \
  "v_mfma_f32_16x16x32_bf16 v[76:79], v[140:143], v[244:247], v[76:79]\n" \
  "v_mfma_f32_16x16x32_bf16 v[20:23], v[192:195], v[250:253], v[68:71]\n" \
  "s_not_b64 s[72:73], s[72:73]\n" \
  "s_nop 6\n" \
  "v_permlane32_swap_b32 v20, v21\n" \
  "v_add_f32 v20, v20, v21\n" \
  ".LVA_FAR_%=:\n" \
  "s_waitcnt lgkmcnt(0)\n" \
  "v_alignbit_b32 v58, v59, v58, v6\n" \
  "v_alignbit_b32 v59, v60, v59, v6\n" \
  "v_alignbit_b32 v60, v61, v60, v6\n" \
  "v_alignbit_b32 v61, v62, v61, v6\n" \
  "v_add_u32 v28, 64, v28\n" \
  "s_cmp_ge_u32 s80, 12\n" \
  "s_cbranch_scc1 .LVA_PD_%=\n" \
  "v_mfma_f32_16x16x32_bf16 v[80:83], v[144:147], v[244:247], v[80:83]\n" \
  "v_mfma_f32_16x16x32_bf16 v[196:199], v[128:131], v[58:61], v[196:199]\n" \
  "s_cmp_ge_u32 s80, 11\n" \
  "s_cbranch_scc1 .LVA_PD_%=\n" \
  "v_mfma_f32_16x16x32_bf16 v[84:87], v[148:151], v[244:247], v[84:87]\n" \
  "v_mfma_f32_16x16x32_bf16 v[200:203], v[132:135], v[58:61], v[200:203]\n" \
  "s_cmp_ge_u32 s80, 10\n" \
  "s_cbranch_scc1 .LVA_PD_%=\n" \
  "v_mfma_f32_16x16x32_bf16 v[88:91], v[152:155], v[244:247], v[88:91]\n" \
  "v_mfma_f32_16x16x32_bf16 v[204:207], v[136:139], v[58:61], v[204:207]\n" \
  "s_cmp_ge_u32 s80, 9\n" \
  "s_cbranch_scc1 .LVA_PD_%=\n" \
  "v_mfma_f32_16x16x32_bf16 v[92:95], v[156:159], v[244:247], v[92:95]\n" \
  "v_mfma_f32_16x16x32_bf16 v[208:211], v[140:143], v[58:61], v[208:211]\n" \
  "s_cmp_ge_u32 s80, 8\n" \
  "s_cbranch_scc1 .LVA_PD_%=\n" \
  "v_mfma_f32_16x16x32_bf16 v[96:99], v[160:163], v[244:247], v[96:99]\n" \
  "v_mfma_f32_16x16x32_bf16 v[212:215], v[144:147], v[58:61], v[212:215]\n" \
  "s_cmp_ge_u32 s80, 7\n" \
  "s_cbranch_scc1 .LVA_PD_%=\n" \
  "v_mfma_f32_16x16x32_bf16 v[100:103], v[164:167], v[244:247], v[100:103]\n" \
  "v_mfma_f32_16x16x32_bf16 v[216:219], v[148:151], v[58:61], v[216:219]\n" \
  "s_cmp_ge_u32 s80, 6\n" \
  "s_cbranch_scc1 .LVA_PD_%=\n" \
  "v_mfma_f32_16x16x32_bf16 v[104:107], v[168:171], v[244:247], v[104:107]\n" \
  "v_mfma_f32_16x16x32_bf16 v[220:223], v[152:155], v[58:61], v[220:223]\n" \
  "s_cmp_ge_u32 s80, 5\n" \
  "s_cbranch_scc1 .LVA_PD_%=\n" \
  "v_mfma_f32_16x16x32_bf16 v[108:111], v[172:175], v[244:247], v[108:111]\n" \
  "v_mfma_f32_16x16x32_bf16 v[224:227], v[156:159], v[58:61], v[224:227]\n" \
  "s_cmp_ge_u32 s80, 4\n" \
  "s_cbranch_scc1 .LVA_PD_%=\n" \
  "v_mfma_f32_16x16x32_bf16 v[112:115], v[176:179], v[244:247], v[112:115]\n" \
  "v_mfma_f32_16x16x32_bf16 v[228:231], v[160:163], v[58:61], v[228:231]\n" \
  "s_cmp_ge_u32 s80, 3\n" \
  "s_cbranch_scc1 .LVA_PD_%=\n" \
  "v_mfma_f32_16x16x32_bf16 v[116:119], v[180:183], v[244:247], v[116:119]\n" \
  "v_mfma_f32_16x16x32_bf16 v[232:235], v[164:167], v[58:61], v[232:235]\n" \
  "s_cmp_ge_u32 s80, 2\n" \
  "s_cbranch_scc1 .LVA_PD_%=\n" \
  "v_mfma_f32_16x16x32_bf16 v[120:123], v[184:187], v[244:247], v[120:123]\n" \
  "v_mfma_f32_16x16x32_bf16 v[236:239], v[168:171], v[58:61], v[236:239]\n" \
  "s_cmp_ge_u32 s80, 1\n" \
  "s_cbranch_scc1 .LVA_PD_%=\n" \
  "v_mfma_f32_16x16x32_bf16 v[124:127], v[188:191], v[244:247], v[124:127]\n" \
  "v_mfma_f32_16x16x32_bf16 v[240:243], v[172:175], v[58:61], v[240:243]\n" \
  ".LVA_PD_%=:\n" \
  "s_cmp_lg_u32 s63, 0\n" \
  "s_cbranch_scc1 .LVA_NOF_%=\n" \
  "v_lshlrev_b32 v56, 6, v7\n" \
  "s_waitcnt vmcnt(0)\n" \
  "s_mov_b64 exec, 15\n" \
  "global_store_dword v56, v5, s[58:59] sc1\n" \
  "s_mov_b64 exec, -1\n" \
  "s_add_u32 s58, s58, 4096\n" \
  "s_addc_u32 s59, s59, 0\n" \
  ".LVA_NOF_%=:\n" \
  "s_add_u32 s74, s62, 12\n" \
  "s_lshl_b32 s76, s63, 1\n" \
  "s_and_b32 s76, s76, 12\n" \
  "s_xor_b32 s74, s74, s76\n" \
  "s_and_b32 s79, s74, 15\n" \
  "s_sub_u32 s74, s62, 4\n" \
  "s_cmp_lt_u32 s74, 48\n" \
  "s_cselect_b32 s79, s79, 1\n" \
  "s_cmp_lg_u32 s79, 0\n" \
  "s_cbranch_scc1 .LVA_NOS_%=\n" \
  "ds_read_u16 v128, v35 offset:0\n" \
  "ds_read_u16 v129, v35 offset:784\n" \
  "ds_read_u16 v130, v35 offset:1568\n" \
  "ds_read_u16 v131, v35 offset:2352\n" \
  "ds_read_u16 v132, v35 offset:3136\n" \
  "ds_read_u16 v133, v35 offset:3920\n" \
  "ds_read_u16 v134, v35 offset:4704\n" \
  "ds_read_u16 v135, v35 offset:5488\n" \
  "v_lshlrev_b32 v136, 16, v44\n" \
  "v_and_b32 v137, 0xffff0000, v44\n" \
  "v_lshlrev_b32 v138, 16, v45\n" \
  "v_and_b32 v139, 0xffff0000, v45\n" \
  "v_lshlrev_b32 v140, 16, v46\n" \
  "v_and_b32 v141, 0xffff0000, v46\n" \
  "v_lshlrev_b32 v142, 16, v47\n" \
  "v_and_b32 v143, 0xffff0000, v47\n" \
  "v_mov_b64 v[44:45], v[48:49]\n" \
  "v_mov_b64 v[46:47], v[50:51]\n" \
  "v_mov_b64 v[48:49], v[52:53]\n" \
  "v_mov_b64 v[50:51], v[54:55]\n" \
  "s_waitcnt lgkmcnt(0)\n" \
  "v_lshlrev_b32 v128, 16, v128\n" \
  "v_lshlrev_b32 v129, 16, v129\n" \
  "v_lshlrev_b32 v130, 16, v130\n" \
  "v_lshlrev_b32 v131, 16, v131\n" \
  "v_lshlrev_b32 v132, 16, v132\n" \
  "v_lshlrev_b32 v133, 16, v133\n" \
  "v_lshlrev_b32 v134, 16, v134\n" \
  "v_lshlrev_b32 v135, 16, v135\n" \
  "v_pk_add_f32 v[128:129], v[128:129], v[136:137]\n" \
  "v_pk_add_f32 v[130:131], v[130:131], v[138:139]\n" \
  "v_pk_add_f32 v[132:133], v[132:133], v[140:141]\n" \
  "v_pk_add_f32 v[134:135], v[134:135], v[142:143]\n" \
  "v_cvt_pk_bf16_f32 v56, v128, v129\n" \
  "v_cvt_pk_bf16_f32 v57, v130, v131\n" \
  "v_cvt_pk_bf16_f32 v58, v132, v133\n" \
  "v_cvt_pk_bf16_f32 v59, v134, v135\n" \
  "buffer_store_dwordx4 v[56:59], v36, s[52:55], s68 offen sc1\n" \
  "s_add_u32 s68, s68, 0x80000\n" \
  "s_mov_b32 s0, 0xbe801d62\n" \
  ".set .Lpad0xb2, . - 4\n" \
  ".LVA_NOS_%=:\n" \
  "s_and_b32 s74, s62, 3\n" \
  "s_cmp_lt_u32 s62, 48\n" \
  "s_cselect_b32 s74, s74, 0\n" \
  "s_cmp_lg_u32 s74, 3\n" \
  "s_cbranch_scc1 .LVA_FIN_%=\n" \
  "ds_read2_b32 v[58:59], v28 offset0:0 offset1:1\n" \
  "ds_read2_b32 v[60:61], v28 offset0:2 offset1:3\n" \
  "ds_read_b32 v62, v28 offset:16\n" \
  "ds_read_u16 v56, v33 offset:0\n" \
  "ds_read_u16 v57, v33 offset:64\n" \
  "v_add_u32 v33, 256, v33\n" \
  "s_mov_b32 s0, 0xbe801d62\n" \
  ".set .Lpad0xb3, . - 4\n" \
  "s_waitcnt lgkmcnt(2)\n" \
  "v_alignbit_b32 v58, v59, v58, v6\n" \
  "v_alignbit_b32 v59, v60, v59, v6\n" \
  "v_alignbit_b32 v60, v61, v60, v6\n" \
  "v_alignbit_b32 v61, v62, v61, v6\n" \
  "s_nop 1\n" \
  "v_mfma_f32_16x16x32_bf16 v[196:199], v[24:27], v[58:61], v[196:199]\n" \
  "s_nop 7\n" \
  "v_permlane32_swap_b32 v196, v197\n" \
  "v_permlane32_swap_b32 v198, v199\n" \
  "v_add_f32 v196, v196, v197\n" \
  "v_add_f32 v198, v198, v199\n" \
  "s_waitcnt lgkmcnt(0)\n" \
  "v_lshlrev_b32 v56, 16, v56\n" \
  "v_lshlrev_b32 v57, 16, v57\n" \
  "v_mul_f32 v56, s64, v56\n" \
  "v_mul_f32 v57, s64, v57\n" \
  "v_cvt_pk_bf16_f32 v197, v196, v198\n" \
  "v_cvt_pk_bf16_f32 v57, v56, v57\n" \
  "v_lshlrev_b32 v196, 16, v197\n" \
  "v_and_b32 v197, 0xffff0000, v197\n" \
  "v_lshlrev_b32 v56, 16, v57\n" \
  "v_and_b32 v57, 0xffff0000, v57\n" \
  "v_add_f32 v56, v56, v196\n" \
  "v_add_f32 v57, v57, v197\n" \
  "v_cvt_pk_bf16_f32 v58, v56, v57\n" \
  "ds_write_b16 v34, v58\n" \
  "ds_write_b16_d16_hi v34, v58 offset:64\n" \
  ".LVA_FIN_%=:\n" \
  "s_cmp_lg_u32 s79, 0\n" \
  "s_cbranch_scc1 .LVA_NOG_%=\n" \
  "v_lshlrev_b32 v60, 6, v7\n" \
  "s_waitcnt vmcnt(0)\n" \
  "s_mov_b64 exec, 255\n" \
  "global_store_dword v60, v5, s[60:61] sc1\n" \
  "s_mov_b64 exec, -1\n" \
  "s_add_u32 s60, s60, 0x10000\n" \
  "s_addc_u32 s61, s61, 0\n" \
  ".LVA_NOG_%=:\n" \
  "s_add_u32 s62, s62, 1\n" \
  "s_cmp_eq_u32 s62, 64\n" \
  "s_cbranch_scc1 .LVA_END_%=\n" \
  ".LVA_TAPS_%=:\n" \
  "s_and_b32 s74, s62, 3\n" \
  "s_lshl_b32 s76, s74, 6\n" \
  "v_add_u32 v56, s76, v5\n" \
  "s_cmp_eq_u32 s74, 3\n" \
  "s_cselect_b32 s74, 0, 256\n" \
  "v_add_u32 v57, s74, v56\n" \
  "ds_read_b128 v[128:131], v56 offset:3840\n" \
  "ds_read_b128 v[132:135], v56 offset:3584\n" \
  "ds_read_b128 v[136:139], v56 offset:3328\n" \
  "ds_read_b128 v[140:143], v56 offset:3072\n" \
  "ds_read_b128 v[192:195], v57 offset:3648\n" \
  "s_lshr_b32 s80, s62, 2\n" \
  "s_cmp_ge_u32 s80, 12\n" \
  "s_cbranch_scc1 .LVA_TOP_%=\n" \
  "ds_read_b128 v[144:147], v56 offset:2816\n" \
  "s_cmp_ge_u32 s80, 11\n" \
  "s_cbranch_scc1 .LVA_TOP_%=\n" \
  "ds_read_b128 v[148:151], v56 offset:2560\n" \
  "s_cmp_ge_u32 s80, 10\n" \
  "s_cbranch_scc1 .LVA_TOP_%=\n" \
  "ds_read_b128 v[152:155], v56 offset:2304\n" \
  "s_cmp_ge_u32 s80, 9\n" \
  "s_cbranch_scc1 .LVA_TOP_%=\n" \
  "ds_read_b128 v[156:159], v56 offset:2048\n" \
  "s_cmp_ge_u32 s80, 8\n" \
  "s_cbranch_scc1 .LVA_TOP_%=\n" \
  "ds_read_b128 v[160:163], v56 offset:1792\n" \
  "s_cmp_ge_u32 s80, 7\n" \
  "s_cbranch_scc1 .LVA_TOP_%=\n" \
  "ds_read_b128 v[164:167], v56 offset:1536\n" \
  "s_cmp_ge_u32 s80, 6\n" \
  "s_cbranch_scc1 .LVA_TOP_%=\n" \
  "ds_read_b128 v[168:171], v56 offset:1280\n" \
  "s_cmp_ge_u32 s80, 5\n" \
  "s_cbranch_scc1 .LVA_TOP_%=\n" \
  "ds_read_b128 v[172:175], v56 offset:1024\n" \
  "s_cmp_ge_u32 s80, 4\n" \
  "s_cbranch_scc1 .LVA_TOP_%=\n" \
  "ds_read_b128 v[176:179], v56 offset:768\n" \
  "s_cmp_ge_u32 s80, 3\n" \
  "s_cbranch_scc1 .LVA_TOP_%=\n" \
  "ds_read_b128 v[180:183], v56 offset:512\n" \
  "s_cmp_ge_u32 s80, 2\n" \
  "s_cbranch_scc1 .LVA_TOP_%=\n" \
  "ds_read_b128 v[184:187], v56 offset:256\n" \
  "s_cmp_ge_u32 s80, 1\n" \
  "s_cbranch_scc1 .LVA_TOP_%=\n" \
  "ds_read_b128 v[188:191], v56 offset:0\n" \
  "s_branch .LVA_TOP_%=\n" \
  ".LVA_MOVE_%=:\n" \
  "s_waitcnt lgkmcnt(0)\n" \
  "v_alignbit_b32 v58, v59, v58, v6\n" \
  "v_alignbit_b32 v59, v60, v59, v6\n" \
  "v_alignbit_b32 v60, v61, v60, v6\n" \
  "v_alignbit_b32 v61, v62, v61, v6\n" \
  "v_add_u32 v28, 64, v28\n" \
  "s_cmp_ge_u32 s80, 12\n" \
  "s_cbranch_scc1 .LVA_PD_%=\n" \
  "v_mfma_f32_16x16x32_bf16 v[80:83], v[144:147], v[244:247], v[84:87]\n" \
  "v_mfma_f32_16x16x32_bf16 v[196:199], v[128:131], v[58:61], v[200:203]\n" \
  "s_cmp_ge_u32 s80, 11\n" \
  "s_cbranch_scc1 .LVA_PD_%=\n" \
  "v_mfma_f32_16x16x32_bf16 v[84:87], v[148:151], v[244:247], v[88:91]\n" \
  "v_mfma_f32_16x16x32_bf16 v[200:203], v[132:135], v[58:61], v[204:207]\n" \
  "s_cmp_ge_u32 s80, 10\n" \
  "s_cbranch_scc1 .LVA_PD_%=\n" \
  "v_mfma_f32_16x16x32_bf16 v[88:91], v[152:155], v[244:247], v[92:95]\n" \
  "v_mfma_f32_16x16x32_bf16 v[204:207], v[136:139], v[58:61], v[208:211]\n" \
  "s_cmp_ge_u32 s80, 9\n" \
  "s_cbranch_scc1 .LVA_PD_%=\n" \
  "v_mfma_f32_16x16x32_bf16 v[92:95], v[156:159], v[244:247], v[96:99]\n" \
  "v_mfma_f32_16x16x32_bf16 v[208:211], v[140:143], v[58:61], v[212:215]\n" \
  "s_cmp_ge_u32 s80, 8\n" \
  "s_cbranch_scc1 .LVA_PD_%=\n" \
  "v_mfma_f32_16x16x32_bf16 v[96:99], v[160:163], v[244:247], v[100:103]\n" \
  "v_mfma_f32_16x16x32_bf16 v[212:215], v[144:147], v[58:61], v[216:219]\n" \
  "s_cmp_ge_u32 s80, 7\n" \
  "s_cbranch_scc1 .LVA_PD_%=\n" \
  "v_mfma_f32_16x16x32_bf16 v[100:103], v[164:167], v[244:247], v[104:107]\n" \
  "v_mfma_f32_16x16x32_bf16 v[216:219], v[148:151], v[58:61], v[220:223]\n" \
  "s_cmp_ge_u32 s80, 6\n" \
  "s_cbranch_scc1 .LVA_PD_%=\n" \
  "v_mfma_f32_16x16x32_bf16 v[104:107], v[168:171], v[244:247], v[108:111]\n" \
  "v_mfma_f32_16x16x32_bf16 v[220:223], v[152:155], v[58:61], v[224:227]\n" \
  "s_cmp_ge_u32 s80, 5\n" \
  "s_cbranch_scc1 .LVA_PD_%=\n" \
  "v_mfma_f32_16x16x32_bf16 v[108:111], v[172:175], v[244:247], v[112:115]\n" \
  "v_mfma_f32_16x16x32_bf16 v[224:227], v[156:159], v[58:61], v[228:231]\n" \
  "s_cmp_ge_u32 s80, 4\n" \
  "s_cbranch_scc1 .LVA_PD_%=\n" \
  "v_mfma_f32_16x16x32_bf16 v[112:115], v[176:179], v[244:247], v[116:119]\n" \
  "v_mfma_f32_16x16x32_bf16 v[228:231], v[160:163], v[58:61], v[232:235]\n" \
  "s_cmp_ge_u32 s80, 3\n" \
  "s_cbranch_scc1 .LVA_PD_%=\n" \
  "v_mfma_f32_16x16x32_bf16 v[116:119], v[180:183], v[244:247], v[120:123]\n" \
  "v_mfma_f32_16x16x32_bf16 v[232:235], v[164:167], v[58:61], v[236:239]\n" \
  "s_cmp_ge_u32 s80, 2\n" \
  "s_cbranch_scc1 .LVA_PD_%=\n" \
  "v_mfma_f32_16x16x32_bf16 v[120:123], v[184:187], v[244:247], v[124:127]\n" \
  "v_mfma_f32_16x16x32_bf16 v[236:239], v[168:171], v[58:61], v[240:243]\n" \
  "s_branch .LVA_PD_%=\n" \
  ".LVA_END_%=:\n" \
  "s_endpgm\n"
__device__ __forceinline__ v4u mk_srd(const void* p, const unsigned bytes) {
  const unsigned long long a = (unsigned long long)p;
  return v4u{(unsigned)a, (unsigned)(a >> 32) & 0xffffu, bytes, 0x00020000u};
}
template <bool ATT>
__device__ __forceinline__ void role_chain_t(const MegaArgs& A, char* smem, const int L, const int h, const int rep) {
  __builtin_amdgcn_s_waitcnt(0x0F70);
  MARK();
  u16* swo = reinterpret_cast<u16*>(smem);
  const int w = __builtin_amdgcn_readfirstlane(threadIdx.x >> 6);
  const int wc = (w + ROT) & 7, psw = PSWH;
  char* ws = A.ws;
  unsigned* FL = (unsigned*)(ws + OFF_FL);
  unsigned* TMO = FL + FL_TMO;
#define LANE_IDS()                         \
  const int l = lane_id();                 \
  const int c = l & 15, g = l >> 4;        \
  (void)c; (void)g;
  float* sgm = reinterpret_cast<float*>(smem + C_X);
  float* sgf = sgm + 8;
  unsigned* FQ = FL + FL_FQ + L * 65 * 32 * 32;
  v8u w0[8], w1[8], w2[8];
  PSV;
  {
    LANE_IDS();
    v4u m0r;
    const unsigned sb = lds_base(smem);
    const int q = (l >> 2) & 3, p = l & 3;
    const int f = (q & 1) | ((g & 1) << 1) | ((q >> 1) << 2);
    const unsigned ra = sb + (g >> 1) * 16384 + (4 * (g & 1) + (q & 1) + 8 * (q >> 1)) * 256 + (w ^ f) * 32 + p * 8;
#define WP(k) (WM(k) == 0 ? A.Wv : WM(k) == 1 ? A.Wk : A.Wq)
#define WC(k) (WJ(k) ^ WXC)
#define W_READ(W, X)                                                          \
  {                                                                           \
    const unsigned a_ = (ra ^ (X)) + j * 4096 + i * 32768;                    \
    const v2u x0 = trd(a_), x1 = trd(a_ + 512);                               \
    W[4 * WC(k) + i] = v4u{x0.x, x0.y, x1.x, x1.y};                           \
  }
    v4u u0[16], u1[16], u2[16], zt;
    v2u ze;
    float f2[4] = {};
    unsigned txt = 0u;
    if (CSL && L) gate(now10() + CSL);
    PST(0);
    asm volatile("s_nop 4\n\tglobal_load_dwordx4 %0, %1, %2" : "=v"(m0r) : "v"(((2 * (c >> 2) + (c & 1)) * DM + 64 * w + 16 * g + 8 * (((c >> 1) ^ (c >> 3)) & 1)) * 2), "s"(A.mem0) : "memory");
#pragma unroll
    for (int k = 0; k < 12; ++k) w_issue<true>(WP(k >> 2), WC(k >> 2), k >> 2, k & 3, h, w, l, sb);
    constexpr int PB = ATT ? 100 : 120;
    tour<PB + 3, PB + 12, 4>(w);
#pragma unroll
    for (int k = 0; k < 13; ++k) {
      const int j = k & 3;
      if (k == (ATT ? 2 : 4)) pad<PB + 3>(txt);
      if (k == (ATT ? 4 : 7)) pad<PB + 4>(txt);
      if (k == (ATT ? 6 : 10)) pad<PB + 1, 600>(txt);
      if (k == (ATT ? 10 : 9)) pad<PB + 5>(txt);
      if (k == ZC + 1) wait_vm<10>();
      else if (k < 12 || ZC == 10) wait_vm<8>();
      if (CTH && L) __builtin_amdgcn_s_sleep(CTH);
      RAW_BAR();
      if (k == 0) {
        asm volatile("" : "+v"(m0r));
        *reinterpret_cast<v4u*>(smem + C_SM + w * 1024 + l * 16) = m0r;
      }
      if (k == IPC) txt = pull_text((h & 7) + 8 * w);
      if (k == IPC + 2) asm volatile("" ::"v"(txt));
      if (k == ZC)
        asm volatile("s_nop 4\n\tglobal_load_dwordx4 %0, %2, %3 sc1\n\tglobal_load_dwordx2 %1, %4, %5 sc1"
                     : "=&v"(zt), "=&v"(ze)
                     : "v"(l * 16), "s"(ws + OFF_TAG + (blockIdx.x & 7) * 1024), "v"(0), "s"(ws + OFF_TAG + 8192)
                     : "memory");
#pragma unroll
      for (int i = 0; i < 4; ++i) {
        if (k < 9) w_issue<true>(WP(k + 3), WC(k + 3), (k + 3) & 3, i, h, w, l, sb);
        else {
          const int jo = k - 9, fd = (i & 1) | ((jo & 1) << 1) | ((w & 1) << 2);
          glds16((const char*)A.Wo + (size_t)(h * DH + 64 * (jo >> 1) + 8 * w + 4 * (jo & 1)) * (DM * 2), (unsigned)(i * 1024 + ((l ^ (fd << 1)) << 4)),
                 sb + w * 16384 + jo * 4096 + i * 1024);
        }
        if (k < 12 && WM(k) == (ATT ? 0 : 2)) { W_READ(u1, 0); W_READ(u2, 128); }
        if (k < 12 && WM(k) == 1) W_READ(u0, 0);
        if (ATT && k > 0 && WM(k - 1) == 0) {
#pragma unroll
          for (int e = 0; e < 4; ++e) {
            f2[e & 1] = dot2sq(u1[4 * WC(k - 1) + i][e], f2[e & 1]);
            f2[2 + (e & 1)] = dot2sq(u2[4 * WC(k - 1) + i][e], f2[2 + (e & 1)]);
          }
          asm volatile("" : "+v"(f2[0]), "+v"(f2[1]), "+v"(f2[2]), "+v"(f2[3]));
        }
      }
      if (k == (WORD ? 10 : 4)) {
        const float f = wave_sum_dpp((f2[0] + f2[1]) + (f2[2] + f2[3]));
        if (l == 0) { sgf[w] = f; sgm[16] = 0.f; sgm[32 + w] = 1.f; }
      }
      if (k == (WORD ? 11 : ZC + 2)) {
        asm volatile("" : "+v"(zt), "+v"(ze));
        if (w == 0) {
          if (__builtin_expect(!zero_seen(zt, ze), 0)) zero_wait(ws);
          const float fs = (sgf[0] + sgf[1]) + (sgf[2] + sgf[3]);
          __builtin_amdgcn_raw_buffer_store_b32(__float_as_uint(fs) | 1u, MKRS(FL + FL_G, L ? 0 : 4 * 512), l * 512 + h * 4, 0, AUX_SC1);
        }
      }
      if (k == 1) PST(1);
      if (k == 3) PST(2);
      if (k == 5) PST(3);
      if (k == 7) PST(4);
      if (k == 9) PST(5);
      if (k == 11) PST(6);
    }
#undef W_READ
#undef WP
#undef WC
#pragma unroll
    for (int t = 0; t < 8; ++t) {
      const v8u a0 = __builtin_shufflevector(u0[2 * t], u0[2 * t + 1], 0, 1, 2, 3, 4, 5, 6, 7), a1 = __builtin_shufflevector(u1[2 * t], u1[2 * t + 1], 0, 1, 2, 3, 4, 5, 6, 7);
      w0[t] = ATT ? a0 : a1;
      w1[t] = ATT ? a1 : a0;
      w2[t] = __builtin_shufflevector(u2[2 * t], u2[2 * t + 1], 0, 1, 2, 3, 4, 5, 6, 7);
    }
    const unsigned rb = sb + (2 * (g >> 1) + (q >> 1)) * 16384 + (4 * (g & 1) + (q & 1)) * 1024 + p * 8;
#pragma unroll
    for (int hf = 0; hf < 2; ++hf) {
      if (hf == 0) WAIT_VM(8); else WAIT_VM(0);
      if (hf == 1) pad<PB + 6>();
      RAW_BAR();
      if (hf == 1 && w == 0) __builtin_amdgcn_raw_buffer_store_b32((now10() + CRD) | 1u, MKRS(FL + FL_S0 + L * 512, 4 * 512), l * 512 + (h >> 3) * 128 + 32 + (h & 7) * 4, 0, AUX_SC1);
      v4u fr[8];
#pragma unroll
      for (int i = 0; i < 8; ++i) {
        const unsigned a_ = rb + hf * 8192 + (i & 1) * 65536 + ((4 * wc + ((i >> 1) ^ psw)) ^ f) * 32;
        const v2u r0 = trd(a_), r1 = trd(a_ + 2048);
        fr[i] = v4u{r0.x, r0.y, r1.x, r1.y};
      }
#pragma unroll
      for (int i = 0; i < 8; ++i) asm volatile("" : "+v"(fr[i]));
      RAW_BAR();
#pragma unroll
      for (int i = 0; i < 8; ++i) *reinterpret_cast<v4u*>(swo + w * 8192 + hf * 4096 + i * 512 + l * 8) = fr[i];
    }
  }
  PST(7);
  WSTAMP2();
  const unsigned sb = lds_base(smem);
  const char* gK = ws + OFF_QK + L * SZ_QK + ((size_t)h * 16 + 2 * w) * 1024;
  const char* gV = ws + OFF_VF + L * SZ_O + ((size_t)h * 8 + 2 * w - 8) * 1024;
  PST(8);
  pad<ATT ? 108 : 128>();
  v8u pv;
  {
    LANE_IDS();
    const unsigned voff = g * 256 + c * 8, wp = sb + w * 16384 + l * 16;
    pv[0] = (((c >> 1) ^ (c >> 3)) & 1) ? 0xEEEE : 0x4444;
    pv[1] = sb + C_SM + l * 16;
    pv[2] = ATT ? sb + ((c & 2) ? C_SK : C_SQ) + ((2 * (c >> 2) + (c & 1)) * QS + 8 * g) * 2 : voff;
    pv[3] = ATT ? l * 8 : wp;
    pv[4] = sb + ((c & 2) ? C_SQ : C_SO) + w * 256 + ((c & 13) + ((g ^ (c >> 2)) & 2)) * 16 + 8 * ((g ^ (c >> 1)) & 1);
    pv[5] = voff;
    pv[6] = wp;
    pv[7] = sb + C_SO + (l >> 5) * 1024 + w * 256 + (l & 31) * 8;
  }
  PST(10);
  PSD();
  SST(8);
  const v4u rsA = mk_srd(ws + OFF_ACC + L * SZ_ACC, SZ_ACC);
  const v4u rsF = mk_srd(FQ, 64 * 32 * 32 * 4);
  const v4u rsMK = mk_srd(ws + OFF_MEMK + L * SZ_MEM, SZ_MEM);
  const v4u rsMV = mk_srd(ATT ? ws + OFF_MEMV + L * SZ_MEM : (char*)(FL + FL_G + REP * 128), SZ_MEM);
  const int wr = (w + RR) & 7;
  const char* pAB = ws + OFF_ACC + L * SZ_ACC + (wc - wr) * 4096;
  const char* pGV = ATT ? (const char*)(size_t)(CDS + (XG(h) >> 1) * CDL) : gV;
  unsigned sc[8];
  sc[0] = ATT ? L * 1024 + XG(h) * 256 : h * 128;
  sc[1] = !ATT ? (h * 4 + w - 4) * 512 : (h & 7) == 1 ? L * 1024 + (h >> 3) * 256 + 128 : 1032 - L * 1024 + h * 4096;
  sc[2] = h * 2048 + w * 512;
  sc[3] = NSEG - 1;
  sc[5] = w;
  sc[6] = sb;
  const char* dbg = ws;
  const unsigned tg = (now10() / GR) * GR;
  const v4u rsT = mk_srd(FL + FL_TK, 2048);
#define CHAIN_OPS                                                                                                          \
  ::"{v[64:71]}"(w0[0]), "{v[72:79]}"(w0[1]), "{v[80:87]}"(w0[2]), "{v[88:95]}"(w0[3]), "{v[96:103]}"(w0[4]),              \
      "{v[104:111]}"(w0[5]), "{v[112:119]}"(w0[6]), "{v[120:127]}"(w0[7]), "{v[128:135]}"(w1[0]), "{v[136:143]}"(w1[1]),    \
      "{v[144:151]}"(w1[2]), "{v[152:159]}"(w1[3]), "{v[160:167]}"(w1[4]), "{v[168:175]}"(w1[5]), "{v[176:183]}"(w1[6]),    \
      "{v[184:191]}"(w1[7]), "{v[192:199]}"(w2[0]), "{v[200:207]}"(w2[1]), "{v[208:215]}"(w2[2]), "{v[216:223]}"(w2[3]),    \
      "{v[224:231]}"(w2[4]), "{v[232:239]}"(w2[5]), "{v[240:247]}"(w2[6]), "{v[248:255]}"(w2[7]), "{v[0:7]}"(pv),          \
      "{s[40:43]}"(rsA), "{s[44:47]}"(rsF), "{s[48:51]}"(rsMK), "{s[52:55]}"(rsMV), "{s[56:57]}"(pAB), "{s60}"(wr * 4096), "{s61}"(h * 1024 - wr * 4096), \
      "{s[62:63]}"(gK), "{s[64:65]}"(pGV), "{s68}"(sc[0]), "{s69}"(sc[1]), "{s70}"(sc[2]),    \
      "{s71}"(sc[3]), "{s73}"(sc[5]), "{s74}"(sc[6]), "{s[36:37]}"(dbg),                   \
      "{s34}"(tg), "{s35}"(psw * 1024), [gp] "n"(GP), [gr] "n"(GR), [gm] "n"(GM), \
      "{s[16:19]}"(rsT), "{s20}"(0), "{s22}"(sb + C_X + 128 + 8 * (w & 3)), "{s24}"(L * 1024), [cd] "n"(CD - CDS), \
      "{s25}"(APA - (APN + 1) * APP + L * (APB - APA + (2 * APN + 1) * APP)), [ka] "n"(APL), [an] "n"(APN), [d0] "n"(16 * CD0), [d1] "n"(CD1), [d2] "n"(CD2), [d3] "n"(CD1 + 63 * APP), [d4] "n"(CD4), [so] "n"((FL_S0 - FL_G) * 4), [sg] "n"(SLP_2), [g1] "n"(63 * APP + CG1 + CD2), [sf] "n"(SLP_1), [rq] "n"(RQM), [pp] "n"(APP) \
      : "memory", "vcc", "scc", "s99", "v63"
  if (ATT) asm volatile("s_mov_b64 exec, -1\ns_nop 4\n" CHAIN_ASM_ATT ".Lcode_end:\n" CHAIN_OPS);
  else asm volatile("s_mov_b64 exec, -1\ns_nop 4\n" CHAIN_ASM_VO CHAIN_OPS);
#undef CHAIN_OPS
#undef LANE_IDS
}

__device__ __forceinline__ void role_chain(const MegaArgs& A, char* smem, const int L, const int h, const int rep) {
  if (__builtin_amdgcn_readfirstlane(threadIdx.x >> 6) < 4) role_chain_t<true>(A, smem, L, h, rep);
  else role_chain_t<false>(A, smem, L, h, rep);
}

constexpr int AW_TK = 0, AW_SO = 114688, AW_M = AW_SO + 16384, AW_P = 24576, AW_MK = AW_P + 2048, AW_CNT = AW_SO + AW_MK + 4096;
__device__ __forceinline__ unsigned umin3(unsigned a, unsigned b, unsigned c) {
  return __builtin_elementwise_min(__builtin_elementwise_min(a, b), c);
}
__device__ __forceinline__ bool all_set(const unsigned r) {
  unsigned long long m;
  asm volatile("v_cmp_eq_u32 %0, 0, %1" : "=s"(m) : "v"(r) : "memory");
  return m == 0ull;
}
__device__ __forceinline__ unsigned long long zero_mask(const unsigned r) {
  unsigned long long m;
  asm volatile("v_cmp_eq_u32 %0, 0, %1" : "=s"(m) : "v"(r) : "memory");
  return m;
}
__device__ __forceinline__ int job_seg(const int n) {
  const int m = n - AWD;
  const int r = (m & 1) ? 64 + (m >> 1) : AWD + (m >> 1);
  return __builtin_amdgcn_readfirstlane(n > 127 ? 128 : (n < AWD || n >= 128 - AWD) ? n : r);
}
template <bool AT>
__device__ __forceinline__ void role_aw_t(const MegaArgs& A, char* smem, const int mode, const int a) {
  __builtin_amdgcn_s_waitcnt(0x0F70);
  MARK();
  const int w = __builtin_amdgcn_readfirstlane(threadIdx.x >> 6);
  const int l = threadIdx.x & 63, c = l & 15, g = l >> 4;
  const int qt = w & 1, hh = (w >> 1) & 1, h = 2 * a + hh;
  char* ws = A.ws;
  unsigned* FL = (unsigned*)(ws + OFF_FL);
  const unsigned sb = lds_base(smem);
  v4u wo[4][8];
  PSV;
  PST(0);
  const unsigned* FQ = FL + FL_FQ + 1024 + h * 32 + 16;
  const v4u rsW = mk_srd(ws + OFF_PW, SZ_PW);
  const v4u rsF = mk_srd(FQ, (129 * 32 - h) * 128 - 64);
  const char* gQ = ws + OFF_QK + ((size_t)h * 16 + 8 * qt) * 1024;
  const char* gV = ws + OFF_VF + ((size_t)h * 8 + 4 * qt) * 1024;
  const char* gM = ws + (qt ? OFF_MEMV : OFF_MEMK) + h * 2048;
  const unsigned mo = sb + AW_M + hh * 4096;
  const unsigned tq0 = __builtin_amdgcn_readfirstlane(sb + AW_TK + (hh * 24 + 8 * qt) * 1024);
  const unsigned tv0 = __builtin_amdgcn_readfirstlane(sb + AW_TK + (hh * 24 + 16 + 4 * qt) * 1024);
  const int gs = job_seg(mode);
  bool got = false;
  {
    const int q = (l >> 2) & 3, p = l & 3, f = q | ((g & 1) << 2);
    const unsigned ra = sb + (4 * g + q) * 256 + (p & 1) * 16;
    unsigned n0 = 0u;
#pragma unroll
    for (int i = 0; i < 8; ++i) wo_issue(A.Wo, a, 2 + (i >> 2), w, i & 3, sb);
#pragma unroll
    for (int ks = 0; ks < 8; ++ks) {
      if (ks >= 2 && ks <= 4) WAIT_VM(12);
      if (ks == 5) wait_vm<8 + (!AT && AWK < 5)>();
      if (ks == 6) wait_vm<4 + (!AT && AWK < 6)>();
      if (ks == 7) WAIT_VM(0);
      if (ks) RAW_BAR();
      if (!AT && ks == AWK)
        asm volatile("s_nop 4\n\tbuffer_load_dword %0, %1, %2, %3 offen sc1" : "=v"(n0) : "v"(0), "s"(rsF), "s"(gs * 4096) : "memory");
      if (ks == 5 && threadIdx.x < 4) ((lu32*)(unsigned long)(sb + AW_CNT))[threadIdx.x] = 0u;
      if (!AT && ks == 7 && __builtin_expect(all_set(n0), 1)) {
#pragma unroll
        for (int i = 0; i < 8; ++i) glds16c(gQ + (size_t)gs * (NH * 16 * 1024) + i * 1024, l * 16, tq0 + i * 1024);
#pragma unroll
        for (int i = 0; i < 4; ++i) glds16c(gV + (size_t)gs * (NH * 8 * 1024) + i * 1024, l * 16, tv0 + i * 1024);
        got = true;
      }
#pragma unroll
      for (int ct = 0; ct < 4; ++ct) {
        if (ks < 4) wo_issue(A.Wo, a, ks + 4, w, ct, sb);
        const int cp = 2 * w + (ct >> 1);
        const unsigned a_ = ra + (ks % 5) * 32768 + (cp >> 2) * 8192 + ((2 * (cp & 3) + (p >> 1)) ^ f) * 32 + (ct & 1) * 8;
        const v2u lo = trd(a_), hi = trd(a_ + 4096);
        wo[ct][ks] = v4u{lo.x, hi.x, lo.y, hi.y};
      }
#pragma unroll
      for (int ct = 0; ct < 4; ++ct) asm volatile("" : "+v"(wo[ct][ks]));
      if (ks == 1) PST(1); if (ks == 3) PST(2); if (ks == 5) PST(3); if (ks == 7) PST(4);
    }
    RAW_BAR();
  }
  if (got) PST(11);
  PSD();
  v8u pv, mk, wr[16];
  {
    const unsigned so = sb + AW_SO + l * 16, ta = sb + AW_TK + hh * 24576 + l * 16;
    const unsigned ix = (c & 2) ? 0xEEEE : 0x4444;
    pv = v8u{AT ? ix : l * 16u, so, (unsigned)(HK3 ? (l >> 3) * 2048 + (l & 7) * 16 : (c >> 1) * 2048 + (c & 1) * 64 + g * 16), AT ? ta : mo + qt * 2048 + l * 16, AT ? ta + qt * 4096 : ix,
             mo + (g * 8 + 2 * (c >> 2) + (c & 1)) * 16, sb + AW_M + 2048 + (g >> 1) * 512 + (c & 3) * 128 + (8 * (g & 1) + (c >> 2)) * 8,
             so + (hh * 2 + qt) * 4096};
#pragma unroll
    for (int i = 0; i < 8; ++i) mk[i] = 16 * (i >> 2) + 4 * g + (i & 3) > 16 * qt + c ? __float_as_uint(-3e38f) : 0u;
#pragma unroll
    for (int i = 0; i < 16; ++i) wr[i] = __builtin_shufflevector(wo[i >> 2][2 * (i & 3)], wo[i >> 2][2 * (i & 3) + 1], 0, 1, 2, 3, 4, 5, 6, 7);
  }
  const char* dbg = ws;
#define AW_OPS                                                                                                               \
  ::"{v[128:135]}"(wr[0]), "{v[136:143]}"(wr[1]), "{v[144:151]}"(wr[2]), "{v[152:159]}"(wr[3]), "{v[160:167]}"(wr[4]),        \
      "{v[168:175]}"(wr[5]), "{v[176:183]}"(wr[6]), "{v[184:191]}"(wr[7]), "{v[192:199]}"(wr[8]), "{v[200:207]}"(wr[9]),      \
      "{v[208:215]}"(wr[10]), "{v[216:223]}"(wr[11]), "{v[224:231]}"(wr[12]), "{v[232:239]}"(wr[13]), "{v[240:247]}"(wr[14]), \
      "{v[248:255]}"(wr[15]), "{v[0:7]}"(pv), "{v[8:15]}"(mk), "{s[40:43]}"(rsW), "{s[44:47]}"(rsF), "{s[48:49]}"(gQ),        \
      "{s[50:51]}"(gV), "{s[52:53]}"(gM), "{s[54:55]}"(FL + FL_FW + (a >> 2) * 32 + (a & 3)), "{s56}"(mode), "{s57}"(gs),      \
      "{s60}"(HK2 ? (a >> 1) * 32768 + w * 256 + (a & 1) * 128 : a * 32768 + w * 256), "{s[92:95]}"(mk_srd(ws, 0)), "{s61}"(AT ? AW_MK + qt * 2048 : tq0), "{s64}"(AT ? 0 : sb + AW_CNT + 8), "{s65}"(sb + AW_CNT + hh * 4), "{s62}"(tv0), "{s63}"(AT ? mo + 2048 - (sb + AW_SO) / 2 : mo + qt * 2048), "{s[84:87]}"(mk_srd(FL + FL_TK + (a >> 2) * 64 + 32, 1028)), "{s[88:89]}"(ws + WS_TOTAL + (28 << 20) + 1024 + qt * 4),            \
      "{s66}"(AT ? AW_P + qt * 1024 + hh * 4 : HK2 ? (a >> 1) * 32768 + (w - 4) * 256 + (a & 1) * 128 : a * 32768 + (w - 4) * 8192), "{s[82:83]}"(w == 4 ? 15ull : 0ull), "{s67}"(__builtin_amdgcn_readfirstlane((int)got)), "{s80}"(0x3e0293ee), "{s[36:37]}"(dbg),        \
      [wd] "n"(AWD), [w2] "n"(128 - 2 * AWD), [w3] "n"(131 - AWD), [dt] "n"(AWDT), [du] "n"(AWDU), [pp] "n"(APP), [mo] "n"(AWMO - 63 * APP), [lm] "n"(63 * APP - AWLM), [ld] "n"(AWLD), [sm] "n"(SLP_M), [sa] "n"(SLP_A) \
      : "memory", "vcc", "scc", "s81", "v127"
  if (AT) asm volatile("s_mov_b64 exec, -1\ns_nop 4\n" AW_ASM_0 AW_OPS);
  else asm volatile("s_mov_b64 exec, -1\ns_nop 4\n" AW_ASM_1 AW_OPS);
#undef AW_OPS
}
__device__ __forceinline__ void role_aw(const MegaArgs& A, char* smem, const int L, const int a) {
  if (__builtin_amdgcn_readfirstlane(threadIdx.x >> 6) < 4) role_aw_t<true>(A, smem, L, a);
  else role_aw_t<false>(A, smem, L, a);
}

__device__ __forceinline__ void role_red(const MegaArgs& A, char* smem, const int idx) {
  const int w = __builtin_amdgcn_readfirstlane(threadIdx.x >> 6);
  const int l = threadIdx.x & 63;
  const int L = w >> 2, gr = (w >> 1) & 1, r = idx * 2 + (w & 1);
  char* ws = A.ws;
  unsigned* FL = (unsigned*)(ws + OFF_FL);
  unsigned* TMO = FL + FL_TMO;
  const __amdgpu_buffer_rsrc_t rsW = MKRS(ws + OFF_PW, SZ_PW);
  const __amdgpu_buffer_rsrc_t rsX = MKRS(ws + OFF_XA, OFF_X3 - OFF_XA + SZ_X);
  volatile lu32* sm32 = (lu32*)(unsigned long)lds_base(smem);
  volatile lu32* sw = sm32 + (w >> 1) * 32;
  if (threadIdx.x < 128) sm32[threadIdx.x] = 0u;
  __syncthreads();
  unsigned tb = now10() + (L * (APN + 2) + 1 + gr) * GPER;
  const int rn1 = idx < 8 ? 96 + idx : 224 + idx;
  (void)rn1;
  if (L) {
    if (RSL) gate(now10() + RSL);
    const __amdgpu_buffer_rsrc_t rz = MKRS(ws + OFF_X2, SZ_X);
    for (int s = gr; s < NSEG; s += 2) ST128((v4u{0u, 0u, 0u, 0u}), rz, l * 16, (s * SEG + r) * DM * 2);
    DRAIN();
    if (l < 4) set_flag(FL + FL_FZ + (2 * l + (idx >> 3)) * 32 + (idx & 7) * 4 + (w & 3));
    if (l == 0) __hip_atomic_fetch_add((lu32*)(unsigned long)(lds_base(smem) + 480), 1u, __ATOMIC_RELAXED, __HIP_MEMORY_SCOPE_WORKGROUP);
  }
  for (unsigned spins = 0; sm32[120] < 4u && spins < 64u * SPIN_CAP; ++spins) __builtin_amdgcn_s_sleep(2);
  const int c = l & 15, g = l >> 4;
  const int pv = g * 32768 + (c >> 2) * 128 + (c & 3) * 16, xv = (16 * (g >> 1) + c) * 512 + (g & 1) * 8, ov = (g >> 1) * 256 + c * 16 + (g & 1) * 8;
  v4u sel0, sel1;
#pragma unroll
  for (int e = 0; e < 4; ++e) {
    const unsigned one = ((c & 7) >> 1) == e ? 0x3f80u << (16 * (c & 1)) : 0u;
    sel0[e] = c < 8 ? one : 0u;
    sel1[e] = c < 8 ? 0u : one;
  }
  unsigned long long en = __hip_atomic_load((gu64*)(ws + OFF_TAG + 8192), RLX_AGENT);
  asm volatile("" : "+v"(en));
  int tries = SPIN_CAP;
  for (int s = gr; s < NSEG; s += 2) {
    const int gs = 64 * L + s;
    const unsigned* fw = FL + FL_FW + (gs * 4 + REP) * 4 * 32;
    const unsigned due = grp_wait<SLP_R, 4>((w & 1) == 0, sw, s + 1, fw, 16, TMO, tb);
    tb = due + (gs >= 125 - AWD || gs < RQ0 ? GATE_RT : 2 * GPER - (L ? GMS : gs == 61 ? GMR + AWLM : GMR));
    {
      const unsigned t = now10(), e = (int)(due - t) > 0 ? due : t;
      __builtin_amdgcn_raw_buffer_store_b32((e + RDT) | 1u, MKRS(FL + FL_TW, r == 2 * RTW && !L ? 2048 : 0), l * 512, s * 4, AUX_SC1);
    }
#define RTS(k, t) TSR(L ? A.rn : rn1, k, s, (w & 6) * 64 + (t))
    RST(0);
    RTS(0, 0);
    RTS(4, 64);
    const int xo_ = L * (int)(OFF_X3 - OFF_XA) + s * 32768 + r * 16;
    const v2u xa = LD64(rsX, xv, xo_), xb = LD64(rsX, xv + 16384, xo_);
#pragma unroll 1
    for (int i = 0; i < 256; ++i) {
      const int d = (int)(due - now10());
      if (d <= 0) break;
      if (d > 8) __builtin_amdgcn_s_sleep(1);
    }
    if (L) {
      const unsigned g0 = sm32[124];
#pragma unroll 1
      for (int i = 0; i < RGW && g0 < NSEG && sm32[124] == g0; ++i) __builtin_amdgcn_s_sleep(1);
    }
    RST(1);
    f32x4 a0, a1;
    for (;; --tries) {
      v4u p[16];
#pragma unroll
      for (int k = 0; k < 16; ++k) {
        p[k] = LD128(rsW, pv + ((k & 1) + (k < 8 ? 2 : 0)) * 512, (gs * 16 + 2 * (k & 6)) * 32768 + (r >> 1) * 2048 + (r & 1) * 64);
        if ((k & 7) == 7) __builtin_amdgcn_sched_barrier(0);
      }
      if ((w & 5) == 1 && l == 0) __hip_atomic_fetch_max((lu32*)(unsigned long)(lds_base(smem) + 496), s + 1u, __ATOMIC_RELAXED, __HIP_MEMORY_SCOPE_WORKGROUP);
      __builtin_amdgcn_sched_barrier(0);
      a0 = f32x4{};
      a1 = f32x4{};
#pragma unroll
      for (int k = 0; k < 8; ++k) a1 = MFMA16(BF8((k & 1) ? sel1 : sel0), BF8(p[k]), a1);
#pragma unroll
      for (int k = 8; k < 16; ++k) a0 = MFMA16(BF8((k & 1) ? sel1 : sel0), BF8(p[k]), a0);
      if (!__any(__builtin_isunordered(a0[0], a1[0]) | __builtin_isunordered(a0[1], a1[1]) | __builtin_isunordered(a0[2], a1[2]) |
                 __builtin_isunordered(a0[3], a1[3])) || tries <= 0) break;
      asm volatile("s_sleep %0" ::"n"(SLP_P) : "memory");
    }
    RST(2);
    const unsigned nz = L ? 0u : 1u;
    const v4u xr = {xa.x, xa.y, xb.x, xb.y}, ar = {pk2(a0[0], a0[1]), pk2(a0[2], a0[3]), pk2(a1[0], a1[1]), pk2(a1[2], a1[3])};
    v4u xo;
#pragma unroll
    for (int e = 0; e < 4; ++e)
      xo[e] = __builtin_elementwise_max(pk2(lo16(xr[e]) + lo16(ar[e]), hi16(xr[e]) + hi16(ar[e])), nz);
    const __amdgpu_buffer_rsrc_t rsO = MKRS(L ? (char*)A.out : ws + OFF_X2, SZ_X);
    ST64((v2u{xo.x, xo.y}), rsO, ov, (s * SEG + r) * DM * 2);
    ST64((v2u{xo.z, xo.w}), rsO, ov + 512, (s * SEG + r) * DM * 2);
    RTS(1, 0);
    RST(3);
    RTS(5, 64);
    if (L == 0) {
      float sm = 0.f, sq = 0.f;
#pragma unroll
      for (int e = 0; e < 4; ++e) {
        const unsigned x = xo[e];
        sm = __builtin_amdgcn_fdot2_f32_bf16(__builtin_bit_cast(bf16x2_t, x), __builtin_bit_cast(bf16x2_t, 0x3f803f80u), sm, false);
        sq = dot2sq(x, sq);
      }
      float t = swap_add32(sm, sq);
      t += dppr<0x121>(t);
      t += dppr<0x122>(t);
      t += dppr<0x124>(t);
      t += dppr<0x128>(t);
      t = xsum16(t) * (1.0f / DM);
      const float ms = __builtin_bit_cast(float, __builtin_amdgcn_readlane(__builtin_bit_cast(int, t), 32));
      const float var = fmaxf(ms - t * t, 0.f);
      const unsigned st = pk2(t, __builtin_amdgcn_sqrtf(lo16(pk2(lo16(pk2(var, var)) + 1e-5f, 0.f))));
      if (l < 4) __hip_atomic_store((gu32*)(FL + FL_FR + ((s * 4 + l) * 4 + (r >> 3)) * 32 + (r & 7)), st, RLX_AGENT);
    }
    RTS(6, 64);
    RST(4);
    RTS(3, 0);
#undef RTS
  }
  RSD();
  if (idx == 0 && threadIdx.x == 384) {
    __hip_atomic_store((gu64*)(ws + OFF_TAG + 8192), en + TAG_STEP, RLX_AGENT);
  }
  if (TOURS) asm volatile("s_endpgm\n\t.p2alignl 12, 0xbf800000\n.Lpad0xa0:\n\ts_setpc_b64 s[98:99]" ::: "memory");
}

constexpr int CUN = 2144;
constexpr int STN = 392;
__device__ __forceinline__ bf16x8 conv_bfrag(const unsigned* up, const unsigned sh) {
  const unsigned d0 = up[0], d1 = up[1], d2 = up[2], d3 = up[3], d4 = up[4];
  const v4u r = {__builtin_amdgcn_alignbit(d1, d0, sh), __builtin_amdgcn_alignbit(d2, d1, sh),
                 __builtin_amdgcn_alignbit(d3, d2, sh), __builtin_amdgcn_alignbit(d4, d3, sh)};
  return BF8(r);
}
__device__ __forceinline__ void role_conv(const MegaArgs& A, char* smem, const int cv) {
  __builtin_amdgcn_s_waitcnt(0x0F70);
  MARK();
  u16* Kr = reinterpret_cast<u16*>(smem);
  u16* Uc = Kr + 8 * 2 * CKN;
  u16* To = Uc + 8 * 2 * CUN;
  u16* Sx = To + 32 * 8;
  u16* St = Sx + 32 * 8;
  const int tid = threadIdx.x;
  const int w = __builtin_amdgcn_readfirstlane(tid >> 6);
  const int l = tid & 63, c = l & 15, g = l >> 4;
  const int d = 8 * cv + w;
  char* ws = A.ws;
  unsigned* FL = (unsigned*)(ws + OFF_FL);
  unsigned* TMO = FL + FL_TMO;
  u16* kr = Kr + w * 2 * CKN;
  float* prm = reinterpret_cast<float*>(kr);
  v4u tx[4];
  f32x4 ac[16] = {};
  LST(16);
  const v2u en = LD64(MKRS(ws + OFF_TAG, 8192 + 8), 0, 8192);
  {
    const unsigned pa = *reinterpret_cast<const unsigned*>(A.logAre + d * NST + 2 * l);
    const unsigned pb = *reinterpret_cast<const unsigned*>(A.Aim + d * NST + 2 * l);
    const unsigned pc = *reinterpret_cast<const unsigned*>(A.Cre + d * NST + 2 * l);
    const unsigned pd = *reinterpret_cast<const unsigned*>(A.Cim + d * NST + 2 * l);
    const float dt = bfr(expf(bf2f(A.logdt[d])));
    tx[0] = __builtin_amdgcn_raw_buffer_load_b128(MKRS(A.x0 + 8 * cv, 128 * DM * 2), tid * (DM * 2), 0, 0);
    tour<193, 200, 8>(w);
    reinterpret_cast<unsigned*>(Uc + w * CUN + ((l & 32) ? 32 + 512 : 0))[(l & 32) ? l & 31 : c] = 0u;
    reinterpret_cast<unsigned*>(Uc + w * CUN + 32 + ((l & 32) ? L_SEQ : 128))[l & 31] = 0u;
    if (l < 50) {
      reinterpret_cast<v4u*>(kr + 1808)[l] = v4u{0u, 0u, 0u, 0u};
      reinterpret_cast<v4u*>(kr + CKN + 1808)[l] = v4u{0u, 0u, 0u, 0u};
    }
#pragma unroll
    for (int e = 0; e < 2; ++e) {
      const float ar = -bfr(expf(e ? hi16(pa) : lo16(pa))) * dt, ai = (e ? hi16(pb) : lo16(pb)) * dt;
      const float ex = expf(ar);
      float sn, cs;
      sincos1(ai, &sn, &cs);
      if (e == 1) pad<187, 1050>(sn);
      prm[2 * l + e] = ar;
      prm[NST + 2 * l + e] = ai;
      prm[2 * NST + 2 * l + e] = (e ? hi16(pc) : lo16(pc)) * dt;
      prm[3 * NST + 2 * l + e] = (e ? hi16(pd) : lo16(pd)) * dt;
      prm[4 * NST + 2 * l + e] = ex * cs;
      prm[5 * NST + 2 * l + e] = ex * sn;
    }
  }
  LST(21); LSC(62);
  const float dsk = bf2f(A.Dskip[d]);
  const u16* ka = kr + (c >> 3) * CKN + 2080 - 16 * (c & 7) + 8 * g;
  const unsigned ush = (c & 1) * 16;
#define KFRAG(D0) (*reinterpret_cast<const bf16x8*>(ka - (D0)))
  const __amdgpu_buffer_rsrc_t rz1 = MKRS(ws + OFF_XA, SZ_X);
  const int t0 = 16 * (4 * (g & 1) + (g >> 1)) + c;
  {
    const u16* uc = Uc + w * CUN;
    const unsigned* ub = reinterpret_cast<const unsigned*>(uc + (c & ~1) + 8 * g + 32);
    const __amdgpu_buffer_rsrc_t rst = MKRS(ws + OFF_GT, L_SEQ * 4);
#define TILE_OUT(dst, av, T)                                                                   \
  {                                                                                            \
    const u16* u_ = Uc + w * CUN + 32 + 128 * (T) + t0;                                        \
    const unsigned uu = pk2(bf2f(u_[0]) * dsk, bf2f(u_[32]) * dsk);                            \
    const unsigned yy = pk2(swap_add32(av[0], av[1]), swap_add32(av[2], av[3]));               \
    const unsigned aa = pk2(lo16(yy) + lo16(uu), hi16(yy) + hi16(uu));                         \
    (dst)[t0] = (u16)aa;                                                                       \
    (dst)[t0 + 32] = (u16)(aa >> 16);                                                          \
  }
#define CHUNK_ST(ck, xr, src, n)                                                               \
  {                                                                                            \
    v4u o;                                                                                     \
    _Pragma("unroll") for (int e = 0; e < 4; ++e)                                              \
      o[e] = pk2(lo16(xr[e]) + bf2f((src)[(2 * e) * (n) + l]), hi16(xr[e]) + bf2f((src)[(2 * e + 1) * (n) + l])); \
    ST128(o, rz1, (l >> 5) * 32768 + (l & 31) * 16, (2 * (ck) * 64 + cv) * 512);               \
  }
#define U_ROW(row, xr_) U_ROW2(row, xr_, __builtin_amdgcn_raw_buffer_load_b32(rst, (row) * 4, 0, AUX_SC1))
#define U_ROW2(row, xr_, stv)                                                                       \
  {                                                                                            \
    const unsigned st_ = (stv);                                                                \
    const float mb_ = lo16(st_), ri_ = 1.0f / hi16(st_);                                       \
    _Pragma("unroll") for (int e = 0; e < 4; ++e) {                                            \
      const unsigned t_ = pk2(lo16(xr_[e]) - mb_, hi16(xr_[e]) - mb_);                         \
      const unsigned q_ = pk2(lo16(t_) * ri_, hi16(t_) * ri_);                                 \
      Uc[(2 * e) * CUN + 32 + (row)] = (u16)q_;                                                \
      Uc[(2 * e + 1) * CUN + 32 + (row)] = (u16)(q_ >> 16);                                    \
    }                                                                                          \
  }
    const unsigned* FS = FL + FL_FS + REP * 32 * 32;
    const int gi = 4 * (g * 8 + (l & 7));
    const int tl = 32 * ((l >> 3) & 1) + 8 * g + (l & 7);
    unsigned khl = 0u, fs = 0u, st0 = 0u;
    const int fo = (w + 8 * ((l >> 3) & 3)) * 32 + (l & 7);
#pragma unroll 1
    for (int ps = 0; ps < 2; ++ps) {
      float v[32];
      {
        const f32x4 ar = *reinterpret_cast<const f32x4*>(prm + gi), ai = *reinterpret_cast<const f32x4*>(prm + NST + gi);
        const f32x4 cr = *reinterpret_cast<const f32x4*>(prm + 2 * NST + gi), ci = *reinterpret_cast<const f32x4*>(prm + 3 * NST + gi);
        const f32x4 wr = *reinterpret_cast<const f32x4*>(prm + 4 * NST + gi), wi = *reinterpret_cast<const f32x4*>(prm + 5 * NST + gi);
        float p0 = (float)(64 * ps + 32 * ((l >> 3) & 1));
        pad<188, 1050>(p0);
        float yr0[4], yi0[4];
#pragma unroll
        for (int k = 0; k < 4; ++k) {
          const float e = __expf(ar[k] * p0);
          float sn, cs;
          __sincosf(ai[k] * p0, &sn, &cs);
          const float zr = e * cs, zi = e * sn;
          yr0[k] = cr[k] * zr - ci[k] * zi;
          yi0[k] = cr[k] * zi + ci[k] * zr;
        }
        RUN_START
#pragma unroll
        for (int i = 0; i < 32; ++i) {
          const f32x2_t sm2 = ya + yb;
          v[i] = sm2.x + sm2.y;
          if (i == 18) pad<189, 1000>(za);
          RUN_STEP
        }
      }
#pragma unroll
      for (int i = 0; i < 16; ++i) v[i] = swap_add32(v[i], v[i + 16]);
#pragma unroll
      for (int i = 0; i < 8; ++i) {
        if (i == 6) pad<190, 1000>(v[i]);
        v[i] = swap_add16(v[i], v[i + 8]);
        v[i] += dppf<0xB1>(0.f, v[i]);
        v[i] += dppf<0x4E>(0.f, v[i]);
        v[i] += dppf<0x141>(0.f, v[i]);
      }
      float s0 = v[0], s1 = v[1], s2 = v[2], s3 = v[3], s4 = v[4], s5 = v[5], s6 = v[6], s7 = v[7];
      asm volatile("" : "+v"(s0), "+v"(s1), "+v"(s2), "+v"(s3), "+v"(s4), "+v"(s5), "+v"(s6), "+v"(s7));
      s0 = (l & 4) ? s4 : s0; s1 = (l & 4) ? s5 : s1; s2 = (l & 4) ? s6 : s2; s3 = (l & 4) ? s7 : s3;
      s0 = (l & 2) ? s2 : s0; s1 = (l & 2) ? s3 : s1;
      const float kv = 2.0f * ((l & 1) ? s1 : s0);
      khl = pk2(kv, 0.f);
      khl = pk2(kv, kv - lo16(khl));
      kr[2080 - 64 * ps - tl] = (u16)khl;
      kr[CKN + 2080 - 64 * ps - tl] = (u16)(khl >> 16);
      SST(ps);
      LST(22 + 6 * ps);
      {
        if (ps == 0) {
          if (w < 2) {
            const unsigned long long m = (((unsigned long long)en.y << 32) | en.x) + TAG_STEP;
            v4u g0;
#pragma unroll 1
            for (unsigned spins = 0; spins < 16u * SPIN_CAP; ++spins) {
              g0 = LD128(MKRS(ws + OFF_G0, 8192), tid * 16, REP * 2048);
              if (__all(((g0.x ^ g0.z) | (g0.y ^ (unsigned)m) | (g0.w ^ (unsigned)(m >> 32))) == 0u)) break;
              asm volatile("s_sleep 2" ::: "memory");
            }
            LST(23);
            U_ROW2(tid, tx[0], g0.x);
          }
          __syncthreads();
          LST(24);
        }
        f32x4 a0t = {};
        pad<191, 1050>(a0t);
#pragma unroll
        for (int b = -1; b < 2; ++b) a0t = MFMA16(KFRAG(-32 * b), conv_bfrag(ub + 16 * b, ush), a0t);
        if (ps) {
#pragma unroll
          for (int b = 2; b < 4; ++b) a0t = MFMA16(KFRAG(-32 * b), conv_bfrag(ub + 16 * b, ush), a0t);
        }
        TILE_OUT(St + w * STN + 128 * ps, a0t, 0);
        __syncthreads();
        LST(25 + 6 * ps);
        if (w == ps) {
          CHUNK_ST(w, tx[0], St + 192 * w, STN);
          LST(26 + 6 * ps);
          pad<192, 900>();
          DRAIN();
          LST(27 + 6 * ps);
          if (ps == 0) {
            if (l < 4) __hip_atomic_store((gu64*)(ws + OFF_X0) + l * 128 + (cv >> 3) * 16 + (cv & 7), (((unsigned long long)en.y << 32) | en.x) + TAG_STEP, RLX_AGENT);
            zero_wait(ws);
          }
          if (l < 8) set_flag(FL + FL_FC + ((2 * w * 4 + l) * 8 + (cv >> 3)) * 32 + (cv & 7));
        }
        SST(4 + ps);
        if (ps == 0 && w >= 2) tx[0] = *reinterpret_cast<const v4u*>(A.x0 + (size_t)tid * DM + 8 * cv);
      }
#define FS_WAIT(o)                                                                             \
  {                                                                                            \
    _Pragma("unroll 1") for (unsigned spins = 0; !__all(fs != 0u) && !spin_timeout(spins, TMO); ++spins) { \
      __builtin_amdgcn_s_sleep(4);                                                             \
      fs = __hip_atomic_load((gu32*)FS + (o), RLX_AGENT);                                      \
    }                                                                                          \
    asm volatile("" ::: "memory");                                                             \
  }
    }
    pad<193, 950>();
    const gu32* kg = (gu32*)(FL + FL_KG + cv * 32 + w);
    fs = __hip_atomic_load((gu32*)FS + w * 32 + (l & 7), RLX_AGENT);
    unsigned kf = __hip_atomic_load(kg, RLX_AGENT);
    tour<180, 185, 8>(w + 6);
    FS_WAIT(w * 32 + (l & 7));
    st0 = __builtin_amdgcn_raw_buffer_load_b32(rst, tid * 4, 0, AUX_SC1);
    SST(6);
    LST(0); LSC(63);
    const __amdgpu_buffer_rsrc_t rk = MKRS(ws + OFF_KF + (size_t)d * 2 * CKN * 2, 2 * CKN * 2);
    v4u t[8];
    if (__builtin_amdgcn_readfirstlane(kf) == 0u) (void)wait_n<8, 1>(FL + FL_KG + cv * 32 + w, 1, TMO);
    asm volatile("" ::: "memory");
    LST(1);
#pragma unroll
    for (int i = 0; i < 4; ++i) {
      t[i] = LD128(rk, (64 * i + l) * 16, 0);
      t[4 + i] = LD128(rk, (64 * i + l) * 16, CKN * 2);
    }
    pad<195, 800>();
    if (w >= 2) U_ROW2(tid, tx[0], st0);
    unsigned* f1 = FL + FL_FC + (64 * w + (cv >> 3)) * 32 + (cv & 7);
    SST(7);
    LST(4);
    {
#pragma unroll
      for (int i = 0; i < 4; ++i)
        if (64 * i + l < 245) {
          reinterpret_cast<v4u*>(kr)[64 * i + l] = t[i];
          reinterpret_cast<v4u*>(kr + CKN)[64 * i + l] = t[4 + i];
        }
      kr[2080 - 64 - tl] = (u16)khl;
      kr[CKN + 2080 - 64 - tl] = (u16)(khl >> 16);
      if (l < 33) kr[l] = kr[CKN + l] = 0;
    }
    pad<196, 1050>();
    RAW_BAR();
    LST(5);
    fs = __hip_atomic_load((gu32*)FS + fo, RLX_AGENT);
    {
      f32x4 a1 = {};
#pragma unroll
      for (int b = -1; b < 8; ++b) a1 = MFMA16(KFRAG(128 - 32 * b), conv_bfrag(ub + 16 * b, ush), a1);
      pad<197, 300>(a1);
      TILE_OUT(St + w * STN, a1, 1);
    }
    bf16x8 kq[16], bw[4];
#pragma unroll
    for (int t = 0; t < 4; ++t) bw[t] = conv_bfrag(ub + 64 * t - 16, ush);
#pragma unroll
    for (int i = 0; i < 16; ++i) kq[i] = KFRAG(128 * i + 32);
    ac[2] = MFMA16(KFRAG(-96), bw[3], ac[2]);
    ac[3] = MFMA16(KFRAG(-96), conv_bfrag(ub + 16 * 15, ush), ac[3]);
    LST(2);
    RAW_BAR();
    if ((w >> 1) == 1) {
      CHUNK_ST(w, tx[0], St + 64 * w - 128, STN);
      DRAIN();
      if (l < 8) set_flag(f1 + l * 256);
    }
    pad<198, 300>();
    FS_WAIT(fo);
    LST(3);
    unsigned sx[4];
#pragma unroll
    for (int i = 1; i < 4; ++i) sx[i] = __builtin_amdgcn_raw_buffer_load_b32(rst, (tid + 512 * i) * 4, 0, AUX_SC1);
#pragma unroll
    for (int i = 2; i < 16; ++i) asm volatile("" : "+v"(ac[i]));
    asm volatile("s_nop 1" : "+v"(bw[0]), "+v"(bw[1]), "+v"(bw[2]), "+v"(bw[3]));
#pragma unroll
    for (int q = 0; q < 4; ++q) {
      unsigned r[20];
      v4u nw[4];
      float ri;
#pragma unroll
      for (int i = 15; i >= -1; --i) {
#pragma unroll
        for (int t = 0; t < 4; ++t) {
          if (i >= 0 && i + t >= 2 && i + t < 16) asm volatile("v_mfma_f32_16x16x32_bf16 %0, %1, %2, %0" : "+v"(ac[i + t]) : "v"(kq[i]), "v"(bw[t]));
          if (q < 3 && t == 0 && i < 15) kq[i + 1] = KFRAG(128 * i + 128 - 32 * q);
          if (q < 3 && (t & 1) && i >= 8 && i < 13) {
            r[10 * (t >> 1) + i - 8] = ub[128 * (t >> 1) + 16 * q + i - 8];
            r[10 * (t >> 1) + i - 3] = ub[128 * (t >> 1) + 16 * q + i + 56];
          }
          if (q < 3 && i >= 2 && i < 6) nw[t][i - 2] = __builtin_amdgcn_alignbit(r[5 * t + i - 1], r[5 * t + i - 2], ush);
          if (q < TXQ && t == 2 && i == 12) tx[q + 1] = *reinterpret_cast<const v4u*>(A.x0 + (size_t)(tid + 512 * (q + 1)) * DM + 8 * cv);
          if (q == TXQ - 1 && t == 2 && i > 9 + TXQ - 1 && i < 12) tx[13 + q - i] = *reinterpret_cast<const v4u*>(A.x0 + (size_t)(tid + 512 * (13 + q - i)) * DM + 8 * cv);
          if (q && t == 2 && i == 8) ri = 1.0f / hi16(sx[q]);
          if (q && (t & 1) && (i == 7 || i == 6)) {
            const int e = 2 * (i & 1) + (t >> 1);
            const unsigned t_ = pk2(lo16(tx[q][e]) - lo16(sx[q]), hi16(tx[q][e]) - lo16(sx[q]));
            const unsigned q_ = pk2(lo16(t_) * ri, hi16(t_) * ri);
            Uc[(2 * e) * CUN + 32 + tid + 512 * q] = (u16)q_;
            Uc[(2 * e + 1) * CUN + 32 + tid + 512 * q] = (u16)(q_ >> 16);
          }
          __builtin_amdgcn_sched_barrier(0);
        }
        if (q == 0 && i == 12) pad<199, 200>();
        if (q == 0 && i == 1) pad<180, 200>();
        if (q == 1 && i == 7) pad<181, 200>();
        if (q == 2 && i == 12) pad<182, 200>();
        if (q == 2 && i == 5) pad<183, 200>();
        if (q == 3 && i == 8) pad<184, 200>();
      }
#pragma unroll
      for (int t = 0; t < 4 && q < 3; ++t) bw[t] = BF8(nw[t]);
    }
    asm volatile("s_nop 7" : "+v"(ac[2]), "+v"(ac[3]));
    pad<194, 300>(ac[3]);
    TILE_OUT(St + w * STN + 128, ac[2], 2);
    TILE_OUT(St + w * STN + 256, ac[3], 3);
    SST(10);
    LST(6); LSC(64);
    RAW_BAR();
    if (w >= 4) {
      CHUNK_ST(w, tx[0], St + 64 * w - 128, STN);
      DRAIN();
      if (l < 8) set_flag(f1 + l * 256);
    }
#undef FS_WAIT
    if (w < 4) tour<176, 180, 4>(w);
    if (w == 1) (void)(wait_n<SLP_V, 32>(FL + FL_FZ + REP * 64, 64, TMO));
    SST(9);
    LST(9);
    LSD();
#undef U_ROW
#undef U_ROW2
#undef TILE_OUT
#undef CHUNK_ST
  }
  const unsigned sb = lds_base(smem);
  const unsigned u1 = sb + (16 * CKN + w * CUN) * 2, u2 = u1 + 16 * CUN, to = sb + (16 * CKN + 16 * CUN) * 2, st = to + 1024;
  const unsigned win = ((c & ~1) + 8 * g) * 2, row = 16 * (g >> 1) + c;
  const bool nil = c < 8 && g < 2;
  if (l < 16) reinterpret_cast<unsigned*>(Uc + (8 + w) * CUN)[l] = 0u;
  bf16x8 th, tw;
#pragma unroll
  for (int i = 0; i < 8; ++i) {
    th[i] = (short)kr[2064 - c + 8 * g + i];
    tw[i] = (short)kr[CKN + 2064 - c + 8 * g + i];
  }
  v4u known;
#pragma unroll
  for (int e = 0; e < 4; ++e) known[e] = (c + 8 * g + 2 * e < 32 ? 0xffffu : 0u) | (c + 8 * g + 2 * e < 31 ? 0xffff0000u : 0u);
  const bf16x8 kz = KFRAG(-96);
  const v8u pa = {u2 + (nil ? 0 : c < 8 ? 32 + 16 * g : 64 + 16 * g), nil ? 0u : 64u, u2 + 64 + 2 * row, to + 16 * row + 2 * w, u2 + win,
                  sb + (unsigned)((w * 2 + (c >> 3)) * CKN + 2080 - 16 * (c & 7) + 8 * g) * 2 - 3904, ush, (unsigned)l * 16u};
  const v8u pb = {u1 + win + 1024, 0u, 0u, 0u, 0u, u1 + (32 + 512 + t0) * 2, st + (w * STN + t0) * 2, st + (64 * (w & 1) + l) * 2};
  const v4u pc = {(unsigned)((l >> 5) * 32768 + (l & 31) * 16), (unsigned)((l >> 1) * (DM * 2) + (l & 1) * 8), (unsigned)((l >> 4) * 128 + ((l >> 1) & 7) * 4),
                  sb + (16 * CKN + (8 + 4 * (l & 1)) * CUN + 32 + (l >> 1)) * 2};
  const char* dbg = ws;
  asm volatile("s_mov_b64 exec, -1\ns_nop 4\n" CONV_ASM
               ::"{v[0:7]}"(pa), "{v[8:11]}"(known), "{v[12:15]}"(th), "{v[16:19]}"(tw), "{v[24:27]}"(kz), "{v[28:35]}"(pb), "{v[36:39]}"(pc),
               "{v40}"(to + (w ? l * 8 : (l & 31) * 16)), "{v[44:47]}"(tx[1]), "{v[48:51]}"(tx[2]), "{v[52:55]}"(tx[3]),
               "{v[196:199]}"(ac[4]), "{v[200:203]}"(ac[5]), "{v[204:207]}"(ac[6]), "{v[208:211]}"(ac[7]), "{v[212:215]}"(ac[8]), "{v[216:219]}"(ac[9]),
               "{v[220:223]}"(ac[10]), "{v[224:227]}"(ac[11]), "{v[228:231]}"(ac[12]), "{v[232:235]}"(ac[13]), "{v[236:239]}"(ac[14]), "{v[240:243]}"(ac[15]),
               "{s[40:43]}"(mk_srd(ws + OFF_X2, SZ_X)), "{s[44:47]}"(mk_srd(FL + FL_FR, 64 * 16 * 128 + 2048)), "{s71}"(64 * 16 * 128 + REP * 512), "{s[48:51]}"(mk_srd(ws + OFF_X3, SZ_X)),
               "{s[52:55]}"(mk_srd(ws + OFF_XA, SZ_X)), "{s[58:59]}"(FL + FL_FC + (64 * 32 + (cv >> 3)) * 32 + (cv & 7)),
               "{s[60:61]}"(FL + FL_FC + (64 * (w + 8) + (cv >> 3)) * 32 + (cv & 7)), "{s62}"(0), "{s63}"(w),
               "{s64}"(__builtin_amdgcn_readfirstlane(__float_as_uint(dsk))), "{s65}"(cv * 16), "{s66}"(REP * 512), "{s67}"(cv * 512),
               "{s68}"((w + 8) * 65536 + cv * 512), "{s69}"(now10()), "{s[72:73]}"(0x0000ffff0000ffffull), "{s[36:37]}"(dbg),
               [lt] "n"(-CVLT), [gv] "n"(GATE_V), [sv] "n"(SLP_V)
               : "memory", "vcc", "scc", "s81", "v255");
#undef KFRAG
}

__device__ __forceinline__ v4u ln_row_packed(const v4u xr) {
  float v[8];
#pragma unroll
  for (int e = 0; e < 4; ++e) { v[2 * e] = lo16(xr[e]); v[2 * e + 1] = hi16(xr[e]); }
  float s = ((v[0] + v[1]) + (v[2] + v[3])) + ((v[4] + v[5]) + (v[6] + v[7]));
  s = wave_sum(s);
  const float mean = s * (1.0f / DM);
  float q = 0.f;
#pragma unroll
  for (int j = 0; j < 8; ++j) { float t = v[j] - mean; q += t * t; }
  q = wave_sum(q);
  const float var = q * (1.0f / DM);
  const float mb = bfr(mean);
  const float sd = bfr(sqrtf(bfr(bfr(var) + 1e-5f)));
  const float ri = 1.0f / sd;
  v4u o;
#pragma unroll
  for (int e = 0; e < 4; ++e) {
    const unsigned t = pk2(v[2 * e] - mb, v[2 * e + 1] - mb);
    const float x0 = lo16(t), x1 = hi16(t);
    float q0 = x0 * ri, q1 = x1 * ri;
    q0 = __builtin_fmaf(__builtin_fmaf(-q0, sd, x0), ri, q0);
    q1 = __builtin_fmaf(__builtin_fmaf(-q1, sd, x1), ri, q1);
    o[e] = pk2(q0, q1);
  }
  return o;
}

__device__ __forceinline__ float bfr1(float x) { return lo16(pk2(x, x)); }
__device__ __forceinline__ void ln4_packed(v4u (&x)[4]) {
  f32x2_t v[16];
#pragma unroll
  for (int e = 0; e < 16; ++e) v[e] = f32x2_t{lo16(x[e >> 2][e & 3]), hi16(x[e >> 2][e & 3])};
  const f32x2_t t = (((v[0] + v[1]) + (v[2] + v[3])) + ((v[4] + v[5]) + (v[6] + v[7]))) +
                    (((v[8] + v[9]) + (v[10] + v[11])) + ((v[12] + v[13]) + (v[14] + v[15])));
  const float mean = red_mod4(t.x + t.y) * (1.0f / DM);
  const float mb = bfr1(mean);
  f32x2_t q0 = {0.f, 0.f}, q1 = {0.f, 0.f};
#pragma unroll
  for (int e = 0; e < 16; e += 2) {
    v[e] -= mb;
    v[e + 1] -= mb;
    q0 += v[e] * v[e];
    q1 += v[e + 1] * v[e + 1];
  }
  q0 += q1;
  const float dm = mean - mb;
  const float var = fmaxf(red_mod4(q0.x + q0.y) * (1.0f / DM) - dm * dm, 0.f);
  const float sd = bfr1(sqrtf(bfr1(bfr1(var) + 1e-5f)));
  const float ri = 1.0f / sd;
#pragma unroll
  for (int e = 0; e < 16; ++e) {
    const unsigned t2 = pk2(v[e].x, v[e].y);
    const f32x2_t q = f32x2_t{lo16(t2), hi16(t2)} * ri;
    x[e >> 2][e & 3] = pk2(q.x, q.y);
  }
}

extern "C" __device__ float mul1(float, float) __asm("llvm.amdgcn.fmul.legacy");
template <bool G0>
__device__ __forceinline__ void role_qkv_t(const MegaArgs& A, char* smem, const int L, const int h) {
  __builtin_amdgcn_s_waitcnt(0x0F70);
  MARK();
  const int w = __builtin_amdgcn_readfirstlane(threadIdx.x >> 6);
  char* ws = A.ws;
  unsigned* FL = (unsigned*)(ws + OFF_FL);
  unsigned* TMO = FL + FL_TMO;
#define LANE_IDS()                         \
  const int l = lane_id();                 \
  const int c = l & 15, g = l >> 4;        \
  (void)c; (void)g;
  unsigned* FC = FL + FL_FC + REP * 8 * 32;
  const int s0 = L * QN0;
  bf16x8 wa[16], wb[16], wv[16];
  const char* Xb = ws + OFF_XA + s0 * 32768 + (w & 3) * 8192;
  const v4u rsF = mk_srd(FC + (w & 3) * 64, ((L ? 64 : QN0) * 32 - REP * 8 - 2 * (w & 3)) * 128);
  constexpr unsigned Q_R = 131072;
  bool zs, got = false;
  v2u n0 = {};
  f32x4 cs[3] = {};
  PSV;
  {
    LANE_IDS();
    const unsigned sb = lds_base(smem);
    const int q = (l >> 2) & 3, p = l & 3, f = q | ((g & 1) << 2);
    const unsigned r0 = sb + (g >> 1) * 16384 + (8 * (g & 1) + q) * 256;
    const unsigned ra = r0 + (w ^ f) * 32 + p * 8, rb = r0 + ((2 * (w & 3) + (p >> 1)) ^ f) * 32 + (p & 1) * 16;
    const v4u one = {0x3f803f80u, 0x3f803f80u, 0x3f803f80u, 0x3f803f80u};
    v4u zt;
    v2u ze;
    unsigned txt = 0u;
#define WP(k) (WM(k) == 0 ? A.Wv : WM(k) == 1 ? A.Wk : A.Wq)
#define W_READ(W, a_)                                    \
  {                                                      \
    const v2u x0 = trd(a_), x1 = trd((a_) + 1024);       \
    W = BF8((v4u{x0.x, x0.y, x1.x, x1.y}));              \
  }
    if (QSL && L) gate(now10() + QSL);
    PST(0);
#pragma unroll
    for (int k = 0; k < 12; ++k) w_issue<false>(WP(k >> 2), WJ(k >> 2), k >> 2, k & 3, h, w, l, sb);
    PST(1);
    constexpr int PB = G0 ? 140 : 160;
    tour<PB + 5, PB + 12, 4>(w);
#pragma unroll
    for (int k = 0; k < 12; ++k) {
      const int j = k & 3;
      if (k == (G0 ? 3 : 3)) pad<PB + 5>(cs[2]);
      if (k == (G0 ? 5 : 7)) pad<PB + 6>(cs[2]);
      if (k == (G0 ? 11 : 9)) pad<PB + 7>(cs[2]);
      if (k == (G0 ? 7 : 11)) pad<PB + 1, 700>(cs[2]);
      if (k == ZK + 1) wait_vm<10>();
      else if (k < 10) wait_vm<8>();
      else if (k == 10) wait_vm<G0 && ZK < 8 ? 5 : 4>();
      else wait_vm<G0 ? 1 : 0>();
      RAW_BAR();
      if (k == IPQ) txt = pull_text((h & 7) + 8 * w);
      if (k == IPQ + 2) asm volatile("" ::"v"(txt));
      if (k == ZK)
        asm volatile("s_nop 4\n\tglobal_load_dwordx4 %0, %2, %3 sc1\n\tglobal_load_dwordx2 %1, %4, %5 sc1"
                     : "=&v"(zt), "=&v"(ze)
                     : "v"(l * 16), "s"(ws + OFF_TAG + (blockIdx.x & 7) * 1024), "v"(0), "s"(ws + OFF_TAG + 8192)
                     : "memory");
#define X_LOOK() asm volatile("s_nop 4\n\tglobal_load_dwordx2 %0, %1, %2 sc1" : "=v"(n0) : "v"((l & 7) * 8 + (l & 8) * 16), "s"(ws + OFF_X0 + REP * 1024 + w * 256) : "memory")
#define X_SEEN() zero_seen(v4u{n0.x, n0.y, n0.x, n0.y}, ze)
      if (k == ZK + 2) {
        asm volatile("" : "+v"(zt), "+v"(ze));
        zs = zero_seen(zt, ze);
        if (G0) X_LOOK();
      }
#define X_ASK()                                                                                                \
  {                                                                                                            \
    asm volatile("" : "+v"(n0));                                                                               \
    got = !L && X_SEEN();                                                                                      \
    if (__builtin_expect(got, 1)) {                                                                            \
      _Pragma("unroll 1") for (int i = 0; i < 8192; i += 1024) glds16c(Xb + i, l * 16, sb + Q_R + w * 8192 + i); \
    }                                                                                                          \
  }
      if (k == 11 && G0) {
        WAIT_VM(0);
        X_ASK();
        X_LOOK();
      }
#pragma unroll
      for (int i = 0; i < 4; ++i) {
        if (k < 9) w_issue<false>(WP(k + 3), WJ(k + 3), (k + 3) & 3, i, h, w, l, sb);
        if (WM(k) == 0) W_READ(wv[4 * WJ(k) + i], ra + j * 4096 + i * 32768);
        if (WM(k) == (G0 ? 1 : 2)) {
          W_READ(wa[4 * WJ(k) + i], rb + j * 4096 + i * 32768);
          W_READ(wb[4 * WJ(k) + i], rb + 8 + j * 4096 + i * 32768);
        }
        if (k > 0 && WM(k - 1) == 0) cs[2] = MFMA16(BF8(one), wv[4 * WJ(k - 1) + i], cs[2]);
        if (k > 0 && WM(k - 1) == (G0 ? 1 : 2)) {
          cs[0] = MFMA16(BF8(one), wa[4 * WJ(k - 1) + i], cs[0]);
          cs[1] = MFMA16(BF8(one), wb[4 * WJ(k - 1) + i], cs[1]);
        }
        asm volatile("" : "+v"(cs[0]), "+v"(cs[1]), "+v"(cs[2]));
      }
      if (k == 1) PST(2);
      if (k == 3) PST(3);
      if (k == 5) PST(4);
      if (k == 7) PST(5);
      if (k == 9) PST(6);
    }
    if (got) PST(11);
#pragma unroll
    for (int i = 12; i < 16 && !G0; ++i) {
      cs[0] = MFMA16(BF8(one), wa[i], cs[0]);
      cs[1] = MFMA16(BF8(one), wb[i], cs[1]);
    }
    asm volatile("s_nop 7" : "+v"(cs[0]), "+v"(cs[1]));
    RAW_BAR();
    PST(8);
    if (G0 && __builtin_expect(!got, 1)) {
      WAIT_VM(0);
      X_ASK();
    }
    if (got) PST(12);
#undef X_LOOK
#undef X_SEEN
#undef X_ASK
#undef W_READ
#undef WP
  }
  WSTAMP2();
  constexpr int Q_X = 98304;
  const unsigned sb = lds_base(smem);
  v2u ea, eb, ev;
  {
    LANE_IDS();
    v2u e[3];
#pragma unroll
    for (int i = 0; i < 3; ++i) {
      const float x = g ? 0.f : cs[i][0], x1 = x - bfr1(x);
      e[i] = v2u{pk2(x, x1), pk2(x1 - bfr1(x1), 0.f)};
    }
    ea = e[0]; eb = e[1]; ev = e[2];
    asm volatile("" : "+v"(ea), "+v"(eb), "+v"(ev));
    if (__builtin_expect(!zs, 0)) zero_wait(ws);
    *reinterpret_cast<v2u*>(smem + Q_X + w * 2048 + (l >> 5) * 16384 + (l & 31) * 8) = v2u{0x3f803f80u, 0x3f803f80u};
  }
  pad<G0 ? 148 : 168>();
  constexpr int Q_ST = Q_X + 8 * 2048 + 256, Q_CNT = Q_X + 11 * 2048 + 256;
  *(lu32*)(unsigned long)(sb + Q_CNT) = 0u;
  v8u pv, wr[24];
  v4u pw;
  unsigned sw;
  {
    LANE_IDS();
    const unsigned fa = sb + Q_R + g * 512 + c * 16;
    pv = v8u{ea.x, ea.y, eb.x, eb.y, ev.x, ev.y, (unsigned)l * 16u, fa};
    pw = v4u{sb + Q_ST + c * 4, sb + Q_ST + g * 16,
             G0 ? (l & 7) * 4 + (l & 8) * 16 : (c & 8) ? sb + Q_X + ((w + 1) & 1) * 128 : fa + (w & 3) * 128, (c & 8) ? 0u : Q_R};
    sw = sb + Q_ST + (w & 3) * 32 + (l & 7) * 4;
#pragma unroll
    for (int i = 0; i < 8; ++i) {
      wr[i] = __builtin_shufflevector(__builtin_bit_cast(v4u, wa[2 * i]), __builtin_bit_cast(v4u, wa[2 * i + 1]), 0, 1, 2, 3, 4, 5, 6, 7);
      wr[8 + i] = __builtin_shufflevector(__builtin_bit_cast(v4u, wb[2 * i]), __builtin_bit_cast(v4u, wb[2 * i + 1]), 0, 1, 2, 3, 4, 5, 6, 7);
      wr[16 + i] = __builtin_shufflevector(__builtin_bit_cast(v4u, wv[2 * i]), __builtin_bit_cast(v4u, wv[2 * i + 1]), 0, 1, 2, 3, 4, 5, 6, 7);
    }
  }
  const v4u rsQ = mk_srd(ws + OFF_QK, 2 * SZ_QK), rsV = mk_srd(ws + OFF_VF, 2 * SZ_O);
  const v4u rsM = mk_srd(ws + (G0 ? OFF_MEMK : OFF_MEMV), 2 * SZ_MEM);
  const unsigned* FQ = FL + FL_FQ + (s0 * 32 + h) * 32;
  const char* dbg = ws;
  PST(9);
  __builtin_amdgcn_s_waitcnt(0x0F70);
  asm volatile("" ::"v"(n0));
  RAW_BAR();
  PST(10);
  PSD();
#define QKV_OPS                                                                                                              \
  ::"{v[64:71]}"(wr[0]), "{v[72:79]}"(wr[1]), "{v[80:87]}"(wr[2]), "{v[88:95]}"(wr[3]), "{v[96:103]}"(wr[4]),                \
      "{v[104:111]}"(wr[5]), "{v[112:119]}"(wr[6]), "{v[120:127]}"(wr[7]), "{v[128:135]}"(wr[8]), "{v[136:143]}"(wr[9]),      \
      "{v[144:151]}"(wr[10]), "{v[152:159]}"(wr[11]), "{v[160:167]}"(wr[12]), "{v[168:175]}"(wr[13]), "{v[176:183]}"(wr[14]), \
      "{v[184:191]}"(wr[15]), "{v[192:199]}"(wr[16]), "{v[200:207]}"(wr[17]), "{v[208:215]}"(wr[18]), "{v[216:223]}"(wr[19]), \
      "{v[224:231]}"(wr[20]), "{v[232:239]}"(wr[21]), "{v[240:247]}"(wr[22]), "{v[248:255]}"(wr[23]), "{v[0:7]}"(pv),        \
      "{v[8:11]}"(pw), "{v62}"(sw), "{s[40:43]}"(rsQ), "{s[44:47]}"(rsV), "{s[48:51]}"(rsM), "{s[52:55]}"(rsF),              \
      "{s[56:57]}"(Xb), "{s58}"(sb + Q_R + (w & 3) * 8192), "{s82}"(sb + Q_CNT + 16 + (w & 3) * 4), "{s69}"(__builtin_amdgcn_readfirstlane((int)got)), "{s59}"((s0 * 512 + h * 16 + (G0 ? 8 : 0) + (w & 3)) * 1024), "{s60}"((s0 * 256 + h * 8 + w) * 1024), \
      "{s61}"(s0 * 65536 + h * 2048 + (w & 3) * 512), "{s62}"(s0 * 4096), "{s63}"(L ? QFAST - QN0 : 0), "{s[64:65]}"(FQ), "{s66}"(L ? NSEG - 1 - QN0 : QN0 - 1), "{s67}"(0), \
      "{s83}"(G0 ? 4 - L : L ? NSEG - 1 - QN0 : QN0 - 1), "{s86}"(1 - L), [n0] "n"(QN0), [x3] "n"(OFF_X3 - OFF_XA - QN0 * 32768),            \
      "{s[70:71]}"(0xaaaaaaaaaaaaaaaaull), "{s[72:73]}"(0xccccccccccccccccull), "{s78}"(w), "{s80}"(sb + Q_CNT), "{s[84:85]}"(FL + FL_S0 + (h >> 3) * 32 + (h & 7)),             \
      "{s[36:37]}"(dbg), [gq] "n"(GATE_Q), [sq] "n"(SLP_Q), [s1] "n"(SLP_Q1 - SLP_Q)                                         \
      : "memory", "vcc", "scc", "s81", "v63"
  if (G0) asm volatile("s_mov_b64 exec, -1\ns_nop 4\n" QKV_ASM_0 QKV_OPS);
  else asm volatile("s_mov_b64 exec, -1\ns_nop 4\n" QKV_ASM_1 QKV_OPS);
#undef QKV_OPS
#undef LANE_IDS
}
__device__ __forceinline__ void role_qkv(const MegaArgs& A, char* smem, const int L, const int h) {
  if (__builtin_amdgcn_readfirstlane(threadIdx.x >> 6) < 4) role_qkv_t<true>(A, smem, L, h);
  else role_qkv_t<false>(A, smem, L, h);
}

constexpr int MEGA_WGS = 256;
constexpr size_t MEGA_LDS = 163840;
static_assert(MEGA_LDS <= 163840 && MEGA_LDS >= 162816 + 256, "lds");
static_assert(AW_SO + 49152 <= MEGA_LDS, "lds");
static_assert(MEGA_LDS >= (size_t)(8 * 2 * CKN + 8 * 2 * CUN + 2 * 32 * 8 + 8 * STN) * 2, "lds");
static_assert(MEGA_LDS >= (size_t)2 * 32 * SMS * 2 && MEGA_LDS >= 131072, "lds");
static_assert(131072 >= (size_t)(40 * NST + 32 * 400) * 4, "lds");
__global__ __launch_bounds__(512, 2) void mega_kernel(const u16* mem0, const u16* Dskip, char* ws, u16* out, const u16* x0,
                                                      const u16* logAre, const u16* Aim, const u16* Cre, const u16* Cim,
                                                      const u16* logdt, const u16* Wq, const u16* Wk, const u16* Wv,
                                                      const u16* Wo) {
  extern __shared__ __attribute__((aligned(16))) char smem[];
  if (TOURS & 4) {
    const unsigned cls = (0xfdac >> (((blockIdx.x & 1) * 4 + (blockIdx.x >> 6)) * 2)) & 3;
#define TO(a, b, c, d, e, f, g, h) "s_branch .Lpad" #a "\n\ts_branch .Lpad" #b "\n\ts_branch .Lpad" #c "\n\ts_branch .Lpad" #d "\n\ts_branch .Lpad" #e \
                                   "\n\ts_branch .Lpad" #f "\n\ts_branch .Lpad" #g "\n\ts_branch .Lpad" #h "\n\t"
    asm volatile("s_load_dword s94, %1, 0\n\ts_load_dword s95, %1, 64\n\ts_getpc_b64 s[96:97]\n\ts_add_u32 s96, s96, %0\n\ts_addc_u32 s97, s97, 0\n\ts_swappc_b64 s[98:99], s[96:97]\n\ts_branch 2f\n\t"
                 TO(0xc8, 0x78, 0x64, 0x79, 0x65, 0x81, 0x6d, 0x6e) TO(0xc8, 0xa0, 0x8c, 0xa1, 0x8d, 0xa9, 0x95, 0x97)
                 TO(0xc8, 0xba, 0xbb, 0xbc, 0xbd, 0xbe, 0xbf, 0xc0) TO(0xc9, 0xca, 0xcb, 0xcc, 0xcd, 0xce, 0xcf, 0xc8) "2:\n\ts_waitcnt lgkmcnt(0)"
                 ::"s"(cls * 32 + (__builtin_amdgcn_readfirstlane(threadIdx.x) >> 4) + 16), "s"(__builtin_amdgcn_kernarg_segment_ptr()) : "s94", "s95", "s96", "s97", "s98", "s99", "scc");
#undef TO
  }
  const int b = __builtin_amdgcn_readfirstlane(role_no());
  const MegaArgs A{mem0, Dskip, ws, out, x0, logAre, Aim, Cre, Cim, logdt, Wq, Wk, Wv, Wo, b};
  {
    const int hi = (b >= 32 && b < 96) ? b - 32 : (b >= 200 && b < 248) ? b - 136 : (b >= 96 && b < 104) ? b + 16 : (b >= 248 ? b - 128 : -1);
    if (hi >= 0) start_kgen(A, smem, hi, b < 248 - XI0 + 8 ? b - 248 + XI0 : -1, (b >= 64 && b < 104) || (b >= 232 && b < 240) ? b & 15 : -1);
  }
  if ((EXP_OFF & 1) && b >= 64 && b < 80) return;
  if ((EXP_OFF & 8) && b >= 200 && b < 232) return;
  if ((EXP_OFF & 16) && b >= 32 && b < 64) return;
  if ((EXP_OFF & 32) && b >= 80 && b < 96) return;
  if (b < 64) role_chain(A, smem, b >> 5, b & 31, 0);
  else if (b < 104 || (b >= 232 && b < 240)) role_aw(A, smem, b < 80 ? 0 : b < 96 ? 2 : 1, b & 15);
  else if (b < 168) role_conv(A, smem, b - 104);
  else if (b < 232) role_qkv(A, smem, (b - 168) >> 5, (b - 168) & 31);
  else role_red(A, smem, b - 240);
}

extern "C" void kernel_launch(void* const* d_in, const int* in_sizes, int n_in, void* d_out, int out_size, void* d_ws,
                              size_t ws_size, hipStream_t stream) {
  MegaArgs A;
  A.x0 = (const u16*)d_in[0];
  A.logAre = (const u16*)d_in[1];
  A.Aim = (const u16*)d_in[2];
  A.Cre = (const u16*)d_in[3];
  A.Cim = (const u16*)d_in[4];
  A.logdt = (const u16*)d_in[5];
  A.Dskip = (const u16*)d_in[6];
  A.mem0 = (const u16*)d_in[8];
  A.Wq = (const u16*)d_in[9];
  A.Wk = (const u16*)d_in[10];
  A.Wv = (const u16*)d_in[11];
  A.Wo = (const u16*)d_in[12];
  A.ws = (char*)d_ws;
  A.out = (u16*)d_out;

  (void)hipFuncSetAttribute((const void*)mega_kernel, hipFuncAttributeMaxDynamicSharedMemorySize, (int)MEGA_LDS);
  mega_kernel<<<MEGA_WGS, 512, MEGA_LDS, stream>>>(A.mem0, A.Dskip, A.ws, A.out, A.x0, A.logAre, A.Aim, A.Cre, A.Cim, A.logdt,
                                                   A.Wq, A.Wk, A.Wv, A.Wo);
}
```

```cpp
#include <hip/hip_runtime.h>
#include <hip/hip_bf16.h>
#include <stdint.h>

constexpr int L_SEQ = 2048;
constexpr int DM    = 512;
constexpr int NST   = 128;
constexpr int NH    = 32;
constexpr int DH    = 128;
constexpr int HD    = NH * DH;
constexpr int MM    = 8;
constexpr int SEG   = 32;
constexpr int NSEG  = L_SEQ / SEG;
constexpr int LK    = MM + SEG;

typedef unsigned short u16;
using bf16x8 = __attribute__((ext_vector_type(8))) short;
using f32x4  = __attribute__((ext_vector_type(4))) float;
using f32x16 = __attribute__((ext_vector_type(16))) float;

static __device__ __forceinline__ u16 f2bf(float f) {
  unsigned u = __float_as_uint(f);
  u += 0x7fffu + ((u >> 16) & 1u);
  return (u16)(u >> 16);
}
static __device__ __forceinline__ float bf2f(u16 h) { return __uint_as_float(((unsigned)h) << 16); }
static __device__ __forceinline__ float bfr(float f) { return bf2f(f2bf(f)); }

constexpr size_t SZ_X      = (size_t)L_SEQ * DM * 2;
constexpr size_t OFF_KF    = 0;
constexpr size_t OFF_UT    = OFF_KF + (size_t)DM * L_SEQ * 4;
constexpr size_t OFF_YT    = OFF_UT + SZ_X;
constexpr size_t OFF_XA    = OFF_YT + SZ_X;
constexpr size_t OFF_H     = OFF_XA + SZ_X;
constexpr size_t OFF_X2    = OFF_H + SZ_X;
constexpr size_t OFF_U2    = OFF_X2 + SZ_X;
constexpr size_t OFF_X3    = OFF_U2 + SZ_X;
constexpr size_t SZ_QK     = (size_t)L_SEQ * 2 * HD * 2;
constexpr size_t OFF_QK    = OFF_X3 + SZ_X;
constexpr size_t SZ_O      = (size_t)L_SEQ * HD * 2;
constexpr size_t OFF_VF    = OFF_QK + 2 * SZ_QK;
constexpr size_t SZ_PW     = (size_t)2 * NSEG * 16 * SEG * DM * 2;
constexpr size_t OFF_PW    = OFF_VF + 2 * SZ_O;
constexpr size_t SZ_MEM    = (size_t)NSEG * MM * HD * 2;
constexpr size_t OFF_MEMK  = OFF_PW + SZ_PW;
constexpr size_t OFF_MEMV  = OFF_MEMK + 2 * SZ_MEM;
constexpr size_t OFF_GT    = OFF_MEMV + 2 * SZ_MEM;
constexpr size_t SZ_ACC    = (size_t)2 * 32768;
constexpr size_t OFF_Z     = OFF_GT + (size_t)64 * DM * 4;
constexpr size_t OFF_ACC   = OFF_Z;
constexpr unsigned ACC_LO = 0x88000000u, ACC_HI = 0x87fffff7u;
constexpr size_t OFF_FL    = OFF_ACC + 2 * SZ_ACC;
constexpr int FL_FW  = 0;
constexpr int FL_FR  = FL_FW + 2 * 64 * 16 * 32;
constexpr int FL_TW  = FL_FR + 64 * 16 * 32;
constexpr int FL_FC  = FL_TW + 4 * 128;
constexpr int FL_FQ  = FL_FC + 2 * 64 * 32 * 32;
constexpr int FL_FS  = FL_FQ + 2 * 65 * 32 * 32;
constexpr int FL_FZ  = FL_FS + 4 * 32 * 32;
constexpr int FL_KG  = FL_FZ + 8 * 32;
constexpr int FL_G   = FL_KG + 64 * 32;
constexpr int FL_TK  = FL_G + 4 * 4 * 32;
constexpr int FL_TMO = FL_TK + 2 * 256;
constexpr int FL_S0  = FL_TMO + 64;
constexpr int FL_END = FL_S0 + 2048;
constexpr size_t Z_BYTES   = OFF_FL + (size_t)FL_END * 4 - OFF_Z;
#define TSH 0
constexpr size_t OFF_TAG   = OFF_Z + Z_BYTES + TSH;
constexpr unsigned long long TAG_STEP = 0x9e3779b97f4a7c15ull;
constexpr size_t OFF_G0    = OFF_TAG + 8 * 1024 + 256;
constexpr size_t OFF_X0    = OFF_G0 + 8192;
constexpr size_t WS_TOTAL  = OFF_X0 + 4096;
static_assert(Z_BYTES % 256 == 0 && OFF_Z % 256 == 0 && Z_BYTES <= 2 * 128 * 8192, "zero region");
#define REP ((blockIdx.x >> 1) & 3)
static_assert(WS_TOTAL + (4 << 20) <= (size_t)256 * 1024 * 1024, "workspace too large");

#define CKNP 96
constexpr int CKN = 2208 + CKNP;
static_assert((size_t)DM * 2 * CKN * 2 <= (size_t)DM * L_SEQ * 4 + SZ_X, "K buffer");
__device__ __forceinline__ float pos_of_index(int p) {
  if (p < 256) return (float)p;
  if (p < 384) return (float)(256 + 2 * (p - 256));
  if (p < 512) return (float)(512 + 4 * (p - 384));
  if (p < 640) return (float)(1024 + 8 * (p - 512));
  return 2048.0f;
}
__device__ __forceinline__ int index_of_l(int l) {
  float pf = bfr((float)l);
  int p = (int)pf;
  if (p < 256) return p;
  if (p < 512) return 256 + ((p - 256) >> 1);
  if (p < 1024) return 384 + ((p - 512) >> 2);
  if (p < 2048) return 512 + ((p - 1024) >> 3);
  return 640;
}

template <int CTRL> __device__ __forceinline__ float dppr(float x) {
  return __builtin_bit_cast(float, __builtin_amdgcn_update_dpp(0, __builtin_bit_cast(int, x), CTRL, 0xf, 0xf, false));
}
__device__ __forceinline__ float wave_sum(float v) {
  v += dppr<0x121>(v);
  v += dppr<0x122>(v);
  v += dppr<0x124>(v);
  v += dppr<0x128>(v);
  {
    auto r = __builtin_amdgcn_permlane16_swap(__float_as_uint(v), __float_as_uint(v), false, false);
    v = __uint_as_float(r[0]) + __uint_as_float(r[1]);
  }
  {
    auto r = __builtin_amdgcn_permlane32_swap(__float_as_uint(v), __float_as_uint(v), false, false);
    v = __uint_as_float(r[0]) + __uint_as_float(r[1]);
  }
  return v;
}
__device__ __forceinline__ void ln_row8(float (&v)[8]) {
  float s = 0.f;
#pragma unroll
  for (int j = 0; j < 8; ++j) s += v[j];
  s = wave_sum(s);
  const float mean = s * (1.0f / DM);
  float q = 0.f;
#pragma unroll
  for (int j = 0; j < 8; ++j) { float t = v[j] - mean; q += t * t; }
  q = wave_sum(q);
  const float var = q * (1.0f / DM);
  const float mb = bfr(mean);
  const float sd = bfr(sqrtf(bfr(bfr(var) + 1e-5f)));
#pragma unroll
  for (int j = 0; j < 8; ++j) v[j] = bfr(bfr(v[j] - mb) / sd);
}

#define RLX_AGENT __ATOMIC_RELAXED, __HIP_MEMORY_SCOPE_AGENT
typedef __attribute__((address_space(1))) unsigned gu32;
typedef __attribute__((address_space(3))) unsigned lu32;
typedef unsigned v4u __attribute__((ext_vector_type(4)));
constexpr int AUX_SC1 = 16;
constexpr unsigned SPIN_CAP = 1u << 18;

__device__ __forceinline__ bool wait_flags(const unsigned* flags, int lane, int nfl, unsigned want, unsigned* tmo) {
  for (unsigned spins = 0;; ++spins) {
    unsigned v = want;
    if (lane < nfl) v = __hip_atomic_load((gu32*)flags + lane * 32, RLX_AGENT);
    if (__all(v >= want)) break;
    if (spins > SPIN_CAP) {
      if (lane == 0) __hip_atomic_store((gu32*)tmo, 1u, RLX_AGENT);
      return false;
    }
  }
  __builtin_amdgcn_fence(__ATOMIC_ACQUIRE, "wavefront");
  return true;
}

#define STAMP(i)
#define STAMP_DUMP()
#define WSTAMP(i)
template <int CTRL> __device__ __forceinline__ float dppf(float old, float x) {
  return __builtin_bit_cast(float, __builtin_amdgcn_update_dpp(__builtin_bit_cast(int, old), __builtin_bit_cast(int, x), CTRL, 0xf, 0xf, false));
}
__device__ __forceinline__ float wave_max_dpp(float x) {
  const float ninf = -3e38f;
  x = fmaxf(x, dppf<0x111>(ninf, x));
  x = fmaxf(x, dppf<0x112>(ninf, x));
  x = fmaxf(x, dppf<0x114>(ninf, x));
  x = fmaxf(x, dppf<0x118>(ninf, x));
  x = fmaxf(x, dppf<0x142>(ninf, x));
  x = fmaxf(x, dppf<0x143>(ninf, x));
  return __builtin_bit_cast(float, __builtin_amdgcn_readlane(__builtin_bit_cast(int, x), 63));
}
__device__ __forceinline__ float wave_sum_dpp(float x) {
  x += dppf<0x111>(0.f, x);
  x += dppf<0x112>(0.f, x);
  x += dppf<0x114>(0.f, x);
  x += dppf<0x118>(0.f, x);
  x += dppf<0x142>(0.f, x);
  x += dppf<0x143>(0.f, x);
  return __builtin_bit_cast(float, __builtin_amdgcn_readlane(__builtin_bit_cast(int, x), 63));
}
constexpr int SMS = 528;
constexpr int QS = 144;
constexpr int VS = 80;
constexpr int SOS = 144;
#define VAR_NREP 4
constexpr int NREP = VAR_NREP;
__device__ __forceinline__ float xsum16(float x) {
  auto r = __builtin_amdgcn_permlane16_swap(__float_as_uint(x), __float_as_uint(x), false, false);
  return __uint_as_float(r[0]) + __uint_as_float(r[1]);
}
__device__ __forceinline__ float xsum32(float x) {
  auto r = __builtin_amdgcn_permlane32_swap(__float_as_uint(x), __float_as_uint(x), false, false);
  return __uint_as_float(r[0]) + __uint_as_float(r[1]);
}
__device__ __forceinline__ float xmax16(float x) {
  auto r = __builtin_amdgcn_permlane16_swap(__float_as_uint(x), __float_as_uint(x), false, false);
  return fmaxf(__uint_as_float(r[0]), __uint_as_float(r[1]));
}
__device__ __forceinline__ float xmax32(float x) {
  auto r = __builtin_amdgcn_permlane32_swap(__float_as_uint(x), __float_as_uint(x), false, false);
  return fmaxf(__uint_as_float(r[0]), __uint_as_float(r[1]));
}
__device__ __forceinline__ float red_mod4(float x) {
  x += dppf<0x124>(0.f, x);
  x += dppf<0x128>(0.f, x);
  return xsum32(xsum16(x));
}
typedef __bf16 bf16x2_t __attribute__((ext_vector_type(2)));
typedef float f32x2_t __attribute__((ext_vector_type(2)));
__device__ __forceinline__ unsigned pk2(float a, float b) {
  f32x2_t v = {a, b};
  bf16x2_t r = __builtin_convertvector(v, bf16x2_t);
  return __builtin_bit_cast(unsigned, r);
}
__device__ __forceinline__ float dot2sq(const unsigned x, const float acc) {
  return __builtin_amdgcn_fdot2_f32_bf16(__builtin_bit_cast(bf16x2_t, x), __builtin_bit_cast(bf16x2_t, x), acc, false);
}
__device__ __forceinline__ float lo16(unsigned u) { return __uint_as_float(u << 16); }
__device__ __forceinline__ float hi16(unsigned u) { return __uint_as_float(u & 0xffff0000u); }
__device__ __forceinline__ void scale_pair(float a, float b, float scale, float& oa, float& ob) {
  unsigned u = pk2(a, b);
  unsigned v = pk2(lo16(u) * scale, hi16(u) * scale);
  oa = lo16(v);
  ob = hi16(v);
}

#define XSPLIT 3
__device__ __forceinline__ int role_no() {
  const int b = blockIdx.x, x = b & 7, j = b >> 3;
  if (XSPLIT >= 3) {
    const int y = XSPLIT == 5 ? x & 3 : x >> 1;
    if (XSPLIT >= 6 && (j >> 2) == 6) return (x & 1) ? 104 + 16 * y + j - 16 : 80 + 4 * y + (j & 3);
    if (XSPLIT >= 7 && (j >> 2) == 4) return (x & 1) ? 104 + 16 * y + j - 16 : 64 + 4 * y + (j & 3);
    if (XSPLIT == 5 ? x < 4 : !(x & 1)) {
      if (j >= 16) return 104 + 16 * y + j - 16;
#define HXX 0
      return XSPLIT == 4 ? 16 * y + j : (j & 8) * 4 + ((8 * y + (j & 7)) ^ HXX);
    }
    if (j < 16) return 168 + (j & 8) * 4 + 8 * y + (j & 7);
    const int i = 4 * y + (j & 3);
    return j < 20 ? 64 + i : j < 24 ? (i < 8 ? 96 + i : 224 + i) : j < 28 ? 80 + i : 240 + i;
  }
  const int L = x >> 2, xx = x & 3;
  if (j < 8) return 32 * L + 8 * xx + j;
  if (j < 12) return 64 + 16 * L + 4 * xx + j - 8;
  if (j < 16) { const int i = 4 * xx + j - 12; return L ? 240 + i : i < 8 ? 96 + i : 224 + i; }
  if (j < 24) return 104 + 8 * x + j - 16;
  return 168 + 32 * L + 8 * xx + j - 24;
}
#define MSTAMP()
#define WSTAMP2()
#define SST(k)
#define TSR(rn, k, ss, t)
#define TS(k, ss, t)
#define RST(k)
#define RSD()
#define LST(k)
#define LSC(k)
#define LSD()
#define PSV
#define PST(k)
#define PSD()
#define PSE()
#define FSD
#define FS(k)
#define FD(ss)
struct MegaArgs {
  const u16* mem0;
  const u16* Dskip;
  char* ws;
  u16* out;
  const u16* x0;
  const u16 *logAre, *Aim, *Cre, *Cim, *logdt;
  const u16 *Wq, *Wk, *Wv, *Wo;
  int rn;
};

__device__ __forceinline__ int lane_id() {
  int l;
  asm volatile("v_mbcnt_lo_u32_b32 %0, -1, 0\n\tv_mbcnt_hi_u32_b32 %0, -1, %0" : "=v"(l));
  return l;
}
__device__ __forceinline__ bool spin_timeout(unsigned spins, unsigned* tmo) {
  if (spins > SPIN_CAP) {
    if (lane_id() == 0) __hip_atomic_store((gu32*)tmo, 1u, RLX_AGENT);
    return true;
  }
  return false;
}
#define PSWH (h & 2)
#define RR 0
#define RQM 63
#define RPM 63
#define CD0 100
#define APN 6
#define APL 300
#define APA 75
#define APB 115
#define APP 203
#define CD 27
#define CDS 30
#define CDL 0
#define XG(h) ((h) >> 3)
#define GP 230
#define GR 50
#define GM 0
#define ROT (4 * (h & 1))
#define CPRIO 1
#define P1PRIO 3
#define QLEAD 2
#define POLL_SLEEP 12
#define SLP_C POLL_SLEEP
#define SLP_A POLL_SLEEP
#define SLP_W POLL_SLEEP
#define SLP_R 4
#define SLP_V 4
#define SLP_Q 4
#define SLP_M 4
#define AWPRIO 2
#define SLP_S 8
#define SPOS 4
#define CVPRE 0
#define QHV 0
#define QCHK 0
#define LEADN 99
#define GATE_V 40
#define GATE_R 380
#define GATE_N 440
#define AWAHEAD 1
#define GATE_M 150
#define GATE_M2 380
#define QPACE 0
#define QPS 6
#define QPL 100
#define GATE_Q 150
#define SLP_Q1 24
#define QPF 3
#define QPV 6
#define QVP 3
#define QP0 2
#define QP1 0
#define QFP 15
#define QUP 7
#define QEP 3
#define AWD 10
#define GPER 200
#define GMA 60
#define GMR 135
#define GMS 0
#define GSTEP 30
#define GATE_RT 130
#define GATE_1 160
#define N1S 20
#define N1E 36
#define PRI_A 3
#define PRI_WA 2
#define PRI_WB 1
#define AWSKIP 3
#define PRI_0 3
#define AWDEF 1
#define SLP_F 2
#define EXP_OFF 0
#define AWSPEC 1
#define CVTHR 48
#define NR_C 8
#define NR_R 8
#define NR_W 8
#define NR_N 2
#define QAHEAD 0
#define NS0 4
#define CVST 16
#define CVNB 9
#define CVFULL 1
#define HK3 0
#define AWDT 120
#define AWMO 0
#define HK2 0
#define RDT 185
#define RQ0 4
#define RGW 40
#define ZK 8
#define ZC 7
#define KBR 8
#define AWE 0
#define AWK 5
#define CRD 40
#define CD2 110
#define AWLM 45
#define AWLD 50
#define TXQ 3
#define QN0 6
#define CD4 70
#define SLP_2 2
#define CD1 80
#define SLP_1 8
#define CSL 0
#define QSL 0
#define RSL 0
#define CTH 0
#define WORD 0
#define WXC 0
#define PFS 0
#define XI0 8
#define QFAST 58
#define SLP_G 4
#define TCH 0
#define CSD 0
#define QX2 1
#define CET 0
#define CG1 110
#define IPC 20
#define IPQ 20
#define AWDU 180
#define RTW 0
#define CVLT 120
#define SLP_P 2
#define SLP_N POLL_SLEEP
template <int SLP, int WPL>
__device__ __forceinline__ bool wait_n(const unsigned* f, int n, unsigned* tmo) {
  const int l = lane_id();
  for (unsigned spins = 0;; ++spins) {
    unsigned v = 1u;
    if (l < n) v = __hip_atomic_load((gu32*)f + (l / WPL) * 32 + l % WPL, RLX_AGENT);
    if (__all(v != 0u)) break;
    if (spin_timeout(spins, tmo)) return false;
    __builtin_amdgcn_s_sleep(SLP);
  }
  asm volatile("" ::: "memory");
  return true;
}
__device__ __forceinline__ unsigned now10() { return (unsigned)__builtin_amdgcn_s_memrealtime(); }
__device__ __forceinline__ void gate(const unsigned t) {
#pragma unroll 1
  for (int i = 0; i < 64; ++i) {
    const int d = (int)(t - now10());
    if (d <= 0) break;
    if (d > 100) __builtin_amdgcn_s_sleep(32);
    else if (d > 25) __builtin_amdgcn_s_sleep(8);
    else __builtin_amdgcn_s_sleep(2);
  }
}
template <int SLP, int WPL>
__device__ __forceinline__ unsigned grp_wait(const bool leader, volatile lu32* sw, const unsigned val, const unsigned* f, int n,
                                             unsigned* tmo, const unsigned t) {
  unsigned r;
  gate(t);
  if (leader) {
    const int l = lane_id();
    unsigned v;
    for (unsigned spins = 0;; ++spins) {
      v = __hip_atomic_load((gu32*)f + ((l & 15) / WPL) * 32 + l % WPL, RLX_AGENT);
      if (__all(v != 0u) || spin_timeout(spins, tmo)) break;
      __builtin_amdgcn_s_sleep(SLP);
    }
    int d = (int)(v - t);
    d = max(d, __builtin_amdgcn_update_dpp(0, d, 0x121, 0xf, 0xf, false));
    d = max(d, __builtin_amdgcn_update_dpp(0, d, 0x122, 0xf, 0xf, false));
    d = max(d, __builtin_amdgcn_update_dpp(0, d, 0x124, 0xf, 0xf, false));
    d = max(d, __builtin_amdgcn_update_dpp(0, d, 0x128, 0xf, 0xf, false));
    r = t + __builtin_amdgcn_readfirstlane(d);
    sw[1 + (val & 2)] = r;
    *sw = val;
  } else {
    for (unsigned spins = 0; __builtin_amdgcn_readfirstlane(*sw) < val && spins <= 64u * SPIN_CAP; ++spins) __builtin_amdgcn_s_sleep(SLP_F);
    r = __builtin_amdgcn_readfirstlane(sw[1 + (val & 2)]);
  }
  asm volatile("" ::: "memory");
  return r;
}
template <int SLP, int WPL>
__device__ __forceinline__ bool wg_wait(const unsigned* f, int n, unsigned* tmo) {
  if (threadIdx.x < 64) (void)wait_n<SLP, WPL>(f, n, tmo);
  __syncthreads();
  asm volatile("" ::: "memory");
  return true;
}
__device__ __forceinline__ void set_flag(unsigned* f) { __hip_atomic_store((gu32*)f, 1u, RLX_AGENT); }
#define DRAIN() asm volatile("s_waitcnt vmcnt(0)" ::: "memory")
#define MKRS(ptr, bytes) __builtin_amdgcn_make_buffer_rsrc((void*)(ptr), 0, (int)(bytes), 0x00020000)
typedef unsigned v2u __attribute__((ext_vector_type(2)));
#define LD128(rs, voff, soff) __builtin_amdgcn_raw_buffer_load_b128(rs, voff, soff, AUX_SC1)
#define LD64(rs, voff, soff) __builtin_amdgcn_raw_buffer_load_b64(rs, voff, soff, AUX_SC1)
#define ST128(v, rs, voff, soff)                                              \
  do {                                                                        \
    const v4u st_d_ = (v);                                                    \
    __builtin_amdgcn_raw_buffer_store_b128(st_d_, rs, voff, soff, AUX_SC1);   \
    asm volatile("s_nop 1" ::"v"(st_d_) : "memory");                          \
  } while (0)
#define ST64(v, rs, voff, soff) __builtin_amdgcn_raw_buffer_store_b64(v, rs, voff, soff, AUX_SC1)
#define BF8(x) __builtin_bit_cast(bf16x8, x)
#define MFMA16(a, b, c) __builtin_amdgcn_mfma_f32_16x16x32_bf16(a, b, c, 0, 0, 0)
typedef short bf16x4 __attribute__((ext_vector_type(4)));
#define MFMA16K(a, b, c) \
  __builtin_amdgcn_mfma_f32_16x16x16bf16_1k(__builtin_bit_cast(bf16x4, a), __builtin_bit_cast(bf16x4, b), c, 0, 0, 0)
__device__ __forceinline__ float fmax_nn(float a, float b) { return __builtin_elementwise_maximum(a, b); }
typedef __bf16 bfv8 __attribute__((ext_vector_type(8)));
typedef __bf16 bfv16 __attribute__((ext_vector_type(16)));
typedef unsigned v8u __attribute__((ext_vector_type(8)));
#define SMF(a, b, c, idx) \
  __builtin_amdgcn_smfmac_f32_16x16x64_bf16(__builtin_bit_cast(bfv8, a), __builtin_bit_cast(bfv16, b), c, idx, 0, 0)
__device__ __forceinline__ float swap_add32(float a, float b) {
  auto r = __builtin_amdgcn_permlane32_swap(__float_as_uint(a), __float_as_uint(b), false, false);
  return __uint_as_float(r[0]) + __uint_as_float(r[1]);
}
__device__ __forceinline__ float swap_add16(float a, float b) {
  auto r = __builtin_amdgcn_permlane16_swap(__float_as_uint(a), __float_as_uint(b), false, false);
  return __uint_as_float(r[0]) + __uint_as_float(r[1]);
}
__device__ __forceinline__ v4u smf_fix(const v4u d) {
  auto x = __builtin_amdgcn_permlane16_swap(d.x, d.z, false, false);
  auto y = __builtin_amdgcn_permlane16_swap(d.y, d.w, false, false);
  return v4u{x[0], x[1], y[0], y[1]};
}
__device__ __forceinline__ v8u smf_bfrag(const u16* p) {
  const v2u a = *reinterpret_cast<const v2u*>(p), b = *reinterpret_cast<const v2u*>(p + 8);
  const v2u c = *reinterpret_cast<const v2u*>(p + 32), d = *reinterpret_cast<const v2u*>(p + 40);
  return v8u{a.x, b.x, a.y, b.y, c.x, d.x, c.y, d.y};
}

#define VAR_PPA 0
#define VAR_PPB 0
#define VAR_THR 0
#define VAR_THR2 0
#define CV_LEAD 100
#define VAR_EARLY 1
#define VAR_DRAIN 1
#define VAR_D1 0
#define VAR_D2 0
#define VAR_DR2 1
#define VAR_S1 0
#define VAR_S2 0
typedef __attribute__((address_space(1))) unsigned long long gu64;
typedef __attribute__((address_space(1))) char gch;
__device__ __forceinline__ int cvt_rpi(float x) {
  int r;
  asm("v_cvt_rpi_i32_f32 %0, %1" : "=v"(r) : "v"(x));
  return r;
}
typedef short v4s __attribute__((ext_vector_type(4)));
typedef __attribute__((address_space(3))) v4s* lp4s;
#define WNT 0
#define WPOL ""
__device__ __forceinline__ void glds16(const void* sbase, const unsigned voff, const unsigned lds) {
  unsigned keep;
  asm volatile("s_nop 4\n\ts_mov_b32 %0, m0\n\ts_mov_b32 m0, %3\n\ts_nop 0\n\tglobal_load_lds_dwordx4 %1, %2" WPOL "\n\ts_mov_b32 m0, %0"
               : "=&s"(keep)
               : "v"(voff), "s"(sbase), "s"(lds)
               : "memory");
}
__device__ __forceinline__ void glds16c(const void* sbase, const unsigned voff, const unsigned lds) {
  unsigned keep;
  asm volatile("s_nop 4\n\ts_mov_b32 %0, m0\n\ts_mov_b32 m0, %3\n\ts_nop 0\n\tglobal_load_lds_dwordx4 %1, %2 sc1\n\ts_mov_b32 m0, %0"
               : "=&s"(keep)
               : "v"(voff), "s"(sbase), "s"(lds)
               : "memory");
}
__device__ __forceinline__ v2u trd(const unsigned a) {
  return __builtin_bit_cast(v2u, __builtin_amdgcn_ds_read_tr16_b64_v4i16((lp4s)(unsigned long)a));
}
__device__ __forceinline__ unsigned lds_base(char* smem) { return (unsigned)(unsigned long)(__attribute__((address_space(3))) char*)smem; }
#define RAW_BAR()                                           \
  do {                                                      \
    asm volatile("s_waitcnt lgkmcnt(0)" ::: "memory");      \
    __builtin_amdgcn_s_barrier();                           \
    asm volatile("" ::: "memory");                          \
  } while (0)
#define WAIT_VM(n) asm volatile("s_waitcnt vmcnt(" #n ")" ::: "memory")
#define TOURS 7
#define MARK() asm volatile(".Lmark%=:" ::)
template <int ID, int MX = 0, class T = int>
__device__ __forceinline__ void pad(T&& x = 0) {
  if (TOURS && MX) asm volatile("s_branch 1f\n\t.p2alignl 12, 0xbf800000, %2\n.Lpad%1:\n\ts_setpc_b64 s[98:99]\n1:" : "+v"(x) : "n"(ID), "n"(MX));
  else if (TOURS) asm volatile("s_cmp_eq_u32 s0, 0xbe801d62\n\t.set .Lpad%1, . - 4" : "+v"(x) : "n"(ID) : "scc");
}
template <int ID>
__device__ __forceinline__ void hop() { asm volatile("s_call_b64 s[98:99], .Lpad%0" ::"n"(ID) : "s98", "s99"); }
template <int B, int E, int D>
__device__ __forceinline__ void hops() {
  if constexpr (B < E) { hop<B>(); hops<B + D, E, D>(); }
}
template <int B, int E, int D, int I = 0>
__device__ __forceinline__ void tour(const int w) {
  if constexpr ((TOURS & 2) && I < D) {
    if ((w & (D - 1)) == I) hops<B + I, E, D>();
    else tour<B, E, D, I + 1>(w);
  }
}
template <bool SP>
__device__ __forceinline__ void w_issue(const u16* Wm, const int j, const int b, const int i, const int h, const int w, const int l, const unsigned sb) {
  const char* base = (const char*)Wm + (size_t)(128 * j + 16 * w) * (HD * 2) + h * (DH * 2);
  const int r = l >> 4;
  const int f = SP ? ((r & 1) | ((i & 1) << 1) | (((i >> 1) & 1) << 2)) : (r | (((i >> 1) & 1) << 2));
  glds16(base, (unsigned)((4 * i + r) * (HD * 2) + (((l & 15) ^ (f << 1)) << 4)), sb + w * 16384 + b * 4096 + i * 1024);
}
#define WM(k) (WORD ? (k) % 3 : (k) >> 2)
#define WJ(k) (WORD ? (k) / 3 : (k) & 3)
template <int N>
__device__ __forceinline__ void wait_vm() { asm volatile("s_waitcnt vmcnt(%0)" ::"n"(N) : "memory"); }

#define PFW 1
#define IPF 1
#define PN1 1
#define PNH 20
#define PWO 1
__device__ __forceinline__ void pull(const unsigned long long b, const unsigned voff, char* smem) {
  unsigned keep;
  asm volatile("s_nop 4\n\ts_mov_b32 %0, m0\n\ts_mov_b32 m0, %3\n\ts_nop 0\n\tglobal_load_lds_dword %1, %2\n\ts_mov_b32 m0, %0"
               : "=&s"(keep)
               : "v"(voff), "s"(b), "s"(lds_base(smem) + 162816)
               : "memory");
}
__device__ __forceinline__ unsigned long long matrix(const MegaArgs& A, const int k) {
  unsigned long long pv = (unsigned long long)A.Wv, pk = (unsigned long long)A.Wk, pq = (unsigned long long)A.Wq;
  asm volatile("" : "+s"(pv), "+s"(pk), "+s"(pq));
  return k == 0 ? pv : k == 1 ? pk : pq;
}
__device__ __forceinline__ void pull_rows(const MegaArgs& A, char* smem, const int i) {
  const int l = lane_id(), y = (blockIdx.x & 7) >> 1;
  const unsigned long long m = matrix(A, i >> 7);
  if (PWO && i >= 384) pull((unsigned long long)A.Wo + ((8 * y + (i & 7)) * 16 + ((i & 127) >> 3)) * 8192, l * 128, smem);
  else pull(m + (i & 127) * (8 * HD) + y * 2048, (l >> 4) * (HD * 2) + (l & 15) * 128, smem);
}
__device__ __forceinline__ void wo_issue(const u16* Wo, const int a, const int st, const int w, const int i, const unsigned sb) {
  const int l = lane_id(), c = l & 15, g = l >> 4, fd = g | ((i & 1) << 2);
  const char* base = (const char*)Wo + ((size_t)((2 * a + (st >> 2)) * DH + 64 * (w & 1) + 16 * (st & 3)) * DM + 128 * (w >> 1)) * 2;
  glds16(base, (unsigned)((4 * i + g) * (DM * 2) + ((c ^ (fd << 1)) << 4)), sb + (st % 5) * 32768 + w * 4096 + i * 1024);
}
__device__ __forceinline__ bool zero_seen(const v4u t, const v2u e) {
  const unsigned long long m = (((unsigned long long)e.y << 32) | e.x) + TAG_STEP;
  const unsigned lo = (unsigned)m, hi = (unsigned)(m >> 32);
  return __all(((t.x ^ lo) | (t.y ^ hi) | (t.z ^ lo) | (t.w ^ hi)) == 0u);
}
__device__ __forceinline__ void zero_wait(char* ws) {
  const __amdgpu_buffer_rsrc_t rs = MKRS(ws + OFF_TAG, 8192 + 8);
#pragma unroll 1
  for (unsigned spins = 0; spins < 16u * SPIN_CAP; ++spins) {
    const v4u t = LD128(rs, lane_id() * 16, (blockIdx.x & 7) * 1024);
    const v2u e = LD64(rs, 0, 8192);
    if (zero_seen(t, e)) break;
    asm volatile("s_sleep 4" ::: "memory");
  }
  asm volatile("" ::: "memory");
}
__device__ __forceinline__ f32x2_t row_sums(const v4u raw) {
  float v[8];
#pragma unroll
  for (int e = 0; e < 4; ++e) { v[2 * e] = lo16(raw[e]); v[2 * e + 1] = hi16(raw[e]); }
  float s = 0.f;
#pragma unroll
  for (int e = 0; e < 8; ++e) s += v[e];
  const float mean = wave_sum(s) * (1.0f / DM);
  float q = 0.f;
#pragma unroll
  for (int e = 0; e < 8; ++e) { const float t = v[e] - mean; q += t * t; }
  return f32x2_t{mean, wave_sum(q)};
}
__device__ __forceinline__ unsigned row_stats(const f32x2_t s) {
  const float sd = bfr(sqrtf(bfr(bfr(s.y * (1.0f / DM)) + 1e-5f)));
  return (unsigned)f2bf(s.x) | ((unsigned)f2bf(sd) << 16);
}
__device__ __forceinline__ void sincos1(const float x, float* sn, float* cs) {
  const float t = __builtin_amdgcn_sinf(x * 0.07957747155f);
  *sn = __builtin_amdgcn_sinf(x * 0.1591549431f);
  *cs = __builtin_fmaf(-2.0f * t, t, 1.0f);
}
#define RUN_START                                                                                                   \
  const f32x2_t wra = {wr[0], wr[1]}, wia = {wi[0], wi[1]}, wrb = {wr[2], wr[3]}, wib = {wi[2], wi[3]};             \
  const f32x2_t pa = wra + wra, pb = wrb + wrb, qa = wra * wra + wia * wia, qb = wrb * wrb + wib * wib;             \
  f32x2_t ya = {yr0[0], yr0[1]}, yb = {yr0[2], yr0[3]};                                                             \
  f32x2_t za = ya * wra - f32x2_t{yi0[0], yi0[1]} * wia, zb = yb * wrb - f32x2_t{yi0[2], yi0[3]} * wib;
#define RUN_STEP                                                                                                    \
  {                                                                                                                 \
    const f32x2_t na = __builtin_elementwise_fma(pa, za, -(qa * ya)), nb = __builtin_elementwise_fma(pb, zb, -(qb * yb)); \
    ya = za; yb = zb; za = na; zb = nb;                                                                             \
  }
__device__ __forceinline__ unsigned pull_text(const int slot) {
  unsigned long long pc;
  int back, len;
  asm volatile("s_getpc_b64 %0\n0:\n\ts_mov_b32 %1, 0b-.Lcode_at\n\ts_max_i32 %2, 0, .Lcode_end-.Lcode_at" : "=s"(pc), "=s"(back), "=s"(len)::"scc");
  pc -= (long long)back;
  unsigned r;
  asm volatile("s_nop 4\n\tbuffer_load_dword %0, %1, %2, 0 offen" : "=v"(r) : "v"(lane_id() * 64 + slot * 4096),
               "s"(v4u{(unsigned)pc, (unsigned)(pc >> 32) & 0xffffu, (unsigned)len, 0x00020000u}) : "memory");
  return r;
}
__device__ __forceinline__ f32x2_t cmul(const f32x2_t z, const f32x2_t e) {
  return __builtin_elementwise_fma(f32x2_t{z.y, z.x}, f32x2_t{-e.y, e.y}, z * e.x);
}
__device__ __forceinline__ void put_row(v2u* p, f32x2_t (&b)[2], const f32x2_t (&z)[2], const f32x2_t (&m)[2]) {
  unsigned h0, h1, l0, l1, t0, t1;
  f32x2_t n0, n1;
  asm("v_cvt_pk_bf16_f32 %0, %8, %9\n\tv_cvt_pk_bf16_f32 %1, %10, %11\n\tv_dot2_f32_bf16 %2, %0, %16, %8\n\tv_dot2_f32_bf16 %4, %0, %17, %9\n\t"
      "v_dot2_f32_bf16 %3, %1, %16, %10\n\tv_dot2_f32_bf16 %5, %1, %17, %11\n\tv_pk_mul_f32 %6, %12, %14 op_sel_hi:[1,0]\n\tv_pk_mul_f32 %7, %13, %15 op_sel_hi:[1,0]\n\t"
      "v_pk_fma_f32 %6, %12, %14, %6 op_sel:[1,1,0] op_sel_hi:[0,1,1] neg_lo:[0,1,0]\n\tv_cvt_pk_bf16_f32 %2, %2, %4\n\t"
      "v_pk_fma_f32 %7, %13, %15, %7 op_sel:[1,1,0] op_sel_hi:[0,1,1] neg_lo:[0,1,0]\n\tv_cvt_pk_bf16_f32 %3, %3, %5"
      : "=&v"(h0), "=&v"(h1), "=&v"(l0), "=&v"(l1), "=&v"(t0), "=&v"(t1), "=&v"(n0), "=&v"(n1)
      : "v"(b[0].x), "v"(b[0].y), "v"(b[1].x), "v"(b[1].y), "v"(z[0]), "v"(z[1]), "v"(m[0]), "v"(m[1]), "s"(0x0000bf80u), "s"(0xbf800000u));
  p[0] = v2u{h0, h1};
  p[64] = v2u{l0, l1};
  b[0] = n0;
  b[1] = n1;
}
__device__ __forceinline__ void start_kgen(const MegaArgs& A, char* smem, const int hi, const int xi, const int aw) {
  const int tid = threadIdx.x;
  const int w = __builtin_amdgcn_readfirstlane(tid >> 6), l = tid & 63, d = 4 * hi + (w & 3);
  const unsigned t0 = AWE ? now10() : 0u;
  PSV;
  PST(0);
  v4u raw[8], ra, rb;
  unsigned txt = 0u;
  if (IPF) {
    unsigned long long pc;
    int len;
    asm volatile("s_getpc_b64 %0\n.Lcode_at:\n\ts_max_i32 %1, 0, .Lcode_end-.Lcode_at" : "=s"(pc), "=s"(len)::"scc");
    const unsigned slot = ((blockIdx.x >> 3) - 8) + 8 * w;
    if ((blockIdx.x >> 6) == 1) txt = __builtin_amdgcn_raw_buffer_load_b32(MKRS(pc, len), l * 64 + slot * 4096, 0, 0);
  }
  const int r0 = 16 * xi + 2 * w, sx = 2 * ((hi + 96) & 127) + w - 6;
  if (xi >= 0) {
    ra = *reinterpret_cast<const v4u*>(A.x0 + (size_t)r0 * DM + l * 8);
    rb = *reinterpret_cast<const v4u*>(A.x0 + (size_t)(r0 + 1) * DM + l * 8);
  }
  const int pn = w < 4 ? DM * NST * 2 : 0;
  const unsigned p1 = __builtin_amdgcn_raw_buffer_load_b32(MKRS(A.logAre, pn), l * 4, d * NST * 2, 0), p2 = __builtin_amdgcn_raw_buffer_load_b32(MKRS(A.Aim, pn), l * 4, d * NST * 2, 0),
                 p3 = __builtin_amdgcn_raw_buffer_load_b32(MKRS(A.Cre, pn), l * 4, d * NST * 2, 0), p4 = __builtin_amdgcn_raw_buffer_load_b32(MKRS(A.Cim, pn), l * 4, d * NST * 2, 0);
  const u16 p0 = A.logdt[d];
  if (w >= 6) {
#pragma unroll
    for (int i = 0; i < 8; ++i) raw[i] = *reinterpret_cast<const v4u*>(A.x0 + (size_t)(8 * sx + i) * DM + l * 8);
  }
  const v2u en = LD64(MKRS(A.ws + OFF_TAG, 8192 + 8), 0, 8192);
  {
    const __amdgpu_buffer_rsrc_t rz = MKRS(A.ws + OFF_Z, Z_BYTES);
    const unsigned i0 = hi < 16 ? ACC_LO : 0u, i1 = hi < 16 ? ACC_HI : 0u;
    ST128((v4u{i0, i1, i0, i1}), rz, tid * 16, hi * 8192);
    ST128((v4u{0u, 0u, 0u, 0u}), rz, tid * 16, (128 + hi) * 8192);
  }
  if (xi >= 0) {
    const f32x2_t sa = row_sums(ra), sb = row_sums(rb);
    const unsigned st = row_stats((l & 1) ? sb : sa);
    const unsigned long long m = (((unsigned long long)en.y << 32) | en.x) + TAG_STEP;
    ST128((v4u{st, (unsigned)m, st, (unsigned)(m >> 32)}), MKRS(A.ws + OFF_G0, 8192), (l >> 1) * 2048 + (l & 1) * 16, r0 * 16);
    SST(11);
  }
  const int pj = (blockIdx.x >> 3) - 8;
  const bool ps = PFW && w < 6 && pj < PNH;
  if (__builtin_expect(ps, 1)) {
    const bool sl = hi < 32;
    const unsigned voff = sl ? (l >> 2) * (HD * 2) + (l & 2) * 512 + (l & 1) * 128 : (l >> 4) * (HD * 2) + (l & 15) * 128;
#pragma unroll
    for (int e = 0; e < 2; ++e) {
      const int i = sl ? 4 * (pj + 8 * w + 48 * e) : pj + PNH * (w + 6 * e);
      const unsigned long long m = matrix(A, i >> 7);
      if (e < PN1 || __builtin_expect(sl, 1))
        pull(m + (i & 127) * (8 * HD) + (blockIdx.x & 6) * 1024 + (sl ? ((1 - (int)(m >> 8)) & 3) * 256 : 0), voff, smem);
    }
  }
  asm volatile("" ::"v"(txt), "v"(p0), "v"(p1), "v"(p2), "v"(p3), "v"(p4) : "memory");
#define CLEARED() do { if (!ps) WAIT_VM(0); else if (hi < 32) WAIT_VM(2); else wait_vm<PN1>(); __builtin_amdgcn_s_barrier(); } while (0)
  if (w >= 4) {
    tour<208, 220, 4>(w);
    PST(1);
    CLEARED();
    PST(2);
  }
  if (w >= 6) {
    __builtin_amdgcn_s_waitcnt(0x0F70);
    if (w == 7 && l < 8)
      __hip_atomic_store((gu64*)(A.ws + OFF_TAG) + l * 128 + hi, (((unsigned long long)en.y << 32) | en.x) + TAG_STEP, RLX_AGENT);
    __builtin_amdgcn_s_setprio(3);
    PST(8);
    v4u wr[32];
    const u16* wop = A.Wo + (size_t)(hi * DH + 64 * (w - 6)) * DM + l * 8;
    if (hi < 32) {
#pragma unroll
      for (int i = 0; i < 32; ++i) wr[i] = *reinterpret_cast<const v4u*>(wop + i * DM);
    }
    f32x2_t sq = {};
#pragma unroll
    for (int i = 0; i < 8; ++i) {
      if (i == 0) pad<203, 1000>(raw[i]);
      if (i == 3) { asm volatile("" : "+v"(sq), "+v"(raw[i])); pad<206>(sq); }
      if (i == 6) { asm volatile("" : "+v"(sq), "+v"(raw[i])); pad<207, 300>(sq); }
      const f32x2_t t = row_sums(raw[i]);
      if (l == i) sq = t;
    }
    pad<218, 500>(sq);
    const unsigned stw = row_stats(sq);
    if (l < 8) __hip_atomic_store((gu32*)(A.ws + OFF_GT) + 8 * sx + l, stw, RLX_AGENT);
    pad<216>();
    PST(9);
    float m = 0.f;
    if (hi < 32) {
      float c0 = 0.f, c1 = 0.f, c2 = 0.f, c3 = 0.f;
#pragma unroll
      for (int i = 0; i < 64; ++i) {
        const v4u x = wr[i & 31];
        if (i < 32) wr[i] = *reinterpret_cast<const v4u*>(wop + (32 + i) * DM);
        if (i == 20) pad<204, 600>(c0);
        c0 = dot2sq(x.x, c0); c1 = dot2sq(x.y, c1); c2 = dot2sq(x.z, c2); c3 = dot2sq(x.w, c3);
      }
      pad<205>(c0);
      m = wave_max_dpp(fmaxf(fmaxf(c0, c1), fmaxf(c2, c3)));
    }
    zero_wait(A.ws);
    DRAIN();
    PST(10);
    if (l < 4) {
      if (hi < 32) __hip_atomic_store((gu32*)(A.ws + OFF_FL) + FL_G + (l * 4 + w - 5) * 32 + hi, __float_as_uint(m) | 1u, RLX_AGENT);
      set_flag((unsigned*)(A.ws + OFF_FL) + FL_FS + (l * 32 + (sx >> 3)) * 32 + (sx & 7));
    }
    __builtin_amdgcn_s_setprio(0);
  } else if (w >= 4) {
    pad<202>();
    __builtin_amdgcn_s_setprio(3);
    if (PFW && hi >= 32 && pj < PNH)
      for (int i = pj + PNH * (6 * PN1 + w - 4); i < (PWO ? 512 : 384); i += 2 * PNH) pull_rows(A, smem, i);
    __builtin_amdgcn_s_setprio(0);
    PST(8);
    if (aw >= 0) {
      pad<219>();
      if (AWE) gate(t0 + AWE);
#pragma unroll 1
      for (int v = 0; v < 8; ++v)
#pragma unroll
        for (int i = 0; i < 4; ++i) wo_issue(A.Wo, aw, w & 1, v, i, lds_base(smem));
    }
    DRAIN();
  } else {
    pad<201, 300>();
    f32x2_t a[2], ew[3][2];
    const float dt = bfr(expf(bf2f(p0)));
#pragma unroll
    for (int e = 0; e < 2; ++e) {
      const float ar = -bfr(expf(e ? hi16(p1) : lo16(p1))) * dt, ai = (e ? hi16(p2) : lo16(p2)) * dt;
      const float th = ai * 0.15915494f, tl = __builtin_fmaf(ai, 0.15915494f, -th) + ai * 6.4206383e-9f;
      a[e] = f32x2_t{(e ? hi16(p3) : lo16(p3)) * dt, (e ? hi16(p4) : lo16(p4)) * dt} * 2.0f;
#pragma unroll
      for (int k = 0; k < 3; ++k) {
        const float st = k == 0 ? 1.0f : k == 1 ? -8.0f : 128.0f;
        const float ex = __expf(ar * fabsf(st)), t = __builtin_fmaf(tl, st, __builtin_amdgcn_fractf(th * st));
        ew[k][e] = f32x2_t{__builtin_amdgcn_cosf(t), __builtin_amdgcn_sinf(t)} * ex;
      }
    }
    pad<217, 300>(a[0]);
    PST(1);
    const int c = l & 15, g = l >> 4;
    v2u* wr = reinterpret_cast<v2u*>(smem + 65536 + w * 16640) + l;
    const bf16x8* rd = reinterpret_cast<const bf16x8*>(smem + 65536 + w * 16640) + 65 * c + g;
    unsigned* kd = reinterpret_cast<unsigned*>(smem + 139264) + w * 516;
    f32x2_t b[2] = {ew[1][0], ew[1][1]};
    wr[0] = v2u{0x3f80u, 0x3f80u};
    wr[64] = v2u{0u, 0u};
#pragma unroll
    for (int n = 1; n < 16; ++n) {
      if (n < 15) put_row(wr + 130 * n, b, b, ew[1]);
      else put_row(wr + 130 * n, b, a, ew[2]);
      if (n == 8) pad<208, 200>(b[0]);
      if (n == KBR) { PST(7); CLEARED(); PST(2); }
    }
    pad<209, 200>(b[0]);
    bf16x8 tp[16];
#pragma unroll
    for (int i = 0; i < 16; ++i) tp[i] = rd[4 * i];
#pragma unroll
    for (int t = 0; t < 2; ++t) {
#pragma unroll
      for (int n = 0; n < 16; ++n) {
        const int cl = 2 * t + (n >> 3), mk = (8 >> cl) - 1;
        if (!(n & mk)) { a[0] = b[0]; a[1] = b[1]; }
        if (!(n & 7) && (cl == 1 || cl == 2)) { ew[0][0] = cmul(ew[0][0], ew[0][0]); ew[0][1] = cmul(ew[0][1], ew[0][1]); }
        if ((n + 1) & mk) put_row(wr + 130 * n, b, b, ew[0]);
        else put_row(wr + 130 * n, b, a, ew[2]);
        if (t == 0 && n == 6) pad<210, 200>(b[0]);
        if (t == 0 && n == 13) pad<211, 200>(b[0]);
        if (t == 1 && n == 0) pad<212, 200>(b[0]);
        if (t == 1 && n == 9) pad<213, 420>(b[0]);
      }
      bf16x8 x[16];
#pragma unroll
      for (int i = 0; i < 16; ++i) x[i] = rd[4 * i];
      __builtin_amdgcn_sched_barrier(0);
      f32x4 s0 = {}, s1 = {}, s2 = {};
      if (t == 1) pad<214, 200>(s0);
#pragma unroll
      for (int i = 0; i < 8; ++i) {
        s0 = MFMA16(tp[i], x[i], s0);
        s1 = MFMA16(tp[8 + i], x[i], s1);
        s2 = MFMA16(tp[i], x[8 + i], s2);
      }
      const int cl = 2 * t + (c >> 3), m = c & 7;
      const int ix = 128 * cl + ((m >> (3 - cl)) << (7 - cl)) + (m & ((8 >> cl) - 1)) + (g << (5 - cl));
#pragma unroll
      for (int r = 0; r < 4; ++r) {
        const float v = s0[r] + (s1[r] + s2[r]);
        kd[ix + (r << (3 - cl))] = pk2(v, v - lo16(pk2(v, v)));
      }
    }
    const float z = wave_sum_dpp(b[0].x + b[1].x);
    if (l == 0) kd[512] = pk2(z, z - lo16(pk2(z, z)));
    PST(4);
    const __amdgpu_buffer_rsrc_t rs = MKRS(A.ws + OFF_KF + d * (4 * CKN), 1960 * 2), rt = MKRS(A.ws + OFF_KF + d * (4 * CKN) + 2 * CKN, 1960 * 2);
    const unsigned kb = lds_base(smem) + 139264 + w * 2064 - 0x4300 * 4;
    float lag = (float)(2080 - 8 * l);
    pad<215, 800>(lag);
#pragma unroll 1
    for (int i = 0; i < 2; ++i) {
      unsigned k0[8], k1[8];
#pragma unroll
      for (int e = 0; e < 8; ++e) {
        const float f = lag - (float)(512 * (e >> 2) + 2 * (e & 3));
        const unsigned pb = pk2(f, f - 1.0f);
        unsigned a0, a1;
        asm("v_mad_u32_u16 %0, %2, 4, %3\n\tv_mad_u32_u16 %1, %2, 4, %3 op_sel:[1,0,0,0]" : "=&v"(a0), "=v"(a1) : "v"(pb), "s"(kb));
        k0[e] = *(lu32*)(unsigned long)a0;
        k1[e] = *(lu32*)(unsigned long)a1;
      }
#pragma unroll
      for (int q = 0; q < 2; ++q) {
        v4u oh, ol;
#pragma unroll
        for (int e = 0; e < 4; ++e) {
          oh[e] = __builtin_amdgcn_perm(k1[4 * q + e], k0[4 * q + e], 0x05040100u);
          ol[e] = __builtin_amdgcn_perm(k1[4 * q + e], k0[4 * q + e], 0x07060302u);
        }
        ST128(oh, rs, 16 * l + 2048 * i + 1024 * q, 0);
        ST128(ol, rt, 16 * l + 2048 * i + 1024 * q, 0);
      }
      lag -= 1024.0f;
    }
    PST(5);
    zero_wait(A.ws);
    DRAIN();
    if (l == 0) set_flag((unsigned*)(A.ws + OFF_FL) + FL_KG + (hi >> 1) * 32 + (d & 7));
  }
  PST(3);
  RAW_BAR();
  PST(6);
  PSE();
  pad<200, 700>();
  SST(3);
}

constexpr int C_SM = 131072, C_SO = C_SM + 8192, C_KT = C_SO + 2048, C_X = C_KT + 16384, C_SQ = C_X + 1024;
constexpr int C_SK = C_SQ + 2432;
static_assert(C_SK + 8 * QS * 2 <= 163808, "lds");
#define CHAIN_ASM_ATT \
  "s_mov_b32 s66, %[d0]\n" \
  "s_mov_b32 s94, 576\n" \
  "s_mov_b32 s95, 288\n" \
  "s_mov_b32 s84, 0x43400000\n" \
  "s_mov_b32 s85, 0x43400000\n" \
  "s_mov_b32 s92, 0x88000000\n" \
  "s_mov_b32 s93, 0x87fffff7\n" \
  "s_mov_b32 s78, 0x3e0293ee\n" \
  "s_mov_b32 s76, 0x33333333\n" \
  "s_mov_b32 s77, 0x33333333\n" \
  "s_add_u32 s58, s56, s60\n" \
  "s_addc_u32 s59, s57, 0\n" \
  "s_xor_b32 s38, s35, 0\n" \
  "s_add_u32 s26, s58, s38\n" \
  "s_addc_u32 s27, s59, 0\n" \
  "s_xor_b32 s38, s35, 1024\n" \
  "s_add_u32 s28, s58, s38\n" \
  "s_addc_u32 s29, s59, 0\n" \
  "s_xor_b32 s38, s35, 2048\n" \
  "s_add_u32 s30, s58, s38\n" \
  "s_addc_u32 s31, s59, 0\n" \
  "s_xor_b32 s38, s35, 3072\n" \
  "s_add_u32 s32, s58, s38\n" \
  "s_addc_u32 s33, s59, 0\n" \
  "s_lshr_b32 s98, s60, 2\n" \
  "s_add_u32 s98, s98, s74\n" \
  "s_add_u32 s98, s98, 131072\n" \
  "s_lshl_b32 s97, s73, 5\n" \
  "s_add_u32 s97, s97, s74\n" \
  "s_add_u32 s97, s97, 161152\n" \
  "s_lshl_b32 s99, s73, 10\n" \
  "s_mov_b32 s65, 9\n" \
  "v_mbcnt_lo_u32_b32 v48, -1, 0\n" \
  "v_mbcnt_hi_u32_b32 v48, -1, v48\n" \
  "v_and_b32 v49, 15, v48\n" \
  "v_lshl_add_u32 v9, v49, 1, s97\n" \
  "v_lshrrev_b32 v49, 4, v48\n" \
  "v_mad_u32_u24 v9, v49, s94, v9\n" \
  "v_add_u32 v7, s99, v1\n" \
  "v_mov_b64 v[12:13], 0\n" \
  "v_mov_b64 v[14:15], 0\n" \
  "v_mov_b64 v[16:17], 0\n" \
  "v_mov_b64 v[18:19], 0\n" \
  "v_mov_b64 v[20:21], 0\n" \
  "v_mov_b64 v[22:23], 0\n" \
  "s_lshl_b32 s88, s73, 10\n" \
  "v_add_u32 v24, s88, v1\n" \
  "ds_read_b128 v[32:35], v24 offset:0\n" \
  "s_waitcnt lgkmcnt(0)\n" \
  "v_mov_b32 v45, 0\n" \
  "v_dot2c_f32_bf16 v45, v32, v32\n" \
  "v_dot2c_f32_bf16 v45, v33, v33\n" \
  "v_dot2c_f32_bf16 v45, v34, v34\n" \
  "v_dot2c_f32_bf16 v45, v35, v35\n" \
  "s_add_u32 s88, s74, 157760\n" \
  "v_mov_b32 v44, s88\n" \
  "s_nop 0\n" \
  "v_cmpx_lt_f32 vcc, 2.0, v45\n" \
  "ds_max_u32 v44, v45\n" \
  "s_mov_b64 exec, -1\n" \
  "s_branch .LCA_AL_%=\n" \
  ".p2alignl 12, 0xbf800000, 3600\n" \
  ".p2align 6\n" \
  ".Lpad0x6d:\n" \
  "s_setpc_b64 s[98:99]\n" \
  ".p2align 6\n" \
  ".LCA_AL_%=:\n" \
  ".LCA_TOP_%=:\n" \
  "s_waitcnt lgkmcnt(0)\n" \
  "s_barrier\n" \
  "ds_read_b32 v8, v1 offset:26688\n" \
  "s_cmp_lg_u32 s73, s65\n" \
  "s_cbranch_scc1 .LCA_NOTK_%=\n" \
  "s_memrealtime s[38:39]\n" \
  "v_mov_b32 v26, 1\n" \
  "s_mul_i32 s23, s71, %[pp]\n" \
  "s_lshr_b32 s62, s66, 4\n" \
  "s_mov_b64 exec, 1\n" \
  "s_waitcnt lgkmcnt(0)\n" \
  "s_mov_b32 s67, s38\n" \
  "s_add_u32 s38, s38, s62\n" \
  "v_mov_b32 v25, s38\n" \
  "buffer_atomic_umax v25, off, s[16:19], s68\n" \
  "buffer_atomic_add v26, off, s[16:19], s68 offset:64\n" \
  "v_add_u32 v27, s23, v25\n" \
  "buffer_store_dword v27, off, s[16:19], s69 sc1\n" \
  "s_mov_b64 exec, -1\n" \
  "s_branch .LCA_SIDE_%=\n" \
  ".LCA_NOTK_%=:\n" \
  "s_cmp_lg_u32 s73, 0\n" \
  "s_cbranch_scc1 .LCA_NOZ_%=\n" \
  "v_mov_b64 v[26:27], s[92:93]\n" \
  "s_xor_b32 s88, s60, 0x8000\n" \
  "s_add_u32 s88, s88, s61\n" \
  "buffer_store_dwordx2 v[26:27], v5, s[40:43], s88 offen sc1\n" \
  ".LCA_NOZ_%=:\n" \
  "s_nop 7\n" \
  ".LCA_SIDE_%=:\n" \
  "s_waitcnt lgkmcnt(0)\n" \
  "v_readfirstlane_b32 s86, v8\n" \
  "s_mov_b64 vcc, s[76:77]\n" \
  "s_lshr_b32 s86, s86, 23\n" \
  "s_add_i32 s86, s86, s83\n" \
  "s_ashr_i32 s86, s86, 1\n" \
  "s_max_i32 s86, s86, s82\n" \
  "s_min_i32 s86, s86, 100\n" \
  "s_lshl_b32 s86, s86, 23\n" \
  "s_add_i32 s75, s86, 0x3c800000\n" \
  "s_sub_u32 s38, s71, 1\n" \
  "s_cmp_ge_u32 s38, 62\n" \
  "ds_read_b128 v[32:35], v1 offset:0\n" \
  "ds_read_b128 v[36:39], v1 offset:1024\n" \
  "ds_read_b128 v[48:51], v1 offset:2048\n" \
  "ds_read_b128 v[52:55], v1 offset:3072\n" \
  "ds_read_b128 v[40:43], v1 offset:4096\n" \
  "ds_read_b128 v[56:59], v1 offset:5120\n" \
  "ds_read_b128 v[60:63], v1 offset:6144\n" \
  "ds_read_b128 v[44:47], v1 offset:7168\n" \
  "s_setprio 3\n" \
  "s_waitcnt lgkmcnt(7)\n" \
  "v_smfmac_f32_16x16x64_bf16 v[12:15], v[32:35], v[64:71], v0\n" \
  "s_waitcnt lgkmcnt(6)\n" \
  "v_smfmac_f32_16x16x64_bf16 v[12:15], v[36:39], v[72:79], v0\n" \
  "s_waitcnt lgkmcnt(5)\n" \
  "v_smfmac_f32_16x16x64_bf16 v[12:15], v[48:51], v[80:87], v0\n" \
  "s_waitcnt lgkmcnt(4)\n" \
  "v_smfmac_f32_16x16x64_bf16 v[12:15], v[52:55], v[88:95], v0\n" \
  "s_waitcnt lgkmcnt(3)\n" \
  "v_smfmac_f32_16x16x64_bf16 v[12:15], v[40:43], v[96:103], v0\n" \
  "s_waitcnt lgkmcnt(2)\n" \
  "v_smfmac_f32_16x16x64_bf16 v[12:15], v[56:59], v[104:111], v0\n" \
  "s_waitcnt lgkmcnt(1)\n" \
  "v_smfmac_f32_16x16x64_bf16 v[12:15], v[60:63], v[112:119], v0\n" \
  "s_waitcnt lgkmcnt(0)\n" \
  "v_smfmac_f32_16x16x64_bf16 v[12:15], v[44:47], v[120:127], v0\n" \
  "ds_read_b128 v[24:27], v1 offset:10240\n" \
  "ds_read_b128 v[28:31], v1 offset:14336\n" \
  "s_nop 5\n" \
  "v_add_f32 v12, v12, v14\n" \
  "v_add_f32 v13, v13, v15\n" \
  "v_cvt_pk_bf16_f32 v12, v12, v13\n" \
  "ds_write_b16 v9, v12 offset:0\n" \
  "ds_write_b16_d16_hi v9, v12 offset:288\n" \
  "s_setprio 0\n" \
  "s_barrier\n" \
  "v_smfmac_f32_16x16x64_bf16 v[16:19], v[32:35], v[128:135], v0\n" \
  "v_smfmac_f32_16x16x64_bf16 v[20:23], v[32:35], v[192:199], v0\n" \
  "ds_read_b128 v[32:35], v1 offset:11264\n" \
  "v_smfmac_f32_16x16x64_bf16 v[16:19], v[36:39], v[136:143], v0\n" \
  "v_smfmac_f32_16x16x64_bf16 v[20:23], v[36:39], v[200:207], v0\n" \
  "ds_read_b128 v[36:39], v1 offset:15360\n" \
  "v_smfmac_f32_16x16x64_bf16 v[16:19], v[48:51], v[144:151], v0\n" \
  "v_smfmac_f32_16x16x64_bf16 v[20:23], v[48:51], v[208:215], v0\n" \
  "v_smfmac_f32_16x16x64_bf16 v[16:19], v[52:55], v[152:159], v0\n" \
  "v_smfmac_f32_16x16x64_bf16 v[20:23], v[52:55], v[216:223], v0\n" \
  "v_smfmac_f32_16x16x64_bf16 v[16:19], v[40:43], v[160:167], v0\n" \
  "v_smfmac_f32_16x16x64_bf16 v[20:23], v[40:43], v[224:231], v0\n" \
  ".fill ((16 - (. - .LCA_AL_%=)) & 31) >> 2, 4, 0xbf800000\n" \
  "s_waitcnt lgkmcnt(2)\n" \
  "s_barrier\n" \
  "s_setprio 3\n" \
  "ds_read_b128 v[48:51], v2 offset:0\n" \
  "ds_read_b128 v[52:55], v2 offset:64\n" \
  "v_smfmac_f32_16x16x64_bf16 v[16:19], v[56:59], v[168:175], v0\n" \
  "v_smfmac_f32_16x16x64_bf16 v[20:23], v[56:59], v[232:239], v0\n" \
  "ds_read_b128 v[56:59], v2 offset:128\n" \
  "s_cbranch_scc1 .LCA_LAST_%=\n" \
  ".LCA_BACK_%=:\n" \
  "s_waitcnt lgkmcnt(2)\n" \
  "v_mfma_f32_16x16x32_bf16 v[12:15], v[24:27], v[48:51], 0\n" \
  "ds_read_b128 v[24:27], v1 offset:12288\n" \
  "v_mfma_f32_16x16x32_bf16 v[8:11], v[28:31], v[48:51], 0\n" \
  "ds_read_b128 v[28:31], v1 offset:16384\n" \
  "v_mfma_f32_16x16x32_bf16 v[40:43], v[48:51], v[48:51], 0\n" \
  "ds_read_b128 v[48:51], v7 offset:18432\n" \
  "v_smfmac_f32_16x16x64_bf16 v[16:19], v[60:63], v[176:183], v0\n" \
  "v_smfmac_f32_16x16x64_bf16 v[20:23], v[60:63], v[240:247], v0\n" \
  "ds_read_b128 v[60:63], v2 offset:192\n" \
  "s_waitcnt lgkmcnt(5)\n" \
  "v_mfma_f32_16x16x32_bf16 v[12:15], v[32:35], v[52:55], v[12:15]\n" \
  "ds_read_b128 v[32:35], v1 offset:13312\n" \
  "v_mfma_f32_16x16x32_bf16 v[8:11], v[36:39], v[52:55], v[8:11]\n" \
  "ds_read_b128 v[36:39], v1 offset:17408\n" \
  "v_mfma_f32_16x16x32_bf16 v[40:43], v[52:55], v[52:55], v[40:43]\n" \
  "ds_read_b128 v[52:55], v7 offset:22528\n" \
  "s_waitcnt lgkmcnt(7)\n" \
  "v_mfma_f32_16x16x32_bf16 v[40:43], v[56:59], v[56:59], v[40:43]\n" \
  "s_waitcnt lgkmcnt(5)\n" \
  "v_mfma_f32_16x16x32_bf16 v[12:15], v[24:27], v[56:59], v[12:15]\n" \
  "ds_read_b128 v[24:27], v6 offset:2048\n" \
  "v_mfma_f32_16x16x32_bf16 v[8:11], v[28:31], v[56:59], v[8:11]\n" \
  "ds_read_b128 v[28:31], v6 offset:3072\n" \
  "s_waitcnt lgkmcnt(5)\n" \
  "v_mfma_f32_16x16x32_bf16 v[40:43], v[60:63], v[60:63], v[40:43]\n" \
  "ds_read_b128 v[56:59], v6 offset:0\n" \
  "s_waitcnt lgkmcnt(4)\n" \
  "v_mfma_f32_16x16x32_bf16 v[12:15], v[32:35], v[60:63], v[12:15]\n" \
  "ds_read_b128 v[32:35], v6 offset:8192\n" \
  "v_mfma_f32_16x16x32_bf16 v[8:11], v[36:39], v[60:63], v[8:11]\n" \
  "ds_read_b128 v[36:39], v6 offset:9216\n" \
  "v_smfmac_f32_16x16x64_bf16 v[16:19], v[44:47], v[184:191], v0\n" \
  "ds_read_b128 v[60:63], v6 offset:1024\n" \
  "s_waitcnt lgkmcnt(6)\n" \
  "v_smfmac_f32_16x16x64_bf16 v[20:23], v[44:47], v[248:255], v0\n" \
  "v_cndmask_b32_dpp v43, v43, v42, vcc quad_perm:[0,1,0,1] row_mask:0xf bank_mask:0xf\n" \
  "v_mov_b32 v41, 0\n" \
  "v_mov_b32 v42, 0\n" \
  "v_cndmask_b32_dpp v12, v8, v12, vcc quad_perm:[0,1,0,1] row_mask:0xf bank_mask:0xf\n" \
  "v_cndmask_b32_dpp v13, v9, v13, vcc quad_perm:[0,1,0,1] row_mask:0xf bank_mask:0xf\n" \
  "v_cndmask_b32_dpp v14, v10, v14, vcc quad_perm:[0,1,0,1] row_mask:0xf bank_mask:0xf\n" \
  "v_cndmask_b32_dpp v15, v11, v15, vcc quad_perm:[0,1,0,1] row_mask:0xf bank_mask:0xf\n" \
  "v_maximum3_f32 v8, v43, v12, v13\n" \
  "v_maximum3_f32 v8, v8, v14, v15\n" \
  "v_add_f32 v16, v16, v18\n" \
  "v_add_f32 v17, v17, v19\n" \
  "v_max_f32_dpp v8, v8, v8 quad_perm:[2,3,0,1] row_mask:0xf bank_mask:0xf\n" \
  "v_mov_b32 v9, v8\n" \
  "v_add_f32 v20, v20, v22\n" \
  "v_add_f32 v21, v21, v23\n" \
  "v_permlane16_swap_b32 v8, v9\n" \
  "v_max_f32 v8, v8, v9\n" \
  "v_mov_b32 v9, v8\n" \
  "v_cvt_pk_bf16_f32 v16, v16, v17\n" \
  "v_cvt_pk_bf16_f32 v17, v20, v21\n" \
  "v_permlane32_swap_b32 v8, v9\n" \
  "v_max_f32 v8, v8, v9\n" \
  "s_barrier\n" \
  "buffer_load_dword v7, v5, s[16:19], s24 offen sc1\n" \
  "v_mul_f32 v8, s78, v8\n" \
  "v_pk_fma_f32 v[12:13], v[12:13], s[78:79], v[8:9] op_sel_hi:[1,0,0] neg_lo:[0,0,1] neg_hi:[0,0,1]\n" \
  "v_pk_fma_f32 v[14:15], v[14:15], s[78:79], v[8:9] op_sel_hi:[1,0,0] neg_lo:[0,0,1] neg_hi:[0,0,1]\n" \
  "v_fma_f32 v43, v43, s78, -v8\n" \
  "v_exp_f32 v12, v12\n" \
  "v_exp_f32 v13, v13\n" \
  "v_exp_f32 v14, v14\n" \
  "v_exp_f32 v15, v15\n" \
  "v_exp_f32 v43, v43\n" \
  "v_cvt_pk_bf16_f32 v44, v12, v13\n" \
  "v_cvt_pk_bf16_f32 v45, v14, v15\n" \
  "v_pk_add_f32 v[18:19], v[12:13], v[14:15]\n" \
  "v_mov_b32_dpp v46, v44 quad_perm:[2,3,0,1] row_mask:0xf bank_mask:0xf\n" \
  "v_mov_b32_dpp v9, v43 quad_perm:[2,3,0,1] row_mask:0xf bank_mask:0xf\n" \
  "v_mov_b32_dpp v47, v45 quad_perm:[2,3,0,1] row_mask:0xf bank_mask:0xf\n" \
  "v_add_f32 v10, v18, v43\n" \
  "v_cvt_pk_bf16_f32 v40, v43, v9\n" \
  "v_add_f32 v10, v10, v19\n" \
  "v_mfma_f32_16x16x32_bf16 v[12:15], v[48:51], v[44:47], 0\n" \
  "v_cvt_pk_bf16_f32 v43, v43, v9\n" \
  "v_add_f32_dpp v10, v10, v10 quad_perm:[2,3,0,1] row_mask:0xf bank_mask:0xf\n" \
  "v_mfma_f32_16x16x32_bf16 v[20:23], v[52:55], v[44:47], 0\n" \
  "v_mul_f32 v11, s75, v10\n" \
  "v_mul_f32 v10, s75, v10\n" \
  "v_mfma_f32_16x16x16_bf16 v[12:15], v[16:17], v[40:41], v[12:15]\n" \
  "ds_read_b128 v[48:51], v6 offset:10240\n" \
  "v_permlane16_swap_b32 v10, v11\n" \
  "v_add_f32 v10, v10, v11\n" \
  "v_mfma_f32_16x16x16_bf16 v[20:23], v[16:17], v[42:43], v[20:23]\n" \
  "v_mov_b32 v11, v10\n" \
  "ds_read_b128 v[52:55], v6 offset:11264\n" \
  "v_mov_b64 v[18:19], s[84:85]\n" \
  "v_permlane32_swap_b32 v10, v11\n" \
  "v_add_f32 v10, v10, v11\n" \
  "v_rcp_f32 v10, v10\n" \
  "s_mov_b32 s0, 0xbe801d62\n" \
  ".set .Lpad0x6e, . - 4\n" \
  "v_pk_mul_f32 v[12:13], v[12:13], v[10:11] op_sel_hi:[1,0]\n" \
  "v_pk_mul_f32 v[14:15], v[14:15], v[10:11] op_sel_hi:[1,0]\n" \
  "v_pk_mul_f32 v[20:21], v[20:21], v[10:11] op_sel_hi:[1,0]\n" \
  "v_pk_mul_f32 v[22:23], v[22:23], v[10:11] op_sel_hi:[1,0]\n" \
  "v_cvt_pk_bf16_f32 v40, v12, v13\n" \
  "v_cvt_pk_bf16_f32 v41, v14, v15\n" \
  "v_cvt_pk_bf16_f32 v42, v20, v21\n" \
  "ds_write_b64 v4, v[40:41]\n" \
  "v_cvt_pk_bf16_f32 v43, v22, v23\n" \
  "ds_write_b64 v4, v[42:43] offset:1024\n" \
  "v_max_u32 v16, 1, v16\n" \
  "v_max_u32 v17, 1, v17\n" \
  "buffer_store_dwordx2 v[16:17], v3, s[52:55], s70 offen sc1\n" \
  "v_mov_b64 v[12:13], s[84:85]\n" \
  "v_mov_b64 v[14:15], s[84:85]\n" \
  "v_mov_b64 v[16:17], s[84:85]\n" \
  "v_mov_b64 v[20:21], s[84:85]\n" \
  "v_mov_b64 v[22:23], s[84:85]\n" \
  "v_mov_b64 v[8:9], s[84:85]\n" \
  "v_mov_b64 v[10:11], s[84:85]\n" \
  "s_setprio 0\n" \
  "s_waitcnt lgkmcnt(0)\n" \
  "s_barrier\n" \
  "ds_read_b128 v[40:43], v1 offset:8192\n" \
  "ds_read_b128 v[44:47], v1 offset:9216\n" \
  "s_waitcnt lgkmcnt(1)\n" \
  "v_smfmac_f32_16x16x64_bf16 v[12:15], v[40:43], v[56:63], v0\n" \
  "s_waitcnt lgkmcnt(0)\n" \
  "v_smfmac_f32_16x16x64_bf16 v[12:15], v[44:47], v[32:39], v0\n" \
  "ds_read_b128 v[56:59], v6 offset:4096\n" \
  "ds_read_b128 v[60:63], v6 offset:5120\n" \
  "v_smfmac_f32_16x16x64_bf16 v[16:19], v[40:43], v[24:31], v0\n" \
  "ds_read_b128 v[32:35], v6 offset:12288\n" \
  "ds_read_b128 v[36:39], v6 offset:13312\n" \
  "v_smfmac_f32_16x16x64_bf16 v[16:19], v[44:47], v[48:55], v0\n" \
  "ds_read_b128 v[24:27], v6 offset:6144\n" \
  "ds_read_b128 v[28:31], v6 offset:7168\n" \
  "v_add_f32 v12, v12, v14\n" \
  "v_add_f32 v13, v13, v15\n" \
  "global_atomic_add_x2 v5, v[12:13], s[26:27]\n" \
  "ds_read_b128 v[48:51], v6 offset:14336\n" \
  "ds_read_b128 v[52:55], v6 offset:15360\n" \
  "s_waitcnt lgkmcnt(6)\n" \
  "v_add_f32 v16, v16, v18\n" \
  "v_add_f32 v17, v17, v19\n" \
  "global_atomic_add_x2 v5, v[16:17], s[28:29]\n" \
  "v_smfmac_f32_16x16x64_bf16 v[20:23], v[40:43], v[56:63], v0\n" \
  "s_waitcnt lgkmcnt(4)\n" \
  "v_smfmac_f32_16x16x64_bf16 v[20:23], v[44:47], v[32:39], v0\n" \
  "s_waitcnt lgkmcnt(2)\n" \
  "v_smfmac_f32_16x16x64_bf16 v[8:11], v[40:43], v[24:31], v0\n" \
  "s_waitcnt lgkmcnt(0)\n" \
  "v_smfmac_f32_16x16x64_bf16 v[8:11], v[44:47], v[48:55], v0\n" \
  "s_nop 4\n" \
  "v_add_f32 v20, v20, v22\n" \
  "v_add_f32 v21, v21, v23\n" \
  "global_atomic_add_x2 v5, v[20:21], s[30:31]\n" \
  "v_add_f32 v8, v8, v10\n" \
  "v_add_f32 v9, v9, v11\n" \
  "global_atomic_add_x2 v5, v[8:9], s[32:33]\n" \
  "s_memrealtime s[62:63]\n" \
  "v_mbcnt_lo_u32_b32 v48, -1, 0\n" \
  "v_mbcnt_hi_u32_b32 v48, -1, v48\n" \
  "v_lshrrev_b32 v49, 3, v48\n" \
  "v_and_b32 v50, 7, v48\n" \
  "v_lshlrev_b32 v50, 4, v50\n" \
  "v_lshl_or_b32 v44, v49, 8, v50\n" \
  "s_add_u32 s20, s20, 32\n" \
  "s_mov_b32 s21, 0x8000\n" \
  "v_mov_b32 v52, s22\n" \
  "v_mov_b32 v54, s20\n" \
  "s_mul_i32 s88, s71, %[pp]\n" \
  "s_lshl_b32 s89, s71, 10\n" \
  "s_sub_u32 s88, s88, s25\n" \
  "s_mul_i32 s38, s24, %[an]\n" \
  "s_cmp_gt_u32 s89, s38\n" \
  "s_cselect_b32 s89, 0x1000, 0\n" \
  "s_waitcnt vmcnt(5)\n" \
  "v_mov_b32 v46, v7\n" \
  "s_branch .LCA_TKE_%=\n" \
  ".LCA_TKR_%=:\n" \
  "s_cmp_lg_u32 s73, 0\n" \
  "s_cbranch_scc1 .LCA_TKW_%=\n" \
  "s_sub_u32 s21, s21, 1\n" \
  "s_cbranch_scc1 .LCA_TKO_%=\n" \
  "s_and_b32 s38, s21, 7\n" \
  "s_cmp_lg_u32 s38, 7\n" \
  "s_cbranch_scc1 .LCA_TKS_%=\n" \
  "buffer_load_dword v46, v5, s[16:19], s24 offen sc1\n" \
  ".LCA_TKS_%=:\n" \
  "s_sleep 1\n" \
  ".LCA_TKE_%=:\n" \
  "v_readlane_b32 s38, v46, 8\n" \
  "v_readlane_b32 s39, v46, 24\n" \
  "v_readlane_b32 s23, v46, 40\n" \
  "v_readlane_b32 s34, v46, 56\n" \
  "s_add_u32 s38, s38, s39\n" \
  "s_add_u32 s23, s23, s34\n" \
  "s_add_u32 s38, s38, s23\n" \
  "s_cmp_lt_u32 s38, s20\n" \
  "s_cbranch_scc1 .LCA_TKR_%=\n" \
  ".LCA_TKO_%=:\n" \
  "v_readlane_b32 s34, v46, 0\n" \
  "v_readlane_b32 s38, v46, 16\n" \
  "v_readlane_b32 s39, v46, 32\n" \
  "v_readlane_b32 s23, v46, 48\n" \
  "s_max_u32 s34, s34, s38\n" \
  "s_max_u32 s39, s39, s23\n" \
  "s_add_u32 s34, s34, %[cd]\n" \
  "v_readlane_b32 s23, v46, 1\n" \
  "s_max_u32 s34, s34, s39\n" \
  "s_sub_u32 s23, s23, s34\n" \
  "s_sub_u32 s23, s23, s88\n" \
  "s_cmp_lt_u32 s23, s89\n" \
  "s_cselect_b32 s23, s23, 0\n" \
  "s_min_u32 s23, s23, %[ka]\n" \
  "s_add_u32 s34, s34, s23\n" \
  "s_add_u32 s34, s34, s64\n" \
  ".LCA_TKD_%=:\n" \
  "v_mov_b32 v55, s34\n" \
  "ds_write_b64 v52, v[54:55]\n" \
  "s_branch .LCA_WAIU_%=\n" \
  ".Lpad0x6f:\n" \
  "s_setpc_b64 s[98:99]\n" \
  ".LCA_WAIS_%=:\n" \
  "s_cmp_lt_i32 s39, -1000\n" \
  "s_cbranch_scc1 .LCA_WAIE_%=\n" \
  "s_sleep 1\n" \
  ".LCA_WAIU_%=:\n" \
  "s_memrealtime s[38:39]\n" \
  "s_waitcnt lgkmcnt(0)\n" \
  "s_sub_i32 s39, s38, s34\n" \
  "s_cmp_lt_i32 s39, -7\n" \
  "s_cbranch_scc1 .LCA_WAIS_%=\n" \
  "s_cmp_lt_i32 s39, 0\n" \
  "s_cbranch_scc1 .LCA_WAIU_%=\n" \
  ".LCA_WAIE_%=:\n" \
  "buffer_load_dwordx4 v[24:27], v44, s[40:43], s60 offen sc1\n" \
  "buffer_load_dwordx4 v[28:31], v44, s[40:43], s60 offen offset:2048 sc1\n" \
  "s_sub_u32 s62, s62, s67\n" \
  "s_lshl_b32 s62, s62, 4\n" \
  "s_sub_i32 s62, s62, s66\n" \
  "s_min_i32 s62, s62, 128\n" \
  "s_max_i32 s62, s62, -256\n" \
  "s_ashr_i32 s62, s62, 2\n" \
  "s_add_i32 s66, s66, s62\n" \
  "s_add_u32 s70, s70, 0x10000\n" \
  "v_mbcnt_lo_u32_b32 v48, -1, 0\n" \
  "v_mbcnt_hi_u32_b32 v48, -1, v48\n" \
  "v_and_b32 v49, 15, v48\n" \
  "v_lshl_add_u32 v9, v49, 1, s97\n" \
  "v_lshrrev_b32 v49, 4, v48\n" \
  "v_mad_u32_u24 v9, v49, s94, v9\n" \
  "v_add_u32 v7, s99, v1\n" \
  "v_mov_b64 v[12:13], 0\n" \
  "v_mov_b64 v[14:15], 0\n" \
  "v_mov_b64 v[16:17], 0\n" \
  "v_mov_b64 v[18:19], 0\n" \
  "v_mov_b64 v[20:21], 0\n" \
  "v_mov_b64 v[22:23], 0\n" \
  "v_and_b32 v49, 3, v48\n" \
  "v_lshl_add_u32 v40, v49, 2, s98\n" \
  "v_lshrrev_b32 v49, 2, v48\n" \
  "v_bfe_u32 v48, v48, 4, 1\n" \
  "v_xor_b32 v49, v49, v48\n" \
  "v_lshl_add_u32 v40, v49, 5, v40\n" \
  "s_add_i32 s80, s86, 0x35000000\n" \
  "s_sub_u32 s91, 0x4a350000, s86\n" \
  "s_min_u32 s91, s91, 0x4ca00000\n" \
  "s_mov_b32 s89, s60\n" \
  "s_xor_b32 s60, s60, 0x80\n" \
  "s_bitcmp0_b32 s60, 7\n" \
  "s_cselect_b32 s88, 0x8000, 0\n" \
  "s_add_u32 s60, s60, s88\n" \
  "s_and_b32 s60, s60, 0xffff\n" \
  "s_add_u32 s58, s56, s60\n" \
  "s_addc_u32 s59, s57, 0\n" \
  "s_xor_b32 s38, s35, 0\n" \
  "s_add_u32 s26, s58, s38\n" \
  "s_addc_u32 s27, s59, 0\n" \
  "s_xor_b32 s38, s35, 1024\n" \
  "s_add_u32 s28, s58, s38\n" \
  "s_addc_u32 s29, s59, 0\n" \
  "s_xor_b32 s38, s35, 2048\n" \
  "s_add_u32 s30, s58, s38\n" \
  "s_addc_u32 s31, s59, 0\n" \
  "s_xor_b32 s38, s35, 3072\n" \
  "s_add_u32 s32, s58, s38\n" \
  "s_addc_u32 s33, s59, 0\n" \
  "s_sub_u32 s71, s71, 1\n" \
  "s_mov_b32 s87, 0x40000\n" \
  "s_branch .LCA_CHK_%=\n" \
  ".LCA_POLL_%=:\n" \
  "buffer_load_dwordx4 v[24:27], v44, s[40:43], s89 offen sc1\n" \
  "buffer_load_dwordx4 v[28:31], v44, s[40:43], s89 offen offset:2048 sc1\n" \
  ".LCA_CHK_%=:\n" \
  "s_sub_u32 s87, s87, 1\n" \
  "s_cbranch_scc1 .LCA_END_%=\n" \
  "s_waitcnt vmcnt(1)\n" \
  "v_cvt_f32_i32 v32, v24\n" \
  "v_cvt_f32_i32 v33, v25\n" \
  "v_cvt_f32_i32 v34, v26\n" \
  "v_cvt_f32_i32 v35, v27\n" \
  "v_pk_mul_f32 v[24:25], v[32:33], s[80:81] op_sel_hi:[1,0]\n" \
  "v_pk_mul_f32 v[26:27], v[34:35], s[80:81] op_sel_hi:[1,0]\n" \
  "v_max3_f32 v45, |v32|, |v33|, |v34|\n" \
  "v_cvt_pk_bf16_f32 v24, v24, v26\n" \
  "v_cvt_pk_bf16_f32 v25, v25, v27\n" \
  "ds_write2_b32 v40, v24, v25 offset0:0 offset1:4\n" \
  "v_max3_f32 v45, |v45|, |v35|, |v35|\n" \
  "s_waitcnt vmcnt(0)\n" \
  "v_cvt_f32_i32 v32, v28\n" \
  "v_cvt_f32_i32 v33, v29\n" \
  "v_cvt_f32_i32 v34, v30\n" \
  "v_cvt_f32_i32 v35, v31\n" \
  "v_pk_mul_f32 v[28:29], v[32:33], s[80:81] op_sel_hi:[1,0]\n" \
  "v_pk_mul_f32 v[30:31], v[34:35], s[80:81] op_sel_hi:[1,0]\n" \
  "v_max3_f32 v45, |v45|, |v32|, |v33|\n" \
  "v_cvt_pk_bf16_f32 v28, v28, v30\n" \
  "v_cvt_pk_bf16_f32 v29, v29, v31\n" \
  "ds_write2_b32 v40, v28, v29 offset0:128 offset1:132\n" \
  "v_max3_f32 v45, |v45|, |v34|, |v35|\n" \
  "v_cmp_lt_f32 vcc, s91, v45\n" \
  "s_cbranch_vccnz .LCA_BIG_%=\n" \
  ".LCA_SML_%=:\n" \
  "s_branch .LCA_TOP_%=\n" \
  ".Lpad0x6b:\n" \
  "s_setpc_b64 s[98:99]\n" \
  ".LCA_BIG_%=:\n" \
  "v_cmp_lt_f32 vcc, 0x4ca00000, v45\n" \
  "s_cbranch_vccnz .LCA_POLL_%=\n" \
  "v_mul_f32 v45, s80, v45\n" \
  "v_mul_f32 v45, v45, v45\n" \
  "s_add_u32 s88, s74, 157760\n" \
  "v_mov_b32 v44, s88\n" \
  "s_nop 0\n" \
  "v_cmpx_lt_f32 vcc, 2.0, v45\n" \
  "ds_max_u32 v44, v45\n" \
  "s_mov_b64 exec, -1\n" \
  "s_branch .LCA_SML_%=\n" \
  ".LCA_TKW_%=:\n" \
  "s_andn2_b32 s38, s22, 24\n" \
  "v_mov_b32 v53, s38\n" \
  ".LCA_TKX_%=:\n" \
  "s_sleep 1\n" \
  "ds_read_b64 v[46:47], v53\n" \
  "s_sub_u32 s21, s21, 1\n" \
  "s_cbranch_scc1 .LCA_TKD_%=\n" \
  "s_waitcnt lgkmcnt(0)\n" \
  "v_readfirstlane_b32 s38, v46\n" \
  "v_readfirstlane_b32 s34, v47\n" \
  "s_cmp_eq_u32 s38, s20\n" \
  "s_cbranch_scc0 .LCA_TKX_%=\n" \
  "s_branch .LCA_TKD_%=\n" \
  ".LCA_FIRST_%=:\n" \
  "ds_read_b64 v[10:11], v1 offset:26624\n" \
  "ds_read_b128 v[24:27], v1 offset:10240\n" \
  "ds_read_b128 v[28:31], v1 offset:14336\n" \
  "ds_read_b128 v[32:35], v1 offset:11264\n" \
  "ds_read_b128 v[36:39], v1 offset:15360\n" \
  "s_mov_b32 s65, 3\n" \
  "s_waitcnt lgkmcnt(4)\n" \
  "v_readfirstlane_b32 s38, v11\n" \
  "v_add_u32 v9, %[d3], v11\n" \
  "s_cmp_eq_u32 s73, 3\n" \
  "s_cselect_b32 s39, s69, 0x1000\n" \
  "s_mov_b64 exec, 1\n" \
  "buffer_store_dword v9, off, s[16:19], s39 sc1\n" \
  "s_mov_b64 exec, -1\n" \
  "s_sub_u32 s67, s38, 40\n" \
  "v_mul_f32 v11, 0x44870000, v10\n" \
  "v_readfirstlane_b32 s83, v10\n" \
  "v_readfirstlane_b32 s82, v11\n" \
  "s_lshr_b32 s82, s82, 23\n" \
  "s_addk_i32 s82, 0xff83\n" \
  "s_ashr_i32 s82, s82, 1\n" \
  "s_max_i32 s82, s82, -20\n" \
  "s_min_i32 s82, s82, 100\n" \
  "s_lshr_b32 s83, s83, 23\n" \
  "s_sub_i32 s83, s83, 242\n" \
  "v_readfirstlane_b32 s86, v8\n" \
  "s_mov_b64 vcc, s[76:77]\n" \
  "s_lshr_b32 s86, s86, 23\n" \
  "s_add_i32 s86, s86, s83\n" \
  "s_ashr_i32 s86, s86, 1\n" \
  "s_max_i32 s86, s86, s82\n" \
  "s_min_i32 s86, s86, 100\n" \
  "s_lshl_b32 s86, s86, 23\n" \
  "s_add_i32 s75, s86, 0x3c800000\n" \
  "s_waitcnt lgkmcnt(0)\n" \
  "s_branch .LCA_BACK_%=\n" \
  ".LCA_LAST_%=:\n" \
  "s_cmp_lg_u32 s71, 0\n" \
  "s_cbranch_scc1 .LCA_FIRST_%=\n" \
  "v_smfmac_f32_16x16x64_bf16 v[16:19], v[60:63], v[176:183], v0\n" \
  "v_smfmac_f32_16x16x64_bf16 v[20:23], v[60:63], v[240:247], v0\n" \
  "v_smfmac_f32_16x16x64_bf16 v[16:19], v[44:47], v[184:191], v0\n" \
  "v_smfmac_f32_16x16x64_bf16 v[20:23], v[44:47], v[248:255], v0\n" \
  "s_nop 7\n" \
  "v_add_f32 v16, v16, v18\n" \
  "v_add_f32 v17, v17, v19\n" \
  "v_add_f32 v20, v20, v22\n" \
  "v_add_f32 v21, v21, v23\n" \
  "v_cvt_pk_bf16_f32 v16, v16, v17\n" \
  "v_cvt_pk_bf16_f32 v17, v20, v21\n" \
  "s_nop 1\n" \
  "s_nop 0\n" \
  "v_max_u32 v16, 1, v16\n" \
  "v_max_u32 v17, 1, v17\n" \
  "buffer_store_dwordx2 v[16:17], v3, s[52:55], s70 offen sc1\n" \
  "s_waitcnt vmcnt(0)\n" \
  ".LCA_END_%=:\n" \
  "s_endpgm\n"
#define CHAIN_ASM_VO \
  "s_mov_b32 s66, %[d0]\n" \
  "s_mov_b32 s94, 576\n" \
  "s_mov_b32 s95, 288\n" \
  "s_mov_b32 s84, 0x43400000\n" \
  "s_mov_b32 s85, 0x43400000\n" \
  "s_mov_b32 s92, 0x88000000\n" \
  "s_mov_b32 s93, 0x87fffff7\n" \
  "s_mov_b32 s78, 0x3e0293ee\n" \
  "s_mov_b32 s76, 0x33333333\n" \
  "s_mov_b32 s77, 0x33333333\n" \
  "s_add_u32 s58, s56, s60\n" \
  "s_addc_u32 s59, s57, 0\n" \
  "s_xor_b32 s38, s35, 0\n" \
  "s_add_u32 s26, s58, s38\n" \
  "s_addc_u32 s27, s59, 0\n" \
  "s_xor_b32 s38, s35, 1024\n" \
  "s_add_u32 s28, s58, s38\n" \
  "s_addc_u32 s29, s59, 0\n" \
  "s_xor_b32 s38, s35, 2048\n" \
  "s_add_u32 s30, s58, s38\n" \
  "s_addc_u32 s31, s59, 0\n" \
  "s_xor_b32 s38, s35, 3072\n" \
  "s_add_u32 s32, s58, s38\n" \
  "s_addc_u32 s33, s59, 0\n" \
  "s_lshr_b32 s98, s60, 2\n" \
  "s_add_u32 s98, s98, s74\n" \
  "s_add_u32 s98, s98, 131072\n" \
  "s_lshl_b32 s97, s73, 5\n" \
  "s_add_u32 s97, s97, s74\n" \
  "s_add_u32 s97, s97, 158592\n" \
  "s_lshl_b32 s99, s73, 11\n" \
  "s_add_u32 s99, s99, s74\n" \
  "s_add_u32 s99, s99, 133120\n" \
  "v_mbcnt_lo_u32_b32 v48, -1, 0\n" \
  "v_mbcnt_hi_u32_b32 v48, -1, v48\n" \
  "s_sub_u32 s88, s73, 4\n" \
  "v_and_b32 v51, 15, v48\n" \
  "v_lshl_add_u32 v4, v51, 1, s97\n" \
  "v_lshrrev_b32 v49, 4, v48\n" \
  "v_mad_u32_u24 v4, v49, s94, v4\n" \
  "v_lshrrev_b32 v49, 2, v51\n" \
  "v_and_b32 v50, 5, v51\n" \
  "v_and_or_b32 v49, v49, 2, v50\n" \
  "s_lshl_b32 s89, s88, 6\n" \
  "s_add_u32 s89, s89, s74\n" \
  "s_add_u32 s89, s89, 161152\n" \
  "v_and_b32 v50, 48, v48\n" \
  "v_add_u32 v6, s89, v50\n" \
  "v_mad_u32_u24 v6, v49, s95, v6\n" \
  "v_lshlrev_b32 v7, 3, v50\n" \
  "v_lshl_add_u32 v7, v49, 4, v7\n" \
  "v_and_b32 v49, 2, v51\n" \
  "v_lshl_add_u32 v7, v49, 29, v7\n" \
  "v_lshlrev_b32 v10, 4, v48\n" \
  "v_mov_b64 v[12:13], 0\n" \
  "v_mov_b64 v[14:15], 0\n" \
  "v_mov_b64 v[16:17], 0\n" \
  "v_mov_b64 v[18:19], 0\n" \
  "v_mov_b64 v[20:21], 0\n" \
  "v_mov_b64 v[22:23], 0\n" \
  "s_lshl_b32 s88, s73, 10\n" \
  "v_add_u32 v24, s88, v1\n" \
  "ds_read_b128 v[32:35], v24 offset:0\n" \
  "s_waitcnt lgkmcnt(0)\n" \
  "v_mov_b32 v45, 0\n" \
  "v_dot2c_f32_bf16 v45, v32, v32\n" \
  "v_dot2c_f32_bf16 v45, v33, v33\n" \
  "v_dot2c_f32_bf16 v45, v34, v34\n" \
  "v_dot2c_f32_bf16 v45, v35, v35\n" \
  "s_add_u32 s88, s74, 157760\n" \
  "v_mov_b32 v44, s88\n" \
  "s_nop 0\n" \
  "v_cmpx_lt_f32 vcc, 2.0, v45\n" \
  "ds_max_u32 v44, v45\n" \
  "s_mov_b64 exec, -1\n" \
  "v_bfe_u32 v60, v10, 2, 7\n" \
  "s_lshl_b32 s38, s24, 1\n" \
  "s_add_u32 s38, s38, %[so]\n" \
  "global_load_dword v61, v60, s[52:53] offset:0 sc1\n" \
  "global_load_dword v62, v60, s[52:53] offset:128 sc1\n" \
  "global_load_dword v63, v60, s[52:53] offset:256 sc1\n" \
  "v_lshrrev_b32 v56, 8, v10\n" \
  "v_lshl_add_u32 v56, v56, 7, s38\n" \
  "v_and_or_b32 v56, v60, 63, v56\n" \
  "s_cmp_lg_u32 s73, 7\n" \
  "s_cbranch_scc1 2\n" \
  "global_load_dword v57, v56, s[52:53] sc1\n" \
  "s_branch .LCV_AL_%=\n" \
  ".p2alignl 12, 0xbf800000, 3000\n" \
  ".p2align 6\n" \
  ".Lpad0x81:\n" \
  "s_setpc_b64 s[98:99]\n" \
  ".p2align 6\n" \
  ".LCV_AL_%=:\n" \
  ".LCV_TOP_%=:\n" \
  "s_waitcnt lgkmcnt(0)\n" \
  "s_barrier\n" \
  "s_setprio 2\n" \
  "ds_read_b128 v[24:27], v1 offset:0\n" \
  "ds_read_b128 v[28:31], v1 offset:1024\n" \
  "ds_read_b128 v[32:35], v1 offset:2048\n" \
  "ds_read_b128 v[36:39], v1 offset:3072\n" \
  "buffer_load_dword v5, off, s[44:47], s68 sc1\n" \
  "s_cmp_eq_u32 s71, 63\n" \
  "s_waitcnt lgkmcnt(3)\n" \
  "v_smfmac_f32_16x16x64_bf16 v[20:23], v[24:27], v[192:199], v0\n" \
  "ds_read_b128 v[40:43], v1 offset:4096\n" \
  "s_waitcnt lgkmcnt(3)\n" \
  "v_smfmac_f32_16x16x64_bf16 v[20:23], v[28:31], v[200:207], v0\n" \
  "ds_read_b128 v[44:47], v1 offset:5120\n" \
  "s_waitcnt lgkmcnt(3)\n" \
  "v_smfmac_f32_16x16x64_bf16 v[20:23], v[32:35], v[208:215], v0\n" \
  "ds_read_b128 v[48:51], v1 offset:6144\n" \
  "s_waitcnt lgkmcnt(3)\n" \
  "v_smfmac_f32_16x16x64_bf16 v[20:23], v[36:39], v[216:223], v0\n" \
  "ds_read_b128 v[52:55], v1 offset:7168\n" \
  "s_waitcnt lgkmcnt(3)\n" \
  "v_smfmac_f32_16x16x64_bf16 v[20:23], v[40:43], v[224:231], v0\n" \
  "s_waitcnt lgkmcnt(2)\n" \
  "v_smfmac_f32_16x16x64_bf16 v[20:23], v[44:47], v[232:239], v0\n" \
  "s_waitcnt lgkmcnt(1)\n" \
  "v_smfmac_f32_16x16x64_bf16 v[20:23], v[48:51], v[240:247], v0\n" \
  "s_waitcnt lgkmcnt(0)\n" \
  "v_smfmac_f32_16x16x64_bf16 v[20:23], v[52:55], v[248:255], v0\n" \
  "s_nop 0\n" \
  "v_smfmac_f32_16x16x64_bf16 v[12:15], v[24:27], v[64:71], v0\n" \
  "s_nop 0\n" \
  "v_smfmac_f32_16x16x64_bf16 v[12:15], v[28:31], v[72:79], v0\n" \
  "s_nop 0\n" \
  "v_smfmac_f32_16x16x64_bf16 v[12:15], v[32:35], v[80:87], v0\n" \
  "ds_read_b32 v11, v1 offset:26688\n" \
  "v_smfmac_f32_16x16x64_bf16 v[12:15], v[36:39], v[88:95], v0\n" \
  "v_add_f32 v20, v20, v22\n" \
  "v_smfmac_f32_16x16x64_bf16 v[12:15], v[40:43], v[96:103], v0\n" \
  "v_add_f32 v21, v21, v23\n" \
  "v_smfmac_f32_16x16x64_bf16 v[12:15], v[44:47], v[104:111], v0\n" \
  "v_cvt_pk_bf16_f32 v20, v20, v21\n" \
  "v_smfmac_f32_16x16x64_bf16 v[12:15], v[48:51], v[112:119], v0\n" \
  "ds_write_b16 v4, v20 offset:0\n" \
  "v_smfmac_f32_16x16x64_bf16 v[12:15], v[52:55], v[120:127], v0\n" \
  "ds_write_b16_d16_hi v4, v20 offset:288\n" \
  "v_smfmac_f32_16x16x64_bf16 v[16:19], v[24:27], v[128:135], v0\n" \
  "s_nop 1\n" \
  "v_smfmac_f32_16x16x64_bf16 v[16:19], v[28:31], v[136:143], v0\n" \
  "s_nop 1\n" \
  "v_smfmac_f32_16x16x64_bf16 v[16:19], v[32:35], v[144:151], v0\n" \
  "v_add_f32 v12, v12, v14\n" \
  "v_smfmac_f32_16x16x64_bf16 v[16:19], v[36:39], v[152:159], v0\n" \
  "v_add_f32 v13, v13, v15\n" \
  "v_smfmac_f32_16x16x64_bf16 v[16:19], v[40:43], v[160:167], v0\n" \
  "v_cvt_pk_bf16_f32 v12, v12, v13\n" \
  "v_smfmac_f32_16x16x64_bf16 v[16:19], v[44:47], v[168:175], v0\n" \
  "ds_write_b16 v4, v12 offset:128\n" \
  "v_smfmac_f32_16x16x64_bf16 v[16:19], v[48:51], v[176:183], v0\n" \
  "ds_write_b16_d16_hi v4, v12 offset:416\n" \
  "v_smfmac_f32_16x16x64_bf16 v[16:19], v[52:55], v[184:191], v0\n" \
  "s_barrier\n" \
  "s_nop 6\n" \
  "v_add_f32 v16, v16, v18\n" \
  "v_add_f32 v17, v17, v19\n" \
  "v_cvt_pk_bf16_f32 v16, v16, v17\n" \
  "ds_write_b16 v4, v16 offset:2560\n" \
  "ds_write_b16_d16_hi v4, v16 offset:2848\n" \
  "s_cbranch_scc1 .LCV_FIRST_%=\n" \
  ".LCV_BACK_%=:\n" \
  "s_waitcnt lgkmcnt(0)\n" \
  "s_barrier\n" \
  "s_setprio 0\n" \
  "ds_read_b128 v[60:63], v6 offset:0\n" \
  "v_readfirstlane_b32 s86, v11\n" \
  "s_add_u32 s88, s74, 157760\n" \
  "v_mov_b32 v8, s88\n" \
  "v_mov_b32 v9, 0\n" \
  "ds_write_b32 v8, v9\n" \
  "s_lshr_b32 s86, s86, 23\n" \
  "s_add_i32 s86, s86, s83\n" \
  "s_ashr_i32 s86, s86, 1\n" \
  "s_max_i32 s86, s86, s82\n" \
  "s_min_i32 s86, s86, 100\n" \
  "s_lshl_b32 s86, s86, 23\n" \
  "s_cmp_eq_u32 s71, 0\n" \
  "s_waitcnt lgkmcnt(1)\n" \
  "v_max_u32 v60, 1, v60\n" \
  "v_max_u32 v61, 1, v61\n" \
  "v_max_u32 v62, 1, v62\n" \
  "v_max_u32 v63, 1, v63\n" \
  "buffer_store_dwordx4 v[60:63], v7, s[48:51], s69 offen sc1\n" \
  "s_cbranch_scc1 .LCV_LAST_%=\n" \
  "s_waitcnt vmcnt(1)\n" \
  "v_cmp_eq_u32 vcc, 0, v5\n" \
  "s_cmp_eq_u64 vcc, 0\n" \
  "s_cselect_b32 s90, 1, 0\n" \
  "s_cselect_b32 s88, s71, 64\n" \
  "s_cmp_lt_u32 s88, 63\n" \
  "s_cselect_b32 s90, 1, 0\n" \
  "s_barrier\n" \
  "ds_read_b128 v[24:27], v3 offset:0\n" \
  "ds_read_b128 v[28:31], v3 offset:1024\n" \
  "ds_read_b128 v[32:35], v3 offset:2048\n" \
  "ds_read_b128 v[36:39], v3 offset:3072\n" \
  "ds_read_b128 v[40:43], v3 offset:8192\n" \
  "ds_read_b128 v[44:47], v3 offset:9216\n" \
  "ds_read_b128 v[48:51], v3 offset:10240\n" \
  "ds_read_b128 v[52:55], v3 offset:11264\n" \
  "ds_read_b128 v[56:59], v3 offset:4096\n" \
  "ds_read_b128 v[60:63], v3 offset:5120\n" \
  "s_cbranch_scc0 .LCV_NOTOK_%=\n" \
  "s_mov_b32 m0, s99\n" \
  "s_add_u32 s88, s99, 8192\n" \
  "global_load_lds_dwordx4 v10, s[62:63] sc1\n" \
  "global_load_lds_dwordx4 v10, s[62:63] offset:1024 sc1\n" \
  "s_mov_b32 m0, s88\n" \
  "s_nop 0\n" \
  "global_load_lds_dwordx4 v10, s[64:65] sc1\n" \
  "global_load_lds_dwordx4 v10, s[64:65] offset:1024 sc1\n" \
  ".LCV_NOTOK_%=:\n" \
  "v_mov_b64 v[12:13], s[84:85]\n" \
  "v_mov_b64 v[14:15], s[84:85]\n" \
  "v_mov_b64 v[16:17], s[84:85]\n" \
  "v_mov_b64 v[18:19], s[84:85]\n" \
  "v_mov_b64 v[20:21], s[84:85]\n" \
  "v_mov_b64 v[22:23], s[84:85]\n" \
  "s_cmp_eq_u32 s90, 1\n" \
  "s_cbranch_scc1 .LCV_NMT_%=\n" \
  "s_sleep 3\n" \
  "s_add_u32 m0, s22, 64\n" \
  "s_mov_b64 exec, 1\n" \
  "buffer_load_dword off, s[44:47], s68 sc1 lds\n" \
  "s_mov_b64 exec, -1\n" \
  ".LCV_NMT_%=:\n" \
  "s_waitcnt lgkmcnt(0)\n" \
  "s_barrier\n" \
  "ds_read_b128 v[4:7], v1 offset:8192\n" \
  "ds_read_b128 v[8:11], v1 offset:9216\n" \
  "s_waitcnt lgkmcnt(1)\n" \
  "v_smfmac_f32_16x16x64_bf16 v[12:15], v[4:7], v[24:31], v0\n" \
  "s_waitcnt lgkmcnt(0)\n" \
  "v_smfmac_f32_16x16x64_bf16 v[12:15], v[8:11], v[40:47], v0\n" \
  "ds_read_b128 v[24:27], v3 offset:12288\n" \
  "ds_read_b128 v[28:31], v3 offset:13312\n" \
  "v_smfmac_f32_16x16x64_bf16 v[16:19], v[4:7], v[32:39], v0\n" \
  "ds_read_b128 v[32:35], v3 offset:6144\n" \
  "ds_read_b128 v[36:39], v3 offset:7168\n" \
  "v_smfmac_f32_16x16x64_bf16 v[16:19], v[8:11], v[48:55], v0\n" \
  "s_nop 1\n" \
  "v_add_f32 v12, v12, v14\n" \
  "v_add_f32 v13, v13, v15\n" \
  "global_atomic_add_x2 v2, v[12:13], s[26:27]\n" \
  "v_smfmac_f32_16x16x64_bf16 v[20:23], v[4:7], v[56:63], v0\n" \
  "ds_read_b128 v[56:59], v3 offset:14336\n" \
  "ds_read_b128 v[60:63], v3 offset:15360\n" \
  "v_add_f32 v16, v16, v18\n" \
  "v_add_f32 v17, v17, v19\n" \
  "global_atomic_add_x2 v2, v[16:17], s[28:29]\n" \
  "v_mov_b64 v[40:41], s[84:85]\n" \
  "v_mov_b64 v[42:43], s[84:85]\n" \
  "s_waitcnt lgkmcnt(4)\n" \
  "v_smfmac_f32_16x16x64_bf16 v[20:23], v[8:11], v[24:31], v0\n" \
  "s_waitcnt lgkmcnt(2)\n" \
  "v_smfmac_f32_16x16x64_bf16 v[40:43], v[4:7], v[32:39], v0\n" \
  "s_waitcnt lgkmcnt(0)\n" \
  "v_smfmac_f32_16x16x64_bf16 v[40:43], v[8:11], v[56:63], v0\n" \
  "s_nop 3\n" \
  "v_add_f32 v20, v20, v22\n" \
  "v_add_f32 v21, v21, v23\n" \
  "global_atomic_add_x2 v2, v[20:21], s[30:31]\n" \
  "s_nop 0\n" \
  "v_add_f32 v40, v40, v42\n" \
  "v_add_f32 v41, v41, v43\n" \
  "global_atomic_add_x2 v2, v[40:41], s[32:33]\n" \
  "v_mbcnt_lo_u32_b32 v48, -1, 0\n" \
  "v_mbcnt_hi_u32_b32 v48, -1, v48\n" \
  "s_add_u32 s20, s20, 32\n" \
  "s_cmp_eq_u32 s90, 1\n" \
  "s_cbranch_scc1 .LCV_HAVE_%=\n" \
  "s_mov_b32 s87, 0x40000\n" \
  "v_lshlrev_b32 v10, 4, v48\n" \
  "s_waitcnt vmcnt(4)\n" \
  "v_mov_b32 v55, s22\n" \
  "s_nop 0\n" \
  "ds_read_b32 v55, v55 offset:64\n" \
  "s_waitcnt lgkmcnt(0)\n" \
  "v_cmp_eq_u32 vcc, 0, v55\n" \
  "s_cmp_eq_u64 vcc, 0\n" \
  "s_cbranch_scc1 .LCV_GOT_%=\n" \
  ".LCV_LATE_%=:\n" \
  "buffer_load_dword v58, off, s[44:47], s68 sc1\n" \
  "s_sub_u32 s87, s87, 1\n" \
  "s_cbranch_scc1 .LCV_END_%=\n" \
  "s_waitcnt vmcnt(0)\n" \
  "v_cmp_eq_u32 vcc, 0, v58\n" \
  "s_cmp_eq_u64 vcc, 0\n" \
  "s_cbranch_scc1 .LCV_GOT_%=\n" \
  "s_sleep 4\n" \
  "s_branch .LCV_LATE_%=\n" \
  ".Lpad0x82:\n" \
  "s_setpc_b64 s[98:99]\n" \
  ".LCV_GOT_%=:\n" \
  "s_mov_b32 m0, s99\n" \
  "s_add_u32 s88, s99, 8192\n" \
  "global_load_lds_dwordx4 v10, s[62:63] sc1\n" \
  "global_load_lds_dwordx4 v10, s[62:63] offset:1024 sc1\n" \
  "s_mov_b32 m0, s88\n" \
  "s_nop 0\n" \
  "global_load_lds_dwordx4 v10, s[64:65] sc1\n" \
  "global_load_lds_dwordx4 v10, s[64:65] offset:1024 sc1\n" \
  ".LCV_HAVE_%=:\n" \
  "v_lshrrev_b32 v49, 3, v48\n" \
  "v_and_b32 v50, 7, v48\n" \
  "v_lshlrev_b32 v50, 4, v50\n" \
  "v_lshl_or_b32 v44, v49, 8, v50\n" \
  "s_mov_b32 s21, 0x8000\n" \
  "v_mov_b32 v52, s22\n" \
  "v_mov_b32 v54, s20\n" \
  "s_branch .LCV_TKF_%=\n" \
  ".LCV_TKE_%=:\n" \
  "s_sleep 1\n" \
  ".LCV_TKF_%=:\n" \
  "ds_read_b64 v[54:55], v52\n" \
  "s_sub_u32 s21, s21, 1\n" \
  "s_cbranch_scc1 .LCV_TKO_%=\n" \
  "s_waitcnt lgkmcnt(0)\n" \
  "v_readfirstlane_b32 s38, v54\n" \
  "v_readfirstlane_b32 s34, v55\n" \
  "s_cmp_eq_u32 s38, s20\n" \
  "s_cbranch_scc0 .LCV_TKE_%=\n" \
  ".LCV_TKO_%=:\n" \
  "s_branch .LCV_WAIU_%=\n" \
  ".Lpad0x7f:\n" \
  "s_setpc_b64 s[98:99]\n" \
  ".LCV_WAIS_%=:\n" \
  "s_cmp_lt_i32 s39, -1000\n" \
  "s_cbranch_scc1 .LCV_WAIE_%=\n" \
  "s_sleep 1\n" \
  ".LCV_WAIU_%=:\n" \
  "s_memrealtime s[38:39]\n" \
  "s_waitcnt lgkmcnt(0)\n" \
  "s_sub_i32 s39, s38, s34\n" \
  "s_cmp_lt_i32 s39, -7\n" \
  "s_cbranch_scc1 .LCV_WAIS_%=\n" \
  "s_cmp_lt_i32 s39, 0\n" \
  "s_cbranch_scc1 .LCV_WAIU_%=\n" \
  ".LCV_WAIE_%=:\n" \
  "buffer_load_dwordx4 v[24:27], v44, s[40:43], s60 offen sc1\n" \
  "buffer_load_dwordx4 v[28:31], v44, s[40:43], s60 offen offset:2048 sc1\n" \
  "v_mbcnt_lo_u32_b32 v48, -1, 0\n" \
  "v_mbcnt_hi_u32_b32 v48, -1, v48\n" \
  "s_sub_u32 s88, s73, 4\n" \
  "v_and_b32 v51, 15, v48\n" \
  "v_lshl_add_u32 v4, v51, 1, s97\n" \
  "v_lshrrev_b32 v49, 4, v48\n" \
  "v_mad_u32_u24 v4, v49, s94, v4\n" \
  "v_lshrrev_b32 v49, 2, v51\n" \
  "v_and_b32 v50, 5, v51\n" \
  "v_and_or_b32 v49, v49, 2, v50\n" \
  "s_lshl_b32 s89, s88, 6\n" \
  "s_add_u32 s89, s89, s74\n" \
  "s_add_u32 s89, s89, 161152\n" \
  "v_and_b32 v50, 48, v48\n" \
  "v_add_u32 v6, s89, v50\n" \
  "v_mad_u32_u24 v6, v49, s95, v6\n" \
  "v_lshlrev_b32 v7, 3, v50\n" \
  "v_lshl_add_u32 v7, v49, 4, v7\n" \
  "v_and_b32 v49, 2, v51\n" \
  "v_lshl_add_u32 v7, v49, 29, v7\n" \
  "v_lshlrev_b32 v10, 4, v48\n" \
  "v_mov_b64 v[12:13], 0\n" \
  "v_mov_b64 v[14:15], 0\n" \
  "v_mov_b64 v[16:17], 0\n" \
  "v_mov_b64 v[18:19], 0\n" \
  "v_mov_b64 v[20:21], 0\n" \
  "v_mov_b64 v[22:23], 0\n" \
  "v_and_b32 v49, 3, v48\n" \
  "v_lshl_add_u32 v40, v49, 2, s98\n" \
  "v_lshrrev_b32 v49, 2, v48\n" \
  "v_bfe_u32 v48, v48, 4, 1\n" \
  "v_xor_b32 v49, v49, v48\n" \
  "v_lshl_add_u32 v40, v49, 5, v40\n" \
  "s_add_u32 s62, s62, 0x80000\n" \
  "s_addc_u32 s63, s63, 0\n" \
  "s_add_u32 s64, s64, 0x40000\n" \
  "s_addc_u32 s65, s65, 0\n" \
  "s_add_u32 s68, s68, 0x1000\n" \
  "s_add_u32 s69, s69, 0x10000\n" \
  "s_add_i32 s80, s86, 0x35000000\n" \
  "s_sub_u32 s91, 0x4a350000, s86\n" \
  "s_min_u32 s91, s91, 0x4ca00000\n" \
  "s_mov_b32 s89, s60\n" \
  "s_xor_b32 s60, s60, 0x80\n" \
  "s_bitcmp0_b32 s60, 7\n" \
  "s_cselect_b32 s88, 0x8000, 0\n" \
  "s_add_u32 s60, s60, s88\n" \
  "s_and_b32 s60, s60, 0xffff\n" \
  "s_add_u32 s58, s56, s60\n" \
  "s_addc_u32 s59, s57, 0\n" \
  "s_xor_b32 s38, s35, 0\n" \
  "s_add_u32 s26, s58, s38\n" \
  "s_addc_u32 s27, s59, 0\n" \
  "s_xor_b32 s38, s35, 1024\n" \
  "s_add_u32 s28, s58, s38\n" \
  "s_addc_u32 s29, s59, 0\n" \
  "s_xor_b32 s38, s35, 2048\n" \
  "s_add_u32 s30, s58, s38\n" \
  "s_addc_u32 s31, s59, 0\n" \
  "s_xor_b32 s38, s35, 3072\n" \
  "s_add_u32 s32, s58, s38\n" \
  "s_addc_u32 s33, s59, 0\n" \
  "s_sub_u32 s71, s71, 1\n" \
  "s_mov_b32 s87, 0x40000\n" \
  "s_branch .LCV_CHK_%=\n" \
  ".LCV_POLL_%=:\n" \
  "buffer_load_dwordx4 v[24:27], v44, s[40:43], s89 offen sc1\n" \
  "buffer_load_dwordx4 v[28:31], v44, s[40:43], s89 offen offset:2048 sc1\n" \
  ".LCV_CHK_%=:\n" \
  "s_sub_u32 s87, s87, 1\n" \
  "s_cbranch_scc1 .LCV_END_%=\n" \
  "s_waitcnt vmcnt(1)\n" \
  "v_cvt_f32_i32 v32, v24\n" \
  "v_cvt_f32_i32 v33, v25\n" \
  "v_cvt_f32_i32 v34, v26\n" \
  "v_cvt_f32_i32 v35, v27\n" \
  "v_pk_mul_f32 v[24:25], v[32:33], s[80:81] op_sel_hi:[1,0]\n" \
  "v_pk_mul_f32 v[26:27], v[34:35], s[80:81] op_sel_hi:[1,0]\n" \
  "v_max3_f32 v45, |v32|, |v33|, |v34|\n" \
  "v_cvt_pk_bf16_f32 v24, v24, v26\n" \
  "v_cvt_pk_bf16_f32 v25, v25, v27\n" \
  "ds_write2_b32 v40, v24, v25 offset0:0 offset1:4\n" \
  "v_max3_f32 v45, |v45|, |v35|, |v35|\n" \
  "s_waitcnt vmcnt(0)\n" \
  "v_cvt_f32_i32 v32, v28\n" \
  "v_cvt_f32_i32 v33, v29\n" \
  "v_cvt_f32_i32 v34, v30\n" \
  "v_cvt_f32_i32 v35, v31\n" \
  "v_pk_mul_f32 v[28:29], v[32:33], s[80:81] op_sel_hi:[1,0]\n" \
  "v_pk_mul_f32 v[30:31], v[34:35], s[80:81] op_sel_hi:[1,0]\n" \
  "v_max3_f32 v45, |v45|, |v32|, |v33|\n" \
  "v_cvt_pk_bf16_f32 v28, v28, v30\n" \
  "v_cvt_pk_bf16_f32 v29, v29, v31\n" \
  "ds_write2_b32 v40, v28, v29 offset0:128 offset1:132\n" \
  "v_max3_f32 v45, |v45|, |v34|, |v35|\n" \
  "v_cmp_lt_f32 vcc, s91, v45\n" \
  "s_cbranch_vccnz .LCV_BIG_%=\n" \
  ".LCV_SML_%=:\n" \
  "s_branch .LCV_TOP_%=\n" \
  ".Lpad0x83:\n" \
  "s_setpc_b64 s[98:99]\n" \
  ".LCV_BIG_%=:\n" \
  "v_cmp_lt_f32 vcc, 0x4ca00000, v45\n" \
  "s_cbranch_vccnz .LCV_POLL_%=\n" \
  "v_mul_f32 v45, s80, v45\n" \
  "v_mul_f32 v45, v45, v45\n" \
  "s_add_u32 s88, s74, 157760\n" \
  "v_mov_b32 v44, s88\n" \
  "s_nop 0\n" \
  "v_cmpx_lt_f32 vcc, 2.0, v45\n" \
  "ds_max_u32 v44, v45\n" \
  "s_mov_b64 exec, -1\n" \
  "s_branch .LCV_SML_%=\n" \
  ".LCV_FIRST_%=:\n" \
  "s_mov_b32 s87, 0x100000\n" \
  "s_cmp_lg_u32 s73, 7\n" \
  "s_cbranch_scc1 .LCV_FON_%=\n" \
  "global_load_dword v58, v56, s[52:53] sc1\n" \
  "s_sleep %[sf]\n" \
  "global_load_dword v59, v56, s[52:53] sc1\n" \
  ".LCV_FASK_%=:\n" \
  "s_sub_u32 s87, s87, 1\n" \
  "s_cbranch_scc1 .LCV_END_%=\n" \
  "s_waitcnt vmcnt(2)\n" \
  "v_mov_b32 v30, v57\n" \
  "v_cmp_eq_u32 vcc, 0, v57\n" \
  "s_cbranch_vccz .LCV_FSEE_%=\n" \
  "s_cmp_eq_u32 s24, 0\n" \
  "s_cbranch_scc1 1\n" \
  "s_sleep %[sg]\n" \
  "global_load_dword v57, v56, s[52:53] sc1\n" \
  "s_waitcnt vmcnt(2)\n" \
  "v_mov_b32 v30, v58\n" \
  "v_cmp_eq_u32 vcc, 0, v58\n" \
  "s_cbranch_vccz .LCV_FSEE_%=\n" \
  "s_cmp_eq_u32 s24, 0\n" \
  "s_cbranch_scc1 1\n" \
  "s_sleep %[sg]\n" \
  "global_load_dword v58, v56, s[52:53] sc1\n" \
  "s_waitcnt vmcnt(2)\n" \
  "v_mov_b32 v30, v59\n" \
  "v_cmp_eq_u32 vcc, 0, v59\n" \
  "s_cbranch_vccz .LCV_FSEE_%=\n" \
  "s_cmp_eq_u32 s24, 0\n" \
  "s_cbranch_scc1 1\n" \
  "s_sleep %[sg]\n" \
  "global_load_dword v59, v56, s[52:53] sc1\n" \
  "s_branch .LCV_FASK_%=\n" \
  ".LCV_FSEE_%=:\n" \
  "buffer_load_dword v9, off, s[16:19], s24 offset:8 sc1\n" \
  "s_sub_u32 s38, s62, 6144\n" \
  "s_subb_u32 s39, s63, 0\n" \
  "s_add_u32 m0, s99, -6144\n" \
  "s_nop 0\n" \
  "global_load_lds_dwordx4 v10, s[38:39] offset:0 sc1\n" \
  "global_load_lds_dwordx4 v10, s[38:39] offset:1024 sc1\n" \
  "global_load_lds_dwordx4 v10, s[38:39] offset:2048 sc1\n" \
  "global_load_lds_dwordx4 v10, s[38:39] offset:3072 sc1\n" \
  "s_sub_u32 s38, s62, 2048\n" \
  "s_subb_u32 s39, s63, 0\n" \
  "s_add_u32 m0, s99, -2048\n" \
  "s_nop 0\n" \
  "global_load_lds_dwordx4 v10, s[38:39] offset:0 sc1\n" \
  "global_load_lds_dwordx4 v10, s[38:39] offset:1024 sc1\n" \
  "global_load_lds_dwordx4 v10, s[38:39] offset:2048 sc1\n" \
  "global_load_lds_dwordx4 v10, s[38:39] offset:3072 sc1\n" \
  "s_sub_u32 s38, s64, 6144\n" \
  "s_subb_u32 s39, s65, 0\n" \
  "s_add_u32 m0, s99, 2048\n" \
  "s_nop 0\n" \
  "global_load_lds_dwordx4 v10, s[38:39] offset:0 sc1\n" \
  "global_load_lds_dwordx4 v10, s[38:39] offset:1024 sc1\n" \
  "global_load_lds_dwordx4 v10, s[38:39] offset:2048 sc1\n" \
  "global_load_lds_dwordx4 v10, s[38:39] offset:3072 sc1\n" \
  "s_sub_u32 s38, s64, 2048\n" \
  "s_subb_u32 s39, s65, 0\n" \
  "s_add_u32 m0, s99, 6144\n" \
  "s_nop 0\n" \
  "global_load_lds_dwordx4 v10, s[38:39] offset:0 sc1\n" \
  "global_load_lds_dwordx4 v10, s[38:39] offset:1024 sc1\n" \
  "global_load_lds_dwordx4 v10, s[38:39] offset:2048 sc1\n" \
  "global_load_lds_dwordx4 v10, s[38:39] offset:3072 sc1\n" \
  "s_memrealtime vcc\n" \
  "s_nop 1\n" \
  "v_max_u32_dpp v30, v30, v30 row_ror:8 row_mask:0xf bank_mask:0xf\n" \
  "s_nop 1\n" \
  "v_max_u32_dpp v30, v30, v30 row_ror:4 row_mask:0xf bank_mask:0xf\n" \
  "s_nop 1\n" \
  "v_max_u32_dpp v30, v30, v30 row_ror:2 row_mask:0xf bank_mask:0xf\n" \
  "s_nop 1\n" \
  "v_max_u32_dpp v30, v30, v30 row_ror:1 row_mask:0xf bank_mask:0xf\n" \
  "v_mov_b32 v31, v30\n" \
  "s_nop 1\n" \
  "v_permlane16_swap_b32 v30, v31\n" \
  "v_max_u32 v30, v30, v31\n" \
  "v_mov_b32 v31, v30\n" \
  "s_nop 1\n" \
  "v_permlane32_swap_b32 v30, v31\n" \
  "v_max_u32 v30, v30, v31\n" \
  "s_lshr_b32 s88, s68, 10\n" \
  "s_lshl_b32 s88, s88, 8\n" \
  "s_add_u32 s88, s88, s24\n" \
  "s_cmp_eq_u32 s24, 0\n" \
  "v_readfirstlane_b32 s39, v30\n" \
  "v_mov_b32 v31, 1\n" \
  "s_cbranch_scc1 .LCV_FNOW_%=\n" \
  "s_waitcnt vmcnt(16)\n" \
  "v_readfirstlane_b32 s89, v9\n" \
  "s_add_u32 s89, s89, s25\n" \
  "s_sub_u32 s89, s89, %[g1]\n" \
  "s_sub_u32 s38, s89, s39\n" \
  "s_cmp_lt_u32 s38, 800\n" \
  "s_cselect_b32 s39, s89, s39\n" \
  ".LCV_FNOW_%=:\n" \
  "s_add_u32 s89, s39, %[d2]\n" \
  "s_waitcnt lgkmcnt(0)\n" \
  "s_add_u32 s38, vcc_lo, %[d4]\n" \
  "s_max_u32 s89, s89, s38\n" \
  "s_add_u32 s38, s89, %[d1]\n" \
  "v_mov_b32 v30, s38\n" \
  "s_mov_b64 exec, 1\n" \
  "buffer_atomic_umax v30, off, s[16:19], s88\n" \
  "buffer_atomic_add v31, off, s[16:19], s88 offset:64\n" \
  "s_mov_b64 exec, -1\n" \
  ".LCV_FON_%=:\n" \
  "s_add_u32 s62, s62, 0x80000\n" \
  "s_addc_u32 s63, s63, 0\n" \
  "s_add_u32 s64, s64, 0x40000\n" \
  "s_addc_u32 s65, s65, 0\n" \
  "s_add_u32 s68, s68, 0x1000\n" \
  "s_cmp_eq_u32 s73, 7\n" \
  "s_cbranch_scc1 .LCV_FCHK_%=\n" \
  "s_branch .LCV_FBND_%=\n" \
  ".LCV_FMORE_%=:\n" \
  "s_sleep %[sf]\n" \
  "global_load_dword v61, v60, s[52:53] offset:0 sc1\n" \
  "global_load_dword v62, v60, s[52:53] offset:128 sc1\n" \
  "global_load_dword v63, v60, s[52:53] offset:256 sc1\n" \
  "s_sub_u32 s87, s87, 1\n" \
  "s_cbranch_scc1 .LCV_END_%=\n" \
  ".LCV_FBND_%=:\n" \
  "s_waitcnt vmcnt(0)\n" \
  ".LCV_FCHK_%=:\n" \
  "v_min3_u32 v32, v61, v62, v63\n" \
  "v_cmp_eq_u32 vcc, 0, v32\n" \
  "s_cbranch_vccnz .LCV_FMORE_%=\n" \
  "v_add_f32 v24, v62, v63\n" \
  "v_mul_f32 v24, v24, v61\n" \
  "s_nop 1\n" \
  "v_max_f32_dpp v24, v24, v24 row_ror:8 row_mask:0xf bank_mask:0xf\n" \
  "s_nop 1\n" \
  "v_max_f32_dpp v24, v24, v24 row_ror:4 row_mask:0xf bank_mask:0xf\n" \
  "s_nop 1\n" \
  "v_max_f32_dpp v24, v24, v24 row_ror:2 row_mask:0xf bank_mask:0xf\n" \
  "s_nop 1\n" \
  "v_max_f32_dpp v24, v24, v24 row_ror:1 row_mask:0xf bank_mask:0xf\n" \
  "v_mov_b32 v25, v24\n" \
  "s_nop 1\n" \
  "v_permlane16_swap_b32 v24, v25\n" \
  "v_max_f32 v24, v24, v25\n" \
  "v_mul_f32 v24, 0x3f88f5c3, v24\n" \
  "s_nop 0\n" \
  "v_mul_f32 v25, 0x44870000, v24\n" \
  "v_readfirstlane_b32 s83, v24\n" \
  "v_readfirstlane_b32 s82, v25\n" \
  "s_lshr_b32 s82, s82, 23\n" \
  "s_addk_i32 s82, 0xff83\n" \
  "s_ashr_i32 s82, s82, 1\n" \
  "s_max_i32 s82, s82, -20\n" \
  "s_min_i32 s82, s82, 100\n" \
  "s_lshr_b32 s83, s83, 23\n" \
  "s_sub_i32 s83, s83, 242\n" \
  "s_cmp_lg_u32 s73, 7\n" \
  "s_cbranch_scc1 .LCV_BACK_%=\n" \
  "v_mov_b32 v25, s89\n" \
  "s_mov_b64 exec, 1\n" \
  "ds_write_b64 v1, v[24:25] offset:26624\n" \
  "s_mov_b64 exec, -1\n" \
  ".LCV_FWAIT_%=:\n" \
  "s_memrealtime s[38:39]\n" \
  "s_waitcnt lgkmcnt(0)\n" \
  "s_sub_u32 s38, s89, s38\n" \
  "s_cmp_lt_u32 s38, 800\n" \
  "s_cbranch_scc1 .LCV_FWAIT_%=\n" \
  "s_waitcnt vmcnt(2)\n" \
  "s_branch .LCV_BACK_%=\n" \
  ".LCV_LAST_%=:\n" \
  "s_waitcnt vmcnt(0)\n" \
  ".LCV_END_%=:\n" \
  "s_endpgm\n" \
  ".p2alignl 12, 0xbf800000\n" \
  ".Lpad0x64:\n" \
  "s_setpc_b64 s[98:99]\n"
#define QKV_ASM_0 \
  "s_cmp_lg_u32 s69, 0\n" \
  "s_mov_b32 s69, 0\n" \
  "s_cbranch_scc1 .LQA_GOT_%=\n" \
  "s_branch .LQA_POLL_%=\n" \
  "s_branch .LQA_AL_%=\n" \
  ".p2alignl 12, 0xbf800000, 2600\n" \
  ".p2align 6\n" \
  ".Lpad0x95:\n" \
  "s_setpc_b64 s[98:99]\n" \
  ".p2align 6\n" \
  ".LQA_AL_%=:\n" \
  ".LQA_TOP_%=:\n" \
  "s_barrier\n" \
  "ds_read_b128 v[36:39], v7 offset:0\n" \
  "ds_read_b128 v[40:43], v7 offset:2048\n" \
  "ds_read_b128 v[44:47], v7 offset:4096\n" \
  "s_setprio 2\n" \
  "s_waitcnt lgkmcnt(2)\n" \
  "v_mfma_f32_16x16x32_bf16 v[12:15], v[64:67], v[36:39], 0\n" \
  "ds_read_b128 v[48:51], v7 offset:6144\n" \
  "v_mfma_f32_16x16x32_bf16 v[16:19], v[128:131], v[36:39], 0\n" \
  "v_mfma_f32_16x16x32_bf16 v[20:23], v[36:39], v[192:195], 0\n" \
  "s_cmp_lg_u32 s69, 1\n" \
  "s_cbranch_scc1 .LQA_FDN_%=\n" \
  "s_waitcnt vmcnt(0)\n" \
  "v_mov_b32 v52, s80\n" \
  "v_mov_b32 v53, 0x10000\n" \
  "s_mov_b64 exec, 1\n" \
  "ds_add_rtn_u32 v54, v52, v53\n" \
  "s_memrealtime s[0:1]\n" \
  "s_mov_b64 exec, -1\n" \
  "s_add_u32 s67, s67, 4\n" \
  "s_add_u32 s64, s64, 4096\n" \
  "s_addc_u32 s65, s65, 0\n" \
  "v_mov_b32 v55, 0\n" \
  "s_waitcnt lgkmcnt(0)\n" \
  "v_readfirstlane_b32 s74, v54\n" \
  "s_and_b32 s76, s74, 0x30000\n" \
  "s_cmp_lg_u32 s76, 0x30000\n" \
  "s_cbranch_scc1 .LQA_FDN_%=\n" \
  "s_mov_b32 s79, 0x100000\n" \
  "s_branch .LQA_FDT_%=\n" \
  ".LQA_FD_%=:\n" \
  "ds_read_b32 v54, v52\n" \
  "s_sub_u32 s79, s79, 1\n" \
  "s_cbranch_scc1 .LQA_FDE_%=\n" \
  "s_waitcnt lgkmcnt(0)\n" \
  "v_readfirstlane_b32 s74, v54\n" \
  ".LQA_FDT_%=:\n" \
  "s_and_b32 s74, s74, 0xffff\n" \
  "s_cmp_lt_u32 s74, s67\n" \
  "s_cbranch_scc1 .LQA_FD_%=\n" \
  ".LQA_FDE_%=:\n" \
  "v_or_b32 v53, 1, s0\n" \
  "s_cmp_lg_u32 s67, s83\n" \
  "s_cbranch_scc1 .LQA_FDX_%=\n" \
  "v_lshlrev_b32 v54, 5, v6\n" \
  "s_mov_b64 exec, 15\n" \
  "global_store_dword v54, v53, s[84:85] sc1\n" \
  ".LQA_FDX_%=:\n" \
  "s_mov_b64 exec, 1\n" \
  "global_store_dword v55, v53, s[64:65] offset:-4096 sc1\n" \
  "s_mov_b64 exec, -1\n" \
  ".LQA_FDN_%=:\n" \
  "s_waitcnt lgkmcnt(2)\n" \
  "v_mfma_f32_16x16x32_bf16 v[12:15], v[68:71], v[40:43], v[12:15]\n" \
  "ds_read_b128 v[36:39], v7 offset:8192\n" \
  "v_mov_b64 v[24:25], 0\n" \
  "v_lshrrev_b32 v26, 1, v6\n" \
  "buffer_store_dwordx2 v[24:25], v26, s[48:51], s61 offen sc1\n" \
  "v_mfma_f32_16x16x32_bf16 v[16:19], v[132:135], v[40:43], v[16:19]\n" \
  "v_mov_b32 v11, s82\n" \
  "v_mfma_f32_16x16x32_bf16 v[20:23], v[40:43], v[196:199], v[20:23]\n" \
  "s_waitcnt lgkmcnt(2)\n" \
  "v_mfma_f32_16x16x32_bf16 v[12:15], v[72:75], v[44:47], v[12:15]\n" \
  "ds_read_b128 v[40:43], v7 offset:10240\n" \
  "v_mfma_f32_16x16x32_bf16 v[16:19], v[136:139], v[44:47], v[16:19]\n" \
  "v_mfma_f32_16x16x32_bf16 v[20:23], v[44:47], v[200:203], v[20:23]\n" \
  "s_waitcnt lgkmcnt(2)\n" \
  "v_mfma_f32_16x16x32_bf16 v[12:15], v[76:79], v[48:51], v[12:15]\n" \
  "ds_read_b128 v[44:47], v7 offset:12288\n" \
  "v_mfma_f32_16x16x32_bf16 v[16:19], v[140:143], v[48:51], v[16:19]\n" \
  "v_mfma_f32_16x16x32_bf16 v[20:23], v[48:51], v[204:207], v[20:23]\n" \
  "s_waitcnt lgkmcnt(2)\n" \
  "v_mfma_f32_16x16x32_bf16 v[12:15], v[80:83], v[36:39], v[12:15]\n" \
  "ds_read_b128 v[48:51], v7 offset:14336\n" \
  "v_mfma_f32_16x16x32_bf16 v[16:19], v[144:147], v[36:39], v[16:19]\n" \
  "v_mfma_f32_16x16x32_bf16 v[20:23], v[36:39], v[208:211], v[20:23]\n" \
  "s_waitcnt lgkmcnt(2)\n" \
  "v_mfma_f32_16x16x32_bf16 v[12:15], v[84:87], v[40:43], v[12:15]\n" \
  "ds_read_b128 v[36:39], v7 offset:16384\n" \
  "v_mfma_f32_16x16x32_bf16 v[16:19], v[148:151], v[40:43], v[16:19]\n" \
  "v_mfma_f32_16x16x32_bf16 v[20:23], v[40:43], v[212:215], v[20:23]\n" \
  "s_waitcnt lgkmcnt(2)\n" \
  "v_mfma_f32_16x16x32_bf16 v[12:15], v[88:91], v[44:47], v[12:15]\n" \
  "ds_read_b128 v[40:43], v7 offset:18432\n" \
  "v_mfma_f32_16x16x32_bf16 v[16:19], v[152:155], v[44:47], v[16:19]\n" \
  "v_mfma_f32_16x16x32_bf16 v[20:23], v[44:47], v[216:219], v[20:23]\n" \
  "s_waitcnt lgkmcnt(2)\n" \
  "v_mfma_f32_16x16x32_bf16 v[12:15], v[92:95], v[48:51], v[12:15]\n" \
  "ds_read_b128 v[44:47], v7 offset:20480\n" \
  "v_mfma_f32_16x16x32_bf16 v[16:19], v[156:159], v[48:51], v[16:19]\n" \
  "v_mfma_f32_16x16x32_bf16 v[20:23], v[48:51], v[220:223], v[20:23]\n" \
  "s_waitcnt lgkmcnt(2)\n" \
  "v_mfma_f32_16x16x32_bf16 v[12:15], v[96:99], v[36:39], v[12:15]\n" \
  "ds_read_b128 v[48:51], v7 offset:22528\n" \
  "v_mfma_f32_16x16x32_bf16 v[16:19], v[160:163], v[36:39], v[16:19]\n" \
  "v_mfma_f32_16x16x32_bf16 v[20:23], v[36:39], v[224:227], v[20:23]\n" \
  "s_waitcnt lgkmcnt(2)\n" \
  "v_mfma_f32_16x16x32_bf16 v[12:15], v[100:103], v[40:43], v[12:15]\n" \
  "ds_read_b128 v[36:39], v7 offset:24576\n" \
  "v_mfma_f32_16x16x32_bf16 v[16:19], v[164:167], v[40:43], v[16:19]\n" \
  "v_mfma_f32_16x16x32_bf16 v[20:23], v[40:43], v[228:231], v[20:23]\n" \
  "s_waitcnt lgkmcnt(2)\n" \
  "v_mfma_f32_16x16x32_bf16 v[12:15], v[104:107], v[44:47], v[12:15]\n" \
  "ds_read_b128 v[40:43], v7 offset:26624\n" \
  "v_mfma_f32_16x16x32_bf16 v[16:19], v[168:171], v[44:47], v[16:19]\n" \
  "v_mfma_f32_16x16x32_bf16 v[20:23], v[44:47], v[232:235], v[20:23]\n" \
  "s_waitcnt lgkmcnt(2)\n" \
  "v_mfma_f32_16x16x32_bf16 v[12:15], v[108:111], v[48:51], v[12:15]\n" \
  "ds_read_b128 v[44:47], v7 offset:28672\n" \
  "v_mfma_f32_16x16x32_bf16 v[16:19], v[172:175], v[48:51], v[16:19]\n" \
  "v_mfma_f32_16x16x32_bf16 v[20:23], v[48:51], v[236:239], v[20:23]\n" \
  "s_waitcnt lgkmcnt(2)\n" \
  "v_mfma_f32_16x16x32_bf16 v[12:15], v[112:115], v[36:39], v[12:15]\n" \
  "ds_read_b128 v[48:51], v7 offset:30720\n" \
  "v_mfma_f32_16x16x32_bf16 v[16:19], v[176:179], v[36:39], v[16:19]\n" \
  "v_mfma_f32_16x16x32_bf16 v[20:23], v[36:39], v[240:243], v[20:23]\n" \
  "s_waitcnt lgkmcnt(2)\n" \
  "v_mfma_f32_16x16x32_bf16 v[12:15], v[116:119], v[40:43], v[12:15]\n" \
  "ds_read_b128 v[36:39], v7 offset:256\n" \
  "v_mfma_f32_16x16x32_bf16 v[16:19], v[180:183], v[40:43], v[16:19]\n" \
  "v_mfma_f32_16x16x32_bf16 v[20:23], v[40:43], v[244:247], v[20:23]\n" \
  "s_waitcnt lgkmcnt(2)\n" \
  "v_mfma_f32_16x16x32_bf16 v[12:15], v[120:123], v[44:47], v[12:15]\n" \
  "ds_read_b128 v[40:43], v7 offset:2304\n" \
  "v_mfma_f32_16x16x32_bf16 v[16:19], v[184:187], v[44:47], v[16:19]\n" \
  "v_mfma_f32_16x16x32_bf16 v[20:23], v[44:47], v[248:251], v[20:23]\n" \
  "s_waitcnt lgkmcnt(2)\n" \
  "v_mfma_f32_16x16x32_bf16 v[12:15], v[124:127], v[48:51], v[12:15]\n" \
  "ds_read_b128 v[44:47], v7 offset:4352\n" \
  "v_mfma_f32_16x16x32_bf16 v[16:19], v[188:191], v[48:51], v[16:19]\n" \
  "v_mfma_f32_16x16x32_bf16 v[20:23], v[48:51], v[252:255], v[20:23]\n" \
  "s_waitcnt lgkmcnt(2)\n" \
  "v_mfma_f32_16x16x32_bf16 v[24:27], v[64:67], v[36:39], 0\n" \
  "ds_read_b128 v[48:51], v7 offset:6400\n" \
  "v_mfma_f32_16x16x32_bf16 v[28:31], v[128:131], v[36:39], 0\n" \
  "v_mfma_f32_16x16x32_bf16 v[32:35], v[36:39], v[192:195], 0\n" \
  "s_waitcnt lgkmcnt(2)\n" \
  "v_mfma_f32_16x16x32_bf16 v[24:27], v[68:71], v[40:43], v[24:27]\n" \
  "ds_read_b128 v[36:39], v7 offset:8448\n" \
  "v_mfma_f32_16x16x32_bf16 v[28:31], v[132:135], v[40:43], v[28:31]\n" \
  "v_mfma_f32_16x16x32_bf16 v[32:35], v[40:43], v[196:199], v[32:35]\n" \
  "s_waitcnt lgkmcnt(2)\n" \
  "v_mfma_f32_16x16x32_bf16 v[24:27], v[72:75], v[44:47], v[24:27]\n" \
  "ds_read_b128 v[40:43], v7 offset:10496\n" \
  "v_mfma_f32_16x16x32_bf16 v[28:31], v[136:139], v[44:47], v[28:31]\n" \
  "v_mfma_f32_16x16x32_bf16 v[32:35], v[44:47], v[200:203], v[32:35]\n" \
  "s_waitcnt lgkmcnt(2)\n" \
  "v_mfma_f32_16x16x32_bf16 v[24:27], v[76:79], v[48:51], v[24:27]\n" \
  "ds_read_b128 v[44:47], v7 offset:12544\n" \
  "v_mfma_f32_16x16x32_bf16 v[28:31], v[140:143], v[48:51], v[28:31]\n" \
  "v_mfma_f32_16x16x32_bf16 v[32:35], v[48:51], v[204:207], v[32:35]\n" \
  "s_waitcnt lgkmcnt(2)\n" \
  "v_mfma_f32_16x16x32_bf16 v[24:27], v[80:83], v[36:39], v[24:27]\n" \
  "ds_read_b128 v[48:51], v7 offset:14592\n" \
  "v_mfma_f32_16x16x32_bf16 v[28:31], v[144:147], v[36:39], v[28:31]\n" \
  "v_mfma_f32_16x16x32_bf16 v[32:35], v[36:39], v[208:211], v[32:35]\n" \
  "s_barrier\n" \
  "s_mov_b32 s0, 0xbe801d62\n" \
  ".set .Lpad0x96, . - 4\n" \
  "s_waitcnt lgkmcnt(2)\n" \
  "v_mfma_f32_16x16x32_bf16 v[24:27], v[84:87], v[40:43], v[24:27]\n" \
  "ds_read_b128 v[36:39], v7 offset:16640\n" \
  "ds_read_b32 v11, v11 offset:0\n" \
  "v_mfma_f32_16x16x32_bf16 v[28:31], v[148:151], v[40:43], v[28:31]\n" \
  "ds_read2_b32 v[52:53], v8 offset0:0 offset1:0\n" \
  "v_mfma_f32_16x16x32_bf16 v[32:35], v[40:43], v[212:215], v[32:35]\n" \
  "s_waitcnt lgkmcnt(4)\n" \
  "v_mfma_f32_16x16x32_bf16 v[24:27], v[88:91], v[44:47], v[24:27]\n" \
  "ds_read_b128 v[40:43], v7 offset:18688\n" \
  "v_mfma_f32_16x16x32_bf16 v[28:31], v[152:155], v[44:47], v[28:31]\n" \
  "ds_read2_b32 v[54:55], v8 offset0:16 offset1:16\n" \
  "v_mfma_f32_16x16x32_bf16 v[32:35], v[44:47], v[216:219], v[32:35]\n" \
  "s_waitcnt lgkmcnt(3)\n" \
  "v_mfma_f32_16x16x32_bf16 v[24:27], v[92:95], v[48:51], v[24:27]\n" \
  "ds_read_b128 v[44:47], v7 offset:20736\n" \
  "v_readfirstlane_b32 s69, v11\n" \
  "s_cmp_eq_u32 s69, 1\n" \
  "s_cselect_b32 s76, 0x100000, s62\n" \
  "v_mfma_f32_16x16x32_bf16 v[28:31], v[156:159], v[48:51], v[28:31]\n" \
  "buffer_load_dword v11, v10, s[52:55], s76 offen sc1\n" \
  "v_mfma_f32_16x16x32_bf16 v[32:35], v[48:51], v[220:223], v[32:35]\n" \
  "ds_read2_b32 v[56:57], v8 offset0:32 offset1:48\n" \
  "v_mfma_f32_16x16x32_bf16 v[24:27], v[96:99], v[36:39], v[24:27]\n" \
  "ds_read_b128 v[48:51], v7 offset:22784\n" \
  "v_mfma_f32_16x16x32_bf16 v[28:31], v[160:163], v[36:39], v[28:31]\n" \
  "v_mfma_f32_16x16x32_bf16 v[32:35], v[36:39], v[224:227], v[32:35]\n" \
  "ds_read_b128 v[58:61], v9 offset:128\n" \
  "s_waitcnt lgkmcnt(6)\n" \
  "v_mfma_f32_16x16x16_bf16 v[12:15], v[0:1], v[52:53], v[12:15]\n" \
  "v_mfma_f32_16x16x16_bf16 v[16:19], v[2:3], v[52:53], v[16:19]\n" \
  "v_mfma_f32_16x16x16_bf16 v[20:23], v[52:53], v[4:5], v[20:23]\n" \
  "s_waitcnt lgkmcnt(4)\n" \
  "v_mfma_f32_16x16x16_bf16 v[24:27], v[0:1], v[54:55], v[24:27]\n" \
  "v_mfma_f32_16x16x16_bf16 v[28:31], v[2:3], v[54:55], v[28:31]\n" \
  "v_mfma_f32_16x16x16_bf16 v[32:35], v[54:55], v[4:5], v[32:35]\n" \
  "v_mfma_f32_16x16x32_bf16 v[24:27], v[100:103], v[40:43], v[24:27]\n" \
  "ds_read_b128 v[36:39], v7 offset:24832\n" \
  "v_mul_f32 v12, v12, v56\n" \
  "v_mfma_f32_16x16x32_bf16 v[28:31], v[164:167], v[40:43], v[28:31]\n" \
  "v_mul_f32 v13, v13, v56\n" \
  "v_mfma_f32_16x16x32_bf16 v[32:35], v[40:43], v[228:231], v[32:35]\n" \
  "ds_read_b128 v[52:55], v9 offset:192\n" \
  "s_waitcnt lgkmcnt(5)\n" \
  "v_mfma_f32_16x16x32_bf16 v[24:27], v[104:107], v[44:47], v[24:27]\n" \
  "ds_read_b128 v[40:43], v7 offset:26880\n" \
  "v_mul_f32 v14, v14, v56\n" \
  "v_mfma_f32_16x16x32_bf16 v[28:31], v[168:171], v[44:47], v[28:31]\n" \
  "v_cvt_pk_bf16_f32 v12, v12, v13\n" \
  "v_mfma_f32_16x16x32_bf16 v[32:35], v[44:47], v[232:235], v[32:35]\n" \
  "v_mul_f32 v15, v15, v56\n" \
  "s_waitcnt lgkmcnt(4)\n" \
  "v_mfma_f32_16x16x32_bf16 v[24:27], v[108:111], v[48:51], v[24:27]\n" \
  "ds_read_b128 v[44:47], v7 offset:28928\n" \
  "v_mul_f32 v16, v16, v56\n" \
  "v_mfma_f32_16x16x32_bf16 v[28:31], v[172:175], v[48:51], v[28:31]\n" \
  "v_cvt_pk_bf16_f32 v13, v14, v15\n" \
  "v_mfma_f32_16x16x32_bf16 v[32:35], v[48:51], v[236:239], v[32:35]\n" \
  "v_mul_f32 v17, v17, v56\n" \
  "s_waitcnt lgkmcnt(3)\n" \
  "v_mfma_f32_16x16x32_bf16 v[24:27], v[112:115], v[36:39], v[24:27]\n" \
  "ds_read_b128 v[48:51], v7 offset:30976\n" \
  "v_mul_f32 v18, v18, v56\n" \
  "v_mfma_f32_16x16x32_bf16 v[28:31], v[176:179], v[36:39], v[28:31]\n" \
  "v_cvt_pk_bf16_f32 v14, v16, v17\n" \
  "v_mfma_f32_16x16x32_bf16 v[32:35], v[36:39], v[240:243], v[32:35]\n" \
  "v_mul_f32 v19, v19, v56\n" \
  "s_waitcnt lgkmcnt(2)\n" \
  "v_mfma_f32_16x16x32_bf16 v[24:27], v[116:119], v[40:43], v[24:27]\n" \
  "v_cvt_pk_bf16_f32 v15, v18, v19\n" \
  "v_mfma_f32_16x16x32_bf16 v[28:31], v[180:183], v[40:43], v[28:31]\n" \
  "buffer_store_dwordx4 v[12:15], v6, s[40:43], s59 offen offset:0 sc1\n" \
  "v_mfma_f32_16x16x32_bf16 v[32:35], v[40:43], v[244:247], v[32:35]\n" \
  "v_mul_f32 v20, v20, v58\n" \
  "s_waitcnt lgkmcnt(1)\n" \
  "v_mfma_f32_16x16x32_bf16 v[24:27], v[120:123], v[44:47], v[24:27]\n" \
  "v_mul_f32 v21, v21, v59\n" \
  "v_mfma_f32_16x16x32_bf16 v[28:31], v[184:187], v[44:47], v[28:31]\n" \
  "v_mul_f32 v22, v22, v60\n" \
  "v_mfma_f32_16x16x32_bf16 v[32:35], v[44:47], v[248:251], v[32:35]\n" \
  "v_mul_f32 v23, v23, v61\n" \
  "s_waitcnt lgkmcnt(0)\n" \
  "v_mfma_f32_16x16x32_bf16 v[24:27], v[124:127], v[48:51], v[24:27]\n" \
  "v_cvt_pk_bf16_f32 v20, v20, v21\n" \
  "v_mfma_f32_16x16x32_bf16 v[28:31], v[188:191], v[48:51], v[28:31]\n" \
  "v_cvt_pk_bf16_f32 v21, v22, v23\n" \
  "v_mfma_f32_16x16x32_bf16 v[32:35], v[48:51], v[252:255], v[32:35]\n" \
  "s_nop 2\n" \
  "s_setprio 2\n" \
  "s_cmp_eq_u32 s69, 1\n" \
  "s_cbranch_scc1 .LQA_EPI_%=\n" \
  "s_waitcnt vmcnt(1)\n" \
  "v_cmp_eq_u32 vcc, 0, v11\n" \
  "s_cmp_eq_u64 vcc, 0\n" \
  "s_cbranch_scc0 .LQA_MISS_%=\n" \
  "s_mov_b32 m0, s58\n" \
  "s_add_u32 s74, s56, 4096\n" \
  "s_addc_u32 s75, s57, 0\n" \
  "s_add_u32 s76, s58, 4096\n" \
  "global_load_lds_dwordx4 v6, s[56:57] offset:0 sc1\n" \
  "global_load_lds_dwordx4 v6, s[56:57] offset:1024 sc1\n" \
  "global_load_lds_dwordx4 v6, s[56:57] offset:2048 sc1\n" \
  "global_load_lds_dwordx4 v6, s[56:57] offset:3072 sc1\n" \
  "s_mov_b32 m0, s76\n" \
  "s_nop 0\n" \
  "global_load_lds_dwordx4 v6, s[74:75] offset:0 sc1\n" \
  "global_load_lds_dwordx4 v6, s[74:75] offset:1024 sc1\n" \
  "global_load_lds_dwordx4 v6, s[74:75] offset:2048 sc1\n" \
  "global_load_lds_dwordx4 v6, s[74:75] offset:3072 sc1\n" \
  "s_mov_b32 s69, 3\n" \
  ".LQA_EPI_%=:\n" \
  "v_mul_f32 v24, v24, v57\n" \
  "v_mul_f32 v25, v25, v57\n" \
  "v_mul_f32 v26, v26, v57\n" \
  "v_cvt_pk_bf16_f32 v24, v24, v25\n" \
  "v_mul_f32 v27, v27, v57\n" \
  "v_mul_f32 v28, v28, v57\n" \
  "v_cvt_pk_bf16_f32 v25, v26, v27\n" \
  "v_mul_f32 v29, v29, v57\n" \
  "v_mul_f32 v30, v30, v57\n" \
  "v_cvt_pk_bf16_f32 v26, v28, v29\n" \
  "v_mul_f32 v31, v31, v57\n" \
  "v_cvt_pk_bf16_f32 v27, v30, v31\n" \
  "s_add_u32 s74, s59, 4096\n" \
  "buffer_store_dwordx4 v[24:27], v6, s[40:43], s74 offen offset:0 sc1\n" \
  "v_mul_f32 v32, v32, v52\n" \
  "v_mul_f32 v33, v33, v53\n" \
  "v_mul_f32 v34, v34, v54\n" \
  "v_mul_f32 v35, v35, v55\n" \
  "v_cvt_pk_bf16_f32 v22, v32, v33\n" \
  "v_cvt_pk_bf16_f32 v23, v34, v35\n" \
  "buffer_store_dwordx4 v[20:23], v6, s[44:47], s60 offen offset:0 sc1\n" \
  "s_setprio 0\n" \
  "s_add_u32 s59, s59, 0x80000\n" \
  "s_add_u32 s60, s60, 0x40000\n" \
  "s_add_u32 s61, s61, 0x10000\n" \
  "v_xor_b32 v7, 0x20000, v7\n" \
  "s_cmp_eq_u32 s69, 1\n" \
  "s_waitcnt vmcnt(3)\n" \
  "s_cbranch_scc1 .LQA_GO_%=\n" \
  "s_lshr_b32 s69, s69, 1\n" \
  "s_waitcnt vmcnt(2)\n" \
  "s_cbranch_scc1 .LQA_GO_%=\n" \
  "v_cmp_eq_u32 vcc, 0, v11\n" \
  "s_cmp_eq_u64 vcc, 0\n" \
  "s_cbranch_scc0 .LQA_NOX_%=\n" \
  "s_mov_b32 m0, s58\n" \
  "s_add_u32 s74, s56, 4096\n" \
  "s_addc_u32 s75, s57, 0\n" \
  "s_add_u32 s76, s58, 4096\n" \
  "global_load_lds_dwordx4 v6, s[56:57] offset:0 sc1\n" \
  "global_load_lds_dwordx4 v6, s[56:57] offset:1024 sc1\n" \
  "global_load_lds_dwordx4 v6, s[56:57] offset:2048 sc1\n" \
  "global_load_lds_dwordx4 v6, s[56:57] offset:3072 sc1\n" \
  "s_mov_b32 m0, s76\n" \
  "s_nop 0\n" \
  "global_load_lds_dwordx4 v6, s[74:75] offset:0 sc1\n" \
  "global_load_lds_dwordx4 v6, s[74:75] offset:1024 sc1\n" \
  "global_load_lds_dwordx4 v6, s[74:75] offset:2048 sc1\n" \
  "global_load_lds_dwordx4 v6, s[74:75] offset:3072 sc1\n" \
  "s_mov_b32 s69, 2\n" \
  "s_waitcnt vmcnt(8)\n" \
  "s_branch .LQA_CNT_%=\n" \
  ".Lpad0x97:\n" \
  "s_setpc_b64 s[98:99]\n" \
  ".LQA_MISS_%=:\n" \
  "buffer_load_dword v11, v10, s[52:55], s62 offen sc1\n" \
  "s_branch .LQA_EPI_%=\n" \
  ".LQA_NOX_%=:\n" \
  "s_waitcnt vmcnt(0)\n" \
  ".LQA_CNT_%=:\n" \
  "v_mov_b32 v52, s80\n" \
  "v_mov_b32 v53, 0x10000\n" \
  "s_mov_b64 exec, 1\n" \
  "ds_add_rtn_u32 v54, v52, v53\n" \
  "s_memrealtime s[0:1]\n" \
  "s_mov_b64 exec, -1\n" \
  "s_add_u32 s67, s67, 4\n" \
  "s_add_u32 s64, s64, 4096\n" \
  "s_addc_u32 s65, s65, 0\n" \
  "v_mov_b32 v55, 0\n" \
  "s_waitcnt lgkmcnt(0)\n" \
  "v_readfirstlane_b32 s74, v54\n" \
  "s_and_b32 s76, s74, 0x30000\n" \
  "s_cmp_lg_u32 s76, 0x30000\n" \
  "s_cbranch_scc1 .LQA_FWN_%=\n" \
  "s_mov_b32 s79, 0x100000\n" \
  "s_branch .LQA_FWT_%=\n" \
  ".LQA_FW_%=:\n" \
  "ds_read_b32 v54, v52\n" \
  "s_sub_u32 s79, s79, 1\n" \
  "s_cbranch_scc1 .LQA_FWE_%=\n" \
  "s_waitcnt lgkmcnt(0)\n" \
  "v_readfirstlane_b32 s74, v54\n" \
  ".LQA_FWT_%=:\n" \
  "s_and_b32 s74, s74, 0xffff\n" \
  "s_cmp_lt_u32 s74, s67\n" \
  "s_cbranch_scc1 .LQA_FW_%=\n" \
  ".LQA_FWE_%=:\n" \
  "v_or_b32 v53, 1, s0\n" \
  "s_cmp_lg_u32 s67, s83\n" \
  "s_cbranch_scc1 .LQA_FWX_%=\n" \
  "v_lshlrev_b32 v54, 5, v6\n" \
  "s_mov_b64 exec, 15\n" \
  "global_store_dword v54, v53, s[84:85] sc1\n" \
  ".LQA_FWX_%=:\n" \
  "s_mov_b64 exec, 1\n" \
  "global_store_dword v55, v53, s[64:65] offset:-4096 sc1\n" \
  "s_mov_b64 exec, -1\n" \
  "s_waitcnt vmcnt(1)\n" \
  "s_branch .LQA_GO_%=\n" \
  ".LQA_FWN_%=:\n" \
  "s_waitcnt vmcnt(0)\n" \
  ".LQA_GO_%=:\n" \
  "s_sub_u32 s66, s66, 1\n" \
  "s_cbranch_scc1 .LQA_LAST_%=\n" \
  "s_cmp_lg_u32 s69, 0\n" \
  "s_cbranch_scc1 .LQA_NEXT_%=\n" \
  ".LQA_GATE_%=:\n" \
  "s_memrealtime s[74:75]\n" \
  "s_waitcnt lgkmcnt(0)\n" \
  "s_sub_i32 s74, s68, s74\n" \
  "s_cmp_gt_u32 s74, %[gq]\n" \
  "s_cbranch_scc1 .LQA_POLL_%=\n" \
  "s_sleep 3\n" \
  "s_branch .LQA_GATE_%=\n" \
  ".LQA_POLL_%=:\n" \
  "s_mov_b32 s79, 0x40000\n" \
  ".LQA_AGAIN_%=:\n" \
  "buffer_load_dword v11, v10, s[52:55], s62 offen sc1\n" \
  "s_sub_u32 s79, s79, 1\n" \
  "s_cbranch_scc1 .LQA_SEEN_%=\n" \
  "s_waitcnt vmcnt(0)\n" \
  "v_cmp_eq_u32 vcc, 0, v11\n" \
  "s_cmp_eq_u64 vcc, 0\n" \
  "s_cbranch_scc1 .LQA_SEEN_%=\n" \
  "s_sleep %[sq]\n" \
  "s_cmp_ge_u32 s66, s63\n" \
  "s_cbranch_scc1 .LQA_AGAIN_%=\n" \
  "s_sleep %[s1]\n" \
  "s_branch .LQA_AGAIN_%=\n" \
  ".LQA_SEEN_%=:\n" \
  "s_mov_b32 m0, s58\n" \
  "s_add_u32 s74, s56, 4096\n" \
  "s_addc_u32 s75, s57, 0\n" \
  "s_add_u32 s76, s58, 4096\n" \
  "global_load_lds_dwordx4 v6, s[56:57] offset:0 sc1\n" \
  "global_load_lds_dwordx4 v6, s[56:57] offset:1024 sc1\n" \
  "global_load_lds_dwordx4 v6, s[56:57] offset:2048 sc1\n" \
  "global_load_lds_dwordx4 v6, s[56:57] offset:3072 sc1\n" \
  "s_mov_b32 m0, s76\n" \
  "s_nop 0\n" \
  "global_load_lds_dwordx4 v6, s[74:75] offset:0 sc1\n" \
  "global_load_lds_dwordx4 v6, s[74:75] offset:1024 sc1\n" \
  "global_load_lds_dwordx4 v6, s[74:75] offset:2048 sc1\n" \
  "global_load_lds_dwordx4 v6, s[74:75] offset:3072 sc1\n" \
  ".LQA_GOT_%=:\n" \
  "s_memrealtime s[76:77]\n" \
  "s_waitcnt lgkmcnt(0)\n" \
  "s_add_u32 s68, s76, %[gq]\n" \
  "s_waitcnt vmcnt(0)\n" \
  ".LQA_NEXT_%=:\n" \
  "s_add_u32 s56, s56, 0x8000\n" \
  "s_addc_u32 s57, s57, 0\n" \
  "s_xor_b32 s58, s58, 0x20000\n" \
  "s_add_u32 s62, s62, 4096\n" \
  "s_branch .LQA_TOP_%=\n" \
  ".LQA_LAST_%=:\n" \
  "s_barrier\n" \
  "s_cmp_eq_u32 s86, 0\n" \
  "s_cbranch_scc1 .LQA_END_%=\n" \
  "s_mov_b32 s86, 0\n" \
  "s_mov_b32 s66, 63\n" \
  "s_mov_b32 s62, 0x40000\n" \
  "s_add_u32 s54, s54, (128 - %[n0]) * 4096\n" \
  "s_add_u32 s59, s59, (64 - %[n0]) * 0x80000\n" \
  "s_add_u32 s60, s60, (64 - %[n0]) * 0x40000\n" \
  "s_add_u32 s61, s61, (64 - %[n0]) * 0x10000\n" \
  "s_add_u32 s64, s64, (65 - %[n0]) * 4096\n" \
  "s_addc_u32 s65, s65, 0\n" \
  "s_add_u32 s56, s56, %[x3]\n" \
  "s_addc_u32 s57, s57, 0\n" \
  "s_add_u32 s84, s84, 2048\n" \
  "s_addc_u32 s85, s85, 0\n" \
  "s_add_u32 s67, s67, 4\n" \
  "s_add_u32 s83, s67, 4\n" \
  "s_branch .LQA_POLL_%=\n" \
  ".LQA_END_%=:\n" \
  "s_endpgm\n" \
  ".p2alignl 12, 0xbf800000\n" \
  ".Lpad0xba:\n" \
  "s_setpc_b64 s[98:99]\n"
#define QKV_ASM_1 \
  ".LQB_GO_%=:\n" \
  "s_barrier\n" \
  "ds_read_b128 v[36:39], v10 offset:0\n" \
  "ds_read_b128 v[40:43], v7 offset:0\n" \
  "ds_read_b128 v[44:47], v10 offset:2048\n" \
  "ds_read_b128 v[48:51], v7 offset:2048\n" \
  "s_branch .LQB_AL_%=\n" \
  ".p2alignl 12, 0xbf800000, 3200\n" \
  ".p2align 6\n" \
  ".Lpad0xa9:\n" \
  "s_setpc_b64 s[98:99]\n" \
  ".p2align 6\n" \
  ".LQB_AL_%=:\n" \
  ".LQB_TOP_%=:\n" \
  "s_setprio 3\n" \
  "s_waitcnt lgkmcnt(3)\n" \
  "v_mfma_f32_16x16x32_bf16 v[56:59], v[36:39], v[36:39], 0\n" \
  "ds_read_b128 v[52:55], v10 offset:4096\n" \
  "s_add_u32 s56, s56, 0x8000\n" \
  "s_addc_u32 s57, s57, 0\n" \
  "s_xor_b32 s58, s58, 0x20000\n" \
  "s_add_u32 s62, s62, 4096\n" \
  "s_waitcnt lgkmcnt(3)\n" \
  "v_mfma_f32_16x16x32_bf16 v[12:15], v[40:43], v[192:195], 0\n" \
  "ds_read_b128 v[36:39], v7 offset:4096\n" \
  "s_waitcnt lgkmcnt(3)\n" \
  "v_mfma_f32_16x16x32_bf16 v[56:59], v[44:47], v[44:47], v[56:59]\n" \
  "ds_read_b128 v[40:43], v10 offset:6144\n" \
  "s_waitcnt lgkmcnt(3)\n" \
  "v_mfma_f32_16x16x32_bf16 v[12:15], v[48:51], v[196:199], v[12:15]\n" \
  "ds_read_b128 v[44:47], v7 offset:6144\n" \
  "s_waitcnt lgkmcnt(3)\n" \
  "v_mfma_f32_16x16x32_bf16 v[56:59], v[52:55], v[52:55], v[56:59]\n" \
  "ds_read_b128 v[48:51], v10 offset:8192\n" \
  "s_waitcnt lgkmcnt(3)\n" \
  "v_mfma_f32_16x16x32_bf16 v[12:15], v[36:39], v[200:203], v[12:15]\n" \
  "ds_read_b128 v[52:55], v7 offset:8192\n" \
  "s_waitcnt lgkmcnt(3)\n" \
  "v_mfma_f32_16x16x32_bf16 v[56:59], v[40:43], v[40:43], v[56:59]\n" \
  "ds_read_b128 v[36:39], v10 offset:10240\n" \
  "s_waitcnt lgkmcnt(3)\n" \
  "v_mfma_f32_16x16x32_bf16 v[12:15], v[44:47], v[204:207], v[12:15]\n" \
  "ds_read_b128 v[40:43], v7 offset:10240\n" \
  "s_waitcnt lgkmcnt(3)\n" \
  "v_mfma_f32_16x16x32_bf16 v[56:59], v[48:51], v[48:51], v[56:59]\n" \
  "ds_read_b128 v[44:47], v10 offset:12288\n" \
  "s_waitcnt lgkmcnt(3)\n" \
  "v_mfma_f32_16x16x32_bf16 v[12:15], v[52:55], v[208:211], v[12:15]\n" \
  "ds_read_b128 v[48:51], v7 offset:12288\n" \
  "s_waitcnt lgkmcnt(3)\n" \
  "v_mfma_f32_16x16x32_bf16 v[56:59], v[36:39], v[36:39], v[56:59]\n" \
  "ds_read_b128 v[52:55], v10 offset:14336\n" \
  "s_waitcnt lgkmcnt(3)\n" \
  "v_mfma_f32_16x16x32_bf16 v[12:15], v[40:43], v[212:215], v[12:15]\n" \
  "ds_read_b128 v[36:39], v7 offset:14336\n" \
  "s_waitcnt lgkmcnt(3)\n" \
  "v_mfma_f32_16x16x32_bf16 v[56:59], v[44:47], v[44:47], v[56:59]\n" \
  "ds_read_b128 v[40:43], v10 offset:16384\n" \
  "s_waitcnt lgkmcnt(3)\n" \
  "v_mfma_f32_16x16x32_bf16 v[12:15], v[48:51], v[216:219], v[12:15]\n" \
  "ds_read_b128 v[44:47], v7 offset:16384\n" \
  "s_waitcnt lgkmcnt(3)\n" \
  "v_mfma_f32_16x16x32_bf16 v[56:59], v[52:55], v[52:55], v[56:59]\n" \
  "ds_read_b128 v[48:51], v10 offset:18432\n" \
  "s_waitcnt lgkmcnt(3)\n" \
  "v_mfma_f32_16x16x32_bf16 v[12:15], v[36:39], v[220:223], v[12:15]\n" \
  "ds_read_b128 v[52:55], v7 offset:18432\n" \
  "s_waitcnt lgkmcnt(3)\n" \
  "v_mfma_f32_16x16x32_bf16 v[56:59], v[40:43], v[40:43], v[56:59]\n" \
  "ds_read_b128 v[36:39], v10 offset:20480\n" \
  "s_waitcnt lgkmcnt(3)\n" \
  "v_mfma_f32_16x16x32_bf16 v[12:15], v[44:47], v[224:227], v[12:15]\n" \
  "ds_read_b128 v[40:43], v7 offset:20480\n" \
  "s_waitcnt lgkmcnt(3)\n" \
  "v_mfma_f32_16x16x32_bf16 v[56:59], v[48:51], v[48:51], v[56:59]\n" \
  "ds_read_b128 v[44:47], v10 offset:22528\n" \
  "s_waitcnt lgkmcnt(3)\n" \
  "v_mfma_f32_16x16x32_bf16 v[12:15], v[52:55], v[228:231], v[12:15]\n" \
  "ds_read_b128 v[48:51], v7 offset:22528\n" \
  "s_waitcnt lgkmcnt(3)\n" \
  "v_mfma_f32_16x16x32_bf16 v[56:59], v[36:39], v[36:39], v[56:59]\n" \
  "ds_read_b128 v[52:55], v10 offset:24576\n" \
  "s_waitcnt lgkmcnt(3)\n" \
  "v_mfma_f32_16x16x32_bf16 v[12:15], v[40:43], v[232:235], v[12:15]\n" \
  "ds_read_b128 v[36:39], v7 offset:24576\n" \
  "s_waitcnt lgkmcnt(3)\n" \
  "v_mfma_f32_16x16x32_bf16 v[56:59], v[44:47], v[44:47], v[56:59]\n" \
  "ds_read_b128 v[40:43], v10 offset:26624\n" \
  "s_waitcnt lgkmcnt(3)\n" \
  "v_mfma_f32_16x16x32_bf16 v[12:15], v[48:51], v[236:239], v[12:15]\n" \
  "ds_read_b128 v[44:47], v7 offset:26624\n" \
  "s_waitcnt lgkmcnt(3)\n" \
  "v_mfma_f32_16x16x32_bf16 v[56:59], v[52:55], v[52:55], v[56:59]\n" \
  "ds_read_b128 v[48:51], v10 offset:28672\n" \
  "s_waitcnt lgkmcnt(3)\n" \
  "v_mfma_f32_16x16x32_bf16 v[12:15], v[36:39], v[240:243], v[12:15]\n" \
  "ds_read_b128 v[52:55], v7 offset:28672\n" \
  "s_waitcnt lgkmcnt(3)\n" \
  "v_mfma_f32_16x16x32_bf16 v[56:59], v[40:43], v[40:43], v[56:59]\n" \
  "ds_read_b128 v[36:39], v10 offset:30720\n" \
  "s_waitcnt lgkmcnt(3)\n" \
  "v_mfma_f32_16x16x32_bf16 v[12:15], v[44:47], v[244:247], v[12:15]\n" \
  "ds_read_b128 v[40:43], v7 offset:30720\n" \
  "s_waitcnt lgkmcnt(3)\n" \
  "v_mfma_f32_16x16x32_bf16 v[56:59], v[48:51], v[48:51], v[56:59]\n" \
  "ds_read_b128 v[44:47], v7 offset:256\n" \
  "s_waitcnt lgkmcnt(3)\n" \
  "v_mfma_f32_16x16x32_bf16 v[12:15], v[52:55], v[248:251], v[12:15]\n" \
  "ds_read_b128 v[48:51], v7 offset:2304\n" \
  "s_waitcnt lgkmcnt(3)\n" \
  "v_mfma_f32_16x16x32_bf16 v[56:59], v[36:39], v[36:39], v[56:59]\n" \
  "ds_read_b128 v[52:55], v7 offset:4352\n" \
  "s_waitcnt lgkmcnt(3)\n" \
  "v_mfma_f32_16x16x32_bf16 v[12:15], v[40:43], v[252:255], v[12:15]\n" \
  "ds_read_b128 v[36:39], v7 offset:6400\n" \
  "s_waitcnt lgkmcnt(3)\n" \
  "v_mfma_f32_16x16x32_bf16 v[16:19], v[44:47], v[192:195], 0\n" \
  "ds_read_b128 v[40:43], v7 offset:8448\n" \
  "s_waitcnt lgkmcnt(3)\n" \
  "v_mfma_f32_16x16x32_bf16 v[16:19], v[48:51], v[196:199], v[16:19]\n" \
  "ds_read_b128 v[44:47], v7 offset:10496\n" \
  "s_waitcnt lgkmcnt(3)\n" \
  "v_mfma_f32_16x16x32_bf16 v[16:19], v[52:55], v[200:203], v[16:19]\n" \
  "ds_read_b128 v[48:51], v7 offset:12544\n" \
  "s_waitcnt lgkmcnt(3)\n" \
  "v_mfma_f32_16x16x32_bf16 v[16:19], v[36:39], v[204:207], v[16:19]\n" \
  "ds_read_b128 v[52:55], v7 offset:14592\n" \
  "s_waitcnt lgkmcnt(3)\n" \
  "v_mfma_f32_16x16x32_bf16 v[16:19], v[40:43], v[208:211], v[16:19]\n" \
  "ds_read_b128 v[36:39], v7 offset:16640\n" \
  "s_waitcnt lgkmcnt(3)\n" \
  "v_mfma_f32_16x16x32_bf16 v[16:19], v[44:47], v[212:215], v[16:19]\n" \
  "ds_read_b128 v[40:43], v7 offset:18688\n" \
  "s_waitcnt lgkmcnt(3)\n" \
  "v_mfma_f32_16x16x32_bf16 v[16:19], v[48:51], v[216:219], v[16:19]\n" \
  "ds_read_b128 v[44:47], v7 offset:20736\n" \
  "s_waitcnt lgkmcnt(3)\n" \
  "v_mfma_f32_16x16x32_bf16 v[16:19], v[52:55], v[220:223], v[16:19]\n" \
  "ds_read_b128 v[48:51], v7 offset:22784\n" \
  "s_waitcnt lgkmcnt(3)\n" \
  "v_mfma_f32_16x16x32_bf16 v[16:19], v[36:39], v[224:227], v[16:19]\n" \
  "ds_read_b128 v[52:55], v7 offset:24832\n" \
  "s_waitcnt lgkmcnt(3)\n" \
  "v_mfma_f32_16x16x32_bf16 v[16:19], v[40:43], v[228:231], v[16:19]\n" \
  "ds_read_b128 v[36:39], v7 offset:26880\n" \
  "s_waitcnt lgkmcnt(3)\n" \
  "v_mfma_f32_16x16x32_bf16 v[16:19], v[44:47], v[232:235], v[16:19]\n" \
  "ds_read_b128 v[40:43], v7 offset:28928\n" \
  "s_waitcnt lgkmcnt(3)\n" \
  "v_mfma_f32_16x16x32_bf16 v[16:19], v[48:51], v[236:239], v[16:19]\n" \
  "ds_read_b128 v[44:47], v7 offset:30976\n" \
  "s_waitcnt lgkmcnt(3)\n" \
  "v_mfma_f32_16x16x32_bf16 v[16:19], v[52:55], v[240:243], v[16:19]\n" \
  "ds_read_b128 v[48:51], v7 offset:0\n" \
  "v_lshrrev_b32 v63, 2, v6\n" \
  "v_and_b32 v63, 0x9c, v63\n" \
  "buffer_load_dword v63, v63, s[52:55], s62 offen sc1\n" \
  "s_waitcnt lgkmcnt(3)\n" \
  "v_mfma_f32_16x16x32_bf16 v[16:19], v[36:39], v[244:247], v[16:19]\n" \
  "ds_read_b128 v[52:55], v7 offset:2048\n" \
  "s_waitcnt lgkmcnt(3)\n" \
  "v_mfma_f32_16x16x32_bf16 v[16:19], v[40:43], v[248:251], v[16:19]\n" \
  "ds_read_b128 v[36:39], v7 offset:4096\n" \
  "s_waitcnt lgkmcnt(3)\n" \
  "v_mfma_f32_16x16x32_bf16 v[16:19], v[44:47], v[252:255], v[16:19]\n" \
  "ds_read_b128 v[40:43], v7 offset:6144\n" \
  "s_setprio 0\n" \
  "s_mov_b32 s0, 0xbe801d62\n" \
  ".set .Lpad0xaa, . - 4\n" \
  "v_cndmask_b32 v56, v56, v57, s[70:71]\n" \
  "v_cndmask_b32 v57, v58, v59, s[70:71]\n" \
  "v_cndmask_b32 v56, v56, v57, s[72:73]\n" \
  "v_mov_b32 v57, v56\n" \
  "v_mov_b32 v58, v56\n" \
  "v_mul_f32 v56, 0x3b000000, v56\n" \
  "v_xor_b32 v10, v11, v10\n" \
  "v_permlane32_swap_b32 v57, v58\n" \
  "v_mul_f32 v57, 0xbb000000, v58\n" \
  "v_fma_f32 v56, -v57, v57, v56\n" \
  "v_max_f32 v56, 0, v56\n" \
  "v_cvt_pk_bf16_f32 v56, v56, v56\n" \
  "v_lshlrev_b32 v56, 16, v56\n" \
  "v_add_f32 v56, 0x3727c5ac, v56\n" \
  "v_cvt_pk_bf16_f32 v56, v56, v56\n" \
  "v_lshlrev_b32 v56, 16, v56\n" \
  "v_sqrt_f32 v56, v56\n" \
  "v_cvt_pk_bf16_f32 v57, v57, v57\n" \
  "v_cvt_pk_bf16_f32 v56, v56, v56\n" \
  "v_lshlrev_b32 v56, 16, v56\n" \
  "v_rcp_f32 v56, v56\n" \
  "s_mov_b64 exec, 0xf0000f\n" \
  "ds_write2_b32 v62, v57, v56 offset1:32\n" \
  "s_mov_b64 exec, -1\n" \
  "s_cmp_eq_u32 s66, s83\n" \
  "s_cbranch_scc1 .LQB_NOE_%=\n" \
  "v_mul_f32 v20, v20, v60\n" \
  "v_mul_f32 v21, v21, v60\n" \
  "v_mul_f32 v22, v22, v60\n" \
  "v_cvt_pk_bf16_f32 v20, v20, v21\n" \
  "v_mul_f32 v23, v23, v60\n" \
  "v_mul_f32 v24, v24, v60\n" \
  "v_cvt_pk_bf16_f32 v21, v22, v23\n" \
  "v_mul_f32 v25, v25, v60\n" \
  "v_mul_f32 v26, v26, v60\n" \
  "v_cvt_pk_bf16_f32 v22, v24, v25\n" \
  "v_mul_f32 v27, v27, v60\n" \
  "v_cvt_pk_bf16_f32 v23, v26, v27\n" \
  "buffer_store_dwordx4 v[20:23], v6, s[40:43], s59 offen offset:0 sc1\n" \
  "s_add_u32 s76, s59, 4096\n" \
  "v_mul_f32 v28, v28, v61\n" \
  "v_mul_f32 v29, v29, v61\n" \
  "v_mul_f32 v30, v30, v61\n" \
  "v_cvt_pk_bf16_f32 v28, v28, v29\n" \
  "v_mul_f32 v31, v31, v61\n" \
  "v_mul_f32 v32, v32, v61\n" \
  "v_cvt_pk_bf16_f32 v29, v30, v31\n" \
  "v_mul_f32 v33, v33, v61\n" \
  "v_mul_f32 v34, v34, v61\n" \
  "v_cvt_pk_bf16_f32 v30, v32, v33\n" \
  "v_mul_f32 v35, v35, v61\n" \
  "v_cvt_pk_bf16_f32 v31, v34, v35\n" \
  "buffer_store_dwordx4 v[28:31], v6, s[40:43], s76 offen offset:0 sc1\n" \
  "s_add_u32 s59, s59, 0x80000\n" \
  "s_waitcnt vmcnt(2)\n" \
  "s_branch .LQB_LOOK_%=\n" \
  ".LQB_NOE_%=:\n" \
  "s_waitcnt vmcnt(0)\n" \
  ".LQB_LOOK_%=:\n" \
  "v_cmp_eq_u32 vcc, 0, v63\n" \
  "v_mov_b32 v58, s82\n" \
  "s_cmp_eq_u64 vcc, 0\n" \
  "s_cselect_b32 s69, 1, 0\n" \
  "v_mov_b32 v59, s69\n" \
  "ds_write_b32 v58, v59\n" \
  "s_cbranch_scc0 .LQB_NOD_%=\n" \
  "s_mov_b32 m0, s58\n" \
  "s_add_u32 s74, s56, 4096\n" \
  "s_addc_u32 s75, s57, 0\n" \
  "s_add_u32 s76, s58, 4096\n" \
  "global_load_lds_dwordx4 v6, s[56:57] offset:0 sc1\n" \
  "global_load_lds_dwordx4 v6, s[56:57] offset:1024 sc1\n" \
  "global_load_lds_dwordx4 v6, s[56:57] offset:2048 sc1\n" \
  "global_load_lds_dwordx4 v6, s[56:57] offset:3072 sc1\n" \
  "s_mov_b32 m0, s76\n" \
  "s_nop 0\n" \
  "global_load_lds_dwordx4 v6, s[74:75] offset:0 sc1\n" \
  "global_load_lds_dwordx4 v6, s[74:75] offset:1024 sc1\n" \
  "global_load_lds_dwordx4 v6, s[74:75] offset:2048 sc1\n" \
  "global_load_lds_dwordx4 v6, s[74:75] offset:3072 sc1\n" \
  ".LQB_NOD_%=:\n" \
  "s_waitcnt lgkmcnt(0)\n" \
  "s_barrier\n" \
  "ds_read2_b32 v[56:57], v8 offset0:0 offset1:0\n" \
  "ds_read2_b32 v[58:59], v8 offset0:16 offset1:16\n" \
  "ds_read_b128 v[28:31], v9 offset:128\n" \
  "ds_read_b128 v[32:35], v9 offset:192\n" \
  "ds_read2_b32 v[60:61], v8 offset0:32 offset1:48\n" \
  "s_setprio 3\n" \
  "s_waitcnt lgkmcnt(3)\n" \
  "v_mfma_f32_16x16x16_bf16 v[12:15], v[56:57], v[4:5], v[12:15]\n" \
  "v_mfma_f32_16x16x16_bf16 v[16:19], v[58:59], v[4:5], v[16:19]\n" \
  "s_setprio 0\n" \
  "s_waitcnt lgkmcnt(0)\n" \
  "s_nop 4\n" \
  "v_mul_f32 v12, v12, v28\n" \
  "v_mul_f32 v13, v13, v29\n" \
  "v_mul_f32 v14, v14, v30\n" \
  "v_mul_f32 v15, v15, v31\n" \
  "v_cvt_pk_bf16_f32 v12, v12, v13\n" \
  "v_cvt_pk_bf16_f32 v13, v14, v15\n" \
  "v_mul_f32 v16, v16, v32\n" \
  "v_mul_f32 v17, v17, v33\n" \
  "v_mul_f32 v18, v18, v34\n" \
  "v_mul_f32 v19, v19, v35\n" \
  "v_cvt_pk_bf16_f32 v14, v16, v17\n" \
  "v_cvt_pk_bf16_f32 v15, v18, v19\n" \
  "buffer_store_dwordx4 v[12:15], v6, s[44:47], s60 offen offset:0 sc1\n" \
  "v_mov_b64 v[16:17], 0\n" \
  "v_lshrrev_b32 v18, 1, v6\n" \
  "buffer_store_dwordx2 v[16:17], v18, s[48:51], s61 offen sc1\n" \
  "s_setprio 0\n" \
  "s_add_u32 s60, s60, 0x40000\n" \
  "s_add_u32 s61, s61, 0x10000\n" \
  "s_mov_b32 s0, 0xbe801d62\n" \
  ".set .Lpad0xab, . - 4\n" \
  "v_mov_b32 v12, s80\n" \
  "v_mov_b32 v13, 1\n" \
  "v_mfma_f32_16x16x16_bf16 v[20:23], v[0:1], v[56:57], 0\n" \
  "v_mfma_f32_16x16x16_bf16 v[24:27], v[2:3], v[56:57], 0\n" \
  "v_mfma_f32_16x16x16_bf16 v[28:31], v[0:1], v[58:59], 0\n" \
  "v_mfma_f32_16x16x16_bf16 v[32:35], v[2:3], v[58:59], 0\n" \
  "v_mfma_f32_16x16x32_bf16 v[20:23], v[64:67], v[48:51], v[20:23]\n" \
  "ds_read_b128 v[44:47], v7 offset:8192\n" \
  "v_mfma_f32_16x16x32_bf16 v[24:27], v[128:131], v[48:51], v[24:27]\n" \
  "v_mfma_f32_16x16x32_bf16 v[20:23], v[68:71], v[52:55], v[20:23]\n" \
  "ds_read_b128 v[48:51], v7 offset:10240\n" \
  "v_mfma_f32_16x16x32_bf16 v[24:27], v[132:135], v[52:55], v[24:27]\n" \
  "v_mfma_f32_16x16x32_bf16 v[20:23], v[72:75], v[36:39], v[20:23]\n" \
  "ds_read_b128 v[52:55], v7 offset:12288\n" \
  "v_mfma_f32_16x16x32_bf16 v[24:27], v[136:139], v[36:39], v[24:27]\n" \
  "v_mfma_f32_16x16x32_bf16 v[20:23], v[76:79], v[40:43], v[20:23]\n" \
  "ds_read_b128 v[36:39], v7 offset:14336\n" \
  "v_mfma_f32_16x16x32_bf16 v[24:27], v[140:143], v[40:43], v[24:27]\n" \
  "s_waitcnt lgkmcnt(3)\n" \
  "v_mfma_f32_16x16x32_bf16 v[20:23], v[80:83], v[44:47], v[20:23]\n" \
  "ds_read_b128 v[40:43], v7 offset:16384\n" \
  "v_mfma_f32_16x16x32_bf16 v[24:27], v[144:147], v[44:47], v[24:27]\n" \
  "s_waitcnt lgkmcnt(3)\n" \
  "v_mfma_f32_16x16x32_bf16 v[20:23], v[84:87], v[48:51], v[20:23]\n" \
  "ds_read_b128 v[44:47], v7 offset:18432\n" \
  "v_mfma_f32_16x16x32_bf16 v[24:27], v[148:151], v[48:51], v[24:27]\n" \
  "s_waitcnt lgkmcnt(3)\n" \
  "v_mfma_f32_16x16x32_bf16 v[20:23], v[88:91], v[52:55], v[20:23]\n" \
  "ds_read_b128 v[48:51], v7 offset:20480\n" \
  "v_mfma_f32_16x16x32_bf16 v[24:27], v[152:155], v[52:55], v[24:27]\n" \
  "s_waitcnt lgkmcnt(3)\n" \
  "v_mfma_f32_16x16x32_bf16 v[20:23], v[92:95], v[36:39], v[20:23]\n" \
  "ds_read_b128 v[52:55], v7 offset:22528\n" \
  "v_mfma_f32_16x16x32_bf16 v[24:27], v[156:159], v[36:39], v[24:27]\n" \
  "s_waitcnt lgkmcnt(3)\n" \
  "v_mfma_f32_16x16x32_bf16 v[20:23], v[96:99], v[40:43], v[20:23]\n" \
  "ds_read_b128 v[36:39], v7 offset:24576\n" \
  "v_mfma_f32_16x16x32_bf16 v[24:27], v[160:163], v[40:43], v[24:27]\n" \
  "s_waitcnt lgkmcnt(3)\n" \
  "v_mfma_f32_16x16x32_bf16 v[20:23], v[100:103], v[44:47], v[20:23]\n" \
  "ds_read_b128 v[40:43], v7 offset:26624\n" \
  "v_mfma_f32_16x16x32_bf16 v[24:27], v[164:167], v[44:47], v[24:27]\n" \
  "s_waitcnt lgkmcnt(3)\n" \
  "v_mfma_f32_16x16x32_bf16 v[20:23], v[104:107], v[48:51], v[20:23]\n" \
  "ds_read_b128 v[44:47], v7 offset:28672\n" \
  "v_mfma_f32_16x16x32_bf16 v[24:27], v[168:171], v[48:51], v[24:27]\n" \
  "s_waitcnt lgkmcnt(3)\n" \
  "v_mfma_f32_16x16x32_bf16 v[20:23], v[108:111], v[52:55], v[20:23]\n" \
  "ds_read_b128 v[48:51], v7 offset:30720\n" \
  "v_mfma_f32_16x16x32_bf16 v[24:27], v[172:175], v[52:55], v[24:27]\n" \
  "s_waitcnt lgkmcnt(3)\n" \
  "v_mfma_f32_16x16x32_bf16 v[20:23], v[112:115], v[36:39], v[20:23]\n" \
  "ds_read_b128 v[52:55], v7 offset:256\n" \
  "v_mfma_f32_16x16x32_bf16 v[24:27], v[176:179], v[36:39], v[24:27]\n" \
  "s_waitcnt lgkmcnt(3)\n" \
  "v_mfma_f32_16x16x32_bf16 v[20:23], v[116:119], v[40:43], v[20:23]\n" \
  "ds_read_b128 v[36:39], v7 offset:2304\n" \
  "v_mfma_f32_16x16x32_bf16 v[24:27], v[180:183], v[40:43], v[24:27]\n" \
  "s_waitcnt lgkmcnt(3)\n" \
  "v_mfma_f32_16x16x32_bf16 v[20:23], v[120:123], v[44:47], v[20:23]\n" \
  "ds_read_b128 v[40:43], v7 offset:4352\n" \
  "v_mfma_f32_16x16x32_bf16 v[24:27], v[184:187], v[44:47], v[24:27]\n" \
  "s_waitcnt lgkmcnt(3)\n" \
  "v_mfma_f32_16x16x32_bf16 v[20:23], v[124:127], v[48:51], v[20:23]\n" \
  "ds_read_b128 v[44:47], v7 offset:6400\n" \
  "v_mfma_f32_16x16x32_bf16 v[24:27], v[188:191], v[48:51], v[24:27]\n" \
  "s_waitcnt lgkmcnt(3)\n" \
  "v_mfma_f32_16x16x32_bf16 v[28:31], v[64:67], v[52:55], v[28:31]\n" \
  "ds_read_b128 v[48:51], v7 offset:8448\n" \
  "v_mfma_f32_16x16x32_bf16 v[32:35], v[128:131], v[52:55], v[32:35]\n" \
  "s_waitcnt lgkmcnt(3)\n" \
  "v_mfma_f32_16x16x32_bf16 v[28:31], v[68:71], v[36:39], v[28:31]\n" \
  "ds_read_b128 v[52:55], v7 offset:10496\n" \
  "v_mfma_f32_16x16x32_bf16 v[32:35], v[132:135], v[36:39], v[32:35]\n" \
  "s_waitcnt lgkmcnt(3)\n" \
  "v_mfma_f32_16x16x32_bf16 v[28:31], v[72:75], v[40:43], v[28:31]\n" \
  "ds_read_b128 v[36:39], v7 offset:12544\n" \
  "v_mfma_f32_16x16x32_bf16 v[32:35], v[136:139], v[40:43], v[32:35]\n" \
  "s_waitcnt lgkmcnt(3)\n" \
  "v_mfma_f32_16x16x32_bf16 v[28:31], v[76:79], v[44:47], v[28:31]\n" \
  "ds_read_b128 v[40:43], v7 offset:14592\n" \
  "v_mfma_f32_16x16x32_bf16 v[32:35], v[140:143], v[44:47], v[32:35]\n" \
  "s_waitcnt lgkmcnt(3)\n" \
  "v_mfma_f32_16x16x32_bf16 v[28:31], v[80:83], v[48:51], v[28:31]\n" \
  "ds_read_b128 v[44:47], v7 offset:16640\n" \
  "v_mfma_f32_16x16x32_bf16 v[32:35], v[144:147], v[48:51], v[32:35]\n" \
  "s_waitcnt lgkmcnt(3)\n" \
  "v_mfma_f32_16x16x32_bf16 v[28:31], v[84:87], v[52:55], v[28:31]\n" \
  "ds_read_b128 v[48:51], v7 offset:18688\n" \
  "v_mfma_f32_16x16x32_bf16 v[32:35], v[148:151], v[52:55], v[32:35]\n" \
  "s_waitcnt lgkmcnt(3)\n" \
  "v_mfma_f32_16x16x32_bf16 v[28:31], v[88:91], v[36:39], v[28:31]\n" \
  "ds_read_b128 v[52:55], v7 offset:20736\n" \
  "s_waitcnt vmcnt(0)\n" \
  "s_mov_b64 exec, 1\n" \
  "ds_add_rtn_u32 v14, v12, v13\n" \
  "s_mov_b64 exec, -1\n" \
  "v_mfma_f32_16x16x32_bf16 v[32:35], v[152:155], v[36:39], v[32:35]\n" \
  "s_waitcnt lgkmcnt(4)\n" \
  "v_mfma_f32_16x16x32_bf16 v[28:31], v[92:95], v[40:43], v[28:31]\n" \
  "ds_read_b128 v[36:39], v7 offset:22784\n" \
  "v_mfma_f32_16x16x32_bf16 v[32:35], v[156:159], v[40:43], v[32:35]\n" \
  "s_waitcnt lgkmcnt(4)\n" \
  "v_mfma_f32_16x16x32_bf16 v[28:31], v[96:99], v[44:47], v[28:31]\n" \
  "ds_read_b128 v[40:43], v7 offset:24832\n" \
  "s_waitcnt lgkmcnt(2)\n" \
  "v_mfma_f32_16x16x32_bf16 v[32:35], v[160:163], v[44:47], v[32:35]\n" \
  "v_readfirstlane_b32 s74, v14\n" \
  "s_cmp_eq_u32 s66, s83\n" \
  "s_cselect_b32 s74, 0, s74\n" \
  "s_and_b32 s74, s74, 3\n" \
  "s_cmp_eq_u32 s74, 3\n" \
  "s_cbranch_scc0 4\n" \
  "s_mov_b64 exec, 1\n" \
  "global_store_dword v16, v13, s[64:65] offset:64 sc1\n" \
  "s_mov_b64 exec, -1\n" \
  "v_mfma_f32_16x16x32_bf16 v[28:31], v[100:103], v[48:51], v[28:31]\n" \
  "ds_read_b128 v[44:47], v7 offset:26880\n" \
  "s_add_u32 s64, s64, 4096\n" \
  "s_addc_u32 s65, s65, 0\n" \
  "v_mfma_f32_16x16x32_bf16 v[32:35], v[164:167], v[48:51], v[32:35]\n" \
  "s_sub_u32 s66, s66, 1\n" \
  "v_mfma_f32_16x16x32_bf16 v[28:31], v[104:107], v[52:55], v[28:31]\n" \
  "ds_read_b128 v[48:51], v7 offset:28928\n" \
  "v_mfma_f32_16x16x32_bf16 v[32:35], v[168:171], v[52:55], v[32:35]\n" \
  "s_waitcnt lgkmcnt(3)\n" \
  "v_mfma_f32_16x16x32_bf16 v[28:31], v[108:111], v[36:39], v[28:31]\n" \
  "ds_read_b128 v[52:55], v7 offset:30976\n" \
  "v_mfma_f32_16x16x32_bf16 v[32:35], v[172:175], v[36:39], v[32:35]\n" \
  "s_waitcnt lgkmcnt(3)\n" \
  "v_mfma_f32_16x16x32_bf16 v[28:31], v[112:115], v[40:43], v[28:31]\n" \
  "v_mfma_f32_16x16x32_bf16 v[32:35], v[176:179], v[40:43], v[32:35]\n" \
  "v_xor_b32 v7, 0x20000, v7\n" \
  "s_waitcnt lgkmcnt(0)\n" \
  "s_barrier\n" \
  "ds_read_b128 v[36:39], v10 offset:0\n" \
  "ds_read_b128 v[40:43], v7 offset:0\n" \
  "v_mfma_f32_16x16x32_bf16 v[28:31], v[116:119], v[44:47], v[28:31]\n" \
  "v_mfma_f32_16x16x32_bf16 v[32:35], v[180:183], v[44:47], v[32:35]\n" \
  "ds_read_b128 v[44:47], v10 offset:2048\n" \
  "v_mfma_f32_16x16x32_bf16 v[28:31], v[120:123], v[48:51], v[28:31]\n" \
  "v_mfma_f32_16x16x32_bf16 v[32:35], v[184:187], v[48:51], v[32:35]\n" \
  "ds_read_b128 v[48:51], v7 offset:2048\n" \
  "v_mfma_f32_16x16x32_bf16 v[28:31], v[124:127], v[52:55], v[28:31]\n" \
  "v_mfma_f32_16x16x32_bf16 v[32:35], v[188:191], v[52:55], v[32:35]\n" \
  "s_cbranch_scc1 .LQB_LAST_%=\n" \
  "s_branch .LQB_TOP_%=\n" \
  ".LQB_LAST_%=:\n" \
  "s_nop 7\n" \
  "v_mul_f32 v20, v20, v60\n" \
  "v_mul_f32 v21, v21, v60\n" \
  "v_mul_f32 v22, v22, v60\n" \
  "v_cvt_pk_bf16_f32 v20, v20, v21\n" \
  "v_mul_f32 v23, v23, v60\n" \
  "v_mul_f32 v24, v24, v60\n" \
  "v_cvt_pk_bf16_f32 v21, v22, v23\n" \
  "v_mul_f32 v25, v25, v60\n" \
  "v_mul_f32 v26, v26, v60\n" \
  "v_cvt_pk_bf16_f32 v22, v24, v25\n" \
  "v_mul_f32 v27, v27, v60\n" \
  "v_cvt_pk_bf16_f32 v23, v26, v27\n" \
  "buffer_store_dwordx4 v[20:23], v6, s[40:43], s59 offen offset:0 sc1\n" \
  "s_add_u32 s76, s59, 4096\n" \
  "v_mul_f32 v28, v28, v61\n" \
  "v_mul_f32 v29, v29, v61\n" \
  "v_mul_f32 v30, v30, v61\n" \
  "v_cvt_pk_bf16_f32 v28, v28, v29\n" \
  "v_mul_f32 v31, v31, v61\n" \
  "v_mul_f32 v32, v32, v61\n" \
  "v_cvt_pk_bf16_f32 v29, v30, v31\n" \
  "v_mul_f32 v33, v33, v61\n" \
  "v_mul_f32 v34, v34, v61\n" \
  "v_cvt_pk_bf16_f32 v30, v32, v33\n" \
  "v_mul_f32 v35, v35, v61\n" \
  "v_cvt_pk_bf16_f32 v31, v34, v35\n" \
  "buffer_store_dwordx4 v[28:31], v6, s[40:43], s76 offen offset:0 sc1\n" \
  "s_add_u32 s59, s59, 0x80000\n" \
  "s_waitcnt vmcnt(0)\n" \
  "s_mov_b64 exec, 1\n" \
  "ds_add_rtn_u32 v14, v12, v13\n" \
  "s_mov_b64 exec, -1\n" \
  "s_waitcnt lgkmcnt(0)\n" \
  "v_readfirstlane_b32 s74, v14\n" \
  "s_cmp_eq_u32 s66, s83\n" \
  "s_cselect_b32 s74, 0, s74\n" \
  "s_and_b32 s74, s74, 3\n" \
  "s_cmp_eq_u32 s74, 3\n" \
  "s_cbranch_scc0 4\n" \
  "s_mov_b64 exec, 1\n" \
  "global_store_dword v16, v13, s[64:65] offset:64 sc1\n" \
  "s_mov_b64 exec, -1\n" \
  "s_cmp_eq_u32 s86, 0\n" \
  "s_cbranch_scc1 .LQB_END_%=\n" \
  "s_mov_b32 s86, 0\n" \
  "s_mov_b32 s66, 63\n" \
  "s_mov_b32 s62, 0x40000\n" \
  "s_add_u32 s54, s54, (128 - %[n0]) * 4096\n" \
  "s_add_u32 s59, s59, (64 - %[n0]) * 0x80000\n" \
  "s_add_u32 s60, s60, (64 - %[n0]) * 0x40000\n" \
  "s_add_u32 s61, s61, (64 - %[n0]) * 0x10000\n" \
  "s_add_u32 s64, s64, (65 - %[n0]) * 4096\n" \
  "s_addc_u32 s65, s65, 0\n" \
  "s_add_u32 s56, s56, %[x3]\n" \
  "s_addc_u32 s57, s57, 0\n" \
  "s_add_u32 s84, s84, 2048\n" \
  "s_addc_u32 s85, s85, 0\n" \
  "s_mov_b32 s83, 63\n" \
  "s_branch .LQB_GO_%=\n" \
  ".LQB_END_%=:\n" \
  "s_endpgm\n" \
  ".p2alignl 12, 0xbf800000\n" \
  ".Lpad0x8c:\n" \
  "s_setpc_b64 s[98:99]\n"
#define AW_ASM_0 \
  "v_add_u32 v17, s61, v1\n" \
  "ds_write_b128 v17, v[8:11]\n" \
  "ds_write_b128 v17, v[12:15] offset:1024\n" \
  "s_branch .LWA_AL_%=\n" \
  ".p2alignl 12, 0xbf800000, 4000\n" \
  ".p2align 6\n" \
  ".Lpad0xdc:\n" \
  "s_setpc_b64 s[98:99]\n" \
  ".p2align 6\n" \
  ".LWA_AL_%=:\n" \
  ".LWA_TOP_%=:\n" \
  "v_mov_b32 v16, s65\n" \
  "v_add_u32 v17, s61, v1\n" \
  "s_add_u32 s64, s64, 2\n" \
  "s_mov_b32 s77, 0x1000000\n" \
  "s_branch .LWA_LAND_%=\n" \
  ".LWA_WAIT_%=:\n" \
  "s_setprio 0\n" \
  "s_sleep 1\n" \
  "s_sub_u32 s77, s77, 1\n" \
  "s_cbranch_scc1 .LWA_GO_%=\n" \
  ".LWA_LAND_%=:\n" \
  "ds_read_b32 v18, v16\n" \
  "s_waitcnt lgkmcnt(0)\n" \
  "v_readfirstlane_b32 s68, v18\n" \
  "s_cmp_lt_u32 s68, s64\n" \
  "s_cbranch_scc1 .LWA_WAIT_%=\n" \
  ".LWA_GO_%=:\n" \
  "ds_read_b128 v[64:67], v17 offset:0\n" \
  "ds_read_b128 v[68:71], v17 offset:1024\n" \
  "s_setprio 3\n" \
  "ds_read_b128 v[16:19], v4 offset:0\n" \
  "ds_read_b128 v[32:35], v3 offset:8192\n" \
  "ds_read_b128 v[48:51], v3 offset:12288\n" \
  "ds_read_b128 v[20:23], v4 offset:1024\n" \
  "ds_read_b128 v[36:39], v3 offset:9216\n" \
  "ds_read_b128 v[52:55], v3 offset:13312\n" \
  "ds_read_b128 v[24:27], v4 offset:2048\n" \
  "ds_read_b128 v[40:43], v3 offset:10240\n" \
  "ds_read_b128 v[56:59], v3 offset:14336\n" \
  "ds_read_b128 v[28:31], v4 offset:3072\n" \
  "ds_read_b128 v[44:47], v3 offset:11264\n" \
  "ds_read_b128 v[60:63], v3 offset:15360\n" \
  "s_waitcnt lgkmcnt(10)\n" \
  "v_mfma_f32_16x16x32_bf16 v[64:67], v[32:35], v[16:19], v[64:67]\n" \
  "ds_read_b128 v[72:75], v3 offset:16384\n" \
  "s_waitcnt lgkmcnt(10)\n" \
  "v_mfma_f32_16x16x32_bf16 v[68:71], v[48:51], v[16:19], v[68:71]\n" \
  "ds_read_b128 v[76:79], v3 offset:17408\n" \
  "s_waitcnt lgkmcnt(9)\n" \
  "v_mfma_f32_16x16x32_bf16 v[64:67], v[36:39], v[20:23], v[64:67]\n" \
  "ds_read_b128 v[80:83], v3 offset:18432\n" \
  "s_waitcnt lgkmcnt(9)\n" \
  "v_mfma_f32_16x16x32_bf16 v[68:71], v[52:55], v[20:23], v[68:71]\n" \
  "ds_read_b128 v[84:87], v3 offset:19456\n" \
  "s_waitcnt lgkmcnt(8)\n" \
  "v_mfma_f32_16x16x32_bf16 v[64:67], v[40:43], v[24:27], v[64:67]\n" \
  "ds_read_b128 v[88:91], v3 offset:20480\n" \
  "s_waitcnt lgkmcnt(8)\n" \
  "v_mfma_f32_16x16x32_bf16 v[68:71], v[56:59], v[24:27], v[68:71]\n" \
  "ds_read_b128 v[92:95], v3 offset:21504\n" \
  "s_waitcnt lgkmcnt(7)\n" \
  "v_mfma_f32_16x16x32_bf16 v[64:67], v[44:47], v[28:31], v[64:67]\n" \
  "ds_read_b128 v[96:99], v3 offset:22528\n" \
  "s_waitcnt lgkmcnt(7)\n" \
  "v_mfma_f32_16x16x32_bf16 v[68:71], v[60:63], v[28:31], v[68:71]\n" \
  "ds_read_b128 v[100:103], v3 offset:23552\n" \
  "s_add_u32 s56, s56, 3\n" \
  "s_sub_u32 s68, s56, %[wd]\n" \
  "s_bitcmp1_b32 s68, 0\n" \
  "s_cselect_b32 s58, 64, %[wd]\n" \
  "v_maximum3_f32 v10, v64, v65, v66\n" \
  "s_lshr_b32 s70, s68, 1\n" \
  "s_add_u32 s58, s58, s70\n" \
  "v_maximum3_f32 v10, v10, v67, v68\n" \
  "v_maximum3_f32 v10, v10, v69, v70\n" \
  "v_max_f32 v8, v10, v71\n" \
  "v_mov_b32 v9, v8\n" \
  "s_cmp_lt_u32 s68, %[w2]\n" \
  "s_cselect_b32 s58, s58, s56\n" \
  "v_permlane16_swap_b32 v8, v9\n" \
  "v_max_f32 v8, v8, v9\n" \
  "v_mov_b32 v9, v8\n" \
  "s_min_u32 s58, s58, 128\n" \
  "s_nop 0\n" \
  "v_permlane32_swap_b32 v8, v9\n" \
  "v_max_f32 v8, v8, v9\n" \
  "v_mul_f32 v10, s80, v8\n" \
  "v_pk_fma_f32 v[64:65], v[64:65], s[80:81], v[10:11] op_sel_hi:[1,0,0] neg_lo:[0,0,1] neg_hi:[0,0,1]\n" \
  "v_pk_fma_f32 v[66:67], v[66:67], s[80:81], v[10:11] op_sel_hi:[1,0,0] neg_lo:[0,0,1] neg_hi:[0,0,1]\n" \
  "v_pk_fma_f32 v[68:69], v[68:69], s[80:81], v[10:11] op_sel_hi:[1,0,0] neg_lo:[0,0,1] neg_hi:[0,0,1]\n" \
  "v_pk_fma_f32 v[70:71], v[70:71], s[80:81], v[10:11] op_sel_hi:[1,0,0] neg_lo:[0,0,1] neg_hi:[0,0,1]\n" \
  "v_exp_f32 v64, v64\n" \
  "v_exp_f32 v65, v65\n" \
  "v_exp_f32 v66, v66\n" \
  "v_exp_f32 v67, v67\n" \
  "v_exp_f32 v68, v68\n" \
  "v_exp_f32 v69, v69\n" \
  "v_exp_f32 v70, v70\n" \
  "v_exp_f32 v71, v71\n" \
  "v_cvt_pk_bf16_f32 v104, v64, v65\n" \
  "v_cvt_pk_bf16_f32 v105, v66, v67\n" \
  "v_cvt_pk_bf16_f32 v106, v68, v69\n" \
  "v_cvt_pk_bf16_f32 v107, v70, v71\n" \
  "v_pk_add_f32 v[64:65], v[64:65], v[66:67]\n" \
  "v_pk_add_f32 v[68:69], v[68:69], v[70:71]\n" \
  "s_waitcnt lgkmcnt(0)\n" \
  "v_pk_add_f32 v[64:65], v[64:65], v[68:69]\n" \
  "v_mfma_f32_16x16x32_bf16 v[32:35], v[72:75], v[104:107], 0\n" \
  "v_mfma_f32_16x16x32_bf16 v[48:51], v[88:91], v[104:107], 0\n" \
  "v_mfma_f32_16x16x32_bf16 v[36:39], v[76:79], v[104:107], 0\n" \
  "v_mfma_f32_16x16x32_bf16 v[52:55], v[92:95], v[104:107], 0\n" \
  "v_mfma_f32_16x16x32_bf16 v[40:43], v[80:83], v[104:107], 0\n" \
  "v_mfma_f32_16x16x32_bf16 v[56:59], v[96:99], v[104:107], 0\n" \
  "v_mfma_f32_16x16x32_bf16 v[44:47], v[84:87], v[104:107], 0\n" \
  "v_mfma_f32_16x16x32_bf16 v[60:63], v[100:103], v[104:107], 0\n" \
  "v_add_f32 v11, v64, v65\n" \
  "s_cmp_lt_u32 s56, %[w3]\n" \
  "s_cselect_b32 s68, s57, 0\n" \
  "s_cmp_ge_u32 s68, 64\n" \
  "s_cbranch_scc1 .LWA_EASY_%=\n" \
  "v_cvt_pk_bf16_f32 v108, v32, v33\n" \
  "v_cvt_pk_bf16_f32 v109, v48, v49\n" \
  "v_cvt_pk_bf16_f32 v110, v34, v35\n" \
  "v_cvt_pk_bf16_f32 v111, v50, v51\n" \
  "ds_write_b128 v7, v[108:111] offset:0\n" \
  "v_cvt_pk_bf16_f32 v112, v36, v37\n" \
  "v_cvt_pk_bf16_f32 v113, v52, v53\n" \
  "v_cvt_pk_bf16_f32 v114, v38, v39\n" \
  "v_cvt_pk_bf16_f32 v115, v54, v55\n" \
  "ds_write_b128 v7, v[112:115] offset:1024\n" \
  "v_cvt_pk_bf16_f32 v116, v40, v41\n" \
  "v_cvt_pk_bf16_f32 v117, v56, v57\n" \
  "v_cvt_pk_bf16_f32 v118, v42, v43\n" \
  "v_cvt_pk_bf16_f32 v119, v58, v59\n" \
  "ds_write_b128 v7, v[116:119] offset:2048\n" \
  "v_cvt_pk_bf16_f32 v120, v44, v45\n" \
  "v_cvt_pk_bf16_f32 v121, v60, v61\n" \
  "v_cvt_pk_bf16_f32 v122, v46, v47\n" \
  "v_cvt_pk_bf16_f32 v123, v62, v63\n" \
  "ds_write_b128 v7, v[120:123] offset:3072\n" \
  "s_mov_b32 s0, 0xbe801d62\n" \
  ".set .Lpad0xdd, . - 4\n" \
  "s_waitcnt vmcnt(0)\n" \
  "s_waitcnt lgkmcnt(0)\n" \
  "s_barrier\n" \
  "s_setprio 2\n" \
  "ds_read_b128 v[48:51], v1 offset:0\n" \
  "ds_read_b128 v[52:55], v1 offset:1024\n" \
  "ds_read_b128 v[56:59], v1 offset:2048\n" \
  "s_waitcnt lgkmcnt(2)\n" \
  "v_mfma_f32_16x16x32_bf16 v[64:67], v[128:131], v[48:51], 0\n" \
  "ds_read_b128 v[60:63], v1 offset:3072\n" \
  "s_lshl_b32 s75, s57, 19\n" \
  "v_mfma_f32_16x16x32_bf16 v[68:71], v[160:163], v[48:51], 0\n" \
  "s_add_u32 s75, s75, s60\n" \
  "v_mfma_f32_16x16x32_bf16 v[72:75], v[192:195], v[48:51], 0\n" \
  "s_add_u32 s76, s75, 16384\n" \
  "v_mfma_f32_16x16x32_bf16 v[76:79], v[224:227], v[48:51], 0\n" \
  "s_waitcnt lgkmcnt(2)\n" \
  "v_mfma_f32_16x16x32_bf16 v[64:67], v[132:135], v[52:55], v[64:67]\n" \
  "ds_read_b128 v[48:51], v1 offset:8192\n" \
  "v_mfma_f32_16x16x32_bf16 v[68:71], v[164:167], v[52:55], v[68:71]\n" \
  "v_mfma_f32_16x16x32_bf16 v[72:75], v[196:199], v[52:55], v[72:75]\n" \
  "v_mfma_f32_16x16x32_bf16 v[76:79], v[228:231], v[52:55], v[76:79]\n" \
  "s_waitcnt lgkmcnt(2)\n" \
  "v_mfma_f32_16x16x32_bf16 v[64:67], v[136:139], v[56:59], v[64:67]\n" \
  "ds_read_b128 v[52:55], v1 offset:9216\n" \
  "v_mfma_f32_16x16x32_bf16 v[68:71], v[168:171], v[56:59], v[68:71]\n" \
  "v_mfma_f32_16x16x32_bf16 v[72:75], v[200:203], v[56:59], v[72:75]\n" \
  "v_mfma_f32_16x16x32_bf16 v[76:79], v[232:235], v[56:59], v[76:79]\n" \
  "s_waitcnt lgkmcnt(2)\n" \
  "v_mfma_f32_16x16x32_bf16 v[64:67], v[140:143], v[60:63], v[64:67]\n" \
  "ds_read_b128 v[56:59], v1 offset:10240\n" \
  "v_mfma_f32_16x16x32_bf16 v[68:71], v[172:175], v[60:63], v[68:71]\n" \
  "v_mfma_f32_16x16x32_bf16 v[72:75], v[204:207], v[60:63], v[72:75]\n" \
  "v_mfma_f32_16x16x32_bf16 v[76:79], v[236:239], v[60:63], v[76:79]\n" \
  "s_waitcnt lgkmcnt(2)\n" \
  "v_mfma_f32_16x16x32_bf16 v[96:99], v[144:147], v[48:51], 0\n" \
  "ds_read_b128 v[60:63], v1 offset:11264\n" \
  "v_mfma_f32_16x16x32_bf16 v[100:103], v[176:179], v[48:51], 0\n" \
  "v_mfma_f32_16x16x32_bf16 v[104:107], v[208:211], v[48:51], 0\n" \
  "v_mfma_f32_16x16x32_bf16 v[108:111], v[240:243], v[48:51], 0\n" \
  "s_waitcnt lgkmcnt(2)\n" \
  "v_mfma_f32_16x16x32_bf16 v[96:99], v[148:151], v[52:55], v[96:99]\n" \
  "ds_read_b128 v[48:51], v1 offset:4096\n" \
  "v_mfma_f32_16x16x32_bf16 v[100:103], v[180:183], v[52:55], v[100:103]\n" \
  "v_mfma_f32_16x16x32_bf16 v[104:107], v[212:215], v[52:55], v[104:107]\n" \
  "v_mfma_f32_16x16x32_bf16 v[108:111], v[244:247], v[52:55], v[108:111]\n" \
  "s_waitcnt lgkmcnt(2)\n" \
  "v_mfma_f32_16x16x32_bf16 v[96:99], v[152:155], v[56:59], v[96:99]\n" \
  "ds_read_b128 v[52:55], v1 offset:5120\n" \
  "v_mfma_f32_16x16x32_bf16 v[100:103], v[184:187], v[56:59], v[100:103]\n" \
  "v_mfma_f32_16x16x32_bf16 v[104:107], v[216:219], v[56:59], v[104:107]\n" \
  "v_mfma_f32_16x16x32_bf16 v[108:111], v[248:251], v[56:59], v[108:111]\n" \
  "s_waitcnt lgkmcnt(2)\n" \
  "v_mfma_f32_16x16x32_bf16 v[96:99], v[156:159], v[60:63], v[96:99]\n" \
  "ds_read_b128 v[56:59], v1 offset:6144\n" \
  "v_mfma_f32_16x16x32_bf16 v[100:103], v[188:191], v[60:63], v[100:103]\n" \
  "v_mfma_f32_16x16x32_bf16 v[104:107], v[220:223], v[60:63], v[104:107]\n" \
  "v_mfma_f32_16x16x32_bf16 v[108:111], v[252:255], v[60:63], v[108:111]\n" \
  "s_waitcnt lgkmcnt(2)\n" \
  "v_mfma_f32_16x16x32_bf16 v[80:83], v[128:131], v[48:51], 0\n" \
  "ds_read_b128 v[60:63], v1 offset:7168\n" \
  "v_mfma_f32_16x16x32_bf16 v[84:87], v[160:163], v[48:51], 0\n" \
  "v_mfma_f32_16x16x32_bf16 v[88:91], v[192:195], v[48:51], 0\n" \
  "v_mfma_f32_16x16x32_bf16 v[92:95], v[224:227], v[48:51], 0\n" \
  "s_waitcnt lgkmcnt(2)\n" \
  "v_mfma_f32_16x16x32_bf16 v[80:83], v[132:135], v[52:55], v[80:83]\n" \
  "ds_read_b128 v[48:51], v1 offset:12288\n" \
  "v_mfma_f32_16x16x32_bf16 v[84:87], v[164:167], v[52:55], v[84:87]\n" \
  "v_mfma_f32_16x16x32_bf16 v[88:91], v[196:199], v[52:55], v[88:91]\n" \
  "v_mfma_f32_16x16x32_bf16 v[92:95], v[228:231], v[52:55], v[92:95]\n" \
  "s_waitcnt lgkmcnt(2)\n" \
  "v_mfma_f32_16x16x32_bf16 v[80:83], v[136:139], v[56:59], v[80:83]\n" \
  "ds_read_b128 v[52:55], v1 offset:13312\n" \
  "v_mfma_f32_16x16x32_bf16 v[84:87], v[168:171], v[56:59], v[84:87]\n" \
  "v_mfma_f32_16x16x32_bf16 v[88:91], v[200:203], v[56:59], v[88:91]\n" \
  "v_mfma_f32_16x16x32_bf16 v[92:95], v[232:235], v[56:59], v[92:95]\n" \
  "s_waitcnt lgkmcnt(2)\n" \
  "v_mfma_f32_16x16x32_bf16 v[80:83], v[140:143], v[60:63], v[80:83]\n" \
  "ds_read_b128 v[56:59], v1 offset:14336\n" \
  "v_mfma_f32_16x16x32_bf16 v[84:87], v[172:175], v[60:63], v[84:87]\n" \
  "v_mfma_f32_16x16x32_bf16 v[88:91], v[204:207], v[60:63], v[88:91]\n" \
  "v_mfma_f32_16x16x32_bf16 v[92:95], v[236:239], v[60:63], v[92:95]\n" \
  "s_waitcnt lgkmcnt(2)\n" \
  "v_mfma_f32_16x16x32_bf16 v[112:115], v[144:147], v[48:51], 0\n" \
  "ds_read_b128 v[60:63], v1 offset:15360\n" \
  "v_mfma_f32_16x16x32_bf16 v[116:119], v[176:179], v[48:51], 0\n" \
  "v_mfma_f32_16x16x32_bf16 v[120:123], v[208:211], v[48:51], 0\n" \
  "v_mfma_f32_16x16x32_bf16 v[124:127], v[240:243], v[48:51], 0\n" \
  "s_waitcnt lgkmcnt(2)\n" \
  "v_mfma_f32_16x16x32_bf16 v[112:115], v[148:151], v[52:55], v[112:115]\n" \
  "v_mfma_f32_16x16x32_bf16 v[116:119], v[180:183], v[52:55], v[116:119]\n" \
  "v_mfma_f32_16x16x32_bf16 v[120:123], v[212:215], v[52:55], v[120:123]\n" \
  "v_mfma_f32_16x16x32_bf16 v[124:127], v[244:247], v[52:55], v[124:127]\n" \
  "s_waitcnt lgkmcnt(1)\n" \
  "v_mfma_f32_16x16x32_bf16 v[112:115], v[152:155], v[56:59], v[112:115]\n" \
  "v_mfma_f32_16x16x32_bf16 v[116:119], v[184:187], v[56:59], v[116:119]\n" \
  "v_mfma_f32_16x16x32_bf16 v[120:123], v[216:219], v[56:59], v[120:123]\n" \
  "v_mfma_f32_16x16x32_bf16 v[124:127], v[248:251], v[56:59], v[124:127]\n" \
  "s_waitcnt lgkmcnt(0)\n" \
  "v_mfma_f32_16x16x32_bf16 v[112:115], v[156:159], v[60:63], v[112:115]\n" \
  "v_mfma_f32_16x16x32_bf16 v[116:119], v[188:191], v[60:63], v[116:119]\n" \
  "v_mfma_f32_16x16x32_bf16 v[120:123], v[220:223], v[60:63], v[120:123]\n" \
  "v_mfma_f32_16x16x32_bf16 v[124:127], v[252:255], v[60:63], v[124:127]\n" \
  "s_setprio 3\n" \
  "s_mov_b32 s0, 0xbe801d62\n" \
  ".set .Lpad0xe7, . - 4\n" \
  "s_barrier\n" \
  "ds_read_b128 v[32:35], v5 offset:0\n" \
  "ds_read_b128 v[36:39], v5 offset:512\n" \
  "ds_read_b128 v[40:43], v5 offset:1024\n" \
  "ds_read_b128 v[44:47], v5 offset:1536\n" \
  "ds_read_b64_tr_b16 v[48:49], v6 offset:0\n" \
  "ds_read_b64_tr_b16 v[50:51], v6 offset:32\n" \
  "ds_read_b64_tr_b16 v[52:53], v6 offset:1024\n" \
  "ds_read_b64_tr_b16 v[54:55], v6 offset:1056\n" \
  "ds_read_b64_tr_b16 v[56:57], v6 offset:4096\n" \
  "ds_read_b64_tr_b16 v[58:59], v6 offset:4128\n" \
  "ds_read_b64_tr_b16 v[60:61], v6 offset:5120\n" \
  "ds_read_b64_tr_b16 v[62:63], v6 offset:5152\n" \
  "s_waitcnt lgkmcnt(11)\n" \
  "v_mfma_f32_16x16x32_bf16 v[12:15], v[32:35], v[16:19], 0\n" \
  "s_waitcnt lgkmcnt(10)\n" \
  "v_mfma_f32_16x16x32_bf16 v[12:15], v[36:39], v[20:23], v[12:15]\n" \
  "s_waitcnt lgkmcnt(9)\n" \
  "v_mfma_f32_16x16x32_bf16 v[12:15], v[40:43], v[24:27], v[12:15]\n" \
  "s_waitcnt lgkmcnt(8)\n" \
  "v_mfma_f32_16x16x32_bf16 v[12:15], v[44:47], v[28:31], v[12:15]\n" \
  "v_mov_b64 v[16:17], 0\n" \
  "v_mov_b64 v[18:19], 0\n" \
  "v_mov_b64 v[20:21], 0\n" \
  "v_mov_b64 v[22:23], 0\n" \
  "v_mov_b64 v[24:25], 0\n" \
  "v_mov_b64 v[26:27], 0\n" \
  "v_mov_b64 v[28:29], 0\n" \
  "v_mov_b64 v[30:31], 0\n" \
  "v_maximum3_f32 v8, v8, v12, v13\n" \
  "v_mov_b32 v9, v8\n" \
  "v_mov_b64 v[32:33], 0\n" \
  "v_mov_b64 v[34:35], 0\n" \
  "v_permlane16_swap_b32 v8, v9\n" \
  "v_max_f32 v8, v8, v9\n" \
  "v_mov_b32 v9, v8\n" \
  "v_mov_b64 v[36:37], 0\n" \
  "v_mov_b64 v[38:39], 0\n" \
  "v_permlane32_swap_b32 v8, v9\n" \
  "v_max_f32 v8, v8, v9\n" \
  "v_mul_f32 v8, s80, v8\n" \
  "v_sub_f32 v14, v10, v8\n" \
  "v_fma_f32 v12, v12, s80, -v8\n" \
  "v_fma_f32 v13, v13, s80, -v8\n" \
  "v_exp_f32 v14, v14\n" \
  "v_exp_f32 v12, v12\n" \
  "v_exp_f32 v13, v13\n" \
  "v_fma_f32 v11, v11, v14, v12\n" \
  "v_add_f32 v11, v11, v13\n" \
  "v_mov_b32 v9, v11\n" \
  "v_mov_b64 v[40:41], 0\n" \
  "v_mov_b64 v[42:43], 0\n" \
  "v_permlane16_swap_b32 v11, v9\n" \
  "v_add_f32 v11, v11, v9\n" \
  "v_mov_b32 v9, v11\n" \
  "v_mov_b64 v[44:45], 0\n" \
  "v_mov_b64 v[46:47], 0\n" \
  "v_permlane32_swap_b32 v11, v9\n" \
  "v_add_f32 v11, v11, v9\n" \
  "v_rcp_f32 v15, v11\n" \
  "v_add_u32 v9, s66, v1\n" \
  "v_mul_f32 v12, v12, v15\n" \
  "v_mul_f32 v13, v13, v15\n" \
  "v_mul_f32 v14, v14, v15\n" \
  "v_cvt_pk_bf16_f32 v12, v12, v13\n" \
  "ds_write2_b32 v9, v12, v14 offset1:2\n" \
  "s_mov_b32 s0, 0xbe801d62\n" \
  ".set .Lpad0xde, . - 4\n" \
  "s_waitcnt lgkmcnt(0)\n" \
  "s_barrier\n" \
  "ds_read_b128 v[8:11], v1 offset:24576\n" \
  "ds_read_b128 v[12:15], v1 offset:25600\n" \
  "s_setprio 0\n" \
  "v_smfmac_f32_16x16x64_bf16 v[16:19], v[48:51], v[128:135], v0\n" \
  "v_smfmac_f32_16x16x64_bf16 v[20:23], v[48:51], v[160:167], v0\n" \
  "v_smfmac_f32_16x16x64_bf16 v[24:27], v[48:51], v[192:199], v0\n" \
  "v_smfmac_f32_16x16x64_bf16 v[28:31], v[48:51], v[224:231], v0\n" \
  "v_smfmac_f32_16x16x64_bf16 v[16:19], v[52:55], v[136:143], v0\n" \
  "v_smfmac_f32_16x16x64_bf16 v[20:23], v[52:55], v[168:175], v0\n" \
  "v_smfmac_f32_16x16x64_bf16 v[24:27], v[52:55], v[200:207], v0\n" \
  "v_smfmac_f32_16x16x64_bf16 v[28:31], v[52:55], v[232:239], v0\n" \
  "v_smfmac_f32_16x16x64_bf16 v[32:35], v[56:59], v[144:151], v0\n" \
  "s_waitcnt lgkmcnt(0)\n" \
  "v_pk_mul_f32 v[64:65], v[64:65], v[10:11] op_sel_hi:[1,0]\n" \
  "v_pk_mul_f32 v[66:67], v[66:67], v[10:11] op_sel_hi:[1,0]\n" \
  "v_smfmac_f32_16x16x64_bf16 v[36:39], v[56:59], v[176:183], v0\n" \
  "v_pk_mul_f32 v[68:69], v[68:69], v[10:11] op_sel_hi:[1,0]\n" \
  "v_pk_mul_f32 v[70:71], v[70:71], v[10:11] op_sel_hi:[1,0]\n" \
  "v_pk_fma_f32 v[64:65], v[96:97], v[10:11], v[64:65] op_sel:[0,1,0]\n" \
  "v_smfmac_f32_16x16x64_bf16 v[40:43], v[56:59], v[208:215], v0\n" \
  "v_pk_fma_f32 v[66:67], v[98:99], v[10:11], v[66:67] op_sel:[0,1,0]\n" \
  "v_pk_fma_f32 v[68:69], v[100:101], v[10:11], v[68:69] op_sel:[0,1,0]\n" \
  "v_pk_fma_f32 v[70:71], v[102:103], v[10:11], v[70:71] op_sel:[0,1,0]\n" \
  "v_smfmac_f32_16x16x64_bf16 v[44:47], v[56:59], v[240:247], v0\n" \
  "v_pk_mul_f32 v[72:73], v[72:73], v[10:11] op_sel_hi:[1,0]\n" \
  "v_pk_mul_f32 v[74:75], v[74:75], v[10:11] op_sel_hi:[1,0]\n" \
  "v_pk_mul_f32 v[76:77], v[76:77], v[10:11] op_sel_hi:[1,0]\n" \
  "v_smfmac_f32_16x16x64_bf16 v[32:35], v[60:63], v[152:159], v0\n" \
  "v_pk_mul_f32 v[78:79], v[78:79], v[10:11] op_sel_hi:[1,0]\n" \
  "v_pk_fma_f32 v[72:73], v[104:105], v[10:11], v[72:73] op_sel:[0,1,0]\n" \
  "v_pk_fma_f32 v[74:75], v[106:107], v[10:11], v[74:75] op_sel:[0,1,0]\n" \
  "v_smfmac_f32_16x16x64_bf16 v[36:39], v[60:63], v[184:191], v0\n" \
  "v_pk_fma_f32 v[76:77], v[108:109], v[10:11], v[76:77] op_sel:[0,1,0]\n" \
  "v_pk_fma_f32 v[78:79], v[110:111], v[10:11], v[78:79] op_sel:[0,1,0]\n" \
  "v_pk_mul_f32 v[80:81], v[80:81], v[14:15] op_sel_hi:[1,0]\n" \
  "v_smfmac_f32_16x16x64_bf16 v[40:43], v[60:63], v[216:223], v0\n" \
  "v_pk_mul_f32 v[82:83], v[82:83], v[14:15] op_sel_hi:[1,0]\n" \
  "v_pk_mul_f32 v[84:85], v[84:85], v[14:15] op_sel_hi:[1,0]\n" \
  "v_pk_mul_f32 v[86:87], v[86:87], v[14:15] op_sel_hi:[1,0]\n" \
  "v_smfmac_f32_16x16x64_bf16 v[44:47], v[60:63], v[248:255], v0\n" \
  "v_pk_fma_f32 v[80:81], v[112:113], v[14:15], v[80:81] op_sel:[0,1,0]\n" \
  "v_pk_fma_f32 v[82:83], v[114:115], v[14:15], v[82:83] op_sel:[0,1,0]\n" \
  "v_pk_fma_f32 v[84:85], v[116:117], v[14:15], v[84:85] op_sel:[0,1,0]\n" \
  "v_pk_fma_f32 v[86:87], v[118:119], v[14:15], v[86:87] op_sel:[0,1,0]\n" \
  "v_pk_mul_f32 v[88:89], v[88:89], v[14:15] op_sel_hi:[1,0]\n" \
  "v_pk_mul_f32 v[90:91], v[90:91], v[14:15] op_sel_hi:[1,0]\n" \
  "v_pk_mul_f32 v[92:93], v[92:93], v[14:15] op_sel_hi:[1,0]\n" \
  "v_pk_mul_f32 v[94:95], v[94:95], v[14:15] op_sel_hi:[1,0]\n" \
  "v_pk_fma_f32 v[88:89], v[120:121], v[14:15], v[88:89] op_sel:[0,1,0]\n" \
  "v_pk_fma_f32 v[90:91], v[122:123], v[14:15], v[90:91] op_sel:[0,1,0]\n" \
  "v_pk_fma_f32 v[92:93], v[124:125], v[14:15], v[92:93] op_sel:[0,1,0]\n" \
  "v_pk_fma_f32 v[94:95], v[126:127], v[14:15], v[94:95] op_sel:[0,1,0]\n" \
  "v_pk_add_f32 v[16:17], v[16:17], v[18:19]\n" \
  "v_pk_add_f32 v[32:33], v[32:33], v[34:35]\n" \
  "v_cvt_pk_bf16_f32 v16, v16, v17\n" \
  "v_cvt_pk_bf16_f32 v17, v32, v33\n" \
  "v_pk_add_f32 v[20:21], v[20:21], v[22:23]\n" \
  "v_pk_add_f32 v[36:37], v[36:37], v[38:39]\n" \
  "v_cvt_pk_bf16_f32 v20, v20, v21\n" \
  "v_cvt_pk_bf16_f32 v21, v36, v37\n" \
  "v_pk_add_f32 v[24:25], v[24:25], v[26:27]\n" \
  "v_pk_add_f32 v[40:41], v[40:41], v[42:43]\n" \
  "v_mfma_f32_16x16x16_bf16 v[64:67], v[16:17], v[8:9], v[64:67]\n" \
  "v_mfma_f32_16x16x16_bf16 v[68:71], v[20:21], v[8:9], v[68:71]\n" \
  "v_cvt_pk_bf16_f32 v24, v24, v25\n" \
  "v_cvt_pk_bf16_f32 v25, v40, v41\n" \
  "v_mfma_f32_16x16x16_bf16 v[80:83], v[16:17], v[12:13], v[80:83]\n" \
  "v_mfma_f32_16x16x16_bf16 v[84:87], v[20:21], v[12:13], v[84:87]\n" \
  "v_pk_add_f32 v[28:29], v[28:29], v[30:31]\n" \
  "v_pk_add_f32 v[44:45], v[44:45], v[46:47]\n" \
  "v_cvt_pk_bf16_f32 v28, v28, v29\n" \
  "v_cvt_pk_bf16_f32 v29, v44, v45\n" \
  "s_nop 1\n" \
  "v_mfma_f32_16x16x16_bf16 v[72:75], v[24:25], v[8:9], v[72:75]\n" \
  "v_mfma_f32_16x16x16_bf16 v[76:79], v[28:29], v[8:9], v[76:79]\n" \
  "v_mfma_f32_16x16x16_bf16 v[88:91], v[24:25], v[12:13], v[88:91]\n" \
  "v_mfma_f32_16x16x16_bf16 v[92:95], v[28:29], v[12:13], v[92:95]\n" \
  "v_cvt_pk_bf16_f32 v64, v64, v65\n" \
  "v_cvt_pk_bf16_f32 v65, v66, v67\n" \
  "v_cvt_pk_bf16_f32 v66, v68, v69\n" \
  "v_cvt_pk_bf16_f32 v67, v70, v71\n" \
  "buffer_store_dwordx4 v[64:67], v2, s[40:43], s75 offen offset:0 sc1\n" \
  "v_cvt_pk_bf16_f32 v80, v80, v81\n" \
  "v_cvt_pk_bf16_f32 v81, v82, v83\n" \
  "v_cvt_pk_bf16_f32 v82, v84, v85\n" \
  "v_cvt_pk_bf16_f32 v83, v86, v87\n" \
  "buffer_store_dwordx4 v[80:83], v2, s[40:43], s76 offen offset:0 sc1\n" \
  "v_cvt_pk_bf16_f32 v72, v72, v73\n" \
  "v_cvt_pk_bf16_f32 v73, v74, v75\n" \
  "v_cvt_pk_bf16_f32 v74, v76, v77\n" \
  "v_cvt_pk_bf16_f32 v75, v78, v79\n" \
  "buffer_store_dwordx4 v[72:75], v2, s[40:43], s75 offen offset:128 sc1\n" \
  "v_cvt_pk_bf16_f32 v88, v88, v89\n" \
  "v_cvt_pk_bf16_f32 v89, v90, v91\n" \
  "v_cvt_pk_bf16_f32 v90, v92, v93\n" \
  "v_cvt_pk_bf16_f32 v91, v94, v95\n" \
  "buffer_store_dwordx4 v[88:91], v2, s[40:43], s76 offen offset:128 sc1\n" \
  ".LWA_NEXT_%=:\n" \
  "v_xor_b32 v3, 0x10000, v3\n" \
  "v_xor_b32 v4, 0x10000, v4\n" \
  "s_mov_b32 s57, s58\n" \
  "s_cmp_eq_u32 s57, 128\n" \
  "s_cbranch_scc1 .LWA_END_%=\n" \
  "s_branch .LWA_TOP_%=\n" \
  ".LWA_END_%=:\n" \
  "s_endpgm\n" \
  ".Lpad0xdf:\n" \
  "s_setpc_b64 s[98:99]\n" \
  ".LWA_EASY_%=:\n" \
  "v_lshrrev_b32 v9, 1, v1\n" \
  "v_mov_b32 v105, 0\n" \
  "v_add_u32 v64, s63, v9\n" \
  "v_mov_b32 v106, 0\n" \
  "s_barrier\n" \
  "s_setprio 3\n" \
  "ds_read_b128 v[72:75], v5 offset:0\n" \
  "ds_read_b128 v[76:79], v5 offset:512\n" \
  "ds_read_b128 v[80:83], v5 offset:1024\n" \
  "ds_read_b128 v[84:87], v5 offset:1536\n" \
  "ds_read_b64 v[88:89], v64 offset:0\n" \
  "ds_read_b64 v[90:91], v64 offset:512\n" \
  "ds_read_b64 v[92:93], v64 offset:1024\n" \
  "ds_read_b64 v[94:95], v64 offset:1536\n" \
  "s_waitcnt lgkmcnt(7)\n" \
  "v_mfma_f32_16x16x32_bf16 v[64:67], v[72:75], v[16:19], 0\n" \
  "s_waitcnt lgkmcnt(6)\n" \
  "v_mfma_f32_16x16x32_bf16 v[64:67], v[76:79], v[20:23], v[64:67]\n" \
  "s_waitcnt lgkmcnt(5)\n" \
  "v_mfma_f32_16x16x32_bf16 v[64:67], v[80:83], v[24:27], v[64:67]\n" \
  "s_waitcnt lgkmcnt(4)\n" \
  "v_mfma_f32_16x16x32_bf16 v[64:67], v[84:87], v[28:31], v[64:67]\n" \
  "s_nop 7\n" \
  "v_maximum3_f32 v8, v8, v64, v65\n" \
  "v_mov_b32 v9, v8\n" \
  "s_nop 1\n" \
  "v_permlane16_swap_b32 v8, v9\n" \
  "v_max_f32 v8, v8, v9\n" \
  "v_mov_b32 v9, v8\n" \
  "s_nop 1\n" \
  "v_permlane32_swap_b32 v8, v9\n" \
  "v_max_f32 v8, v8, v9\n" \
  "v_mul_f32 v8, s80, v8\n" \
  "v_sub_f32 v66, v10, v8\n" \
  "v_fma_f32 v64, v64, s80, -v8\n" \
  "v_fma_f32 v65, v65, s80, -v8\n" \
  "v_exp_f32 v66, v66\n" \
  "v_exp_f32 v64, v64\n" \
  "v_exp_f32 v65, v65\n" \
  "v_fma_f32 v11, v11, v66, v64\n" \
  "v_add_f32 v11, v11, v65\n" \
  "v_mov_b32 v9, v11\n" \
  "s_nop 1\n" \
  "v_permlane16_swap_b32 v11, v9\n" \
  "v_add_f32 v11, v11, v9\n" \
  "v_mov_b32 v9, v11\n" \
  "s_nop 1\n" \
  "v_permlane32_swap_b32 v11, v9\n" \
  "v_add_f32 v11, v11, v9\n" \
  "v_rcp_f32 v68, v11\n" \
  "s_waitcnt lgkmcnt(0)\n" \
  "v_mul_f32 v66, v66, v68\n" \
  "v_mul_f32 v64, v64, v68\n" \
  "v_mul_f32 v65, v65, v68\n" \
  "v_pk_mul_f32 v[32:33], v[32:33], v[66:67] op_sel_hi:[1,0]\n" \
  "v_cvt_pk_bf16_f32 v104, v64, v65\n" \
  "v_pk_mul_f32 v[34:35], v[34:35], v[66:67] op_sel_hi:[1,0]\n" \
  "v_cvt_pk_bf16_f32 v107, v64, v65\n" \
  "v_mul_f32 v48, v48, v66\n" \
  "v_mul_f32 v49, v49, v66\n" \
  "v_mul_f32 v50, v50, v66\n" \
  "v_mul_f32 v51, v51, v66\n" \
  "v_mfma_f32_16x16x16_bf16 v[32:35], v[88:89], v[104:105], v[32:35]\n" \
  "v_mul_f32 v36, v36, v66\n" \
  "v_mul_f32 v37, v37, v66\n" \
  "v_mul_f32 v38, v38, v66\n" \
  "v_mul_f32 v39, v39, v66\n" \
  "v_mfma_f32_16x16x16_bf16 v[48:51], v[88:89], v[106:107], v[48:51]\n" \
  "v_mul_f32 v52, v52, v66\n" \
  "v_mul_f32 v53, v53, v66\n" \
  "v_mul_f32 v54, v54, v66\n" \
  "v_mul_f32 v55, v55, v66\n" \
  "v_mfma_f32_16x16x16_bf16 v[36:39], v[90:91], v[104:105], v[36:39]\n" \
  "v_mul_f32 v40, v40, v66\n" \
  "v_mul_f32 v41, v41, v66\n" \
  "v_mul_f32 v42, v42, v66\n" \
  "v_mul_f32 v43, v43, v66\n" \
  "v_mfma_f32_16x16x16_bf16 v[52:55], v[90:91], v[106:107], v[52:55]\n" \
  "v_mul_f32 v56, v56, v66\n" \
  "v_mul_f32 v57, v57, v66\n" \
  "v_mul_f32 v58, v58, v66\n" \
  "v_mul_f32 v59, v59, v66\n" \
  "v_mfma_f32_16x16x16_bf16 v[40:43], v[92:93], v[104:105], v[40:43]\n" \
  "v_mul_f32 v44, v44, v66\n" \
  "v_mul_f32 v45, v45, v66\n" \
  "v_mul_f32 v46, v46, v66\n" \
  "v_mul_f32 v47, v47, v66\n" \
  "v_mfma_f32_16x16x16_bf16 v[56:59], v[92:93], v[106:107], v[56:59]\n" \
  "v_mul_f32 v60, v60, v66\n" \
  "v_mul_f32 v61, v61, v66\n" \
  "v_mul_f32 v62, v62, v66\n" \
  "v_mul_f32 v63, v63, v66\n" \
  "v_mfma_f32_16x16x16_bf16 v[44:47], v[94:95], v[104:105], v[44:47]\n" \
  "v_cvt_pk_bf16_f32 v108, v32, v33\n" \
  "v_cvt_pk_bf16_f32 v109, v48, v49\n" \
  "v_cvt_pk_bf16_f32 v110, v34, v35\n" \
  "v_cvt_pk_bf16_f32 v111, v50, v51\n" \
  "ds_write_b128 v7, v[108:111] offset:32768\n" \
  "v_mfma_f32_16x16x16_bf16 v[60:63], v[94:95], v[106:107], v[60:63]\n" \
  "v_cvt_pk_bf16_f32 v112, v36, v37\n" \
  "v_cvt_pk_bf16_f32 v113, v52, v53\n" \
  "v_cvt_pk_bf16_f32 v114, v38, v39\n" \
  "v_cvt_pk_bf16_f32 v115, v54, v55\n" \
  "ds_write_b128 v7, v[112:115] offset:33792\n" \
  "v_cvt_pk_bf16_f32 v116, v40, v41\n" \
  "v_cvt_pk_bf16_f32 v117, v56, v57\n" \
  "v_cvt_pk_bf16_f32 v118, v42, v43\n" \
  "v_cvt_pk_bf16_f32 v119, v58, v59\n" \
  "ds_write_b128 v7, v[116:119] offset:34816\n" \
  "v_cvt_pk_bf16_f32 v120, v44, v45\n" \
  "v_cvt_pk_bf16_f32 v121, v60, v61\n" \
  "v_cvt_pk_bf16_f32 v122, v46, v47\n" \
  "v_cvt_pk_bf16_f32 v123, v62, v63\n" \
  "ds_write_b128 v7, v[120:123] offset:35840\n" \
  "s_mov_b32 s0, 0xbe801d62\n" \
  ".set .Lpad0xe0, . - 4\n" \
  "s_waitcnt lgkmcnt(0)\n" \
  "s_barrier\n" \
  "s_setprio 2\n" \
  "ds_read_b128 v[48:51], v1 offset:32768\n" \
  "ds_read_b128 v[52:55], v1 offset:33792\n" \
  "ds_read_b128 v[56:59], v1 offset:34816\n" \
  "s_waitcnt lgkmcnt(2)\n" \
  "v_mfma_f32_16x16x32_bf16 v[64:67], v[128:131], v[48:51], 0\n" \
  "ds_read_b128 v[60:63], v1 offset:35840\n" \
  "s_lshl_b32 s75, s57, 19\n" \
  "v_mfma_f32_16x16x32_bf16 v[68:71], v[160:163], v[48:51], 0\n" \
  "s_add_u32 s75, s75, s60\n" \
  "v_mfma_f32_16x16x32_bf16 v[72:75], v[192:195], v[48:51], 0\n" \
  "s_add_u32 s76, s75, 16384\n" \
  "v_mfma_f32_16x16x32_bf16 v[76:79], v[224:227], v[48:51], 0\n" \
  "s_waitcnt lgkmcnt(2)\n" \
  "v_mfma_f32_16x16x32_bf16 v[64:67], v[132:135], v[52:55], v[64:67]\n" \
  "ds_read_b128 v[48:51], v1 offset:40960\n" \
  "v_mfma_f32_16x16x32_bf16 v[68:71], v[164:167], v[52:55], v[68:71]\n" \
  "v_mfma_f32_16x16x32_bf16 v[72:75], v[196:199], v[52:55], v[72:75]\n" \
  "v_mfma_f32_16x16x32_bf16 v[76:79], v[228:231], v[52:55], v[76:79]\n" \
  "s_waitcnt lgkmcnt(2)\n" \
  "v_mfma_f32_16x16x32_bf16 v[64:67], v[136:139], v[56:59], v[64:67]\n" \
  "ds_read_b128 v[52:55], v1 offset:41984\n" \
  "v_mfma_f32_16x16x32_bf16 v[68:71], v[168:171], v[56:59], v[68:71]\n" \
  "v_mfma_f32_16x16x32_bf16 v[72:75], v[200:203], v[56:59], v[72:75]\n" \
  "v_mfma_f32_16x16x32_bf16 v[76:79], v[232:235], v[56:59], v[76:79]\n" \
  "s_waitcnt lgkmcnt(2)\n" \
  "v_mfma_f32_16x16x32_bf16 v[64:67], v[140:143], v[60:63], v[64:67]\n" \
  "ds_read_b128 v[56:59], v1 offset:43008\n" \
  "v_mfma_f32_16x16x32_bf16 v[68:71], v[172:175], v[60:63], v[68:71]\n" \
  "v_mfma_f32_16x16x32_bf16 v[72:75], v[204:207], v[60:63], v[72:75]\n" \
  "v_mfma_f32_16x16x32_bf16 v[76:79], v[236:239], v[60:63], v[76:79]\n" \
  "s_waitcnt lgkmcnt(2)\n" \
  "v_mfma_f32_16x16x32_bf16 v[64:67], v[144:147], v[48:51], v[64:67]\n" \
  "ds_read_b128 v[60:63], v1 offset:44032\n" \
  "v_mfma_f32_16x16x32_bf16 v[68:71], v[176:179], v[48:51], v[68:71]\n" \
  "v_mfma_f32_16x16x32_bf16 v[72:75], v[208:211], v[48:51], v[72:75]\n" \
  "v_mfma_f32_16x16x32_bf16 v[76:79], v[240:243], v[48:51], v[76:79]\n" \
  "s_waitcnt lgkmcnt(2)\n" \
  "v_mfma_f32_16x16x32_bf16 v[64:67], v[148:151], v[52:55], v[64:67]\n" \
  "ds_read_b128 v[48:51], v1 offset:36864\n" \
  "v_mfma_f32_16x16x32_bf16 v[68:71], v[180:183], v[52:55], v[68:71]\n" \
  "v_mfma_f32_16x16x32_bf16 v[72:75], v[212:215], v[52:55], v[72:75]\n" \
  "v_mfma_f32_16x16x32_bf16 v[76:79], v[244:247], v[52:55], v[76:79]\n" \
  "s_waitcnt lgkmcnt(2)\n" \
  "v_mfma_f32_16x16x32_bf16 v[64:67], v[152:155], v[56:59], v[64:67]\n" \
  "ds_read_b128 v[52:55], v1 offset:37888\n" \
  "v_mfma_f32_16x16x32_bf16 v[68:71], v[184:187], v[56:59], v[68:71]\n" \
  "v_mfma_f32_16x16x32_bf16 v[72:75], v[216:219], v[56:59], v[72:75]\n" \
  "v_mfma_f32_16x16x32_bf16 v[76:79], v[248:251], v[56:59], v[76:79]\n" \
  "s_waitcnt lgkmcnt(2)\n" \
  "v_mfma_f32_16x16x32_bf16 v[64:67], v[156:159], v[60:63], v[64:67]\n" \
  "ds_read_b128 v[56:59], v1 offset:38912\n" \
  "v_mfma_f32_16x16x32_bf16 v[68:71], v[188:191], v[60:63], v[68:71]\n" \
  "v_mfma_f32_16x16x32_bf16 v[72:75], v[220:223], v[60:63], v[72:75]\n" \
  "v_mfma_f32_16x16x32_bf16 v[76:79], v[252:255], v[60:63], v[76:79]\n" \
  "s_waitcnt lgkmcnt(2)\n" \
  "v_mfma_f32_16x16x32_bf16 v[80:83], v[128:131], v[48:51], 0\n" \
  "ds_read_b128 v[60:63], v1 offset:39936\n" \
  "v_mfma_f32_16x16x32_bf16 v[84:87], v[160:163], v[48:51], 0\n" \
  "v_mfma_f32_16x16x32_bf16 v[88:91], v[192:195], v[48:51], 0\n" \
  "v_mfma_f32_16x16x32_bf16 v[92:95], v[224:227], v[48:51], 0\n" \
  "s_waitcnt lgkmcnt(2)\n" \
  "v_mfma_f32_16x16x32_bf16 v[80:83], v[132:135], v[52:55], v[80:83]\n" \
  "ds_read_b128 v[48:51], v1 offset:45056\n" \
  "v_mfma_f32_16x16x32_bf16 v[84:87], v[164:167], v[52:55], v[84:87]\n" \
  "v_cvt_pk_bf16_f32 v64, v64, v65\n" \
  "v_mfma_f32_16x16x32_bf16 v[88:91], v[196:199], v[52:55], v[88:91]\n" \
  "v_cvt_pk_bf16_f32 v65, v66, v67\n" \
  "v_mfma_f32_16x16x32_bf16 v[92:95], v[228:231], v[52:55], v[92:95]\n" \
  "s_waitcnt lgkmcnt(2)\n" \
  "v_mfma_f32_16x16x32_bf16 v[80:83], v[136:139], v[56:59], v[80:83]\n" \
  "ds_read_b128 v[52:55], v1 offset:46080\n" \
  "v_mfma_f32_16x16x32_bf16 v[84:87], v[168:171], v[56:59], v[84:87]\n" \
  "v_cvt_pk_bf16_f32 v66, v68, v69\n" \
  "v_mfma_f32_16x16x32_bf16 v[88:91], v[200:203], v[56:59], v[88:91]\n" \
  "v_cvt_pk_bf16_f32 v67, v70, v71\n" \
  "v_mfma_f32_16x16x32_bf16 v[92:95], v[232:235], v[56:59], v[92:95]\n" \
  "s_waitcnt lgkmcnt(2)\n" \
  "v_mfma_f32_16x16x32_bf16 v[80:83], v[140:143], v[60:63], v[80:83]\n" \
  "ds_read_b128 v[56:59], v1 offset:47104\n" \
  "v_mfma_f32_16x16x32_bf16 v[84:87], v[172:175], v[60:63], v[84:87]\n" \
  "buffer_store_dwordx4 v[64:67], v2, s[40:43], s75 offen offset:0 sc1\n" \
  "v_mfma_f32_16x16x32_bf16 v[88:91], v[204:207], v[60:63], v[88:91]\n" \
  "v_cvt_pk_bf16_f32 v72, v72, v73\n" \
  "v_mfma_f32_16x16x32_bf16 v[92:95], v[236:239], v[60:63], v[92:95]\n" \
  "s_waitcnt lgkmcnt(2)\n" \
  "v_mfma_f32_16x16x32_bf16 v[80:83], v[144:147], v[48:51], v[80:83]\n" \
  "ds_read_b128 v[60:63], v1 offset:48128\n" \
  "v_mfma_f32_16x16x32_bf16 v[84:87], v[176:179], v[48:51], v[84:87]\n" \
  "v_cvt_pk_bf16_f32 v73, v74, v75\n" \
  "v_mfma_f32_16x16x32_bf16 v[88:91], v[208:211], v[48:51], v[88:91]\n" \
  "v_cvt_pk_bf16_f32 v74, v76, v77\n" \
  "v_mfma_f32_16x16x32_bf16 v[92:95], v[240:243], v[48:51], v[92:95]\n" \
  "s_waitcnt lgkmcnt(2)\n" \
  "v_mfma_f32_16x16x32_bf16 v[80:83], v[148:151], v[52:55], v[80:83]\n" \
  "v_mfma_f32_16x16x32_bf16 v[84:87], v[180:183], v[52:55], v[84:87]\n" \
  "v_cvt_pk_bf16_f32 v75, v78, v79\n" \
  "v_mfma_f32_16x16x32_bf16 v[88:91], v[212:215], v[52:55], v[88:91]\n" \
  "buffer_store_dwordx4 v[72:75], v2, s[40:43], s75 offen offset:128 sc1\n" \
  "v_mfma_f32_16x16x32_bf16 v[92:95], v[244:247], v[52:55], v[92:95]\n" \
  "s_waitcnt lgkmcnt(1)\n" \
  "v_mfma_f32_16x16x32_bf16 v[80:83], v[152:155], v[56:59], v[80:83]\n" \
  "v_mfma_f32_16x16x32_bf16 v[84:87], v[184:187], v[56:59], v[84:87]\n" \
  "v_mfma_f32_16x16x32_bf16 v[88:91], v[216:219], v[56:59], v[88:91]\n" \
  "v_mfma_f32_16x16x32_bf16 v[92:95], v[248:251], v[56:59], v[92:95]\n" \
  "s_waitcnt lgkmcnt(0)\n" \
  "v_mfma_f32_16x16x32_bf16 v[80:83], v[156:159], v[60:63], v[80:83]\n" \
  "v_mfma_f32_16x16x32_bf16 v[84:87], v[188:191], v[60:63], v[84:87]\n" \
  "v_mfma_f32_16x16x32_bf16 v[88:91], v[220:223], v[60:63], v[88:91]\n" \
  "v_mfma_f32_16x16x32_bf16 v[92:95], v[252:255], v[60:63], v[92:95]\n" \
  "s_nop 4\n" \
  "v_cvt_pk_bf16_f32 v80, v80, v81\n" \
  "v_cvt_pk_bf16_f32 v81, v82, v83\n" \
  "v_cvt_pk_bf16_f32 v82, v84, v85\n" \
  "v_cvt_pk_bf16_f32 v83, v86, v87\n" \
  "buffer_store_dwordx4 v[80:83], v2, s[40:43], s76 offen offset:0 sc1\n" \
  "v_cvt_pk_bf16_f32 v88, v88, v89\n" \
  "v_cvt_pk_bf16_f32 v89, v90, v91\n" \
  "v_cvt_pk_bf16_f32 v90, v92, v93\n" \
  "v_cvt_pk_bf16_f32 v91, v94, v95\n" \
  "buffer_store_dwordx4 v[88:91], v2, s[40:43], s76 offen offset:128 sc1\n" \
  "s_branch .LWA_NEXT_%=\n" \
  ".p2alignl 12, 0xbf800000\n" \
  ".Lpad0x78:\n" \
  "s_setpc_b64 s[98:99]\n"
#define AW_ASM_1 \
  "v_lshlrev_b32 v7, 5, v0\n" \
  "s_mov_b32 s79, -1\n" \
  "s_cmp_eq_u32 s67, 0\n" \
  "s_cbranch_scc1 .LWB_LATE_%=\n" \
  "s_waitcnt vmcnt(0)\n" \
  "v_mov_b32 v12, s65\n" \
  "v_mov_b32 v13, s67\n" \
  "s_mov_b64 exec, 1\n" \
  "ds_add_u32 v12, v13\n" \
  "s_mov_b64 exec, -1\n" \
  "s_branch .LWB_AL_%=\n" \
  ".p2alignl 12, 0xbf800000, 4000\n" \
  ".p2align 6\n" \
  ".Lpad0xe1:\n" \
  "s_setpc_b64 s[98:99]\n" \
  ".p2align 6\n" \
  ".LWB_AL_%=:\n" \
  ".LWB_TOP_%=:\n" \
  "s_add_u32 s56, s56, 3\n" \
  "s_mov_b32 s77, 0\n" \
  "s_cmp_lt_u32 s56, %[w3]\n" \
  "s_cselect_b32 s68, s57, 0\n" \
  "s_cmp_ge_u32 s68, 64\n" \
  "s_cbranch_scc0 .LWB_HARD_%=\n" \
  "s_mov_b32 m0, s63\n" \
  "s_lshl_b32 s72, s57, 16\n" \
  "s_add_u32 s72, s72, s52\n" \
  "s_addc_u32 s73, s53, 0\n" \
  "global_load_lds_dwordx4 v0, s[72:73] offset:0 sc1\n" \
  "global_load_lds_dwordx4 v0, s[72:73] offset:1024 sc1\n" \
  ".LWB_HARD_%=:\n" \
  "s_sub_u32 s68, s56, %[wd]\n" \
  "s_bitcmp1_b32 s68, 0\n" \
  "s_cselect_b32 s58, 64, %[wd]\n" \
  "s_lshr_b32 s70, s68, 1\n" \
  "s_add_u32 s58, s58, s70\n" \
  "s_cmp_lt_u32 s68, %[w2]\n" \
  "s_cselect_b32 s58, s58, s56\n" \
  "s_min_u32 s58, s58, 128\n" \
  "s_lshr_b32 s68, s58, 6\n" \
  "s_add_u32 s68, s68, s58\n" \
  "s_lshl_b32 s68, s68, 12\n" \
  "buffer_load_dword v5, off, s[44:47], s68 sc1\n" \
  "s_xor_b32 s61, s61, 0x10000\n" \
  "s_xor_b32 s62, s62, 0x10000\n" \
  "s_mov_b32 s67, 0\n" \
  "s_cmp_lt_u32 s56, %[w3]\n" \
  "s_cselect_b32 s68, s57, 0\n" \
  "s_cmp_ge_u32 s68, 64\n" \
  "s_cbranch_scc1 .LWB_ASKEDE_%=\n" \
  "s_and_b32 s75, s57, 64\n" \
  "s_lshl_b32 s75, s75, 4\n" \
  "buffer_load_dword v14, off, s[84:87], s75 sc1\n" \
  "s_and_b32 s76, s57, 63\n" \
  "s_cmp_eq_u32 s76, 63\n" \
  "s_mul_i32 s76, s76, %[pp]\n" \
  "s_cselect_b32 s76, %[lm], s76\n" \
  "s_add_u32 s76, s76, %[mo]\n" \
  "s_lshl_b32 s59, s57, 19\n" \
  "s_add_u32 s59, s59, s66\n" \
  "s_add_u32 s78, s59, 4096\n" \
  "v_mov_b64 v[8:9], -1\n" \
  "v_mov_b64 v[10:11], -1\n" \
  "s_barrier\n" \
  "s_setprio 0\n" \
  "buffer_store_dwordx4 v[8:11], v0, s[40:43], s59 offen offset:0 sc1\n" \
  "buffer_store_dwordx4 v[8:11], v0, s[40:43], s59 offen offset:1024 sc1\n" \
  "buffer_store_dwordx4 v[8:11], v0, s[40:43], s59 offen offset:2048 sc1\n" \
  "buffer_store_dwordx4 v[8:11], v0, s[40:43], s59 offen offset:3072 sc1\n" \
  "buffer_store_dwordx4 v[8:11], v0, s[40:43], s78 offen offset:0 sc1\n" \
  "buffer_store_dwordx4 v[8:11], v0, s[40:43], s78 offen offset:1024 sc1\n" \
  "buffer_store_dwordx4 v[8:11], v0, s[40:43], s78 offen offset:2048 sc1\n" \
  "buffer_store_dwordx4 v[8:11], v0, s[40:43], s78 offen offset:3072 sc1\n" \
  "v_mov_b64 v[16:17], 0\n" \
  "v_mov_b64 v[18:19], 0\n" \
  "v_mov_b64 v[20:21], 0\n" \
  "v_mov_b64 v[22:23], 0\n" \
  "v_mov_b64 v[24:25], 0\n" \
  "v_mov_b64 v[26:27], 0\n" \
  "v_mov_b64 v[28:29], 0\n" \
  "v_mov_b64 v[30:31], 0\n" \
  "v_mov_b64 v[32:33], 0\n" \
  "v_mov_b64 v[34:35], 0\n" \
  "v_mov_b64 v[36:37], 0\n" \
  "v_mov_b64 v[38:39], 0\n" \
  "v_mov_b64 v[40:41], 0\n" \
  "v_mov_b64 v[42:43], 0\n" \
  "v_mov_b64 v[44:45], 0\n" \
  "v_mov_b64 v[46:47], 0\n" \
  "s_waitcnt vmcnt(8)\n" \
  "v_readfirstlane_b32 s74, v14\n" \
  "s_add_u32 s74, s74, s76\n" \
  "s_cmp_lt_i32 s79, 0\n" \
  "s_cbranch_scc1 .LWB_NOD_%=\n" \
  "v_mov_b32 v12, s64\n" \
  "v_mov_b32 v13, 1\n" \
  "s_mov_b64 exec, 1\n" \
  "ds_add_rtn_u32 v15, v12, v13\n" \
  "s_mov_b64 exec, -1\n" \
  "s_waitcnt lgkmcnt(0)\n" \
  "v_readfirstlane_b32 s68, v15\n" \
  "s_and_b32 s68, s68, 3\n" \
  "s_cmp_lg_u32 s68, 3\n" \
  "s_cbranch_scc1 .LWB_NOD_%=\n" \
  "s_memrealtime s[68:69]\n" \
  "s_lshl_b32 s72, s79, 11\n" \
  "s_add_u32 s72, s72, s54\n" \
  "s_addc_u32 s73, s55, 0\n" \
  "s_waitcnt lgkmcnt(0)\n" \
  "s_add_u32 s70, s68, 0\n" \
  "s_or_b32 s70, s70, 1\n" \
  "v_mov_b32 v13, s70\n" \
  "s_mov_b64 exec, 15\n" \
  "global_store_dword v7, v13, s[72:73] sc1\n" \
  "s_mov_b64 exec, -1\n" \
  ".LWB_NOD_%=:\n" \
  "s_mov_b32 s79, -1\n" \
  "v_cmp_eq_u32 vcc, 0, v5\n" \
  "s_cmp_eq_u64 vcc, 0\n" \
  "s_cbranch_scc0 .LWB_NOT_%=\n" \
  "s_mov_b32 m0, s61\n" \
  "s_lshl_b32 s68, s58, 19\n" \
  "s_add_u32 s68, s68, s48\n" \
  "s_addc_u32 s69, s49, 0\n" \
  "global_load_lds_dwordx4 v0, s[68:69] offset:0 sc1\n" \
  "global_load_lds_dwordx4 v0, s[68:69] offset:1024 sc1\n" \
  "global_load_lds_dwordx4 v0, s[68:69] offset:2048 sc1\n" \
  "global_load_lds_dwordx4 v0, s[68:69] offset:3072 sc1\n" \
  "s_add_u32 m0, s61, 4096\n" \
  "s_add_u32 s70, s68, 4096\n" \
  "s_addc_u32 s71, s69, 0\n" \
  "global_load_lds_dwordx4 v0, s[70:71] offset:0 sc1\n" \
  "global_load_lds_dwordx4 v0, s[70:71] offset:1024 sc1\n" \
  "global_load_lds_dwordx4 v0, s[70:71] offset:2048 sc1\n" \
  "global_load_lds_dwordx4 v0, s[70:71] offset:3072 sc1\n" \
  "s_mov_b32 m0, s62\n" \
  "s_lshl_b32 s72, s58, 18\n" \
  "s_add_u32 s72, s72, s50\n" \
  "s_addc_u32 s73, s51, 0\n" \
  "global_load_lds_dwordx4 v0, s[72:73] offset:0 sc1\n" \
  "global_load_lds_dwordx4 v0, s[72:73] offset:1024 sc1\n" \
  "global_load_lds_dwordx4 v0, s[72:73] offset:2048 sc1\n" \
  "global_load_lds_dwordx4 v0, s[72:73] offset:3072 sc1\n" \
  "s_mov_b32 s67, 1\n" \
  ".LWB_NOT_%=:\n" \
  "s_mov_b32 s0, 0xbe801d62\n" \
  ".set .Lpad0xe2, . - 4\n" \
  "ds_read_b128 v[48:51], v1 offset:0\n" \
  "ds_read_b128 v[52:55], v1 offset:1024\n" \
  "ds_read_b128 v[56:59], v1 offset:2048\n" \
  "s_waitcnt lgkmcnt(2)\n" \
  "v_mfma_f32_16x16x32_bf16 v[64:67], v[128:131], v[48:51], 0\n" \
  "ds_read_b128 v[60:63], v1 offset:3072\n" \
  "v_mfma_f32_16x16x32_bf16 v[68:71], v[160:163], v[48:51], 0\n" \
  "v_mfma_f32_16x16x32_bf16 v[72:75], v[192:195], v[48:51], 0\n" \
  "v_mfma_f32_16x16x32_bf16 v[76:79], v[224:227], v[48:51], 0\n" \
  "s_waitcnt lgkmcnt(2)\n" \
  "v_mfma_f32_16x16x32_bf16 v[64:67], v[132:135], v[52:55], v[64:67]\n" \
  "ds_read_b128 v[48:51], v1 offset:8192\n" \
  "v_mfma_f32_16x16x32_bf16 v[68:71], v[164:167], v[52:55], v[68:71]\n" \
  "v_mfma_f32_16x16x32_bf16 v[72:75], v[196:199], v[52:55], v[72:75]\n" \
  "v_mfma_f32_16x16x32_bf16 v[76:79], v[228:231], v[52:55], v[76:79]\n" \
  "s_waitcnt lgkmcnt(2)\n" \
  "v_mfma_f32_16x16x32_bf16 v[64:67], v[136:139], v[56:59], v[64:67]\n" \
  "ds_read_b128 v[52:55], v1 offset:9216\n" \
  "s_memrealtime s[68:69]\n" \
  "v_mfma_f32_16x16x32_bf16 v[68:71], v[168:171], v[56:59], v[68:71]\n" \
  "v_mfma_f32_16x16x32_bf16 v[72:75], v[200:203], v[56:59], v[72:75]\n" \
  "v_mfma_f32_16x16x32_bf16 v[76:79], v[232:235], v[56:59], v[76:79]\n" \
  "s_waitcnt lgkmcnt(2)\n" \
  "v_mfma_f32_16x16x32_bf16 v[64:67], v[140:143], v[60:63], v[64:67]\n" \
  "ds_read_b128 v[56:59], v1 offset:10240\n" \
  "s_waitcnt lgkmcnt(0)\n" \
  "s_sub_i32 s70, s74, s68\n" \
  "s_cmp_gt_i32 s70, 0\n" \
  "s_cbranch_scc1 .LWB_NOTYET_%=\n" \
  "s_mov_b32 m0, s63\n" \
  "s_lshl_b32 s72, s57, 16\n" \
  "s_add_u32 s72, s72, s52\n" \
  "s_addc_u32 s73, s53, 0\n" \
  "global_load_lds_dwordx4 v0, s[72:73] offset:0 sc1\n" \
  "global_load_lds_dwordx4 v0, s[72:73] offset:1024 sc1\n" \
  "s_mov_b32 s77, 1\n" \
  ".LWB_NOTYET_%=:\n" \
  "s_lshl_b32 s70, s67, 7\n" \
  "s_or_b32 s70, s70, s58\n" \
  "s_lshr_b32 s68, s70, 6\n" \
  "s_add_u32 s68, s68, s70\n" \
  "s_lshl_b32 s68, s68, 12\n" \
  "buffer_load_dword v5, off, s[44:47], s68 sc1\n" \
  "v_mfma_f32_16x16x32_bf16 v[68:71], v[172:175], v[60:63], v[68:71]\n" \
  "v_mfma_f32_16x16x32_bf16 v[72:75], v[204:207], v[60:63], v[72:75]\n" \
  "v_mfma_f32_16x16x32_bf16 v[76:79], v[236:239], v[60:63], v[76:79]\n" \
  "s_waitcnt lgkmcnt(2)\n" \
  "v_mfma_f32_16x16x32_bf16 v[96:99], v[144:147], v[48:51], 0\n" \
  "ds_read_b128 v[60:63], v1 offset:11264\n" \
  "v_mfma_f32_16x16x32_bf16 v[100:103], v[176:179], v[48:51], 0\n" \
  "v_mfma_f32_16x16x32_bf16 v[104:107], v[208:211], v[48:51], 0\n" \
  "v_mfma_f32_16x16x32_bf16 v[108:111], v[240:243], v[48:51], 0\n" \
  "s_waitcnt lgkmcnt(2)\n" \
  "v_mfma_f32_16x16x32_bf16 v[96:99], v[148:151], v[52:55], v[96:99]\n" \
  "ds_read_b128 v[48:51], v1 offset:4096\n" \
  "v_mfma_f32_16x16x32_bf16 v[100:103], v[180:183], v[52:55], v[100:103]\n" \
  "v_mfma_f32_16x16x32_bf16 v[104:107], v[212:215], v[52:55], v[104:107]\n" \
  "v_mfma_f32_16x16x32_bf16 v[108:111], v[244:247], v[52:55], v[108:111]\n" \
  "s_waitcnt lgkmcnt(2)\n" \
  "v_mfma_f32_16x16x32_bf16 v[96:99], v[152:155], v[56:59], v[96:99]\n" \
  "ds_read_b128 v[52:55], v1 offset:5120\n" \
  "v_mfma_f32_16x16x32_bf16 v[100:103], v[184:187], v[56:59], v[100:103]\n" \
  "v_mfma_f32_16x16x32_bf16 v[104:107], v[216:219], v[56:59], v[104:107]\n" \
  "v_mfma_f32_16x16x32_bf16 v[108:111], v[248:251], v[56:59], v[108:111]\n" \
  "s_waitcnt lgkmcnt(2)\n" \
  "v_mfma_f32_16x16x32_bf16 v[96:99], v[156:159], v[60:63], v[96:99]\n" \
  "ds_read_b128 v[56:59], v1 offset:6144\n" \
  "v_mfma_f32_16x16x32_bf16 v[100:103], v[188:191], v[60:63], v[100:103]\n" \
  "v_mfma_f32_16x16x32_bf16 v[104:107], v[220:223], v[60:63], v[104:107]\n" \
  "v_mfma_f32_16x16x32_bf16 v[108:111], v[252:255], v[60:63], v[108:111]\n" \
  "s_waitcnt lgkmcnt(2)\n" \
  "v_mfma_f32_16x16x32_bf16 v[80:83], v[128:131], v[48:51], 0\n" \
  "ds_read_b128 v[60:63], v1 offset:7168\n" \
  "v_mfma_f32_16x16x32_bf16 v[84:87], v[160:163], v[48:51], 0\n" \
  "v_mfma_f32_16x16x32_bf16 v[88:91], v[192:195], v[48:51], 0\n" \
  "v_mfma_f32_16x16x32_bf16 v[92:95], v[224:227], v[48:51], 0\n" \
  "s_waitcnt lgkmcnt(2)\n" \
  "v_mfma_f32_16x16x32_bf16 v[80:83], v[132:135], v[52:55], v[80:83]\n" \
  "ds_read_b128 v[48:51], v1 offset:12288\n" \
  "v_mfma_f32_16x16x32_bf16 v[84:87], v[164:167], v[52:55], v[84:87]\n" \
  "v_mfma_f32_16x16x32_bf16 v[88:91], v[196:199], v[52:55], v[88:91]\n" \
  "v_mfma_f32_16x16x32_bf16 v[92:95], v[228:231], v[52:55], v[92:95]\n" \
  "s_waitcnt lgkmcnt(2)\n" \
  "v_mfma_f32_16x16x32_bf16 v[80:83], v[136:139], v[56:59], v[80:83]\n" \
  "ds_read_b128 v[52:55], v1 offset:13312\n" \
  "v_mfma_f32_16x16x32_bf16 v[84:87], v[168:171], v[56:59], v[84:87]\n" \
  "v_mfma_f32_16x16x32_bf16 v[88:91], v[200:203], v[56:59], v[88:91]\n" \
  "v_mfma_f32_16x16x32_bf16 v[92:95], v[232:235], v[56:59], v[92:95]\n" \
  "s_waitcnt lgkmcnt(2)\n" \
  "v_mfma_f32_16x16x32_bf16 v[80:83], v[140:143], v[60:63], v[80:83]\n" \
  "ds_read_b128 v[56:59], v1 offset:14336\n" \
  "v_mfma_f32_16x16x32_bf16 v[84:87], v[172:175], v[60:63], v[84:87]\n" \
  "v_mfma_f32_16x16x32_bf16 v[88:91], v[204:207], v[60:63], v[88:91]\n" \
  "v_mfma_f32_16x16x32_bf16 v[92:95], v[236:239], v[60:63], v[92:95]\n" \
  "s_waitcnt lgkmcnt(2)\n" \
  "v_mfma_f32_16x16x32_bf16 v[112:115], v[144:147], v[48:51], 0\n" \
  "ds_read_b128 v[60:63], v1 offset:15360\n" \
  "v_mfma_f32_16x16x32_bf16 v[116:119], v[176:179], v[48:51], 0\n" \
  "v_mfma_f32_16x16x32_bf16 v[120:123], v[208:211], v[48:51], 0\n" \
  "v_mfma_f32_16x16x32_bf16 v[124:127], v[240:243], v[48:51], 0\n" \
  "s_waitcnt lgkmcnt(2)\n" \
  "v_mfma_f32_16x16x32_bf16 v[112:115], v[148:151], v[52:55], v[112:115]\n" \
  "v_mfma_f32_16x16x32_bf16 v[116:119], v[180:183], v[52:55], v[116:119]\n" \
  "v_mfma_f32_16x16x32_bf16 v[120:123], v[212:215], v[52:55], v[120:123]\n" \
  "s_waitcnt vmcnt(0)\n" \
  "ds_read_b128 v[8:11], v3 offset:0\n" \
  "ds_read_b128 v[12:15], v3 offset:1024\n" \
  "v_mfma_f32_16x16x32_bf16 v[124:127], v[244:247], v[52:55], v[124:127]\n" \
  "s_waitcnt lgkmcnt(3)\n" \
  "v_mfma_f32_16x16x32_bf16 v[112:115], v[152:155], v[56:59], v[112:115]\n" \
  "v_mfma_f32_16x16x32_bf16 v[116:119], v[184:187], v[56:59], v[116:119]\n" \
  "v_mfma_f32_16x16x32_bf16 v[120:123], v[216:219], v[56:59], v[120:123]\n" \
  "v_mfma_f32_16x16x32_bf16 v[124:127], v[248:251], v[56:59], v[124:127]\n" \
  "s_waitcnt lgkmcnt(2)\n" \
  "v_mfma_f32_16x16x32_bf16 v[112:115], v[156:159], v[60:63], v[112:115]\n" \
  "s_waitcnt lgkmcnt(0)\n" \
  "v_min3_u32 v8, v8, v9, v10\n" \
  "v_mfma_f32_16x16x32_bf16 v[116:119], v[188:191], v[60:63], v[116:119]\n" \
  "v_min3_u32 v12, v12, v13, v14\n" \
  "v_mfma_f32_16x16x32_bf16 v[120:123], v[220:223], v[60:63], v[120:123]\n" \
  "v_min3_u32 v8, v8, v11, v15\n" \
  "v_mfma_f32_16x16x32_bf16 v[124:127], v[252:255], v[60:63], v[124:127]\n" \
  "v_min_u32 v8, v8, v12\n" \
  "v_cmp_eq_u32 vcc, 0, v5\n" \
  "s_cmp_eq_u64 vcc, 0\n" \
  "s_cbranch_scc0 .LWB_NOU_%=\n" \
  "s_mov_b32 m0, s61\n" \
  "s_lshl_b32 s68, s58, 19\n" \
  "s_add_u32 s68, s68, s48\n" \
  "s_addc_u32 s69, s49, 0\n" \
  "global_load_lds_dwordx4 v0, s[68:69] offset:0 sc1\n" \
  "global_load_lds_dwordx4 v0, s[68:69] offset:1024 sc1\n" \
  "global_load_lds_dwordx4 v0, s[68:69] offset:2048 sc1\n" \
  "global_load_lds_dwordx4 v0, s[68:69] offset:3072 sc1\n" \
  "s_add_u32 m0, s61, 4096\n" \
  "s_add_u32 s70, s68, 4096\n" \
  "s_addc_u32 s71, s69, 0\n" \
  "global_load_lds_dwordx4 v0, s[70:71] offset:0 sc1\n" \
  "global_load_lds_dwordx4 v0, s[70:71] offset:1024 sc1\n" \
  "global_load_lds_dwordx4 v0, s[70:71] offset:2048 sc1\n" \
  "global_load_lds_dwordx4 v0, s[70:71] offset:3072 sc1\n" \
  "s_mov_b32 m0, s62\n" \
  "s_lshl_b32 s72, s58, 18\n" \
  "s_add_u32 s72, s72, s50\n" \
  "s_addc_u32 s73, s51, 0\n" \
  "global_load_lds_dwordx4 v0, s[72:73] offset:0 sc1\n" \
  "global_load_lds_dwordx4 v0, s[72:73] offset:1024 sc1\n" \
  "global_load_lds_dwordx4 v0, s[72:73] offset:2048 sc1\n" \
  "global_load_lds_dwordx4 v0, s[72:73] offset:3072 sc1\n" \
  "s_mov_b32 s67, 1\n" \
  ".LWB_NOU_%=:\n" \
  "s_mov_b32 s0, 0xbe801d62\n" \
  ".set .Lpad0xe3, . - 4\n" \
  "s_cmp_eq_u32 s77, 1\n" \
  "s_cbranch_scc1 .LWB_HERE_%=\n" \
  "s_memrealtime s[68:69]\n" \
  "s_waitcnt lgkmcnt(0)\n" \
  "s_sub_i32 s70, s74, s68\n" \
  "s_cmp_lt_i32 s70, 50\n" \
  "s_cbranch_scc1 .LWB_SOON_%=\n" \
  ".LWB_GATEA_%=:\n" \
  "s_memrealtime s[68:69]\n" \
  "s_waitcnt lgkmcnt(0)\n" \
  "s_sub_i32 s70, s74, s68\n" \
  "s_sub_u32 s70, s70, 65\n" \
  "s_cmp_gt_u32 s70, 2000\n" \
  "s_cbranch_scc1 .LWB_NEWS_%=\n" \
  "s_sleep 1\n" \
  "s_cmp_lt_u32 s70, 40\n" \
  "s_cbranch_scc1 .LWB_GATEA_%=\n" \
  "s_sleep 8\n" \
  "s_branch .LWB_GATEA_%=\n" \
  ".LWB_HERE_%=:\n" \
  "v_mov_b32 v12, s64\n" \
  "v_mov_b32 v13, 1\n" \
  "s_mov_b64 exec, 1\n" \
  "ds_add_rtn_u32 v15, v12, v13 offset:4\n" \
  "s_mov_b64 exec, -1\n" \
  "s_waitcnt lgkmcnt(0)\n" \
  "v_readfirstlane_b32 s68, v15\n" \
  "s_and_b32 s68, s68, 3\n" \
  "s_cmp_lg_u32 s68, 3\n" \
  "s_cbranch_scc1 .LWB_TEST_%=\n" \
  "s_memrealtime s[68:69]\n" \
  "s_lshl_b32 s72, s57, 11\n" \
  "s_add_u32 s72, s72, s54\n" \
  "s_addc_u32 s73, s55, 0\n" \
  "s_waitcnt lgkmcnt(0)\n" \
  "s_cmp_eq_u32 s57, 127\n" \
  "s_cselect_b32 s70, %[ld], 0\n" \
  "s_sub_u32 s68, s68, s70\n" \
  "s_add_u32 s70, s68, %[dt]\n" \
  "s_or_b32 s70, s70, 1\n" \
  "v_mov_b32 v13, s70\n" \
  "s_mov_b64 exec, 15\n" \
  "global_store_dword v7, v13, s[72:73] sc1\n" \
  "s_mov_b64 exec, -1\n" \
  "s_branch .LWB_TEST_%=\n" \
  ".LWB_NEWS_%=:\n" \
  "buffer_load_dword v14, off, s[84:87], s75 sc1\n" \
  "s_waitcnt vmcnt(0)\n" \
  "v_readfirstlane_b32 s74, v14\n" \
  "s_add_u32 s74, s74, s76\n" \
  ".LWB_SOON_%=:\n" \
  "v_mov_b32 v12, s64\n" \
  "v_mov_b32 v13, 1\n" \
  "s_mov_b64 exec, 1\n" \
  "ds_add_rtn_u32 v15, v12, v13 offset:4\n" \
  "s_mov_b64 exec, -1\n" \
  "s_waitcnt lgkmcnt(0)\n" \
  "v_readfirstlane_b32 s68, v15\n" \
  "s_and_b32 s68, s68, 3\n" \
  "s_cmp_lg_u32 s68, 3\n" \
  "s_cbranch_scc1 .LWB_GATEB_%=\n" \
  "s_memrealtime s[68:69]\n" \
  "s_lshl_b32 s72, s57, 11\n" \
  "s_add_u32 s72, s72, s54\n" \
  "s_addc_u32 s73, s55, 0\n" \
  "s_waitcnt lgkmcnt(0)\n" \
  "s_sub_i32 s70, s74, s68\n" \
  "s_cmp_gt_u32 s70, 2000\n" \
  "s_cselect_b32 s70, 0, s70\n" \
  "s_add_u32 s68, s68, s70\n" \
  "s_cmp_eq_u32 s57, 127\n" \
  "s_cselect_b32 s70, %[ld], 0\n" \
  "s_sub_u32 s68, s68, s70\n" \
  "s_add_u32 s70, s68, %[du]\n" \
  "s_or_b32 s70, s70, 1\n" \
  "v_mov_b32 v13, s70\n" \
  "s_mov_b64 exec, 15\n" \
  "global_store_dword v7, v13, s[72:73] sc1\n" \
  "s_mov_b64 exec, -1\n" \
  ".LWB_GATEB_%=:\n" \
  "s_memrealtime s[68:69]\n" \
  "s_waitcnt lgkmcnt(0)\n" \
  "s_sub_i32 s70, s74, s68\n" \
  "s_sub_u32 s70, s70, 0\n" \
  "s_cmp_gt_u32 s70, 2000\n" \
  "s_cbranch_scc1 .LWB_ASK_%=\n" \
  "s_sleep 1\n" \
  "s_cmp_lt_u32 s70, 40\n" \
  "s_cbranch_scc1 .LWB_GATEB_%=\n" \
  "s_sleep 8\n" \
  "s_branch .LWB_GATEB_%=\n" \
  ".LWB_AGAIN_%=:\n" \
  "s_sleep %[sm]\n" \
  ".LWB_ASK_%=:\n" \
  "s_mov_b32 m0, s63\n" \
  "s_lshl_b32 s72, s57, 16\n" \
  "s_add_u32 s72, s72, s52\n" \
  "s_addc_u32 s73, s53, 0\n" \
  "global_load_lds_dwordx4 v0, s[72:73] offset:0 sc1\n" \
  "global_load_lds_dwordx4 v0, s[72:73] offset:1024 sc1\n" \
  "s_add_u32 s77, s77, 1\n" \
  ".LWB_ASKED_%=:\n" \
  "s_waitcnt vmcnt(0)\n" \
  "ds_read_b128 v[8:11], v3 offset:0\n" \
  "ds_read_b128 v[12:15], v3 offset:1024\n" \
  "s_cmp_gt_u32 s77, 0x40000\n" \
  "s_cbranch_scc1 .LWB_OK_%=\n" \
  "s_waitcnt lgkmcnt(1)\n" \
  "v_min3_u32 v8, v8, v9, v10\n" \
  "s_waitcnt lgkmcnt(0)\n" \
  "v_min3_u32 v12, v12, v13, v14\n" \
  "v_min3_u32 v8, v8, v11, v15\n" \
  "v_min_u32 v8, v8, v12\n" \
  ".LWB_TEST_%=:\n" \
  "v_cmp_eq_u32 vcc, 0, v8\n" \
  "s_cmp_eq_u64 vcc, 0\n" \
  "s_cbranch_scc0 .LWB_AGAIN_%=\n" \
  ".LWB_OK_%=:\n" \
  "s_waitcnt lgkmcnt(0)\n" \
  "s_barrier\n" \
  "ds_read_b64_tr_b16 v[48:49], v6 offset:0\n" \
  "ds_read_b64_tr_b16 v[50:51], v6 offset:32\n" \
  "ds_read_b64_tr_b16 v[52:53], v6 offset:1024\n" \
  "ds_read_b64_tr_b16 v[54:55], v6 offset:1056\n" \
  "ds_read_b64_tr_b16 v[56:57], v6 offset:4096\n" \
  "ds_read_b64_tr_b16 v[58:59], v6 offset:4128\n" \
  "ds_read_b64_tr_b16 v[60:61], v6 offset:5120\n" \
  "ds_read_b64_tr_b16 v[62:63], v6 offset:5152\n" \
  "s_waitcnt lgkmcnt(6)\n" \
  "v_smfmac_f32_16x16x64_bf16 v[16:19], v[48:51], v[128:135], v4\n" \
  "v_smfmac_f32_16x16x64_bf16 v[20:23], v[48:51], v[160:167], v4\n" \
  "v_smfmac_f32_16x16x64_bf16 v[24:27], v[48:51], v[192:199], v4\n" \
  "v_smfmac_f32_16x16x64_bf16 v[28:31], v[48:51], v[224:231], v4\n" \
  "s_waitcnt lgkmcnt(4)\n" \
  "v_smfmac_f32_16x16x64_bf16 v[16:19], v[52:55], v[136:143], v4\n" \
  "v_smfmac_f32_16x16x64_bf16 v[20:23], v[52:55], v[168:175], v4\n" \
  "v_smfmac_f32_16x16x64_bf16 v[24:27], v[52:55], v[200:207], v4\n" \
  "v_smfmac_f32_16x16x64_bf16 v[28:31], v[52:55], v[232:239], v4\n" \
  "s_waitcnt lgkmcnt(2)\n" \
  "v_smfmac_f32_16x16x64_bf16 v[32:35], v[56:59], v[144:151], v4\n" \
  "v_smfmac_f32_16x16x64_bf16 v[36:39], v[56:59], v[176:183], v4\n" \
  "v_smfmac_f32_16x16x64_bf16 v[40:43], v[56:59], v[208:215], v4\n" \
  "v_smfmac_f32_16x16x64_bf16 v[44:47], v[56:59], v[240:247], v4\n" \
  "s_waitcnt lgkmcnt(0)\n" \
  "v_smfmac_f32_16x16x64_bf16 v[32:35], v[60:63], v[152:159], v4\n" \
  "v_smfmac_f32_16x16x64_bf16 v[36:39], v[60:63], v[184:191], v4\n" \
  "v_smfmac_f32_16x16x64_bf16 v[40:43], v[60:63], v[216:223], v4\n" \
  "v_smfmac_f32_16x16x64_bf16 v[44:47], v[60:63], v[248:255], v4\n" \
  "s_nop 1\n" \
  "s_lshl_b32 s75, s57, 19\n" \
  "s_add_u32 s75, s75, s60\n" \
  "s_add_u32 s76, s75, 16384\n" \
  "v_pk_add_f32 v[16:17], v[16:17], v[18:19]\n" \
  "v_pk_add_f32 v[32:33], v[32:33], v[34:35]\n" \
  "v_cvt_pk_bf16_f32 v16, v16, v17\n" \
  "v_cvt_pk_bf16_f32 v17, v32, v33\n" \
  "v_pk_add_f32 v[20:21], v[20:21], v[22:23]\n" \
  "v_pk_add_f32 v[36:37], v[36:37], v[38:39]\n" \
  "v_cvt_pk_bf16_f32 v20, v20, v21\n" \
  "v_cvt_pk_bf16_f32 v21, v36, v37\n" \
  "v_pk_add_f32 v[24:25], v[24:25], v[26:27]\n" \
  "v_pk_add_f32 v[40:41], v[40:41], v[42:43]\n" \
  "v_cvt_pk_bf16_f32 v24, v24, v25\n" \
  "v_cvt_pk_bf16_f32 v25, v40, v41\n" \
  "v_pk_add_f32 v[28:29], v[28:29], v[30:31]\n" \
  "v_pk_add_f32 v[44:45], v[44:45], v[46:47]\n" \
  "v_cvt_pk_bf16_f32 v28, v28, v29\n" \
  "v_cvt_pk_bf16_f32 v29, v44, v45\n" \
  "s_mov_b32 s0, 0xbe801d62\n" \
  ".set .Lpad0xe4, . - 4\n" \
  "s_barrier\n" \
  "ds_read_b128 v[32:35], v1 offset:24576\n" \
  "ds_read_b128 v[36:39], v1 offset:25600\n" \
  "s_setprio 1\n" \
  "s_waitcnt lgkmcnt(0)\n" \
  "v_pk_mul_f32 v[64:65], v[64:65], v[34:35] op_sel_hi:[1,0]\n" \
  "v_pk_mul_f32 v[66:67], v[66:67], v[34:35] op_sel_hi:[1,0]\n" \
  "v_pk_mul_f32 v[68:69], v[68:69], v[34:35] op_sel_hi:[1,0]\n" \
  "v_pk_mul_f32 v[70:71], v[70:71], v[34:35] op_sel_hi:[1,0]\n" \
  "v_pk_fma_f32 v[64:65], v[96:97], v[34:35], v[64:65] op_sel:[0,1,0]\n" \
  "v_pk_fma_f32 v[66:67], v[98:99], v[34:35], v[66:67] op_sel:[0,1,0]\n" \
  "v_pk_fma_f32 v[68:69], v[100:101], v[34:35], v[68:69] op_sel:[0,1,0]\n" \
  "v_pk_fma_f32 v[70:71], v[102:103], v[34:35], v[70:71] op_sel:[0,1,0]\n" \
  "s_nop 1\n" \
  "v_mfma_f32_16x16x16_bf16 v[64:67], v[16:17], v[32:33], v[64:67]\n" \
  "v_mfma_f32_16x16x16_bf16 v[68:71], v[20:21], v[32:33], v[68:71]\n" \
  "v_pk_mul_f32 v[72:73], v[72:73], v[34:35] op_sel_hi:[1,0]\n" \
  "v_pk_mul_f32 v[74:75], v[74:75], v[34:35] op_sel_hi:[1,0]\n" \
  "v_pk_mul_f32 v[76:77], v[76:77], v[34:35] op_sel_hi:[1,0]\n" \
  "v_pk_mul_f32 v[78:79], v[78:79], v[34:35] op_sel_hi:[1,0]\n" \
  "v_pk_fma_f32 v[72:73], v[104:105], v[34:35], v[72:73] op_sel:[0,1,0]\n" \
  "v_pk_fma_f32 v[74:75], v[106:107], v[34:35], v[74:75] op_sel:[0,1,0]\n" \
  "v_pk_fma_f32 v[76:77], v[108:109], v[34:35], v[76:77] op_sel:[0,1,0]\n" \
  "v_pk_fma_f32 v[78:79], v[110:111], v[34:35], v[78:79] op_sel:[0,1,0]\n" \
  "s_nop 1\n" \
  "v_mfma_f32_16x16x16_bf16 v[72:75], v[24:25], v[32:33], v[72:75]\n" \
  "v_mfma_f32_16x16x16_bf16 v[76:79], v[28:29], v[32:33], v[76:79]\n" \
  "v_cvt_pk_bf16_f32 v64, v64, v65\n" \
  "v_cvt_pk_bf16_f32 v65, v66, v67\n" \
  "v_cvt_pk_bf16_f32 v66, v68, v69\n" \
  "v_cvt_pk_bf16_f32 v67, v70, v71\n" \
  "buffer_store_dwordx4 v[64:67], v2, s[40:43], s75 offen offset:0 sc1\n" \
  "v_pk_mul_f32 v[80:81], v[80:81], v[38:39] op_sel_hi:[1,0]\n" \
  "v_pk_mul_f32 v[82:83], v[82:83], v[38:39] op_sel_hi:[1,0]\n" \
  "v_pk_mul_f32 v[84:85], v[84:85], v[38:39] op_sel_hi:[1,0]\n" \
  "v_pk_mul_f32 v[86:87], v[86:87], v[38:39] op_sel_hi:[1,0]\n" \
  "v_pk_fma_f32 v[80:81], v[112:113], v[38:39], v[80:81] op_sel:[0,1,0]\n" \
  "v_pk_fma_f32 v[82:83], v[114:115], v[38:39], v[82:83] op_sel:[0,1,0]\n" \
  "v_pk_fma_f32 v[84:85], v[116:117], v[38:39], v[84:85] op_sel:[0,1,0]\n" \
  "v_pk_fma_f32 v[86:87], v[118:119], v[38:39], v[86:87] op_sel:[0,1,0]\n" \
  "s_nop 1\n" \
  "v_mfma_f32_16x16x16_bf16 v[80:83], v[16:17], v[36:37], v[80:83]\n" \
  "v_mfma_f32_16x16x16_bf16 v[84:87], v[20:21], v[36:37], v[84:87]\n" \
  "v_cvt_pk_bf16_f32 v72, v72, v73\n" \
  "v_cvt_pk_bf16_f32 v73, v74, v75\n" \
  "v_cvt_pk_bf16_f32 v74, v76, v77\n" \
  "v_cvt_pk_bf16_f32 v75, v78, v79\n" \
  "buffer_store_dwordx4 v[72:75], v2, s[40:43], s75 offen offset:128 sc1\n" \
  "v_pk_mul_f32 v[88:89], v[88:89], v[38:39] op_sel_hi:[1,0]\n" \
  "v_pk_mul_f32 v[90:91], v[90:91], v[38:39] op_sel_hi:[1,0]\n" \
  "v_pk_mul_f32 v[92:93], v[92:93], v[38:39] op_sel_hi:[1,0]\n" \
  "v_pk_mul_f32 v[94:95], v[94:95], v[38:39] op_sel_hi:[1,0]\n" \
  "v_pk_fma_f32 v[88:89], v[120:121], v[38:39], v[88:89] op_sel:[0,1,0]\n" \
  "v_pk_fma_f32 v[90:91], v[122:123], v[38:39], v[90:91] op_sel:[0,1,0]\n" \
  "v_pk_fma_f32 v[92:93], v[124:125], v[38:39], v[92:93] op_sel:[0,1,0]\n" \
  "v_pk_fma_f32 v[94:95], v[126:127], v[38:39], v[94:95] op_sel:[0,1,0]\n" \
  "s_nop 1\n" \
  "v_mfma_f32_16x16x16_bf16 v[88:91], v[24:25], v[36:37], v[88:91]\n" \
  "v_mfma_f32_16x16x16_bf16 v[92:95], v[28:29], v[36:37], v[92:95]\n" \
  "v_cvt_pk_bf16_f32 v80, v80, v81\n" \
  "v_cvt_pk_bf16_f32 v81, v82, v83\n" \
  "v_cvt_pk_bf16_f32 v82, v84, v85\n" \
  "v_cvt_pk_bf16_f32 v83, v86, v87\n" \
  "buffer_store_dwordx4 v[80:83], v2, s[40:43], s76 offen offset:0 sc1\n" \
  "s_nop 2\n" \
  "v_cvt_pk_bf16_f32 v88, v88, v89\n" \
  "v_cvt_pk_bf16_f32 v89, v90, v91\n" \
  "v_cvt_pk_bf16_f32 v90, v92, v93\n" \
  "v_cvt_pk_bf16_f32 v91, v94, v95\n" \
  "buffer_store_dwordx4 v[88:91], v2, s[40:43], s76 offen offset:128 sc1\n" \
  "s_waitcnt vmcnt(4)\n" \
  "v_mov_b32 v12, s65\n" \
  "v_mov_b32 v13, s67\n" \
  "s_mov_b64 exec, 1\n" \
  "ds_add_u32 v12, v13\n" \
  "s_mov_b64 exec, -1\n" \
  ".LWB_NEXT_%=:\n" \
  "s_setprio 0\n" \
  "s_mov_b32 s57, s58\n" \
  "s_cmp_eq_u32 s67, 0\n" \
  "s_cbranch_scc1 .LWB_MISS_%=\n" \
  "s_branch .LWB_TOP_%=\n" \
  ".LWB_MISS_%=:\n" \
  "s_cmp_eq_u32 s57, 128\n" \
  "s_cbranch_scc1 .LWB_END_%=\n" \
  ".LWB_LATE_%=:\n" \
  "s_mov_b32 s77, 0x40000\n" \
  ".LWB_POLL_%=:\n" \
  "s_lshr_b32 s68, s57, 6\n" \
  "s_add_u32 s68, s68, s57\n" \
  "s_lshl_b32 s68, s68, 12\n" \
  "buffer_load_dword v5, off, s[44:47], s68 sc1\n" \
  "s_sub_u32 s77, s77, 1\n" \
  "s_cbranch_scc1 .LWB_SEEN_%=\n" \
  "s_waitcnt vmcnt(0)\n" \
  "v_cmp_eq_u32 vcc, 0, v5\n" \
  "s_cmp_eq_u64 vcc, 0\n" \
  "s_cbranch_scc1 .LWB_SEEN_%=\n" \
  "s_sleep %[sa]\n" \
  "s_branch .LWB_POLL_%=\n" \
  ".LWB_SEEN_%=:\n" \
  "s_mov_b32 m0, s61\n" \
  "s_lshl_b32 s68, s57, 19\n" \
  "s_add_u32 s68, s68, s48\n" \
  "s_addc_u32 s69, s49, 0\n" \
  "global_load_lds_dwordx4 v0, s[68:69] offset:0 sc1\n" \
  "global_load_lds_dwordx4 v0, s[68:69] offset:1024 sc1\n" \
  "global_load_lds_dwordx4 v0, s[68:69] offset:2048 sc1\n" \
  "global_load_lds_dwordx4 v0, s[68:69] offset:3072 sc1\n" \
  "s_add_u32 m0, s61, 4096\n" \
  "s_add_u32 s70, s68, 4096\n" \
  "s_addc_u32 s71, s69, 0\n" \
  "global_load_lds_dwordx4 v0, s[70:71] offset:0 sc1\n" \
  "global_load_lds_dwordx4 v0, s[70:71] offset:1024 sc1\n" \
  "global_load_lds_dwordx4 v0, s[70:71] offset:2048 sc1\n" \
  "global_load_lds_dwordx4 v0, s[70:71] offset:3072 sc1\n" \
  "s_mov_b32 m0, s62\n" \
  "s_lshl_b32 s72, s57, 18\n" \
  "s_add_u32 s72, s72, s50\n" \
  "s_addc_u32 s73, s51, 0\n" \
  "global_load_lds_dwordx4 v0, s[72:73] offset:0 sc1\n" \
  "global_load_lds_dwordx4 v0, s[72:73] offset:1024 sc1\n" \
  "global_load_lds_dwordx4 v0, s[72:73] offset:2048 sc1\n" \
  "global_load_lds_dwordx4 v0, s[72:73] offset:3072 sc1\n" \
  "s_mov_b32 s67, 1\n" \
  "s_waitcnt vmcnt(0)\n" \
  "v_mov_b32 v12, s65\n" \
  "v_mov_b32 v13, s67\n" \
  "s_mov_b64 exec, 1\n" \
  "ds_add_u32 v12, v13\n" \
  "s_mov_b64 exec, -1\n" \
  "s_branch .LWB_TOP_%=\n" \
  ".LWB_END_%=:\n" \
  "s_endpgm\n" \
  ".Lpad0xe5:\n" \
  "s_setpc_b64 s[98:99]\n" \
  ".LWB_AGAINE_%=:\n" \
  "s_sleep %[sm]\n" \
  ".LWB_ASKE_%=:\n" \
  "s_mov_b32 m0, s63\n" \
  "s_lshl_b32 s72, s57, 16\n" \
  "s_add_u32 s72, s72, s52\n" \
  "s_addc_u32 s73, s53, 0\n" \
  "global_load_lds_dwordx4 v0, s[72:73] offset:0 sc1\n" \
  "global_load_lds_dwordx4 v0, s[72:73] offset:1024 sc1\n" \
  "s_add_u32 s77, s77, 1\n" \
  ".LWB_ASKEDE_%=:\n" \
  "s_waitcnt vmcnt(0)\n" \
  "ds_read_b128 v[8:11], v3 offset:0\n" \
  "ds_read_b128 v[12:15], v3 offset:1024\n" \
  "s_cmp_gt_u32 s77, 0x40000\n" \
  "s_cbranch_scc1 .LWB_OKE_%=\n" \
  "s_waitcnt lgkmcnt(1)\n" \
  "v_min3_u32 v8, v8, v9, v10\n" \
  "s_waitcnt lgkmcnt(0)\n" \
  "v_min3_u32 v12, v12, v13, v14\n" \
  "v_min3_u32 v8, v8, v11, v15\n" \
  "v_min_u32 v8, v8, v12\n" \
  ".LWB_TESTE_%=:\n" \
  "v_cmp_eq_u32 vcc, 0, v8\n" \
  "s_cmp_eq_u64 vcc, 0\n" \
  "s_cbranch_scc0 .LWB_AGAINE_%=\n" \
  ".LWB_OKE_%=:\n" \
  "s_waitcnt lgkmcnt(0)\n" \
  "s_barrier\n" \
  "v_cmp_eq_u32 vcc, 0, v5\n" \
  "s_cmp_eq_u64 vcc, 0\n" \
  "s_cbranch_scc0 .LWB_NOE_%=\n" \
  "s_mov_b32 m0, s61\n" \
  "s_lshl_b32 s68, s58, 19\n" \
  "s_add_u32 s68, s68, s48\n" \
  "s_addc_u32 s69, s49, 0\n" \
  "global_load_lds_dwordx4 v0, s[68:69] offset:0 sc1\n" \
  "global_load_lds_dwordx4 v0, s[68:69] offset:1024 sc1\n" \
  "global_load_lds_dwordx4 v0, s[68:69] offset:2048 sc1\n" \
  "global_load_lds_dwordx4 v0, s[68:69] offset:3072 sc1\n" \
  "s_add_u32 m0, s61, 4096\n" \
  "s_add_u32 s70, s68, 4096\n" \
  "s_addc_u32 s71, s69, 0\n" \
  "global_load_lds_dwordx4 v0, s[70:71] offset:0 sc1\n" \
  "global_load_lds_dwordx4 v0, s[70:71] offset:1024 sc1\n" \
  "global_load_lds_dwordx4 v0, s[70:71] offset:2048 sc1\n" \
  "global_load_lds_dwordx4 v0, s[70:71] offset:3072 sc1\n" \
  "s_mov_b32 m0, s62\n" \
  "s_lshl_b32 s72, s58, 18\n" \
  "s_add_u32 s72, s72, s50\n" \
  "s_addc_u32 s73, s51, 0\n" \
  "global_load_lds_dwordx4 v0, s[72:73] offset:0 sc1\n" \
  "global_load_lds_dwordx4 v0, s[72:73] offset:1024 sc1\n" \
  "global_load_lds_dwordx4 v0, s[72:73] offset:2048 sc1\n" \
  "global_load_lds_dwordx4 v0, s[72:73] offset:3072 sc1\n" \
  "s_mov_b32 s67, 1\n" \
  ".LWB_NOE_%=:\n" \
  "s_lshl_b32 s70, s67, 7\n" \
  "s_or_b32 s70, s70, s58\n" \
  "s_lshr_b32 s68, s70, 6\n" \
  "s_add_u32 s68, s68, s70\n" \
  "s_lshl_b32 s68, s68, 12\n" \
  "buffer_load_dword v5, off, s[44:47], s68 sc1\n" \
  "s_mov_b32 s77, 0\n" \
  "s_mov_b32 s0, 0xbe801d62\n" \
  ".set .Lpad0xe6, . - 4\n" \
  "s_barrier\n" \
  "s_setprio 0\n" \
  "s_waitcnt vmcnt(0)\n" \
  "v_mov_b32 v12, s65\n" \
  "v_mov_b32 v13, s67\n" \
  "s_mov_b64 exec, 1\n" \
  "ds_add_u32 v12, v13\n" \
  "s_mov_b64 exec, -1\n" \
  "v_cmp_eq_u32 vcc, 0, v5\n" \
  "s_cmp_eq_u64 vcc, 0\n" \
  "s_cbranch_scc0 .LWB_NOF_%=\n" \
  "s_mov_b32 m0, s61\n" \
  "s_lshl_b32 s68, s58, 19\n" \
  "s_add_u32 s68, s68, s48\n" \
  "s_addc_u32 s69, s49, 0\n" \
  "global_load_lds_dwordx4 v0, s[68:69] offset:0 sc1\n" \
  "global_load_lds_dwordx4 v0, s[68:69] offset:1024 sc1\n" \
  "global_load_lds_dwordx4 v0, s[68:69] offset:2048 sc1\n" \
  "global_load_lds_dwordx4 v0, s[68:69] offset:3072 sc1\n" \
  "s_add_u32 m0, s61, 4096\n" \
  "s_add_u32 s70, s68, 4096\n" \
  "s_addc_u32 s71, s69, 0\n" \
  "global_load_lds_dwordx4 v0, s[70:71] offset:0 sc1\n" \
  "global_load_lds_dwordx4 v0, s[70:71] offset:1024 sc1\n" \
  "global_load_lds_dwordx4 v0, s[70:71] offset:2048 sc1\n" \
  "global_load_lds_dwordx4 v0, s[70:71] offset:3072 sc1\n" \
  "s_mov_b32 m0, s62\n" \
  "s_lshl_b32 s72, s58, 18\n" \
  "s_add_u32 s72, s72, s50\n" \
  "s_addc_u32 s73, s51, 0\n" \
  "global_load_lds_dwordx4 v0, s[72:73] offset:0 sc1\n" \
  "global_load_lds_dwordx4 v0, s[72:73] offset:1024 sc1\n" \
  "global_load_lds_dwordx4 v0, s[72:73] offset:2048 sc1\n" \
  "global_load_lds_dwordx4 v0, s[72:73] offset:3072 sc1\n" \
  "s_mov_b32 s67, 1\n" \
  "s_mov_b32 s77, 1\n" \
  ".LWB_NOF_%=:\n" \
  "ds_read_b128 v[48:51], v1 offset:32768\n" \
  "ds_read_b128 v[52:55], v1 offset:33792\n" \
  "ds_read_b128 v[56:59], v1 offset:34816\n" \
  "s_waitcnt lgkmcnt(2)\n" \
  "v_mfma_f32_16x16x32_bf16 v[64:67], v[128:131], v[48:51], 0\n" \
  "ds_read_b128 v[60:63], v1 offset:35840\n" \
  "s_lshl_b32 s75, s57, 19\n" \
  "v_mfma_f32_16x16x32_bf16 v[68:71], v[160:163], v[48:51], 0\n" \
  "s_add_u32 s75, s75, s60\n" \
  "v_mfma_f32_16x16x32_bf16 v[72:75], v[192:195], v[48:51], 0\n" \
  "s_add_u32 s76, s75, 16384\n" \
  "v_mfma_f32_16x16x32_bf16 v[76:79], v[224:227], v[48:51], 0\n" \
  "s_lshl_b32 s70, s67, 7\n" \
  "s_or_b32 s70, s70, s58\n" \
  "s_lshr_b32 s68, s70, 6\n" \
  "s_add_u32 s68, s68, s70\n" \
  "s_lshl_b32 s68, s68, 12\n" \
  "buffer_load_dword v5, off, s[44:47], s68 sc1\n" \
  "s_waitcnt lgkmcnt(2)\n" \
  "v_mfma_f32_16x16x32_bf16 v[64:67], v[132:135], v[52:55], v[64:67]\n" \
  "ds_read_b128 v[48:51], v1 offset:40960\n" \
  "v_mfma_f32_16x16x32_bf16 v[68:71], v[164:167], v[52:55], v[68:71]\n" \
  "v_mfma_f32_16x16x32_bf16 v[72:75], v[196:199], v[52:55], v[72:75]\n" \
  "v_mfma_f32_16x16x32_bf16 v[76:79], v[228:231], v[52:55], v[76:79]\n" \
  "s_waitcnt lgkmcnt(2)\n" \
  "v_mfma_f32_16x16x32_bf16 v[64:67], v[136:139], v[56:59], v[64:67]\n" \
  "ds_read_b128 v[52:55], v1 offset:41984\n" \
  "v_mfma_f32_16x16x32_bf16 v[68:71], v[168:171], v[56:59], v[68:71]\n" \
  "v_mfma_f32_16x16x32_bf16 v[72:75], v[200:203], v[56:59], v[72:75]\n" \
  "v_mfma_f32_16x16x32_bf16 v[76:79], v[232:235], v[56:59], v[76:79]\n" \
  "s_waitcnt lgkmcnt(2)\n" \
  "v_mfma_f32_16x16x32_bf16 v[64:67], v[140:143], v[60:63], v[64:67]\n" \
  "ds_read_b128 v[56:59], v1 offset:43008\n" \
  "v_mfma_f32_16x16x32_bf16 v[68:71], v[172:175], v[60:63], v[68:71]\n" \
  "v_mfma_f32_16x16x32_bf16 v[72:75], v[204:207], v[60:63], v[72:75]\n" \
  "s_cmp_lg_u32 s67, 0\n" \
  "s_cbranch_scc1 .LWB_NOG_%=\n" \
  "s_waitcnt vmcnt(0)\n" \
  "v_cmp_eq_u32 vcc, 0, v5\n" \
  "s_cmp_eq_u64 vcc, 0\n" \
  "s_cbranch_scc0 .LWB_NOG_%=\n" \
  "s_mov_b32 m0, s61\n" \
  "s_lshl_b32 s68, s58, 19\n" \
  "s_add_u32 s68, s68, s48\n" \
  "s_addc_u32 s69, s49, 0\n" \
  "global_load_lds_dwordx4 v0, s[68:69] offset:0 sc1\n" \
  "global_load_lds_dwordx4 v0, s[68:69] offset:1024 sc1\n" \
  "global_load_lds_dwordx4 v0, s[68:69] offset:2048 sc1\n" \
  "global_load_lds_dwordx4 v0, s[68:69] offset:3072 sc1\n" \
  "s_add_u32 m0, s61, 4096\n" \
  "s_add_u32 s70, s68, 4096\n" \
  "s_addc_u32 s71, s69, 0\n" \
  "global_load_lds_dwordx4 v0, s[70:71] offset:0 sc1\n" \
  "global_load_lds_dwordx4 v0, s[70:71] offset:1024 sc1\n" \
  "global_load_lds_dwordx4 v0, s[70:71] offset:2048 sc1\n" \
  "global_load_lds_dwordx4 v0, s[70:71] offset:3072 sc1\n" \
  "s_mov_b32 m0, s62\n" \
  "s_lshl_b32 s72, s58, 18\n" \
  "s_add_u32 s72, s72, s50\n" \
  "s_addc_u32 s73, s51, 0\n" \
  "global_load_lds_dwordx4 v0, s[72:73] offset:0 sc1\n" \
  "global_load_lds_dwordx4 v0, s[72:73] offset:1024 sc1\n" \
  "global_load_lds_dwordx4 v0, s[72:73] offset:2048 sc1\n" \
  "global_load_lds_dwordx4 v0, s[72:73] offset:3072 sc1\n" \
  "s_mov_b32 s67, 1\n" \
  "s_mov_b32 s77, 1\n" \
  ".LWB_NOG_%=:\n" \
  "v_mfma_f32_16x16x32_bf16 v[76:79], v[236:239], v[60:63], v[76:79]\n" \
  "s_waitcnt lgkmcnt(2)\n" \
  "v_mfma_f32_16x16x32_bf16 v[64:67], v[144:147], v[48:51], v[64:67]\n" \
  "ds_read_b128 v[60:63], v1 offset:44032\n" \
  "v_mfma_f32_16x16x32_bf16 v[68:71], v[176:179], v[48:51], v[68:71]\n" \
  "v_mfma_f32_16x16x32_bf16 v[72:75], v[208:211], v[48:51], v[72:75]\n" \
  "v_mfma_f32_16x16x32_bf16 v[76:79], v[240:243], v[48:51], v[76:79]\n" \
  "s_waitcnt lgkmcnt(2)\n" \
  "v_mfma_f32_16x16x32_bf16 v[64:67], v[148:151], v[52:55], v[64:67]\n" \
  "ds_read_b128 v[48:51], v1 offset:36864\n" \
  "v_mfma_f32_16x16x32_bf16 v[68:71], v[180:183], v[52:55], v[68:71]\n" \
  "v_mfma_f32_16x16x32_bf16 v[72:75], v[212:215], v[52:55], v[72:75]\n" \
  "v_mfma_f32_16x16x32_bf16 v[76:79], v[244:247], v[52:55], v[76:79]\n" \
  "s_waitcnt lgkmcnt(2)\n" \
  "v_mfma_f32_16x16x32_bf16 v[64:67], v[152:155], v[56:59], v[64:67]\n" \
  "ds_read_b128 v[52:55], v1 offset:37888\n" \
  "v_mfma_f32_16x16x32_bf16 v[68:71], v[184:187], v[56:59], v[68:71]\n" \
  "v_mfma_f32_16x16x32_bf16 v[72:75], v[216:219], v[56:59], v[72:75]\n" \
  "v_mfma_f32_16x16x32_bf16 v[76:79], v[248:251], v[56:59], v[76:79]\n" \
  "s_waitcnt lgkmcnt(2)\n" \
  "v_mfma_f32_16x16x32_bf16 v[64:67], v[156:159], v[60:63], v[64:67]\n" \
  "ds_read_b128 v[56:59], v1 offset:38912\n" \
  "v_mfma_f32_16x16x32_bf16 v[68:71], v[188:191], v[60:63], v[68:71]\n" \
  "v_mfma_f32_16x16x32_bf16 v[72:75], v[220:223], v[60:63], v[72:75]\n" \
  "v_mfma_f32_16x16x32_bf16 v[76:79], v[252:255], v[60:63], v[76:79]\n" \
  "s_waitcnt lgkmcnt(2)\n" \
  "v_mfma_f32_16x16x32_bf16 v[80:83], v[128:131], v[48:51], 0\n" \
  "ds_read_b128 v[60:63], v1 offset:39936\n" \
  "v_mfma_f32_16x16x32_bf16 v[84:87], v[160:163], v[48:51], 0\n" \
  "v_mfma_f32_16x16x32_bf16 v[88:91], v[192:195], v[48:51], 0\n" \
  "s_waitcnt vmcnt(0)\n" \
  "v_mov_b32 v12, s65\n" \
  "v_mov_b32 v13, s77\n" \
  "s_mov_b64 exec, 1\n" \
  "ds_add_u32 v12, v13\n" \
  "s_mov_b64 exec, -1\n" \
  "v_mfma_f32_16x16x32_bf16 v[92:95], v[224:227], v[48:51], 0\n" \
  "s_lshl_b32 s70, s67, 7\n" \
  "s_or_b32 s70, s70, s58\n" \
  "s_lshr_b32 s68, s70, 6\n" \
  "s_add_u32 s68, s68, s70\n" \
  "s_lshl_b32 s68, s68, 12\n" \
  "buffer_load_dword v5, off, s[44:47], s68 sc1\n" \
  "s_waitcnt lgkmcnt(3)\n" \
  "v_mfma_f32_16x16x32_bf16 v[80:83], v[132:135], v[52:55], v[80:83]\n" \
  "ds_read_b128 v[48:51], v1 offset:45056\n" \
  "v_mfma_f32_16x16x32_bf16 v[84:87], v[164:167], v[52:55], v[84:87]\n" \
  "v_cvt_pk_bf16_f32 v64, v64, v65\n" \
  "v_mfma_f32_16x16x32_bf16 v[88:91], v[196:199], v[52:55], v[88:91]\n" \
  "v_cvt_pk_bf16_f32 v65, v66, v67\n" \
  "v_mfma_f32_16x16x32_bf16 v[92:95], v[228:231], v[52:55], v[92:95]\n" \
  "s_waitcnt lgkmcnt(3)\n" \
  "v_mfma_f32_16x16x32_bf16 v[80:83], v[136:139], v[56:59], v[80:83]\n" \
  "ds_read_b128 v[52:55], v1 offset:46080\n" \
  "v_mfma_f32_16x16x32_bf16 v[84:87], v[168:171], v[56:59], v[84:87]\n" \
  "v_cvt_pk_bf16_f32 v66, v68, v69\n" \
  "v_mfma_f32_16x16x32_bf16 v[88:91], v[200:203], v[56:59], v[88:91]\n" \
  "v_cvt_pk_bf16_f32 v67, v70, v71\n" \
  "v_mfma_f32_16x16x32_bf16 v[92:95], v[232:235], v[56:59], v[92:95]\n" \
  "s_waitcnt lgkmcnt(3)\n" \
  "v_mfma_f32_16x16x32_bf16 v[80:83], v[140:143], v[60:63], v[80:83]\n" \
  "ds_read_b128 v[56:59], v1 offset:47104\n" \
  "v_mfma_f32_16x16x32_bf16 v[84:87], v[172:175], v[60:63], v[84:87]\n" \
  "buffer_store_dwordx4 v[64:67], v2, s[40:43], s75 offen offset:0 sc1\n" \
  "v_mfma_f32_16x16x32_bf16 v[88:91], v[204:207], v[60:63], v[88:91]\n" \
  "v_cvt_pk_bf16_f32 v72, v72, v73\n" \
  "v_mfma_f32_16x16x32_bf16 v[92:95], v[236:239], v[60:63], v[92:95]\n" \
  "s_waitcnt lgkmcnt(2)\n" \
  "v_mfma_f32_16x16x32_bf16 v[80:83], v[144:147], v[48:51], v[80:83]\n" \
  "ds_read_b128 v[60:63], v1 offset:48128\n" \
  "v_mfma_f32_16x16x32_bf16 v[84:87], v[176:179], v[48:51], v[84:87]\n" \
  "v_cvt_pk_bf16_f32 v73, v74, v75\n" \
  "v_mfma_f32_16x16x32_bf16 v[88:91], v[208:211], v[48:51], v[88:91]\n" \
  "v_cvt_pk_bf16_f32 v74, v76, v77\n" \
  "v_mfma_f32_16x16x32_bf16 v[92:95], v[240:243], v[48:51], v[92:95]\n" \
  "s_waitcnt lgkmcnt(2)\n" \
  "v_mfma_f32_16x16x32_bf16 v[80:83], v[148:151], v[52:55], v[80:83]\n" \
  "v_mfma_f32_16x16x32_bf16 v[84:87], v[180:183], v[52:55], v[84:87]\n" \
  "v_cvt_pk_bf16_f32 v75, v78, v79\n" \
  "v_mfma_f32_16x16x32_bf16 v[88:91], v[212:215], v[52:55], v[88:91]\n" \
  "buffer_store_dwordx4 v[72:75], v2, s[40:43], s75 offen offset:128 sc1\n" \
  "v_mfma_f32_16x16x32_bf16 v[92:95], v[244:247], v[52:55], v[92:95]\n" \
  "s_waitcnt lgkmcnt(1)\n" \
  "v_mfma_f32_16x16x32_bf16 v[80:83], v[152:155], v[56:59], v[80:83]\n" \
  "v_mfma_f32_16x16x32_bf16 v[84:87], v[184:187], v[56:59], v[84:87]\n" \
  "v_mfma_f32_16x16x32_bf16 v[88:91], v[216:219], v[56:59], v[88:91]\n" \
  "v_mfma_f32_16x16x32_bf16 v[92:95], v[248:251], v[56:59], v[92:95]\n" \
  "s_waitcnt lgkmcnt(0)\n" \
  "v_mfma_f32_16x16x32_bf16 v[80:83], v[156:159], v[60:63], v[80:83]\n" \
  "v_mfma_f32_16x16x32_bf16 v[84:87], v[188:191], v[60:63], v[84:87]\n" \
  "v_mfma_f32_16x16x32_bf16 v[88:91], v[220:223], v[60:63], v[88:91]\n" \
  "v_mfma_f32_16x16x32_bf16 v[92:95], v[252:255], v[60:63], v[92:95]\n" \
  "s_nop 4\n" \
  "v_cvt_pk_bf16_f32 v80, v80, v81\n" \
  "v_cvt_pk_bf16_f32 v81, v82, v83\n" \
  "v_cvt_pk_bf16_f32 v82, v84, v85\n" \
  "v_cvt_pk_bf16_f32 v83, v86, v87\n" \
  "buffer_store_dwordx4 v[80:83], v2, s[40:43], s76 offen offset:0 sc1\n" \
  "v_cvt_pk_bf16_f32 v88, v88, v89\n" \
  "v_cvt_pk_bf16_f32 v89, v90, v91\n" \
  "v_cvt_pk_bf16_f32 v90, v92, v93\n" \
  "v_cvt_pk_bf16_f32 v91, v94, v95\n" \
  "buffer_store_dwordx4 v[88:91], v2, s[40:43], s76 offen offset:128 sc1\n" \
  "s_mov_b32 s79, s57\n" \
  "s_waitcnt vmcnt(4)\n" \
  "v_cmp_eq_u32 vcc, 0, v5\n" \
  "s_cmp_eq_u64 vcc, 0\n" \
  "s_cbranch_scc0 .LWB_NEXT_%=\n" \
  "s_mov_b32 s57, s58\n" \
  "s_setprio 0\n" \
  "s_branch .LWB_SEEN_%=\n"
#define CONV_ASM \
  "v_mov_b64 v[64:65], 0\n" \
  "v_mov_b64 v[66:67], 0\n" \
  "v_mov_b64 v[68:69], 0\n" \
  "v_mov_b64 v[70:71], 0\n" \
  "v_mov_b64 v[72:73], 0\n" \
  "v_mov_b64 v[74:75], 0\n" \
  "v_mov_b64 v[76:77], 0\n" \
  "v_mov_b64 v[78:79], 0\n" \
  "v_mov_b64 v[80:81], 0\n" \
  "v_mov_b64 v[82:83], 0\n" \
  "v_mov_b64 v[84:85], 0\n" \
  "v_mov_b64 v[86:87], 0\n" \
  "v_mov_b64 v[88:89], 0\n" \
  "v_mov_b64 v[90:91], 0\n" \
  "v_mov_b64 v[92:93], 0\n" \
  "v_mov_b64 v[94:95], 0\n" \
  "v_mov_b64 v[96:97], 0\n" \
  "v_mov_b64 v[98:99], 0\n" \
  "v_mov_b64 v[100:101], 0\n" \
  "v_mov_b64 v[102:103], 0\n" \
  "v_mov_b64 v[104:105], 0\n" \
  "v_mov_b64 v[106:107], 0\n" \
  "v_mov_b64 v[108:109], 0\n" \
  "v_mov_b64 v[110:111], 0\n" \
  "v_mov_b64 v[112:113], 0\n" \
  "v_mov_b64 v[114:115], 0\n" \
  "v_mov_b64 v[116:117], 0\n" \
  "v_mov_b64 v[118:119], 0\n" \
  "v_mov_b64 v[120:121], 0\n" \
  "v_mov_b64 v[122:123], 0\n" \
  "v_mov_b64 v[124:125], 0\n" \
  "v_mov_b64 v[126:127], 0\n" \
  "v_mov_b32 v20, 0\n" \
  "v_mov_b32 v41, 0\n" \
  "v_mov_b32 v29, 0\n" \
  "s_branch .LVA_TAPS_%=\n" \
  "s_branch .LVA_AL_%=\n" \
  ".p2alignl 12, 0xbf800000, 3700\n" \
  ".p2align 6\n" \
  ".Lpad0xb0:\n" \
  "s_setpc_b64 s[98:99]\n" \
  ".p2align 6\n" \
  ".LVA_AL_%=:\n" \
  ".LVA_TOP_%=:\n" \
  "s_cmp_lg_u32 s63, 1\n" \
  "s_cbranch_scc1 .LVA_BAR_%=\n" \
  "s_waitcnt vmcnt(0)\n" \
  "v_min3_u32 v56, v42, v43, v41\n" \
  "v_readfirstlane_b32 s78, v29\n" \
  "v_cmp_eq_u32 vcc, 0, v56\n" \
  "s_cbranch_vccz .LVA_SEEN_%=\n" \
  "s_mov_b32 s79, 0\n" \
  "s_cmp_lg_u32 s78, 0\n" \
  "s_cbranch_scc1 .LVA_KNOWN_%=\n" \
  ".LVA_GATE_%=:\n" \
  "s_memrealtime s[74:75]\n" \
  "s_waitcnt lgkmcnt(0)\n" \
  "s_sub_i32 s74, s69, s74\n" \
  "s_cmp_gt_u32 s74, %[gv]\n" \
  "s_cbranch_scc1 .LVA_WHEN_%=\n" \
  "s_sleep 2\n" \
  "s_branch .LVA_GATE_%=\n" \
  ".LVA_NONE_%=:\n" \
  "s_sleep %[sv]\n" \
  ".LVA_WHEN_%=:\n" \
  "buffer_load_dword v29, off, s[44:47], s71 sc1\n" \
  "s_add_u32 s79, s79, 1\n" \
  "s_cmp_gt_u32 s79, 0x40000\n" \
  "s_cbranch_scc1 .LVA_KNOWN_%=\n" \
  "s_waitcnt vmcnt(0)\n" \
  "v_readfirstlane_b32 s78, v29\n" \
  "s_cmp_eq_u32 s78, 0\n" \
  "s_cbranch_scc1 .LVA_NONE_%=\n" \
  ".LVA_KNOWN_%=:\n" \
  "s_memrealtime s[74:75]\n" \
  "s_waitcnt lgkmcnt(0)\n" \
  "s_sub_i32 s74, s78, s74\n" \
  "s_cmp_lt_i32 s74, %[lt]\n" \
  "s_cselect_b32 s70, 0, 0x200000\n" \
  "s_branch .LVA_DUE_%=\n" \
  ".LVA_SOON_%=:\n" \
  "s_cmp_lt_u32 s74, 8\n" \
  "s_cbranch_scc1 .LVA_NOW_%=\n" \
  "s_sleep 1\n" \
  ".LVA_NOW_%=:\n" \
  "s_memrealtime s[74:75]\n" \
  "s_waitcnt lgkmcnt(0)\n" \
  "s_sub_i32 s74, s78, s74\n" \
  ".LVA_DUE_%=:\n" \
  "s_cmp_lt_u32 s74, 1000\n" \
  "s_cbranch_scc1 .LVA_SOON_%=\n" \
  "s_branch .LVA_ASK_%=\n" \
  ".LVA_AGAIN_%=:\n" \
  "s_sleep %[sv]\n" \
  ".LVA_ASK_%=:\n" \
  "buffer_load_dwordx2 v[42:43], v37, s[40:43], s65 offen sc1\n" \
  "buffer_load_dword v41, v38, s[44:47], s66 offen sc1\n" \
  "s_add_u32 s79, s79, 1\n" \
  "s_cmp_gt_u32 s79, 0x40000\n" \
  "s_cbranch_scc1 .LVA_SEEN_%=\n" \
  "s_waitcnt vmcnt(0)\n" \
  "v_min3_u32 v56, v42, v43, v41\n" \
  "v_cmp_eq_u32 vcc, 0, v56\n" \
  "s_cbranch_vccnz .LVA_AGAIN_%=\n" \
  ".LVA_SEEN_%=:\n" \
  "v_and_b32 v62, 0xffff0000, v41\n" \
  "v_lshlrev_b32 v60, 16, v41\n" \
  "v_lshlrev_b32 v56, 16, v42\n" \
  "v_and_b32 v57, 0xffff0000, v42\n" \
  "v_lshlrev_b32 v58, 16, v43\n" \
  "v_and_b32 v59, 0xffff0000, v43\n" \
  "v_rcp_f32 v62, v62\n" \
  "v_pk_add_f32 v[56:57], v[56:57], v[60:61] op_sel_hi:[1,0] neg_lo:[0,1] neg_hi:[0,1]\n" \
  "v_pk_add_f32 v[58:59], v[58:59], v[60:61] op_sel_hi:[1,0] neg_lo:[0,1] neg_hi:[0,1]\n" \
  "v_cvt_pk_bf16_f32 v57, v56, v57\n" \
  "v_cvt_pk_bf16_f32 v59, v58, v59\n" \
  "v_lshlrev_b32 v56, 16, v57\n" \
  "v_and_b32 v57, 0xffff0000, v57\n" \
  "v_lshlrev_b32 v58, 16, v59\n" \
  "v_and_b32 v59, 0xffff0000, v59\n" \
  "v_pk_mul_f32 v[56:57], v[56:57], v[62:63] op_sel_hi:[1,0]\n" \
  "v_pk_mul_f32 v[58:59], v[58:59], v[62:63] op_sel_hi:[1,0]\n" \
  "v_cvt_pk_bf16_f32 v56, v56, v57\n" \
  "v_cvt_pk_bf16_f32 v58, v58, v59\n" \
  "ds_write_b16 v39, v56\n" \
  "ds_write_b16_d16_hi v39, v56 offset:4288\n" \
  "ds_write_b16 v39, v58 offset:8576\n" \
  "ds_write_b16_d16_hi v39, v58 offset:12864\n" \
  "ds_write_b64 v40, v[42:43] offset:512\n" \
  ".LVA_BAR_%=:\n" \
  "s_waitcnt lgkmcnt(0)\n" \
  "s_barrier\n" \
  "ds_read_b128 v[56:59], v0 offset:0\n" \
  "ds_read_u16 v60, v2 offset:0\n" \
  "ds_read_u16 v61, v3 offset:512\n" \
  "ds_read2_b32 v[244:245], v4 offset0:0 offset1:1\n" \
  "ds_read2_b32 v[246:247], v4 offset0:2 offset1:3\n" \
  "ds_read_b32 v248, v4 offset:16\n" \
  "ds_read2_b32 v[250:251], v4 offset0:16 offset1:17\n" \
  "ds_read2_b32 v[252:253], v4 offset0:18 offset1:19\n" \
  "ds_read_b32 v254, v4 offset:80\n" \
  "s_setprio 3\n" \
  "s_waitcnt lgkmcnt(8)\n" \
  "v_mfma_f32_16x16x32_bf16 v[20:23], v[56:59], v[12:15], v[20:23]\n" \
  "v_mfma_f32_16x16x32_bf16 v[20:23], v[56:59], v[16:19], v[20:23]\n" \
  "s_waitcnt lgkmcnt(6)\n" \
  "v_lshlrev_b32 v60, 16, v60\n" \
  "v_lshlrev_b32 v61, 16, v61\n" \
  "v_mul_f32 v60, s64, v60\n" \
  "v_add_u32 v0, v1, v0\n" \
  "v_add_u32 v2, 64, v2\n" \
  "v_add_u32 v4, 64, v4\n" \
  "s_nop 0\n" \
  "v_cvt_pk_bf16_f32 v62, v20, v60\n" \
  "v_lshlrev_b32 v63, 16, v62\n" \
  "v_and_b32 v62, 0xffff0000, v62\n" \
  "v_add_f32 v62, v62, v63\n" \
  "v_cvt_pk_bf16_f32 v62, v62, v62\n" \
  "v_lshlrev_b32 v62, 16, v62\n" \
  "v_add_f32 v62, v62, v61\n" \
  "v_cvt_pk_bf16_f32 v62, v62, v62\n" \
  "s_mov_b64 exec, s[72:73]\n" \
  "ds_write_b16 v3, v62\n" \
  "s_mov_b64 exec, -1\n" \
  "s_cmp_gt_u32 s63, 1\n" \
  "s_waitcnt lgkmcnt(0)\n" \
  "s_barrier\n" \
  "s_cbranch_scc1 .LVA_REST_%=\n" \
  "s_cmp_eq_u32 s63, 1\n" \
  "s_cbranch_scc1 .LVA_ONE_%=\n" \
  "ds_read_b128 v[56:59], v40 offset:0\n" \
  "s_mov_b32 exec_hi, 0\n" \
  "s_waitcnt lgkmcnt(0)\n" \
  "buffer_store_dwordx4 v[56:59], v7, s[48:51], s67 offen sc1\n" \
  "s_mov_b32 exec_hi, -1\n" \
  "s_add_u32 s67, s67, 0x8000\n" \
  "s_branch .LVA_GO_%=\n" \
  ".LVA_ONE_%=:\n" \
  "s_add_u32 s65, s65, 0x8000\n" \
  "s_add_u32 s66, s66, 2048\n" \
  "s_add_u32 s71, s71, 4\n" \
  "s_max_u32 s76, s65, s70\n" \
  "s_max_u32 s78, s66, s70\n" \
  "buffer_load_dword v29, off, s[44:47], s71 sc1\n" \
  "buffer_load_dwordx2 v[42:43], v37, s[40:43], s76 offen sc1\n" \
  "buffer_load_dword v41, v38, s[44:47], s78 offen sc1\n" \
  "s_memrealtime s[74:75]\n" \
  "v_add_u32 v39, 64, v39\n" \
  "s_waitcnt lgkmcnt(0)\n" \
  "s_add_u32 s69, s74, %[gv]\n" \
  "s_branch .LVA_GO_%=\n" \
  ".LVA_REST_%=:\n" \
  "s_setprio 0\n" \
  ".LVA_GO_%=:\n" \
  "ds_read2_b32 v[58:59], v28 offset0:0 offset1:1\n" \
  "ds_read2_b32 v[60:61], v28 offset0:2 offset1:3\n" \
  "ds_read_b32 v62, v28 offset:16\n" \
  "v_alignbit_b32 v244, v245, v244, v6\n" \
  "v_alignbit_b32 v245, v246, v245, v6\n" \
  "v_alignbit_b32 v246, v247, v246, v6\n" \
  "v_alignbit_b32 v247, v248, v247, v6\n" \
  "v_alignbit_b32 v250, v251, v250, v6\n" \
  "v_alignbit_b32 v251, v252, v251, v6\n" \
  "v_alignbit_b32 v252, v253, v252, v6\n" \
  "v_alignbit_b32 v253, v254, v253, v6\n" \
  "v_and_b32 v250, v250, v8\n" \
  "v_and_b32 v251, v251, v9\n" \
  "v_and_b32 v252, v252, v10\n" \
  "v_and_b32 v253, v253, v11\n" \
  "s_bitcmp1_b32 s62, 1\n" \
  "s_cbranch_scc1 .LVA_QH_%=\n" \
  "s_bitcmp1_b32 s62, 0\n" \
  "s_cbranch_scc1 .LVA_QB_%=\n" \
  ".LVA_QA_%=:\n" \
  "v_mfma_f32_16x16x32_bf16 v[64:67], v[128:131], v[244:247], v[68:71]\n" \
  "v_mfma_f32_16x16x32_bf16 v[68:71], v[132:135], v[244:247], v[72:75]\n" \
  "v_mfma_f32_16x16x32_bf16 v[72:75], v[136:139], v[244:247], v[76:79]\n" \
  "v_mfma_f32_16x16x32_bf16 v[76:79], v[140:143], v[244:247], v[80:83]\n" \
  "v_mfma_f32_16x16x32_bf16 v[20:23], v[192:195], v[250:253], v[64:67]\n" \
  "s_nop 7\n" \
  "v_permlane32_swap_b32 v22, v23\n" \
  "v_add_f32 v20, v22, v23\n" \
  "s_cmp_lg_u32 s62, 0\n" \
  "s_cbranch_scc1 .LVA_MOVE_%=\n" \
  "s_branch .LVA_FAR_%=\n" \
  ".Lpad0xb1:\n" \
  "s_setpc_b64 s[98:99]\n" \
  ".LVA_QB_%=:\n" \
  "v_mfma_f32_16x16x32_bf16 v[64:67], v[128:131], v[244:247], v[64:67]\n" \
  "v_mfma_f32_16x16x32_bf16 v[68:71], v[132:135], v[244:247], v[68:71]\n" \
  "v_mfma_f32_16x16x32_bf16 v[72:75], v[136:139], v[244:247], v[72:75]\n" \
  "v_mfma_f32_16x16x32_bf16 v[76:79], v[140:143], v[244:247], v[76:79]\n" \
  "v_mfma_f32_16x16x32_bf16 v[20:23], v[192:195], v[250:253], v[64:67]\n" \
  "s_not_b64 s[72:73], s[72:73]\n" \
  "s_nop 6\n" \
  "v_permlane32_swap_b32 v20, v21\n" \
  "v_add_f32 v20, v20, v21\n" \
  "s_branch .LVA_FAR_%=\n" \
  ".LVA_QH_%=:\n" \
  "s_bitcmp1_b32 s62, 0\n" \
  "s_cbranch_scc1 .LVA_QD_%=\n" \
  ".LVA_QC_%=:\n" \
  "v_mfma_f32_16x16x32_bf16 v[64:67], v[128:131], v[244:247], v[64:67]\n" \
  "v_mfma_f32_16x16x32_bf16 v[68:71], v[132:135], v[244:247], v[68:71]\n" \
  "v_mfma_f32_16x16x32_bf16 v[72:75], v[136:139], v[244:247], v[72:75]\n" \
  "v_mfma_f32_16x16x32_bf16 v[76:79], v[140:143], v[244:247], v[76:79]\n" \
  "v_mfma_f32_16x16x32_bf16 v[20:23], v[192:195], v[250:253], v[64:67]\n" \
  "s_nop 7\n" \
  "v_permlane32_swap_b32 v22, v23\n" \
  "v_add_f32 v20, v22, v23\n" \
  "s_branch .LVA_FAR_%=\n" \
  ".LVA_QD_%=:\n" \
  "v_mfma_f32_16x16x32_bf16 v[64:67], v[128:131], v[244:247], v[64:67]\n" \
  "v_mfma_f32_16x16x32_bf16 v[68:71], v[132:135], v[244:247], v[68:71]\n" \
  "v_mfma_f32_16x16x32_bf16 v[72:75], v[136:139], v[244:247], v[72:75]\n" \
  "v_mfma_f32_16x16x32_bf16 v[76:79], v[140:143], v[244:247], v[76:79]\n" \
  "v_mfma_f32_16x16x32_bf16 v[20:23], v[192:195], v[250:253], v[68:71]\n" \
  "s_not_b64 s[72:73], s[72:73]\n" \
  "s_nop 6\n" \
  "v_permlane32_swap_b32 v20, v21\n" \
  "v_add_f32 v20, v20, v21\n" \
  ".LVA_FAR_%=:\n" \
  "s_waitcnt lgkmcnt(0)\n" \
  "v_alignbit_b32 v58, v59, v58, v6\n" \
  "v_alignbit_b32 v59, v60, v59, v6\n" \
  "v_alignbit_b32 v60, v61, v60, v6\n" \
  "v_alignbit_b32 v61, v62, v61, v6\n" \
  "v_add_u32 v28, 64, v28\n" \
  "s_cmp_ge_u32 s80, 12\n" \
  "s_cbranch_scc1 .LVA_PD_%=\n" \
  "v_mfma_f32_16x16x32_bf16 v[80:83], v[144:147], v[244:247], v[80:83]\n" \
  "v_mfma_f32_16x16x32_bf16 v[196:199], v[128:131], v[58:61], v[196:199]\n" \
  "s_cmp_ge_u32 s80, 11\n" \
  "s_cbranch_scc1 .LVA_PD_%=\n" \
  "v_mfma_f32_16x16x32_bf16 v[84:87], v[148:151], v[244:247], v[84:87]\n" \
  "v_mfma_f32_16x16x32_bf16 v[200:203], v[132:135], v[58:61], v[200:203]\n" \
  "s_cmp_ge_u32 s80, 10\n" \
  "s_cbranch_scc1 .LVA_PD_%=\n" \
  "v_mfma_f32_16x16x32_bf16 v[88:91], v[152:155], v[244:247], v[88:91]\n" \
  "v_mfma_f32_16x16x32_bf16 v[204:207], v[136:139], v[58:61], v[204:207]\n" \
  "s_cmp_ge_u32 s80, 9\n" \
  "s_cbranch_scc1 .LVA_PD_%=\n" \
  "v_mfma_f32_16x16x32_bf16 v[92:95], v[156:159], v[244:247], v[92:95]\n" \
  "v_mfma_f32_16x16x32_bf16 v[208:211], v[140:143], v[58:61], v[208:211]\n" \
  "s_cmp_ge_u32 s80, 8\n" \
  "s_cbranch_scc1 .LVA_PD_%=\n" \
  "v_mfma_f32_16x16x32_bf16 v[96:99], v[160:163], v[244:247], v[96:99]\n" \
  "v_mfma_f32_16x16x32_bf16 v[212:215], v[144:147], v[58:61], v[212:215]\n" \
  "s_cmp_ge_u32 s80, 7\n" \
  "s_cbranch_scc1 .LVA_PD_%=\n" \
  "v_mfma_f32_16x16x32_bf16 v[100:103], v[164:167], v[244:247], v[100:103]\n" \
  "v_mfma_f32_16x16x32_bf16 v[216:219], v[148:151], v[58:61], v[216:219]\n" \
  "s_cmp_ge_u32 s80, 6\n" \
  "s_cbranch_scc1 .LVA_PD_%=\n" \
  "v_mfma_f32_16x16x32_bf16 v[104:107], v[168:171], v[244:247], v[104:107]\n" \
  "v_mfma_f32_16x16x32_bf16 v[220:223], v[152:155], v[58:61], v[220:223]\n" \
  "s_cmp_ge_u32 s80, 5\n" \
  "s_cbranch_scc1 .LVA_PD_%=\n" \
  "v_mfma_f32_16x16x32_bf16 v[108:111], v[172:175], v[244:247], v[108:111]\n" \
  "v_mfma_f32_16x16x32_bf16 v[224:227], v[156:159], v[58:61], v[224:227]\n" \
  "s_cmp_ge_u32 s80, 4\n" \
  "s_cbranch_scc1 .LVA_PD_%=\n" \
  "v_mfma_f32_16x16x32_bf16 v[112:115], v[176:179], v[244:247], v[112:115]\n" \
  "v_mfma_f32_16x16x32_bf16 v[228:231], v[160:163], v[58:61], v[228:231]\n" \
  "s_cmp_ge_u32 s80, 3\n" \
  "s_cbranch_scc1 .LVA_PD_%=\n" \
  "v_mfma_f32_16x16x32_bf16 v[116:119], v[180:183], v[244:247], v[116:119]\n" \
  "v_mfma_f32_16x16x32_bf16 v[232:235], v[164:167], v[58:61], v[232:235]\n" \
  "s_cmp_ge_u32 s80, 2\n" \
  "s_cbranch_scc1 .LVA_PD_%=\n" \
  "v_mfma_f32_16x16x32_bf16 v[120:123], v[184:187], v[244:247], v[120:123]\n" \
  "v_mfma_f32_16x16x32_bf16 v[236:239], v[168:171], v[58:61], v[236:239]\n" \
  "s_cmp_ge_u32 s80, 1\n" \
  "s_cbranch_scc1 .LVA_PD_%=\n" \
  "v_mfma_f32_16x16x32_bf16 v[124:127], v[188:191], v[244:247], v[124:127]\n" \
  "v_mfma_f32_16x16x32_bf16 v[240:243], v[172:175], v[58:61], v[240:243]\n" \
  ".LVA_PD_%=:\n" \
  "s_cmp_lg_u32 s63, 0\n" \
  "s_cbranch_scc1 .LVA_NOF_%=\n" \
  "v_lshlrev_b32 v56, 6, v7\n" \
  "s_waitcnt vmcnt(0)\n" \
  "s_mov_b64 exec, 15\n" \
  "global_store_dword v56, v5, s[58:59] sc1\n" \
  "s_mov_b64 exec, -1\n" \
  "s_add_u32 s58, s58, 4096\n" \
  "s_addc_u32 s59, s59, 0\n" \
  ".LVA_NOF_%=:\n" \
  "s_add_u32 s74, s62, 12\n" \
  "s_lshl_b32 s76, s63, 1\n" \
  "s_and_b32 s76, s76, 12\n" \
  "s_xor_b32 s74, s74, s76\n" \
  "s_and_b32 s79, s74, 15\n" \
  "s_sub_u32 s74, s62, 4\n" \
  "s_cmp_lt_u32 s74, 48\n" \
  "s_cselect_b32 s79, s79, 1\n" \
  "s_cmp_lg_u32 s79, 0\n" \
  "s_cbranch_scc1 .LVA_NOS_%=\n" \
  "ds_read_u16 v128, v35 offset:0\n" \
  "ds_read_u16 v129, v35 offset:784\n" \
  "ds_read_u16 v130, v35 offset:1568\n" \
  "ds_read_u16 v131, v35 offset:2352\n" \
  "ds_read_u16 v132, v35 offset:3136\n" \
  "ds_read_u16 v133, v35 offset:3920\n" \
  "ds_read_u16 v134, v35 offset:4704\n" \
  "ds_read_u16 v135, v35 offset:5488\n" \
  "v_lshlrev_b32 v136, 16, v44\n" \
  "v_and_b32 v137, 0xffff0000, v44\n" \
  "v_lshlrev_b32 v138, 16, v45\n" \
  "v_and_b32 v139, 0xffff0000, v45\n" \
  "v_lshlrev_b32 v140, 16, v46\n" \
  "v_and_b32 v141, 0xffff0000, v46\n" \
  "v_lshlrev_b32 v142, 16, v47\n" \
  "v_and_b32 v143, 0xffff0000, v47\n" \
  "v_mov_b64 v[44:45], v[48:49]\n" \
  "v_mov_b64 v[46:47], v[50:51]\n" \
  "v_mov_b64 v[48:49], v[52:53]\n" \
  "v_mov_b64 v[50:51], v[54:55]\n" \
  "s_waitcnt lgkmcnt(0)\n" \
  "v_lshlrev_b32 v128, 16, v128\n" \
  "v_lshlrev_b32 v129, 16, v129\n" \
  "v_lshlrev_b32 v130, 16, v130\n" \
  "v_lshlrev_b32 v131, 16, v131\n" \
  "v_lshlrev_b32 v132, 16, v132\n" \
  "v_lshlrev_b32 v133, 16, v133\n" \
  "v_lshlrev_b32 v134, 16, v134\n" \
  "v_lshlrev_b32 v135, 16, v135\n" \
  "v_pk_add_f32 v[128:129], v[128:129], v[136:137]\n" \
  "v_pk_add_f32 v[130:131], v[130:131], v[138:139]\n" \
  "v_pk_add_f32 v[132:133], v[132:133], v[140:141]\n" \
  "v_pk_add_f32 v[134:135], v[134:135], v[142:143]\n" \
  "v_cvt_pk_bf16_f32 v56, v128, v129\n" \
  "v_cvt_pk_bf16_f32 v57, v130, v131\n" \
  "v_cvt_pk_bf16_f32 v58, v132, v133\n" \
  "v_cvt_pk_bf16_f32 v59, v134, v135\n" \
  "buffer_store_dwordx4 v[56:59], v36, s[52:55], s68 offen sc1\n" \
  "s_add_u32 s68, s68, 0x80000\n" \
  "s_mov_b32 s0, 0xbe801d62\n" \
  ".set .Lpad0xb2, . - 4\n" \
  ".LVA_NOS_%=:\n" \
  "s_and_b32 s74, s62, 3\n" \
  "s_cmp_lt_u32 s62, 48\n" \
  "s_cselect_b32 s74, s74, 0\n" \
  "s_cmp_lg_u32 s74, 3\n" \
  "s_cbranch_scc1 .LVA_FIN_%=\n" \
  "ds_read2_b32 v[58:59], v28 offset0:0 offset1:1\n" \
  "ds_read2_b32 v[60:61], v28 offset0:2 offset1:3\n" \
  "ds_read_b32 v62, v28 offset:16\n" \
  "ds_read_u16 v56, v33 offset:0\n" \
  "ds_read_u16 v57, v33 offset:64\n" \
  "v_add_u32 v33, 256, v33\n" \
  "s_mov_b32 s0, 0xbe801d62\n" \
  ".set .Lpad0xb3, . - 4\n" \
  "s_waitcnt lgkmcnt(2)\n" \
  "v_alignbit_b32 v58, v59, v58, v6\n" \
  "v_alignbit_b32 v59, v60, v59, v6\n" \
  "v_alignbit_b32 v60, v61, v60, v6\n" \
  "v_alignbit_b32 v61, v62, v61, v6\n" \
  "s_nop 1\n" \
  "v_mfma_f32_16x16x32_bf16 v[196:199], v[24:27], v[58:61], v[196:199]\n" \
  "s_nop 7\n" \
  "v_permlane32_swap_b32 v196, v197\n" \
  "v_permlane32_swap_b32 v198, v199\n" \
  "v_add_f32 v196, v196, v197\n" \
  "v_add_f32 v198, v198, v199\n" \
  "s_waitcnt lgkmcnt(0)\n" \
  "v_lshlrev_b32 v56, 16, v56\n" \
  "v_lshlrev_b32 v57, 16, v57\n" \
  "v_mul_f32 v56, s64, v56\n" \
  "v_mul_f32 v57, s64, v57\n" \
  "v_cvt_pk_bf16_f32 v197, v196, v198\n" \
  "v_cvt_pk_bf16_f32 v57, v56, v57\n" \
  "v_lshlrev_b32 v196, 16, v197\n" \
  "v_and_b32 v197, 0xffff0000, v197\n" \
  "v_lshlrev_b32 v56, 16, v57\n" \
  "v_and_b32 v57, 0xffff0000, v57\n" \
  "v_add_f32 v56, v56, v196\n" \
  "v_add_f32 v57, v57, v197\n" \
  "v_cvt_pk_bf16_f32 v58, v56, v57\n" \
  "ds_write_b16 v34, v58\n" \
  "ds_write_b16_d16_hi v34, v58 offset:64\n" \
  ".LVA_FIN_%=:\n" \
  "s_cmp_lg_u32 s79, 0\n" \
  "s_cbranch_scc1 .LVA_NOG_%=\n" \
  "v_lshlrev_b32 v60, 6, v7\n" \
  "s_waitcnt vmcnt(0)\n" \
  "s_mov_b64 exec, 255\n" \
  "global_store_dword v60, v5, s[60:61] sc1\n" \
  "s_mov_b64 exec, -1\n" \
  "s_add_u32 s60, s60, 0x10000\n" \
  "s_addc_u32 s61, s61, 0\n" \
  ".LVA_NOG_%=:\n" \
  "s_add_u32 s62, s62, 1\n" \
  "s_cmp_eq_u32 s62, 64\n" \
  "s_cbranch_scc1 .LVA_END_%=\n" \
  ".LVA_TAPS_%=:\n" \
  "s_and_b32 s74, s62, 3\n" \
  "s_lshl_b32 s76, s74, 6\n" \
  "v_add_u32 v56, s76, v5\n" \
  "s_cmp_eq_u32 s74, 3\n" \
  "s_cselect_b32 s74, 0, 256\n" \
  "v_add_u32 v57, s74, v56\n" \
  "ds_read_b128 v[128:131], v56 offset:3840\n" \
  "ds_read_b128 v[132:135], v56 offset:3584\n" \
  "ds_read_b128 v[136:139], v56 offset:3328\n" \
  "ds_read_b128 v[140:143], v56 offset:3072\n" \
  "ds_read_b128 v[192:195], v57 offset:3648\n" \
  "s_lshr_b32 s80, s62, 2\n" \
  "s_cmp_ge_u32 s80, 12\n" \
  "s_cbranch_scc1 .LVA_TOP_%=\n" \
  "ds_read_b128 v[144:147], v56 offset:2816\n" \
  "s_cmp_ge_u32 s80, 11\n" \
  "s_cbranch_scc1 .LVA_TOP_%=\n" \
  "ds_read_b128 v[148:151], v56 offset:2560\n" \
  "s_cmp_ge_u32 s80, 10\n" \
  "s_cbranch_scc1 .LVA_TOP_%=\n" \
  "ds_read_b128 v[152:155], v56 offset:2304\n" \
  "s_cmp_ge_u32 s80, 9\n" \
  "s_cbranch_scc1 .LVA_TOP_%=\n" \
  "ds_read_b128 v[156:159], v56 offset:2048\n" \
  "s_cmp_ge_u32 s80, 8\n" \
  "s_cbranch_scc1 .LVA_TOP_%=\n" \
  "ds_read_b128 v[160:163], v56 offset:1792\n" \
  "s_cmp_ge_u32 s80, 7\n" \
  "s_cbranch_scc1 .LVA_TOP_%=\n" \
  "ds_read_b128 v[164:167], v56 offset:1536\n" \
  "s_cmp_ge_u32 s80, 6\n" \
  "s_cbranch_scc1 .LVA_TOP_%=\n" \
  "ds_read_b128 v[168:171], v56 offset:1280\n" \
  "s_cmp_ge_u32 s80, 5\n" \
  "s_cbranch_scc1 .LVA_TOP_%=\n" \
  "ds_read_b128 v[172:175], v56 offset:1024\n" \
  "s_cmp_ge_u32 s80, 4\n" \
  "s_cbranch_scc1 .LVA_TOP_%=\n" \
  "ds_read_b128 v[176:179], v56 offset:768\n" \
  "s_cmp_ge_u32 s80, 3\n" \
  "s_cbranch_scc1 .LVA_TOP_%=\n" \
  "ds_read_b128 v[180:183], v56 offset:512\n" \
  "s_cmp_ge_u32 s80, 2\n" \
  "s_cbranch_scc1 .LVA_TOP_%=\n" \
  "ds_read_b128 v[184:187], v56 offset:256\n" \
  "s_cmp_ge_u32 s80, 1\n" \
  "s_cbranch_scc1 .LVA_TOP_%=\n" \
  "ds_read_b128 v[188:191], v56 offset:0\n" \
  "s_branch .LVA_TOP_%=\n" \
  ".LVA_MOVE_%=:\n" \
  "s_waitcnt lgkmcnt(0)\n" \
  "v_alignbit_b32 v58, v59, v58, v6\n" \
  "v_alignbit_b32 v59, v60, v59, v6\n" \
  "v_alignbit_b32 v60, v61, v60, v6\n" \
  "v_alignbit_b32 v61, v62, v61, v6\n" \
  "v_add_u32 v28, 64, v28\n" \
  "s_cmp_ge_u32 s80, 12\n" \
  "s_cbranch_scc1 .LVA_PD_%=\n" \
  "v_mfma_f32_16x16x32_bf16 v[80:83], v[144:147], v[244:247], v[84:87]\n" \
  "v_mfma_f32_16x16x32_bf16 v[196:199], v[128:131], v[58:61], v[200:203]\n" \
  "s_cmp_ge_u32 s80, 11\n" \
  "s_cbranch_scc1 .LVA_PD_%=\n" \
  "v_mfma_f32_16x16x32_bf16 v[84:87], v[148:151], v[244:247], v[88:91]\n" \
  "v_mfma_f32_16x16x32_bf16 v[200:203], v[132:135], v[58:61], v[204:207]\n" \
  "s_cmp_ge_u32 s80, 10\n" \
  "s_cbranch_scc1 .LVA_PD_%=\n" \
  "v_mfma_f32_16x16x32_bf16 v[88:91], v[152:155], v[244:247], v[92:95]\n" \
  "v_mfma_f32_16x16x32_bf16 v[204:207], v[136:139], v[58:61], v[208:211]\n" \
  "s_cmp_ge_u32 s80, 9\n" \
  "s_cbranch_scc1 .LVA_PD_%=\n" \
  "v_mfma_f32_16x16x32_bf16 v[92:95], v[156:159], v[244:247], v[96:99]\n" \
  "v_mfma_f32_16x16x32_bf16 v[208:211], v[140:143], v[58:61], v[212:215]\n" \
  "s_cmp_ge_u32 s80, 8\n" \
  "s_cbranch_scc1 .LVA_PD_%=\n" \
  "v_mfma_f32_16x16x32_bf16 v[96:99], v[160:163], v[244:247], v[100:103]\n" \
  "v_mfma_f32_16x16x32_bf16 v[212:215], v[144:147], v[58:61], v[216:219]\n" \
  "s_cmp_ge_u32 s80, 7\n" \
  "s_cbranch_scc1 .LVA_PD_%=\n" \
  "v_mfma_f32_16x16x32_bf16 v[100:103], v[164:167], v[244:247], v[104:107]\n" \
  "v_mfma_f32_16x16x32_bf16 v[216:219], v[148:151], v[58:61], v[220:223]\n" \
  "s_cmp_ge_u32 s80, 6\n" \
  "s_cbranch_scc1 .LVA_PD_%=\n" \
  "v_mfma_f32_16x16x32_bf16 v[104:107], v[168:171], v[244:247], v[108:111]\n" \
  "v_mfma_f32_16x16x32_bf16 v[220:223], v[152:155], v[58:61], v[224:227]\n" \
  "s_cmp_ge_u32 s80, 5\n" \
  "s_cbranch_scc1 .LVA_PD_%=\n" \
  "v_mfma_f32_16x16x32_bf16 v[108:111], v[172:175], v[244:247], v[112:115]\n" \
  "v_mfma_f32_16x16x32_bf16 v[224:227], v[156:159], v[58:61], v[228:231]\n" \
  "s_cmp_ge_u32 s80, 4\n" \
  "s_cbranch_scc1 .LVA_PD_%=\n" \
  "v_mfma_f32_16x16x32_bf16 v[112:115], v[176:179], v[244:247], v[116:119]\n" \
  "v_mfma_f32_16x16x32_bf16 v[228:231], v[160:163], v[58:61], v[232:235]\n" \
  "s_cmp_ge_u32 s80, 3\n" \
  "s_cbranch_scc1 .LVA_PD_%=\n" \
  "v_mfma_f32_16x16x32_bf16 v[116:119], v[180:183], v[244:247], v[120:123]\n" \
  "v_mfma_f32_16x16x32_bf16 v[232:235], v[164:167], v[58:61], v[236:239]\n" \
  "s_cmp_ge_u32 s80, 2\n" \
  "s_cbranch_scc1 .LVA_PD_%=\n" \
  "v_mfma_f32_16x16x32_bf16 v[120:123], v[184:187], v[244:247], v[124:127]\n" \
  "v_mfma_f32_16x16x32_bf16 v[236:239], v[168:171], v[58:61], v[240:243]\n" \
  "s_branch .LVA_PD_%=\n" \
  ".LVA_END_%=:\n" \
  "s_endpgm\n"
__device__ __forceinline__ v4u mk_srd(const void* p, const unsigned bytes) {
  const unsigned long long a = (unsigned long long)p;
  return v4u{(unsigned)a, (unsigned)(a >> 32) & 0xffffu, bytes, 0x00020000u};
}
template <bool ATT>
__device__ __forceinline__ void role_chain_t(const MegaArgs& A, char* smem, const int L, const int h, const int rep) {
  __builtin_amdgcn_s_waitcnt(0x0F70);
  MARK();
  u16* swo = reinterpret_cast<u16*>(smem);
  const int w = __builtin_amdgcn_readfirstlane(threadIdx.x >> 6);
  const int wc = (w + ROT) & 7, psw = PSWH;
  char* ws = A.ws;
  unsigned* FL = (unsigned*)(ws + OFF_FL);
  unsigned* TMO = FL + FL_TMO;
#define LANE_IDS()                         \
  const int l = lane_id();                 \
  const int c = l & 15, g = l >> 4;        \
  (void)c; (void)g;
  float* sgm = reinterpret_cast<float*>(smem + C_X);
  float* sgf = sgm + 8;
  unsigned* FQ = FL + FL_FQ + L * 65 * 32 * 32;
  v8u w0[8], w1[8], w2[8];
  PSV;
  {
    LANE_IDS();
    v4u m0r;
    const unsigned sb = lds_base(smem);
    const int q = (l >> 2) & 3, p = l & 3;
    const int f = (q & 1) | ((g & 1) << 1) | ((q >> 1) << 2);
    const unsigned ra = sb + (g >> 1) * 16384 + (4 * (g & 1) + (q & 1) + 8 * (q >> 1)) * 256 + (w ^ f) * 32 + p * 8;
#define WP(k) (WM(k) == 0 ? A.Wv : WM(k) == 1 ? A.Wk : A.Wq)
#define WC(k) (WJ(k) ^ WXC)
#define W_READ(W, X)                                                          \
  {                                                                           \
    const unsigned a_ = (ra ^ (X)) + j * 4096 + i * 32768;                    \
    const v2u x0 = trd(a_), x1 = trd(a_ + 512);                               \
    W[4 * WC(k) + i] = v4u{x0.x, x0.y, x1.x, x1.y};                           \
  }
    v4u u0[16], u1[16], u2[16], zt;
    v2u ze;
    float f2[4] = {};
    unsigned txt = 0u;
    if (CSL && L) gate(now10() + CSL);
    PST(0);
    asm volatile("s_nop 4\n\tglobal_load_dwordx4 %0, %1, %2" : "=v"(m0r) : "v"(((2 * (c >> 2) + (c & 1)) * DM + 64 * w + 16 * g + 8 * (((c >> 1) ^ (c >> 3)) & 1)) * 2), "s"(A.mem0) : "memory");
#pragma unroll
    for (int k = 0; k < 12; ++k) w_issue<true>(WP(k >> 2), WC(k >> 2), k >> 2, k & 3, h, w, l, sb);
    constexpr int PB = ATT ? 100 : 120;
    tour<PB + 3, PB + 12, 4>(w);
#pragma unroll
    for (int k = 0; k < 13; ++k) {
      const int j = k & 3;
      if (k == (ATT ? 2 : 4)) pad<PB + 3>(txt);
      if (k == (ATT ? 4 : 7)) pad<PB + 4>(txt);
      if (k == (ATT ? 6 : 10)) pad<PB + 1, 600>(txt);
      if (k == (ATT ? 10 : 9)) pad<PB + 5>(txt);
      if (k == ZC + 1) wait_vm<10>();
      else if (k < 12 || ZC == 10) wait_vm<8>();
      if (CTH && L) __builtin_amdgcn_s_sleep(CTH);
      RAW_BAR();
      if (k == 0) {
        asm volatile("" : "+v"(m0r));
        *reinterpret_cast<v4u*>(smem + C_SM + w * 1024 + l * 16) = m0r;
      }
      if (k == IPC) txt = pull_text((h & 7) + 8 * w);
      if (k == IPC + 2) asm volatile("" ::"v"(txt));
      if (k == ZC)
        asm volatile("s_nop 4\n\tglobal_load_dwordx4 %0, %2, %3 sc1\n\tglobal_load_dwordx2 %1, %4, %5 sc1"
                     : "=&v"(zt), "=&v"(ze)
                     : "v"(l * 16), "s"(ws + OFF_TAG + (blockIdx.x & 7) * 1024), "v"(0), "s"(ws + OFF_TAG + 8192)
                     : "memory");
#pragma unroll
      for (int i = 0; i < 4; ++i) {
        if (k < 9) w_issue<true>(WP(k + 3), WC(k + 3), (k + 3) & 3, i, h, w, l, sb);
        else {
          const int jo = k - 9, fd = (i & 1) | ((jo & 1) << 1) | ((w & 1) << 2);
          glds16((const char*)A.Wo + (size_t)(h * DH + 64 * (jo >> 1) + 8 * w + 4 * (jo & 1)) * (DM * 2), (unsigned)(i * 1024 + ((l ^ (fd << 1)) << 4)),
                 sb + w * 16384 + jo * 4096 + i * 1024);
        }
        if (k < 12 && WM(k) == (ATT ? 0 : 2)) { W_READ(u1, 0); W_READ(u2, 128); }
        if (k < 12 && WM(k) == 1) W_READ(u0, 0);
        if (ATT && k > 0 && WM(k - 1) == 0) {
#pragma unroll
          for (int e = 0; e < 4; ++e) {
            f2[e & 1] = dot2sq(u1[4 * WC(k - 1) + i][e], f2[e & 1]);
            f2[2 + (e & 1)] = dot2sq(u2[4 * WC(k - 1) + i][e], f2[2 + (e & 1)]);
          }
          asm volatile("" : "+v"(f2[0]), "+v"(f2[1]), "+v"(f2[2]), "+v"(f2[3]));
        }
      }
      if (k == (WORD ? 10 : 4)) {
        const float f = wave_sum_dpp((f2[0] + f2[1]) + (f2[2] + f2[3]));
        if (l == 0) { sgf[w] = f; sgm[16] = 0.f; sgm[32 + w] = 1.f; }
      }
      if (k == (WORD ? 11 : ZC + 2)) {
        asm volatile("" : "+v"(zt), "+v"(ze));
        if (w == 0) {
          if (__builtin_expect(!zero_seen(zt, ze), 0)) zero_wait(ws);
          const float fs = (sgf[0] + sgf[1]) + (sgf[2] + sgf[3]);
          __builtin_amdgcn_raw_buffer_store_b32(__float_as_uint(fs) | 1u, MKRS(FL + FL_G, L ? 0 : 4 * 512), l * 512 + h * 4, 0, AUX_SC1);
        }
      }
      if (k == 1) PST(1);
      if (k == 3) PST(2);
      if (k == 5) PST(3);
      if (k == 7) PST(4);
      if (k == 9) PST(5);
      if (k == 11) PST(6);
    }
#undef W_READ
#undef WP
#undef WC
#pragma unroll
    for (int t = 0; t < 8; ++t) {
      const v8u a0 = __builtin_shufflevector(u0[2 * t], u0[2 * t + 1], 0, 1, 2, 3, 4, 5, 6, 7), a1 = __builtin_shufflevector(u1[2 * t], u1[2 * t + 1], 0, 1, 2, 3, 4, 5, 6, 7);
      w0[t] = ATT ? a0 : a1;
      w1[t] = ATT ? a1 : a0;
      w2[t] = __builtin_shufflevector(u2[2 * t], u2[2 * t + 1], 0, 1, 2, 3, 4, 5, 6, 7);
    }
    const unsigned rb = sb + (2 * (g >> 1) + (q >> 1)) * 16384 + (4 * (g & 1) + (q & 1)) * 1024 + p * 8;
#pragma unroll
    for (int hf = 0; hf < 2; ++hf) {
      if (hf == 0) WAIT_VM(8); else WAIT_VM(0);
      if (hf == 1) pad<PB + 6>();
      RAW_BAR();
      if (hf == 1 && w == 0) __builtin_amdgcn_raw_buffer_store_b32((now10() + CRD) | 1u, MKRS(FL + FL_S0 + L * 512, 4 * 512), l * 512 + (h >> 3) * 128 + 32 + (h & 7) * 4, 0, AUX_SC1);
      v4u fr[8];
#pragma unroll
      for (int i = 0; i < 8; ++i) {
        const unsigned a_ = rb + hf * 8192 + (i & 1) * 65536 + ((4 * wc + ((i >> 1) ^ psw)) ^ f) * 32;
        const v2u r0 = trd(a_), r1 = trd(a_ + 2048);
        fr[i] = v4u{r0.x, r0.y, r1.x, r1.y};
      }
#pragma unroll
      for (int i = 0; i < 8; ++i) asm volatile("" : "+v"(fr[i]));
      RAW_BAR();
#pragma unroll
      for (int i = 0; i < 8; ++i) *reinterpret_cast<v4u*>(swo + w * 8192 + hf * 4096 + i * 512 + l * 8) = fr[i];
    }
  }
  PST(7);
  WSTAMP2();
  const unsigned sb = lds_base(smem);
  const char* gK = ws + OFF_QK + L * SZ_QK + ((size_t)h * 16 + 2 * w) * 1024;
  const char* gV = ws + OFF_VF + L * SZ_O + ((size_t)h * 8 + 2 * w - 8) * 1024;
  PST(8);
  pad<ATT ? 108 : 128>();
  v8u pv;
  {
    LANE_IDS();
    const unsigned voff = g * 256 + c * 8, wp = sb + w * 16384 + l * 16;
    pv[0] = (((c >> 1) ^ (c >> 3)) & 1) ? 0xEEEE : 0x4444;
    pv[1] = sb + C_SM + l * 16;
    pv[2] = ATT ? sb + ((c & 2) ? C_SK : C_SQ) + ((2 * (c >> 2) + (c & 1)) * QS + 8 * g) * 2 : voff;
    pv[3] = ATT ? l * 8 : wp;
    pv[4] = sb + ((c & 2) ? C_SQ : C_SO) + w * 256 + ((c & 13) + ((g ^ (c >> 2)) & 2)) * 16 + 8 * ((g ^ (c >> 1)) & 1);
    pv[5] = voff;
    pv[6] = wp;
    pv[7] = sb + C_SO + (l >> 5) * 1024 + w * 256 + (l & 31) * 8;
  }
  PST(10);
  PSD();
  SST(8);
  const v4u rsA = mk_srd(ws + OFF_ACC + L * SZ_ACC, SZ_ACC);
  const v4u rsF = mk_srd(FQ, 64 * 32 * 32 * 4);
  const v4u rsMK = mk_srd(ws + OFF_MEMK + L * SZ_MEM, SZ_MEM);
  const v4u rsMV = mk_srd(ATT ? ws + OFF_MEMV + L * SZ_MEM : (char*)(FL + FL_G + REP * 128), SZ_MEM);
  const int wr = (w + RR) & 7;
  const char* pAB = ws + OFF_ACC + L * SZ_ACC + (wc - wr) * 4096;
  const char* pGV = ATT ? (const char*)(size_t)(CDS + (XG(h) >> 1) * CDL) : gV;
  unsigned sc[8];
  sc[0] = ATT ? L * 1024 + XG(h) * 256 : h * 128;
  sc[1] = !ATT ? (h * 4 + w - 4) * 512 : (h & 7) == 1 ? L * 1024 + (h >> 3) * 256 + 128 : 1032 - L * 1024 + h * 4096;
  sc[2] = h * 2048 + w * 512;
  sc[3] = NSEG - 1;
  sc[5] = w;
  sc[6] = sb;
  const char* dbg = ws;
  const unsigned tg = (now10() / GR) * GR;
  const v4u rsT = mk_srd(FL + FL_TK, 2048);
#define CHAIN_OPS                                                                                                          \
  ::"{v[64:71]}"(w0[0]), "{v[72:79]}"(w0[1]), "{v[80:87]}"(w0[2]), "{v[88:95]}"(w0[3]), "{v[96:103]}"(w0[4]),              \
      "{v[104:111]}"(w0[5]), "{v[112:119]}"(w0[6]), "{v[120:127]}"(w0[7]), "{v[128:135]}"(w1[0]), "{v[136:143]}"(w1[1]),    \
      "{v[144:151]}"(w1[2]), "{v[152:159]}"(w1[3]), "{v[160:167]}"(w1[4]), "{v[168:175]}"(w1[5]), "{v[176:183]}"(w1[6]),    \
      "{v[184:191]}"(w1[7]), "{v[192:199]}"(w2[0]), "{v[200:207]}"(w2[1]), "{v[208:215]}"(w2[2]), "{v[216:223]}"(w2[3]),    \
      "{v[224:231]}"(w2[4]), "{v[232:239]}"(w2[5]), "{v[240:247]}"(w2[6]), "{v[248:255]}"(w2[7]), "{v[0:7]}"(pv),          \
      "{s[40:43]}"(rsA), "{s[44:47]}"(rsF), "{s[48:51]}"(rsMK), "{s[52:55]}"(rsMV), "{s[56:57]}"(pAB), "{s60}"(wr * 4096), "{s61}"(h * 1024 - wr * 4096), \
      "{s[62:63]}"(gK), "{s[64:65]}"(pGV), "{s68}"(sc[0]), "{s69}"(sc[1]), "{s70}"(sc[2]),    \
      "{s71}"(sc[3]), "{s73}"(sc[5]), "{s74}"(sc[6]), "{s[36:37]}"(dbg),                   \
      "{s34}"(tg), "{s35}"(psw * 1024), [gp] "n"(GP), [gr] "n"(GR), [gm] "n"(GM), \
      "{s[16:19]}"(rsT), "{s20}"(0), "{s22}"(sb + C_X + 128 + 8 * (w & 3)), "{s24}"(L * 1024), [cd] "n"(CD - CDS), \
      "{s25}"(APA - (APN + 1) * APP + L * (APB - APA + (2 * APN + 1) * APP)), [ka] "n"(APL), [an] "n"(APN), [d0] "n"(16 * CD0), [d1] "n"(CD1), [d2] "n"(CD2), [d3] "n"(CD1 + 63 * APP), [d4] "n"(CD4), [so] "n"((FL_S0 - FL_G) * 4), [sg] "n"(SLP_2), [g1] "n"(63 * APP + CG1 + CD2), [sf] "n"(SLP_1), [rq] "n"(RQM), [pp] "n"(APP) \
      : "memory", "vcc", "scc", "s99", "v63"
  if (ATT) asm volatile("s_mov_b64 exec, -1\ns_nop 4\n" CHAIN_ASM_ATT ".Lcode_end:\n" CHAIN_OPS);
  else asm volatile("s_mov_b64 exec, -1\ns_nop 4\n" CHAIN_ASM_VO CHAIN_OPS);
#undef CHAIN_OPS
#undef LANE_IDS
}

__device__ __forceinline__ void role_chain(const MegaArgs& A, char* smem, const int L, const int h, const int rep) {
  if (__builtin_amdgcn_readfirstlane(threadIdx.x >> 6) < 4) role_chain_t<true>(A, smem, L, h, rep);
  else role_chain_t<false>(A, smem, L, h, rep);
}

constexpr int AW_TK = 0, AW_SO = 114688, AW_M = AW_SO + 16384, AW_P = 24576, AW_MK = AW_P + 2048, AW_CNT = AW_SO + AW_MK + 4096;
__device__ __forceinline__ unsigned umin3(unsigned a, unsigned b, unsigned c) {
  return __builtin_elementwise_min(__builtin_elementwise_min(a, b), c);
}
__device__ __forceinline__ bool all_set(const unsigned r) {
  unsigned long long m;
  asm volatile("v_cmp_eq_u32 %0, 0, %1" : "=s"(m) : "v"(r) : "memory");
  return m == 0ull;
}
__device__ __forceinline__ unsigned long long zero_mask(const unsigned r) {
  unsigned long long m;
  asm volatile("v_cmp_eq_u32 %0, 0, %1" : "=s"(m) : "v"(r) : "memory");
  return m;
}
__device__ __forceinline__ int job_seg(const int n) {
  const int m = n - AWD;
  const int r = (m & 1) ? 64 + (m >> 1) : AWD + (m >> 1);
  return __builtin_amdgcn_readfirstlane(n > 127 ? 128 : (n < AWD || n >= 128 - AWD) ? n : r);
}
template <bool AT>
__device__ __forceinline__ void role_aw_t(const MegaArgs& A, char* smem, const int mode, const int a) {
  __builtin_amdgcn_s_waitcnt(0x0F70);
  MARK();
  const int w = __builtin_amdgcn_readfirstlane(threadIdx.x >> 6);
  const int l = threadIdx.x & 63, c = l & 15, g = l >> 4;
  const int qt = w & 1, hh = (w >> 1) & 1, h = 2 * a + hh;
  char* ws = A.ws;
  unsigned* FL = (unsigned*)(ws + OFF_FL);
  const unsigned sb = lds_base(smem);
  v4u wo[4][8];
  PSV;
  PST(0);
  const unsigned* FQ = FL + FL_FQ + 1024 + h * 32 + 16;
  const v4u rsW = mk_srd(ws + OFF_PW, SZ_PW);
  const v4u rsF = mk_srd(FQ, (129 * 32 - h) * 128 - 64);
  const char* gQ = ws + OFF_QK + ((size_t)h * 16 + 8 * qt) * 1024;
  const char* gV = ws + OFF_VF + ((size_t)h * 8 + 4 * qt) * 1024;
  const char* gM = ws + (qt ? OFF_MEMV : OFF_MEMK) + h * 2048;
  const unsigned mo = sb + AW_M + hh * 4096;
  const unsigned tq0 = __builtin_amdgcn_readfirstlane(sb + AW_TK + (hh * 24 + 8 * qt) * 1024);
  const unsigned tv0 = __builtin_amdgcn_readfirstlane(sb + AW_TK + (hh * 24 + 16 + 4 * qt) * 1024);
  const int gs = job_seg(mode);
  bool got = false;
  {
    const int q = (l >> 2) & 3, p = l & 3, f = q | ((g & 1) << 2);
    const unsigned ra = sb + (4 * g + q) * 256 + (p & 1) * 16;
    unsigned n0 = 0u;
#pragma unroll
    for (int i = 0; i < 8; ++i) wo_issue(A.Wo, a, 2 + (i >> 2), w, i & 3, sb);
#pragma unroll
    for (int ks = 0; ks < 8; ++ks) {
      if (ks >= 2 && ks <= 4) WAIT_VM(12);
      if (ks == 5) wait_vm<8 + (!AT && AWK < 5)>();
      if (ks == 6) wait_vm<4 + (!AT && AWK < 6)>();
      if (ks == 7) WAIT_VM(0);
      if (ks) RAW_BAR();
      if (!AT && ks == AWK)
        asm volatile("s_nop 4\n\tbuffer_load_dword %0, %1, %2, %3 offen sc1" : "=v"(n0) : "v"(0), "s"(rsF), "s"(gs * 4096) : "memory");
      if (ks == 5 && threadIdx.x < 4) ((lu32*)(unsigned long)(sb + AW_CNT))[threadIdx.x] = 0u;
      if (!AT && ks == 7 && __builtin_expect(all_set(n0), 1)) {
#pragma unroll
        for (int i = 0; i < 8; ++i) glds16c(gQ + (size_t)gs * (NH * 16 * 1024) + i * 1024, l * 16, tq0 + i * 1024);
#pragma unroll
        for (int i = 0; i < 4; ++i) glds16c(gV + (size_t)gs * (NH * 8 * 1024) + i * 1024, l * 16, tv0 + i * 1024);
        got = true;
      }
#pragma unroll
      for (int ct = 0; ct < 4; ++ct) {
        if (ks < 4) wo_issue(A.Wo, a, ks + 4, w, ct, sb);
        const int cp = 2 * w + (ct >> 1);
        const unsigned a_ = ra + (ks % 5) * 32768 + (cp >> 2) * 8192 + ((2 * (cp & 3) + (p >> 1)) ^ f) * 32 + (ct & 1) * 8;
        const v2u lo = trd(a_), hi = trd(a_ + 4096);
        wo[ct][ks] = v4u{lo.x, hi.x, lo.y, hi.y};
      }
#pragma unroll
      for (int ct = 0; ct < 4; ++ct) asm volatile("" : "+v"(wo[ct][ks]));
      if (ks == 1) PST(1); if (ks == 3) PST(2); if (ks == 5) PST(3); if (ks == 7) PST(4);
    }
    RAW_BAR();
  }
  if (got) PST(11);
  PSD();
  if (AT) tour<220, 232, 4>(w);
  v8u pv, mk, wr[16];
  {
    const unsigned so = sb + AW_SO + l * 16, ta = sb + AW_TK + hh * 24576 + l * 16;
    const unsigned ix = (c & 2) ? 0xEEEE : 0x4444;
    pv = v8u{AT ? ix : l * 16u, so, (unsigned)(HK3 ? (l >> 3) * 2048 + (l & 7) * 16 : (c >> 1) * 2048 + (c & 1) * 64 + g * 16), AT ? ta : mo + qt * 2048 + l * 16, AT ? ta + qt * 4096 : ix,
             mo + (g * 8 + 2 * (c >> 2) + (c & 1)) * 16, sb + AW_M + 2048 + (g >> 1) * 512 + (c & 3) * 128 + (8 * (g & 1) + (c >> 2)) * 8,
             so + (hh * 2 + qt) * 4096};
#pragma unroll
    for (int i = 0; i < 8; ++i) mk[i] = 16 * (i >> 2) + 4 * g + (i & 3) > 16 * qt + c ? __float_as_uint(-3e38f) : 0u;
#pragma unroll
    for (int i = 0; i < 16; ++i) wr[i] = __builtin_shufflevector(wo[i >> 2][2 * (i & 3)], wo[i >> 2][2 * (i & 3) + 1], 0, 1, 2, 3, 4, 5, 6, 7);
  }
  const char* dbg = ws;
#define AW_OPS                                                                                                               \
  ::"{v[128:135]}"(wr[0]), "{v[136:143]}"(wr[1]), "{v[144:151]}"(wr[2]), "{v[152:159]}"(wr[3]), "{v[160:167]}"(wr[4]),        \
      "{v[168:175]}"(wr[5]), "{v[176:183]}"(wr[6]), "{v[184:191]}"(wr[7]), "{v[192:199]}"(wr[8]), "{v[200:207]}"(wr[9]),      \
      "{v[208:215]}"(wr[10]), "{v[216:223]}"(wr[11]), "{v[224:231]}"(wr[12]), "{v[232:239]}"(wr[13]), "{v[240:247]}"(wr[14]), \
      "{v[248:255]}"(wr[15]), "{v[0:7]}"(pv), "{v[8:15]}"(mk), "{s[40:43]}"(rsW), "{s[44:47]}"(rsF), "{s[48:49]}"(gQ),        \
      "{s[50:51]}"(gV), "{s[52:53]}"(gM), "{s[54:55]}"(FL + FL_FW + (a >> 2) * 32 + (a & 3)), "{s56}"(mode), "{s57}"(gs),      \
      "{s60}"(HK2 ? (a >> 1) * 32768 + w * 256 + (a & 1) * 128 : a * 32768 + w * 256), "{s[92:95]}"(mk_srd(ws, 0)), "{s61}"(AT ? AW_MK + qt * 2048 : tq0), "{s64}"(AT ? 0 : sb + AW_CNT + 8), "{s65}"(sb + AW_CNT + hh * 4), "{s62}"(tv0), "{s63}"(AT ? mo + 2048 - (sb + AW_SO) / 2 : mo + qt * 2048), "{s[84:87]}"(mk_srd(FL + FL_TK + (a >> 2) * 64 + 32, 1028)), "{s[88:89]}"(ws + WS_TOTAL + (28 << 20) + 1024 + qt * 4),            \
      "{s66}"(AT ? AW_P + qt * 1024 + hh * 4 : HK2 ? (a >> 1) * 32768 + (w - 4) * 256 + (a & 1) * 128 : a * 32768 + (w - 4) * 8192), "{s[82:83]}"(w == 4 ? 15ull : 0ull), "{s67}"(__builtin_amdgcn_readfirstlane((int)got)), "{s80}"(0x3e0293ee), "{s[36:37]}"(dbg),        \
      [wd] "n"(AWD), [w2] "n"(128 - 2 * AWD), [w3] "n"(131 - AWD), [dt] "n"(AWDT), [du] "n"(AWDU), [pp] "n"(APP), [mo] "n"(AWMO - 63 * APP), [lm] "n"(63 * APP - AWLM), [ld] "n"(AWLD), [sm] "n"(SLP_M), [sa] "n"(SLP_A) \
      : "memory", "vcc", "scc", "s81", "v127"
  if (AT) asm volatile("s_mov_b64 exec, -1\ns_nop 4\n" AW_ASM_0 AW_OPS);
  else asm volatile("s_mov_b64 exec, -1\ns_nop 4\n" AW_ASM_1 AW_OPS);
#undef AW_OPS
}
__device__ __forceinline__ void role_aw(const MegaArgs& A, char* smem, const int L, const int a) {
  if (__builtin_amdgcn_readfirstlane(threadIdx.x >> 6) < 4) role_aw_t<true>(A, smem, L, a);
  else role_aw_t<false>(A, smem, L, a);
}

__device__ __forceinline__ void role_red(const MegaArgs& A, char* smem, const int idx) {
  const int w = __builtin_amdgcn_readfirstlane(threadIdx.x >> 6);
  const int l = threadIdx.x & 63;
  const int L = w >> 2, gr = (w >> 1) & 1, r = idx * 2 + (w & 1);
  char* ws = A.ws;
  unsigned* FL = (unsigned*)(ws + OFF_FL);
  unsigned* TMO = FL + FL_TMO;
  const __amdgpu_buffer_rsrc_t rsW = MKRS(ws + OFF_PW, SZ_PW);
  const __amdgpu_buffer_rsrc_t rsX = MKRS(ws + OFF_XA, OFF_X3 - OFF_XA + SZ_X);
  volatile lu32* sm32 = (lu32*)(unsigned long)lds_base(smem);
  volatile lu32* sw = sm32 + (w >> 1) * 32;
  if (threadIdx.x < 128) sm32[threadIdx.x] = 0u;
  __syncthreads();
  unsigned tb = now10() + (L * (APN + 2) + 1 + gr) * GPER;
  const int rn1 = idx < 8 ? 96 + idx : 224 + idx;
  (void)rn1;
  if (L) {
    if (RSL) gate(now10() + RSL);
    const __amdgpu_buffer_rsrc_t rz = MKRS(ws + OFF_X2, SZ_X);
    for (int s = gr; s < NSEG; s += 2) ST128((v4u{0u, 0u, 0u, 0u}), rz, l * 16, (s * SEG + r) * DM * 2);
    DRAIN();
    if (l < 4) set_flag(FL + FL_FZ + (2 * l + (idx >> 3)) * 32 + (idx & 7) * 4 + (w & 3));
    if (l == 0) __hip_atomic_fetch_add((lu32*)(unsigned long)(lds_base(smem) + 480), 1u, __ATOMIC_RELAXED, __HIP_MEMORY_SCOPE_WORKGROUP);
  }
  for (unsigned spins = 0; sm32[120] < 4u && spins < 64u * SPIN_CAP; ++spins) __builtin_amdgcn_s_sleep(2);
  const int c = l & 15, g = l >> 4;
  const int pv = g * 32768 + (c >> 2) * 128 + (c & 3) * 16, xv = (16 * (g >> 1) + c) * 512 + (g & 1) * 8, ov = (g >> 1) * 256 + c * 16 + (g & 1) * 8;
  v4u sel0, sel1;
#pragma unroll
  for (int e = 0; e < 4; ++e) {
    const unsigned one = ((c & 7) >> 1) == e ? 0x3f80u << (16 * (c & 1)) : 0u;
    sel0[e] = c < 8 ? one : 0u;
    sel1[e] = c < 8 ? 0u : one;
  }
  unsigned long long en = __hip_atomic_load((gu64*)(ws + OFF_TAG + 8192), RLX_AGENT);
  asm volatile("" : "+v"(en));
  int tries = SPIN_CAP;
  tour<232, 234, 2>(w);
  for (int s = gr; s < NSEG; s += 2) {
    const int gs = 64 * L + s;
    const unsigned* fw = FL + FL_FW + (gs * 4 + REP) * 4 * 32;
    const unsigned due = grp_wait<SLP_R, 4>((w & 1) == 0, sw, s + 1, fw, 16, TMO, tb);
    tb = due + (gs >= 125 - AWD || gs < RQ0 ? GATE_RT : 2 * GPER - (L ? GMS : gs == 61 ? GMR + AWLM : GMR));
    {
      const unsigned t = now10(), e = (int)(due - t) > 0 ? due : t;
      __builtin_amdgcn_raw_buffer_store_b32((e + RDT) | 1u, MKRS(FL + FL_TW, r == 2 * RTW && !L ? 2048 : 0), l * 512, s * 4, AUX_SC1);
    }
#define RTS(k, t) TSR(L ? A.rn : rn1, k, s, (w & 6) * 64 + (t))
    pad<232>();
    RST(0);
    RTS(0, 0);
    RTS(4, 64);
    const int xo_ = L * (int)(OFF_X3 - OFF_XA) + s * 32768 + r * 16;
    const v2u xa = LD64(rsX, xv, xo_), xb = LD64(rsX, xv + 16384, xo_);
#pragma unroll 1
    for (int i = 0; i < 256; ++i) {
      const int d = (int)(due - now10());
      if (d <= 0) break;
      if (d > 8) __builtin_amdgcn_s_sleep(1);
    }
    if (L) {
      const unsigned g0 = sm32[124];
#pragma unroll 1
      for (int i = 0; i < RGW && g0 < NSEG && sm32[124] == g0; ++i) __builtin_amdgcn_s_sleep(1);
    }
    RST(1);
    f32x4 a0, a1;
    for (;; --tries) {
      v4u p[16];
#pragma unroll
      for (int k = 0; k < 16; ++k) {
        p[k] = LD128(rsW, pv + ((k & 1) + (k < 8 ? 2 : 0)) * 512, (gs * 16 + 2 * (k & 6)) * 32768 + (r >> 1) * 2048 + (r & 1) * 64);
        if ((k & 7) == 7) __builtin_amdgcn_sched_barrier(0);
      }
      if ((w & 5) == 1 && l == 0) __hip_atomic_fetch_max((lu32*)(unsigned long)(lds_base(smem) + 496), s + 1u, __ATOMIC_RELAXED, __HIP_MEMORY_SCOPE_WORKGROUP);
      __builtin_amdgcn_sched_barrier(0);
      a0 = f32x4{};
      a1 = f32x4{};
#pragma unroll
      for (int k = 0; k < 8; ++k) a1 = MFMA16(BF8((k & 1) ? sel1 : sel0), BF8(p[k]), a1);
#pragma unroll
      for (int k = 8; k < 16; ++k) a0 = MFMA16(BF8((k & 1) ? sel1 : sel0), BF8(p[k]), a0);
      if (!__any(__builtin_isunordered(a0[0], a1[0]) | __builtin_isunordered(a0[1], a1[1]) | __builtin_isunordered(a0[2], a1[2]) |
                 __builtin_isunordered(a0[3], a1[3])) || tries <= 0) break;
      asm volatile("s_sleep %0" ::"n"(SLP_P) : "memory");
    }
    pad<233>();
    RST(2);
    const unsigned nz = L ? 0u : 1u;
    const v4u xr = {xa.x, xa.y, xb.x, xb.y}, ar = {pk2(a0[0], a0[1]), pk2(a0[2], a0[3]), pk2(a1[0], a1[1]), pk2(a1[2], a1[3])};
    v4u xo;
#pragma unroll
    for (int e = 0; e < 4; ++e)
      xo[e] = __builtin_elementwise_max(pk2(lo16(xr[e]) + lo16(ar[e]), hi16(xr[e]) + hi16(ar[e])), nz);
    const __amdgpu_buffer_rsrc_t rsO = MKRS(L ? (char*)A.out : ws + OFF_X2, SZ_X);
    ST64((v2u{xo.x, xo.y}), rsO, ov, (s * SEG + r) * DM * 2);
    ST64((v2u{xo.z, xo.w}), rsO, ov + 512, (s * SEG + r) * DM * 2);
    RTS(1, 0);
    RST(3);
    RTS(5, 64);
    if (L == 0) {
      float sm = 0.f, sq = 0.f;
#pragma unroll
      for (int e = 0; e < 4; ++e) {
        const unsigned x = xo[e];
        sm = __builtin_amdgcn_fdot2_f32_bf16(__builtin_bit_cast(bf16x2_t, x), __builtin_bit_cast(bf16x2_t, 0x3f803f80u), sm, false);
        sq = dot2sq(x, sq);
      }
      float t = swap_add32(sm, sq);
      t += dppr<0x121>(t);
      t += dppr<0x122>(t);
      t += dppr<0x124>(t);
      t += dppr<0x128>(t);
      t = xsum16(t) * (1.0f / DM);
      const float ms = __builtin_bit_cast(float, __builtin_amdgcn_readlane(__builtin_bit_cast(int, t), 32));
      const float var = fmaxf(ms - t * t, 0.f);
      const unsigned st = pk2(t, __builtin_amdgcn_sqrtf(lo16(pk2(lo16(pk2(var, var)) + 1e-5f, 0.f))));
      if (l < 4) __hip_atomic_store((gu32*)(FL + FL_FR + ((s * 4 + l) * 4 + (r >> 3)) * 32 + (r & 7)), st, RLX_AGENT);
    }
    RTS(6, 64);
    RST(4);
    RTS(3, 0);
#undef RTS
  }
  RSD();
  if (idx == 0 && threadIdx.x == 384) {
    __hip_atomic_store((gu64*)(ws + OFF_TAG + 8192), en + TAG_STEP, RLX_AGENT);
  }
  if (TOURS) asm volatile("s_endpgm\n\t.p2alignl 12, 0xbf800000\n.Lpad0xa0:\n\ts_setpc_b64 s[98:99]" ::: "memory");
}

constexpr int CUN = 2144;
constexpr int STN = 392;
__device__ __forceinline__ bf16x8 conv_bfrag(const unsigned* up, const unsigned sh) {
  const unsigned d0 = up[0], d1 = up[1], d2 = up[2], d3 = up[3], d4 = up[4];
  const v4u r = {__builtin_amdgcn_alignbit(d1, d0, sh), __builtin_amdgcn_alignbit(d2, d1, sh),
                 __builtin_amdgcn_alignbit(d3, d2, sh), __builtin_amdgcn_alignbit(d4, d3, sh)};
  return BF8(r);
}
__device__ __forceinline__ void role_conv(const MegaArgs& A, char* smem, const int cv) {
  __builtin_amdgcn_s_waitcnt(0x0F70);
  MARK();
  u16* Kr = reinterpret_cast<u16*>(smem);
  u16* Uc = Kr + 8 * 2 * CKN;
  u16* To = Uc + 8 * 2 * CUN;
  u16* Sx = To + 32 * 8;
  u16* St = Sx + 32 * 8;
  const int tid = threadIdx.x;
  const int w = __builtin_amdgcn_readfirstlane(tid >> 6);
  const int l = tid & 63, c = l & 15, g = l >> 4;
  const int d = 8 * cv + w;
  char* ws = A.ws;
  unsigned* FL = (unsigned*)(ws + OFF_FL);
  unsigned* TMO = FL + FL_TMO;
  u16* kr = Kr + w * 2 * CKN;
  float* prm = reinterpret_cast<float*>(kr);
  v4u tx[4];
  f32x4 ac[16] = {};
  LST(16);
  const v2u en = LD64(MKRS(ws + OFF_TAG, 8192 + 8), 0, 8192);
  {
    const unsigned pa = *reinterpret_cast<const unsigned*>(A.logAre + d * NST + 2 * l);
    const unsigned pb = *reinterpret_cast<const unsigned*>(A.Aim + d * NST + 2 * l);
    const unsigned pc = *reinterpret_cast<const unsigned*>(A.Cre + d * NST + 2 * l);
    const unsigned pd = *reinterpret_cast<const unsigned*>(A.Cim + d * NST + 2 * l);
    const float dt = bfr(expf(bf2f(A.logdt[d])));
    tx[0] = __builtin_amdgcn_raw_buffer_load_b128(MKRS(A.x0 + 8 * cv, 128 * DM * 2), tid * (DM * 2), 0, 0);
    tour<193, 200, 8>(w);
    reinterpret_cast<unsigned*>(Uc + w * CUN + ((l & 32) ? 32 + 512 : 0))[(l & 32) ? l & 31 : c] = 0u;
    reinterpret_cast<unsigned*>(Uc + w * CUN + 32 + ((l & 32) ? L_SEQ : 128))[l & 31] = 0u;
    if (l < 50) {
      reinterpret_cast<v4u*>(kr + 1808)[l] = v4u{0u, 0u, 0u, 0u};
      reinterpret_cast<v4u*>(kr + CKN + 1808)[l] = v4u{0u, 0u, 0u, 0u};
    }
#pragma unroll
    for (int e = 0; e < 2; ++e) {
      const float ar = -bfr(expf(e ? hi16(pa) : lo16(pa))) * dt, ai = (e ? hi16(pb) : lo16(pb)) * dt;
      const float ex = expf(ar);
      float sn, cs;
      sincos1(ai, &sn, &cs);
      if (e == 1) pad<187, 1050>(sn);
      prm[2 * l + e] = ar;
      prm[NST + 2 * l + e] = ai;
      prm[2 * NST + 2 * l + e] = (e ? hi16(pc) : lo16(pc)) * dt;
      prm[3 * NST + 2 * l + e] = (e ? hi16(pd) : lo16(pd)) * dt;
      prm[4 * NST + 2 * l + e] = ex * cs;
      prm[5 * NST + 2 * l + e] = ex * sn;
    }
  }
  LST(21); LSC(62);
  const float dsk = bf2f(A.Dskip[d]);
  const u16* ka = kr + (c >> 3) * CKN + 2080 - 16 * (c & 7) + 8 * g;
  const unsigned ush = (c & 1) * 16;
#define KFRAG(D0) (*reinterpret_cast<const bf16x8*>(ka - (D0)))
  const __amdgpu_buffer_rsrc_t rz1 = MKRS(ws + OFF_XA, SZ_X);
  const int t0 = 16 * (4 * (g & 1) + (g >> 1)) + c;
  {
    const u16* uc = Uc + w * CUN;
    const unsigned* ub = reinterpret_cast<const unsigned*>(uc + (c & ~1) + 8 * g + 32);
    const __amdgpu_buffer_rsrc_t rst = MKRS(ws + OFF_GT, L_SEQ * 4);
#define TILE_OUT(dst, av, T)                                                                   \
  {                                                                                            \
    const u16* u_ = Uc + w * CUN + 32 + 128 * (T) + t0;                                        \
    const unsigned uu = pk2(bf2f(u_[0]) * dsk, bf2f(u_[32]) * dsk);                            \
    const unsigned yy = pk2(swap_add32(av[0], av[1]), swap_add32(av[2], av[3]));               \
    const unsigned aa = pk2(lo16(yy) + lo16(uu), hi16(yy) + hi16(uu));                         \
    (dst)[t0] = (u16)aa;                                                                       \
    (dst)[t0 + 32] = (u16)(aa >> 16);                                                          \
  }
#define CHUNK_ST(ck, xr, src, n)                                                               \
  {                                                                                            \
    v4u o;                                                                                     \
    _Pragma("unroll") for (int e = 0; e < 4; ++e)                                              \
      o[e] = pk2(lo16(xr[e]) + bf2f((src)[(2 * e) * (n) + l]), hi16(xr[e]) + bf2f((src)[(2 * e + 1) * (n) + l])); \
    ST128(o, rz1, (l >> 5) * 32768 + (l & 31) * 16, (2 * (ck) * 64 + cv) * 512);               \
  }
#define U_ROW(row, xr_) U_ROW2(row, xr_, __builtin_amdgcn_raw_buffer_load_b32(rst, (row) * 4, 0, AUX_SC1))
#define U_ROW2(row, xr_, stv)                                                                       \
  {                                                                                            \
    const unsigned st_ = (stv);                                                                \
    const float mb_ = lo16(st_), ri_ = 1.0f / hi16(st_);                                       \
    _Pragma("unroll") for (int e = 0; e < 4; ++e) {                                            \
      const unsigned t_ = pk2(lo16(xr_[e]) - mb_, hi16(xr_[e]) - mb_);                         \
      const unsigned q_ = pk2(lo16(t_) * ri_, hi16(t_) * ri_);                                 \
      Uc[(2 * e) * CUN + 32 + (row)] = (u16)q_;                                                \
      Uc[(2 * e + 1) * CUN + 32 + (row)] = (u16)(q_ >> 16);                                    \
    }                                                                                          \
  }
    const unsigned* FS = FL + FL_FS + REP * 32 * 32;
    const int gi = 4 * (g * 8 + (l & 7));
    const int tl = 32 * ((l >> 3) & 1) + 8 * g + (l & 7);
    unsigned khl = 0u, fs = 0u, st0 = 0u;
    const int fo = (w + 8 * ((l >> 3) & 3)) * 32 + (l & 7);
#pragma unroll 1
    for (int ps = 0; ps < 2; ++ps) {
      float v[32];
      {
        const f32x4 ar = *reinterpret_cast<const f32x4*>(prm + gi), ai = *reinterpret_cast<const f32x4*>(prm + NST + gi);
        const f32x4 cr = *reinterpret_cast<const f32x4*>(prm + 2 * NST + gi), ci = *reinterpret_cast<const f32x4*>(prm + 3 * NST + gi);
        const f32x4 wr = *reinterpret_cast<const f32x4*>(prm + 4 * NST + gi), wi = *reinterpret_cast<const f32x4*>(prm + 5 * NST + gi);
        float p0 = (float)(64 * ps + 32 * ((l >> 3) & 1));
        pad<188, 1050>(p0);
        float yr0[4], yi0[4];
#pragma unroll
        for (int k = 0; k < 4; ++k) {
          const float e = __expf(ar[k] * p0);
          float sn, cs;
          __sincosf(ai[k] * p0, &sn, &cs);
          const float zr = e * cs, zi = e * sn;
          yr0[k] = cr[k] * zr - ci[k] * zi;
          yi0[k] = cr[k] * zi + ci[k] * zr;
        }
        RUN_START
#pragma unroll
        for (int i = 0; i < 32; ++i) {
          const f32x2_t sm2 = ya + yb;
          v[i] = sm2.x + sm2.y;
          if (i == 18) pad<189, 1000>(za);
          RUN_STEP
        }
      }
#pragma unroll
      for (int i = 0; i < 16; ++i) v[i] = swap_add32(v[i], v[i + 16]);
#pragma unroll
      for (int i = 0; i < 8; ++i) {
        if (i == 6) pad<190, 1000>(v[i]);
        v[i] = swap_add16(v[i], v[i + 8]);
        v[i] += dppf<0xB1>(0.f, v[i]);
        v[i] += dppf<0x4E>(0.f, v[i]);
        v[i] += dppf<0x141>(0.f, v[i]);
      }
      float s0 = v[0], s1 = v[1], s2 = v[2], s3 = v[3], s4 = v[4], s5 = v[5], s6 = v[6], s7 = v[7];
      asm volatile("" : "+v"(s0), "+v"(s1), "+v"(s2), "+v"(s3), "+v"(s4), "+v"(s5), "+v"(s6), "+v"(s7));
      s0 = (l & 4) ? s4 : s0; s1 = (l & 4) ? s5 : s1; s2 = (l & 4) ? s6 : s2; s3 = (l & 4) ? s7 : s3;
      s0 = (l & 2) ? s2 : s0; s1 = (l & 2) ? s3 : s1;
      const float kv = 2.0f * ((l & 1) ? s1 : s0);
      khl = pk2(kv, 0.f);
      khl = pk2(kv, kv - lo16(khl));
      kr[2080 - 64 * ps - tl] = (u16)khl;
      kr[CKN + 2080 - 64 * ps - tl] = (u16)(khl >> 16);
      SST(ps);
      LST(22 + 6 * ps);
      {
        if (ps == 0) {
          if (w < 2) {
            const unsigned long long m = (((unsigned long long)en.y << 32) | en.x) + TAG_STEP;
            v4u g0;
#pragma unroll 1
            for (unsigned spins = 0; spins < 16u * SPIN_CAP; ++spins) {
              g0 = LD128(MKRS(ws + OFF_G0, 8192), tid * 16, REP * 2048);
              if (__all(((g0.x ^ g0.z) | (g0.y ^ (unsigned)m) | (g0.w ^ (unsigned)(m >> 32))) == 0u)) break;
              asm volatile("s_sleep 2" ::: "memory");
            }
            LST(23);
            U_ROW2(tid, tx[0], g0.x);
          }
          __syncthreads();
          LST(24);
        }
        f32x4 a0t = {};
        pad<191, 1050>(a0t);
#pragma unroll
        for (int b = -1; b < 2; ++b) a0t = MFMA16(KFRAG(-32 * b), conv_bfrag(ub + 16 * b, ush), a0t);
        if (ps) {
#pragma unroll
          for (int b = 2; b < 4; ++b) a0t = MFMA16(KFRAG(-32 * b), conv_bfrag(ub + 16 * b, ush), a0t);
        }
        TILE_OUT(St + w * STN + 128 * ps, a0t, 0);
        __syncthreads();
        LST(25 + 6 * ps);
        if (w == ps) {
          CHUNK_ST(w, tx[0], St + 192 * w, STN);
          LST(26 + 6 * ps);
          pad<192, 900>();
          DRAIN();
          LST(27 + 6 * ps);
          if (ps == 0) {
            if (l < 4) __hip_atomic_store((gu64*)(ws + OFF_X0) + l * 128 + (cv >> 3) * 16 + (cv & 7), (((unsigned long long)en.y << 32) | en.x) + TAG_STEP, RLX_AGENT);
            zero_wait(ws);
          }
          if (l < 8) set_flag(FL + FL_FC + ((2 * w * 4 + l) * 8 + (cv >> 3)) * 32 + (cv & 7));
        }
        SST(4 + ps);
        if (ps == 0 && w >= 2) tx[0] = *reinterpret_cast<const v4u*>(A.x0 + (size_t)tid * DM + 8 * cv);
      }
#define FS_WAIT(o)                                                                             \
  {                                                                                            \
    _Pragma("unroll 1") for (unsigned spins = 0; !__all(fs != 0u) && !spin_timeout(spins, TMO); ++spins) { \
      __builtin_amdgcn_s_sleep(4);                                                             \
      fs = __hip_atomic_load((gu32*)FS + (o), RLX_AGENT);                                      \
    }                                                                                          \
    asm volatile("" ::: "memory");                                                             \
  }
    }
    pad<193, 950>();
    const gu32* kg = (gu32*)(FL + FL_KG + cv * 32 + w);
    fs = __hip_atomic_load((gu32*)FS + w * 32 + (l & 7), RLX_AGENT);
    unsigned kf = __hip_atomic_load(kg, RLX_AGENT);
    tour<180, 185, 8>(w + 6);
    FS_WAIT(w * 32 + (l & 7));
    st0 = __builtin_amdgcn_raw_buffer_load_b32(rst, tid * 4, 0, AUX_SC1);
    SST(6);
    LST(0); LSC(63);
    const __amdgpu_buffer_rsrc_t rk = MKRS(ws + OFF_KF + (size_t)d * 2 * CKN * 2, 2 * CKN * 2);
    v4u t[8];
    if (__builtin_amdgcn_readfirstlane(kf) == 0u) (void)wait_n<8, 1>(FL + FL_KG + cv * 32 + w, 1, TMO);
    asm volatile("" ::: "memory");
    LST(1);
#pragma unroll
    for (int i = 0; i < 4; ++i) {
      t[i] = LD128(rk, (64 * i + l) * 16, 0);
      t[4 + i] = LD128(rk, (64 * i + l) * 16, CKN * 2);
    }
    pad<195, 800>();
    if (w >= 2) U_ROW2(tid, tx[0], st0);
    unsigned* f1 = FL + FL_FC + (64 * w + (cv >> 3)) * 32 + (cv & 7);
    SST(7);
    LST(4);
    {
#pragma unroll
      for (int i = 0; i < 4; ++i)
        if (64 * i + l < 245) {
          reinterpret_cast<v4u*>(kr)[64 * i + l] = t[i];
          reinterpret_cast<v4u*>(kr + CKN)[64 * i + l] = t[4 + i];
        }
      kr[2080 - 64 - tl] = (u16)khl;
      kr[CKN + 2080 - 64 - tl] = (u16)(khl >> 16);
      if (l < 33) kr[l] = kr[CKN + l] = 0;
    }
    pad<196, 1050>();
    RAW_BAR();
    LST(5);
    fs = __hip_atomic_load((gu32*)FS + fo, RLX_AGENT);
    {
      f32x4 a1 = {};
#pragma unroll
      for (int b = -1; b < 8; ++b) a1 = MFMA16(KFRAG(128 - 32 * b), conv_bfrag(ub + 16 * b, ush), a1);
      pad<197, 300>(a1);
      TILE_OUT(St + w * STN, a1, 1);
    }
    bf16x8 kq[16], bw[4];
#pragma unroll
    for (int t = 0; t < 4; ++t) bw[t] = conv_bfrag(ub + 64 * t - 16, ush);
#pragma unroll
    for (int i = 0; i < 16; ++i) kq[i] = KFRAG(128 * i + 32);
    ac[2] = MFMA16(KFRAG(-96), bw[3], ac[2]);
    ac[3] = MFMA16(KFRAG(-96), conv_bfrag(ub + 16 * 15, ush), ac[3]);
    LST(2);
    RAW_BAR();
    if ((w >> 1) == 1) {
      CHUNK_ST(w, tx[0], St + 64 * w - 128, STN);
      DRAIN();
      if (l < 8) set_flag(f1 + l * 256);
    }
    pad<198, 300>();
    FS_WAIT(fo);
    LST(3);
    unsigned sx[4];
#pragma unroll
    for (int i = 1; i < 4; ++i) sx[i] = __builtin_amdgcn_raw_buffer_load_b32(rst, (tid + 512 * i) * 4, 0, AUX_SC1);
#pragma unroll
    for (int i = 2; i < 16; ++i) asm volatile("" : "+v"(ac[i]));
    asm volatile("s_nop 1" : "+v"(bw[0]), "+v"(bw[1]), "+v"(bw[2]), "+v"(bw[3]));
#pragma unroll
    for (int q = 0; q < 4; ++q) {
      unsigned r[20];
      v4u nw[4];
      float ri;
#pragma unroll
      for (int i = 15; i >= -1; --i) {
#pragma unroll
        for (int t = 0; t < 4; ++t) {
          if (i >= 0 && i + t >= 2 && i + t < 16) asm volatile("v_mfma_f32_16x16x32_bf16 %0, %1, %2, %0" : "+v"(ac[i + t]) : "v"(kq[i]), "v"(bw[t]));
          if (q < 3 && t == 0 && i < 15) kq[i + 1] = KFRAG(128 * i + 128 - 32 * q);
          if (q < 3 && (t & 1) && i >= 8 && i < 13) {
            r[10 * (t >> 1) + i - 8] = ub[128 * (t >> 1) + 16 * q + i - 8];
            r[10 * (t >> 1) + i - 3] = ub[128 * (t >> 1) + 16 * q + i + 56];
          }
          if (q < 3 && i >= 2 && i < 6) nw[t][i - 2] = __builtin_amdgcn_alignbit(r[5 * t + i - 1], r[5 * t + i - 2], ush);
          if (q < TXQ && t == 2 && i == 12) tx[q + 1] = *reinterpret_cast<const v4u*>(A.x0 + (size_t)(tid + 512 * (q + 1)) * DM + 8 * cv);
          if (q == TXQ - 1 && t == 2 && i > 9 + TXQ - 1 && i < 12) tx[13 + q - i] = *reinterpret_cast<const v4u*>(A.x0 + (size_t)(tid + 512 * (13 + q - i)) * DM + 8 * cv);
          if (q && t == 2 && i == 8) ri = 1.0f / hi16(sx[q]);
          if (q && (t & 1) && (i == 7 || i == 6)) {
            const int e = 2 * (i & 1) + (t >> 1);
            const unsigned t_ = pk2(lo16(tx[q][e]) - lo16(sx[q]), hi16(tx[q][e]) - lo16(sx[q]));
            const unsigned q_ = pk2(lo16(t_) * ri, hi16(t_) * ri);
            Uc[(2 * e) * CUN + 32 + tid + 512 * q] = (u16)q_;
            Uc[(2 * e + 1) * CUN + 32 + tid + 512 * q] = (u16)(q_ >> 16);
          }
          __builtin_amdgcn_sched_barrier(0);
        }
        if (q == 0 && i == 12) pad<199, 200>();
        if (q == 0 && i == 1) pad<180, 200>();
        if (q == 1 && i == 7) pad<181, 200>();
        if (q == 2 && i == 12) pad<182, 200>();
        if (q == 2 && i == 5) pad<183, 200>();
        if (q == 3 && i == 8) pad<184, 200>();
      }
#pragma unroll
      for (int t = 0; t < 4 && q < 3; ++t) bw[t] = BF8(nw[t]);
    }
    asm volatile("s_nop 7" : "+v"(ac[2]), "+v"(ac[3]));
    pad<194, 300>(ac[3]);
    TILE_OUT(St + w * STN + 128, ac[2], 2);
    TILE_OUT(St + w * STN + 256, ac[3], 3);
    SST(10);
    LST(6); LSC(64);
    RAW_BAR();
    if (w >= 4) {
      CHUNK_ST(w, tx[0], St + 64 * w - 128, STN);
      DRAIN();
      if (l < 8) set_flag(f1 + l * 256);
    }
#undef FS_WAIT
    if (w < 4) tour<176, 180, 4>(w);
    if (w == 1) (void)(wait_n<SLP_V, 32>(FL + FL_FZ + REP * 64, 64, TMO));
    SST(9);
    LST(9);
    LSD();
#undef U_ROW
#undef U_ROW2
#undef TILE_OUT
#undef CHUNK_ST
  }
  const unsigned sb = lds_base(smem);
  const unsigned u1 = sb + (16 * CKN + w * CUN) * 2, u2 = u1 + 16 * CUN, to = sb + (16 * CKN + 16 * CUN) * 2, st = to + 1024;
  const unsigned win = ((c & ~1) + 8 * g) * 2, row = 16 * (g >> 1) + c;
  const bool nil = c < 8 && g < 2;
  if (l < 16) reinterpret_cast<unsigned*>(Uc + (8 + w) * CUN)[l] = 0u;
  bf16x8 th, tw;
#pragma unroll
  for (int i = 0; i < 8; ++i) {
    th[i] = (short)kr[2064 - c + 8 * g + i];
    tw[i] = (short)kr[CKN + 2064 - c + 8 * g + i];
  }
  v4u known;
#pragma unroll
  for (int e = 0; e < 4; ++e) known[e] = (c + 8 * g + 2 * e < 32 ? 0xffffu : 0u) | (c + 8 * g + 2 * e < 31 ? 0xffff0000u : 0u);
  const bf16x8 kz = KFRAG(-96);
  const v8u pa = {u2 + (nil ? 0 : c < 8 ? 32 + 16 * g : 64 + 16 * g), nil ? 0u : 64u, u2 + 64 + 2 * row, to + 16 * row + 2 * w, u2 + win,
                  sb + (unsigned)((w * 2 + (c >> 3)) * CKN + 2080 - 16 * (c & 7) + 8 * g) * 2 - 3904, ush, (unsigned)l * 16u};
  const v8u pb = {u1 + win + 1024, 0u, 0u, 0u, 0u, u1 + (32 + 512 + t0) * 2, st + (w * STN + t0) * 2, st + (64 * (w & 1) + l) * 2};
  const v4u pc = {(unsigned)((l >> 5) * 32768 + (l & 31) * 16), (unsigned)((l >> 1) * (DM * 2) + (l & 1) * 8), (unsigned)((l >> 4) * 128 + ((l >> 1) & 7) * 4),
                  sb + (16 * CKN + (8 + 4 * (l & 1)) * CUN + 32 + (l >> 1)) * 2};
  const char* dbg = ws;
  asm volatile("s_mov_b64 exec, -1\ns_nop 4\n" CONV_ASM
               ::"{v[0:7]}"(pa), "{v[8:11]}"(known), "{v[12:15]}"(th), "{v[16:19]}"(tw), "{v[24:27]}"(kz), "{v[28:35]}"(pb), "{v[36:39]}"(pc),
               "{v40}"(to + (w ? l * 8 : (l & 31) * 16)), "{v[44:47]}"(tx[1]), "{v[48:51]}"(tx[2]), "{v[52:55]}"(tx[3]),
               "{v[196:199]}"(ac[4]), "{v[200:203]}"(ac[5]), "{v[204:207]}"(ac[6]), "{v[208:211]}"(ac[7]), "{v[212:215]}"(ac[8]), "{v[216:219]}"(ac[9]),
               "{v[220:223]}"(ac[10]), "{v[224:227]}"(ac[11]), "{v[228:231]}"(ac[12]), "{v[232:235]}"(ac[13]), "{v[236:239]}"(ac[14]), "{v[240:243]}"(ac[15]),
               "{s[40:43]}"(mk_srd(ws + OFF_X2, SZ_X)), "{s[44:47]}"(mk_srd(FL + FL_FR, 64 * 16 * 128 + 2048)), "{s71}"(64 * 16 * 128 + REP * 512), "{s[48:51]}"(mk_srd(ws + OFF_X3, SZ_X)),
               "{s[52:55]}"(mk_srd(ws + OFF_XA, SZ_X)), "{s[58:59]}"(FL + FL_FC + (64 * 32 + (cv >> 3)) * 32 + (cv & 7)),
               "{s[60:61]}"(FL + FL_FC + (64 * (w + 8) + (cv >> 3)) * 32 + (cv & 7)), "{s62}"(0), "{s63}"(w),
               "{s64}"(__builtin_amdgcn_readfirstlane(__float_as_uint(dsk))), "{s65}"(cv * 16), "{s66}"(REP * 512), "{s67}"(cv * 512),
               "{s68}"((w + 8) * 65536 + cv * 512), "{s69}"(now10()), "{s[72:73]}"(0x0000ffff0000ffffull), "{s[36:37]}"(dbg),
               [lt] "n"(-CVLT), [gv] "n"(GATE_V), [sv] "n"(SLP_V)
               : "memory", "vcc", "scc", "s81", "v255");
#undef KFRAG
}

__device__ __forceinline__ v4u ln_row_packed(const v4u xr) {
  float v[8];
#pragma unroll
  for (int e = 0; e < 4; ++e) { v[2 * e] = lo16(xr[e]); v[2 * e + 1] = hi16(xr[e]); }
  float s = ((v[0] + v[1]) + (v[2] + v[3])) + ((v[4] + v[5]) + (v[6] + v[7]));
  s = wave_sum(s);
  const float mean = s * (1.0f / DM);
  float q = 0.f;
#pragma unroll
  for (int j = 0; j < 8; ++j) { float t = v[j] - mean; q += t * t; }
  q = wave_sum(q);
  const float var = q * (1.0f / DM);
  const float mb = bfr(mean);
  const float sd = bfr(sqrtf(bfr(bfr(var) + 1e-5f)));
  const float ri = 1.0f / sd;
  v4u o;
#pragma unroll
  for (int e = 0; e < 4; ++e) {
    const unsigned t = pk2(v[2 * e] - mb, v[2 * e + 1] - mb);
    const float x0 = lo16(t), x1 = hi16(t);
    float q0 = x0 * ri, q1 = x1 * ri;
    q0 = __builtin_fmaf(__builtin_fmaf(-q0, sd, x0), ri, q0);
    q1 = __builtin_fmaf(__builtin_fmaf(-q1, sd, x1), ri, q1);
    o[e] = pk2(q0, q1);
  }
  return o;
}

__device__ __forceinline__ float bfr1(float x) { return lo16(pk2(x, x)); }
__device__ __forceinline__ void ln4_packed(v4u (&x)[4]) {
  f32x2_t v[16];
#pragma unroll
  for (int e = 0; e < 16; ++e) v[e] = f32x2_t{lo16(x[e >> 2][e & 3]), hi16(x[e >> 2][e & 3])};
  const f32x2_t t = (((v[0] + v[1]) + (v[2] + v[3])) + ((v[4] + v[5]) + (v[6] + v[7]))) +
                    (((v[8] + v[9]) + (v[10] + v[11])) + ((v[12] + v[13]) + (v[14] + v[15])));
  const float mean = red_mod4(t.x + t.y) * (1.0f / DM);
  const float mb = bfr1(mean);
  f32x2_t q0 = {0.f, 0.f}, q1 = {0.f, 0.f};
#pragma unroll
  for (int e = 0; e < 16; e += 2) {
    v[e] -= mb;
    v[e + 1] -= mb;
    q0 += v[e] * v[e];
    q1 += v[e + 1] * v[e + 1];
  }
  q0 += q1;
  const float dm = mean - mb;
  const float var = fmaxf(red_mod4(q0.x + q0.y) * (1.0f / DM) - dm * dm, 0.f);
  const float sd = bfr1(sqrtf(bfr1(bfr1(var) + 1e-5f)));
  const float ri = 1.0f / sd;
#pragma unroll
  for (int e = 0; e < 16; ++e) {
    const unsigned t2 = pk2(v[e].x, v[e].y);
    const f32x2_t q = f32x2_t{lo16(t2), hi16(t2)} * ri;
    x[e >> 2][e & 3] = pk2(q.x, q.y);
  }
}

extern "C" __device__ float mul1(float, float) __asm("llvm.amdgcn.fmul.legacy");
template <bool G0>
__device__ __forceinline__ void role_qkv_t(const MegaArgs& A, char* smem, const int L, const int h) {
  __builtin_amdgcn_s_waitcnt(0x0F70);
  MARK();
  const int w = __builtin_amdgcn_readfirstlane(threadIdx.x >> 6);
  char* ws = A.ws;
  unsigned* FL = (unsigned*)(ws + OFF_FL);
  unsigned* TMO = FL + FL_TMO;
#define LANE_IDS()                         \
  const int l = lane_id();                 \
  const int c = l & 15, g = l >> 4;        \
  (void)c; (void)g;
  unsigned* FC = FL + FL_FC + REP * 8 * 32;
  const int s0 = L * QN0;
  bf16x8 wa[16], wb[16], wv[16];
  const char* Xb = ws + OFF_XA + s0 * 32768 + (w & 3) * 8192;
  const v4u rsF = mk_srd(FC + (w & 3) * 64, ((L ? 64 : QN0) * 32 - REP * 8 - 2 * (w & 3)) * 128);
  constexpr unsigned Q_R = 131072;
  bool zs, got = false;
  v2u n0 = {};
  f32x4 cs[3] = {};
  PSV;
  {
    LANE_IDS();
    const unsigned sb = lds_base(smem);
    const int q = (l >> 2) & 3, p = l & 3, f = q | ((g & 1) << 2);
    const unsigned r0 = sb + (g >> 1) * 16384 + (8 * (g & 1) + q) * 256;
    const unsigned ra = r0 + (w ^ f) * 32 + p * 8, rb = r0 + ((2 * (w & 3) + (p >> 1)) ^ f) * 32 + (p & 1) * 16;
    const v4u one = {0x3f803f80u, 0x3f803f80u, 0x3f803f80u, 0x3f803f80u};
    v4u zt;
    v2u ze;
    unsigned txt = 0u;
#define WP(k) (WM(k) == 0 ? A.Wv : WM(k) == 1 ? A.Wk : A.Wq)
#define W_READ(W, a_)                                    \
  {                                                      \
    const v2u x0 = trd(a_), x1 = trd((a_) + 1024);       \
    W = BF8((v4u{x0.x, x0.y, x1.x, x1.y}));              \
  }
    if (QSL && L) gate(now10() + QSL);
    PST(0);
#pragma unroll
    for (int k = 0; k < 12; ++k) w_issue<false>(WP(k >> 2), WJ(k >> 2), k >> 2, k & 3, h, w, l, sb);
    PST(1);
    constexpr int PB = G0 ? 140 : 160;
    tour<PB + 5, PB + 12, 4>(w);
#pragma unroll
    for (int k = 0; k < 12; ++k) {
      const int j = k & 3;
      if (k == (G0 ? 3 : 3)) pad<PB + 5>(cs[2]);
      if (k == (G0 ? 5 : 7)) pad<PB + 6>(cs[2]);
      if (k == (G0 ? 11 : 9)) pad<PB + 7>(cs[2]);
      if (k == (G0 ? 7 : 11)) pad<PB + 1, 700>(cs[2]);
      if (k == ZK + 1) wait_vm<10>();
      else if (k < 10) wait_vm<8>();
      else if (k == 10) wait_vm<G0 && ZK < 8 ? 5 : 4>();
      else wait_vm<G0 ? 1 : 0>();
      RAW_BAR();
      if (k == IPQ) txt = pull_text((h & 7) + 8 * w);
      if (k == IPQ + 2) asm volatile("" ::"v"(txt));
      if (k == ZK)
        asm volatile("s_nop 4\n\tglobal_load_dwordx4 %0, %2, %3 sc1\n\tglobal_load_dwordx2 %1, %4, %5 sc1"
                     : "=&v"(zt), "=&v"(ze)
                     : "v"(l * 16), "s"(ws + OFF_TAG + (blockIdx.x & 7) * 1024), "v"(0), "s"(ws + OFF_TAG + 8192)
                     : "memory");
#define X_LOOK() asm volatile("s_nop 4\n\tglobal_load_dwordx2 %0, %1, %2 sc1" : "=v"(n0) : "v"((l & 7) * 8 + (l & 8) * 16), "s"(ws + OFF_X0 + REP * 1024 + w * 256) : "memory")
#define X_SEEN() zero_seen(v4u{n0.x, n0.y, n0.x, n0.y}, ze)
      if (k == ZK + 2) {
        asm volatile("" : "+v"(zt), "+v"(ze));
        zs = zero_seen(zt, ze);
        if (G0) X_LOOK();
      }
#define X_ASK()                                                                                                \
  {                                                                                                            \
    asm volatile("" : "+v"(n0));                                                                               \
    got = !L && X_SEEN();                                                                                      \
    if (__builtin_expect(got, 1)) {                                                                            \
      _Pragma("unroll 1") for (int i = 0; i < 8192; i += 1024) glds16c(Xb + i, l * 16, sb + Q_R + w * 8192 + i); \
    }                                                                                                          \
  }
      if (k == 11 && G0) {
        WAIT_VM(0);
        X_ASK();
        X_LOOK();
      }
#pragma unroll
      for (int i = 0; i < 4; ++i) {
        if (k < 9) w_issue<false>(WP(k + 3), WJ(k + 3), (k + 3) & 3, i, h, w, l, sb);
        if (WM(k) == 0) W_READ(wv[4 * WJ(k) + i], ra + j * 4096 + i * 32768);
        if (WM(k) == (G0 ? 1 : 2)) {
          W_READ(wa[4 * WJ(k) + i], rb + j * 4096 + i * 32768);
          W_READ(wb[4 * WJ(k) + i], rb + 8 + j * 4096 + i * 32768);
        }
        if (k > 0 && WM(k - 1) == 0) cs[2] = MFMA16(BF8(one), wv[4 * WJ(k - 1) + i], cs[2]);
        if (k > 0 && WM(k - 1) == (G0 ? 1 : 2)) {
          cs[0] = MFMA16(BF8(one), wa[4 * WJ(k - 1) + i], cs[0]);
          cs[1] = MFMA16(BF8(one), wb[4 * WJ(k - 1) + i], cs[1]);
        }
        asm volatile("" : "+v"(cs[0]), "+v"(cs[1]), "+v"(cs[2]));
      }
      if (k == 1) PST(2);
      if (k == 3) PST(3);
      if (k == 5) PST(4);
      if (k == 7) PST(5);
      if (k == 9) PST(6);
    }
    if (got) PST(11);
#pragma unroll
    for (int i = 12; i < 16 && !G0; ++i) {
      cs[0] = MFMA16(BF8(one), wa[i], cs[0]);
      cs[1] = MFMA16(BF8(one), wb[i], cs[1]);
    }
    asm volatile("s_nop 7" : "+v"(cs[0]), "+v"(cs[1]));
    RAW_BAR();
    PST(8);
    if (G0 && __builtin_expect(!got, 1)) {
      WAIT_VM(0);
      X_ASK();
    }
    if (got) PST(12);
#undef X_LOOK
#undef X_SEEN
#undef X_ASK
#undef W_READ
#undef WP
  }
  WSTAMP2();
  constexpr int Q_X = 98304;
  const unsigned sb = lds_base(smem);
  v2u ea, eb, ev;
  {
    LANE_IDS();
    v2u e[3];
#pragma unroll
    for (int i = 0; i < 3; ++i) {
      const float x = g ? 0.f : cs[i][0], x1 = x - bfr1(x);
      e[i] = v2u{pk2(x, x1), pk2(x1 - bfr1(x1), 0.f)};
    }
    ea = e[0]; eb = e[1]; ev = e[2];
    asm volatile("" : "+v"(ea), "+v"(eb), "+v"(ev));
    if (__builtin_expect(!zs, 0)) zero_wait(ws);
    *reinterpret_cast<v2u*>(smem + Q_X + w * 2048 + (l >> 5) * 16384 + (l & 31) * 8) = v2u{0x3f803f80u, 0x3f803f80u};
  }
  pad<G0 ? 148 : 168>();
  constexpr int Q_ST = Q_X + 8 * 2048 + 256, Q_CNT = Q_X + 11 * 2048 + 256;
  *(lu32*)(unsigned long)(sb + Q_CNT + (threadIdx.x & 1) * 4) = 0u;
  v8u pv, wr[24];
  v4u pw;
  unsigned sw;
  {
    LANE_IDS();
    const unsigned fa = sb + Q_R + g * 512 + c * 16;
    pv = v8u{ea.x, ea.y, eb.x, eb.y, ev.x, ev.y, (unsigned)l * 16u, fa};
    pw = v4u{sb + Q_ST + c * 4, sb + Q_ST + g * 16,
             G0 ? (l & 7) * 4 + (l & 8) * 16 : (c & 8) ? sb + Q_X + ((w + 1) & 1) * 128 : fa + (w & 3) * 128, (c & 8) ? 0u : Q_R};
    sw = sb + Q_ST + (w & 3) * 32 + (l & 7) * 4;
#pragma unroll
    for (int i = 0; i < 8; ++i) {
      wr[i] = __builtin_shufflevector(__builtin_bit_cast(v4u, wa[2 * i]), __builtin_bit_cast(v4u, wa[2 * i + 1]), 0, 1, 2, 3, 4, 5, 6, 7);
      wr[8 + i] = __builtin_shufflevector(__builtin_bit_cast(v4u, wb[2 * i]), __builtin_bit_cast(v4u, wb[2 * i + 1]), 0, 1, 2, 3, 4, 5, 6, 7);
      wr[16 + i] = __builtin_shufflevector(__builtin_bit_cast(v4u, wv[2 * i]), __builtin_bit_cast(v4u, wv[2 * i + 1]), 0, 1, 2, 3, 4, 5, 6, 7);
    }
  }
  const v4u rsQ = mk_srd(ws + OFF_QK, 2 * SZ_QK), rsV = mk_srd(ws + OFF_VF, 2 * SZ_O);
  const v4u rsM = mk_srd(ws + (G0 ? OFF_MEMK : OFF_MEMV), 2 * SZ_MEM);
  const unsigned* FQ = FL + FL_FQ + (s0 * 32 + h) * 32;
  const char* dbg = ws;
  PST(9);
  __builtin_amdgcn_s_waitcnt(0x0F70);
  asm volatile("" ::"v"(n0));
  RAW_BAR();
  PST(10);
  PSD();
#define QKV_OPS                                                                                                              \
  ::"{v[64:71]}"(wr[0]), "{v[72:79]}"(wr[1]), "{v[80:87]}"(wr[2]), "{v[88:95]}"(wr[3]), "{v[96:103]}"(wr[4]),                \
      "{v[104:111]}"(wr[5]), "{v[112:119]}"(wr[6]), "{v[120:127]}"(wr[7]), "{v[128:135]}"(wr[8]), "{v[136:143]}"(wr[9]),      \
      "{v[144:151]}"(wr[10]), "{v[152:159]}"(wr[11]), "{v[160:167]}"(wr[12]), "{v[168:175]}"(wr[13]), "{v[176:183]}"(wr[14]), \
      "{v[184:191]}"(wr[15]), "{v[192:199]}"(wr[16]), "{v[200:207]}"(wr[17]), "{v[208:215]}"(wr[18]), "{v[216:223]}"(wr[19]), \
      "{v[224:231]}"(wr[20]), "{v[232:239]}"(wr[21]), "{v[240:247]}"(wr[22]), "{v[248:255]}"(wr[23]), "{v[0:7]}"(pv),        \
      "{v[8:11]}"(pw), "{v62}"(sw), "{s[40:43]}"(rsQ), "{s[44:47]}"(rsV), "{s[48:51]}"(rsM), "{s[52:55]}"(rsF),              \
      "{s[56:57]}"(Xb), "{s58}"(sb + Q_R + (w & 3) * 8192), "{s82}"(sb + Q_CNT + 16 + (w & 3) * 4), "{s69}"(__builtin_amdgcn_readfirstlane((int)got)), "{s59}"((s0 * 512 + h * 16 + (G0 ? 8 : 0) + (w & 3)) * 1024), "{s60}"((s0 * 256 + h * 8 + w) * 1024), \
      "{s61}"(s0 * 65536 + h * 2048 + (w & 3) * 512), "{s62}"(s0 * 4096), "{s63}"(L ? QFAST - QN0 : 0), "{s[64:65]}"(FQ), "{s66}"(L ? NSEG - 1 - QN0 : QN0 - 1), "{s67}"(0), \
      "{s83}"(G0 ? 4 - L : L ? NSEG - 1 - QN0 : QN0 - 1), "{s86}"(1 - L), [n0] "n"(QN0), [x3] "n"(OFF_X3 - OFF_XA - QN0 * 32768),            \
      "{s[70:71]}"(0xaaaaaaaaaaaaaaaaull), "{s[72:73]}"(0xccccccccccccccccull), "{s78}"(w), "{s80}"(sb + Q_CNT), "{s[84:85]}"(FL + FL_S0 + (h >> 3) * 32 + (h & 7)),             \
      "{s[36:37]}"(dbg), [gq] "n"(GATE_Q), [sq] "n"(SLP_Q), [s1] "n"(SLP_Q1 - SLP_Q)                                         \
      : "memory", "vcc", "scc", "s81", "v63"
  if (G0) asm volatile("s_mov_b64 exec, -1\ns_nop 4\n" QKV_ASM_0 QKV_OPS);
  else asm volatile("s_mov_b64 exec, -1\ns_nop 4\n" QKV_ASM_1 QKV_OPS);
#undef QKV_OPS
#undef LANE_IDS
}
__device__ __forceinline__ void role_qkv(const MegaArgs& A, char* smem, const int L, const int h) {
  if (__builtin_amdgcn_readfirstlane(threadIdx.x >> 6) < 4) role_qkv_t<true>(A, smem, L, h);
  else role_qkv_t<false>(A, smem, L, h);
}

constexpr int MEGA_WGS = 256;
constexpr size_t MEGA_LDS = 163840;
static_assert(MEGA_LDS <= 163840 && MEGA_LDS >= 162816 + 256, "lds");
static_assert(AW_SO + 49152 <= MEGA_LDS, "lds");
static_assert(MEGA_LDS >= (size_t)(8 * 2 * CKN + 8 * 2 * CUN + 2 * 32 * 8 + 8 * STN) * 2, "lds");
static_assert(MEGA_LDS >= (size_t)2 * 32 * SMS * 2 && MEGA_LDS >= 131072, "lds");
static_assert(131072 >= (size_t)(40 * NST + 32 * 400) * 4, "lds");
__global__ __launch_bounds__(512, 2) void mega_kernel(const u16* mem0, const u16* Dskip, char* ws, u16* out, const u16* x0,
                                                      const u16* logAre, const u16* Aim, const u16* Cre, const u16* Cim,
                                                      const u16* logdt, const u16* Wq, const u16* Wk, const u16* Wv,
                                                      const u16* Wo) {
  extern __shared__ __attribute__((aligned(16))) char smem[];
  if (TOURS & 4) {
    const unsigned cls = (0xfdac >> (((blockIdx.x & 1) * 4 + (blockIdx.x >> 6)) * 2)) & 3;
#define TO(a, b, c, d, e, f, g, h) "s_branch .Lpad" #a "\n\ts_branch .Lpad" #b "\n\ts_branch .Lpad" #c "\n\ts_branch .Lpad" #d "\n\ts_branch .Lpad" #e \
                                   "\n\ts_branch .Lpad" #f "\n\ts_branch .Lpad" #g "\n\ts_branch .Lpad" #h "\n\t"
    asm volatile("s_load_dword s94, %1, 0\n\ts_load_dword s95, %1, 64\n\ts_getpc_b64 s[96:97]\n\ts_add_u32 s96, s96, %0\n\ts_addc_u32 s97, s97, 0\n\ts_swappc_b64 s[98:99], s[96:97]\n\ts_branch 2f\n\t"
                 TO(0xc8, 0x78, 0x64, 0x79, 0x65, 0x81, 0x6d, 0x6e) TO(0xc8, 0xa0, 0x8c, 0xa1, 0x8d, 0xa9, 0x95, 0x97)
                 TO(0xc8, 0xba, 0xbb, 0xbc, 0xbd, 0xbe, 0xbf, 0xc0) TO(0xc9, 0xca, 0xcb, 0xcc, 0xcd, 0xce, 0xcf, 0xc8) "2:\n\ts_waitcnt lgkmcnt(0)"
                 ::"s"(cls * 32 + (__builtin_amdgcn_readfirstlane(threadIdx.x) >> 4) + 16), "s"(__builtin_amdgcn_kernarg_segment_ptr()) : "s94", "s95", "s96", "s97", "s98", "s99", "scc");
#undef TO
  }
  const int b = __builtin_amdgcn_readfirstlane(role_no());
  const MegaArgs A{mem0, Dskip, ws, out, x0, logAre, Aim, Cre, Cim, logdt, Wq, Wk, Wv, Wo, b};
  {
    const int hi = (b >= 32 && b < 96) ? b - 32 : (b >= 200 && b < 248) ? b - 136 : (b >= 96 && b < 104) ? b + 16 : (b >= 248 ? b - 128 : -1);
    if (hi >= 0) start_kgen(A, smem, hi, b < 248 - XI0 + 8 ? b - 248 + XI0 : -1, (b >= 64 && b < 104) || (b >= 232 && b < 240) ? b & 15 : -1);
  }
  if ((EXP_OFF & 1) && b >= 64 && b < 80) return;
  if ((EXP_OFF & 8) && b >= 200 && b < 232) return;
  if ((EXP_OFF & 16) && b >= 32 && b < 64) return;
  if ((EXP_OFF & 32) && b >= 80 && b < 96) return;
  if (b < 64) role_chain(A, smem, b >> 5, b & 31, 0);
  else if (b < 104 || (b >= 232 && b < 240)) role_aw(A, smem, b < 80 ? 0 : b < 96 ? 2 : 1, b & 15);
  else if (b < 168) role_conv(A, smem, b - 104);
  else if (b < 232) role_qkv(A, smem, (b - 168) >> 5, (b - 168) & 31);
  else role_red(A, smem, b - 240);
}

extern "C" void kernel_launch(void* const* d_in, const int* in_sizes, int n_in, void* d_out, int out_size, void* d_ws,
                              size_t ws_size, hipStream_t stream) {
  MegaArgs A;
  A.x0 = (const u16*)d_in[0];
  A.logAre = (const u16*)d_in[1];
  A.Aim = (const u16*)d_in[2];
  A.Cre = (const u16*)d_in[3];
  A.Cim = (const u16*)d_in[4];
  A.logdt = (const u16*)d_in[5];
  A.Dskip = (const u16*)d_in[6];
  A.mem0 = (const u16*)d_in[8];
  A.Wq = (const u16*)d_in[9];
  A.Wk = (const u16*)d_in[10];
  A.Wv = (const u16*)d_in[11];
  A.Wo = (const u16*)d_in[12];
  A.ws = (char*)d_ws;
  A.out = (u16*)d_out;

  (void)hipFuncSetAttribute((const void*)mega_kernel, hipFuncAttributeMaxDynamicSharedMemorySize, (int)MEGA_LDS);
  mega_kernel<<<MEGA_WGS, 512, MEGA_LDS, stream>>>(A.mem0, A.Dskip, A.ws, A.out, A.x0, A.logAre, A.Aim, A.Cre, A.Cim, A.logdt,
                                                   A.Wq, A.Wk, A.Wv, A.Wo);
}
```

```cpp
#include <hip/hip_runtime.h>
#include <hip/hip_bf16.h>
#include <stdint.h>

constexpr int L_SEQ = 2048;
constexpr int DM    = 512;
constexpr int NST   = 128;
constexpr int NH    = 32;
constexpr int DH    = 128;
constexpr int HD    = NH * DH;
constexpr int MM    = 8;
constexpr int SEG   = 32;
constexpr int NSEG  = L_SEQ / SEG;
constexpr int LK    = MM + SEG;

typedef unsigned short u16;
using bf16x8 = __attribute__((ext_vector_type(8))) short;
using f32x4  = __attribute__((ext_vector_type(4))) float;
using f32x16 = __attribute__((ext_vector_type(16))) float;

static __device__ __forceinline__ u16 f2bf(float f) {
  unsigned u = __float_as_uint(f);
  u += 0x7fffu + ((u >> 16) & 1u);
  return (u16)(u >> 16);
}
static __device__ __forceinline__ float bf2f(u16 h) { return __uint_as_float(((unsigned)h) << 16); }
static __device__ __forceinline__ float bfr(float f) { return bf2f(f2bf(f)); }

constexpr size_t SZ_X      = (size_t)L_SEQ * DM * 2;
constexpr size_t OFF_KF    = 0;
constexpr size_t OFF_UT    = OFF_KF + (size_t)DM * L_SEQ * 4;
constexpr size_t OFF_YT    = OFF_UT + SZ_X;
constexpr size_t OFF_XA    = OFF_YT + SZ_X;
constexpr size_t OFF_H     = OFF_XA + SZ_X;
constexpr size_t OFF_X2    = OFF_H + SZ_X;
constexpr size_t OFF_U2    = OFF_X2 + SZ_X;
constexpr size_t OFF_X3    = OFF_U2 + SZ_X;
constexpr size_t SZ_QK     = (size_t)L_SEQ * 2 * HD * 2;
constexpr size_t OFF_QK    = OFF_X3 + SZ_X;
constexpr size_t SZ_O      = (size_t)L_SEQ * HD * 2;
constexpr size_t OFF_VF    = OFF_QK + 2 * SZ_QK;
constexpr size_t SZ_PW     = (size_t)2 * NSEG * 16 * SEG * DM * 2;
constexpr size_t OFF_PW    = OFF_VF + 2 * SZ_O;
constexpr size_t SZ_MEM    = (size_t)NSEG * MM * HD * 2;
constexpr size_t OFF_MEMK  = OFF_PW + SZ_PW;
constexpr size_t OFF_MEMV  = OFF_MEMK + 2 * SZ_MEM;
constexpr size_t OFF_GT    = OFF_MEMV + 2 * SZ_MEM;
constexpr size_t SZ_ACC    = (size_t)2 * 32768;
constexpr size_t OFF_Z     = OFF_GT + (size_t)64 * DM * 4;
constexpr size_t OFF_ACC   = OFF_Z;
constexpr unsigned ACC_LO = 0x88000000u, ACC_HI = 0x87fffff7u;
constexpr size_t OFF_FL    = OFF_ACC + 2 * SZ_ACC;
constexpr int FL_FW  = 0;
constexpr int FL_FR  = FL_FW + 2 * 64 * 16 * 32;
constexpr int FL_TW  = FL_FR + 64 * 16 * 32;
constexpr int FL_FC  = FL_TW + 4 * 128;
constexpr int FL_FQ  = FL_FC + 2 * 64 * 32 * 32;
constexpr int FL_FS  = FL_FQ + 2 * 65 * 32 * 32;
constexpr int FL_FZ  = FL_FS + 4 * 32 * 32;
constexpr int FL_KG  = FL_FZ + 8 * 32;
constexpr int FL_G   = FL_KG + 64 * 32;
constexpr int FL_TK  = FL_G + 4 * 4 * 32;
constexpr int FL_TMO = FL_TK + 2 * 256;
constexpr int FL_S0  = FL_TMO + 64;
constexpr int FL_END = FL_S0 + 2048;
constexpr size_t Z_BYTES   = OFF_FL + (size_t)FL_END * 4 - OFF_Z;
#define TSH 0
constexpr size_t OFF_TAG   = OFF_Z + Z_BYTES + TSH;
constexpr unsigned long long TAG_STEP = 0x9e3779b97f4a7c15ull;
constexpr size_t OFF_G0    = OFF_TAG + 8 * 1024 + 256;
constexpr size_t OFF_X0    = OFF_G0 + 8192;
constexpr size_t WS_TOTAL  = OFF_X0 + 4096;
static_assert(Z_BYTES % 256 == 0 && OFF_Z % 256 == 0 && Z_BYTES <= 2 * 128 * 8192, "zero region");
#define REP ((blockIdx.x >> 1) & 3)
static_assert(WS_TOTAL + (4 << 20) <= (size_t)256 * 1024 * 1024, "workspace too large");

#define CKNP 96
constexpr int CKN = 2208 + CKNP;
static_assert((size_t)DM * 2 * CKN * 2 <= (size_t)DM * L_SEQ * 4 + SZ_X, "K buffer");
__device__ __forceinline__ float pos_of_index(int p) {
  if (p < 256) return (float)p;
  if (p < 384) return (float)(256 + 2 * (p - 256));
  if (p < 512) return (float)(512 + 4 * (p - 384));
  if (p < 640) return (float)(1024 + 8 * (p - 512));
  return 2048.0f;
}
__device__ __forceinline__ int index_of_l(int l) {
  float pf = bfr((float)l);
  int p = (int)pf;
  if (p < 256) return p;
  if (p < 512) return 256 + ((p - 256) >> 1);
  if (p < 1024) return 384 + ((p - 512) >> 2);
  if (p < 2048) return 512 + ((p - 1024) >> 3);
  return 640;
}

template <int CTRL> __device__ __forceinline__ float dppr(float x) {
  return __builtin_bit_cast(float, __builtin_amdgcn_update_dpp(0, __builtin_bit_cast(int, x), CTRL, 0xf, 0xf, false));
}
__device__ __forceinline__ float wave_sum(float v) {
  v += dppr<0x121>(v);
  v += dppr<0x122>(v);
  v += dppr<0x124>(v);
  v += dppr<0x128>(v);
  {
    auto r = __builtin_amdgcn_permlane16_swap(__float_as_uint(v), __float_as_uint(v), false, false);
    v = __uint_as_float(r[0]) + __uint_as_float(r[1]);
  }
  {
    auto r = __builtin_amdgcn_permlane32_swap(__float_as_uint(v), __float_as_uint(v), false, false);
    v = __uint_as_float(r[0]) + __uint_as_float(r[1]);
  }
  return v;
}
__device__ __forceinline__ void ln_row8(float (&v)[8]) {
  float s = 0.f;
#pragma unroll
  for (int j = 0; j < 8; ++j) s += v[j];
  s = wave_sum(s);
  const float mean = s * (1.0f / DM);
  float q = 0.f;
#pragma unroll
  for (int j = 0; j < 8; ++j) { float t = v[j] - mean; q += t * t; }
  q = wave_sum(q);
  const float var = q * (1.0f / DM);
  const float mb = bfr(mean);
  const float sd = bfr(sqrtf(bfr(bfr(var) + 1e-5f)));
#pragma unroll
  for (int j = 0; j < 8; ++j) v[j] = bfr(bfr(v[j] - mb) / sd);
}

#define RLX_AGENT __ATOMIC_RELAXED, __HIP_MEMORY_SCOPE_AGENT
typedef __attribute__((address_space(1))) unsigned gu32;
typedef __attribute__((address_space(3))) unsigned lu32;
typedef unsigned v4u __attribute__((ext_vector_type(4)));
constexpr int AUX_SC1 = 16;
constexpr unsigned SPIN_CAP = 1u << 18;

__device__ __forceinline__ bool wait_flags(const unsigned* flags, int lane, int nfl, unsigned want, unsigned* tmo) {
  for (unsigned spins = 0;; ++spins) {
    unsigned v = want;
    if (lane < nfl) v = __hip_atomic_load((gu32*)flags + lane * 32, RLX_AGENT);
    if (__all(v >= want)) break;
    if (spins > SPIN_CAP) {
      if (lane == 0) __hip_atomic_store((gu32*)tmo, 1u, RLX_AGENT);
      return false;
    }
  }
  __builtin_amdgcn_fence(__ATOMIC_ACQUIRE, "wavefront");
  return true;
}

#define STAMP(i)
#define STAMP_DUMP()
#define WSTAMP(i)
template <int CTRL> __device__ __forceinline__ float dppf(float old, float x) {
  return __builtin_bit_cast(float, __builtin_amdgcn_update_dpp(__builtin_bit_cast(int, old), __builtin_bit_cast(int, x), CTRL, 0xf, 0xf, false));
}
__device__ __forceinline__ float wave_max_dpp(float x) {
  const float ninf = -3e38f;
  x = fmaxf(x, dppf<0x111>(ninf, x));
  x = fmaxf(x, dppf<0x112>(ninf, x));
  x = fmaxf(x, dppf<0x114>(ninf, x));
  x = fmaxf(x, dppf<0x118>(ninf, x));
  x = fmaxf(x, dppf<0x142>(ninf, x));
  x = fmaxf(x, dppf<0x143>(ninf, x));
  return __builtin_bit_cast(float, __builtin_amdgcn_readlane(__builtin_bit_cast(int, x), 63));
}
__device__ __forceinline__ float wave_sum_dpp(float x) {
  x += dppf<0x111>(0.f, x);
  x += dppf<0x112>(0.f, x);
  x += dppf<0x114>(0.f, x);
  x += dppf<0x118>(0.f, x);
  x += dppf<0x142>(0.f, x);
  x += dppf<0x143>(0.f, x);
  return __builtin_bit_cast(float, __builtin_amdgcn_readlane(__builtin_bit_cast(int, x), 63));
}
constexpr int SMS = 528;
constexpr int QS = 144;
constexpr int VS = 80;
constexpr int SOS = 144;
#define VAR_NREP 4
constexpr int NREP = VAR_NREP;
__device__ __forceinline__ float xsum16(float x) {
  auto r = __builtin_amdgcn_permlane16_swap(__float_as_uint(x), __float_as_uint(x), false, false);
  return __uint_as_float(r[0]) + __uint_as_float(r[1]);
}
__device__ __forceinline__ float xsum32(float x) {
  auto r = __builtin_amdgcn_permlane32_swap(__float_as_uint(x), __float_as_uint(x), false, false);
  return __uint_as_float(r[0]) + __uint_as_float(r[1]);
}
__device__ __forceinline__ float xmax16(float x) {
  auto r = __builtin_amdgcn_permlane16_swap(__float_as_uint(x), __float_as_uint(x), false, false);
  return fmaxf(__uint_as_float(r[0]), __uint_as_float(r[1]));
}
__device__ __forceinline__ float xmax32(float x) {
  auto r = __builtin_amdgcn_permlane32_swap(__float_as_uint(x), __float_as_uint(x), false, false);
  return fmaxf(__uint_as_float(r[0]), __uint_as_float(r[1]));
}
__device__ __forceinline__ float red_mod4(float x) {
  x += dppf<0x124>(0.f, x);
  x += dppf<0x128>(0.f, x);
  return xsum32(xsum16(x));
}
typedef __bf16 bf16x2_t __attribute__((ext_vector_type(2)));
typedef float f32x2_t __attribute__((ext_vector_type(2)));
__device__ __forceinline__ unsigned pk2(float a, float b) {
  f32x2_t v = {a, b};
  bf16x2_t r = __builtin_convertvector(v, bf16x2_t);
  return __builtin_bit_cast(unsigned, r);
}
__device__ __forceinline__ float dot2sq(const unsigned x, const float acc) {
  return __builtin_amdgcn_fdot2_f32_bf16(__builtin_bit_cast(bf16x2_t, x), __builtin_bit_cast(bf16x2_t, x), acc, false);
}
__device__ __forceinline__ float lo16(unsigned u) { return __uint_as_float(u << 16); }
__device__ __forceinline__ float hi16(unsigned u) { return __uint_as_float(u & 0xffff0000u); }
__device__ __forceinline__ void scale_pair(float a, float b, float scale, float& oa, float& ob) {
  unsigned u = pk2(a, b);
  unsigned v = pk2(lo16(u) * scale, hi16(u) * scale);
  oa = lo16(v);
  ob = hi16(v);
}

#define XSPLIT 3
__device__ __forceinline__ int role_no() {
  const int b = blockIdx.x, x = b & 7, j = b >> 3;
  if (XSPLIT >= 3) {
    const int y = XSPLIT == 5 ? x & 3 : x >> 1;
    if (XSPLIT >= 6 && (j >> 2) == 6) return (x & 1) ? 104 + 16 * y + j - 16 : 80 + 4 * y + (j & 3);
    if (XSPLIT >= 7 && (j >> 2) == 4) return (x & 1) ? 104 + 16 * y + j - 16 : 64 + 4 * y + (j & 3);
    if (XSPLIT == 5 ? x < 4 : !(x & 1)) {
      if (j >= 16) return 104 + 16 * y + j - 16;
#define HXX 0
      return XSPLIT == 4 ? 16 * y + j : (j & 8) * 4 + ((8 * y + (j & 7)) ^ HXX);
    }
    if (j < 16) return 168 + (j & 8) * 4 + 8 * y + (j & 7);
    const int i = 4 * y + (j & 3);
    return j < 20 ? 64 + i : j < 24 ? (i < 8 ? 96 + i : 224 + i) : j < 28 ? 80 + i : 240 + i;
  }
  const int L = x >> 2, xx = x & 3;
  if (j < 8) return 32 * L + 8 * xx + j;
  if (j < 12) return 64 + 16 * L + 4 * xx + j - 8;
  if (j < 16) { const int i = 4 * xx + j - 12; return L ? 240 + i : i < 8 ? 96 + i : 224 + i; }
  if (j < 24) return 104 + 8 * x + j - 16;
  return 168 + 32 * L + 8 * xx + j - 24;
}
#define MSTAMP()
#define WSTAMP2()
#define SST(k)
#define TSR(rn, k, ss, t)
#define TS(k, ss, t)
#define RST(k)
#define RSD()
#define LST(k)
#define LSC(k)
#define LSD()
#define PSV
#define PST(k)
#define PSD()
#define PSE()
#define FSD
#define FS(k)
#define FD(ss)
struct MegaArgs {
  const u16* mem0;
  const u16* Dskip;
  char* ws;
  u16* out;
  const u16* x0;
  const u16 *logAre, *Aim, *Cre, *Cim, *logdt;
  const u16 *Wq, *Wk, *Wv, *Wo;
  int rn;
};

__device__ __forceinline__ int lane_id() {
  int l;
  asm volatile("v_mbcnt_lo_u32_b32 %0, -1, 0\n\tv_mbcnt_hi_u32_b32 %0, -1, %0" : "=v"(l));
  return l;
}
__device__ __forceinline__ bool spin_timeout(unsigned spins, unsigned* tmo) {
  if (spins > SPIN_CAP) {
    if (lane_id() == 0) __hip_atomic_store((gu32*)tmo, 1u, RLX_AGENT);
    return true;
  }
  return false;
}
#define PSWH (h & 2)
#define RR 0
#define RQM 63
#define RPM 63
#define CD0 100
#define APN 6
#define APL 300
#define APA 75
#define APB 115
#define APP 203
#define CD 27
#define CDS 30
#define CDL 0
#define XG(h) ((h) >> 3)
#define GP 230
#define GR 50
#define GM 0
#define ROT (4 * (h & 1))
#define CPRIO 1
#define P1PRIO 3
#define QLEAD 2
#define POLL_SLEEP 12
#define SLP_C POLL_SLEEP
#define SLP_A POLL_SLEEP
#define SLP_W POLL_SLEEP
#define SLP_R 4
#define SLP_V 4
#define SLP_Q 4
#define SLP_M 4
#define AWPRIO 2
#define SLP_S 8
#define SPOS 4
#define CVPRE 0
#define QHV 0
#define QCHK 0
#define LEADN 99
#define GATE_V 40
#define GATE_R 380
#define GATE_N 440
#define AWAHEAD 1
#define GATE_M 150
#define GATE_M2 380
#define QPACE 0
#define QPS 6
#define QPL 100
#define GATE_Q 150
#define SLP_Q1 24
#define QPF 3
#define QPV 6
#define QVP 3
#define QP0 2
#define QP1 0
#define QFP 15
#define QUP 7
#define QEP 3
#define AWD 10
#define GPER 200
#define GMA 60
#define GMR 135
#define GMS 0
#define GSTEP 30
#define GATE_RT 130
#define GATE_1 160
#define N1S 20
#define N1E 36
#define PRI_A 3
#define PRI_WA 2
#define PRI_WB 1
#define AWSKIP 3
#define PRI_0 3
#define AWDEF 1
#define SLP_F 2
#define EXP_OFF 0
#define AWSPEC 1
#define CVTHR 48
#define NR_C 8
#define NR_R 8
#define NR_W 8
#define NR_N 2
#define QAHEAD 0
#define NS0 4
#define CVST 16
#define CVNB 9
#define CVFULL 1
#define HK3 0
#define AWDT 120
#define AWMO 0
#define HK2 0
#define RDT 185
#define RQ0 4
#define RGW 40
#define ZK 8
#define ZC 7
#define KBR 8
#define AWE 0
#define AWK 5
#define CRD 40
#define CD2 110
#define AWLM 45
#define AWLD 50
#define TXQ 3
#define QN0 6
#define CD4 70
#define SLP_2 2
#define CD1 80
#define SLP_1 8
#define CSL 0
#define QSL 0
#define RSL 0
#define CTH 0
#define WORD 0
#define WXC 0
#define PFS 0
#define XI0 8
#define QFAST 58
#define SLP_G 4
#define TCH 0
#define CSD 0
#define QX2 1
#define CET 0
#define CG1 110
#define IPC 20
#define IPQ 20
#define AWDU 180
#define RTW 0
#define CVLT 120
#define SLP_P 2
#define SLP_N POLL_SLEEP
template <int SLP, int WPL>
__device__ __forceinline__ bool wait_n(const unsigned* f, int n, unsigned* tmo) {
  const int l = lane_id();
  for (unsigned spins = 0;; ++spins) {
    unsigned v = 1u;
    if (l < n) v = __hip_atomic_load((gu32*)f + (l / WPL) * 32 + l % WPL, RLX_AGENT);
    if (__all(v != 0u)) break;
    if (spin_timeout(spins, tmo)) return false;
    __builtin_amdgcn_s_sleep(SLP);
  }
  asm volatile("" ::: "memory");
  return true;
}
__device__ __forceinline__ unsigned now10() { return (unsigned)__builtin_amdgcn_s_memrealtime(); }
__device__ __forceinline__ void gate(const unsigned t) {
#pragma unroll 1
  for (int i = 0; i < 64; ++i) {
    const int d = (int)(t - now10());
    if (d <= 0) break;
    if (d > 100) __builtin_amdgcn_s_sleep(32);
    else if (d > 25) __builtin_amdgcn_s_sleep(8);
    else __builtin_amdgcn_s_sleep(2);
  }
}
template <int SLP, int WPL>
__device__ __forceinline__ unsigned grp_wait(const bool leader, volatile lu32* sw, const unsigned val, const unsigned* f, int n,
                                             unsigned* tmo, const unsigned t) {
  unsigned r;
  gate(t);
  if (leader) {
    const int l = lane_id();
    unsigned v;
    for (unsigned spins = 0;; ++spins) {
      v = __hip_atomic_load((gu32*)f + ((l & 15) / WPL) * 32 + l % WPL, RLX_AGENT);
      if (__all(v != 0u) || spin_timeout(spins, tmo)) break;
      __builtin_amdgcn_s_sleep(SLP);
    }
    int d = (int)(v - t);
    d = max(d, __builtin_amdgcn_update_dpp(0, d, 0x121, 0xf, 0xf, false));
    d = max(d, __builtin_amdgcn_update_dpp(0, d, 0x122, 0xf, 0xf, false));
    d = max(d, __builtin_amdgcn_update_dpp(0, d, 0x124, 0xf, 0xf, false));
    d = max(d, __builtin_amdgcn_update_dpp(0, d, 0x128, 0xf, 0xf, false));
    r = t + __builtin_amdgcn_readfirstlane(d);
    sw[1 + (val & 2)] = r;
    *sw = val;
  } else {
    for (unsigned spins = 0; __builtin_amdgcn_readfirstlane(*sw) < val && spins <= 64u * SPIN_CAP; ++spins) __builtin_amdgcn_s_sleep(SLP_F);
    r = __builtin_amdgcn_readfirstlane(sw[1 + (val & 2)]);
  }
  asm volatile("" ::: "memory");
  return r;
}
template <int SLP, int WPL>
__device__ __forceinline__ bool wg_wait(const unsigned* f, int n, unsigned* tmo) {
  if (threadIdx.x < 64) (void)wait_n<SLP, WPL>(f, n, tmo);
  __syncthreads();
  asm volatile("" ::: "memory");
  return true;
}
__device__ __forceinline__ void set_flag(unsigned* f) { __hip_atomic_store((gu32*)f, 1u, RLX_AGENT); }
#define DRAIN() asm volatile("s_waitcnt vmcnt(0)" ::: "memory")
#define MKRS(ptr, bytes) __builtin_amdgcn_make_buffer_rsrc((void*)(ptr), 0, (int)(bytes), 0x00020000)
typedef unsigned v2u __attribute__((ext_vector_type(2)));
#define LD128(rs, voff, soff) __builtin_amdgcn_raw_buffer_load_b128(rs, voff, soff, AUX_SC1)
#define LD64(rs, voff, soff) __builtin_amdgcn_raw_buffer_load_b64(rs, voff, soff, AUX_SC1)
#define ST128(v, rs, voff, soff)                                              \
  do {                                                                        \
    const v4u st_d_ = (v);                                                    \
    __builtin_amdgcn_raw_buffer_store_b128(st_d_, rs, voff, soff, AUX_SC1);   \
    asm volatile("s_nop 1" ::"v"(st_d_) : "memory");                          \
  } while (0)
#define ST64(v, rs, voff, soff) __builtin_amdgcn_raw_buffer_store_b64(v, rs, voff, soff, AUX_SC1)
#define BF8(x) __builtin_bit_cast(bf16x8, x)
#define MFMA16(a, b, c) __builtin_amdgcn_mfma_f32_16x16x32_bf16(a, b, c, 0, 0, 0)
typedef short bf16x4 __attribute__((ext_vector_type(4)));
#define MFMA16K(a, b, c) \
  __builtin_amdgcn_mfma_f32_16x16x16bf16_1k(__builtin_bit_cast(bf16x4, a), __builtin_bit_cast(bf16x4, b), c, 0, 0, 0)
__device__ __forceinline__ float fmax_nn(float a, float b) { return __builtin_elementwise_maximum(a, b); }
typedef __bf16 bfv8 __attribute__((ext_vector_type(8)));
typedef __bf16 bfv16 __attribute__((ext_vector_type(16)));
typedef unsigned v8u __attribute__((ext_vector_type(8)));
#define SMF(a, b, c, idx) \
  __builtin_amdgcn_smfmac_f32_16x16x64_bf16(__builtin_bit_cast(bfv8, a), __builtin_bit_cast(bfv16, b), c, idx, 0, 0)
__device__ __forceinline__ float swap_add32(float a, float b) {
  auto r = __builtin_amdgcn_permlane32_swap(__float_as_uint(a), __float_as_uint(b), false, false);
  return __uint_as_float(r[0]) + __uint_as_float(r[1]);
}
__device__ __forceinline__ float swap_add16(float a, float b) {
  auto r = __builtin_amdgcn_permlane16_swap(__float_as_uint(a), __float_as_uint(b), false, false);
  return __uint_as_float(r[0]) + __uint_as_float(r[1]);
}
__device__ __forceinline__ v4u smf_fix(const v4u d) {
  auto x = __builtin_amdgcn_permlane16_swap(d.x, d.z, false, false);
  auto y = __builtin_amdgcn_permlane16_swap(d.y, d.w, false, false);
  return v4u{x[0], x[1], y[0], y[1]};
}
__device__ __forceinline__ v8u smf_bfrag(const u16* p) {
  const v2u a = *reinterpret_cast<const v2u*>(p), b = *reinterpret_cast<const v2u*>(p + 8);
  const v2u c = *reinterpret_cast<const v2u*>(p + 32), d = *reinterpret_cast<const v2u*>(p + 40);
  return v8u{a.x, b.x, a.y, b.y, c.x, d.x, c.y, d.y};
}

#define VAR_PPA 0
#define VAR_PPB 0
#define VAR_THR 0
#define VAR_THR2 0
#define CV_LEAD 100
#define VAR_EARLY 1
#define VAR_DRAIN 1
#define VAR_D1 0
#define VAR_D2 0
#define VAR_DR2 1
#define VAR_S1 0
#define VAR_S2 0
typedef __attribute__((address_space(1))) unsigned long long gu64;
typedef __attribute__((address_space(1))) char gch;
__device__ __forceinline__ int cvt_rpi(float x) {
  int r;
  asm("v_cvt_rpi_i32_f32 %0, %1" : "=v"(r) : "v"(x));
  return r;
}
typedef short v4s __attribute__((ext_vector_type(4)));
typedef __attribute__((address_space(3))) v4s* lp4s;
#define WNT 0
#define WPOL ""
__device__ __forceinline__ void glds16(const void* sbase, const unsigned voff, const unsigned lds) {
  unsigned keep;
  asm volatile("s_nop 4\n\ts_mov_b32 %0, m0\n\ts_mov_b32 m0, %3\n\ts_nop 0\n\tglobal_load_lds_dwordx4 %1, %2" WPOL "\n\ts_mov_b32 m0, %0"
               : "=&s"(keep)
               : "v"(voff), "s"(sbase), "s"(lds)
               : "memory");
}
__device__ __forceinline__ void glds16c(const void* sbase, const unsigned voff, const unsigned lds) {
  unsigned keep;
  asm volatile("s_nop 4\n\ts_mov_b32 %0, m0\n\ts_mov_b32 m0, %3\n\ts_nop 0\n\tglobal_load_lds_dwordx4 %1, %2 sc1\n\ts_mov_b32 m0, %0"
               : "=&s"(keep)
               : "v"(voff), "s"(sbase), "s"(lds)
               : "memory");
}
__device__ __forceinline__ v2u trd(const unsigned a) {
  return __builtin_bit_cast(v2u, __builtin_amdgcn_ds_read_tr16_b64_v4i16((lp4s)(unsigned long)a));
}
__device__ __forceinline__ unsigned lds_base(char* smem) { return (unsigned)(unsigned long)(__attribute__((address_space(3))) char*)smem; }
#define RAW_BAR()                                           \
  do {                                                      \
    asm volatile("s_waitcnt lgkmcnt(0)" ::: "memory");      \
    __builtin_amdgcn_s_barrier();                           \
    asm volatile("" ::: "memory");                          \
  } while (0)
#define WAIT_VM(n) asm volatile("s_waitcnt vmcnt(" #n ")" ::: "memory")
#define TOURS 7
#define MARK() asm volatile(".Lmark%=:" ::)
template <int ID, int MX = 0, class T = int>
__device__ __forceinline__ void pad(T&& x = 0) {
  if (TOURS && MX) asm volatile("s_branch 1f\n\t.p2alignl 12, 0xbf800000, %2\n.Lpad%1:\n\ts_setpc_b64 s[98:99]\n1:" : "+v"(x) : "n"(ID), "n"(MX));
  else if (TOURS) asm volatile("s_cmp_eq_u32 s0, 0xbe801d62\n\t.set .Lpad%1, . - 4" : "+v"(x) : "n"(ID) : "scc");
}
template <int ID>
__device__ __forceinline__ void hop() { asm volatile("s_call_b64 s[98:99], .Lpad%0" ::"n"(ID) : "s98", "s99"); }
template <int B, int E, int D>
__device__ __forceinline__ void hops() {
  if constexpr (B < E) { hop<B>(); hops<B + D, E, D>(); }
}
template <int B, int E, int D, int I = 0>
__device__ __forceinline__ void tour(const int w) {
  if constexpr ((TOURS & 2) && I < D) {
    if ((w & (D - 1)) == I) hops<B + I, E, D>();
    else tour<B, E, D, I + 1>(w);
  }
}
template <bool SP>
__device__ __forceinline__ void w_issue(const u16* Wm, const int j, const int b, const int i, const int h, const int w, const int l, const unsigned sb) {
  const char* base = (const char*)Wm + (size_t)(128 * j + 16 * w) * (HD * 2) + h * (DH * 2);
  const int r = l >> 4;
  const int f = SP ? ((r & 1) | ((i & 1) << 1) | (((i >> 1) & 1) << 2)) : (r | (((i >> 1) & 1) << 2));
  glds16(base, (unsigned)((4 * i + r) * (HD * 2) + (((l & 15) ^ (f << 1)) << 4)), sb + w * 16384 + b * 4096 + i * 1024);
}
#define WM(k) (WORD ? (k) % 3 : (k) >> 2)
#define WJ(k) (WORD ? (k) / 3 : (k) & 3)
template <int N>
__device__ __forceinline__ void wait_vm() { asm volatile("s_waitcnt vmcnt(%0)" ::"n"(N) : "memory"); }

#define PFW 1
#define IPF 1
#define PN1 1
#define PNH 20
#define PWO 1
__device__ __forceinline__ void pull(const unsigned long long b, const unsigned voff, char* smem) {
  unsigned keep;
  asm volatile("s_nop 4\n\ts_mov_b32 %0, m0\n\ts_mov_b32 m0, %3\n\ts_nop 0\n\tglobal_load_lds_dword %1, %2\n\ts_mov_b32 m0, %0"
               : "=&s"(keep)
               : "v"(voff), "s"(b), "s"(lds_base(smem) + 162816)
               : "memory");
}
__device__ __forceinline__ unsigned long long matrix(const MegaArgs& A, const int k) {
  unsigned long long pv = (unsigned long long)A.Wv, pk = (unsigned long long)A.Wk, pq = (unsigned long long)A.Wq;
  asm volatile("" : "+s"(pv), "+s"(pk), "+s"(pq));
  return k == 0 ? pv : k == 1 ? pk : pq;
}
__device__ __forceinline__ void pull_rows(const MegaArgs& A, char* smem, const int i) {
  const int l = lane_id(), y = (blockIdx.x & 7) >> 1;
  const unsigned long long m = matrix(A, i >> 7);
  if (PWO && i >= 384) pull((unsigned long long)A.Wo + ((8 * y + (i & 7)) * 16 + ((i & 127) >> 3)) * 8192, l * 128, smem);
  else pull(m + (i & 127) * (8 * HD) + y * 2048, (l >> 4) * (HD * 2) + (l & 15) * 128, smem);
}
__device__ __forceinline__ void wo_issue(const u16* Wo, const int a, const int st, const int w, const int i, const unsigned sb) {
  const int l = lane_id(), c = l & 15, g = l >> 4, fd = g | ((i & 1) << 2);
  const char* base = (const char*)Wo + ((size_t)((2 * a + (st >> 2)) * DH + 64 * (w & 1) + 16 * (st & 3)) * DM + 128 * (w >> 1)) * 2;
  glds16(base, (unsigned)((4 * i + g) * (DM * 2) + ((c ^ (fd << 1)) << 4)), sb + (st % 5) * 32768 + w * 4096 + i * 1024);
}
__device__ __forceinline__ bool zero_seen(const v4u t, const v2u e) {
  const unsigned long long m = (((unsigned long long)e.y << 32) | e.x) + TAG_STEP;
  const unsigned lo = (unsigned)m, hi = (unsigned)(m >> 32);
  return __all(((t.x ^ lo) | (t.y ^ hi) | (t.z ^ lo) | (t.w ^ hi)) == 0u);
}
__device__ __forceinline__ void zero_wait(char* ws) {
  const __amdgpu_buffer_rsrc_t rs = MKRS(ws + OFF_TAG, 8192 + 8);
#pragma unroll 1
  for (unsigned spins = 0; spins < 16u * SPIN_CAP; ++spins) {
    const v4u t = LD128(rs, lane_id() * 16, (blockIdx.x & 7) * 1024);
    const v2u e = LD64(rs, 0, 8192);
    if (zero_seen(t, e)) break;
    asm volatile("s_sleep 4" ::: "memory");
  }
  asm volatile("" ::: "memory");
}
__device__ __forceinline__ f32x2_t row_sums(const v4u raw) {
  float v[8];
#pragma unroll
  for (int e = 0; e < 4; ++e) { v[2 * e] = lo16(raw[e]); v[2 * e + 1] = hi16(raw[e]); }
  float s = 0.f;
#pragma unroll
  for (int e = 0; e < 8; ++e) s += v[e];
  const float mean = wave_sum(s) * (1.0f / DM);
  float q = 0.f;
#pragma unroll
  for (int e = 0; e < 8; ++e) { const float t = v[e] - mean; q += t * t; }
  return f32x2_t{mean, wave_sum(q)};
}
__device__ __forceinline__ unsigned row_stats(const f32x2_t s) {
  const float sd = bfr(sqrtf(bfr(bfr(s.y * (1.0f / DM)) + 1e-5f)));
  return (unsigned)f2bf(s.x) | ((unsigned)f2bf(sd) << 16);
}
__device__ __forceinline__ void sincos1(const float x, float* sn, float* cs) {
  const float t = __builtin_amdgcn_sinf(x * 0.07957747155f);
  *sn = __builtin_amdgcn_sinf(x * 0.1591549431f);
  *cs = __builtin_fmaf(-2.0f * t, t, 1.0f);
}
#define RUN_START                                                                                                   \
  const f32x2_t wra = {wr[0], wr[1]}, wia = {wi[0], wi[1]}, wrb = {wr[2], wr[3]}, wib = {wi[2], wi[3]};             \
  const f32x2_t pa = wra + wra, pb = wrb + wrb, qa = wra * wra + wia * wia, qb = wrb * wrb + wib * wib;             \
  f32x2_t ya = {yr0[0], yr0[1]}, yb = {yr0[2], yr0[3]};                                                             \
  f32x2_t za = ya * wra - f32x2_t{yi0[0], yi0[1]} * wia, zb = yb * wrb - f32x2_t{yi0[2], yi0[3]} * wib;
#define RUN_STEP                                                                                                    \
  {                                                                                                                 \
    const f32x2_t na = __builtin_elementwise_fma(pa, za, -(qa * ya)), nb = __builtin_elementwise_fma(pb, zb, -(qb * yb)); \
    ya = za; yb = zb; za = na; zb = nb;                                                                             \
  }
__device__ __forceinline__ unsigned pull_text(const int slot) {
  unsigned long long pc;
  int back, len;
  asm volatile("s_getpc_b64 %0\n0:\n\ts_mov_b32 %1, 0b-.Lcode_at\n\ts_max_i32 %2, 0, .Lcode_end-.Lcode_at" : "=s"(pc), "=s"(back), "=s"(len)::"scc");
  pc -= (long long)back;
  unsigned r;
  asm volatile("s_nop 4\n\tbuffer_load_dword %0, %1, %2, 0 offen" : "=v"(r) : "v"(lane_id() * 64 + slot * 4096),
               "s"(v4u{(unsigned)pc, (unsigned)(pc >> 32) & 0xffffu, (unsigned)len, 0x00020000u}) : "memory");
  return r;
}
__device__ __forceinline__ f32x2_t cmul(const f32x2_t z, const f32x2_t e) {
  return __builtin_elementwise_fma(f32x2_t{z.y, z.x}, f32x2_t{-e.y, e.y}, z * e.x);
}
__device__ __forceinline__ void put_row(v2u* p, f32x2_t (&b)[2], const f32x2_t (&z)[2], const f32x2_t (&m)[2]) {
  unsigned h0, h1, l0, l1, t0, t1;
  f32x2_t n0, n1;
  asm("v_cvt_pk_bf16_f32 %0, %8, %9\n\tv_cvt_pk_bf16_f32 %1, %10, %11\n\tv_dot2_f32_bf16 %2, %0, %16, %8\n\tv_dot2_f32_bf16 %4, %0, %17, %9\n\t"
      "v_dot2_f32_bf16 %3, %1, %16, %10\n\tv_dot2_f32_bf16 %5, %1, %17, %11\n\tv_pk_mul_f32 %6, %12, %14 op_sel_hi:[1,0]\n\tv_pk_mul_f32 %7, %13, %15 op_sel_hi:[1,0]\n\t"
      "v_pk_fma_f32 %6, %12, %14, %6 op_sel:[1,1,0] op_sel_hi:[0,1,1] neg_lo:[0,1,0]\n\tv_cvt_pk_bf16_f32 %2, %2, %4\n\t"
      "v_pk_fma_f32 %7, %13, %15, %7 op_sel:[1,1,0] op_sel_hi:[0,1,1] neg_lo:[0,1,0]\n\tv_cvt_pk_bf16_f32 %3, %3, %5"
      : "=&v"(h0), "=&v"(h1), "=&v"(l0), "=&v"(l1), "=&v"(t0), "=&v"(t1), "=&v"(n0), "=&v"(n1)
      : "v"(b[0].x), "v"(b[0].y), "v"(b[1].x), "v"(b[1].y), "v"(z[0]), "v"(z[1]), "v"(m[0]), "v"(m[1]), "s"(0x0000bf80u), "s"(0xbf800000u));
  p[0] = v2u{h0, h1};
  p[64] = v2u{l0, l1};
  b[0] = n0;
  b[1] = n1;
}
__device__ __forceinline__ void start_kgen(const MegaArgs& A, char* smem, const int hi, const int xi, const int aw) {
  const int tid = threadIdx.x;
  const int w = __builtin_amdgcn_readfirstlane(tid >> 6), l = tid & 63, d = 4 * hi + (w & 3);
  const unsigned t0 = AWE ? now10() : 0u;
  PSV;
  PST(0);
  v4u raw[8], ra, rb;
  unsigned txt = 0u;
  if (IPF) {
    unsigned long long pc;
    int len;
    asm volatile("s_getpc_b64 %0\n.Lcode_at:\n\ts_max_i32 %1, 0, .Lcode_end-.Lcode_at" : "=s"(pc), "=s"(len)::"scc");
    const unsigned slot = ((blockIdx.x >> 3) - 8) + 8 * w;
    if ((blockIdx.x >> 6) == 1) txt = __builtin_amdgcn_raw_buffer_load_b32(MKRS(pc, len), l * 64 + slot * 4096, 0, 0);
  }
  const int r0 = 16 * xi + 2 * w, sx = 2 * ((hi + 96) & 127) + w - 6;
  if (xi >= 0) {
    ra = *reinterpret_cast<const v4u*>(A.x0 + (size_t)r0 * DM + l * 8);
    rb = *reinterpret_cast<const v4u*>(A.x0 + (size_t)(r0 + 1) * DM + l * 8);
  }
  const int pn = w < 4 ? DM * NST * 2 : 0;
  const unsigned p1 = __builtin_amdgcn_raw_buffer_load_b32(MKRS(A.logAre, pn), l * 4, d * NST * 2, 0), p2 = __builtin_amdgcn_raw_buffer_load_b32(MKRS(A.Aim, pn), l * 4, d * NST * 2, 0),
                 p3 = __builtin_amdgcn_raw_buffer_load_b32(MKRS(A.Cre, pn), l * 4, d * NST * 2, 0), p4 = __builtin_amdgcn_raw_buffer_load_b32(MKRS(A.Cim, pn), l * 4, d * NST * 2, 0);
  const u16 p0 = A.logdt[d];
  if (w >= 6) {
#pragma unroll
    for (int i = 0; i < 8; ++i) raw[i] = *reinterpret_cast<const v4u*>(A.x0 + (size_t)(8 * sx + i) * DM + l * 8);
  }
  const v2u en = LD64(MKRS(A.ws + OFF_TAG, 8192 + 8), 0, 8192);
  {
    const __amdgpu_buffer_rsrc_t rz = MKRS(A.ws + OFF_Z, Z_BYTES);
    const unsigned i0 = hi < 16 ? ACC_LO : 0u, i1 = hi < 16 ? ACC_HI : 0u;
    ST128((v4u{i0, i1, i0, i1}), rz, tid * 16, hi * 8192);
    ST128((v4u{0u, 0u, 0u, 0u}), rz, tid * 16, (128 + hi) * 8192);
  }
  if (xi >= 0) {
    const f32x2_t sa = row_sums(ra), sb = row_sums(rb);
    const unsigned st = row_stats((l & 1) ? sb : sa);
    const unsigned long long m = (((unsigned long long)en.y << 32) | en.x) + TAG_STEP;
    ST128((v4u{st, (unsigned)m, st, (unsigned)(m >> 32)}), MKRS(A.ws + OFF_G0, 8192), (l >> 1) * 2048 + (l & 1) * 16, r0 * 16);
    SST(11);
  }
  const int pj = (blockIdx.x >> 3) - 8;
  const bool ps = PFW && w < 6 && pj < PNH;
  if (__builtin_expect(ps, 1)) {
    const bool sl = hi < 32;
    const unsigned voff = sl ? (l >> 2) * (HD * 2) + (l & 2) * 512 + (l & 1) * 128 : (l >> 4) * (HD * 2) + (l & 15) * 128;
#pragma unroll
    for (int e = 0; e < 2; ++e) {
      const int i = sl ? 4 * (pj + 8 * w + 48 * e) : pj + PNH * (w + 6 * e);
      const unsigned long long m = matrix(A, i >> 7);
      if (e < PN1 || __builtin_expect(sl, 1))
        pull(m + (i & 127) * (8 * HD) + (blockIdx.x & 6) * 1024 + (sl ? ((1 - (int)(m >> 8)) & 3) * 256 : 0), voff, smem);
    }
  }
  asm volatile("" ::"v"(txt), "v"(p0), "v"(p1), "v"(p2), "v"(p3), "v"(p4) : "memory");
#define CLEARED() do { if (!ps) WAIT_VM(0); else if (hi < 32) WAIT_VM(2); else wait_vm<PN1>(); __builtin_amdgcn_s_barrier(); } while (0)
  if (w >= 4) {
    tour<208, 220, 4>(w);
    PST(1);
    CLEARED();
    PST(2);
  }
  if (w >= 6) {
    __builtin_amdgcn_s_waitcnt(0x0F70);
    if (w == 7 && l < 8)
      __hip_atomic_store((gu64*)(A.ws + OFF_TAG) + l * 128 + hi, (((unsigned long long)en.y << 32) | en.x) + TAG_STEP, RLX_AGENT);
    __builtin_amdgcn_s_setprio(3);
    PST(8);
    v4u wr[32];
    const u16* wop = A.Wo + (size_t)(hi * DH + 64 * (w - 6)) * DM + l * 8;
    if (hi < 32) {
#pragma unroll
      for (int i = 0; i < 32; ++i) wr[i] = *reinterpret_cast<const v4u*>(wop + i * DM);
    }
    f32x2_t sq = {};
#pragma unroll
    for (int i = 0; i < 8; ++i) {
      if (i == 0) pad<203, 1000>(raw[i]);
      if (i == 3) { asm volatile("" : "+v"(sq), "+v"(raw[i])); pad<206>(sq); }
      if (i == 6) { asm volatile("" : "+v"(sq), "+v"(raw[i])); pad<207, 300>(sq); }
      const f32x2_t t = row_sums(raw[i]);
      if (l == i) sq = t;
    }
    pad<218, 500>(sq);
    const unsigned stw = row_stats(sq);
    if (l < 8) __hip_atomic_store((gu32*)(A.ws + OFF_GT) + 8 * sx + l, stw, RLX_AGENT);
    pad<216>();
    PST(9);
    float m = 0.f;
    if (hi < 32) {
      float c0 = 0.f, c1 = 0.f, c2 = 0.f, c3 = 0.f;
#pragma unroll
      for (int i = 0; i < 64; ++i) {
        const v4u x = wr[i & 31];
        if (i < 32) wr[i] = *reinterpret_cast<const v4u*>(wop + (32 + i) * DM);
        if (i == 20) pad<204, 600>(c0);
        c0 = dot2sq(x.x, c0); c1 = dot2sq(x.y, c1); c2 = dot2sq(x.z, c2); c3 = dot2sq(x.w, c3);
      }
      pad<205>(c0);
      m = wave_max_dpp(fmaxf(fmaxf(c0, c1), fmaxf(c2, c3)));
    }
    zero_wait(A.ws);
    DRAIN();
    PST(10);
    if (l < 4) {
      if (hi < 32) __hip_atomic_store((gu32*)(A.ws + OFF_FL) + FL_G + (l * 4 + w - 5) * 32 + hi, __float_as_uint(m) | 1u, RLX_AGENT);
      set_flag((unsigned*)(A.ws + OFF_FL) + FL_FS + (l * 32 + (sx >> 3)) * 32 + (sx & 7));
    }
    __builtin_amdgcn_s_setprio(0);
  } else if (w >= 4) {
    pad<202>();
    __builtin_amdgcn_s_setprio(3);
    if (PFW && hi >= 32 && pj < PNH)
      for (int i = pj + PNH * (6 * PN1 + w - 4); i < (PWO ? 512 : 384); i += 2 * PNH) pull_rows(A, smem, i);
    __builtin_amdgcn_s_setprio(0);
    PST(8);
    if (aw >= 0) {
      pad<219>();
      if (AWE) gate(t0 + AWE);
#pragma unroll 1
      for (int v = 0; v < 8; ++v)
#pragma unroll
        for (int i = 0; i < 4; ++i) wo_issue(A.Wo, aw, w & 1, v, i, lds_base(smem));
    }
    DRAIN();
  } else {
    pad<201, 300>();
    f32x2_t a[2], ew[3][2];
    const float dt = bfr(expf(bf2f(p0)));
#pragma unroll
    for (int e = 0; e < 2; ++e) {
      const float ar = -bfr(expf(e ? hi16(p1) : lo16(p1))) * dt, ai = (e ? hi16(p2) : lo16(p2)) * dt;
      const float th = ai * 0.15915494f, tl = __builtin_fmaf(ai, 0.15915494f, -th) + ai * 6.4206383e-9f;
      a[e] = f32x2_t{(e ? hi16(p3) : lo16(p3)) * dt, (e ? hi16(p4) : lo16(p4)) * dt} * 2.0f;
#pragma unroll
      for (int k = 0; k < 3; ++k) {
        const float st = k == 0 ? 1.0f : k == 1 ? -8.0f : 128.0f;
        const float ex = __expf(ar * fabsf(st)), t = __builtin_fmaf(tl, st, __builtin_amdgcn_fractf(th * st));
        ew[k][e] = f32x2_t{__builtin_amdgcn_cosf(t), __builtin_amdgcn_sinf(t)} * ex;
      }
    }
    PST(1);
    const int c = l & 15, g = l >> 4;
    v2u* wr = reinterpret_cast<v2u*>(smem + 65536 + w * 16640) + l;
    const bf16x8* rd = reinterpret_cast<const bf16x8*>(smem + 65536 + w * 16640) + 65 * c + g;
    unsigned* kd = reinterpret_cast<unsigned*>(smem + 139264) + w * 516;
    f32x2_t b[2] = {ew[1][0], ew[1][1]};
    wr[0] = v2u{0x3f80u, 0x3f80u};
    wr[64] = v2u{0u, 0u};
#pragma unroll
    for (int n = 1; n < 16; ++n) {
      if (n < 15) put_row(wr + 130 * n, b, b, ew[1]);
      else put_row(wr + 130 * n, b, a, ew[2]);
      if (n == 2) pad<217, 200>(b[0]);
      if (n == KBR) { PST(7); CLEARED(); PST(2); }
      if (n == 8) pad<208, 200>(b[0]);
    }
    pad<209, 200>(b[0]);
    bf16x8 tp[16];
#pragma unroll
    for (int i = 0; i < 16; ++i) tp[i] = rd[4 * i];
#pragma unroll
    for (int t = 0; t < 2; ++t) {
#pragma unroll
      for (int n = 0; n < 16; ++n) {
        const int cl = 2 * t + (n >> 3), mk = (8 >> cl) - 1;
        if (!(n & mk)) { a[0] = b[0]; a[1] = b[1]; }
        if (!(n & 7) && (cl == 1 || cl == 2)) { ew[0][0] = cmul(ew[0][0], ew[0][0]); ew[0][1] = cmul(ew[0][1], ew[0][1]); }
        if ((n + 1) & mk) put_row(wr + 130 * n, b, b, ew[0]);
        else put_row(wr + 130 * n, b, a, ew[2]);
        if (t == 0 && n == 6) pad<210, 200>(b[0]);
        if (t == 0 && n == 13) pad<211, 200>(b[0]);
        if (t == 1 && n == 0) pad<212, 200>(b[0]);
        if (t == 1 && n == 9) pad<213, 420>(b[0]);
      }
      bf16x8 x[16];
#pragma unroll
      for (int i = 0; i < 16; ++i) x[i] = rd[4 * i];
      __builtin_amdgcn_sched_barrier(0);
      f32x4 s0 = {}, s1 = {}, s2 = {};
      if (t == 1) pad<214, 200>(s0);
#pragma unroll
      for (int i = 0; i < 8; ++i) {
        s0 = MFMA16(tp[i], x[i], s0);
        s1 = MFMA16(tp[8 + i], x[i], s1);
        s2 = MFMA16(tp[i], x[8 + i], s2);
      }
      const int cl = 2 * t + (c >> 3), m = c & 7;
      const int ix = 128 * cl + ((m >> (3 - cl)) << (7 - cl)) + (m & ((8 >> cl) - 1)) + (g << (5 - cl));
#pragma unroll
      for (int r = 0; r < 4; ++r) {
        const float v = s0[r] + (s1[r] + s2[r]);
        kd[ix + (r << (3 - cl))] = pk2(v, v - lo16(pk2(v, v)));
      }
    }
    const float z = wave_sum_dpp(b[0].x + b[1].x);
    if (l == 0) kd[512] = pk2(z, z - lo16(pk2(z, z)));
    PST(4);
    const __amdgpu_buffer_rsrc_t rs = MKRS(A.ws + OFF_KF + d * (4 * CKN), 1960 * 2), rt = MKRS(A.ws + OFF_KF + d * (4 * CKN) + 2 * CKN, 1960 * 2);
    const unsigned kb = lds_base(smem) + 139264 + w * 2064 - 0x4300 * 4;
    float lag = (float)(2080 - 8 * l);
    pad<215, 800>(lag);
#pragma unroll 1
    for (int i = 0; i < 2; ++i) {
      unsigned k0[8], k1[8];
#pragma unroll
      for (int e = 0; e < 8; ++e) {
        const float f = lag - (float)(512 * (e >> 2) + 2 * (e & 3));
        const unsigned pb = pk2(f, f - 1.0f);
        unsigned a0, a1;
        asm("v_mad_u32_u16 %0, %2, 4, %3\n\tv_mad_u32_u16 %1, %2, 4, %3 op_sel:[1,0,0,0]" : "=&v"(a0), "=v"(a1) : "v"(pb), "s"(kb));
        k0[e] = *(lu32*)(unsigned long)a0;
        k1[e] = *(lu32*)(unsigned long)a1;
      }
#pragma unroll
      for (int q = 0; q < 2; ++q) {
        v4u oh, ol;
#pragma unroll
        for (int e = 0; e < 4; ++e) {
          oh[e] = __builtin_amdgcn_perm(k1[4 * q + e], k0[4 * q + e], 0x05040100u);
          ol[e] = __builtin_amdgcn_perm(k1[4 * q + e], k0[4 * q + e], 0x07060302u);
        }
        ST128(oh, rs, 16 * l + 2048 * i + 1024 * q, 0);
        ST128(ol, rt, 16 * l + 2048 * i + 1024 * q, 0);
      }
      lag -= 1024.0f;
    }
    PST(5);
    zero_wait(A.ws);
    DRAIN();
    if (l == 0) set_flag((unsigned*)(A.ws + OFF_FL) + FL_KG + (hi >> 1) * 32 + (d & 7));
  }
  PST(3);
  RAW_BAR();
  PST(6);
  PSE();
  pad<200, 700>();
  SST(3);
}

constexpr int C_SM = 131072, C_SO = C_SM + 8192, C_KT = C_SO + 2048, C_X = C_KT + 16384, C_SQ = C_X + 1024;
constexpr int C_SK = C_SQ + 2432;
static_assert(C_SK + 8 * QS * 2 <= 163808, "lds");
#define CHAIN_ASM_ATT \
  "s_mov_b32 s66, %[d0]\n" \
  "s_mov_b32 s94, 576\n" \
  "s_mov_b32 s95, 288\n" \
  "s_mov_b32 s84, 0x43400000\n" \
  "s_mov_b32 s85, 0x43400000\n" \
  "s_mov_b32 s92, 0x88000000\n" \
  "s_mov_b32 s93, 0x87fffff7\n" \
  "s_mov_b32 s78, 0x3e0293ee\n" \
  "s_mov_b32 s76, 0x33333333\n" \
  "s_mov_b32 s77, 0x33333333\n" \
  "s_add_u32 s58, s56, s60\n" \
  "s_addc_u32 s59, s57, 0\n" \
  "s_xor_b32 s38, s35, 0\n" \
  "s_add_u32 s26, s58, s38\n" \
  "s_addc_u32 s27, s59, 0\n" \
  "s_xor_b32 s38, s35, 1024\n" \
  "s_add_u32 s28, s58, s38\n" \
  "s_addc_u32 s29, s59, 0\n" \
  "s_xor_b32 s38, s35, 2048\n" \
  "s_add_u32 s30, s58, s38\n" \
  "s_addc_u32 s31, s59, 0\n" \
  "s_xor_b32 s38, s35, 3072\n" \
  "s_add_u32 s32, s58, s38\n" \
  "s_addc_u32 s33, s59, 0\n" \
  "s_lshr_b32 s98, s60, 2\n" \
  "s_add_u32 s98, s98, s74\n" \
  "s_add_u32 s98, s98, 131072\n" \
  "s_lshl_b32 s97, s73, 5\n" \
  "s_add_u32 s97, s97, s74\n" \
  "s_add_u32 s97, s97, 161152\n" \
  "s_lshl_b32 s99, s73, 10\n" \
  "s_mov_b32 s65, 9\n" \
  "v_mbcnt_lo_u32_b32 v48, -1, 0\n" \
  "v_mbcnt_hi_u32_b32 v48, -1, v48\n" \
  "v_and_b32 v49, 15, v48\n" \
  "v_lshl_add_u32 v9, v49, 1, s97\n" \
  "v_lshrrev_b32 v49, 4, v48\n" \
  "v_mad_u32_u24 v9, v49, s94, v9\n" \
  "v_add_u32 v7, s99, v1\n" \
  "v_mov_b64 v[12:13], 0\n" \
  "v_mov_b64 v[14:15], 0\n" \
  "v_mov_b64 v[16:17], 0\n" \
  "v_mov_b64 v[18:19], 0\n" \
  "v_mov_b64 v[20:21], 0\n" \
  "v_mov_b64 v[22:23], 0\n" \
  "s_lshl_b32 s88, s73, 10\n" \
  "v_add_u32 v24, s88, v1\n" \
  "ds_read_b128 v[32:35], v24 offset:0\n" \
  "s_waitcnt lgkmcnt(0)\n" \
  "v_mov_b32 v45, 0\n" \
  "v_dot2c_f32_bf16 v45, v32, v32\n" \
  "v_dot2c_f32_bf16 v45, v33, v33\n" \
  "v_dot2c_f32_bf16 v45, v34, v34\n" \
  "v_dot2c_f32_bf16 v45, v35, v35\n" \
  "s_add_u32 s88, s74, 157760\n" \
  "v_mov_b32 v44, s88\n" \
  "s_nop 0\n" \
  "v_cmpx_lt_f32 vcc, 2.0, v45\n" \
  "ds_max_u32 v44, v45\n" \
  "s_mov_b64 exec, -1\n" \
  "s_branch .LCA_AL_%=\n" \
  ".p2alignl 12, 0xbf800000, 3600\n" \
  ".p2align 6\n" \
  ".Lpad0x6d:\n" \
  "s_setpc_b64 s[98:99]\n" \
  ".p2align 6\n" \
  ".LCA_AL_%=:\n" \
  ".LCA_TOP_%=:\n" \
  "s_waitcnt lgkmcnt(0)\n" \
  "s_barrier\n" \
  "ds_read_b32 v8, v1 offset:26688\n" \
  "s_cmp_lg_u32 s73, s65\n" \
  "s_cbranch_scc1 .LCA_NOTK_%=\n" \
  "s_memrealtime s[38:39]\n" \
  "v_mov_b32 v26, 1\n" \
  "s_mul_i32 s23, s71, %[pp]\n" \
  "s_lshr_b32 s62, s66, 4\n" \
  "s_mov_b64 exec, 1\n" \
  "s_waitcnt lgkmcnt(0)\n" \
  "s_mov_b32 s67, s38\n" \
  "s_add_u32 s38, s38, s62\n" \
  "v_mov_b32 v25, s38\n" \
  "buffer_atomic_umax v25, off, s[16:19], s68\n" \
  "buffer_atomic_add v26, off, s[16:19], s68 offset:64\n" \
  "v_add_u32 v27, s23, v25\n" \
  "buffer_store_dword v27, off, s[16:19], s69 sc1\n" \
  "s_mov_b64 exec, -1\n" \
  "s_branch .LCA_SIDE_%=\n" \
  ".Lpad0x66:\n" \
  "s_setpc_b64 s[98:99]\n" \
  ".LCA_NOTK_%=:\n" \
  "s_cmp_lg_u32 s73, 0\n" \
  "s_cbranch_scc1 .LCA_NOZ_%=\n" \
  "v_mov_b64 v[26:27], s[92:93]\n" \
  "s_xor_b32 s88, s60, 0x8000\n" \
  "s_add_u32 s88, s88, s61\n" \
  "buffer_store_dwordx2 v[26:27], v5, s[40:43], s88 offen sc1\n" \
  ".LCA_NOZ_%=:\n" \
  "s_nop 7\n" \
  ".LCA_SIDE_%=:\n" \
  "s_waitcnt lgkmcnt(0)\n" \
  "v_readfirstlane_b32 s86, v8\n" \
  "s_mov_b64 vcc, s[76:77]\n" \
  "s_lshr_b32 s86, s86, 23\n" \
  "s_add_i32 s86, s86, s83\n" \
  "s_ashr_i32 s86, s86, 1\n" \
  "s_max_i32 s86, s86, s82\n" \
  "s_min_i32 s86, s86, 100\n" \
  "s_lshl_b32 s86, s86, 23\n" \
  "s_add_i32 s75, s86, 0x3c800000\n" \
  "s_sub_u32 s38, s71, 1\n" \
  "s_cmp_ge_u32 s38, 62\n" \
  "ds_read_b128 v[32:35], v1 offset:0\n" \
  "ds_read_b128 v[36:39], v1 offset:1024\n" \
  "ds_read_b128 v[48:51], v1 offset:2048\n" \
  "ds_read_b128 v[52:55], v1 offset:3072\n" \
  "ds_read_b128 v[40:43], v1 offset:4096\n" \
  "ds_read_b128 v[56:59], v1 offset:5120\n" \
  "ds_read_b128 v[60:63], v1 offset:6144\n" \
  "ds_read_b128 v[44:47], v1 offset:7168\n" \
  "s_setprio 3\n" \
  "s_waitcnt lgkmcnt(7)\n" \
  "v_smfmac_f32_16x16x64_bf16 v[12:15], v[32:35], v[64:71], v0\n" \
  "s_waitcnt lgkmcnt(6)\n" \
  "v_smfmac_f32_16x16x64_bf16 v[12:15], v[36:39], v[72:79], v0\n" \
  "s_waitcnt lgkmcnt(5)\n" \
  "v_smfmac_f32_16x16x64_bf16 v[12:15], v[48:51], v[80:87], v0\n" \
  "s_waitcnt lgkmcnt(4)\n" \
  "v_smfmac_f32_16x16x64_bf16 v[12:15], v[52:55], v[88:95], v0\n" \
  "s_waitcnt lgkmcnt(3)\n" \
  "v_smfmac_f32_16x16x64_bf16 v[12:15], v[40:43], v[96:103], v0\n" \
  "s_waitcnt lgkmcnt(2)\n" \
  "v_smfmac_f32_16x16x64_bf16 v[12:15], v[56:59], v[104:111], v0\n" \
  "s_waitcnt lgkmcnt(1)\n" \
  "v_smfmac_f32_16x16x64_bf16 v[12:15], v[60:63], v[112:119], v0\n" \
  "s_waitcnt lgkmcnt(0)\n" \
  "v_smfmac_f32_16x16x64_bf16 v[12:15], v[44:47], v[120:127], v0\n" \
  "ds_read_b128 v[24:27], v1 offset:10240\n" \
  "ds_read_b128 v[28:31], v1 offset:14336\n" \
  "s_nop 5\n" \
  "v_add_f32 v12, v12, v14\n" \
  "v_add_f32 v13, v13, v15\n" \
  "v_cvt_pk_bf16_f32 v12, v12, v13\n" \
  "ds_write_b16 v9, v12 offset:0\n" \
  "ds_write_b16_d16_hi v9, v12 offset:288\n" \
  "s_setprio 0\n" \
  "s_barrier\n" \
  "v_smfmac_f32_16x16x64_bf16 v[16:19], v[32:35], v[128:135], v0\n" \
  "v_smfmac_f32_16x16x64_bf16 v[20:23], v[32:35], v[192:199], v0\n" \
  "ds_read_b128 v[32:35], v1 offset:11264\n" \
  "v_smfmac_f32_16x16x64_bf16 v[16:19], v[36:39], v[136:143], v0\n" \
  "v_smfmac_f32_16x16x64_bf16 v[20:23], v[36:39], v[200:207], v0\n" \
  "ds_read_b128 v[36:39], v1 offset:15360\n" \
  "v_smfmac_f32_16x16x64_bf16 v[16:19], v[48:51], v[144:151], v0\n" \
  "v_smfmac_f32_16x16x64_bf16 v[20:23], v[48:51], v[208:215], v0\n" \
  "v_smfmac_f32_16x16x64_bf16 v[16:19], v[52:55], v[152:159], v0\n" \
  "v_smfmac_f32_16x16x64_bf16 v[20:23], v[52:55], v[216:223], v0\n" \
  "v_smfmac_f32_16x16x64_bf16 v[16:19], v[40:43], v[160:167], v0\n" \
  "v_smfmac_f32_16x16x64_bf16 v[20:23], v[40:43], v[224:231], v0\n" \
  ".fill ((24 - (. - .LCA_AL_%=)) & 31) >> 2, 4, 0xbf800000\n" \
  "s_waitcnt lgkmcnt(2)\n" \
  "s_barrier\n" \
  "s_setprio 3\n" \
  "ds_read_b128 v[48:51], v2 offset:0\n" \
  "ds_read_b128 v[52:55], v2 offset:64\n" \
  "v_smfmac_f32_16x16x64_bf16 v[16:19], v[56:59], v[168:175], v0\n" \
  "v_smfmac_f32_16x16x64_bf16 v[20:23], v[56:59], v[232:239], v0\n" \
  "ds_read_b128 v[56:59], v2 offset:128\n" \
  "s_cbranch_scc1 .LCA_LAST_%=\n" \
  ".LCA_BACK_%=:\n" \
  "s_waitcnt lgkmcnt(2)\n" \
  "v_mfma_f32_16x16x32_bf16 v[12:15], v[24:27], v[48:51], 0\n" \
  "ds_read_b128 v[24:27], v1 offset:12288\n" \
  "v_mfma_f32_16x16x32_bf16 v[8:11], v[28:31], v[48:51], 0\n" \
  "ds_read_b128 v[28:31], v1 offset:16384\n" \
  "v_mfma_f32_16x16x32_bf16 v[40:43], v[48:51], v[48:51], 0\n" \
  "ds_read_b128 v[48:51], v7 offset:18432\n" \
  "v_smfmac_f32_16x16x64_bf16 v[16:19], v[60:63], v[176:183], v0\n" \
  "v_smfmac_f32_16x16x64_bf16 v[20:23], v[60:63], v[240:247], v0\n" \
  "ds_read_b128 v[60:63], v2 offset:192\n" \
  "s_waitcnt lgkmcnt(5)\n" \
  "v_mfma_f32_16x16x32_bf16 v[12:15], v[32:35], v[52:55], v[12:15]\n" \
  "ds_read_b128 v[32:35], v1 offset:13312\n" \
  "v_mfma_f32_16x16x32_bf16 v[8:11], v[36:39], v[52:55], v[8:11]\n" \
  "ds_read_b128 v[36:39], v1 offset:17408\n" \
  "v_mfma_f32_16x16x32_bf16 v[40:43], v[52:55], v[52:55], v[40:43]\n" \
  "ds_read_b128 v[52:55], v7 offset:22528\n" \
  "s_waitcnt lgkmcnt(7)\n" \
  "v_mfma_f32_16x16x32_bf16 v[40:43], v[56:59], v[56:59], v[40:43]\n" \
  "s_waitcnt lgkmcnt(5)\n" \
  "v_mfma_f32_16x16x32_bf16 v[12:15], v[24:27], v[56:59], v[12:15]\n" \
  "ds_read_b128 v[24:27], v6 offset:2048\n" \
  "v_mfma_f32_16x16x32_bf16 v[8:11], v[28:31], v[56:59], v[8:11]\n" \
  "ds_read_b128 v[28:31], v6 offset:3072\n" \
  "s_waitcnt lgkmcnt(5)\n" \
  "v_mfma_f32_16x16x32_bf16 v[40:43], v[60:63], v[60:63], v[40:43]\n" \
  "ds_read_b128 v[56:59], v6 offset:0\n" \
  "s_waitcnt lgkmcnt(4)\n" \
  "v_mfma_f32_16x16x32_bf16 v[12:15], v[32:35], v[60:63], v[12:15]\n" \
  "ds_read_b128 v[32:35], v6 offset:8192\n" \
  "v_mfma_f32_16x16x32_bf16 v[8:11], v[36:39], v[60:63], v[8:11]\n" \
  "ds_read_b128 v[36:39], v6 offset:9216\n" \
  "v_smfmac_f32_16x16x64_bf16 v[16:19], v[44:47], v[184:191], v0\n" \
  "ds_read_b128 v[60:63], v6 offset:1024\n" \
  "s_waitcnt lgkmcnt(6)\n" \
  "v_smfmac_f32_16x16x64_bf16 v[20:23], v[44:47], v[248:255], v0\n" \
  "v_cndmask_b32_dpp v43, v43, v42, vcc quad_perm:[0,1,0,1] row_mask:0xf bank_mask:0xf\n" \
  "v_mov_b32 v41, 0\n" \
  "v_mov_b32 v42, 0\n" \
  "v_cndmask_b32_dpp v12, v8, v12, vcc quad_perm:[0,1,0,1] row_mask:0xf bank_mask:0xf\n" \
  "v_cndmask_b32_dpp v13, v9, v13, vcc quad_perm:[0,1,0,1] row_mask:0xf bank_mask:0xf\n" \
  "v_cndmask_b32_dpp v14, v10, v14, vcc quad_perm:[0,1,0,1] row_mask:0xf bank_mask:0xf\n" \
  "v_cndmask_b32_dpp v15, v11, v15, vcc quad_perm:[0,1,0,1] row_mask:0xf bank_mask:0xf\n" \
  "v_maximum3_f32 v8, v43, v12, v13\n" \
  "v_maximum3_f32 v8, v8, v14, v15\n" \
  "v_add_f32 v16, v16, v18\n" \
  "v_add_f32 v17, v17, v19\n" \
  "v_max_f32_dpp v8, v8, v8 quad_perm:[2,3,0,1] row_mask:0xf bank_mask:0xf\n" \
  "v_mov_b32 v9, v8\n" \
  "v_add_f32 v20, v20, v22\n" \
  "v_add_f32 v21, v21, v23\n" \
  "v_permlane16_swap_b32 v8, v9\n" \
  "v_max_f32 v8, v8, v9\n" \
  "v_mov_b32 v9, v8\n" \
  "v_cvt_pk_bf16_f32 v16, v16, v17\n" \
  "v_cvt_pk_bf16_f32 v17, v20, v21\n" \
  "v_permlane32_swap_b32 v8, v9\n" \
  "v_max_f32 v8, v8, v9\n" \
  "s_barrier\n" \
  "buffer_load_dword v7, v5, s[16:19], s24 offen sc1\n" \
  "v_mul_f32 v8, s78, v8\n" \
  "v_pk_fma_f32 v[12:13], v[12:13], s[78:79], v[8:9] op_sel_hi:[1,0,0] neg_lo:[0,0,1] neg_hi:[0,0,1]\n" \
  "v_pk_fma_f32 v[14:15], v[14:15], s[78:79], v[8:9] op_sel_hi:[1,0,0] neg_lo:[0,0,1] neg_hi:[0,0,1]\n" \
  "v_fma_f32 v43, v43, s78, -v8\n" \
  "v_exp_f32 v12, v12\n" \
  "v_exp_f32 v13, v13\n" \
  "v_exp_f32 v14, v14\n" \
  "v_exp_f32 v15, v15\n" \
  "v_exp_f32 v43, v43\n" \
  "v_cvt_pk_bf16_f32 v44, v12, v13\n" \
  "v_cvt_pk_bf16_f32 v45, v14, v15\n" \
  "v_pk_add_f32 v[18:19], v[12:13], v[14:15]\n" \
  "v_mov_b32_dpp v46, v44 quad_perm:[2,3,0,1] row_mask:0xf bank_mask:0xf\n" \
  "v_mov_b32_dpp v9, v43 quad_perm:[2,3,0,1] row_mask:0xf bank_mask:0xf\n" \
  "v_mov_b32_dpp v47, v45 quad_perm:[2,3,0,1] row_mask:0xf bank_mask:0xf\n" \
  "v_add_f32 v10, v18, v43\n" \
  "v_cvt_pk_bf16_f32 v40, v43, v9\n" \
  "v_add_f32 v10, v10, v19\n" \
  "v_mfma_f32_16x16x32_bf16 v[12:15], v[48:51], v[44:47], 0\n" \
  "v_cvt_pk_bf16_f32 v43, v43, v9\n" \
  "v_add_f32_dpp v10, v10, v10 quad_perm:[2,3,0,1] row_mask:0xf bank_mask:0xf\n" \
  "v_mfma_f32_16x16x32_bf16 v[20:23], v[52:55], v[44:47], 0\n" \
  "v_mul_f32 v11, s75, v10\n" \
  "v_mul_f32 v10, s75, v10\n" \
  "v_mfma_f32_16x16x16_bf16 v[12:15], v[16:17], v[40:41], v[12:15]\n" \
  "ds_read_b128 v[48:51], v6 offset:10240\n" \
  "v_permlane16_swap_b32 v10, v11\n" \
  "v_add_f32 v10, v10, v11\n" \
  "v_mfma_f32_16x16x16_bf16 v[20:23], v[16:17], v[42:43], v[20:23]\n" \
  "v_mov_b32 v11, v10\n" \
  "ds_read_b128 v[52:55], v6 offset:11264\n" \
  "v_mov_b64 v[18:19], s[84:85]\n" \
  "v_permlane32_swap_b32 v10, v11\n" \
  "v_add_f32 v10, v10, v11\n" \
  "v_rcp_f32 v10, v10\n" \
  "s_mov_b32 s0, 0xbe801d62\n" \
  ".set .Lpad0x6e, . - 4\n" \
  "v_pk_mul_f32 v[12:13], v[12:13], v[10:11] op_sel_hi:[1,0]\n" \
  "v_pk_mul_f32 v[14:15], v[14:15], v[10:11] op_sel_hi:[1,0]\n" \
  "v_pk_mul_f32 v[20:21], v[20:21], v[10:11] op_sel_hi:[1,0]\n" \
  "v_pk_mul_f32 v[22:23], v[22:23], v[10:11] op_sel_hi:[1,0]\n" \
  "v_cvt_pk_bf16_f32 v40, v12, v13\n" \
  "v_cvt_pk_bf16_f32 v41, v14, v15\n" \
  "v_cvt_pk_bf16_f32 v42, v20, v21\n" \
  "ds_write_b64 v4, v[40:41]\n" \
  "v_cvt_pk_bf16_f32 v43, v22, v23\n" \
  "ds_write_b64 v4, v[42:43] offset:1024\n" \
  "v_max_u32 v16, 1, v16\n" \
  "v_max_u32 v17, 1, v17\n" \
  "buffer_store_dwordx2 v[16:17], v3, s[52:55], s70 offen sc1\n" \
  "v_mov_b64 v[12:13], s[84:85]\n" \
  "v_mov_b64 v[14:15], s[84:85]\n" \
  "v_mov_b64 v[16:17], s[84:85]\n" \
  "v_mov_b64 v[20:21], s[84:85]\n" \
  "v_mov_b64 v[22:23], s[84:85]\n" \
  "v_mov_b64 v[8:9], s[84:85]\n" \
  "v_mov_b64 v[10:11], s[84:85]\n" \
  "s_setprio 0\n" \
  "s_waitcnt lgkmcnt(0)\n" \
  "s_barrier\n" \
  "ds_read_b128 v[40:43], v1 offset:8192\n" \
  "ds_read_b128 v[44:47], v1 offset:9216\n" \
  "s_waitcnt lgkmcnt(1)\n" \
  "v_smfmac_f32_16x16x64_bf16 v[12:15], v[40:43], v[56:63], v0\n" \
  "s_waitcnt lgkmcnt(0)\n" \
  "v_smfmac_f32_16x16x64_bf16 v[12:15], v[44:47], v[32:39], v0\n" \
  "ds_read_b128 v[56:59], v6 offset:4096\n" \
  "ds_read_b128 v[60:63], v6 offset:5120\n" \
  "v_smfmac_f32_16x16x64_bf16 v[16:19], v[40:43], v[24:31], v0\n" \
  "ds_read_b128 v[32:35], v6 offset:12288\n" \
  "ds_read_b128 v[36:39], v6 offset:13312\n" \
  "v_smfmac_f32_16x16x64_bf16 v[16:19], v[44:47], v[48:55], v0\n" \
  "ds_read_b128 v[24:27], v6 offset:6144\n" \
  "ds_read_b128 v[28:31], v6 offset:7168\n" \
  "v_add_f32 v12, v12, v14\n" \
  "v_add_f32 v13, v13, v15\n" \
  "global_atomic_add_x2 v5, v[12:13], s[26:27]\n" \
  "ds_read_b128 v[48:51], v6 offset:14336\n" \
  "ds_read_b128 v[52:55], v6 offset:15360\n" \
  "s_waitcnt lgkmcnt(6)\n" \
  "v_add_f32 v16, v16, v18\n" \
  "v_add_f32 v17, v17, v19\n" \
  "global_atomic_add_x2 v5, v[16:17], s[28:29]\n" \
  "v_smfmac_f32_16x16x64_bf16 v[20:23], v[40:43], v[56:63], v0\n" \
  "s_waitcnt lgkmcnt(4)\n" \
  "v_smfmac_f32_16x16x64_bf16 v[20:23], v[44:47], v[32:39], v0\n" \
  "s_waitcnt lgkmcnt(2)\n" \
  "v_smfmac_f32_16x16x64_bf16 v[8:11], v[40:43], v[24:31], v0\n" \
  "s_waitcnt lgkmcnt(0)\n" \
  "v_smfmac_f32_16x16x64_bf16 v[8:11], v[44:47], v[48:55], v0\n" \
  "s_nop 4\n" \
  "v_add_f32 v20, v20, v22\n" \
  "v_add_f32 v21, v21, v23\n" \
  "global_atomic_add_x2 v5, v[20:21], s[30:31]\n" \
  "v_add_f32 v8, v8, v10\n" \
  "v_add_f32 v9, v9, v11\n" \
  "global_atomic_add_x2 v5, v[8:9], s[32:33]\n" \
  "s_memrealtime s[62:63]\n" \
  "v_mbcnt_lo_u32_b32 v48, -1, 0\n" \
  "v_mbcnt_hi_u32_b32 v48, -1, v48\n" \
  "v_lshrrev_b32 v49, 3, v48\n" \
  "v_and_b32 v50, 7, v48\n" \
  "v_lshlrev_b32 v50, 4, v50\n" \
  "v_lshl_or_b32 v44, v49, 8, v50\n" \
  "s_add_u32 s20, s20, 32\n" \
  "s_mov_b32 s21, 0x8000\n" \
  "v_mov_b32 v52, s22\n" \
  "v_mov_b32 v54, s20\n" \
  "s_mul_i32 s88, s71, %[pp]\n" \
  "s_lshl_b32 s89, s71, 10\n" \
  "s_sub_u32 s88, s88, s25\n" \
  "s_mul_i32 s38, s24, %[an]\n" \
  "s_cmp_gt_u32 s89, s38\n" \
  "s_cselect_b32 s89, 0x1000, 0\n" \
  "s_waitcnt vmcnt(5)\n" \
  "v_mov_b32 v46, v7\n" \
  "s_branch .LCA_TKE_%=\n" \
  ".LCA_TKR_%=:\n" \
  "s_cmp_lg_u32 s73, 0\n" \
  "s_cbranch_scc1 .LCA_TKW_%=\n" \
  "s_sub_u32 s21, s21, 1\n" \
  "s_cbranch_scc1 .LCA_TKO_%=\n" \
  "s_and_b32 s38, s21, 7\n" \
  "s_cmp_lg_u32 s38, 7\n" \
  "s_cbranch_scc1 .LCA_TKS_%=\n" \
  "buffer_load_dword v46, v5, s[16:19], s24 offen sc1\n" \
  ".LCA_TKS_%=:\n" \
  "s_sleep 1\n" \
  ".LCA_TKE_%=:\n" \
  "v_readlane_b32 s38, v46, 8\n" \
  "v_readlane_b32 s39, v46, 24\n" \
  "v_readlane_b32 s23, v46, 40\n" \
  "v_readlane_b32 s34, v46, 56\n" \
  "s_add_u32 s38, s38, s39\n" \
  "s_add_u32 s23, s23, s34\n" \
  "s_add_u32 s38, s38, s23\n" \
  "s_cmp_lt_u32 s38, s20\n" \
  "s_cbranch_scc1 .LCA_TKR_%=\n" \
  ".LCA_TKO_%=:\n" \
  "v_readlane_b32 s34, v46, 0\n" \
  "v_readlane_b32 s38, v46, 16\n" \
  "v_readlane_b32 s39, v46, 32\n" \
  "v_readlane_b32 s23, v46, 48\n" \
  "s_max_u32 s34, s34, s38\n" \
  "s_max_u32 s39, s39, s23\n" \
  "s_add_u32 s34, s34, %[cd]\n" \
  "v_readlane_b32 s23, v46, 1\n" \
  "s_max_u32 s34, s34, s39\n" \
  "s_sub_u32 s23, s23, s34\n" \
  "s_sub_u32 s23, s23, s88\n" \
  "s_cmp_lt_u32 s23, s89\n" \
  "s_cselect_b32 s23, s23, 0\n" \
  "s_min_u32 s23, s23, %[ka]\n" \
  "s_add_u32 s34, s34, s23\n" \
  "s_add_u32 s34, s34, s64\n" \
  ".LCA_TKD_%=:\n" \
  "v_mov_b32 v55, s34\n" \
  "ds_write_b64 v52, v[54:55]\n" \
  "s_branch .LCA_WAIU_%=\n" \
  ".Lpad0x6f:\n" \
  "s_setpc_b64 s[98:99]\n" \
  ".LCA_WAIS_%=:\n" \
  "s_cmp_lt_i32 s39, -1000\n" \
  "s_cbranch_scc1 .LCA_WAIE_%=\n" \
  "s_sleep 1\n" \
  ".LCA_WAIU_%=:\n" \
  "s_memrealtime s[38:39]\n" \
  "s_waitcnt lgkmcnt(0)\n" \
  "s_sub_i32 s39, s38, s34\n" \
  "s_cmp_lt_i32 s39, -7\n" \
  "s_cbranch_scc1 .LCA_WAIS_%=\n" \
  "s_cmp_lt_i32 s39, 0\n" \
  "s_cbranch_scc1 .LCA_WAIU_%=\n" \
  ".LCA_WAIE_%=:\n" \
  "buffer_load_dwordx4 v[24:27], v44, s[40:43], s60 offen sc1\n" \
  "buffer_load_dwordx4 v[28:31], v44, s[40:43], s60 offen offset:2048 sc1\n" \
  "s_sub_u32 s62, s62, s67\n" \
  "s_lshl_b32 s62, s62, 4\n" \
  "s_sub_i32 s62, s62, s66\n" \
  "s_min_i32 s62, s62, 128\n" \
  "s_max_i32 s62, s62, -256\n" \
  "s_ashr_i32 s62, s62, 2\n" \
  "s_add_i32 s66, s66, s62\n" \
  "s_add_u32 s70, s70, 0x10000\n" \
  "v_mbcnt_lo_u32_b32 v48, -1, 0\n" \
  "v_mbcnt_hi_u32_b32 v48, -1, v48\n" \
  "v_and_b32 v49, 15, v48\n" \
  "v_lshl_add_u32 v9, v49, 1, s97\n" \
  "v_lshrrev_b32 v49, 4, v48\n" \
  "v_mad_u32_u24 v9, v49, s94, v9\n" \
  "v_add_u32 v7, s99, v1\n" \
  "v_mov_b64 v[12:13], 0\n" \
  "v_mov_b64 v[14:15], 0\n" \
  "v_mov_b64 v[16:17], 0\n" \
  "v_mov_b64 v[18:19], 0\n" \
  "v_mov_b64 v[20:21], 0\n" \
  "v_mov_b64 v[22:23], 0\n" \
  "v_and_b32 v49, 3, v48\n" \
  "v_lshl_add_u32 v40, v49, 2, s98\n" \
  "v_lshrrev_b32 v49, 2, v48\n" \
  "v_bfe_u32 v48, v48, 4, 1\n" \
  "v_xor_b32 v49, v49, v48\n" \
  "v_lshl_add_u32 v40, v49, 5, v40\n" \
  "s_add_i32 s80, s86, 0x35000000\n" \
  "s_sub_u32 s91, 0x4a350000, s86\n" \
  "s_min_u32 s91, s91, 0x4ca00000\n" \
  "s_mov_b32 s89, s60\n" \
  "s_xor_b32 s60, s60, 0x80\n" \
  "s_bitcmp0_b32 s60, 7\n" \
  "s_cselect_b32 s88, 0x8000, 0\n" \
  "s_add_u32 s60, s60, s88\n" \
  "s_and_b32 s60, s60, 0xffff\n" \
  "s_add_u32 s58, s56, s60\n" \
  "s_addc_u32 s59, s57, 0\n" \
  "s_xor_b32 s38, s35, 0\n" \
  "s_add_u32 s26, s58, s38\n" \
  "s_addc_u32 s27, s59, 0\n" \
  "s_xor_b32 s38, s35, 1024\n" \
  "s_add_u32 s28, s58, s38\n" \
  "s_addc_u32 s29, s59, 0\n" \
  "s_xor_b32 s38, s35, 2048\n" \
  "s_add_u32 s30, s58, s38\n" \
  "s_addc_u32 s31, s59, 0\n" \
  "s_xor_b32 s38, s35, 3072\n" \
  "s_add_u32 s32, s58, s38\n" \
  "s_addc_u32 s33, s59, 0\n" \
  "s_sub_u32 s71, s71, 1\n" \
  "s_mov_b32 s87, 0x40000\n" \
  "s_branch .LCA_CHK_%=\n" \
  ".LCA_POLL_%=:\n" \
  "buffer_load_dwordx4 v[24:27], v44, s[40:43], s89 offen sc1\n" \
  "buffer_load_dwordx4 v[28:31], v44, s[40:43], s89 offen offset:2048 sc1\n" \
  ".LCA_CHK_%=:\n" \
  "s_sub_u32 s87, s87, 1\n" \
  "s_cbranch_scc1 .LCA_END_%=\n" \
  "s_waitcnt vmcnt(1)\n" \
  "v_cvt_f32_i32 v32, v24\n" \
  "v_cvt_f32_i32 v33, v25\n" \
  "v_cvt_f32_i32 v34, v26\n" \
  "v_cvt_f32_i32 v35, v27\n" \
  "v_pk_mul_f32 v[24:25], v[32:33], s[80:81] op_sel_hi:[1,0]\n" \
  "v_pk_mul_f32 v[26:27], v[34:35], s[80:81] op_sel_hi:[1,0]\n" \
  "v_max3_f32 v45, |v32|, |v33|, |v34|\n" \
  "v_cvt_pk_bf16_f32 v24, v24, v26\n" \
  "v_cvt_pk_bf16_f32 v25, v25, v27\n" \
  "ds_write2_b32 v40, v24, v25 offset0:0 offset1:4\n" \
  "v_max3_f32 v45, |v45|, |v35|, |v35|\n" \
  "s_waitcnt vmcnt(0)\n" \
  "v_cvt_f32_i32 v32, v28\n" \
  "v_cvt_f32_i32 v33, v29\n" \
  "v_cvt_f32_i32 v34, v30\n" \
  "v_cvt_f32_i32 v35, v31\n" \
  "v_pk_mul_f32 v[28:29], v[32:33], s[80:81] op_sel_hi:[1,0]\n" \
  "v_pk_mul_f32 v[30:31], v[34:35], s[80:81] op_sel_hi:[1,0]\n" \
  "v_max3_f32 v45, |v45|, |v32|, |v33|\n" \
  "v_cvt_pk_bf16_f32 v28, v28, v30\n" \
  "v_cvt_pk_bf16_f32 v29, v29, v31\n" \
  "ds_write2_b32 v40, v28, v29 offset0:128 offset1:132\n" \
  "v_max3_f32 v45, |v45|, |v34|, |v35|\n" \
  "v_cmp_lt_f32 vcc, s91, v45\n" \
  "s_cbranch_vccnz .LCA_BIG_%=\n" \
  ".LCA_SML_%=:\n" \
  "s_branch .LCA_TOP_%=\n" \
  ".Lpad0x6b:\n" \
  "s_setpc_b64 s[98:99]\n" \
  ".LCA_BIG_%=:\n" \
  "v_cmp_lt_f32 vcc, 0x4ca00000, v45\n" \
  "s_cbranch_vccnz .LCA_POLL_%=\n" \
  "v_mul_f32 v45, s80, v45\n" \
  "v_mul_f32 v45, v45, v45\n" \
  "s_add_u32 s88, s74, 157760\n" \
  "v_mov_b32 v44, s88\n" \
  "s_nop 0\n" \
  "v_cmpx_lt_f32 vcc, 2.0, v45\n" \
  "ds_max_u32 v44, v45\n" \
  "s_mov_b64 exec, -1\n" \
  "s_branch .LCA_SML_%=\n" \
  ".LCA_TKW_%=:\n" \
  "s_andn2_b32 s38, s22, 24\n" \
  "v_mov_b32 v53, s38\n" \
  ".LCA_TKX_%=:\n" \
  "s_sleep 1\n" \
  "ds_read_b64 v[46:47], v53\n" \
  "s_sub_u32 s21, s21, 1\n" \
  "s_cbranch_scc1 .LCA_TKD_%=\n" \
  "s_waitcnt lgkmcnt(0)\n" \
  "v_readfirstlane_b32 s38, v46\n" \
  "v_readfirstlane_b32 s34, v47\n" \
  "s_cmp_eq_u32 s38, s20\n" \
  "s_cbranch_scc0 .LCA_TKX_%=\n" \
  "s_branch .LCA_TKD_%=\n" \
  ".LCA_FIRST_%=:\n" \
  "ds_read_b64 v[10:11], v1 offset:26624\n" \
  "ds_read_b128 v[24:27], v1 offset:10240\n" \
  "ds_read_b128 v[28:31], v1 offset:14336\n" \
  "ds_read_b128 v[32:35], v1 offset:11264\n" \
  "ds_read_b128 v[36:39], v1 offset:15360\n" \
  "s_mov_b32 s65, 3\n" \
  "s_waitcnt lgkmcnt(4)\n" \
  "v_readfirstlane_b32 s38, v11\n" \
  "v_add_u32 v9, %[d3], v11\n" \
  "s_cmp_eq_u32 s73, 3\n" \
  "s_cselect_b32 s39, s69, 0x1000\n" \
  "s_mov_b64 exec, 1\n" \
  "buffer_store_dword v9, off, s[16:19], s39 sc1\n" \
  "s_mov_b64 exec, -1\n" \
  "s_sub_u32 s67, s38, 40\n" \
  "v_mul_f32 v11, 0x44870000, v10\n" \
  "v_readfirstlane_b32 s83, v10\n" \
  "v_readfirstlane_b32 s82, v11\n" \
  "s_lshr_b32 s82, s82, 23\n" \
  "s_addk_i32 s82, 0xff83\n" \
  "s_ashr_i32 s82, s82, 1\n" \
  "s_max_i32 s82, s82, -20\n" \
  "s_min_i32 s82, s82, 100\n" \
  "s_lshr_b32 s83, s83, 23\n" \
  "s_sub_i32 s83, s83, 242\n" \
  "v_readfirstlane_b32 s86, v8\n" \
  "s_mov_b64 vcc, s[76:77]\n" \
  "s_lshr_b32 s86, s86, 23\n" \
  "s_add_i32 s86, s86, s83\n" \
  "s_ashr_i32 s86, s86, 1\n" \
  "s_max_i32 s86, s86, s82\n" \
  "s_min_i32 s86, s86, 100\n" \
  "s_lshl_b32 s86, s86, 23\n" \
  "s_add_i32 s75, s86, 0x3c800000\n" \
  "s_waitcnt lgkmcnt(0)\n" \
  "s_branch .LCA_BACK_%=\n" \
  ".LCA_LAST_%=:\n" \
  "s_cmp_lg_u32 s71, 0\n" \
  "s_cbranch_scc1 .LCA_FIRST_%=\n" \
  "v_smfmac_f32_16x16x64_bf16 v[16:19], v[60:63], v[176:183], v0\n" \
  "v_smfmac_f32_16x16x64_bf16 v[20:23], v[60:63], v[240:247], v0\n" \
  "v_smfmac_f32_16x16x64_bf16 v[16:19], v[44:47], v[184:191], v0\n" \
  "v_smfmac_f32_16x16x64_bf16 v[20:23], v[44:47], v[248:255], v0\n" \
  "s_nop 7\n" \
  "v_add_f32 v16, v16, v18\n" \
  "v_add_f32 v17, v17, v19\n" \
  "v_add_f32 v20, v20, v22\n" \
  "v_add_f32 v21, v21, v23\n" \
  "v_cvt_pk_bf16_f32 v16, v16, v17\n" \
  "v_cvt_pk_bf16_f32 v17, v20, v21\n" \
  "s_nop 1\n" \
  "s_nop 0\n" \
  "v_max_u32 v16, 1, v16\n" \
  "v_max_u32 v17, 1, v17\n" \
  "buffer_store_dwordx2 v[16:17], v3, s[52:55], s70 offen sc1\n" \
  "s_waitcnt vmcnt(0)\n" \
  ".LCA_END_%=:\n" \
  "s_endpgm\n"
#define CHAIN_ASM_VO \
  "s_mov_b32 s66, %[d0]\n" \
  "s_mov_b32 s94, 576\n" \
  "s_mov_b32 s95, 288\n" \
  "s_mov_b32 s84, 0x43400000\n" \
  "s_mov_b32 s85, 0x43400000\n" \
  "s_mov_b32 s92, 0x88000000\n" \
  "s_mov_b32 s93, 0x87fffff7\n" \
  "s_mov_b32 s78, 0x3e0293ee\n" \
  "s_mov_b32 s76, 0x33333333\n" \
  "s_mov_b32 s77, 0x33333333\n" \
  "s_add_u32 s58, s56, s60\n" \
  "s_addc_u32 s59, s57, 0\n" \
  "s_xor_b32 s38, s35, 0\n" \
  "s_add_u32 s26, s58, s38\n" \
  "s_addc_u32 s27, s59, 0\n" \
  "s_xor_b32 s38, s35, 1024\n" \
  "s_add_u32 s28, s58, s38\n" \
  "s_addc_u32 s29, s59, 0\n" \
  "s_xor_b32 s38, s35, 2048\n" \
  "s_add_u32 s30, s58, s38\n" \
  "s_addc_u32 s31, s59, 0\n" \
  "s_xor_b32 s38, s35, 3072\n" \
  "s_add_u32 s32, s58, s38\n" \
  "s_addc_u32 s33, s59, 0\n" \
  "s_lshr_b32 s98, s60, 2\n" \
  "s_add_u32 s98, s98, s74\n" \
  "s_add_u32 s98, s98, 131072\n" \
  "s_lshl_b32 s97, s73, 5\n" \
  "s_add_u32 s97, s97, s74\n" \
  "s_add_u32 s97, s97, 158592\n" \
  "s_lshl_b32 s99, s73, 11\n" \
  "s_add_u32 s99, s99, s74\n" \
  "s_add_u32 s99, s99, 133120\n" \
  "v_mbcnt_lo_u32_b32 v48, -1, 0\n" \
  "v_mbcnt_hi_u32_b32 v48, -1, v48\n" \
  "s_sub_u32 s88, s73, 4\n" \
  "v_and_b32 v51, 15, v48\n" \
  "v_lshl_add_u32 v4, v51, 1, s97\n" \
  "v_lshrrev_b32 v49, 4, v48\n" \
  "v_mad_u32_u24 v4, v49, s94, v4\n" \
  "v_lshrrev_b32 v49, 2, v51\n" \
  "v_and_b32 v50, 5, v51\n" \
  "v_and_or_b32 v49, v49, 2, v50\n" \
  "s_lshl_b32 s89, s88, 6\n" \
  "s_add_u32 s89, s89, s74\n" \
  "s_add_u32 s89, s89, 161152\n" \
  "v_and_b32 v50, 48, v48\n" \
  "v_add_u32 v6, s89, v50\n" \
  "v_mad_u32_u24 v6, v49, s95, v6\n" \
  "v_lshlrev_b32 v7, 3, v50\n" \
  "v_lshl_add_u32 v7, v49, 4, v7\n" \
  "v_and_b32 v49, 2, v51\n" \
  "v_lshl_add_u32 v7, v49, 29, v7\n" \
  "v_lshlrev_b32 v10, 4, v48\n" \
  "v_mov_b64 v[12:13], 0\n" \
  "v_mov_b64 v[14:15], 0\n" \
  "v_mov_b64 v[16:17], 0\n" \
  "v_mov_b64 v[18:19], 0\n" \
  "v_mov_b64 v[20:21], 0\n" \
  "v_mov_b64 v[22:23], 0\n" \
  "s_lshl_b32 s88, s73, 10\n" \
  "v_add_u32 v24, s88, v1\n" \
  "ds_read_b128 v[32:35], v24 offset:0\n" \
  "s_waitcnt lgkmcnt(0)\n" \
  "v_mov_b32 v45, 0\n" \
  "v_dot2c_f32_bf16 v45, v32, v32\n" \
  "v_dot2c_f32_bf16 v45, v33, v33\n" \
  "v_dot2c_f32_bf16 v45, v34, v34\n" \
  "v_dot2c_f32_bf16 v45, v35, v35\n" \
  "s_add_u32 s88, s74, 157760\n" \
  "v_mov_b32 v44, s88\n" \
  "s_nop 0\n" \
  "v_cmpx_lt_f32 vcc, 2.0, v45\n" \
  "ds_max_u32 v44, v45\n" \
  "s_mov_b64 exec, -1\n" \
  "v_bfe_u32 v60, v10, 2, 7\n" \
  "s_lshl_b32 s38, s24, 1\n" \
  "s_add_u32 s38, s38, %[so]\n" \
  "global_load_dword v61, v60, s[52:53] offset:0 sc1\n" \
  "global_load_dword v62, v60, s[52:53] offset:128 sc1\n" \
  "global_load_dword v63, v60, s[52:53] offset:256 sc1\n" \
  "v_lshrrev_b32 v56, 8, v10\n" \
  "v_lshl_add_u32 v56, v56, 7, s38\n" \
  "v_and_or_b32 v56, v60, 63, v56\n" \
  "s_cmp_lg_u32 s73, 7\n" \
  "s_cbranch_scc1 2\n" \
  "global_load_dword v57, v56, s[52:53] sc1\n" \
  "s_branch .LCV_AL_%=\n" \
  ".p2alignl 12, 0xbf800000, 3000\n" \
  ".p2align 6\n" \
  ".Lpad0x81:\n" \
  "s_setpc_b64 s[98:99]\n" \
  ".p2align 6\n" \
  ".LCV_AL_%=:\n" \
  ".LCV_TOP_%=:\n" \
  "s_waitcnt lgkmcnt(0)\n" \
  "s_barrier\n" \
  "s_setprio 2\n" \
  "ds_read_b128 v[24:27], v1 offset:0\n" \
  "ds_read_b128 v[28:31], v1 offset:1024\n" \
  "ds_read_b128 v[32:35], v1 offset:2048\n" \
  "buffer_load_dword v5, off, s[44:47], s68 sc1\n" \
  "s_cmp_eq_u32 s71, 63\n" \
  "s_waitcnt lgkmcnt(2)\n" \
  "v_smfmac_f32_16x16x64_bf16 v[20:23], v[24:27], v[192:199], v0\n" \
  "ds_read_b128 v[36:39], v1 offset:3072\n" \
  "s_waitcnt lgkmcnt(2)\n" \
  "v_smfmac_f32_16x16x64_bf16 v[20:23], v[28:31], v[200:207], v0\n" \
  "ds_read_b128 v[40:43], v1 offset:4096\n" \
  "s_waitcnt lgkmcnt(2)\n" \
  "v_smfmac_f32_16x16x64_bf16 v[20:23], v[32:35], v[208:215], v0\n" \
  "ds_read_b128 v[44:47], v1 offset:5120\n" \
  "s_waitcnt lgkmcnt(2)\n" \
  "v_smfmac_f32_16x16x64_bf16 v[20:23], v[36:39], v[216:223], v0\n" \
  "ds_read_b128 v[48:51], v1 offset:6144\n" \
  "s_waitcnt lgkmcnt(2)\n" \
  "v_smfmac_f32_16x16x64_bf16 v[20:23], v[40:43], v[224:231], v0\n" \
  "ds_read_b128 v[52:55], v1 offset:7168\n" \
  "s_waitcnt lgkmcnt(2)\n" \
  "v_smfmac_f32_16x16x64_bf16 v[20:23], v[44:47], v[232:239], v0\n" \
  "s_waitcnt lgkmcnt(1)\n" \
  "v_smfmac_f32_16x16x64_bf16 v[20:23], v[48:51], v[240:247], v0\n" \
  "s_waitcnt lgkmcnt(0)\n" \
  "v_smfmac_f32_16x16x64_bf16 v[20:23], v[52:55], v[248:255], v0\n" \
  "s_nop 0\n" \
  "v_smfmac_f32_16x16x64_bf16 v[12:15], v[24:27], v[64:71], v0\n" \
  "s_nop 0\n" \
  "v_smfmac_f32_16x16x64_bf16 v[12:15], v[28:31], v[72:79], v0\n" \
  "s_nop 0\n" \
  "v_smfmac_f32_16x16x64_bf16 v[12:15], v[32:35], v[80:87], v0\n" \
  "ds_read_b32 v11, v1 offset:26688\n" \
  "v_smfmac_f32_16x16x64_bf16 v[12:15], v[36:39], v[88:95], v0\n" \
  "v_add_f32 v20, v20, v22\n" \
  "v_smfmac_f32_16x16x64_bf16 v[12:15], v[40:43], v[96:103], v0\n" \
  "v_add_f32 v21, v21, v23\n" \
  "v_smfmac_f32_16x16x64_bf16 v[12:15], v[44:47], v[104:111], v0\n" \
  "v_cvt_pk_bf16_f32 v20, v20, v21\n" \
  "v_smfmac_f32_16x16x64_bf16 v[12:15], v[48:51], v[112:119], v0\n" \
  "ds_write_b16 v4, v20 offset:0\n" \
  "v_smfmac_f32_16x16x64_bf16 v[12:15], v[52:55], v[120:127], v0\n" \
  "ds_write_b16_d16_hi v4, v20 offset:288\n" \
  "v_smfmac_f32_16x16x64_bf16 v[16:19], v[24:27], v[128:135], v0\n" \
  "s_nop 1\n" \
  "v_smfmac_f32_16x16x64_bf16 v[16:19], v[28:31], v[136:143], v0\n" \
  "s_nop 1\n" \
  "v_smfmac_f32_16x16x64_bf16 v[16:19], v[32:35], v[144:151], v0\n" \
  "v_add_f32 v12, v12, v14\n" \
  "v_smfmac_f32_16x16x64_bf16 v[16:19], v[36:39], v[152:159], v0\n" \
  "v_add_f32 v13, v13, v15\n" \
  "v_smfmac_f32_16x16x64_bf16 v[16:19], v[40:43], v[160:167], v0\n" \
  "v_cvt_pk_bf16_f32 v12, v12, v13\n" \
  "v_smfmac_f32_16x16x64_bf16 v[16:19], v[44:47], v[168:175], v0\n" \
  "ds_write_b16 v4, v12 offset:128\n" \
  "v_smfmac_f32_16x16x64_bf16 v[16:19], v[48:51], v[176:183], v0\n" \
  "ds_write_b16_d16_hi v4, v12 offset:416\n" \
  "v_smfmac_f32_16x16x64_bf16 v[16:19], v[52:55], v[184:191], v0\n" \
  "s_barrier\n" \
  "s_nop 6\n" \
  "v_add_f32 v16, v16, v18\n" \
  "v_add_f32 v17, v17, v19\n" \
  "v_cvt_pk_bf16_f32 v16, v16, v17\n" \
  "ds_write_b16 v4, v16 offset:2560\n" \
  "ds_write_b16_d16_hi v4, v16 offset:2848\n" \
  "s_cbranch_scc1 .LCV_FIRST_%=\n" \
  ".LCV_BACK_%=:\n" \
  ".fill ((4 - (. - .LCV_AL_%=)) & 31) >> 2, 4, 0xbf800000\n" \
  "s_waitcnt lgkmcnt(0)\n" \
  "s_barrier\n" \
  "s_setprio 0\n" \
  "ds_read_b128 v[60:63], v6 offset:0\n" \
  "v_readfirstlane_b32 s86, v11\n" \
  "s_add_u32 s88, s74, 157760\n" \
  "v_mov_b32 v8, s88\n" \
  "v_mov_b32 v9, 0\n" \
  "ds_write_b32 v8, v9\n" \
  "s_lshr_b32 s86, s86, 23\n" \
  "s_add_i32 s86, s86, s83\n" \
  "s_ashr_i32 s86, s86, 1\n" \
  "s_max_i32 s86, s86, s82\n" \
  "s_min_i32 s86, s86, 100\n" \
  "s_lshl_b32 s86, s86, 23\n" \
  "s_cmp_eq_u32 s71, 0\n" \
  "s_waitcnt lgkmcnt(1)\n" \
  "v_max_u32 v60, 1, v60\n" \
  "v_max_u32 v61, 1, v61\n" \
  "v_max_u32 v62, 1, v62\n" \
  "v_max_u32 v63, 1, v63\n" \
  "buffer_store_dwordx4 v[60:63], v7, s[48:51], s69 offen sc1\n" \
  "s_cbranch_scc1 .LCV_LAST_%=\n" \
  "s_waitcnt vmcnt(1)\n" \
  "v_cmp_eq_u32 vcc, 0, v5\n" \
  "s_cmp_eq_u64 vcc, 0\n" \
  "s_cselect_b32 s90, 1, 0\n" \
  "s_cselect_b32 s88, s71, 64\n" \
  "s_cmp_lt_u32 s88, 63\n" \
  "s_cselect_b32 s90, 1, 0\n" \
  "s_barrier\n" \
  "ds_read_b128 v[24:27], v3 offset:0\n" \
  "ds_read_b128 v[28:31], v3 offset:1024\n" \
  "ds_read_b128 v[32:35], v3 offset:2048\n" \
  "ds_read_b128 v[36:39], v3 offset:3072\n" \
  "ds_read_b128 v[40:43], v3 offset:8192\n" \
  "ds_read_b128 v[44:47], v3 offset:9216\n" \
  "ds_read_b128 v[48:51], v3 offset:10240\n" \
  "ds_read_b128 v[52:55], v3 offset:11264\n" \
  "ds_read_b128 v[56:59], v3 offset:4096\n" \
  "ds_read_b128 v[60:63], v3 offset:5120\n" \
  "s_cbranch_scc0 .LCV_NOTOK_%=\n" \
  "s_mov_b32 m0, s99\n" \
  "s_add_u32 s88, s99, 8192\n" \
  "global_load_lds_dwordx4 v10, s[62:63] sc1\n" \
  "global_load_lds_dwordx4 v10, s[62:63] offset:1024 sc1\n" \
  "s_mov_b32 m0, s88\n" \
  "s_nop 0\n" \
  "global_load_lds_dwordx4 v10, s[64:65] sc1\n" \
  "global_load_lds_dwordx4 v10, s[64:65] offset:1024 sc1\n" \
  ".LCV_NOTOK_%=:\n" \
  "v_mov_b64 v[12:13], s[84:85]\n" \
  "v_mov_b64 v[14:15], s[84:85]\n" \
  "v_mov_b64 v[16:17], s[84:85]\n" \
  "v_mov_b64 v[18:19], s[84:85]\n" \
  "v_mov_b64 v[20:21], s[84:85]\n" \
  "v_mov_b64 v[22:23], s[84:85]\n" \
  "s_cmp_eq_u32 s90, 1\n" \
  "s_cbranch_scc1 .LCV_NMT_%=\n" \
  "s_sleep 3\n" \
  "s_add_u32 m0, s22, 64\n" \
  "s_mov_b64 exec, 1\n" \
  "buffer_load_dword off, s[44:47], s68 sc1 lds\n" \
  "s_mov_b64 exec, -1\n" \
  ".LCV_NMT_%=:\n" \
  "s_waitcnt lgkmcnt(0)\n" \
  "s_barrier\n" \
  "ds_read_b128 v[4:7], v1 offset:8192\n" \
  "ds_read_b128 v[8:11], v1 offset:9216\n" \
  "s_waitcnt lgkmcnt(1)\n" \
  "v_smfmac_f32_16x16x64_bf16 v[12:15], v[4:7], v[24:31], v0\n" \
  "ds_read_b128 v[24:27], v3 offset:12288\n" \
  "ds_read_b128 v[28:31], v3 offset:13312\n" \
  "v_smfmac_f32_16x16x64_bf16 v[16:19], v[4:7], v[32:39], v0\n" \
  "ds_read_b128 v[32:35], v3 offset:6144\n" \
  "ds_read_b128 v[36:39], v3 offset:7168\n" \
  "s_waitcnt lgkmcnt(4)\n" \
  "v_smfmac_f32_16x16x64_bf16 v[12:15], v[8:11], v[40:47], v0\n" \
  "v_smfmac_f32_16x16x64_bf16 v[16:19], v[8:11], v[48:55], v0\n" \
  "s_nop 1\n" \
  "v_smfmac_f32_16x16x64_bf16 v[20:23], v[4:7], v[56:63], v0\n" \
  "ds_read_b128 v[56:59], v3 offset:14336\n" \
  "ds_read_b128 v[60:63], v3 offset:15360\n" \
  "v_mov_b64 v[40:41], s[84:85]\n" \
  "v_mov_b64 v[42:43], s[84:85]\n" \
  "v_add_f32 v12, v12, v14\n" \
  "v_add_f32 v13, v13, v15\n" \
  "global_atomic_add_x2 v2, v[12:13], s[26:27]\n" \
  "s_waitcnt lgkmcnt(4)\n" \
  "v_smfmac_f32_16x16x64_bf16 v[20:23], v[8:11], v[24:31], v0\n" \
  "v_add_f32 v16, v16, v18\n" \
  "v_add_f32 v17, v17, v19\n" \
  "global_atomic_add_x2 v2, v[16:17], s[28:29]\n" \
  "s_waitcnt lgkmcnt(2)\n" \
  "v_smfmac_f32_16x16x64_bf16 v[40:43], v[4:7], v[32:39], v0\n" \
  "s_waitcnt lgkmcnt(0)\n" \
  "v_smfmac_f32_16x16x64_bf16 v[40:43], v[8:11], v[56:63], v0\n" \
  "s_nop 1\n" \
  "v_add_f32 v20, v20, v22\n" \
  "v_add_f32 v21, v21, v23\n" \
  "global_atomic_add_x2 v2, v[20:21], s[30:31]\n" \
  "s_nop 3\n" \
  "v_add_f32 v40, v40, v42\n" \
  "v_add_f32 v41, v41, v43\n" \
  "global_atomic_add_x2 v2, v[40:41], s[32:33]\n" \
  "v_mbcnt_lo_u32_b32 v48, -1, 0\n" \
  "v_mbcnt_hi_u32_b32 v48, -1, v48\n" \
  "s_add_u32 s20, s20, 32\n" \
  "s_cmp_eq_u32 s90, 1\n" \
  "s_cbranch_scc1 .LCV_HAVE_%=\n" \
  "s_mov_b32 s87, 0x40000\n" \
  "v_lshlrev_b32 v10, 4, v48\n" \
  "s_waitcnt vmcnt(4)\n" \
  "v_mov_b32 v55, s22\n" \
  "s_mov_b32 s0, 0xbe801d62\n" \
  ".set .Lpad0x7a, . - 4\n" \
  "ds_read_b32 v55, v55 offset:64\n" \
  "s_waitcnt lgkmcnt(0)\n" \
  "v_cmp_eq_u32 vcc, 0, v55\n" \
  "s_cmp_eq_u64 vcc, 0\n" \
  "s_cbranch_scc1 .LCV_GOT_%=\n" \
  ".LCV_LATE_%=:\n" \
  "buffer_load_dword v58, off, s[44:47], s68 sc1\n" \
  "s_sub_u32 s87, s87, 1\n" \
  "s_cbranch_scc1 .LCV_END_%=\n" \
  "s_waitcnt vmcnt(0)\n" \
  "v_cmp_eq_u32 vcc, 0, v58\n" \
  "s_cmp_eq_u64 vcc, 0\n" \
  "s_cbranch_scc1 .LCV_GOT_%=\n" \
  "s_sleep 4\n" \
  "s_branch .LCV_LATE_%=\n" \
  ".Lpad0x82:\n" \
  "s_setpc_b64 s[98:99]\n" \
  ".LCV_GOT_%=:\n" \
  "s_mov_b32 m0, s99\n" \
  "s_add_u32 s88, s99, 8192\n" \
  "global_load_lds_dwordx4 v10, s[62:63] sc1\n" \
  "global_load_lds_dwordx4 v10, s[62:63] offset:1024 sc1\n" \
  "s_mov_b32 m0, s88\n" \
  "s_nop 0\n" \
  "global_load_lds_dwordx4 v10, s[64:65] sc1\n" \
  "global_load_lds_dwordx4 v10, s[64:65] offset:1024 sc1\n" \
  ".LCV_HAVE_%=:\n" \
  "v_lshrrev_b32 v49, 3, v48\n" \
  "v_and_b32 v50, 7, v48\n" \
  "v_lshlrev_b32 v50, 4, v50\n" \
  "v_lshl_or_b32 v44, v49, 8, v50\n" \
  "s_mov_b32 s21, 0x8000\n" \
  "v_mov_b32 v52, s22\n" \
  "v_mov_b32 v54, s20\n" \
  "s_branch .LCV_TKF_%=\n" \
  ".LCV_TKE_%=:\n" \
  "s_sleep 1\n" \
  ".LCV_TKF_%=:\n" \
  "ds_read_b64 v[54:55], v52\n" \
  "s_sub_u32 s21, s21, 1\n" \
  "s_cbranch_scc1 .LCV_TKO_%=\n" \
  "s_waitcnt lgkmcnt(0)\n" \
  "v_readfirstlane_b32 s38, v54\n" \
  "v_readfirstlane_b32 s34, v55\n" \
  "s_cmp_eq_u32 s38, s20\n" \
  "s_cbranch_scc0 .LCV_TKE_%=\n" \
  ".LCV_TKO_%=:\n" \
  "s_branch .LCV_WAIU_%=\n" \
  ".Lpad0x7f:\n" \
  "s_setpc_b64 s[98:99]\n" \
  ".LCV_WAIS_%=:\n" \
  "s_cmp_lt_i32 s39, -1000\n" \
  "s_cbranch_scc1 .LCV_WAIE_%=\n" \
  "s_sleep 1\n" \
  ".LCV_WAIU_%=:\n" \
  "s_memrealtime s[38:39]\n" \
  "s_waitcnt lgkmcnt(0)\n" \
  "s_sub_i32 s39, s38, s34\n" \
  "s_cmp_lt_i32 s39, -7\n" \
  "s_cbranch_scc1 .LCV_WAIS_%=\n" \
  "s_cmp_lt_i32 s39, 0\n" \
  "s_cbranch_scc1 .LCV_WAIU_%=\n" \
  ".LCV_WAIE_%=:\n" \
  "buffer_load_dwordx4 v[24:27], v44, s[40:43], s60 offen sc1\n" \
  "buffer_load_dwordx4 v[28:31], v44, s[40:43], s60 offen offset:2048 sc1\n" \
  "v_mbcnt_lo_u32_b32 v48, -1, 0\n" \
  "v_mbcnt_hi_u32_b32 v48, -1, v48\n" \
  "s_sub_u32 s88, s73, 4\n" \
  "v_and_b32 v51, 15, v48\n" \
  "v_lshl_add_u32 v4, v51, 1, s97\n" \
  "v_lshrrev_b32 v49, 4, v48\n" \
  "v_mad_u32_u24 v4, v49, s94, v4\n" \
  "v_lshrrev_b32 v49, 2, v51\n" \
  "v_and_b32 v50, 5, v51\n" \
  "v_and_or_b32 v49, v49, 2, v50\n" \
  "s_lshl_b32 s89, s88, 6\n" \
  "s_add_u32 s89, s89, s74\n" \
  "s_add_u32 s89, s89, 161152\n" \
  "v_and_b32 v50, 48, v48\n" \
  "v_add_u32 v6, s89, v50\n" \
  "v_mad_u32_u24 v6, v49, s95, v6\n" \
  "v_lshlrev_b32 v7, 3, v50\n" \
  "v_lshl_add_u32 v7, v49, 4, v7\n" \
  "v_and_b32 v49, 2, v51\n" \
  "v_lshl_add_u32 v7, v49, 29, v7\n" \
  "v_lshlrev_b32 v10, 4, v48\n" \
  "v_mov_b64 v[12:13], 0\n" \
  "v_mov_b64 v[14:15], 0\n" \
  "v_mov_b64 v[16:17], 0\n" \
  "v_mov_b64 v[18:19], 0\n" \
  "v_mov_b64 v[20:21], 0\n" \
  "v_mov_b64 v[22:23], 0\n" \
  "v_and_b32 v49, 3, v48\n" \
  "v_lshl_add_u32 v40, v49, 2, s98\n" \
  "v_lshrrev_b32 v49, 2, v48\n" \
  "v_bfe_u32 v48, v48, 4, 1\n" \
  "v_xor_b32 v49, v49, v48\n" \
  "v_lshl_add_u32 v40, v49, 5, v40\n" \
  "s_add_u32 s62, s62, 0x80000\n" \
  "s_addc_u32 s63, s63, 0\n" \
  "s_add_u32 s64, s64, 0x40000\n" \
  "s_addc_u32 s65, s65, 0\n" \
  "s_add_u32 s68, s68, 0x1000\n" \
  "s_add_u32 s69, s69, 0x10000\n" \
  "s_add_i32 s80, s86, 0x35000000\n" \
  "s_sub_u32 s91, 0x4a350000, s86\n" \
  "s_min_u32 s91, s91, 0x4ca00000\n" \
  "s_mov_b32 s89, s60\n" \
  "s_xor_b32 s60, s60, 0x80\n" \
  "s_bitcmp0_b32 s60, 7\n" \
  "s_cselect_b32 s88, 0x8000, 0\n" \
  "s_add_u32 s60, s60, s88\n" \
  "s_and_b32 s60, s60, 0xffff\n" \
  "s_add_u32 s58, s56, s60\n" \
  "s_addc_u32 s59, s57, 0\n" \
  "s_xor_b32 s38, s35, 0\n" \
  "s_add_u32 s26, s58, s38\n" \
  "s_addc_u32 s27, s59, 0\n" \
  "s_xor_b32 s38, s35, 1024\n" \
  "s_add_u32 s28, s58, s38\n" \
  "s_addc_u32 s29, s59, 0\n" \
  "s_xor_b32 s38, s35, 2048\n" \
  "s_add_u32 s30, s58, s38\n" \
  "s_addc_u32 s31, s59, 0\n" \
  "s_xor_b32 s38, s35, 3072\n" \
  "s_add_u32 s32, s58, s38\n" \
  "s_addc_u32 s33, s59, 0\n" \
  "s_sub_u32 s71, s71, 1\n" \
  "s_mov_b32 s87, 0x40000\n" \
  "s_branch .LCV_CHK_%=\n" \
  ".LCV_POLL_%=:\n" \
  "buffer_load_dwordx4 v[24:27], v44, s[40:43], s89 offen sc1\n" \
  "buffer_load_dwordx4 v[28:31], v44, s[40:43], s89 offen offset:2048 sc1\n" \
  ".LCV_CHK_%=:\n" \
  "s_sub_u32 s87, s87, 1\n" \
  "s_cbranch_scc1 .LCV_END_%=\n" \
  "s_waitcnt vmcnt(1)\n" \
  "v_cvt_f32_i32 v32, v24\n" \
  "v_cvt_f32_i32 v33, v25\n" \
  "v_cvt_f32_i32 v34, v26\n" \
  "v_cvt_f32_i32 v35, v27\n" \
  "v_pk_mul_f32 v[24:25], v[32:33], s[80:81] op_sel_hi:[1,0]\n" \
  "v_pk_mul_f32 v[26:27], v[34:35], s[80:81] op_sel_hi:[1,0]\n" \
  "v_max3_f32 v45, |v32|, |v33|, |v34|\n" \
  "v_cvt_pk_bf16_f32 v24, v24, v26\n" \
  "v_cvt_pk_bf16_f32 v25, v25, v27\n" \
  "ds_write2_b32 v40, v24, v25 offset0:0 offset1:4\n" \
  "v_max3_f32 v45, |v45|, |v35|, |v35|\n" \
  "s_waitcnt vmcnt(0)\n" \
  "v_cvt_f32_i32 v32, v28\n" \
  "v_cvt_f32_i32 v33, v29\n" \
  "v_cvt_f32_i32 v34, v30\n" \
  "v_cvt_f32_i32 v35, v31\n" \
  "v_pk_mul_f32 v[28:29], v[32:33], s[80:81] op_sel_hi:[1,0]\n" \
  "v_pk_mul_f32 v[30:31], v[34:35], s[80:81] op_sel_hi:[1,0]\n" \
  "v_max3_f32 v45, |v45|, |v32|, |v33|\n" \
  "v_cvt_pk_bf16_f32 v28, v28, v30\n" \
  "v_cvt_pk_bf16_f32 v29, v29, v31\n" \
  "ds_write2_b32 v40, v28, v29 offset0:128 offset1:132\n" \
  "v_max3_f32 v45, |v45|, |v34|, |v35|\n" \
  "v_cmp_lt_f32 vcc, s91, v45\n" \
  "s_cbranch_vccnz .LCV_BIG_%=\n" \
  ".LCV_SML_%=:\n" \
  "s_branch .LCV_TOP_%=\n" \
  ".Lpad0x83:\n" \
  "s_setpc_b64 s[98:99]\n" \
  ".LCV_BIG_%=:\n" \
  "v_cmp_lt_f32 vcc, 0x4ca00000, v45\n" \
  "s_cbranch_vccnz .LCV_POLL_%=\n" \
  "v_mul_f32 v45, s80, v45\n" \
  "v_mul_f32 v45, v45, v45\n" \
  "s_add_u32 s88, s74, 157760\n" \
  "v_mov_b32 v44, s88\n" \
  "s_nop 0\n" \
  "v_cmpx_lt_f32 vcc, 2.0, v45\n" \
  "ds_max_u32 v44, v45\n" \
  "s_mov_b64 exec, -1\n" \
  "s_branch .LCV_SML_%=\n" \
  ".LCV_FIRST_%=:\n" \
  "s_mov_b32 s87, 0x100000\n" \
  "s_cmp_lg_u32 s73, 7\n" \
  "s_cbranch_scc1 .LCV_FON_%=\n" \
  "global_load_dword v58, v56, s[52:53] sc1\n" \
  "s_sleep %[sf]\n" \
  "global_load_dword v59, v56, s[52:53] sc1\n" \
  ".LCV_FASK_%=:\n" \
  "s_sub_u32 s87, s87, 1\n" \
  "s_cbranch_scc1 .LCV_END_%=\n" \
  "s_waitcnt vmcnt(2)\n" \
  "v_mov_b32 v30, v57\n" \
  "v_cmp_eq_u32 vcc, 0, v57\n" \
  "s_cbranch_vccz .LCV_FSEE_%=\n" \
  "s_cmp_eq_u32 s24, 0\n" \
  "s_cbranch_scc1 1\n" \
  "s_sleep %[sg]\n" \
  "global_load_dword v57, v56, s[52:53] sc1\n" \
  "s_waitcnt vmcnt(2)\n" \
  "v_mov_b32 v30, v58\n" \
  "v_cmp_eq_u32 vcc, 0, v58\n" \
  "s_cbranch_vccz .LCV_FSEE_%=\n" \
  "s_cmp_eq_u32 s24, 0\n" \
  "s_cbranch_scc1 1\n" \
  "s_sleep %[sg]\n" \
  "global_load_dword v58, v56, s[52:53] sc1\n" \
  "s_waitcnt vmcnt(2)\n" \
  "v_mov_b32 v30, v59\n" \
  "v_cmp_eq_u32 vcc, 0, v59\n" \
  "s_cbranch_vccz .LCV_FSEE_%=\n" \
  "s_cmp_eq_u32 s24, 0\n" \
  "s_cbranch_scc1 1\n" \
  "s_sleep %[sg]\n" \
  "global_load_dword v59, v56, s[52:53] sc1\n" \
  "s_branch .LCV_FASK_%=\n" \
  ".LCV_FSEE_%=:\n" \
  "buffer_load_dword v9, off, s[16:19], s24 offset:8 sc1\n" \
  "s_sub_u32 s38, s62, 6144\n" \
  "s_subb_u32 s39, s63, 0\n" \
  "s_add_u32 m0, s99, -6144\n" \
  "s_nop 0\n" \
  "global_load_lds_dwordx4 v10, s[38:39] offset:0 sc1\n" \
  "global_load_lds_dwordx4 v10, s[38:39] offset:1024 sc1\n" \
  "global_load_lds_dwordx4 v10, s[38:39] offset:2048 sc1\n" \
  "global_load_lds_dwordx4 v10, s[38:39] offset:3072 sc1\n" \
  "s_sub_u32 s38, s62, 2048\n" \
  "s_subb_u32 s39, s63, 0\n" \
  "s_add_u32 m0, s99, -2048\n" \
  "s_nop 0\n" \
  "global_load_lds_dwordx4 v10, s[38:39] offset:0 sc1\n" \
  "global_load_lds_dwordx4 v10, s[38:39] offset:1024 sc1\n" \
  "global_load_lds_dwordx4 v10, s[38:39] offset:2048 sc1\n" \
  "global_load_lds_dwordx4 v10, s[38:39] offset:3072 sc1\n" \
  "s_sub_u32 s38, s64, 6144\n" \
  "s_subb_u32 s39, s65, 0\n" \
  "s_add_u32 m0, s99, 2048\n" \
  "s_nop 0\n" \
  "global_load_lds_dwordx4 v10, s[38:39] offset:0 sc1\n" \
  "global_load_lds_dwordx4 v10, s[38:39] offset:1024 sc1\n" \
  "global_load_lds_dwordx4 v10, s[38:39] offset:2048 sc1\n" \
  "global_load_lds_dwordx4 v10, s[38:39] offset:3072 sc1\n" \
  "s_sub_u32 s38, s64, 2048\n" \
  "s_subb_u32 s39, s65, 0\n" \
  "s_add_u32 m0, s99, 6144\n" \
  "s_nop 0\n" \
  "global_load_lds_dwordx4 v10, s[38:39] offset:0 sc1\n" \
  "global_load_lds_dwordx4 v10, s[38:39] offset:1024 sc1\n" \
  "global_load_lds_dwordx4 v10, s[38:39] offset:2048 sc1\n" \
  "global_load_lds_dwordx4 v10, s[38:39] offset:3072 sc1\n" \
  "s_memrealtime vcc\n" \
  "s_nop 1\n" \
  "v_max_u32_dpp v30, v30, v30 row_ror:8 row_mask:0xf bank_mask:0xf\n" \
  "s_nop 1\n" \
  "v_max_u32_dpp v30, v30, v30 row_ror:4 row_mask:0xf bank_mask:0xf\n" \
  "s_nop 1\n" \
  "v_max_u32_dpp v30, v30, v30 row_ror:2 row_mask:0xf bank_mask:0xf\n" \
  "s_nop 1\n" \
  "v_max_u32_dpp v30, v30, v30 row_ror:1 row_mask:0xf bank_mask:0xf\n" \
  "v_mov_b32 v31, v30\n" \
  "s_nop 1\n" \
  "v_permlane16_swap_b32 v30, v31\n" \
  "v_max_u32 v30, v30, v31\n" \
  "v_mov_b32 v31, v30\n" \
  "s_nop 1\n" \
  "v_permlane32_swap_b32 v30, v31\n" \
  "v_max_u32 v30, v30, v31\n" \
  "s_lshr_b32 s88, s68, 10\n" \
  "s_lshl_b32 s88, s88, 8\n" \
  "s_add_u32 s88, s88, s24\n" \
  "s_cmp_eq_u32 s24, 0\n" \
  "v_readfirstlane_b32 s39, v30\n" \
  "v_mov_b32 v31, 1\n" \
  "s_cbranch_scc1 .LCV_FNOW_%=\n" \
  "s_waitcnt vmcnt(16)\n" \
  "v_readfirstlane_b32 s89, v9\n" \
  "s_add_u32 s89, s89, s25\n" \
  "s_sub_u32 s89, s89, %[g1]\n" \
  "s_sub_u32 s38, s89, s39\n" \
  "s_cmp_lt_u32 s38, 800\n" \
  "s_cselect_b32 s39, s89, s39\n" \
  ".LCV_FNOW_%=:\n" \
  "s_add_u32 s89, s39, %[d2]\n" \
  "s_waitcnt lgkmcnt(0)\n" \
  "s_add_u32 s38, vcc_lo, %[d4]\n" \
  "s_max_u32 s89, s89, s38\n" \
  "s_add_u32 s38, s89, %[d1]\n" \
  "v_mov_b32 v30, s38\n" \
  "s_mov_b64 exec, 1\n" \
  "buffer_atomic_umax v30, off, s[16:19], s88\n" \
  "buffer_atomic_add v31, off, s[16:19], s88 offset:64\n" \
  "s_mov_b64 exec, -1\n" \
  ".LCV_FON_%=:\n" \
  "s_add_u32 s62, s62, 0x80000\n" \
  "s_addc_u32 s63, s63, 0\n" \
  "s_add_u32 s64, s64, 0x40000\n" \
  "s_addc_u32 s65, s65, 0\n" \
  "s_add_u32 s68, s68, 0x1000\n" \
  "s_cmp_eq_u32 s73, 7\n" \
  "s_cbranch_scc1 .LCV_FCHK_%=\n" \
  "s_branch .LCV_FBND_%=\n" \
  ".LCV_FMORE_%=:\n" \
  "s_sleep %[sf]\n" \
  "global_load_dword v61, v60, s[52:53] offset:0 sc1\n" \
  "global_load_dword v62, v60, s[52:53] offset:128 sc1\n" \
  "global_load_dword v63, v60, s[52:53] offset:256 sc1\n" \
  "s_sub_u32 s87, s87, 1\n" \
  "s_cbranch_scc1 .LCV_END_%=\n" \
  ".LCV_FBND_%=:\n" \
  "s_waitcnt vmcnt(0)\n" \
  ".LCV_FCHK_%=:\n" \
  "v_min3_u32 v32, v61, v62, v63\n" \
  "v_cmp_eq_u32 vcc, 0, v32\n" \
  "s_cbranch_vccnz .LCV_FMORE_%=\n" \
  "v_add_f32 v24, v62, v63\n" \
  "v_mul_f32 v24, v24, v61\n" \
  "s_nop 1\n" \
  "v_max_f32_dpp v24, v24, v24 row_ror:8 row_mask:0xf bank_mask:0xf\n" \
  "s_nop 1\n" \
  "v_max_f32_dpp v24, v24, v24 row_ror:4 row_mask:0xf bank_mask:0xf\n" \
  "s_nop 1\n" \
  "v_max_f32_dpp v24, v24, v24 row_ror:2 row_mask:0xf bank_mask:0xf\n" \
  "s_nop 1\n" \
  "v_max_f32_dpp v24, v24, v24 row_ror:1 row_mask:0xf bank_mask:0xf\n" \
  "v_mov_b32 v25, v24\n" \
  "s_nop 1\n" \
  "v_permlane16_swap_b32 v24, v25\n" \
  "v_max_f32 v24, v24, v25\n" \
  "v_mul_f32 v24, 0x3f88f5c3, v24\n" \
  "s_nop 0\n" \
  "v_mul_f32 v25, 0x44870000, v24\n" \
  "v_readfirstlane_b32 s83, v24\n" \
  "v_readfirstlane_b32 s82, v25\n" \
  "s_lshr_b32 s82, s82, 23\n" \
  "s_addk_i32 s82, 0xff83\n" \
  "s_ashr_i32 s82, s82, 1\n" \
  "s_max_i32 s82, s82, -20\n" \
  "s_min_i32 s82, s82, 100\n" \
  "s_lshr_b32 s83, s83, 23\n" \
  "s_sub_i32 s83, s83, 242\n" \
  "s_cmp_lg_u32 s73, 7\n" \
  "s_cbranch_scc1 .LCV_BACK_%=\n" \
  "v_mov_b32 v25, s89\n" \
  "s_mov_b64 exec, 1\n" \
  "ds_write_b64 v1, v[24:25] offset:26624\n" \
  "s_mov_b64 exec, -1\n" \
  ".LCV_FWAIT_%=:\n" \
  "s_memrealtime s[38:39]\n" \
  "s_waitcnt lgkmcnt(0)\n" \
  "s_sub_u32 s38, s89, s38\n" \
  "s_cmp_lt_u32 s38, 800\n" \
  "s_cbranch_scc1 .LCV_FWAIT_%=\n" \
  "s_waitcnt vmcnt(2)\n" \
  "s_branch .LCV_BACK_%=\n" \
  ".LCV_LAST_%=:\n" \
  "s_waitcnt vmcnt(0)\n" \
  ".LCV_END_%=:\n" \
  "s_endpgm\n" \
  ".p2alignl 12, 0xbf800000\n" \
  ".Lpad0x64:\n" \
  "s_setpc_b64 s[98:99]\n"
#define QKV_ASM_0 \
  "s_cmp_lg_u32 s69, 0\n" \
  "s_mov_b32 s69, 0\n" \
  "s_cbranch_scc1 .LQA_GOT_%=\n" \
  "s_branch .LQA_POLL_%=\n" \
  "s_branch .LQA_AL_%=\n" \
  ".p2alignl 12, 0xbf800000, 2600\n" \
  ".p2align 6\n" \
  ".Lpad0x95:\n" \
  "s_setpc_b64 s[98:99]\n" \
  ".p2align 6\n" \
  ".LQA_AL_%=:\n" \
  ".LQA_TOP_%=:\n" \
  "s_barrier\n" \
  "ds_read_b128 v[36:39], v7 offset:0\n" \
  "ds_read_b128 v[40:43], v7 offset:2048\n" \
  "ds_read_b128 v[44:47], v7 offset:4096\n" \
  "s_setprio 2\n" \
  "s_waitcnt lgkmcnt(2)\n" \
  "v_mfma_f32_16x16x32_bf16 v[12:15], v[64:67], v[36:39], 0\n" \
  "ds_read_b128 v[48:51], v7 offset:6144\n" \
  "v_mfma_f32_16x16x32_bf16 v[16:19], v[128:131], v[36:39], 0\n" \
  "v_mfma_f32_16x16x32_bf16 v[20:23], v[36:39], v[192:195], 0\n" \
  "s_cmp_lg_u32 s69, 1\n" \
  "s_cbranch_scc1 .LQA_FDN_%=\n" \
  "s_waitcnt vmcnt(0)\n" \
  "v_mov_b32 v52, s80\n" \
  "v_mov_b32 v53, 0x10000\n" \
  "s_mov_b64 exec, 1\n" \
  "ds_add_rtn_u32 v54, v52, v53\n" \
  "s_memrealtime s[0:1]\n" \
  "s_mov_b64 exec, -1\n" \
  "s_add_u32 s67, s67, 4\n" \
  "s_add_u32 s64, s64, 4096\n" \
  "s_addc_u32 s65, s65, 0\n" \
  "v_mov_b32 v55, 0\n" \
  "s_waitcnt lgkmcnt(0)\n" \
  "v_readfirstlane_b32 s74, v54\n" \
  "s_and_b32 s76, s74, 0x30000\n" \
  "s_cmp_lg_u32 s76, 0x30000\n" \
  "s_cbranch_scc1 .LQA_FDN_%=\n" \
  "s_mov_b32 s79, 0x100000\n" \
  "s_branch .LQA_FDT_%=\n" \
  ".LQA_FD_%=:\n" \
  "ds_read_b32 v54, v52\n" \
  "s_sub_u32 s79, s79, 1\n" \
  "s_cbranch_scc1 .LQA_FDE_%=\n" \
  "s_waitcnt lgkmcnt(0)\n" \
  "v_readfirstlane_b32 s74, v54\n" \
  ".LQA_FDT_%=:\n" \
  "s_and_b32 s74, s74, 0xffff\n" \
  "s_cmp_lt_u32 s74, s67\n" \
  "s_cbranch_scc1 .LQA_FD_%=\n" \
  ".LQA_FDE_%=:\n" \
  "v_or_b32 v53, 1, s0\n" \
  "s_cmp_lg_u32 s67, s83\n" \
  "s_cbranch_scc1 .LQA_FDX_%=\n" \
  "v_lshlrev_b32 v54, 5, v6\n" \
  "s_mov_b64 exec, 15\n" \
  "global_store_dword v54, v53, s[84:85] sc1\n" \
  ".LQA_FDX_%=:\n" \
  "s_mov_b64 exec, 1\n" \
  "global_store_dword v55, v53, s[64:65] offset:-4096 sc1\n" \
  "s_mov_b64 exec, -1\n" \
  ".LQA_FDN_%=:\n" \
  "s_waitcnt lgkmcnt(2)\n" \
  "v_mfma_f32_16x16x32_bf16 v[12:15], v[68:71], v[40:43], v[12:15]\n" \
  "ds_read_b128 v[36:39], v7 offset:8192\n" \
  "v_mov_b64 v[24:25], 0\n" \
  "v_lshrrev_b32 v26, 1, v6\n" \
  "buffer_store_dwordx2 v[24:25], v26, s[48:51], s61 offen sc1\n" \
  "v_mfma_f32_16x16x32_bf16 v[16:19], v[132:135], v[40:43], v[16:19]\n" \
  "v_mov_b32 v11, s82\n" \
  "v_mfma_f32_16x16x32_bf16 v[20:23], v[40:43], v[196:199], v[20:23]\n" \
  "s_waitcnt lgkmcnt(2)\n" \
  "v_mfma_f32_16x16x32_bf16 v[12:15], v[72:75], v[44:47], v[12:15]\n" \
  "ds_read_b128 v[40:43], v7 offset:10240\n" \
  "v_mfma_f32_16x16x32_bf16 v[16:19], v[136:139], v[44:47], v[16:19]\n" \
  "v_mfma_f32_16x16x32_bf16 v[20:23], v[44:47], v[200:203], v[20:23]\n" \
  "s_waitcnt lgkmcnt(2)\n" \
  "v_mfma_f32_16x16x32_bf16 v[12:15], v[76:79], v[48:51], v[12:15]\n" \
  "ds_read_b128 v[44:47], v7 offset:12288\n" \
  "v_mfma_f32_16x16x32_bf16 v[16:19], v[140:143], v[48:51], v[16:19]\n" \
  "v_mfma_f32_16x16x32_bf16 v[20:23], v[48:51], v[204:207], v[20:23]\n" \
  "s_waitcnt lgkmcnt(2)\n" \
  "v_mfma_f32_16x16x32_bf16 v[12:15], v[80:83], v[36:39], v[12:15]\n" \
  "ds_read_b128 v[48:51], v7 offset:14336\n" \
  "v_mfma_f32_16x16x32_bf16 v[16:19], v[144:147], v[36:39], v[16:19]\n" \
  "v_mfma_f32_16x16x32_bf16 v[20:23], v[36:39], v[208:211], v[20:23]\n" \
  "s_waitcnt lgkmcnt(2)\n" \
  "v_mfma_f32_16x16x32_bf16 v[12:15], v[84:87], v[40:43], v[12:15]\n" \
  "ds_read_b128 v[36:39], v7 offset:16384\n" \
  "v_mfma_f32_16x16x32_bf16 v[16:19], v[148:151], v[40:43], v[16:19]\n" \
  "v_mfma_f32_16x16x32_bf16 v[20:23], v[40:43], v[212:215], v[20:23]\n" \
  "s_waitcnt lgkmcnt(2)\n" \
  "v_mfma_f32_16x16x32_bf16 v[12:15], v[88:91], v[44:47], v[12:15]\n" \
  "ds_read_b128 v[40:43], v7 offset:18432\n" \
  "v_mfma_f32_16x16x32_bf16 v[16:19], v[152:155], v[44:47], v[16:19]\n" \
  "v_mfma_f32_16x16x32_bf16 v[20:23], v[44:47], v[216:219], v[20:23]\n" \
  "s_waitcnt lgkmcnt(2)\n" \
  "v_mfma_f32_16x16x32_bf16 v[12:15], v[92:95], v[48:51], v[12:15]\n" \
  "ds_read_b128 v[44:47], v7 offset:20480\n" \
  "v_mfma_f32_16x16x32_bf16 v[16:19], v[156:159], v[48:51], v[16:19]\n" \
  "v_mfma_f32_16x16x32_bf16 v[20:23], v[48:51], v[220:223], v[20:23]\n" \
  "s_waitcnt lgkmcnt(2)\n" \
  "v_mfma_f32_16x16x32_bf16 v[12:15], v[96:99], v[36:39], v[12:15]\n" \
  "ds_read_b128 v[48:51], v7 offset:22528\n" \
  "v_mfma_f32_16x16x32_bf16 v[16:19], v[160:163], v[36:39], v[16:19]\n" \
  "v_mfma_f32_16x16x32_bf16 v[20:23], v[36:39], v[224:227], v[20:23]\n" \
  "s_waitcnt lgkmcnt(2)\n" \
  "v_mfma_f32_16x16x32_bf16 v[12:15], v[100:103], v[40:43], v[12:15]\n" \
  "ds_read_b128 v[36:39], v7 offset:24576\n" \
  "v_mfma_f32_16x16x32_bf16 v[16:19], v[164:167], v[40:43], v[16:19]\n" \
  "v_mfma_f32_16x16x32_bf16 v[20:23], v[40:43], v[228:231], v[20:23]\n" \
  "s_waitcnt lgkmcnt(2)\n" \
  "v_mfma_f32_16x16x32_bf16 v[12:15], v[104:107], v[44:47], v[12:15]\n" \
  "ds_read_b128 v[40:43], v7 offset:26624\n" \
  "v_mfma_f32_16x16x32_bf16 v[16:19], v[168:171], v[44:47], v[16:19]\n" \
  "v_mfma_f32_16x16x32_bf16 v[20:23], v[44:47], v[232:235], v[20:23]\n" \
  "s_waitcnt lgkmcnt(2)\n" \
  "v_mfma_f32_16x16x32_bf16 v[12:15], v[108:111], v[48:51], v[12:15]\n" \
  "ds_read_b128 v[44:47], v7 offset:28672\n" \
  "v_mfma_f32_16x16x32_bf16 v[16:19], v[172:175], v[48:51], v[16:19]\n" \
  "v_mfma_f32_16x16x32_bf16 v[20:23], v[48:51], v[236:239], v[20:23]\n" \
  "s_waitcnt lgkmcnt(2)\n" \
  "v_mfma_f32_16x16x32_bf16 v[12:15], v[112:115], v[36:39], v[12:15]\n" \
  "ds_read_b128 v[48:51], v7 offset:30720\n" \
  "v_mfma_f32_16x16x32_bf16 v[16:19], v[176:179], v[36:39], v[16:19]\n" \
  "v_mfma_f32_16x16x32_bf16 v[20:23], v[36:39], v[240:243], v[20:23]\n" \
  "s_waitcnt lgkmcnt(2)\n" \
  "v_mfma_f32_16x16x32_bf16 v[12:15], v[116:119], v[40:43], v[12:15]\n" \
  "ds_read_b128 v[36:39], v7 offset:256\n" \
  "v_mfma_f32_16x16x32_bf16 v[16:19], v[180:183], v[40:43], v[16:19]\n" \
  "v_mfma_f32_16x16x32_bf16 v[20:23], v[40:43], v[244:247], v[20:23]\n" \
  "s_waitcnt lgkmcnt(2)\n" \
  "v_mfma_f32_16x16x32_bf16 v[12:15], v[120:123], v[44:47], v[12:15]\n" \
  "ds_read_b128 v[40:43], v7 offset:2304\n" \
  "v_mfma_f32_16x16x32_bf16 v[16:19], v[184:187], v[44:47], v[16:19]\n" \
  "v_mfma_f32_16x16x32_bf16 v[20:23], v[44:47], v[248:251], v[20:23]\n" \
  "s_waitcnt lgkmcnt(2)\n" \
  "v_mfma_f32_16x16x32_bf16 v[12:15], v[124:127], v[48:51], v[12:15]\n" \
  "ds_read_b128 v[44:47], v7 offset:4352\n" \
  "v_mfma_f32_16x16x32_bf16 v[16:19], v[188:191], v[48:51], v[16:19]\n" \
  "v_mfma_f32_16x16x32_bf16 v[20:23], v[48:51], v[252:255], v[20:23]\n" \
  "s_waitcnt lgkmcnt(2)\n" \
  "v_mfma_f32_16x16x32_bf16 v[24:27], v[64:67], v[36:39], 0\n" \
  "ds_read_b128 v[48:51], v7 offset:6400\n" \
  "v_mfma_f32_16x16x32_bf16 v[28:31], v[128:131], v[36:39], 0\n" \
  "v_mfma_f32_16x16x32_bf16 v[32:35], v[36:39], v[192:195], 0\n" \
  "s_waitcnt lgkmcnt(2)\n" \
  "v_mfma_f32_16x16x32_bf16 v[24:27], v[68:71], v[40:43], v[24:27]\n" \
  "ds_read_b128 v[36:39], v7 offset:8448\n" \
  "v_mfma_f32_16x16x32_bf16 v[28:31], v[132:135], v[40:43], v[28:31]\n" \
  "v_mfma_f32_16x16x32_bf16 v[32:35], v[40:43], v[196:199], v[32:35]\n" \
  "s_waitcnt lgkmcnt(2)\n" \
  "v_mfma_f32_16x16x32_bf16 v[24:27], v[72:75], v[44:47], v[24:27]\n" \
  "ds_read_b128 v[40:43], v7 offset:10496\n" \
  "v_mfma_f32_16x16x32_bf16 v[28:31], v[136:139], v[44:47], v[28:31]\n" \
  "v_mfma_f32_16x16x32_bf16 v[32:35], v[44:47], v[200:203], v[32:35]\n" \
  "s_waitcnt lgkmcnt(2)\n" \
  "v_mfma_f32_16x16x32_bf16 v[24:27], v[76:79], v[48:51], v[24:27]\n" \
  "ds_read_b128 v[44:47], v7 offset:12544\n" \
  "v_mfma_f32_16x16x32_bf16 v[28:31], v[140:143], v[48:51], v[28:31]\n" \
  "v_mfma_f32_16x16x32_bf16 v[32:35], v[48:51], v[204:207], v[32:35]\n" \
  "s_waitcnt lgkmcnt(2)\n" \
  "v_mfma_f32_16x16x32_bf16 v[24:27], v[80:83], v[36:39], v[24:27]\n" \
  "ds_read_b128 v[48:51], v7 offset:14592\n" \
  "v_mfma_f32_16x16x32_bf16 v[28:31], v[144:147], v[36:39], v[28:31]\n" \
  "v_mfma_f32_16x16x32_bf16 v[32:35], v[36:39], v[208:211], v[32:35]\n" \
  "s_barrier\n" \
  "s_mov_b32 s0, 0xbe801d62\n" \
  ".set .Lpad0x96, . - 4\n" \
  "s_waitcnt lgkmcnt(2)\n" \
  "v_mfma_f32_16x16x32_bf16 v[24:27], v[84:87], v[40:43], v[24:27]\n" \
  "ds_read_b128 v[36:39], v7 offset:16640\n" \
  "ds_read_b32 v11, v11 offset:0\n" \
  "v_mfma_f32_16x16x32_bf16 v[28:31], v[148:151], v[40:43], v[28:31]\n" \
  "ds_read2_b32 v[52:53], v8 offset0:0 offset1:0\n" \
  "v_mfma_f32_16x16x32_bf16 v[32:35], v[40:43], v[212:215], v[32:35]\n" \
  "s_waitcnt lgkmcnt(4)\n" \
  "v_mfma_f32_16x16x32_bf16 v[24:27], v[88:91], v[44:47], v[24:27]\n" \
  "ds_read_b128 v[40:43], v7 offset:18688\n" \
  "v_mfma_f32_16x16x32_bf16 v[28:31], v[152:155], v[44:47], v[28:31]\n" \
  "ds_read2_b32 v[54:55], v8 offset0:16 offset1:16\n" \
  "v_mfma_f32_16x16x32_bf16 v[32:35], v[44:47], v[216:219], v[32:35]\n" \
  "s_waitcnt lgkmcnt(3)\n" \
  "v_mfma_f32_16x16x32_bf16 v[24:27], v[92:95], v[48:51], v[24:27]\n" \
  "ds_read_b128 v[44:47], v7 offset:20736\n" \
  "v_readfirstlane_b32 s69, v11\n" \
  "s_cmp_eq_u32 s69, 1\n" \
  "s_cselect_b32 s76, 0x100000, s62\n" \
  "v_mfma_f32_16x16x32_bf16 v[28:31], v[156:159], v[48:51], v[28:31]\n" \
  "buffer_load_dword v11, v10, s[52:55], s76 offen sc1\n" \
  "v_mfma_f32_16x16x32_bf16 v[32:35], v[48:51], v[220:223], v[32:35]\n" \
  "ds_read2_b32 v[56:57], v8 offset0:32 offset1:48\n" \
  "v_mfma_f32_16x16x32_bf16 v[24:27], v[96:99], v[36:39], v[24:27]\n" \
  "ds_read_b128 v[48:51], v7 offset:22784\n" \
  "v_mfma_f32_16x16x32_bf16 v[28:31], v[160:163], v[36:39], v[28:31]\n" \
  "v_mfma_f32_16x16x32_bf16 v[32:35], v[36:39], v[224:227], v[32:35]\n" \
  "ds_read_b128 v[58:61], v9 offset:128\n" \
  "s_waitcnt lgkmcnt(6)\n" \
  "v_mfma_f32_16x16x16_bf16 v[12:15], v[0:1], v[52:53], v[12:15]\n" \
  "v_mfma_f32_16x16x16_bf16 v[16:19], v[2:3], v[52:53], v[16:19]\n" \
  "v_mfma_f32_16x16x16_bf16 v[20:23], v[52:53], v[4:5], v[20:23]\n" \
  "s_waitcnt lgkmcnt(4)\n" \
  "v_mfma_f32_16x16x16_bf16 v[24:27], v[0:1], v[54:55], v[24:27]\n" \
  "v_mfma_f32_16x16x16_bf16 v[28:31], v[2:3], v[54:55], v[28:31]\n" \
  "v_mfma_f32_16x16x16_bf16 v[32:35], v[54:55], v[4:5], v[32:35]\n" \
  "v_mfma_f32_16x16x32_bf16 v[24:27], v[100:103], v[40:43], v[24:27]\n" \
  "ds_read_b128 v[36:39], v7 offset:24832\n" \
  "v_mul_f32 v12, v12, v56\n" \
  "v_mfma_f32_16x16x32_bf16 v[28:31], v[164:167], v[40:43], v[28:31]\n" \
  "v_mul_f32 v13, v13, v56\n" \
  "v_mfma_f32_16x16x32_bf16 v[32:35], v[40:43], v[228:231], v[32:35]\n" \
  "ds_read_b128 v[52:55], v9 offset:192\n" \
  "s_waitcnt lgkmcnt(5)\n" \
  "v_mfma_f32_16x16x32_bf16 v[24:27], v[104:107], v[44:47], v[24:27]\n" \
  "ds_read_b128 v[40:43], v7 offset:26880\n" \
  "v_mul_f32 v14, v14, v56\n" \
  "v_mfma_f32_16x16x32_bf16 v[28:31], v[168:171], v[44:47], v[28:31]\n" \
  "v_cvt_pk_bf16_f32 v12, v12, v13\n" \
  "v_mfma_f32_16x16x32_bf16 v[32:35], v[44:47], v[232:235], v[32:35]\n" \
  "v_mul_f32 v15, v15, v56\n" \
  "s_waitcnt lgkmcnt(4)\n" \
  "v_mfma_f32_16x16x32_bf16 v[24:27], v[108:111], v[48:51], v[24:27]\n" \
  "ds_read_b128 v[44:47], v7 offset:28928\n" \
  "v_mul_f32 v16, v16, v56\n" \
  "v_mfma_f32_16x16x32_bf16 v[28:31], v[172:175], v[48:51], v[28:31]\n" \
  "v_cvt_pk_bf16_f32 v13, v14, v15\n" \
  "v_mfma_f32_16x16x32_bf16 v[32:35], v[48:51], v[236:239], v[32:35]\n" \
  "v_mul_f32 v17, v17, v56\n" \
  "s_waitcnt lgkmcnt(3)\n" \
  "v_mfma_f32_16x16x32_bf16 v[24:27], v[112:115], v[36:39], v[24:27]\n" \
  "ds_read_b128 v[48:51], v7 offset:30976\n" \
  "v_mul_f32 v18, v18, v56\n" \
  "v_mfma_f32_16x16x32_bf16 v[28:31], v[176:179], v[36:39], v[28:31]\n" \
  "v_cvt_pk_bf16_f32 v14, v16, v17\n" \
  "v_mfma_f32_16x16x32_bf16 v[32:35], v[36:39], v[240:243], v[32:35]\n" \
  "v_mul_f32 v19, v19, v56\n" \
  "s_waitcnt lgkmcnt(2)\n" \
  "v_mfma_f32_16x16x32_bf16 v[24:27], v[116:119], v[40:43], v[24:27]\n" \
  "v_cvt_pk_bf16_f32 v15, v18, v19\n" \
  "v_mfma_f32_16x16x32_bf16 v[28:31], v[180:183], v[40:43], v[28:31]\n" \
  "buffer_store_dwordx4 v[12:15], v6, s[40:43], s59 offen offset:0 sc1\n" \
  "v_mfma_f32_16x16x32_bf16 v[32:35], v[40:43], v[244:247], v[32:35]\n" \
  "v_mul_f32 v20, v20, v58\n" \
  "s_waitcnt lgkmcnt(1)\n" \
  "v_mfma_f32_16x16x32_bf16 v[24:27], v[120:123], v[44:47], v[24:27]\n" \
  "v_mul_f32 v21, v21, v59\n" \
  "v_mfma_f32_16x16x32_bf16 v[28:31], v[184:187], v[44:47], v[28:31]\n" \
  "v_mul_f32 v22, v22, v60\n" \
  "v_mfma_f32_16x16x32_bf16 v[32:35], v[44:47], v[248:251], v[32:35]\n" \
  "v_mul_f32 v23, v23, v61\n" \
  "s_waitcnt lgkmcnt(0)\n" \
  "v_mfma_f32_16x16x32_bf16 v[24:27], v[124:127], v[48:51], v[24:27]\n" \
  "v_cvt_pk_bf16_f32 v20, v20, v21\n" \
  "v_mfma_f32_16x16x32_bf16 v[28:31], v[188:191], v[48:51], v[28:31]\n" \
  "v_cvt_pk_bf16_f32 v21, v22, v23\n" \
  "v_mfma_f32_16x16x32_bf16 v[32:35], v[48:51], v[252:255], v[32:35]\n" \
  "s_nop 2\n" \
  "s_setprio 2\n" \
  "s_cmp_eq_u32 s69, 1\n" \
  "s_cbranch_scc1 .LQA_EPI_%=\n" \
  "s_waitcnt vmcnt(1)\n" \
  "v_cmp_eq_u32 vcc, 0, v11\n" \
  "s_cmp_eq_u64 vcc, 0\n" \
  "s_cbranch_scc0 .LQA_MISS_%=\n" \
  "s_mov_b32 m0, s58\n" \
  "s_add_u32 s74, s56, 4096\n" \
  "s_addc_u32 s75, s57, 0\n" \
  "s_add_u32 s76, s58, 4096\n" \
  "global_load_lds_dwordx4 v6, s[56:57] offset:0 sc1\n" \
  "global_load_lds_dwordx4 v6, s[56:57] offset:1024 sc1\n" \
  "global_load_lds_dwordx4 v6, s[56:57] offset:2048 sc1\n" \
  "global_load_lds_dwordx4 v6, s[56:57] offset:3072 sc1\n" \
  "s_mov_b32 m0, s76\n" \
  "s_nop 0\n" \
  "global_load_lds_dwordx4 v6, s[74:75] offset:0 sc1\n" \
  "global_load_lds_dwordx4 v6, s[74:75] offset:1024 sc1\n" \
  "global_load_lds_dwordx4 v6, s[74:75] offset:2048 sc1\n" \
  "global_load_lds_dwordx4 v6, s[74:75] offset:3072 sc1\n" \
  "s_mov_b32 s69, 3\n" \
  ".LQA_EPI_%=:\n" \
  "v_mul_f32 v24, v24, v57\n" \
  "v_mul_f32 v25, v25, v57\n" \
  "v_mul_f32 v26, v26, v57\n" \
  "v_cvt_pk_bf16_f32 v24, v24, v25\n" \
  "v_mul_f32 v27, v27, v57\n" \
  "v_mul_f32 v28, v28, v57\n" \
  "v_cvt_pk_bf16_f32 v25, v26, v27\n" \
  "v_mul_f32 v29, v29, v57\n" \
  "v_mul_f32 v30, v30, v57\n" \
  "v_cvt_pk_bf16_f32 v26, v28, v29\n" \
  "v_mul_f32 v31, v31, v57\n" \
  "v_cvt_pk_bf16_f32 v27, v30, v31\n" \
  "s_add_u32 s74, s59, 4096\n" \
  "buffer_store_dwordx4 v[24:27], v6, s[40:43], s74 offen offset:0 sc1\n" \
  "v_mul_f32 v32, v32, v52\n" \
  "v_mul_f32 v33, v33, v53\n" \
  "v_mul_f32 v34, v34, v54\n" \
  "v_mul_f32 v35, v35, v55\n" \
  "v_cvt_pk_bf16_f32 v22, v32, v33\n" \
  "v_cvt_pk_bf16_f32 v23, v34, v35\n" \
  "buffer_store_dwordx4 v[20:23], v6, s[44:47], s60 offen offset:0 sc1\n" \
  "s_setprio 0\n" \
  "s_add_u32 s59, s59, 0x80000\n" \
  "s_add_u32 s60, s60, 0x40000\n" \
  "s_add_u32 s61, s61, 0x10000\n" \
  "v_xor_b32 v7, 0x20000, v7\n" \
  "s_cmp_eq_u32 s69, 1\n" \
  "s_waitcnt vmcnt(3)\n" \
  "s_cbranch_scc1 .LQA_GO_%=\n" \
  "s_lshr_b32 s69, s69, 1\n" \
  "s_waitcnt vmcnt(2)\n" \
  "s_cbranch_scc1 .LQA_GO_%=\n" \
  "v_cmp_eq_u32 vcc, 0, v11\n" \
  "s_cmp_eq_u64 vcc, 0\n" \
  "s_cbranch_scc0 .LQA_NOX_%=\n" \
  "s_mov_b32 m0, s58\n" \
  "s_add_u32 s74, s56, 4096\n" \
  "s_addc_u32 s75, s57, 0\n" \
  "s_add_u32 s76, s58, 4096\n" \
  "global_load_lds_dwordx4 v6, s[56:57] offset:0 sc1\n" \
  "global_load_lds_dwordx4 v6, s[56:57] offset:1024 sc1\n" \
  "global_load_lds_dwordx4 v6, s[56:57] offset:2048 sc1\n" \
  "global_load_lds_dwordx4 v6, s[56:57] offset:3072 sc1\n" \
  "s_mov_b32 m0, s76\n" \
  "s_nop 0\n" \
  "global_load_lds_dwordx4 v6, s[74:75] offset:0 sc1\n" \
  "global_load_lds_dwordx4 v6, s[74:75] offset:1024 sc1\n" \
  "global_load_lds_dwordx4 v6, s[74:75] offset:2048 sc1\n" \
  "global_load_lds_dwordx4 v6, s[74:75] offset:3072 sc1\n" \
  "s_mov_b32 s69, 2\n" \
  "s_waitcnt vmcnt(8)\n" \
  "s_branch .LQA_CNT_%=\n" \
  ".Lpad0x97:\n" \
  "s_setpc_b64 s[98:99]\n" \
  ".LQA_MISS_%=:\n" \
  "buffer_load_dword v11, v10, s[52:55], s62 offen sc1\n" \
  "s_branch .LQA_EPI_%=\n" \
  ".LQA_NOX_%=:\n" \
  "s_waitcnt vmcnt(0)\n" \
  ".LQA_CNT_%=:\n" \
  "v_mov_b32 v52, s80\n" \
  "v_mov_b32 v53, 0x10000\n" \
  "s_mov_b64 exec, 1\n" \
  "ds_add_rtn_u32 v54, v52, v53\n" \
  "s_memrealtime s[0:1]\n" \
  "s_mov_b64 exec, -1\n" \
  "s_add_u32 s67, s67, 4\n" \
  "s_add_u32 s64, s64, 4096\n" \
  "s_addc_u32 s65, s65, 0\n" \
  "v_mov_b32 v55, 0\n" \
  "s_waitcnt lgkmcnt(0)\n" \
  "v_readfirstlane_b32 s74, v54\n" \
  "s_and_b32 s76, s74, 0x30000\n" \
  "s_cmp_lg_u32 s76, 0x30000\n" \
  "s_cbranch_scc1 .LQA_FWN_%=\n" \
  "s_mov_b32 s79, 0x100000\n" \
  "s_branch .LQA_FWT_%=\n" \
  ".LQA_FW_%=:\n" \
  "ds_read_b32 v54, v52\n" \
  "s_sub_u32 s79, s79, 1\n" \
  "s_cbranch_scc1 .LQA_FWE_%=\n" \
  "s_waitcnt lgkmcnt(0)\n" \
  "v_readfirstlane_b32 s74, v54\n" \
  ".LQA_FWT_%=:\n" \
  "s_and_b32 s74, s74, 0xffff\n" \
  "s_cmp_lt_u32 s74, s67\n" \
  "s_cbranch_scc1 .LQA_FW_%=\n" \
  ".LQA_FWE_%=:\n" \
  "v_or_b32 v53, 1, s0\n" \
  "s_cmp_lg_u32 s67, s83\n" \
  "s_cbranch_scc1 .LQA_FWX_%=\n" \
  "v_lshlrev_b32 v54, 5, v6\n" \
  "s_mov_b64 exec, 15\n" \
  "global_store_dword v54, v53, s[84:85] sc1\n" \
  ".LQA_FWX_%=:\n" \
  "s_mov_b64 exec, 1\n" \
  "global_store_dword v55, v53, s[64:65] offset:-4096 sc1\n" \
  "s_mov_b64 exec, -1\n" \
  "s_waitcnt vmcnt(1)\n" \
  "s_branch .LQA_GO_%=\n" \
  ".LQA_FWN_%=:\n" \
  "s_waitcnt vmcnt(0)\n" \
  ".LQA_GO_%=:\n" \
  "s_sub_u32 s66, s66, 1\n" \
  "s_cbranch_scc1 .LQA_LAST_%=\n" \
  "s_cmp_lg_u32 s69, 0\n" \
  "s_cbranch_scc1 .LQA_NEXT_%=\n" \
  ".LQA_GATE_%=:\n" \
  "s_memrealtime s[74:75]\n" \
  "s_waitcnt lgkmcnt(0)\n" \
  "s_sub_i32 s74, s68, s74\n" \
  "s_cmp_gt_u32 s74, %[gq]\n" \
  "s_cbranch_scc1 .LQA_POLL_%=\n" \
  "s_sleep 3\n" \
  "s_branch .LQA_GATE_%=\n" \
  ".LQA_POLL_%=:\n" \
  "s_mov_b32 s79, 0x40000\n" \
  ".LQA_AGAIN_%=:\n" \
  "buffer_load_dword v11, v10, s[52:55], s62 offen sc1\n" \
  "s_sub_u32 s79, s79, 1\n" \
  "s_cbranch_scc1 .LQA_SEEN_%=\n" \
  "s_waitcnt vmcnt(0)\n" \
  "v_cmp_eq_u32 vcc, 0, v11\n" \
  "s_cmp_eq_u64 vcc, 0\n" \
  "s_cbranch_scc1 .LQA_SEEN_%=\n" \
  "s_sleep %[sq]\n" \
  "s_cmp_ge_u32 s66, s63\n" \
  "s_cbranch_scc1 .LQA_AGAIN_%=\n" \
  "s_sleep %[s1]\n" \
  "s_branch .LQA_AGAIN_%=\n" \
  ".LQA_SEEN_%=:\n" \
  "s_mov_b32 m0, s58\n" \
  "s_add_u32 s74, s56, 4096\n" \
  "s_addc_u32 s75, s57, 0\n" \
  "s_add_u32 s76, s58, 4096\n" \
  "global_load_lds_dwordx4 v6, s[56:57] offset:0 sc1\n" \
  "global_load_lds_dwordx4 v6, s[56:57] offset:1024 sc1\n" \
  "global_load_lds_dwordx4 v6, s[56:57] offset:2048 sc1\n" \
  "global_load_lds_dwordx4 v6, s[56:57] offset:3072 sc1\n" \
  "s_mov_b32 m0, s76\n" \
  "s_nop 0\n" \
  "global_load_lds_dwordx4 v6, s[74:75] offset:0 sc1\n" \
  "global_load_lds_dwordx4 v6, s[74:75] offset:1024 sc1\n" \
  "global_load_lds_dwordx4 v6, s[74:75] offset:2048 sc1\n" \
  "global_load_lds_dwordx4 v6, s[74:75] offset:3072 sc1\n" \
  ".LQA_GOT_%=:\n" \
  "s_memrealtime s[76:77]\n" \
  "s_waitcnt lgkmcnt(0)\n" \
  "s_add_u32 s68, s76, %[gq]\n" \
  "s_waitcnt vmcnt(0)\n" \
  ".LQA_NEXT_%=:\n" \
  "s_add_u32 s56, s56, 0x8000\n" \
  "s_addc_u32 s57, s57, 0\n" \
  "s_xor_b32 s58, s58, 0x20000\n" \
  "s_add_u32 s62, s62, 4096\n" \
  "s_branch .LQA_TOP_%=\n" \
  ".LQA_LAST_%=:\n" \
  "s_barrier\n" \
  "s_cmp_eq_u32 s86, 0\n" \
  "s_cbranch_scc1 .LQA_END_%=\n" \
  "s_mov_b32 s86, 0\n" \
  "s_mov_b32 s66, 63\n" \
  "s_mov_b32 s62, 0x40000\n" \
  "s_add_u32 s54, s54, (128 - %[n0]) * 4096\n" \
  "s_add_u32 s59, s59, (64 - %[n0]) * 0x80000\n" \
  "s_add_u32 s60, s60, (64 - %[n0]) * 0x40000\n" \
  "s_add_u32 s61, s61, (64 - %[n0]) * 0x10000\n" \
  "s_add_u32 s64, s64, (65 - %[n0]) * 4096\n" \
  "s_addc_u32 s65, s65, 0\n" \
  "s_add_u32 s56, s56, %[x3]\n" \
  "s_addc_u32 s57, s57, 0\n" \
  "s_add_u32 s84, s84, 2048\n" \
  "s_addc_u32 s85, s85, 0\n" \
  "s_add_u32 s67, s67, 4\n" \
  "s_add_u32 s83, s67, 4\n" \
  "s_branch .LQA_POLL_%=\n" \
  ".LQA_END_%=:\n" \
  "s_endpgm\n" \
  ".p2alignl 12, 0xbf800000\n" \
  ".Lpad0xba:\n" \
  "s_setpc_b64 s[98:99]\n"
#define QKV_ASM_1 \
  ".LQB_GO_%=:\n" \
  "s_barrier\n" \
  "ds_read_b128 v[36:39], v10 offset:0\n" \
  "ds_read_b128 v[40:43], v7 offset:0\n" \
  "ds_read_b128 v[44:47], v10 offset:2048\n" \
  "ds_read_b128 v[48:51], v7 offset:2048\n" \
  "s_branch .LQB_AL_%=\n" \
  ".p2alignl 12, 0xbf800000, 3200\n" \
  ".p2align 6\n" \
  ".Lpad0xa9:\n" \
  "s_setpc_b64 s[98:99]\n" \
  ".p2align 6\n" \
  ".LQB_AL_%=:\n" \
  ".LQB_TOP_%=:\n" \
  "s_setprio 3\n" \
  "s_waitcnt lgkmcnt(3)\n" \
  "v_mfma_f32_16x16x32_bf16 v[56:59], v[36:39], v[36:39], 0\n" \
  "ds_read_b128 v[52:55], v10 offset:4096\n" \
  "s_add_u32 s56, s56, 0x8000\n" \
  "s_addc_u32 s57, s57, 0\n" \
  "s_xor_b32 s58, s58, 0x20000\n" \
  "s_add_u32 s62, s62, 4096\n" \
  "s_waitcnt lgkmcnt(3)\n" \
  "v_mfma_f32_16x16x32_bf16 v[12:15], v[40:43], v[192:195], 0\n" \
  "ds_read_b128 v[36:39], v7 offset:4096\n" \
  "s_waitcnt lgkmcnt(3)\n" \
  "v_mfma_f32_16x16x32_bf16 v[56:59], v[44:47], v[44:47], v[56:59]\n" \
  "ds_read_b128 v[40:43], v10 offset:6144\n" \
  "s_waitcnt lgkmcnt(3)\n" \
  "v_mfma_f32_16x16x32_bf16 v[12:15], v[48:51], v[196:199], v[12:15]\n" \
  "ds_read_b128 v[44:47], v7 offset:6144\n" \
  "s_waitcnt lgkmcnt(3)\n" \
  "v_mfma_f32_16x16x32_bf16 v[56:59], v[52:55], v[52:55], v[56:59]\n" \
  "ds_read_b128 v[48:51], v10 offset:8192\n" \
  "s_waitcnt lgkmcnt(3)\n" \
  "v_mfma_f32_16x16x32_bf16 v[12:15], v[36:39], v[200:203], v[12:15]\n" \
  "ds_read_b128 v[52:55], v7 offset:8192\n" \
  "s_waitcnt lgkmcnt(3)\n" \
  "v_mfma_f32_16x16x32_bf16 v[56:59], v[40:43], v[40:43], v[56:59]\n" \
  "ds_read_b128 v[36:39], v10 offset:10240\n" \
  "s_waitcnt lgkmcnt(3)\n" \
  "v_mfma_f32_16x16x32_bf16 v[12:15], v[44:47], v[204:207], v[12:15]\n" \
  "ds_read_b128 v[40:43], v7 offset:10240\n" \
  "s_waitcnt lgkmcnt(3)\n" \
  "v_mfma_f32_16x16x32_bf16 v[56:59], v[48:51], v[48:51], v[56:59]\n" \
  "ds_read_b128 v[44:47], v10 offset:12288\n" \
  "s_waitcnt lgkmcnt(3)\n" \
  "v_mfma_f32_16x16x32_bf16 v[12:15], v[52:55], v[208:211], v[12:15]\n" \
  "ds_read_b128 v[48:51], v7 offset:12288\n" \
  "s_waitcnt lgkmcnt(3)\n" \
  "v_mfma_f32_16x16x32_bf16 v[56:59], v[36:39], v[36:39], v[56:59]\n" \
  "ds_read_b128 v[52:55], v10 offset:14336\n" \
  "s_waitcnt lgkmcnt(3)\n" \
  "v_mfma_f32_16x16x32_bf16 v[12:15], v[40:43], v[212:215], v[12:15]\n" \
  "ds_read_b128 v[36:39], v7 offset:14336\n" \
  "s_waitcnt lgkmcnt(3)\n" \
  "v_mfma_f32_16x16x32_bf16 v[56:59], v[44:47], v[44:47], v[56:59]\n" \
  "ds_read_b128 v[40:43], v10 offset:16384\n" \
  "s_waitcnt lgkmcnt(3)\n" \
  "v_mfma_f32_16x16x32_bf16 v[12:15], v[48:51], v[216:219], v[12:15]\n" \
  "ds_read_b128 v[44:47], v7 offset:16384\n" \
  "s_waitcnt lgkmcnt(3)\n" \
  "v_mfma_f32_16x16x32_bf16 v[56:59], v[52:55], v[52:55], v[56:59]\n" \
  "ds_read_b128 v[48:51], v10 offset:18432\n" \
  "s_waitcnt lgkmcnt(3)\n" \
  "v_mfma_f32_16x16x32_bf16 v[12:15], v[36:39], v[220:223], v[12:15]\n" \
  "ds_read_b128 v[52:55], v7 offset:18432\n" \
  "s_waitcnt lgkmcnt(3)\n" \
  "v_mfma_f32_16x16x32_bf16 v[56:59], v[40:43], v[40:43], v[56:59]\n" \
  "ds_read_b128 v[36:39], v10 offset:20480\n" \
  "s_waitcnt lgkmcnt(3)\n" \
  "v_mfma_f32_16x16x32_bf16 v[12:15], v[44:47], v[224:227], v[12:15]\n" \
  "ds_read_b128 v[40:43], v7 offset:20480\n" \
  "s_waitcnt lgkmcnt(3)\n" \
  "v_mfma_f32_16x16x32_bf16 v[56:59], v[48:51], v[48:51], v[56:59]\n" \
  "ds_read_b128 v[44:47], v10 offset:22528\n" \
  "s_waitcnt lgkmcnt(3)\n" \
  "v_mfma_f32_16x16x32_bf16 v[12:15], v[52:55], v[228:231], v[12:15]\n" \
  "ds_read_b128 v[48:51], v7 offset:22528\n" \
  "s_waitcnt lgkmcnt(3)\n" \
  "v_mfma_f32_16x16x32_bf16 v[56:59], v[36:39], v[36:39], v[56:59]\n" \
  "ds_read_b128 v[52:55], v10 offset:24576\n" \
  "s_waitcnt lgkmcnt(3)\n" \
  "v_mfma_f32_16x16x32_bf16 v[12:15], v[40:43], v[232:235], v[12:15]\n" \
  "ds_read_b128 v[36:39], v7 offset:24576\n" \
  "s_waitcnt lgkmcnt(3)\n" \
  "v_mfma_f32_16x16x32_bf16 v[56:59], v[44:47], v[44:47], v[56:59]\n" \
  "ds_read_b128 v[40:43], v10 offset:26624\n" \
  "s_waitcnt lgkmcnt(3)\n" \
  "v_mfma_f32_16x16x32_bf16 v[12:15], v[48:51], v[236:239], v[12:15]\n" \
  "ds_read_b128 v[44:47], v7 offset:26624\n" \
  "s_waitcnt lgkmcnt(3)\n" \
  "v_mfma_f32_16x16x32_bf16 v[56:59], v[52:55], v[52:55], v[56:59]\n" \
  "ds_read_b128 v[48:51], v10 offset:28672\n" \
  "s_waitcnt lgkmcnt(3)\n" \
  "v_mfma_f32_16x16x32_bf16 v[12:15], v[36:39], v[240:243], v[12:15]\n" \
  "ds_read_b128 v[52:55], v7 offset:28672\n" \
  "s_waitcnt lgkmcnt(3)\n" \
  "v_mfma_f32_16x16x32_bf16 v[56:59], v[40:43], v[40:43], v[56:59]\n" \
  "ds_read_b128 v[36:39], v10 offset:30720\n" \
  "s_waitcnt lgkmcnt(3)\n" \
  "v_mfma_f32_16x16x32_bf16 v[12:15], v[44:47], v[244:247], v[12:15]\n" \
  "ds_read_b128 v[40:43], v7 offset:30720\n" \
  "s_waitcnt lgkmcnt(3)\n" \
  "v_mfma_f32_16x16x32_bf16 v[56:59], v[48:51], v[48:51], v[56:59]\n" \
  "ds_read_b128 v[44:47], v7 offset:256\n" \
  "s_waitcnt lgkmcnt(3)\n" \
  "v_mfma_f32_16x16x32_bf16 v[12:15], v[52:55], v[248:251], v[12:15]\n" \
  "ds_read_b128 v[48:51], v7 offset:2304\n" \
  "s_waitcnt lgkmcnt(3)\n" \
  "v_mfma_f32_16x16x32_bf16 v[56:59], v[36:39], v[36:39], v[56:59]\n" \
  "ds_read_b128 v[52:55], v7 offset:4352\n" \
  "s_waitcnt lgkmcnt(3)\n" \
  "v_mfma_f32_16x16x32_bf16 v[12:15], v[40:43], v[252:255], v[12:15]\n" \
  "ds_read_b128 v[36:39], v7 offset:6400\n" \
  "s_waitcnt lgkmcnt(3)\n" \
  "v_mfma_f32_16x16x32_bf16 v[16:19], v[44:47], v[192:195], 0\n" \
  "ds_read_b128 v[40:43], v7 offset:8448\n" \
  "s_waitcnt lgkmcnt(3)\n" \
  "v_mfma_f32_16x16x32_bf16 v[16:19], v[48:51], v[196:199], v[16:19]\n" \
  "ds_read_b128 v[44:47], v7 offset:10496\n" \
  "s_waitcnt lgkmcnt(3)\n" \
  "v_mfma_f32_16x16x32_bf16 v[16:19], v[52:55], v[200:203], v[16:19]\n" \
  "ds_read_b128 v[48:51], v7 offset:12544\n" \
  "s_waitcnt lgkmcnt(3)\n" \
  "v_mfma_f32_16x16x32_bf16 v[16:19], v[36:39], v[204:207], v[16:19]\n" \
  "ds_read_b128 v[52:55], v7 offset:14592\n" \
  "s_waitcnt lgkmcnt(3)\n" \
  "v_mfma_f32_16x16x32_bf16 v[16:19], v[40:43], v[208:211], v[16:19]\n" \
  "ds_read_b128 v[36:39], v7 offset:16640\n" \
  "s_waitcnt lgkmcnt(3)\n" \
  "v_mfma_f32_16x16x32_bf16 v[16:19], v[44:47], v[212:215], v[16:19]\n" \
  "ds_read_b128 v[40:43], v7 offset:18688\n" \
  "s_waitcnt lgkmcnt(3)\n" \
  "v_mfma_f32_16x16x32_bf16 v[16:19], v[48:51], v[216:219], v[16:19]\n" \
  "ds_read_b128 v[44:47], v7 offset:20736\n" \
  "s_waitcnt lgkmcnt(3)\n" \
  "v_mfma_f32_16x16x32_bf16 v[16:19], v[52:55], v[220:223], v[16:19]\n" \
  "ds_read_b128 v[48:51], v7 offset:22784\n" \
  "s_waitcnt lgkmcnt(3)\n" \
  "v_mfma_f32_16x16x32_bf16 v[16:19], v[36:39], v[224:227], v[16:19]\n" \
  "ds_read_b128 v[52:55], v7 offset:24832\n" \
  "s_waitcnt lgkmcnt(3)\n" \
  "v_mfma_f32_16x16x32_bf16 v[16:19], v[40:43], v[228:231], v[16:19]\n" \
  "ds_read_b128 v[36:39], v7 offset:26880\n" \
  "s_waitcnt lgkmcnt(3)\n" \
  "v_mfma_f32_16x16x32_bf16 v[16:19], v[44:47], v[232:235], v[16:19]\n" \
  "ds_read_b128 v[40:43], v7 offset:28928\n" \
  "s_waitcnt lgkmcnt(3)\n" \
  "v_mfma_f32_16x16x32_bf16 v[16:19], v[48:51], v[236:239], v[16:19]\n" \
  "ds_read_b128 v[44:47], v7 offset:30976\n" \
  "s_waitcnt lgkmcnt(3)\n" \
  "v_mfma_f32_16x16x32_bf16 v[16:19], v[52:55], v[240:243], v[16:19]\n" \
  "ds_read_b128 v[48:51], v7 offset:0\n" \
  "v_lshrrev_b32 v63, 2, v6\n" \
  "v_and_b32 v63, 0x9c, v63\n" \
  "buffer_load_dword v63, v63, s[52:55], s62 offen sc1\n" \
  "s_waitcnt lgkmcnt(3)\n" \
  "v_mfma_f32_16x16x32_bf16 v[16:19], v[36:39], v[244:247], v[16:19]\n" \
  "ds_read_b128 v[52:55], v7 offset:2048\n" \
  "s_waitcnt lgkmcnt(3)\n" \
  "v_mfma_f32_16x16x32_bf16 v[16:19], v[40:43], v[248:251], v[16:19]\n" \
  "ds_read_b128 v[36:39], v7 offset:4096\n" \
  "s_waitcnt lgkmcnt(3)\n" \
  "v_mfma_f32_16x16x32_bf16 v[16:19], v[44:47], v[252:255], v[16:19]\n" \
  "ds_read_b128 v[40:43], v7 offset:6144\n" \
  "s_setprio 0\n" \
  "s_mov_b32 s0, 0xbe801d62\n" \
  ".set .Lpad0xaa, . - 4\n" \
  "v_cndmask_b32 v56, v56, v57, s[70:71]\n" \
  "v_cndmask_b32 v57, v58, v59, s[70:71]\n" \
  "v_cndmask_b32 v56, v56, v57, s[72:73]\n" \
  "v_mov_b32 v57, v56\n" \
  "v_mov_b32 v58, v56\n" \
  "v_mul_f32 v56, 0x3b000000, v56\n" \
  "v_xor_b32 v10, v11, v10\n" \
  "v_permlane32_swap_b32 v57, v58\n" \
  "v_mul_f32 v57, 0xbb000000, v58\n" \
  "v_fma_f32 v56, -v57, v57, v56\n" \
  "v_max_f32 v56, 0, v56\n" \
  "v_cvt_pk_bf16_f32 v56, v56, v56\n" \
  "v_lshlrev_b32 v56, 16, v56\n" \
  "v_add_f32 v56, 0x3727c5ac, v56\n" \
  "v_cvt_pk_bf16_f32 v56, v56, v56\n" \
  "v_lshlrev_b32 v56, 16, v56\n" \
  "v_sqrt_f32 v56, v56\n" \
  "v_cvt_pk_bf16_f32 v57, v57, v57\n" \
  "v_cvt_pk_bf16_f32 v56, v56, v56\n" \
  "v_lshlrev_b32 v56, 16, v56\n" \
  "v_rcp_f32 v56, v56\n" \
  "s_mov_b64 exec, 0xf0000f\n" \
  "ds_write2_b32 v62, v57, v56 offset1:32\n" \
  "s_mov_b64 exec, -1\n" \
  "s_cmp_eq_u32 s66, s83\n" \
  "s_cbranch_scc1 .LQB_NOE_%=\n" \
  "v_mul_f32 v20, v20, v60\n" \
  "v_mul_f32 v21, v21, v60\n" \
  "v_mul_f32 v22, v22, v60\n" \
  "v_cvt_pk_bf16_f32 v20, v20, v21\n" \
  "v_mul_f32 v23, v23, v60\n" \
  "v_mul_f32 v24, v24, v60\n" \
  "v_cvt_pk_bf16_f32 v21, v22, v23\n" \
  "v_mul_f32 v25, v25, v60\n" \
  "v_mul_f32 v26, v26, v60\n" \
  "v_cvt_pk_bf16_f32 v22, v24, v25\n" \
  "v_mul_f32 v27, v27, v60\n" \
  "v_cvt_pk_bf16_f32 v23, v26, v27\n" \
  "buffer_store_dwordx4 v[20:23], v6, s[40:43], s59 offen offset:0 sc1\n" \
  "s_add_u32 s76, s59, 4096\n" \
  "v_mul_f32 v28, v28, v61\n" \
  "v_mul_f32 v29, v29, v61\n" \
  "v_mul_f32 v30, v30, v61\n" \
  "v_cvt_pk_bf16_f32 v28, v28, v29\n" \
  "v_mul_f32 v31, v31, v61\n" \
  "v_mul_f32 v32, v32, v61\n" \
  "v_cvt_pk_bf16_f32 v29, v30, v31\n" \
  "v_mul_f32 v33, v33, v61\n" \
  "v_mul_f32 v34, v34, v61\n" \
  "v_cvt_pk_bf16_f32 v30, v32, v33\n" \
  "v_mul_f32 v35, v35, v61\n" \
  "v_cvt_pk_bf16_f32 v31, v34, v35\n" \
  "buffer_store_dwordx4 v[28:31], v6, s[40:43], s76 offen offset:0 sc1\n" \
  "s_add_u32 s59, s59, 0x80000\n" \
  "s_waitcnt vmcnt(2)\n" \
  "s_branch .LQB_LOOK_%=\n" \
  ".LQB_NOE_%=:\n" \
  "s_waitcnt vmcnt(0)\n" \
  ".LQB_LOOK_%=:\n" \
  "v_cmp_eq_u32 vcc, 0, v63\n" \
  "v_mov_b32 v58, s82\n" \
  "s_cmp_eq_u64 vcc, 0\n" \
  "s_cselect_b32 s69, 1, 0\n" \
  "v_mov_b32 v59, s69\n" \
  "ds_write_b32 v58, v59\n" \
  "s_cbranch_scc0 .LQB_NOD_%=\n" \
  "s_mov_b32 m0, s58\n" \
  "s_add_u32 s74, s56, 4096\n" \
  "s_addc_u32 s75, s57, 0\n" \
  "s_add_u32 s76, s58, 4096\n" \
  "global_load_lds_dwordx4 v6, s[56:57] offset:0 sc1\n" \
  "global_load_lds_dwordx4 v6, s[56:57] offset:1024 sc1\n" \
  "global_load_lds_dwordx4 v6, s[56:57] offset:2048 sc1\n" \
  "global_load_lds_dwordx4 v6, s[56:57] offset:3072 sc1\n" \
  "s_mov_b32 m0, s76\n" \
  "s_nop 0\n" \
  "global_load_lds_dwordx4 v6, s[74:75] offset:0 sc1\n" \
  "global_load_lds_dwordx4 v6, s[74:75] offset:1024 sc1\n" \
  "global_load_lds_dwordx4 v6, s[74:75] offset:2048 sc1\n" \
  "global_load_lds_dwordx4 v6, s[74:75] offset:3072 sc1\n" \
  ".LQB_NOD_%=:\n" \
  "s_waitcnt lgkmcnt(0)\n" \
  "s_barrier\n" \
  "ds_read2_b32 v[56:57], v8 offset0:0 offset1:0\n" \
  "ds_read2_b32 v[58:59], v8 offset0:16 offset1:16\n" \
  "ds_read_b128 v[28:31], v9 offset:128\n" \
  "ds_read_b128 v[32:35], v9 offset:192\n" \
  "ds_read2_b32 v[60:61], v8 offset0:32 offset1:48\n" \
  "s_setprio 3\n" \
  "s_waitcnt lgkmcnt(3)\n" \
  "v_mfma_f32_16x16x16_bf16 v[12:15], v[56:57], v[4:5], v[12:15]\n" \
  "v_mfma_f32_16x16x16_bf16 v[16:19], v[58:59], v[4:5], v[16:19]\n" \
  "s_setprio 0\n" \
  "s_waitcnt lgkmcnt(0)\n" \
  "s_nop 4\n" \
  "v_mul_f32 v12, v12, v28\n" \
  "v_mul_f32 v13, v13, v29\n" \
  "v_mul_f32 v14, v14, v30\n" \
  "v_mul_f32 v15, v15, v31\n" \
  "v_cvt_pk_bf16_f32 v12, v12, v13\n" \
  "v_cvt_pk_bf16_f32 v13, v14, v15\n" \
  "v_mul_f32 v16, v16, v32\n" \
  "v_mul_f32 v17, v17, v33\n" \
  "v_mul_f32 v18, v18, v34\n" \
  "v_mul_f32 v19, v19, v35\n" \
  "v_cvt_pk_bf16_f32 v14, v16, v17\n" \
  "v_cvt_pk_bf16_f32 v15, v18, v19\n" \
  "buffer_store_dwordx4 v[12:15], v6, s[44:47], s60 offen offset:0 sc1\n" \
  "v_mov_b64 v[16:17], 0\n" \
  "v_lshrrev_b32 v18, 1, v6\n" \
  "buffer_store_dwordx2 v[16:17], v18, s[48:51], s61 offen sc1\n" \
  "s_setprio 0\n" \
  "s_add_u32 s60, s60, 0x40000\n" \
  "s_add_u32 s61, s61, 0x10000\n" \
  "s_mov_b32 s0, 0xbe801d62\n" \
  ".set .Lpad0xab, . - 4\n" \
  "v_mov_b32 v12, s80\n" \
  "v_mov_b32 v13, 1\n" \
  "v_mfma_f32_16x16x16_bf16 v[20:23], v[0:1], v[56:57], 0\n" \
  "v_mfma_f32_16x16x16_bf16 v[24:27], v[2:3], v[56:57], 0\n" \
  "v_mfma_f32_16x16x16_bf16 v[28:31], v[0:1], v[58:59], 0\n" \
  "v_mfma_f32_16x16x16_bf16 v[32:35], v[2:3], v[58:59], 0\n" \
  "v_mfma_f32_16x16x32_bf16 v[20:23], v[64:67], v[48:51], v[20:23]\n" \
  "ds_read_b128 v[44:47], v7 offset:8192\n" \
  "v_mfma_f32_16x16x32_bf16 v[24:27], v[128:131], v[48:51], v[24:27]\n" \
  "v_mfma_f32_16x16x32_bf16 v[20:23], v[68:71], v[52:55], v[20:23]\n" \
  "ds_read_b128 v[48:51], v7 offset:10240\n" \
  "v_mfma_f32_16x16x32_bf16 v[24:27], v[132:135], v[52:55], v[24:27]\n" \
  "v_mfma_f32_16x16x32_bf16 v[20:23], v[72:75], v[36:39], v[20:23]\n" \
  "ds_read_b128 v[52:55], v7 offset:12288\n" \
  "v_mfma_f32_16x16x32_bf16 v[24:27], v[136:139], v[36:39], v[24:27]\n" \
  "v_mfma_f32_16x16x32_bf16 v[20:23], v[76:79], v[40:43], v[20:23]\n" \
  "ds_read_b128 v[36:39], v7 offset:14336\n" \
  "v_mfma_f32_16x16x32_bf16 v[24:27], v[140:143], v[40:43], v[24:27]\n" \
  "s_waitcnt lgkmcnt(3)\n" \
  "v_mfma_f32_16x16x32_bf16 v[20:23], v[80:83], v[44:47], v[20:23]\n" \
  "ds_read_b128 v[40:43], v7 offset:16384\n" \
  "v_mfma_f32_16x16x32_bf16 v[24:27], v[144:147], v[44:47], v[24:27]\n" \
  "s_waitcnt lgkmcnt(3)\n" \
  "v_mfma_f32_16x16x32_bf16 v[20:23], v[84:87], v[48:51], v[20:23]\n" \
  "ds_read_b128 v[44:47], v7 offset:18432\n" \
  "v_mfma_f32_16x16x32_bf16 v[24:27], v[148:151], v[48:51], v[24:27]\n" \
  "s_waitcnt lgkmcnt(3)\n" \
  "v_mfma_f32_16x16x32_bf16 v[20:23], v[88:91], v[52:55], v[20:23]\n" \
  "ds_read_b128 v[48:51], v7 offset:20480\n" \
  "v_mfma_f32_16x16x32_bf16 v[24:27], v[152:155], v[52:55], v[24:27]\n" \
  "s_waitcnt lgkmcnt(3)\n" \
  "v_mfma_f32_16x16x32_bf16 v[20:23], v[92:95], v[36:39], v[20:23]\n" \
  "ds_read_b128 v[52:55], v7 offset:22528\n" \
  "v_mfma_f32_16x16x32_bf16 v[24:27], v[156:159], v[36:39], v[24:27]\n" \
  "s_waitcnt lgkmcnt(3)\n" \
  "v_mfma_f32_16x16x32_bf16 v[20:23], v[96:99], v[40:43], v[20:23]\n" \
  "ds_read_b128 v[36:39], v7 offset:24576\n" \
  "v_mfma_f32_16x16x32_bf16 v[24:27], v[160:163], v[40:43], v[24:27]\n" \
  "s_waitcnt lgkmcnt(3)\n" \
  "v_mfma_f32_16x16x32_bf16 v[20:23], v[100:103], v[44:47], v[20:23]\n" \
  "ds_read_b128 v[40:43], v7 offset:26624\n" \
  "v_mfma_f32_16x16x32_bf16 v[24:27], v[164:167], v[44:47], v[24:27]\n" \
  "s_waitcnt lgkmcnt(3)\n" \
  "v_mfma_f32_16x16x32_bf16 v[20:23], v[104:107], v[48:51], v[20:23]\n" \
  "ds_read_b128 v[44:47], v7 offset:28672\n" \
  "v_mfma_f32_16x16x32_bf16 v[24:27], v[168:171], v[48:51], v[24:27]\n" \
  "s_waitcnt lgkmcnt(3)\n" \
  "v_mfma_f32_16x16x32_bf16 v[20:23], v[108:111], v[52:55], v[20:23]\n" \
  "ds_read_b128 v[48:51], v7 offset:30720\n" \
  "v_mfma_f32_16x16x32_bf16 v[24:27], v[172:175], v[52:55], v[24:27]\n" \
  "s_waitcnt lgkmcnt(3)\n" \
  "v_mfma_f32_16x16x32_bf16 v[20:23], v[112:115], v[36:39], v[20:23]\n" \
  "ds_read_b128 v[52:55], v7 offset:256\n" \
  "v_mfma_f32_16x16x32_bf16 v[24:27], v[176:179], v[36:39], v[24:27]\n" \
  "s_waitcnt lgkmcnt(3)\n" \
  "v_mfma_f32_16x16x32_bf16 v[20:23], v[116:119], v[40:43], v[20:23]\n" \
  "ds_read_b128 v[36:39], v7 offset:2304\n" \
  "v_mfma_f32_16x16x32_bf16 v[24:27], v[180:183], v[40:43], v[24:27]\n" \
  "s_waitcnt lgkmcnt(3)\n" \
  "v_mfma_f32_16x16x32_bf16 v[20:23], v[120:123], v[44:47], v[20:23]\n" \
  "ds_read_b128 v[40:43], v7 offset:4352\n" \
  "v_mfma_f32_16x16x32_bf16 v[24:27], v[184:187], v[44:47], v[24:27]\n" \
  "s_waitcnt lgkmcnt(3)\n" \
  "v_mfma_f32_16x16x32_bf16 v[20:23], v[124:127], v[48:51], v[20:23]\n" \
  "ds_read_b128 v[44:47], v7 offset:6400\n" \
  "v_mfma_f32_16x16x32_bf16 v[24:27], v[188:191], v[48:51], v[24:27]\n" \
  "s_waitcnt lgkmcnt(3)\n" \
  "v_mfma_f32_16x16x32_bf16 v[28:31], v[64:67], v[52:55], v[28:31]\n" \
  "ds_read_b128 v[48:51], v7 offset:8448\n" \
  "v_mfma_f32_16x16x32_bf16 v[32:35], v[128:131], v[52:55], v[32:35]\n" \
  "s_waitcnt lgkmcnt(3)\n" \
  "v_mfma_f32_16x16x32_bf16 v[28:31], v[68:71], v[36:39], v[28:31]\n" \
  "ds_read_b128 v[52:55], v7 offset:10496\n" \
  "v_mfma_f32_16x16x32_bf16 v[32:35], v[132:135], v[36:39], v[32:35]\n" \
  "s_waitcnt lgkmcnt(3)\n" \
  "v_mfma_f32_16x16x32_bf16 v[28:31], v[72:75], v[40:43], v[28:31]\n" \
  "ds_read_b128 v[36:39], v7 offset:12544\n" \
  "v_mfma_f32_16x16x32_bf16 v[32:35], v[136:139], v[40:43], v[32:35]\n" \
  "s_waitcnt lgkmcnt(3)\n" \
  "v_mfma_f32_16x16x32_bf16 v[28:31], v[76:79], v[44:47], v[28:31]\n" \
  "ds_read_b128 v[40:43], v7 offset:14592\n" \
  "v_mfma_f32_16x16x32_bf16 v[32:35], v[140:143], v[44:47], v[32:35]\n" \
  "s_waitcnt lgkmcnt(3)\n" \
  "v_mfma_f32_16x16x32_bf16 v[28:31], v[80:83], v[48:51], v[28:31]\n" \
  "ds_read_b128 v[44:47], v7 offset:16640\n" \
  "v_mfma_f32_16x16x32_bf16 v[32:35], v[144:147], v[48:51], v[32:35]\n" \
  "s_waitcnt lgkmcnt(3)\n" \
  "v_mfma_f32_16x16x32_bf16 v[28:31], v[84:87], v[52:55], v[28:31]\n" \
  "ds_read_b128 v[48:51], v7 offset:18688\n" \
  "v_mfma_f32_16x16x32_bf16 v[32:35], v[148:151], v[52:55], v[32:35]\n" \
  "s_waitcnt lgkmcnt(3)\n" \
  "v_mfma_f32_16x16x32_bf16 v[28:31], v[88:91], v[36:39], v[28:31]\n" \
  "ds_read_b128 v[52:55], v7 offset:20736\n" \
  "s_waitcnt vmcnt(0)\n" \
  "s_mov_b64 exec, 1\n" \
  "ds_add_rtn_u32 v14, v12, v13\n" \
  "s_mov_b64 exec, -1\n" \
  "v_mfma_f32_16x16x32_bf16 v[32:35], v[152:155], v[36:39], v[32:35]\n" \
  "s_waitcnt lgkmcnt(4)\n" \
  "v_mfma_f32_16x16x32_bf16 v[28:31], v[92:95], v[40:43], v[28:31]\n" \
  "ds_read_b128 v[36:39], v7 offset:22784\n" \
  "v_mfma_f32_16x16x32_bf16 v[32:35], v[156:159], v[40:43], v[32:35]\n" \
  "s_waitcnt lgkmcnt(4)\n" \
  "v_mfma_f32_16x16x32_bf16 v[28:31], v[96:99], v[44:47], v[28:31]\n" \
  "ds_read_b128 v[40:43], v7 offset:24832\n" \
  "s_waitcnt lgkmcnt(2)\n" \
  "v_mfma_f32_16x16x32_bf16 v[32:35], v[160:163], v[44:47], v[32:35]\n" \
  "v_readfirstlane_b32 s74, v14\n" \
  "s_cmp_eq_u32 s66, s83\n" \
  "s_cselect_b32 s74, 0, s74\n" \
  "s_and_b32 s74, s74, 3\n" \
  "s_cmp_eq_u32 s74, 3\n" \
  "s_cbranch_scc0 4\n" \
  "s_mov_b64 exec, 1\n" \
  "global_store_dword v16, v13, s[64:65] offset:64 sc1\n" \
  "s_mov_b64 exec, -1\n" \
  "v_mfma_f32_16x16x32_bf16 v[28:31], v[100:103], v[48:51], v[28:31]\n" \
  "ds_read_b128 v[44:47], v7 offset:26880\n" \
  "s_add_u32 s64, s64, 4096\n" \
  "s_addc_u32 s65, s65, 0\n" \
  "v_mfma_f32_16x16x32_bf16 v[32:35], v[164:167], v[48:51], v[32:35]\n" \
  "s_sub_u32 s66, s66, 1\n" \
  "v_mfma_f32_16x16x32_bf16 v[28:31], v[104:107], v[52:55], v[28:31]\n" \
  "ds_read_b128 v[48:51], v7 offset:28928\n" \
  "v_mfma_f32_16x16x32_bf16 v[32:35], v[168:171], v[52:55], v[32:35]\n" \
  "s_waitcnt lgkmcnt(3)\n" \
  "v_mfma_f32_16x16x32_bf16 v[28:31], v[108:111], v[36:39], v[28:31]\n" \
  "ds_read_b128 v[52:55], v7 offset:30976\n" \
  "v_mfma_f32_16x16x32_bf16 v[32:35], v[172:175], v[36:39], v[32:35]\n" \
  "s_waitcnt lgkmcnt(3)\n" \
  "v_mfma_f32_16x16x32_bf16 v[28:31], v[112:115], v[40:43], v[28:31]\n" \
  "v_mfma_f32_16x16x32_bf16 v[32:35], v[176:179], v[40:43], v[32:35]\n" \
  "v_xor_b32 v7, 0x20000, v7\n" \
  "s_waitcnt lgkmcnt(0)\n" \
  "s_barrier\n" \
  "ds_read_b128 v[36:39], v10 offset:0\n" \
  "ds_read_b128 v[40:43], v7 offset:0\n" \
  "v_mfma_f32_16x16x32_bf16 v[28:31], v[116:119], v[44:47], v[28:31]\n" \
  "v_mfma_f32_16x16x32_bf16 v[32:35], v[180:183], v[44:47], v[32:35]\n" \
  "ds_read_b128 v[44:47], v10 offset:2048\n" \
  "v_mfma_f32_16x16x32_bf16 v[28:31], v[120:123], v[48:51], v[28:31]\n" \
  "v_mfma_f32_16x16x32_bf16 v[32:35], v[184:187], v[48:51], v[32:35]\n" \
  "ds_read_b128 v[48:51], v7 offset:2048\n" \
  "v_mfma_f32_16x16x32_bf16 v[28:31], v[124:127], v[52:55], v[28:31]\n" \
  "v_mfma_f32_16x16x32_bf16 v[32:35], v[188:191], v[52:55], v[32:35]\n" \
  "s_cbranch_scc1 .LQB_LAST_%=\n" \
  "s_branch .LQB_TOP_%=\n" \
  ".Lpad0xac:\n" \
  "s_setpc_b64 s[98:99]\n" \
  ".LQB_LAST_%=:\n" \
  "s_nop 7\n" \
  "v_mul_f32 v20, v20, v60\n" \
  "v_mul_f32 v21, v21, v60\n" \
  "v_mul_f32 v22, v22, v60\n" \
  "v_cvt_pk_bf16_f32 v20, v20, v21\n" \
  "v_mul_f32 v23, v23, v60\n" \
  "v_mul_f32 v24, v24, v60\n" \
  "v_cvt_pk_bf16_f32 v21, v22, v23\n" \
  "v_mul_f32 v25, v25, v60\n" \
  "v_mul_f32 v26, v26, v60\n" \
  "v_cvt_pk_bf16_f32 v22, v24, v25\n" \
  "v_mul_f32 v27, v27, v60\n" \
  "v_cvt_pk_bf16_f32 v23, v26, v27\n" \
  "buffer_store_dwordx4 v[20:23], v6, s[40:43], s59 offen offset:0 sc1\n" \
  "s_add_u32 s76, s59, 4096\n" \
  "v_mul_f32 v28, v28, v61\n" \
  "v_mul_f32 v29, v29, v61\n" \
  "v_mul_f32 v30, v30, v61\n" \
  "v_cvt_pk_bf16_f32 v28, v28, v29\n" \
  "v_mul_f32 v31, v31, v61\n" \
  "v_mul_f32 v32, v32, v61\n" \
  "v_cvt_pk_bf16_f32 v29, v30, v31\n" \
  "v_mul_f32 v33, v33, v61\n" \
  "v_mul_f32 v34, v34, v61\n" \
  "v_cvt_pk_bf16_f32 v30, v32, v33\n" \
  "v_mul_f32 v35, v35, v61\n" \
  "v_cvt_pk_bf16_f32 v31, v34, v35\n" \
  "buffer_store_dwordx4 v[28:31], v6, s[40:43], s76 offen offset:0 sc1\n" \
  "s_add_u32 s59, s59, 0x80000\n" \
  "s_waitcnt vmcnt(0)\n" \
  "s_mov_b64 exec, 1\n" \
  "ds_add_rtn_u32 v14, v12, v13\n" \
  "s_mov_b64 exec, -1\n" \
  "s_waitcnt lgkmcnt(0)\n" \
  "v_readfirstlane_b32 s74, v14\n" \
  "s_cmp_eq_u32 s66, s83\n" \
  "s_cselect_b32 s74, 0, s74\n" \
  "s_and_b32 s74, s74, 3\n" \
  "s_cmp_eq_u32 s74, 3\n" \
  "s_cbranch_scc0 4\n" \
  "s_mov_b64 exec, 1\n" \
  "global_store_dword v16, v13, s[64:65] offset:64 sc1\n" \
  "s_mov_b64 exec, -1\n" \
  "s_cmp_eq_u32 s86, 0\n" \
  "s_cbranch_scc1 .LQB_END_%=\n" \
  "s_mov_b32 s86, 0\n" \
  "s_mov_b32 s66, 63\n" \
  "s_mov_b32 s62, 0x40000\n" \
  "s_add_u32 s54, s54, (128 - %[n0]) * 4096\n" \
  "s_add_u32 s59, s59, (64 - %[n0]) * 0x80000\n" \
  "s_add_u32 s60, s60, (64 - %[n0]) * 0x40000\n" \
  "s_add_u32 s61, s61, (64 - %[n0]) * 0x10000\n" \
  "s_add_u32 s64, s64, (65 - %[n0]) * 4096\n" \
  "s_addc_u32 s65, s65, 0\n" \
  "s_add_u32 s56, s56, %[x3]\n" \
  "s_addc_u32 s57, s57, 0\n" \
  "s_add_u32 s84, s84, 2048\n" \
  "s_addc_u32 s85, s85, 0\n" \
  "s_mov_b32 s83, 63\n" \
  "s_branch .LQB_GO_%=\n" \
  ".LQB_END_%=:\n" \
  "s_endpgm\n" \
  ".p2alignl 12, 0xbf800000\n" \
  ".Lpad0x8c:\n" \
  "s_setpc_b64 s[98:99]\n"
#define AW_ASM_0 \
  "v_add_u32 v17, s61, v1\n" \
  "ds_write_b128 v17, v[8:11]\n" \
  "ds_write_b128 v17, v[12:15] offset:1024\n" \
  "s_branch .LWA_AL_%=\n" \
  ".p2alignl 12, 0xbf800000, 4000\n" \
  ".p2align 6\n" \
  ".Lpad0xdc:\n" \
  "s_setpc_b64 s[98:99]\n" \
  ".p2align 6\n" \
  ".LWA_AL_%=:\n" \
  ".LWA_TOP_%=:\n" \
  "v_mov_b32 v16, s65\n" \
  "v_add_u32 v17, s61, v1\n" \
  "s_add_u32 s64, s64, 2\n" \
  "s_mov_b32 s77, 0x1000000\n" \
  "s_branch .LWA_LAND_%=\n" \
  ".LWA_WAIT_%=:\n" \
  "s_setprio 0\n" \
  "s_sleep 1\n" \
  "s_sub_u32 s77, s77, 1\n" \
  "s_cbranch_scc1 .LWA_GO_%=\n" \
  ".LWA_LAND_%=:\n" \
  "ds_read_b32 v18, v16\n" \
  "s_waitcnt lgkmcnt(0)\n" \
  "v_readfirstlane_b32 s68, v18\n" \
  "s_cmp_lt_u32 s68, s64\n" \
  "s_cbranch_scc1 .LWA_WAIT_%=\n" \
  ".LWA_GO_%=:\n" \
  "ds_read_b128 v[64:67], v17 offset:0\n" \
  "ds_read_b128 v[68:71], v17 offset:1024\n" \
  "s_setprio 3\n" \
  "ds_read_b128 v[16:19], v4 offset:0\n" \
  "ds_read_b128 v[32:35], v3 offset:8192\n" \
  "ds_read_b128 v[48:51], v3 offset:12288\n" \
  "ds_read_b128 v[20:23], v4 offset:1024\n" \
  "ds_read_b128 v[36:39], v3 offset:9216\n" \
  "ds_read_b128 v[52:55], v3 offset:13312\n" \
  "ds_read_b128 v[24:27], v4 offset:2048\n" \
  "ds_read_b128 v[40:43], v3 offset:10240\n" \
  "ds_read_b128 v[56:59], v3 offset:14336\n" \
  "ds_read_b128 v[28:31], v4 offset:3072\n" \
  "ds_read_b128 v[44:47], v3 offset:11264\n" \
  "ds_read_b128 v[60:63], v3 offset:15360\n" \
  "s_waitcnt lgkmcnt(10)\n" \
  "v_mfma_f32_16x16x32_bf16 v[64:67], v[32:35], v[16:19], v[64:67]\n" \
  "ds_read_b128 v[72:75], v3 offset:16384\n" \
  "s_waitcnt lgkmcnt(10)\n" \
  "v_mfma_f32_16x16x32_bf16 v[68:71], v[48:51], v[16:19], v[68:71]\n" \
  "ds_read_b128 v[76:79], v3 offset:17408\n" \
  "s_waitcnt lgkmcnt(9)\n" \
  "v_mfma_f32_16x16x32_bf16 v[64:67], v[36:39], v[20:23], v[64:67]\n" \
  "ds_read_b128 v[80:83], v3 offset:18432\n" \
  "s_waitcnt lgkmcnt(9)\n" \
  "v_mfma_f32_16x16x32_bf16 v[68:71], v[52:55], v[20:23], v[68:71]\n" \
  "ds_read_b128 v[84:87], v3 offset:19456\n" \
  "s_waitcnt lgkmcnt(8)\n" \
  "v_mfma_f32_16x16x32_bf16 v[64:67], v[40:43], v[24:27], v[64:67]\n" \
  "ds_read_b128 v[88:91], v3 offset:20480\n" \
  "s_waitcnt lgkmcnt(8)\n" \
  "v_mfma_f32_16x16x32_bf16 v[68:71], v[56:59], v[24:27], v[68:71]\n" \
  "ds_read_b128 v[92:95], v3 offset:21504\n" \
  "s_waitcnt lgkmcnt(7)\n" \
  "v_mfma_f32_16x16x32_bf16 v[64:67], v[44:47], v[28:31], v[64:67]\n" \
  "ds_read_b128 v[96:99], v3 offset:22528\n" \
  "s_waitcnt lgkmcnt(7)\n" \
  "v_mfma_f32_16x16x32_bf16 v[68:71], v[60:63], v[28:31], v[68:71]\n" \
  "ds_read_b128 v[100:103], v3 offset:23552\n" \
  "s_add_u32 s56, s56, 3\n" \
  "s_sub_u32 s68, s56, %[wd]\n" \
  "s_bitcmp1_b32 s68, 0\n" \
  "s_cselect_b32 s58, 64, %[wd]\n" \
  "v_maximum3_f32 v10, v64, v65, v66\n" \
  "s_lshr_b32 s70, s68, 1\n" \
  "s_add_u32 s58, s58, s70\n" \
  "v_maximum3_f32 v10, v10, v67, v68\n" \
  "v_maximum3_f32 v10, v10, v69, v70\n" \
  "v_max_f32 v8, v10, v71\n" \
  "v_mov_b32 v9, v8\n" \
  "s_cmp_lt_u32 s68, %[w2]\n" \
  "s_cselect_b32 s58, s58, s56\n" \
  "v_permlane16_swap_b32 v8, v9\n" \
  "v_max_f32 v8, v8, v9\n" \
  "v_mov_b32 v9, v8\n" \
  "s_min_u32 s58, s58, 128\n" \
  "s_nop 0\n" \
  "v_permlane32_swap_b32 v8, v9\n" \
  "v_max_f32 v8, v8, v9\n" \
  "v_mul_f32 v10, s80, v8\n" \
  "v_pk_fma_f32 v[64:65], v[64:65], s[80:81], v[10:11] op_sel_hi:[1,0,0] neg_lo:[0,0,1] neg_hi:[0,0,1]\n" \
  "v_pk_fma_f32 v[66:67], v[66:67], s[80:81], v[10:11] op_sel_hi:[1,0,0] neg_lo:[0,0,1] neg_hi:[0,0,1]\n" \
  "v_pk_fma_f32 v[68:69], v[68:69], s[80:81], v[10:11] op_sel_hi:[1,0,0] neg_lo:[0,0,1] neg_hi:[0,0,1]\n" \
  "v_pk_fma_f32 v[70:71], v[70:71], s[80:81], v[10:11] op_sel_hi:[1,0,0] neg_lo:[0,0,1] neg_hi:[0,0,1]\n" \
  "v_exp_f32 v64, v64\n" \
  "v_exp_f32 v65, v65\n" \
  "v_exp_f32 v66, v66\n" \
  "v_exp_f32 v67, v67\n" \
  "v_exp_f32 v68, v68\n" \
  "v_exp_f32 v69, v69\n" \
  "v_exp_f32 v70, v70\n" \
  "v_exp_f32 v71, v71\n" \
  "v_cvt_pk_bf16_f32 v104, v64, v65\n" \
  "v_cvt_pk_bf16_f32 v105, v66, v67\n" \
  "v_cvt_pk_bf16_f32 v106, v68, v69\n" \
  "v_cvt_pk_bf16_f32 v107, v70, v71\n" \
  "v_pk_add_f32 v[64:65], v[64:65], v[66:67]\n" \
  "v_pk_add_f32 v[68:69], v[68:69], v[70:71]\n" \
  "s_waitcnt lgkmcnt(0)\n" \
  "v_pk_add_f32 v[64:65], v[64:65], v[68:69]\n" \
  "v_mfma_f32_16x16x32_bf16 v[32:35], v[72:75], v[104:107], 0\n" \
  "v_mfma_f32_16x16x32_bf16 v[48:51], v[88:91], v[104:107], 0\n" \
  "v_mfma_f32_16x16x32_bf16 v[36:39], v[76:79], v[104:107], 0\n" \
  "v_mfma_f32_16x16x32_bf16 v[52:55], v[92:95], v[104:107], 0\n" \
  "v_mfma_f32_16x16x32_bf16 v[40:43], v[80:83], v[104:107], 0\n" \
  "v_mfma_f32_16x16x32_bf16 v[56:59], v[96:99], v[104:107], 0\n" \
  "v_mfma_f32_16x16x32_bf16 v[44:47], v[84:87], v[104:107], 0\n" \
  "v_mfma_f32_16x16x32_bf16 v[60:63], v[100:103], v[104:107], 0\n" \
  "v_add_f32 v11, v64, v65\n" \
  "s_cmp_lt_u32 s56, %[w3]\n" \
  "s_cselect_b32 s68, s57, 0\n" \
  "s_cmp_ge_u32 s68, 64\n" \
  "s_cbranch_scc1 .LWA_EASY_%=\n" \
  "v_cvt_pk_bf16_f32 v108, v32, v33\n" \
  "v_cvt_pk_bf16_f32 v109, v48, v49\n" \
  "v_cvt_pk_bf16_f32 v110, v34, v35\n" \
  "v_cvt_pk_bf16_f32 v111, v50, v51\n" \
  "ds_write_b128 v7, v[108:111] offset:0\n" \
  "v_cvt_pk_bf16_f32 v112, v36, v37\n" \
  "v_cvt_pk_bf16_f32 v113, v52, v53\n" \
  "v_cvt_pk_bf16_f32 v114, v38, v39\n" \
  "v_cvt_pk_bf16_f32 v115, v54, v55\n" \
  "ds_write_b128 v7, v[112:115] offset:1024\n" \
  "v_cvt_pk_bf16_f32 v116, v40, v41\n" \
  "v_cvt_pk_bf16_f32 v117, v56, v57\n" \
  "v_cvt_pk_bf16_f32 v118, v42, v43\n" \
  "v_cvt_pk_bf16_f32 v119, v58, v59\n" \
  "ds_write_b128 v7, v[116:119] offset:2048\n" \
  "v_cvt_pk_bf16_f32 v120, v44, v45\n" \
  "v_cvt_pk_bf16_f32 v121, v60, v61\n" \
  "v_cvt_pk_bf16_f32 v122, v46, v47\n" \
  "v_cvt_pk_bf16_f32 v123, v62, v63\n" \
  "ds_write_b128 v7, v[120:123] offset:3072\n" \
  "s_mov_b32 s0, 0xbe801d62\n" \
  ".set .Lpad0xdd, . - 4\n" \
  "s_waitcnt vmcnt(0)\n" \
  "s_waitcnt lgkmcnt(0)\n" \
  "s_barrier\n" \
  "s_setprio 2\n" \
  "ds_read_b128 v[48:51], v1 offset:0\n" \
  "ds_read_b128 v[52:55], v1 offset:1024\n" \
  "ds_read_b128 v[56:59], v1 offset:2048\n" \
  "s_waitcnt lgkmcnt(2)\n" \
  "v_mfma_f32_16x16x32_bf16 v[64:67], v[128:131], v[48:51], 0\n" \
  "ds_read_b128 v[60:63], v1 offset:3072\n" \
  "s_lshl_b32 s75, s57, 19\n" \
  "v_mfma_f32_16x16x32_bf16 v[68:71], v[160:163], v[48:51], 0\n" \
  "s_add_u32 s75, s75, s60\n" \
  "v_mfma_f32_16x16x32_bf16 v[72:75], v[192:195], v[48:51], 0\n" \
  "s_add_u32 s76, s75, 16384\n" \
  "v_mfma_f32_16x16x32_bf16 v[76:79], v[224:227], v[48:51], 0\n" \
  "s_waitcnt lgkmcnt(2)\n" \
  "v_mfma_f32_16x16x32_bf16 v[64:67], v[132:135], v[52:55], v[64:67]\n" \
  "ds_read_b128 v[48:51], v1 offset:8192\n" \
  "v_mfma_f32_16x16x32_bf16 v[68:71], v[164:167], v[52:55], v[68:71]\n" \
  "v_mfma_f32_16x16x32_bf16 v[72:75], v[196:199], v[52:55], v[72:75]\n" \
  "v_mfma_f32_16x16x32_bf16 v[76:79], v[228:231], v[52:55], v[76:79]\n" \
  "s_waitcnt lgkmcnt(2)\n" \
  "v_mfma_f32_16x16x32_bf16 v[64:67], v[136:139], v[56:59], v[64:67]\n" \
  "ds_read_b128 v[52:55], v1 offset:9216\n" \
  "v_mfma_f32_16x16x32_bf16 v[68:71], v[168:171], v[56:59], v[68:71]\n" \
  "v_mfma_f32_16x16x32_bf16 v[72:75], v[200:203], v[56:59], v[72:75]\n" \
  "v_mfma_f32_16x16x32_bf16 v[76:79], v[232:235], v[56:59], v[76:79]\n" \
  "s_waitcnt lgkmcnt(2)\n" \
  "v_mfma_f32_16x16x32_bf16 v[64:67], v[140:143], v[60:63], v[64:67]\n" \
  "ds_read_b128 v[56:59], v1 offset:10240\n" \
  "v_mfma_f32_16x16x32_bf16 v[68:71], v[172:175], v[60:63], v[68:71]\n" \
  "v_mfma_f32_16x16x32_bf16 v[72:75], v[204:207], v[60:63], v[72:75]\n" \
  "v_mfma_f32_16x16x32_bf16 v[76:79], v[236:239], v[60:63], v[76:79]\n" \
  "s_waitcnt lgkmcnt(2)\n" \
  "v_mfma_f32_16x16x32_bf16 v[96:99], v[144:147], v[48:51], 0\n" \
  "ds_read_b128 v[60:63], v1 offset:11264\n" \
  "v_mfma_f32_16x16x32_bf16 v[100:103], v[176:179], v[48:51], 0\n" \
  "v_mfma_f32_16x16x32_bf16 v[104:107], v[208:211], v[48:51], 0\n" \
  "v_mfma_f32_16x16x32_bf16 v[108:111], v[240:243], v[48:51], 0\n" \
  "s_waitcnt lgkmcnt(2)\n" \
  "v_mfma_f32_16x16x32_bf16 v[96:99], v[148:151], v[52:55], v[96:99]\n" \
  "ds_read_b128 v[48:51], v1 offset:4096\n" \
  "v_mfma_f32_16x16x32_bf16 v[100:103], v[180:183], v[52:55], v[100:103]\n" \
  "v_mfma_f32_16x16x32_bf16 v[104:107], v[212:215], v[52:55], v[104:107]\n" \
  "v_mfma_f32_16x16x32_bf16 v[108:111], v[244:247], v[52:55], v[108:111]\n" \
  "s_waitcnt lgkmcnt(2)\n" \
  "v_mfma_f32_16x16x32_bf16 v[96:99], v[152:155], v[56:59], v[96:99]\n" \
  "ds_read_b128 v[52:55], v1 offset:5120\n" \
  "v_mfma_f32_16x16x32_bf16 v[100:103], v[184:187], v[56:59], v[100:103]\n" \
  "v_mfma_f32_16x16x32_bf16 v[104:107], v[216:219], v[56:59], v[104:107]\n" \
  "v_mfma_f32_16x16x32_bf16 v[108:111], v[248:251], v[56:59], v[108:111]\n" \
  "s_waitcnt lgkmcnt(2)\n" \
  "v_mfma_f32_16x16x32_bf16 v[96:99], v[156:159], v[60:63], v[96:99]\n" \
  "ds_read_b128 v[56:59], v1 offset:6144\n" \
  "v_mfma_f32_16x16x32_bf16 v[100:103], v[188:191], v[60:63], v[100:103]\n" \
  "v_mfma_f32_16x16x32_bf16 v[104:107], v[220:223], v[60:63], v[104:107]\n" \
  "v_mfma_f32_16x16x32_bf16 v[108:111], v[252:255], v[60:63], v[108:111]\n" \
  "s_waitcnt lgkmcnt(2)\n" \
  "v_mfma_f32_16x16x32_bf16 v[80:83], v[128:131], v[48:51], 0\n" \
  "ds_read_b128 v[60:63], v1 offset:7168\n" \
  "v_mfma_f32_16x16x32_bf16 v[84:87], v[160:163], v[48:51], 0\n" \
  "v_mfma_f32_16x16x32_bf16 v[88:91], v[192:195], v[48:51], 0\n" \
  "v_mfma_f32_16x16x32_bf16 v[92:95], v[224:227], v[48:51], 0\n" \
  "s_waitcnt lgkmcnt(2)\n" \
  "v_mfma_f32_16x16x32_bf16 v[80:83], v[132:135], v[52:55], v[80:83]\n" \
  "ds_read_b128 v[48:51], v1 offset:12288\n" \
  "v_mfma_f32_16x16x32_bf16 v[84:87], v[164:167], v[52:55], v[84:87]\n" \
  "v_mfma_f32_16x16x32_bf16 v[88:91], v[196:199], v[52:55], v[88:91]\n" \
  "v_mfma_f32_16x16x32_bf16 v[92:95], v[228:231], v[52:55], v[92:95]\n" \
  "s_waitcnt lgkmcnt(2)\n" \
  "v_mfma_f32_16x16x32_bf16 v[80:83], v[136:139], v[56:59], v[80:83]\n" \
  "ds_read_b128 v[52:55], v1 offset:13312\n" \
  "v_mfma_f32_16x16x32_bf16 v[84:87], v[168:171], v[56:59], v[84:87]\n" \
  "v_mfma_f32_16x16x32_bf16 v[88:91], v[200:203], v[56:59], v[88:91]\n" \
  "v_mfma_f32_16x16x32_bf16 v[92:95], v[232:235], v[56:59], v[92:95]\n" \
  "s_waitcnt lgkmcnt(2)\n" \
  "v_mfma_f32_16x16x32_bf16 v[80:83], v[140:143], v[60:63], v[80:83]\n" \
  "ds_read_b128 v[56:59], v1 offset:14336\n" \
  "v_mfma_f32_16x16x32_bf16 v[84:87], v[172:175], v[60:63], v[84:87]\n" \
  "v_mfma_f32_16x16x32_bf16 v[88:91], v[204:207], v[60:63], v[88:91]\n" \
  "v_mfma_f32_16x16x32_bf16 v[92:95], v[236:239], v[60:63], v[92:95]\n" \
  "s_waitcnt lgkmcnt(2)\n" \
  "v_mfma_f32_16x16x32_bf16 v[112:115], v[144:147], v[48:51], 0\n" \
  "ds_read_b128 v[60:63], v1 offset:15360\n" \
  "v_mfma_f32_16x16x32_bf16 v[116:119], v[176:179], v[48:51], 0\n" \
  "v_mfma_f32_16x16x32_bf16 v[120:123], v[208:211], v[48:51], 0\n" \
  "v_mfma_f32_16x16x32_bf16 v[124:127], v[240:243], v[48:51], 0\n" \
  "s_waitcnt lgkmcnt(2)\n" \
  "v_mfma_f32_16x16x32_bf16 v[112:115], v[148:151], v[52:55], v[112:115]\n" \
  "v_mfma_f32_16x16x32_bf16 v[116:119], v[180:183], v[52:55], v[116:119]\n" \
  "v_mfma_f32_16x16x32_bf16 v[120:123], v[212:215], v[52:55], v[120:123]\n" \
  "v_mfma_f32_16x16x32_bf16 v[124:127], v[244:247], v[52:55], v[124:127]\n" \
  "s_waitcnt lgkmcnt(1)\n" \
  "v_mfma_f32_16x16x32_bf16 v[112:115], v[152:155], v[56:59], v[112:115]\n" \
  "v_mfma_f32_16x16x32_bf16 v[116:119], v[184:187], v[56:59], v[116:119]\n" \
  "v_mfma_f32_16x16x32_bf16 v[120:123], v[216:219], v[56:59], v[120:123]\n" \
  "v_mfma_f32_16x16x32_bf16 v[124:127], v[248:251], v[56:59], v[124:127]\n" \
  "s_waitcnt lgkmcnt(0)\n" \
  "v_mfma_f32_16x16x32_bf16 v[112:115], v[156:159], v[60:63], v[112:115]\n" \
  "v_mfma_f32_16x16x32_bf16 v[116:119], v[188:191], v[60:63], v[116:119]\n" \
  "v_mfma_f32_16x16x32_bf16 v[120:123], v[220:223], v[60:63], v[120:123]\n" \
  "v_mfma_f32_16x16x32_bf16 v[124:127], v[252:255], v[60:63], v[124:127]\n" \
  "s_setprio 3\n" \
  "s_mov_b32 s0, 0xbe801d62\n" \
  ".set .Lpad0xe7, . - 4\n" \
  "s_barrier\n" \
  "ds_read_b128 v[32:35], v5 offset:0\n" \
  "ds_read_b128 v[36:39], v5 offset:512\n" \
  "ds_read_b128 v[40:43], v5 offset:1024\n" \
  "ds_read_b128 v[44:47], v5 offset:1536\n" \
  "ds_read_b64_tr_b16 v[48:49], v6 offset:0\n" \
  "ds_read_b64_tr_b16 v[50:51], v6 offset:32\n" \
  "ds_read_b64_tr_b16 v[52:53], v6 offset:1024\n" \
  "ds_read_b64_tr_b16 v[54:55], v6 offset:1056\n" \
  "ds_read_b64_tr_b16 v[56:57], v6 offset:4096\n" \
  "ds_read_b64_tr_b16 v[58:59], v6 offset:4128\n" \
  "ds_read_b64_tr_b16 v[60:61], v6 offset:5120\n" \
  "ds_read_b64_tr_b16 v[62:63], v6 offset:5152\n" \
  "s_waitcnt lgkmcnt(11)\n" \
  "v_mfma_f32_16x16x32_bf16 v[12:15], v[32:35], v[16:19], 0\n" \
  "s_waitcnt lgkmcnt(10)\n" \
  "v_mfma_f32_16x16x32_bf16 v[12:15], v[36:39], v[20:23], v[12:15]\n" \
  "s_waitcnt lgkmcnt(9)\n" \
  "v_mfma_f32_16x16x32_bf16 v[12:15], v[40:43], v[24:27], v[12:15]\n" \
  "s_waitcnt lgkmcnt(8)\n" \
  "v_mfma_f32_16x16x32_bf16 v[12:15], v[44:47], v[28:31], v[12:15]\n" \
  "v_mov_b64 v[16:17], 0\n" \
  "v_mov_b64 v[18:19], 0\n" \
  "v_mov_b64 v[20:21], 0\n" \
  "v_mov_b64 v[22:23], 0\n" \
  "v_mov_b64 v[24:25], 0\n" \
  "v_mov_b64 v[26:27], 0\n" \
  "v_mov_b64 v[28:29], 0\n" \
  "v_mov_b64 v[30:31], 0\n" \
  "v_maximum3_f32 v8, v8, v12, v13\n" \
  "v_mov_b32 v9, v8\n" \
  "v_mov_b64 v[32:33], 0\n" \
  "v_mov_b64 v[34:35], 0\n" \
  "v_permlane16_swap_b32 v8, v9\n" \
  "v_max_f32 v8, v8, v9\n" \
  "v_mov_b32 v9, v8\n" \
  "v_mov_b64 v[36:37], 0\n" \
  "v_mov_b64 v[38:39], 0\n" \
  "v_permlane32_swap_b32 v8, v9\n" \
  "v_max_f32 v8, v8, v9\n" \
  "v_mul_f32 v8, s80, v8\n" \
  "v_sub_f32 v14, v10, v8\n" \
  "v_fma_f32 v12, v12, s80, -v8\n" \
  "v_fma_f32 v13, v13, s80, -v8\n" \
  "v_exp_f32 v14, v14\n" \
  "v_exp_f32 v12, v12\n" \
  "v_exp_f32 v13, v13\n" \
  "v_fma_f32 v11, v11, v14, v12\n" \
  "v_add_f32 v11, v11, v13\n" \
  "v_mov_b32 v9, v11\n" \
  "v_mov_b64 v[40:41], 0\n" \
  "v_mov_b64 v[42:43], 0\n" \
  "v_permlane16_swap_b32 v11, v9\n" \
  "v_add_f32 v11, v11, v9\n" \
  "v_mov_b32 v9, v11\n" \
  "v_mov_b64 v[44:45], 0\n" \
  "v_mov_b64 v[46:47], 0\n" \
  "v_permlane32_swap_b32 v11, v9\n" \
  "v_add_f32 v11, v11, v9\n" \
  "v_rcp_f32 v15, v11\n" \
  "v_add_u32 v9, s66, v1\n" \
  "v_mul_f32 v12, v12, v15\n" \
  "v_mul_f32 v13, v13, v15\n" \
  "v_mul_f32 v14, v14, v15\n" \
  "v_cvt_pk_bf16_f32 v12, v12, v13\n" \
  "ds_write2_b32 v9, v12, v14 offset1:2\n" \
  "s_mov_b32 s0, 0xbe801d62\n" \
  ".set .Lpad0xde, . - 4\n" \
  "s_waitcnt lgkmcnt(0)\n" \
  "s_barrier\n" \
  "ds_read_b128 v[8:11], v1 offset:24576\n" \
  "ds_read_b128 v[12:15], v1 offset:25600\n" \
  "s_setprio 0\n" \
  "v_smfmac_f32_16x16x64_bf16 v[16:19], v[48:51], v[128:135], v0\n" \
  "v_smfmac_f32_16x16x64_bf16 v[20:23], v[48:51], v[160:167], v0\n" \
  "v_smfmac_f32_16x16x64_bf16 v[24:27], v[48:51], v[192:199], v0\n" \
  "v_smfmac_f32_16x16x64_bf16 v[28:31], v[48:51], v[224:231], v0\n" \
  "v_smfmac_f32_16x16x64_bf16 v[16:19], v[52:55], v[136:143], v0\n" \
  "v_smfmac_f32_16x16x64_bf16 v[20:23], v[52:55], v[168:175], v0\n" \
  "v_smfmac_f32_16x16x64_bf16 v[24:27], v[52:55], v[200:207], v0\n" \
  "v_smfmac_f32_16x16x64_bf16 v[28:31], v[52:55], v[232:239], v0\n" \
  "v_smfmac_f32_16x16x64_bf16 v[32:35], v[56:59], v[144:151], v0\n" \
  "s_waitcnt lgkmcnt(0)\n" \
  "v_pk_mul_f32 v[64:65], v[64:65], v[10:11] op_sel_hi:[1,0]\n" \
  "v_pk_mul_f32 v[66:67], v[66:67], v[10:11] op_sel_hi:[1,0]\n" \
  "v_smfmac_f32_16x16x64_bf16 v[36:39], v[56:59], v[176:183], v0\n" \
  "v_pk_mul_f32 v[68:69], v[68:69], v[10:11] op_sel_hi:[1,0]\n" \
  "v_pk_mul_f32 v[70:71], v[70:71], v[10:11] op_sel_hi:[1,0]\n" \
  "v_pk_fma_f32 v[64:65], v[96:97], v[10:11], v[64:65] op_sel:[0,1,0]\n" \
  "v_smfmac_f32_16x16x64_bf16 v[40:43], v[56:59], v[208:215], v0\n" \
  "v_pk_fma_f32 v[66:67], v[98:99], v[10:11], v[66:67] op_sel:[0,1,0]\n" \
  "v_pk_fma_f32 v[68:69], v[100:101], v[10:11], v[68:69] op_sel:[0,1,0]\n" \
  "v_pk_fma_f32 v[70:71], v[102:103], v[10:11], v[70:71] op_sel:[0,1,0]\n" \
  "v_smfmac_f32_16x16x64_bf16 v[44:47], v[56:59], v[240:247], v0\n" \
  "v_pk_mul_f32 v[72:73], v[72:73], v[10:11] op_sel_hi:[1,0]\n" \
  "v_pk_mul_f32 v[74:75], v[74:75], v[10:11] op_sel_hi:[1,0]\n" \
  "v_pk_mul_f32 v[76:77], v[76:77], v[10:11] op_sel_hi:[1,0]\n" \
  "v_smfmac_f32_16x16x64_bf16 v[32:35], v[60:63], v[152:159], v0\n" \
  "v_pk_mul_f32 v[78:79], v[78:79], v[10:11] op_sel_hi:[1,0]\n" \
  "v_pk_fma_f32 v[72:73], v[104:105], v[10:11], v[72:73] op_sel:[0,1,0]\n" \
  "v_pk_fma_f32 v[74:75], v[106:107], v[10:11], v[74:75] op_sel:[0,1,0]\n" \
  "v_smfmac_f32_16x16x64_bf16 v[36:39], v[60:63], v[184:191], v0\n" \
  "v_pk_fma_f32 v[76:77], v[108:109], v[10:11], v[76:77] op_sel:[0,1,0]\n" \
  "v_pk_fma_f32 v[78:79], v[110:111], v[10:11], v[78:79] op_sel:[0,1,0]\n" \
  "v_pk_mul_f32 v[80:81], v[80:81], v[14:15] op_sel_hi:[1,0]\n" \
  "v_smfmac_f32_16x16x64_bf16 v[40:43], v[60:63], v[216:223], v0\n" \
  "v_pk_mul_f32 v[82:83], v[82:83], v[14:15] op_sel_hi:[1,0]\n" \
  "v_pk_mul_f32 v[84:85], v[84:85], v[14:15] op_sel_hi:[1,0]\n" \
  "v_pk_mul_f32 v[86:87], v[86:87], v[14:15] op_sel_hi:[1,0]\n" \
  "v_smfmac_f32_16x16x64_bf16 v[44:47], v[60:63], v[248:255], v0\n" \
  "v_pk_fma_f32 v[80:81], v[112:113], v[14:15], v[80:81] op_sel:[0,1,0]\n" \
  "v_pk_fma_f32 v[82:83], v[114:115], v[14:15], v[82:83] op_sel:[0,1,0]\n" \
  "v_pk_fma_f32 v[84:85], v[116:117], v[14:15], v[84:85] op_sel:[0,1,0]\n" \
  "v_pk_fma_f32 v[86:87], v[118:119], v[14:15], v[86:87] op_sel:[0,1,0]\n" \
  "v_pk_mul_f32 v[88:89], v[88:89], v[14:15] op_sel_hi:[1,0]\n" \
  "v_pk_mul_f32 v[90:91], v[90:91], v[14:15] op_sel_hi:[1,0]\n" \
  "v_pk_mul_f32 v[92:93], v[92:93], v[14:15] op_sel_hi:[1,0]\n" \
  "v_pk_mul_f32 v[94:95], v[94:95], v[14:15] op_sel_hi:[1,0]\n" \
  "v_pk_fma_f32 v[88:89], v[120:121], v[14:15], v[88:89] op_sel:[0,1,0]\n" \
  "v_pk_fma_f32 v[90:91], v[122:123], v[14:15], v[90:91] op_sel:[0,1,0]\n" \
  "v_pk_fma_f32 v[92:93], v[124:125], v[14:15], v[92:93] op_sel:[0,1,0]\n" \
  "v_pk_fma_f32 v[94:95], v[126:127], v[14:15], v[94:95] op_sel:[0,1,0]\n" \
  "v_pk_add_f32 v[16:17], v[16:17], v[18:19]\n" \
  "v_pk_add_f32 v[32:33], v[32:33], v[34:35]\n" \
  "v_cvt_pk_bf16_f32 v16, v16, v17\n" \
  "v_cvt_pk_bf16_f32 v17, v32, v33\n" \
  "v_pk_add_f32 v[20:21], v[20:21], v[22:23]\n" \
  "v_pk_add_f32 v[36:37], v[36:37], v[38:39]\n" \
  "v_cvt_pk_bf16_f32 v20, v20, v21\n" \
  "v_cvt_pk_bf16_f32 v21, v36, v37\n" \
  "v_pk_add_f32 v[24:25], v[24:25], v[26:27]\n" \
  "v_pk_add_f32 v[40:41], v[40:41], v[42:43]\n" \
  "v_mfma_f32_16x16x16_bf16 v[64:67], v[16:17], v[8:9], v[64:67]\n" \
  "v_mfma_f32_16x16x16_bf16 v[68:71], v[20:21], v[8:9], v[68:71]\n" \
  "v_cvt_pk_bf16_f32 v24, v24, v25\n" \
  "v_cvt_pk_bf16_f32 v25, v40, v41\n" \
  "v_mfma_f32_16x16x16_bf16 v[80:83], v[16:17], v[12:13], v[80:83]\n" \
  "v_mfma_f32_16x16x16_bf16 v[84:87], v[20:21], v[12:13], v[84:87]\n" \
  "v_pk_add_f32 v[28:29], v[28:29], v[30:31]\n" \
  "v_pk_add_f32 v[44:45], v[44:45], v[46:47]\n" \
  "v_cvt_pk_bf16_f32 v28, v28, v29\n" \
  "v_cvt_pk_bf16_f32 v29, v44, v45\n" \
  "s_nop 1\n" \
  "v_mfma_f32_16x16x16_bf16 v[72:75], v[24:25], v[8:9], v[72:75]\n" \
  "v_mfma_f32_16x16x16_bf16 v[76:79], v[28:29], v[8:9], v[76:79]\n" \
  "v_mfma_f32_16x16x16_bf16 v[88:91], v[24:25], v[12:13], v[88:91]\n" \
  "v_mfma_f32_16x16x16_bf16 v[92:95], v[28:29], v[12:13], v[92:95]\n" \
  "v_cvt_pk_bf16_f32 v64, v64, v65\n" \
  "v_cvt_pk_bf16_f32 v65, v66, v67\n" \
  "v_cvt_pk_bf16_f32 v66, v68, v69\n" \
  "v_cvt_pk_bf16_f32 v67, v70, v71\n" \
  "buffer_store_dwordx4 v[64:67], v2, s[40:43], s75 offen offset:0 sc1\n" \
  "v_cvt_pk_bf16_f32 v80, v80, v81\n" \
  "v_cvt_pk_bf16_f32 v81, v82, v83\n" \
  "v_cvt_pk_bf16_f32 v82, v84, v85\n" \
  "v_cvt_pk_bf16_f32 v83, v86, v87\n" \
  "buffer_store_dwordx4 v[80:83], v2, s[40:43], s76 offen offset:0 sc1\n" \
  "v_cvt_pk_bf16_f32 v72, v72, v73\n" \
  "v_cvt_pk_bf16_f32 v73, v74, v75\n" \
  "v_cvt_pk_bf16_f32 v74, v76, v77\n" \
  "v_cvt_pk_bf16_f32 v75, v78, v79\n" \
  "buffer_store_dwordx4 v[72:75], v2, s[40:43], s75 offen offset:128 sc1\n" \
  "v_cvt_pk_bf16_f32 v88, v88, v89\n" \
  "v_cvt_pk_bf16_f32 v89, v90, v91\n" \
  "v_cvt_pk_bf16_f32 v90, v92, v93\n" \
  "v_cvt_pk_bf16_f32 v91, v94, v95\n" \
  "buffer_store_dwordx4 v[88:91], v2, s[40:43], s76 offen offset:128 sc1\n" \
  ".LWA_NEXT_%=:\n" \
  "v_xor_b32 v3, 0x10000, v3\n" \
  "v_xor_b32 v4, 0x10000, v4\n" \
  "s_mov_b32 s57, s58\n" \
  "s_cmp_eq_u32 s57, 128\n" \
  "s_cbranch_scc1 .LWA_END_%=\n" \
  "s_branch .LWA_TOP_%=\n" \
  ".LWA_END_%=:\n" \
  "s_endpgm\n" \
  ".Lpad0xdf:\n" \
  "s_setpc_b64 s[98:99]\n" \
  ".LWA_EASY_%=:\n" \
  "v_lshrrev_b32 v9, 1, v1\n" \
  "v_mov_b32 v105, 0\n" \
  "v_add_u32 v64, s63, v9\n" \
  "v_mov_b32 v106, 0\n" \
  "s_barrier\n" \
  "s_setprio 3\n" \
  "ds_read_b128 v[72:75], v5 offset:0\n" \
  "ds_read_b128 v[76:79], v5 offset:512\n" \
  "ds_read_b128 v[80:83], v5 offset:1024\n" \
  "ds_read_b128 v[84:87], v5 offset:1536\n" \
  "ds_read_b64 v[88:89], v64 offset:0\n" \
  "ds_read_b64 v[90:91], v64 offset:512\n" \
  "ds_read_b64 v[92:93], v64 offset:1024\n" \
  "ds_read_b64 v[94:95], v64 offset:1536\n" \
  "s_waitcnt lgkmcnt(7)\n" \
  "v_mfma_f32_16x16x32_bf16 v[64:67], v[72:75], v[16:19], 0\n" \
  "s_waitcnt lgkmcnt(6)\n" \
  "v_mfma_f32_16x16x32_bf16 v[64:67], v[76:79], v[20:23], v[64:67]\n" \
  "s_waitcnt lgkmcnt(5)\n" \
  "v_mfma_f32_16x16x32_bf16 v[64:67], v[80:83], v[24:27], v[64:67]\n" \
  "s_waitcnt lgkmcnt(4)\n" \
  "v_mfma_f32_16x16x32_bf16 v[64:67], v[84:87], v[28:31], v[64:67]\n" \
  "s_nop 7\n" \
  "v_maximum3_f32 v8, v8, v64, v65\n" \
  "v_mov_b32 v9, v8\n" \
  "s_nop 1\n" \
  "v_permlane16_swap_b32 v8, v9\n" \
  "v_max_f32 v8, v8, v9\n" \
  "v_mov_b32 v9, v8\n" \
  "s_nop 1\n" \
  "v_permlane32_swap_b32 v8, v9\n" \
  "v_max_f32 v8, v8, v9\n" \
  "v_mul_f32 v8, s80, v8\n" \
  "v_sub_f32 v66, v10, v8\n" \
  "v_fma_f32 v64, v64, s80, -v8\n" \
  "v_fma_f32 v65, v65, s80, -v8\n" \
  "v_exp_f32 v66, v66\n" \
  "v_exp_f32 v64, v64\n" \
  "v_exp_f32 v65, v65\n" \
  "v_fma_f32 v11, v11, v66, v64\n" \
  "v_add_f32 v11, v11, v65\n" \
  "v_mov_b32 v9, v11\n" \
  "s_nop 1\n" \
  "v_permlane16_swap_b32 v11, v9\n" \
  "v_add_f32 v11, v11, v9\n" \
  "v_mov_b32 v9, v11\n" \
  "s_nop 1\n" \
  "v_permlane32_swap_b32 v11, v9\n" \
  "v_add_f32 v11, v11, v9\n" \
  "v_rcp_f32 v68, v11\n" \
  "s_waitcnt lgkmcnt(0)\n" \
  "v_mul_f32 v66, v66, v68\n" \
  "v_mul_f32 v64, v64, v68\n" \
  "v_mul_f32 v65, v65, v68\n" \
  "v_pk_mul_f32 v[32:33], v[32:33], v[66:67] op_sel_hi:[1,0]\n" \
  "v_cvt_pk_bf16_f32 v104, v64, v65\n" \
  "v_pk_mul_f32 v[34:35], v[34:35], v[66:67] op_sel_hi:[1,0]\n" \
  "v_cvt_pk_bf16_f32 v107, v64, v65\n" \
  "v_mul_f32 v48, v48, v66\n" \
  "v_mul_f32 v49, v49, v66\n" \
  "v_mul_f32 v50, v50, v66\n" \
  "v_mul_f32 v51, v51, v66\n" \
  "v_mfma_f32_16x16x16_bf16 v[32:35], v[88:89], v[104:105], v[32:35]\n" \
  "v_mul_f32 v36, v36, v66\n" \
  "v_mul_f32 v37, v37, v66\n" \
  "v_mul_f32 v38, v38, v66\n" \
  "v_mul_f32 v39, v39, v66\n" \
  "v_mfma_f32_16x16x16_bf16 v[48:51], v[88:89], v[106:107], v[48:51]\n" \
  "v_mul_f32 v52, v52, v66\n" \
  "v_mul_f32 v53, v53, v66\n" \
  "v_mul_f32 v54, v54, v66\n" \
  "v_mul_f32 v55, v55, v66\n" \
  "v_mfma_f32_16x16x16_bf16 v[36:39], v[90:91], v[104:105], v[36:39]\n" \
  "v_mul_f32 v40, v40, v66\n" \
  "v_mul_f32 v41, v41, v66\n" \
  "v_mul_f32 v42, v42, v66\n" \
  "v_mul_f32 v43, v43, v66\n" \
  "v_mfma_f32_16x16x16_bf16 v[52:55], v[90:91], v[106:107], v[52:55]\n" \
  "v_mul_f32 v56, v56, v66\n" \
  "v_mul_f32 v57, v57, v66\n" \
  "v_mul_f32 v58, v58, v66\n" \
  "v_mul_f32 v59, v59, v66\n" \
  "v_mfma_f32_16x16x16_bf16 v[40:43], v[92:93], v[104:105], v[40:43]\n" \
  "v_mul_f32 v44, v44, v66\n" \
  "v_mul_f32 v45, v45, v66\n" \
  "v_mul_f32 v46, v46, v66\n" \
  "v_mul_f32 v47, v47, v66\n" \
  "v_mfma_f32_16x16x16_bf16 v[56:59], v[92:93], v[106:107], v[56:59]\n" \
  "v_mul_f32 v60, v60, v66\n" \
  "v_mul_f32 v61, v61, v66\n" \
  "v_mul_f32 v62, v62, v66\n" \
  "v_mul_f32 v63, v63, v66\n" \
  "v_mfma_f32_16x16x16_bf16 v[44:47], v[94:95], v[104:105], v[44:47]\n" \
  "v_cvt_pk_bf16_f32 v108, v32, v33\n" \
  "v_cvt_pk_bf16_f32 v109, v48, v49\n" \
  "v_cvt_pk_bf16_f32 v110, v34, v35\n" \
  "v_cvt_pk_bf16_f32 v111, v50, v51\n" \
  "ds_write_b128 v7, v[108:111] offset:32768\n" \
  "v_mfma_f32_16x16x16_bf16 v[60:63], v[94:95], v[106:107], v[60:63]\n" \
  "v_cvt_pk_bf16_f32 v112, v36, v37\n" \
  "v_cvt_pk_bf16_f32 v113, v52, v53\n" \
  "v_cvt_pk_bf16_f32 v114, v38, v39\n" \
  "v_cvt_pk_bf16_f32 v115, v54, v55\n" \
  "ds_write_b128 v7, v[112:115] offset:33792\n" \
  "v_cvt_pk_bf16_f32 v116, v40, v41\n" \
  "v_cvt_pk_bf16_f32 v117, v56, v57\n" \
  "v_cvt_pk_bf16_f32 v118, v42, v43\n" \
  "v_cvt_pk_bf16_f32 v119, v58, v59\n" \
  "ds_write_b128 v7, v[116:119] offset:34816\n" \
  "v_cvt_pk_bf16_f32 v120, v44, v45\n" \
  "v_cvt_pk_bf16_f32 v121, v60, v61\n" \
  "v_cvt_pk_bf16_f32 v122, v46, v47\n" \
  "v_cvt_pk_bf16_f32 v123, v62, v63\n" \
  "ds_write_b128 v7, v[120:123] offset:35840\n" \
  "s_mov_b32 s0, 0xbe801d62\n" \
  ".set .Lpad0xe0, . - 4\n" \
  "s_waitcnt lgkmcnt(0)\n" \
  "s_barrier\n" \
  "s_setprio 2\n" \
  "ds_read_b128 v[48:51], v1 offset:32768\n" \
  "ds_read_b128 v[52:55], v1 offset:33792\n" \
  "ds_read_b128 v[56:59], v1 offset:34816\n" \
  "s_waitcnt lgkmcnt(2)\n" \
  "v_mfma_f32_16x16x32_bf16 v[64:67], v[128:131], v[48:51], 0\n" \
  "ds_read_b128 v[60:63], v1 offset:35840\n" \
  "s_lshl_b32 s75, s57, 19\n" \
  "v_mfma_f32_16x16x32_bf16 v[68:71], v[160:163], v[48:51], 0\n" \
  "s_add_u32 s75, s75, s60\n" \
  "v_mfma_f32_16x16x32_bf16 v[72:75], v[192:195], v[48:51], 0\n" \
  "s_add_u32 s76, s75, 16384\n" \
  "v_mfma_f32_16x16x32_bf16 v[76:79], v[224:227], v[48:51], 0\n" \
  "s_waitcnt lgkmcnt(2)\n" \
  "v_mfma_f32_16x16x32_bf16 v[64:67], v[132:135], v[52:55], v[64:67]\n" \
  "ds_read_b128 v[48:51], v1 offset:40960\n" \
  "v_mfma_f32_16x16x32_bf16 v[68:71], v[164:167], v[52:55], v[68:71]\n" \
  "v_mfma_f32_16x16x32_bf16 v[72:75], v[196:199], v[52:55], v[72:75]\n" \
  "v_mfma_f32_16x16x32_bf16 v[76:79], v[228:231], v[52:55], v[76:79]\n" \
  "s_waitcnt lgkmcnt(2)\n" \
  "v_mfma_f32_16x16x32_bf16 v[64:67], v[136:139], v[56:59], v[64:67]\n" \
  "ds_read_b128 v[52:55], v1 offset:41984\n" \
  "v_mfma_f32_16x16x32_bf16 v[68:71], v[168:171], v[56:59], v[68:71]\n" \
  "v_mfma_f32_16x16x32_bf16 v[72:75], v[200:203], v[56:59], v[72:75]\n" \
  "v_mfma_f32_16x16x32_bf16 v[76:79], v[232:235], v[56:59], v[76:79]\n" \
  "s_waitcnt lgkmcnt(2)\n" \
  "v_mfma_f32_16x16x32_bf16 v[64:67], v[140:143], v[60:63], v[64:67]\n" \
  "ds_read_b128 v[56:59], v1 offset:43008\n" \
  "v_mfma_f32_16x16x32_bf16 v[68:71], v[172:175], v[60:63], v[68:71]\n" \
  "v_mfma_f32_16x16x32_bf16 v[72:75], v[204:207], v[60:63], v[72:75]\n" \
  "v_mfma_f32_16x16x32_bf16 v[76:79], v[236:239], v[60:63], v[76:79]\n" \
  "s_waitcnt lgkmcnt(2)\n" \
  "v_mfma_f32_16x16x32_bf16 v[64:67], v[144:147], v[48:51], v[64:67]\n" \
  "ds_read_b128 v[60:63], v1 offset:44032\n" \
  "v_mfma_f32_16x16x32_bf16 v[68:71], v[176:179], v[48:51], v[68:71]\n" \
  "v_mfma_f32_16x16x32_bf16 v[72:75], v[208:211], v[48:51], v[72:75]\n" \
  "v_mfma_f32_16x16x32_bf16 v[76:79], v[240:243], v[48:51], v[76:79]\n" \
  "s_waitcnt lgkmcnt(2)\n" \
  "v_mfma_f32_16x16x32_bf16 v[64:67], v[148:151], v[52:55], v[64:67]\n" \
  "ds_read_b128 v[48:51], v1 offset:36864\n" \
  "v_mfma_f32_16x16x32_bf16 v[68:71], v[180:183], v[52:55], v[68:71]\n" \
  "v_mfma_f32_16x16x32_bf16 v[72:75], v[212:215], v[52:55], v[72:75]\n" \
  "v_mfma_f32_16x16x32_bf16 v[76:79], v[244:247], v[52:55], v[76:79]\n" \
  "s_waitcnt lgkmcnt(2)\n" \
  "v_mfma_f32_16x16x32_bf16 v[64:67], v[152:155], v[56:59], v[64:67]\n" \
  "ds_read_b128 v[52:55], v1 offset:37888\n" \
  "v_mfma_f32_16x16x32_bf16 v[68:71], v[184:187], v[56:59], v[68:71]\n" \
  "v_mfma_f32_16x16x32_bf16 v[72:75], v[216:219], v[56:59], v[72:75]\n" \
  "v_mfma_f32_16x16x32_bf16 v[76:79], v[248:251], v[56:59], v[76:79]\n" \
  "s_waitcnt lgkmcnt(2)\n" \
  "v_mfma_f32_16x16x32_bf16 v[64:67], v[156:159], v[60:63], v[64:67]\n" \
  "ds_read_b128 v[56:59], v1 offset:38912\n" \
  "v_mfma_f32_16x16x32_bf16 v[68:71], v[188:191], v[60:63], v[68:71]\n" \
  "v_mfma_f32_16x16x32_bf16 v[72:75], v[220:223], v[60:63], v[72:75]\n" \
  "v_mfma_f32_16x16x32_bf16 v[76:79], v[252:255], v[60:63], v[76:79]\n" \
  "s_waitcnt lgkmcnt(2)\n" \
  "v_mfma_f32_16x16x32_bf16 v[80:83], v[128:131], v[48:51], 0\n" \
  "ds_read_b128 v[60:63], v1 offset:39936\n" \
  "v_mfma_f32_16x16x32_bf16 v[84:87], v[160:163], v[48:51], 0\n" \
  "v_mfma_f32_16x16x32_bf16 v[88:91], v[192:195], v[48:51], 0\n" \
  "v_mfma_f32_16x16x32_bf16 v[92:95], v[224:227], v[48:51], 0\n" \
  "s_waitcnt lgkmcnt(2)\n" \
  "v_mfma_f32_16x16x32_bf16 v[80:83], v[132:135], v[52:55], v[80:83]\n" \
  "ds_read_b128 v[48:51], v1 offset:45056\n" \
  "v_mfma_f32_16x16x32_bf16 v[84:87], v[164:167], v[52:55], v[84:87]\n" \
  "v_cvt_pk_bf16_f32 v64, v64, v65\n" \
  "v_mfma_f32_16x16x32_bf16 v[88:91], v[196:199], v[52:55], v[88:91]\n" \
  "v_cvt_pk_bf16_f32 v65, v66, v67\n" \
  "v_mfma_f32_16x16x32_bf16 v[92:95], v[228:231], v[52:55], v[92:95]\n" \
  "s_waitcnt lgkmcnt(2)\n" \
  "v_mfma_f32_16x16x32_bf16 v[80:83], v[136:139], v[56:59], v[80:83]\n" \
  "ds_read_b128 v[52:55], v1 offset:46080\n" \
  "v_mfma_f32_16x16x32_bf16 v[84:87], v[168:171], v[56:59], v[84:87]\n" \
  "v_cvt_pk_bf16_f32 v66, v68, v69\n" \
  "v_mfma_f32_16x16x32_bf16 v[88:91], v[200:203], v[56:59], v[88:91]\n" \
  "v_cvt_pk_bf16_f32 v67, v70, v71\n" \
  "v_mfma_f32_16x16x32_bf16 v[92:95], v[232:235], v[56:59], v[92:95]\n" \
  "s_waitcnt lgkmcnt(2)\n" \
  "v_mfma_f32_16x16x32_bf16 v[80:83], v[140:143], v[60:63], v[80:83]\n" \
  "ds_read_b128 v[56:59], v1 offset:47104\n" \
  "v_mfma_f32_16x16x32_bf16 v[84:87], v[172:175], v[60:63], v[84:87]\n" \
  "buffer_store_dwordx4 v[64:67], v2, s[40:43], s75 offen offset:0 sc1\n" \
  "v_mfma_f32_16x16x32_bf16 v[88:91], v[204:207], v[60:63], v[88:91]\n" \
  "v_cvt_pk_bf16_f32 v72, v72, v73\n" \
  "v_mfma_f32_16x16x32_bf16 v[92:95], v[236:239], v[60:63], v[92:95]\n" \
  "s_waitcnt lgkmcnt(2)\n" \
  "v_mfma_f32_16x16x32_bf16 v[80:83], v[144:147], v[48:51], v[80:83]\n" \
  "ds_read_b128 v[60:63], v1 offset:48128\n" \
  "v_mfma_f32_16x16x32_bf16 v[84:87], v[176:179], v[48:51], v[84:87]\n" \
  "v_cvt_pk_bf16_f32 v73, v74, v75\n" \
  "v_mfma_f32_16x16x32_bf16 v[88:91], v[208:211], v[48:51], v[88:91]\n" \
  "v_cvt_pk_bf16_f32 v74, v76, v77\n" \
  "v_mfma_f32_16x16x32_bf16 v[92:95], v[240:243], v[48:51], v[92:95]\n" \
  "s_waitcnt lgkmcnt(2)\n" \
  "v_mfma_f32_16x16x32_bf16 v[80:83], v[148:151], v[52:55], v[80:83]\n" \
  "v_mfma_f32_16x16x32_bf16 v[84:87], v[180:183], v[52:55], v[84:87]\n" \
  "v_cvt_pk_bf16_f32 v75, v78, v79\n" \
  "v_mfma_f32_16x16x32_bf16 v[88:91], v[212:215], v[52:55], v[88:91]\n" \
  "buffer_store_dwordx4 v[72:75], v2, s[40:43], s75 offen offset:128 sc1\n" \
  "v_mfma_f32_16x16x32_bf16 v[92:95], v[244:247], v[52:55], v[92:95]\n" \
  "s_waitcnt lgkmcnt(1)\n" \
  "v_mfma_f32_16x16x32_bf16 v[80:83], v[152:155], v[56:59], v[80:83]\n" \
  "v_mfma_f32_16x16x32_bf16 v[84:87], v[184:187], v[56:59], v[84:87]\n" \
  "v_mfma_f32_16x16x32_bf16 v[88:91], v[216:219], v[56:59], v[88:91]\n" \
  "v_mfma_f32_16x16x32_bf16 v[92:95], v[248:251], v[56:59], v[92:95]\n" \
  "s_waitcnt lgkmcnt(0)\n" \
  "v_mfma_f32_16x16x32_bf16 v[80:83], v[156:159], v[60:63], v[80:83]\n" \
  "v_mfma_f32_16x16x32_bf16 v[84:87], v[188:191], v[60:63], v[84:87]\n" \
  "v_mfma_f32_16x16x32_bf16 v[88:91], v[220:223], v[60:63], v[88:91]\n" \
  "v_mfma_f32_16x16x32_bf16 v[92:95], v[252:255], v[60:63], v[92:95]\n" \
  "s_nop 4\n" \
  "v_cvt_pk_bf16_f32 v80, v80, v81\n" \
  "v_cvt_pk_bf16_f32 v81, v82, v83\n" \
  "v_cvt_pk_bf16_f32 v82, v84, v85\n" \
  "v_cvt_pk_bf16_f32 v83, v86, v87\n" \
  "buffer_store_dwordx4 v[80:83], v2, s[40:43], s76 offen offset:0 sc1\n" \
  "v_cvt_pk_bf16_f32 v88, v88, v89\n" \
  "v_cvt_pk_bf16_f32 v89, v90, v91\n" \
  "v_cvt_pk_bf16_f32 v90, v92, v93\n" \
  "v_cvt_pk_bf16_f32 v91, v94, v95\n" \
  "buffer_store_dwordx4 v[88:91], v2, s[40:43], s76 offen offset:128 sc1\n" \
  "s_branch .LWA_NEXT_%=\n" \
  ".p2alignl 12, 0xbf800000\n" \
  ".Lpad0x78:\n" \
  "s_setpc_b64 s[98:99]\n"
#define AW_ASM_1 \
  "v_lshlrev_b32 v7, 5, v0\n" \
  "s_mov_b32 s79, -1\n" \
  "s_cmp_eq_u32 s67, 0\n" \
  "s_cbranch_scc1 .LWB_LATE_%=\n" \
  "s_waitcnt vmcnt(0)\n" \
  "v_mov_b32 v12, s65\n" \
  "v_mov_b32 v13, s67\n" \
  "s_mov_b64 exec, 1\n" \
  "ds_add_u32 v12, v13\n" \
  "s_mov_b64 exec, -1\n" \
  "s_branch .LWB_AL_%=\n" \
  ".p2alignl 12, 0xbf800000, 4000\n" \
  ".p2align 6\n" \
  ".Lpad0xe1:\n" \
  "s_setpc_b64 s[98:99]\n" \
  ".p2align 6\n" \
  ".LWB_AL_%=:\n" \
  ".LWB_TOP_%=:\n" \
  "s_add_u32 s56, s56, 3\n" \
  "s_mov_b32 s77, 0\n" \
  "s_cmp_lt_u32 s56, %[w3]\n" \
  "s_cselect_b32 s68, s57, 0\n" \
  "s_cmp_ge_u32 s68, 64\n" \
  "s_cbranch_scc0 .LWB_HARD_%=\n" \
  "s_mov_b32 m0, s63\n" \
  "s_lshl_b32 s72, s57, 16\n" \
  "s_add_u32 s72, s72, s52\n" \
  "s_addc_u32 s73, s53, 0\n" \
  "global_load_lds_dwordx4 v0, s[72:73] offset:0 sc1\n" \
  "global_load_lds_dwordx4 v0, s[72:73] offset:1024 sc1\n" \
  ".LWB_HARD_%=:\n" \
  "s_sub_u32 s68, s56, %[wd]\n" \
  "s_bitcmp1_b32 s68, 0\n" \
  "s_cselect_b32 s58, 64, %[wd]\n" \
  "s_lshr_b32 s70, s68, 1\n" \
  "s_add_u32 s58, s58, s70\n" \
  "s_cmp_lt_u32 s68, %[w2]\n" \
  "s_cselect_b32 s58, s58, s56\n" \
  "s_min_u32 s58, s58, 128\n" \
  "s_lshr_b32 s68, s58, 6\n" \
  "s_add_u32 s68, s68, s58\n" \
  "s_lshl_b32 s68, s68, 12\n" \
  "buffer_load_dword v5, off, s[44:47], s68 sc1\n" \
  "s_xor_b32 s61, s61, 0x10000\n" \
  "s_xor_b32 s62, s62, 0x10000\n" \
  "s_mov_b32 s67, 0\n" \
  "s_cmp_lt_u32 s56, %[w3]\n" \
  "s_cselect_b32 s68, s57, 0\n" \
  "s_cmp_ge_u32 s68, 64\n" \
  "s_cbranch_scc1 .LWB_ASKEDE_%=\n" \
  "s_and_b32 s75, s57, 64\n" \
  "s_lshl_b32 s75, s75, 4\n" \
  "buffer_load_dword v14, off, s[84:87], s75 sc1\n" \
  "s_and_b32 s76, s57, 63\n" \
  "s_cmp_eq_u32 s76, 63\n" \
  "s_mul_i32 s76, s76, %[pp]\n" \
  "s_cselect_b32 s76, %[lm], s76\n" \
  "s_add_u32 s76, s76, %[mo]\n" \
  "s_lshl_b32 s59, s57, 19\n" \
  "s_add_u32 s59, s59, s66\n" \
  "s_add_u32 s78, s59, 4096\n" \
  "v_mov_b64 v[8:9], -1\n" \
  "v_mov_b64 v[10:11], -1\n" \
  "s_barrier\n" \
  "s_setprio 0\n" \
  "buffer_store_dwordx4 v[8:11], v0, s[40:43], s59 offen offset:0 sc1\n" \
  "buffer_store_dwordx4 v[8:11], v0, s[40:43], s59 offen offset:1024 sc1\n" \
  "buffer_store_dwordx4 v[8:11], v0, s[40:43], s59 offen offset:2048 sc1\n" \
  "buffer_store_dwordx4 v[8:11], v0, s[40:43], s59 offen offset:3072 sc1\n" \
  "buffer_store_dwordx4 v[8:11], v0, s[40:43], s78 offen offset:0 sc1\n" \
  "buffer_store_dwordx4 v[8:11], v0, s[40:43], s78 offen offset:1024 sc1\n" \
  "buffer_store_dwordx4 v[8:11], v0, s[40:43], s78 offen offset:2048 sc1\n" \
  "buffer_store_dwordx4 v[8:11], v0, s[40:43], s78 offen offset:3072 sc1\n" \
  "v_mov_b64 v[16:17], 0\n" \
  "v_mov_b64 v[18:19], 0\n" \
  "v_mov_b64 v[20:21], 0\n" \
  "v_mov_b64 v[22:23], 0\n" \
  "v_mov_b64 v[24:25], 0\n" \
  "v_mov_b64 v[26:27], 0\n" \
  "v_mov_b64 v[28:29], 0\n" \
  "v_mov_b64 v[30:31], 0\n" \
  "v_mov_b64 v[32:33], 0\n" \
  "v_mov_b64 v[34:35], 0\n" \
  "v_mov_b64 v[36:37], 0\n" \
  "v_mov_b64 v[38:39], 0\n" \
  "v_mov_b64 v[40:41], 0\n" \
  "v_mov_b64 v[42:43], 0\n" \
  "v_mov_b64 v[44:45], 0\n" \
  "v_mov_b64 v[46:47], 0\n" \
  "s_waitcnt vmcnt(8)\n" \
  "v_readfirstlane_b32 s74, v14\n" \
  "s_add_u32 s74, s74, s76\n" \
  "s_cmp_lt_i32 s79, 0\n" \
  "s_cbranch_scc1 .LWB_NOD_%=\n" \
  "v_mov_b32 v12, s64\n" \
  "v_mov_b32 v13, 1\n" \
  "s_mov_b64 exec, 1\n" \
  "ds_add_rtn_u32 v15, v12, v13\n" \
  "s_mov_b64 exec, -1\n" \
  "s_waitcnt lgkmcnt(0)\n" \
  "v_readfirstlane_b32 s68, v15\n" \
  "s_and_b32 s68, s68, 3\n" \
  "s_cmp_lg_u32 s68, 3\n" \
  "s_cbranch_scc1 .LWB_NOD_%=\n" \
  "s_memrealtime s[68:69]\n" \
  "s_lshl_b32 s72, s79, 11\n" \
  "s_add_u32 s72, s72, s54\n" \
  "s_addc_u32 s73, s55, 0\n" \
  "s_waitcnt lgkmcnt(0)\n" \
  "s_add_u32 s70, s68, 0\n" \
  "s_or_b32 s70, s70, 1\n" \
  "v_mov_b32 v13, s70\n" \
  "s_mov_b64 exec, 15\n" \
  "global_store_dword v7, v13, s[72:73] sc1\n" \
  "s_mov_b64 exec, -1\n" \
  ".LWB_NOD_%=:\n" \
  "s_mov_b32 s79, -1\n" \
  "v_cmp_eq_u32 vcc, 0, v5\n" \
  "s_cmp_eq_u64 vcc, 0\n" \
  "s_cbranch_scc0 .LWB_NOT_%=\n" \
  "s_mov_b32 m0, s61\n" \
  "s_lshl_b32 s68, s58, 19\n" \
  "s_add_u32 s68, s68, s48\n" \
  "s_addc_u32 s69, s49, 0\n" \
  "global_load_lds_dwordx4 v0, s[68:69] offset:0 sc1\n" \
  "global_load_lds_dwordx4 v0, s[68:69] offset:1024 sc1\n" \
  "global_load_lds_dwordx4 v0, s[68:69] offset:2048 sc1\n" \
  "global_load_lds_dwordx4 v0, s[68:69] offset:3072 sc1\n" \
  "s_add_u32 m0, s61, 4096\n" \
  "s_add_u32 s70, s68, 4096\n" \
  "s_addc_u32 s71, s69, 0\n" \
  "global_load_lds_dwordx4 v0, s[70:71] offset:0 sc1\n" \
  "global_load_lds_dwordx4 v0, s[70:71] offset:1024 sc1\n" \
  "global_load_lds_dwordx4 v0, s[70:71] offset:2048 sc1\n" \
  "global_load_lds_dwordx4 v0, s[70:71] offset:3072 sc1\n" \
  "s_mov_b32 m0, s62\n" \
  "s_lshl_b32 s72, s58, 18\n" \
  "s_add_u32 s72, s72, s50\n" \
  "s_addc_u32 s73, s51, 0\n" \
  "global_load_lds_dwordx4 v0, s[72:73] offset:0 sc1\n" \
  "global_load_lds_dwordx4 v0, s[72:73] offset:1024 sc1\n" \
  "global_load_lds_dwordx4 v0, s[72:73] offset:2048 sc1\n" \
  "global_load_lds_dwordx4 v0, s[72:73] offset:3072 sc1\n" \
  "s_mov_b32 s67, 1\n" \
  ".LWB_NOT_%=:\n" \
  "s_mov_b32 s0, 0xbe801d62\n" \
  ".set .Lpad0xe2, . - 4\n" \
  "ds_read_b128 v[48:51], v1 offset:0\n" \
  "ds_read_b128 v[52:55], v1 offset:1024\n" \
  "ds_read_b128 v[56:59], v1 offset:2048\n" \
  "s_waitcnt lgkmcnt(2)\n" \
  "v_mfma_f32_16x16x32_bf16 v[64:67], v[128:131], v[48:51], 0\n" \
  "ds_read_b128 v[60:63], v1 offset:3072\n" \
  "v_mfma_f32_16x16x32_bf16 v[68:71], v[160:163], v[48:51], 0\n" \
  "v_mfma_f32_16x16x32_bf16 v[72:75], v[192:195], v[48:51], 0\n" \
  "v_mfma_f32_16x16x32_bf16 v[76:79], v[224:227], v[48:51], 0\n" \
  "s_waitcnt lgkmcnt(2)\n" \
  "v_mfma_f32_16x16x32_bf16 v[64:67], v[132:135], v[52:55], v[64:67]\n" \
  "ds_read_b128 v[48:51], v1 offset:8192\n" \
  "v_mfma_f32_16x16x32_bf16 v[68:71], v[164:167], v[52:55], v[68:71]\n" \
  "v_mfma_f32_16x16x32_bf16 v[72:75], v[196:199], v[52:55], v[72:75]\n" \
  "v_mfma_f32_16x16x32_bf16 v[76:79], v[228:231], v[52:55], v[76:79]\n" \
  "s_waitcnt lgkmcnt(2)\n" \
  "v_mfma_f32_16x16x32_bf16 v[64:67], v[136:139], v[56:59], v[64:67]\n" \
  "ds_read_b128 v[52:55], v1 offset:9216\n" \
  "s_memrealtime s[68:69]\n" \
  "v_mfma_f32_16x16x32_bf16 v[68:71], v[168:171], v[56:59], v[68:71]\n" \
  "v_mfma_f32_16x16x32_bf16 v[72:75], v[200:203], v[56:59], v[72:75]\n" \
  "v_mfma_f32_16x16x32_bf16 v[76:79], v[232:235], v[56:59], v[76:79]\n" \
  "s_waitcnt lgkmcnt(2)\n" \
  "v_mfma_f32_16x16x32_bf16 v[64:67], v[140:143], v[60:63], v[64:67]\n" \
  "ds_read_b128 v[56:59], v1 offset:10240\n" \
  "s_waitcnt lgkmcnt(0)\n" \
  "s_sub_i32 s70, s74, s68\n" \
  "s_cmp_gt_i32 s70, 0\n" \
  "s_cbranch_scc1 .LWB_NOTYET_%=\n" \
  "s_mov_b32 m0, s63\n" \
  "s_lshl_b32 s72, s57, 16\n" \
  "s_add_u32 s72, s72, s52\n" \
  "s_addc_u32 s73, s53, 0\n" \
  "global_load_lds_dwordx4 v0, s[72:73] offset:0 sc1\n" \
  "global_load_lds_dwordx4 v0, s[72:73] offset:1024 sc1\n" \
  "s_mov_b32 s77, 1\n" \
  ".LWB_NOTYET_%=:\n" \
  "s_lshl_b32 s70, s67, 7\n" \
  "s_or_b32 s70, s70, s58\n" \
  "s_lshr_b32 s68, s70, 6\n" \
  "s_add_u32 s68, s68, s70\n" \
  "s_lshl_b32 s68, s68, 12\n" \
  "buffer_load_dword v5, off, s[44:47], s68 sc1\n" \
  "v_mfma_f32_16x16x32_bf16 v[68:71], v[172:175], v[60:63], v[68:71]\n" \
  "v_mfma_f32_16x16x32_bf16 v[72:75], v[204:207], v[60:63], v[72:75]\n" \
  "v_mfma_f32_16x16x32_bf16 v[76:79], v[236:239], v[60:63], v[76:79]\n" \
  "s_waitcnt lgkmcnt(2)\n" \
  "v_mfma_f32_16x16x32_bf16 v[96:99], v[144:147], v[48:51], 0\n" \
  "ds_read_b128 v[60:63], v1 offset:11264\n" \
  "v_mfma_f32_16x16x32_bf16 v[100:103], v[176:179], v[48:51], 0\n" \
  "v_mfma_f32_16x16x32_bf16 v[104:107], v[208:211], v[48:51], 0\n" \
  "v_mfma_f32_16x16x32_bf16 v[108:111], v[240:243], v[48:51], 0\n" \
  "s_waitcnt lgkmcnt(2)\n" \
  "v_mfma_f32_16x16x32_bf16 v[96:99], v[148:151], v[52:55], v[96:99]\n" \
  "ds_read_b128 v[48:51], v1 offset:4096\n" \
  "v_mfma_f32_16x16x32_bf16 v[100:103], v[180:183], v[52:55], v[100:103]\n" \
  "v_mfma_f32_16x16x32_bf16 v[104:107], v[212:215], v[52:55], v[104:107]\n" \
  "v_mfma_f32_16x16x32_bf16 v[108:111], v[244:247], v[52:55], v[108:111]\n" \
  "s_waitcnt lgkmcnt(2)\n" \
  "v_mfma_f32_16x16x32_bf16 v[96:99], v[152:155], v[56:59], v[96:99]\n" \
  "ds_read_b128 v[52:55], v1 offset:5120\n" \
  "v_mfma_f32_16x16x32_bf16 v[100:103], v[184:187], v[56:59], v[100:103]\n" \
  "v_mfma_f32_16x16x32_bf16 v[104:107], v[216:219], v[56:59], v[104:107]\n" \
  "v_mfma_f32_16x16x32_bf16 v[108:111], v[248:251], v[56:59], v[108:111]\n" \
  "s_waitcnt lgkmcnt(2)\n" \
  "v_mfma_f32_16x16x32_bf16 v[96:99], v[156:159], v[60:63], v[96:99]\n" \
  "ds_read_b128 v[56:59], v1 offset:6144\n" \
  "v_mfma_f32_16x16x32_bf16 v[100:103], v[188:191], v[60:63], v[100:103]\n" \
  "v_mfma_f32_16x16x32_bf16 v[104:107], v[220:223], v[60:63], v[104:107]\n" \
  "v_mfma_f32_16x16x32_bf16 v[108:111], v[252:255], v[60:63], v[108:111]\n" \
  "s_waitcnt lgkmcnt(2)\n" \
  "v_mfma_f32_16x16x32_bf16 v[80:83], v[128:131], v[48:51], 0\n" \
  "ds_read_b128 v[60:63], v1 offset:7168\n" \
  "v_mfma_f32_16x16x32_bf16 v[84:87], v[160:163], v[48:51], 0\n" \
  "v_mfma_f32_16x16x32_bf16 v[88:91], v[192:195], v[48:51], 0\n" \
  "v_mfma_f32_16x16x32_bf16 v[92:95], v[224:227], v[48:51], 0\n" \
  "s_waitcnt lgkmcnt(2)\n" \
  "v_mfma_f32_16x16x32_bf16 v[80:83], v[132:135], v[52:55], v[80:83]\n" \
  "ds_read_b128 v[48:51], v1 offset:12288\n" \
  "v_mfma_f32_16x16x32_bf16 v[84:87], v[164:167], v[52:55], v[84:87]\n" \
  "v_mfma_f32_16x16x32_bf16 v[88:91], v[196:199], v[52:55], v[88:91]\n" \
  "v_mfma_f32_16x16x32_bf16 v[92:95], v[228:231], v[52:55], v[92:95]\n" \
  "s_waitcnt lgkmcnt(2)\n" \
  "v_mfma_f32_16x16x32_bf16 v[80:83], v[136:139], v[56:59], v[80:83]\n" \
  "ds_read_b128 v[52:55], v1 offset:13312\n" \
  "v_mfma_f32_16x16x32_bf16 v[84:87], v[168:171], v[56:59], v[84:87]\n" \
  "v_mfma_f32_16x16x32_bf16 v[88:91], v[200:203], v[56:59], v[88:91]\n" \
  "v_mfma_f32_16x16x32_bf16 v[92:95], v[232:235], v[56:59], v[92:95]\n" \
  "s_waitcnt lgkmcnt(2)\n" \
  "v_mfma_f32_16x16x32_bf16 v[80:83], v[140:143], v[60:63], v[80:83]\n" \
  "ds_read_b128 v[56:59], v1 offset:14336\n" \
  "v_mfma_f32_16x16x32_bf16 v[84:87], v[172:175], v[60:63], v[84:87]\n" \
  "v_mfma_f32_16x16x32_bf16 v[88:91], v[204:207], v[60:63], v[88:91]\n" \
  "v_mfma_f32_16x16x32_bf16 v[92:95], v[236:239], v[60:63], v[92:95]\n" \
  "s_waitcnt lgkmcnt(2)\n" \
  "v_mfma_f32_16x16x32_bf16 v[112:115], v[144:147], v[48:51], 0\n" \
  "ds_read_b128 v[60:63], v1 offset:15360\n" \
  "v_mfma_f32_16x16x32_bf16 v[116:119], v[176:179], v[48:51], 0\n" \
  "v_mfma_f32_16x16x32_bf16 v[120:123], v[208:211], v[48:51], 0\n" \
  "v_mfma_f32_16x16x32_bf16 v[124:127], v[240:243], v[48:51], 0\n" \
  "s_waitcnt lgkmcnt(2)\n" \
  "v_mfma_f32_16x16x32_bf16 v[112:115], v[148:151], v[52:55], v[112:115]\n" \
  "v_mfma_f32_16x16x32_bf16 v[116:119], v[180:183], v[52:55], v[116:119]\n" \
  "v_mfma_f32_16x16x32_bf16 v[120:123], v[212:215], v[52:55], v[120:123]\n" \
  "s_waitcnt vmcnt(0)\n" \
  "ds_read_b128 v[8:11], v3 offset:0\n" \
  "ds_read_b128 v[12:15], v3 offset:1024\n" \
  "v_mfma_f32_16x16x32_bf16 v[124:127], v[244:247], v[52:55], v[124:127]\n" \
  "s_waitcnt lgkmcnt(3)\n" \
  "v_mfma_f32_16x16x32_bf16 v[112:115], v[152:155], v[56:59], v[112:115]\n" \
  "v_mfma_f32_16x16x32_bf16 v[116:119], v[184:187], v[56:59], v[116:119]\n" \
  "v_mfma_f32_16x16x32_bf16 v[120:123], v[216:219], v[56:59], v[120:123]\n" \
  "v_mfma_f32_16x16x32_bf16 v[124:127], v[248:251], v[56:59], v[124:127]\n" \
  "s_waitcnt lgkmcnt(2)\n" \
  "v_mfma_f32_16x16x32_bf16 v[112:115], v[156:159], v[60:63], v[112:115]\n" \
  "s_waitcnt lgkmcnt(0)\n" \
  "v_min3_u32 v8, v8, v9, v10\n" \
  "v_mfma_f32_16x16x32_bf16 v[116:119], v[188:191], v[60:63], v[116:119]\n" \
  "v_min3_u32 v12, v12, v13, v14\n" \
  "v_mfma_f32_16x16x32_bf16 v[120:123], v[220:223], v[60:63], v[120:123]\n" \
  "v_min3_u32 v8, v8, v11, v15\n" \
  "v_mfma_f32_16x16x32_bf16 v[124:127], v[252:255], v[60:63], v[124:127]\n" \
  "v_min_u32 v8, v8, v12\n" \
  "v_cmp_eq_u32 vcc, 0, v5\n" \
  "s_cmp_eq_u64 vcc, 0\n" \
  "s_cbranch_scc0 .LWB_NOU_%=\n" \
  "s_mov_b32 m0, s61\n" \
  "s_lshl_b32 s68, s58, 19\n" \
  "s_add_u32 s68, s68, s48\n" \
  "s_addc_u32 s69, s49, 0\n" \
  "global_load_lds_dwordx4 v0, s[68:69] offset:0 sc1\n" \
  "global_load_lds_dwordx4 v0, s[68:69] offset:1024 sc1\n" \
  "global_load_lds_dwordx4 v0, s[68:69] offset:2048 sc1\n" \
  "global_load_lds_dwordx4 v0, s[68:69] offset:3072 sc1\n" \
  "s_add_u32 m0, s61, 4096\n" \
  "s_add_u32 s70, s68, 4096\n" \
  "s_addc_u32 s71, s69, 0\n" \
  "global_load_lds_dwordx4 v0, s[70:71] offset:0 sc1\n" \
  "global_load_lds_dwordx4 v0, s[70:71] offset:1024 sc1\n" \
  "global_load_lds_dwordx4 v0, s[70:71] offset:2048 sc1\n" \
  "global_load_lds_dwordx4 v0, s[70:71] offset:3072 sc1\n" \
  "s_mov_b32 m0, s62\n" \
  "s_lshl_b32 s72, s58, 18\n" \
  "s_add_u32 s72, s72, s50\n" \
  "s_addc_u32 s73, s51, 0\n" \
  "global_load_lds_dwordx4 v0, s[72:73] offset:0 sc1\n" \
  "global_load_lds_dwordx4 v0, s[72:73] offset:1024 sc1\n" \
  "global_load_lds_dwordx4 v0, s[72:73] offset:2048 sc1\n" \
  "global_load_lds_dwordx4 v0, s[72:73] offset:3072 sc1\n" \
  "s_mov_b32 s67, 1\n" \
  ".LWB_NOU_%=:\n" \
  "s_mov_b32 s0, 0xbe801d62\n" \
  ".set .Lpad0xe3, . - 4\n" \
  "s_cmp_eq_u32 s77, 1\n" \
  "s_cbranch_scc1 .LWB_HERE_%=\n" \
  "s_memrealtime s[68:69]\n" \
  "s_waitcnt lgkmcnt(0)\n" \
  "s_sub_i32 s70, s74, s68\n" \
  "s_cmp_lt_i32 s70, 50\n" \
  "s_cbranch_scc1 .LWB_SOON_%=\n" \
  ".LWB_GATEA_%=:\n" \
  "s_memrealtime s[68:69]\n" \
  "s_waitcnt lgkmcnt(0)\n" \
  "s_sub_i32 s70, s74, s68\n" \
  "s_sub_u32 s70, s70, 65\n" \
  "s_cmp_gt_u32 s70, 2000\n" \
  "s_cbranch_scc1 .LWB_NEWS_%=\n" \
  "s_sleep 1\n" \
  "s_cmp_lt_u32 s70, 40\n" \
  "s_cbranch_scc1 .LWB_GATEA_%=\n" \
  "s_sleep 8\n" \
  "s_branch .LWB_GATEA_%=\n" \
  ".LWB_HERE_%=:\n" \
  "v_mov_b32 v12, s64\n" \
  "v_mov_b32 v13, 1\n" \
  "s_mov_b64 exec, 1\n" \
  "ds_add_rtn_u32 v15, v12, v13 offset:4\n" \
  "s_mov_b64 exec, -1\n" \
  "s_waitcnt lgkmcnt(0)\n" \
  "v_readfirstlane_b32 s68, v15\n" \
  "s_and_b32 s68, s68, 3\n" \
  "s_cmp_lg_u32 s68, 3\n" \
  "s_cbranch_scc1 .LWB_TEST_%=\n" \
  "s_memrealtime s[68:69]\n" \
  "s_lshl_b32 s72, s57, 11\n" \
  "s_add_u32 s72, s72, s54\n" \
  "s_addc_u32 s73, s55, 0\n" \
  "s_waitcnt lgkmcnt(0)\n" \
  "s_cmp_eq_u32 s57, 127\n" \
  "s_cselect_b32 s70, %[ld], 0\n" \
  "s_sub_u32 s68, s68, s70\n" \
  "s_add_u32 s70, s68, %[dt]\n" \
  "s_or_b32 s70, s70, 1\n" \
  "v_mov_b32 v13, s70\n" \
  "s_mov_b64 exec, 15\n" \
  "global_store_dword v7, v13, s[72:73] sc1\n" \
  "s_mov_b64 exec, -1\n" \
  "s_branch .LWB_TEST_%=\n" \
  ".LWB_NEWS_%=:\n" \
  "buffer_load_dword v14, off, s[84:87], s75 sc1\n" \
  "s_waitcnt vmcnt(0)\n" \
  "v_readfirstlane_b32 s74, v14\n" \
  "s_add_u32 s74, s74, s76\n" \
  ".LWB_SOON_%=:\n" \
  "v_mov_b32 v12, s64\n" \
  "v_mov_b32 v13, 1\n" \
  "s_mov_b64 exec, 1\n" \
  "ds_add_rtn_u32 v15, v12, v13 offset:4\n" \
  "s_mov_b64 exec, -1\n" \
  "s_waitcnt lgkmcnt(0)\n" \
  "v_readfirstlane_b32 s68, v15\n" \
  "s_and_b32 s68, s68, 3\n" \
  "s_cmp_lg_u32 s68, 3\n" \
  "s_cbranch_scc1 .LWB_GATEB_%=\n" \
  "s_memrealtime s[68:69]\n" \
  "s_lshl_b32 s72, s57, 11\n" \
  "s_add_u32 s72, s72, s54\n" \
  "s_addc_u32 s73, s55, 0\n" \
  "s_waitcnt lgkmcnt(0)\n" \
  "s_sub_i32 s70, s74, s68\n" \
  "s_cmp_gt_u32 s70, 2000\n" \
  "s_cselect_b32 s70, 0, s70\n" \
  "s_add_u32 s68, s68, s70\n" \
  "s_cmp_eq_u32 s57, 127\n" \
  "s_cselect_b32 s70, %[ld], 0\n" \
  "s_sub_u32 s68, s68, s70\n" \
  "s_add_u32 s70, s68, %[du]\n" \
  "s_or_b32 s70, s70, 1\n" \
  "v_mov_b32 v13, s70\n" \
  "s_mov_b64 exec, 15\n" \
  "global_store_dword v7, v13, s[72:73] sc1\n" \
  "s_mov_b64 exec, -1\n" \
  ".LWB_GATEB_%=:\n" \
  "s_memrealtime s[68:69]\n" \
  "s_waitcnt lgkmcnt(0)\n" \
  "s_sub_i32 s70, s74, s68\n" \
  "s_sub_u32 s70, s70, 0\n" \
  "s_cmp_gt_u32 s70, 2000\n" \
  "s_cbranch_scc1 .LWB_ASK_%=\n" \
  "s_sleep 1\n" \
  "s_cmp_lt_u32 s70, 40\n" \
  "s_cbranch_scc1 .LWB_GATEB_%=\n" \
  "s_sleep 8\n" \
  "s_branch .LWB_GATEB_%=\n" \
  ".LWB_AGAIN_%=:\n" \
  "s_sleep %[sm]\n" \
  ".LWB_ASK_%=:\n" \
  "s_mov_b32 m0, s63\n" \
  "s_lshl_b32 s72, s57, 16\n" \
  "s_add_u32 s72, s72, s52\n" \
  "s_addc_u32 s73, s53, 0\n" \
  "global_load_lds_dwordx4 v0, s[72:73] offset:0 sc1\n" \
  "global_load_lds_dwordx4 v0, s[72:73] offset:1024 sc1\n" \
  "s_add_u32 s77, s77, 1\n" \
  ".LWB_ASKED_%=:\n" \
  "s_waitcnt vmcnt(0)\n" \
  "ds_read_b128 v[8:11], v3 offset:0\n" \
  "ds_read_b128 v[12:15], v3 offset:1024\n" \
  "s_cmp_gt_u32 s77, 0x40000\n" \
  "s_cbranch_scc1 .LWB_OK_%=\n" \
  "s_waitcnt lgkmcnt(1)\n" \
  "v_min3_u32 v8, v8, v9, v10\n" \
  "s_waitcnt lgkmcnt(0)\n" \
  "v_min3_u32 v12, v12, v13, v14\n" \
  "v_min3_u32 v8, v8, v11, v15\n" \
  "v_min_u32 v8, v8, v12\n" \
  ".LWB_TEST_%=:\n" \
  "v_cmp_eq_u32 vcc, 0, v8\n" \
  "s_cmp_eq_u64 vcc, 0\n" \
  "s_cbranch_scc0 .LWB_AGAIN_%=\n" \
  ".LWB_OK_%=:\n" \
  "s_waitcnt lgkmcnt(0)\n" \
  "s_barrier\n" \
  "ds_read_b64_tr_b16 v[48:49], v6 offset:0\n" \
  "ds_read_b64_tr_b16 v[50:51], v6 offset:32\n" \
  "ds_read_b64_tr_b16 v[52:53], v6 offset:1024\n" \
  "ds_read_b64_tr_b16 v[54:55], v6 offset:1056\n" \
  "ds_read_b64_tr_b16 v[56:57], v6 offset:4096\n" \
  "ds_read_b64_tr_b16 v[58:59], v6 offset:4128\n" \
  "ds_read_b64_tr_b16 v[60:61], v6 offset:5120\n" \
  "ds_read_b64_tr_b16 v[62:63], v6 offset:5152\n" \
  "s_waitcnt lgkmcnt(6)\n" \
  "v_smfmac_f32_16x16x64_bf16 v[16:19], v[48:51], v[128:135], v4\n" \
  "v_smfmac_f32_16x16x64_bf16 v[20:23], v[48:51], v[160:167], v4\n" \
  "v_smfmac_f32_16x16x64_bf16 v[24:27], v[48:51], v[192:199], v4\n" \
  "v_smfmac_f32_16x16x64_bf16 v[28:31], v[48:51], v[224:231], v4\n" \
  "s_waitcnt lgkmcnt(4)\n" \
  "v_smfmac_f32_16x16x64_bf16 v[16:19], v[52:55], v[136:143], v4\n" \
  "v_smfmac_f32_16x16x64_bf16 v[20:23], v[52:55], v[168:175], v4\n" \
  "v_smfmac_f32_16x16x64_bf16 v[24:27], v[52:55], v[200:207], v4\n" \
  "v_smfmac_f32_16x16x64_bf16 v[28:31], v[52:55], v[232:239], v4\n" \
  "s_waitcnt lgkmcnt(2)\n" \
  "v_smfmac_f32_16x16x64_bf16 v[32:35], v[56:59], v[144:151], v4\n" \
  "v_smfmac_f32_16x16x64_bf16 v[36:39], v[56:59], v[176:183], v4\n" \
  "v_smfmac_f32_16x16x64_bf16 v[40:43], v[56:59], v[208:215], v4\n" \
  "v_smfmac_f32_16x16x64_bf16 v[44:47], v[56:59], v[240:247], v4\n" \
  "s_waitcnt lgkmcnt(0)\n" \
  "v_smfmac_f32_16x16x64_bf16 v[32:35], v[60:63], v[152:159], v4\n" \
  "v_smfmac_f32_16x16x64_bf16 v[36:39], v[60:63], v[184:191], v4\n" \
  "v_smfmac_f32_16x16x64_bf16 v[40:43], v[60:63], v[216:223], v4\n" \
  "v_smfmac_f32_16x16x64_bf16 v[44:47], v[60:63], v[248:255], v4\n" \
  "s_nop 1\n" \
  "s_lshl_b32 s75, s57, 19\n" \
  "s_add_u32 s75, s75, s60\n" \
  "s_add_u32 s76, s75, 16384\n" \
  "v_pk_add_f32 v[16:17], v[16:17], v[18:19]\n" \
  "v_pk_add_f32 v[32:33], v[32:33], v[34:35]\n" \
  "v_cvt_pk_bf16_f32 v16, v16, v17\n" \
  "v_cvt_pk_bf16_f32 v17, v32, v33\n" \
  "v_pk_add_f32 v[20:21], v[20:21], v[22:23]\n" \
  "v_pk_add_f32 v[36:37], v[36:37], v[38:39]\n" \
  "v_cvt_pk_bf16_f32 v20, v20, v21\n" \
  "v_cvt_pk_bf16_f32 v21, v36, v37\n" \
  "v_pk_add_f32 v[24:25], v[24:25], v[26:27]\n" \
  "v_pk_add_f32 v[40:41], v[40:41], v[42:43]\n" \
  "v_cvt_pk_bf16_f32 v24, v24, v25\n" \
  "v_cvt_pk_bf16_f32 v25, v40, v41\n" \
  "v_pk_add_f32 v[28:29], v[28:29], v[30:31]\n" \
  "v_pk_add_f32 v[44:45], v[44:45], v[46:47]\n" \
  "v_cvt_pk_bf16_f32 v28, v28, v29\n" \
  "v_cvt_pk_bf16_f32 v29, v44, v45\n" \
  "s_mov_b32 s0, 0xbe801d62\n" \
  ".set .Lpad0xe4, . - 4\n" \
  "s_barrier\n" \
  "ds_read_b128 v[32:35], v1 offset:24576\n" \
  "ds_read_b128 v[36:39], v1 offset:25600\n" \
  "s_setprio 1\n" \
  "s_waitcnt lgkmcnt(0)\n" \
  "v_pk_mul_f32 v[64:65], v[64:65], v[34:35] op_sel_hi:[1,0]\n" \
  "v_pk_mul_f32 v[66:67], v[66:67], v[34:35] op_sel_hi:[1,0]\n" \
  "v_pk_mul_f32 v[68:69], v[68:69], v[34:35] op_sel_hi:[1,0]\n" \
  "v_pk_mul_f32 v[70:71], v[70:71], v[34:35] op_sel_hi:[1,0]\n" \
  "v_pk_fma_f32 v[64:65], v[96:97], v[34:35], v[64:65] op_sel:[0,1,0]\n" \
  "v_pk_fma_f32 v[66:67], v[98:99], v[34:35], v[66:67] op_sel:[0,1,0]\n" \
  "v_pk_fma_f32 v[68:69], v[100:101], v[34:35], v[68:69] op_sel:[0,1,0]\n" \
  "v_pk_fma_f32 v[70:71], v[102:103], v[34:35], v[70:71] op_sel:[0,1,0]\n" \
  "s_nop 1\n" \
  "v_mfma_f32_16x16x16_bf16 v[64:67], v[16:17], v[32:33], v[64:67]\n" \
  "v_mfma_f32_16x16x16_bf16 v[68:71], v[20:21], v[32:33], v[68:71]\n" \
  "v_pk_mul_f32 v[72:73], v[72:73], v[34:35] op_sel_hi:[1,0]\n" \
  "v_pk_mul_f32 v[74:75], v[74:75], v[34:35] op_sel_hi:[1,0]\n" \
  "v_pk_mul_f32 v[76:77], v[76:77], v[34:35] op_sel_hi:[1,0]\n" \
  "v_pk_mul_f32 v[78:79], v[78:79], v[34:35] op_sel_hi:[1,0]\n" \
  "v_pk_fma_f32 v[72:73], v[104:105], v[34:35], v[72:73] op_sel:[0,1,0]\n" \
  "v_pk_fma_f32 v[74:75], v[106:107], v[34:35], v[74:75] op_sel:[0,1,0]\n" \
  "v_pk_fma_f32 v[76:77], v[108:109], v[34:35], v[76:77] op_sel:[0,1,0]\n" \
  "v_pk_fma_f32 v[78:79], v[110:111], v[34:35], v[78:79] op_sel:[0,1,0]\n" \
  "s_nop 1\n" \
  "v_mfma_f32_16x16x16_bf16 v[72:75], v[24:25], v[32:33], v[72:75]\n" \
  "v_mfma_f32_16x16x16_bf16 v[76:79], v[28:29], v[32:33], v[76:79]\n" \
  "v_cvt_pk_bf16_f32 v64, v64, v65\n" \
  "v_cvt_pk_bf16_f32 v65, v66, v67\n" \
  "v_cvt_pk_bf16_f32 v66, v68, v69\n" \
  "v_cvt_pk_bf16_f32 v67, v70, v71\n" \
  "buffer_store_dwordx4 v[64:67], v2, s[40:43], s75 offen offset:0 sc1\n" \
  "v_pk_mul_f32 v[80:81], v[80:81], v[38:39] op_sel_hi:[1,0]\n" \
  "v_pk_mul_f32 v[82:83], v[82:83], v[38:39] op_sel_hi:[1,0]\n" \
  "v_pk_mul_f32 v[84:85], v[84:85], v[38:39] op_sel_hi:[1,0]\n" \
  "v_pk_mul_f32 v[86:87], v[86:87], v[38:39] op_sel_hi:[1,0]\n" \
  "v_pk_fma_f32 v[80:81], v[112:113], v[38:39], v[80:81] op_sel:[0,1,0]\n" \
  "v_pk_fma_f32 v[82:83], v[114:115], v[38:39], v[82:83] op_sel:[0,1,0]\n" \
  "v_pk_fma_f32 v[84:85], v[116:117], v[38:39], v[84:85] op_sel:[0,1,0]\n" \
  "v_pk_fma_f32 v[86:87], v[118:119], v[38:39], v[86:87] op_sel:[0,1,0]\n" \
  "s_nop 1\n" \
  "v_mfma_f32_16x16x16_bf16 v[80:83], v[16:17], v[36:37], v[80:83]\n" \
  "v_mfma_f32_16x16x16_bf16 v[84:87], v[20:21], v[36:37], v[84:87]\n" \
  "v_cvt_pk_bf16_f32 v72, v72, v73\n" \
  "v_cvt_pk_bf16_f32 v73, v74, v75\n" \
  "v_cvt_pk_bf16_f32 v74, v76, v77\n" \
  "v_cvt_pk_bf16_f32 v75, v78, v79\n" \
  "buffer_store_dwordx4 v[72:75], v2, s[40:43], s75 offen offset:128 sc1\n" \
  "v_pk_mul_f32 v[88:89], v[88:89], v[38:39] op_sel_hi:[1,0]\n" \
  "v_pk_mul_f32 v[90:91], v[90:91], v[38:39] op_sel_hi:[1,0]\n" \
  "v_pk_mul_f32 v[92:93], v[92:93], v[38:39] op_sel_hi:[1,0]\n" \
  "v_pk_mul_f32 v[94:95], v[94:95], v[38:39] op_sel_hi:[1,0]\n" \
  "v_pk_fma_f32 v[88:89], v[120:121], v[38:39], v[88:89] op_sel:[0,1,0]\n" \
  "v_pk_fma_f32 v[90:91], v[122:123], v[38:39], v[90:91] op_sel:[0,1,0]\n" \
  "v_pk_fma_f32 v[92:93], v[124:125], v[38:39], v[92:93] op_sel:[0,1,0]\n" \
  "v_pk_fma_f32 v[94:95], v[126:127], v[38:39], v[94:95] op_sel:[0,1,0]\n" \
  "s_nop 1\n" \
  "v_mfma_f32_16x16x16_bf16 v[88:91], v[24:25], v[36:37], v[88:91]\n" \
  "v_mfma_f32_16x16x16_bf16 v[92:95], v[28:29], v[36:37], v[92:95]\n" \
  "v_cvt_pk_bf16_f32 v80, v80, v81\n" \
  "v_cvt_pk_bf16_f32 v81, v82, v83\n" \
  "v_cvt_pk_bf16_f32 v82, v84, v85\n" \
  "v_cvt_pk_bf16_f32 v83, v86, v87\n" \
  "buffer_store_dwordx4 v[80:83], v2, s[40:43], s76 offen offset:0 sc1\n" \
  "s_nop 2\n" \
  "v_cvt_pk_bf16_f32 v88, v88, v89\n" \
  "v_cvt_pk_bf16_f32 v89, v90, v91\n" \
  "v_cvt_pk_bf16_f32 v90, v92, v93\n" \
  "v_cvt_pk_bf16_f32 v91, v94, v95\n" \
  "buffer_store_dwordx4 v[88:91], v2, s[40:43], s76 offen offset:128 sc1\n" \
  "s_waitcnt vmcnt(4)\n" \
  "v_mov_b32 v12, s65\n" \
  "v_mov_b32 v13, s67\n" \
  "s_mov_b64 exec, 1\n" \
  "ds_add_u32 v12, v13\n" \
  "s_mov_b64 exec, -1\n" \
  ".LWB_NEXT_%=:\n" \
  "s_setprio 0\n" \
  "s_mov_b32 s57, s58\n" \
  "s_cmp_eq_u32 s67, 0\n" \
  "s_cbranch_scc1 .LWB_MISS_%=\n" \
  "s_branch .LWB_TOP_%=\n" \
  ".LWB_MISS_%=:\n" \
  "s_cmp_eq_u32 s57, 128\n" \
  "s_cbranch_scc1 .LWB_END_%=\n" \
  ".LWB_LATE_%=:\n" \
  "s_mov_b32 s77, 0x40000\n" \
  ".LWB_POLL_%=:\n" \
  "s_lshr_b32 s68, s57, 6\n" \
  "s_add_u32 s68, s68, s57\n" \
  "s_lshl_b32 s68, s68, 12\n" \
  "buffer_load_dword v5, off, s[44:47], s68 sc1\n" \
  "s_sub_u32 s77, s77, 1\n" \
  "s_cbranch_scc1 .LWB_SEEN_%=\n" \
  "s_waitcnt vmcnt(0)\n" \
  "v_cmp_eq_u32 vcc, 0, v5\n" \
  "s_cmp_eq_u64 vcc, 0\n" \
  "s_cbranch_scc1 .LWB_SEEN_%=\n" \
  "s_sleep %[sa]\n" \
  "s_branch .LWB_POLL_%=\n" \
  ".LWB_SEEN_%=:\n" \
  "s_mov_b32 m0, s61\n" \
  "s_lshl_b32 s68, s57, 19\n" \
  "s_add_u32 s68, s68, s48\n" \
  "s_addc_u32 s69, s49, 0\n" \
  "global_load_lds_dwordx4 v0, s[68:69] offset:0 sc1\n" \
  "global_load_lds_dwordx4 v0, s[68:69] offset:1024 sc1\n" \
  "global_load_lds_dwordx4 v0, s[68:69] offset:2048 sc1\n" \
  "global_load_lds_dwordx4 v0, s[68:69] offset:3072 sc1\n" \
  "s_add_u32 m0, s61, 4096\n" \
  "s_add_u32 s70, s68, 4096\n" \
  "s_addc_u32 s71, s69, 0\n" \
  "global_load_lds_dwordx4 v0, s[70:71] offset:0 sc1\n" \
  "global_load_lds_dwordx4 v0, s[70:71] offset:1024 sc1\n" \
  "global_load_lds_dwordx4 v0, s[70:71] offset:2048 sc1\n" \
  "global_load_lds_dwordx4 v0, s[70:71] offset:3072 sc1\n" \
  "s_mov_b32 m0, s62\n" \
  "s_lshl_b32 s72, s57, 18\n" \
  "s_add_u32 s72, s72, s50\n" \
  "s_addc_u32 s73, s51, 0\n" \
  "global_load_lds_dwordx4 v0, s[72:73] offset:0 sc1\n" \
  "global_load_lds_dwordx4 v0, s[72:73] offset:1024 sc1\n" \
  "global_load_lds_dwordx4 v0, s[72:73] offset:2048 sc1\n" \
  "global_load_lds_dwordx4 v0, s[72:73] offset:3072 sc1\n" \
  "s_mov_b32 s67, 1\n" \
  "s_waitcnt vmcnt(0)\n" \
  "v_mov_b32 v12, s65\n" \
  "v_mov_b32 v13, s67\n" \
  "s_mov_b64 exec, 1\n" \
  "ds_add_u32 v12, v13\n" \
  "s_mov_b64 exec, -1\n" \
  "s_branch .LWB_TOP_%=\n" \
  ".LWB_END_%=:\n" \
  "s_endpgm\n" \
  ".Lpad0xe5:\n" \
  "s_setpc_b64 s[98:99]\n" \
  ".LWB_AGAINE_%=:\n" \
  "s_sleep %[sm]\n" \
  ".LWB_ASKE_%=:\n" \
  "s_mov_b32 m0, s63\n" \
  "s_lshl_b32 s72, s57, 16\n" \
  "s_add_u32 s72, s72, s52\n" \
  "s_addc_u32 s73, s53, 0\n" \
  "global_load_lds_dwordx4 v0, s[72:73] offset:0 sc1\n" \
  "global_load_lds_dwordx4 v0, s[72:73] offset:1024 sc1\n" \
  "s_add_u32 s77, s77, 1\n" \
  ".LWB_ASKEDE_%=:\n" \
  "s_waitcnt vmcnt(0)\n" \
  "ds_read_b128 v[8:11], v3 offset:0\n" \
  "ds_read_b128 v[12:15], v3 offset:1024\n" \
  "s_cmp_gt_u32 s77, 0x40000\n" \
  "s_cbranch_scc1 .LWB_OKE_%=\n" \
  "s_waitcnt lgkmcnt(1)\n" \
  "v_min3_u32 v8, v8, v9, v10\n" \
  "s_waitcnt lgkmcnt(0)\n" \
  "v_min3_u32 v12, v12, v13, v14\n" \
  "v_min3_u32 v8, v8, v11, v15\n" \
  "v_min_u32 v8, v8, v12\n" \
  ".LWB_TESTE_%=:\n" \
  "v_cmp_eq_u32 vcc, 0, v8\n" \
  "s_cmp_eq_u64 vcc, 0\n" \
  "s_cbranch_scc0 .LWB_AGAINE_%=\n" \
  ".LWB_OKE_%=:\n" \
  "s_waitcnt lgkmcnt(0)\n" \
  "s_barrier\n" \
  "v_cmp_eq_u32 vcc, 0, v5\n" \
  "s_cmp_eq_u64 vcc, 0\n" \
  "s_cbranch_scc0 .LWB_NOE_%=\n" \
  "s_mov_b32 m0, s61\n" \
  "s_lshl_b32 s68, s58, 19\n" \
  "s_add_u32 s68, s68, s48\n" \
  "s_addc_u32 s69, s49, 0\n" \
  "global_load_lds_dwordx4 v0, s[68:69] offset:0 sc1\n" \
  "global_load_lds_dwordx4 v0, s[68:69] offset:1024 sc1\n" \
  "global_load_lds_dwordx4 v0, s[68:69] offset:2048 sc1\n" \
  "global_load_lds_dwordx4 v0, s[68:69] offset:3072 sc1\n" \
  "s_add_u32 m0, s61, 4096\n" \
  "s_add_u32 s70, s68, 4096\n" \
  "s_addc_u32 s71, s69, 0\n" \
  "global_load_lds_dwordx4 v0, s[70:71] offset:0 sc1\n" \
  "global_load_lds_dwordx4 v0, s[70:71] offset:1024 sc1\n" \
  "global_load_lds_dwordx4 v0, s[70:71] offset:2048 sc1\n" \
  "global_load_lds_dwordx4 v0, s[70:71] offset:3072 sc1\n" \
  "s_mov_b32 m0, s62\n" \
  "s_lshl_b32 s72, s58, 18\n" \
  "s_add_u32 s72, s72, s50\n" \
  "s_addc_u32 s73, s51, 0\n" \
  "global_load_lds_dwordx4 v0, s[72:73] offset:0 sc1\n" \
  "global_load_lds_dwordx4 v0, s[72:73] offset:1024 sc1\n" \
  "global_load_lds_dwordx4 v0, s[72:73] offset:2048 sc1\n" \
  "global_load_lds_dwordx4 v0, s[72:73] offset:3072 sc1\n" \
  "s_mov_b32 s67, 1\n" \
  ".LWB_NOE_%=:\n" \
  "s_lshl_b32 s70, s67, 7\n" \
  "s_or_b32 s70, s70, s58\n" \
  "s_lshr_b32 s68, s70, 6\n" \
  "s_add_u32 s68, s68, s70\n" \
  "s_lshl_b32 s68, s68, 12\n" \
  "buffer_load_dword v5, off, s[44:47], s68 sc1\n" \
  "s_mov_b32 s77, 0\n" \
  "s_mov_b32 s0, 0xbe801d62\n" \
  ".set .Lpad0xe6, . - 4\n" \
  "s_barrier\n" \
  "s_setprio 0\n" \
  "s_waitcnt vmcnt(0)\n" \
  "v_mov_b32 v12, s65\n" \
  "v_mov_b32 v13, s67\n" \
  "s_mov_b64 exec, 1\n" \
  "ds_add_u32 v12, v13\n" \
  "s_mov_b64 exec, -1\n" \
  "v_cmp_eq_u32 vcc, 0, v5\n" \
  "s_cmp_eq_u64 vcc, 0\n" \
  "s_cbranch_scc0 .LWB_NOF_%=\n" \
  "s_mov_b32 m0, s61\n" \
  "s_lshl_b32 s68, s58, 19\n" \
  "s_add_u32 s68, s68, s48\n" \
  "s_addc_u32 s69, s49, 0\n" \
  "global_load_lds_dwordx4 v0, s[68:69] offset:0 sc1\n" \
  "global_load_lds_dwordx4 v0, s[68:69] offset:1024 sc1\n" \
  "global_load_lds_dwordx4 v0, s[68:69] offset:2048 sc1\n" \
  "global_load_lds_dwordx4 v0, s[68:69] offset:3072 sc1\n" \
  "s_add_u32 m0, s61, 4096\n" \
  "s_add_u32 s70, s68, 4096\n" \
  "s_addc_u32 s71, s69, 0\n" \
  "global_load_lds_dwordx4 v0, s[70:71] offset:0 sc1\n" \
  "global_load_lds_dwordx4 v0, s[70:71] offset:1024 sc1\n" \
  "global_load_lds_dwordx4 v0, s[70:71] offset:2048 sc1\n" \
  "global_load_lds_dwordx4 v0, s[70:71] offset:3072 sc1\n" \
  "s_mov_b32 m0, s62\n" \
  "s_lshl_b32 s72, s58, 18\n" \
  "s_add_u32 s72, s72, s50\n" \
  "s_addc_u32 s73, s51, 0\n" \
  "global_load_lds_dwordx4 v0, s[72:73] offset:0 sc1\n" \
  "global_load_lds_dwordx4 v0, s[72:73] offset:1024 sc1\n" \
  "global_load_lds_dwordx4 v0, s[72:73] offset:2048 sc1\n" \
  "global_load_lds_dwordx4 v0, s[72:73] offset:3072 sc1\n" \
  "s_mov_b32 s67, 1\n" \
  "s_mov_b32 s77, 1\n" \
  ".LWB_NOF_%=:\n" \
  "ds_read_b128 v[48:51], v1 offset:32768\n" \
  "ds_read_b128 v[52:55], v1 offset:33792\n" \
  "ds_read_b128 v[56:59], v1 offset:34816\n" \
  "s_waitcnt lgkmcnt(2)\n" \
  "v_mfma_f32_16x16x32_bf16 v[64:67], v[128:131], v[48:51], 0\n" \
  "ds_read_b128 v[60:63], v1 offset:35840\n" \
  "s_lshl_b32 s75, s57, 19\n" \
  "v_mfma_f32_16x16x32_bf16 v[68:71], v[160:163], v[48:51], 0\n" \
  "s_add_u32 s75, s75, s60\n" \
  "v_mfma_f32_16x16x32_bf16 v[72:75], v[192:195], v[48:51], 0\n" \
  "s_add_u32 s76, s75, 16384\n" \
  "v_mfma_f32_16x16x32_bf16 v[76:79], v[224:227], v[48:51], 0\n" \
  "s_lshl_b32 s70, s67, 7\n" \
  "s_or_b32 s70, s70, s58\n" \
  "s_lshr_b32 s68, s70, 6\n" \
  "s_add_u32 s68, s68, s70\n" \
  "s_lshl_b32 s68, s68, 12\n" \
  "buffer_load_dword v5, off, s[44:47], s68 sc1\n" \
  "s_waitcnt lgkmcnt(2)\n" \
  "v_mfma_f32_16x16x32_bf16 v[64:67], v[132:135], v[52:55], v[64:67]\n" \
  "ds_read_b128 v[48:51], v1 offset:40960\n" \
  "v_mfma_f32_16x16x32_bf16 v[68:71], v[164:167], v[52:55], v[68:71]\n" \
  "v_mfma_f32_16x16x32_bf16 v[72:75], v[196:199], v[52:55], v[72:75]\n" \
  "v_mfma_f32_16x16x32_bf16 v[76:79], v[228:231], v[52:55], v[76:79]\n" \
  "s_waitcnt lgkmcnt(2)\n" \
  "v_mfma_f32_16x16x32_bf16 v[64:67], v[136:139], v[56:59], v[64:67]\n" \
  "ds_read_b128 v[52:55], v1 offset:41984\n" \
  "v_mfma_f32_16x16x32_bf16 v[68:71], v[168:171], v[56:59], v[68:71]\n" \
  "v_mfma_f32_16x16x32_bf16 v[72:75], v[200:203], v[56:59], v[72:75]\n" \
  "v_mfma_f32_16x16x32_bf16 v[76:79], v[232:235], v[56:59], v[76:79]\n" \
  "s_waitcnt lgkmcnt(2)\n" \
  "v_mfma_f32_16x16x32_bf16 v[64:67], v[140:143], v[60:63], v[64:67]\n" \
  "ds_read_b128 v[56:59], v1 offset:43008\n" \
  "v_mfma_f32_16x16x32_bf16 v[68:71], v[172:175], v[60:63], v[68:71]\n" \
  "v_mfma_f32_16x16x32_bf16 v[72:75], v[204:207], v[60:63], v[72:75]\n" \
  "s_cmp_lg_u32 s67, 0\n" \
  "s_cbranch_scc1 .LWB_NOG_%=\n" \
  "s_waitcnt vmcnt(0)\n" \
  "v_cmp_eq_u32 vcc, 0, v5\n" \
  "s_cmp_eq_u64 vcc, 0\n" \
  "s_cbranch_scc0 .LWB_NOG_%=\n" \
  "s_mov_b32 m0, s61\n" \
  "s_lshl_b32 s68, s58, 19\n" \
  "s_add_u32 s68, s68, s48\n" \
  "s_addc_u32 s69, s49, 0\n" \
  "global_load_lds_dwordx4 v0, s[68:69] offset:0 sc1\n" \
  "global_load_lds_dwordx4 v0, s[68:69] offset:1024 sc1\n" \
  "global_load_lds_dwordx4 v0, s[68:69] offset:2048 sc1\n" \
  "global_load_lds_dwordx4 v0, s[68:69] offset:3072 sc1\n" \
  "s_add_u32 m0, s61, 4096\n" \
  "s_add_u32 s70, s68, 4096\n" \
  "s_addc_u32 s71, s69, 0\n" \
  "global_load_lds_dwordx4 v0, s[70:71] offset:0 sc1\n" \
  "global_load_lds_dwordx4 v0, s[70:71] offset:1024 sc1\n" \
  "global_load_lds_dwordx4 v0, s[70:71] offset:2048 sc1\n" \
  "global_load_lds_dwordx4 v0, s[70:71] offset:3072 sc1\n" \
  "s_mov_b32 m0, s62\n" \
  "s_lshl_b32 s72, s58, 18\n" \
  "s_add_u32 s72, s72, s50\n" \
  "s_addc_u32 s73, s51, 0\n" \
  "global_load_lds_dwordx4 v0, s[72:73] offset:0 sc1\n" \
  "global_load_lds_dwordx4 v0, s[72:73] offset:1024 sc1\n" \
  "global_load_lds_dwordx4 v0, s[72:73] offset:2048 sc1\n" \
  "global_load_lds_dwordx4 v0, s[72:73] offset:3072 sc1\n" \
  "s_mov_b32 s67, 1\n" \
  "s_mov_b32 s77, 1\n" \
  ".LWB_NOG_%=:\n" \
  "v_mfma_f32_16x16x32_bf16 v[76:79], v[236:239], v[60:63], v[76:79]\n" \
  "s_waitcnt lgkmcnt(2)\n" \
  "v_mfma_f32_16x16x32_bf16 v[64:67], v[144:147], v[48:51], v[64:67]\n" \
  "ds_read_b128 v[60:63], v1 offset:44032\n" \
  "v_mfma_f32_16x16x32_bf16 v[68:71], v[176:179], v[48:51], v[68:71]\n" \
  "v_mfma_f32_16x16x32_bf16 v[72:75], v[208:211], v[48:51], v[72:75]\n" \
  "v_mfma_f32_16x16x32_bf16 v[76:79], v[240:243], v[48:51], v[76:79]\n" \
  "s_waitcnt lgkmcnt(2)\n" \
  "v_mfma_f32_16x16x32_bf16 v[64:67], v[148:151], v[52:55], v[64:67]\n" \
  "ds_read_b128 v[48:51], v1 offset:36864\n" \
  "v_mfma_f32_16x16x32_bf16 v[68:71], v[180:183], v[52:55], v[68:71]\n" \
  "v_mfma_f32_16x16x32_bf16 v[72:75], v[212:215], v[52:55], v[72:75]\n" \
  "v_mfma_f32_16x16x32_bf16 v[76:79], v[244:247], v[52:55], v[76:79]\n" \
  "s_waitcnt lgkmcnt(2)\n" \
  "v_mfma_f32_16x16x32_bf16 v[64:67], v[152:155], v[56:59], v[64:67]\n" \
  "ds_read_b128 v[52:55], v1 offset:37888\n" \
  "v_mfma_f32_16x16x32_bf16 v[68:71], v[184:187], v[56:59], v[68:71]\n" \
  "v_mfma_f32_16x16x32_bf16 v[72:75], v[216:219], v[56:59], v[72:75]\n" \
  "v_mfma_f32_16x16x32_bf16 v[76:79], v[248:251], v[56:59], v[76:79]\n" \
  "s_waitcnt lgkmcnt(2)\n" \
  "v_mfma_f32_16x16x32_bf16 v[64:67], v[156:159], v[60:63], v[64:67]\n" \
  "ds_read_b128 v[56:59], v1 offset:38912\n" \
  "v_mfma_f32_16x16x32_bf16 v[68:71], v[188:191], v[60:63], v[68:71]\n" \
  "v_mfma_f32_16x16x32_bf16 v[72:75], v[220:223], v[60:63], v[72:75]\n" \
  "v_mfma_f32_16x16x32_bf16 v[76:79], v[252:255], v[60:63], v[76:79]\n" \
  "s_waitcnt lgkmcnt(2)\n" \
  "v_mfma_f32_16x16x32_bf16 v[80:83], v[128:131], v[48:51], 0\n" \
  "ds_read_b128 v[60:63], v1 offset:39936\n" \
  "v_mfma_f32_16x16x32_bf16 v[84:87], v[160:163], v[48:51], 0\n" \
  "v_mfma_f32_16x16x32_bf16 v[88:91], v[192:195], v[48:51], 0\n" \
  "s_waitcnt vmcnt(0)\n" \
  "v_mov_b32 v12, s65\n" \
  "v_mov_b32 v13, s77\n" \
  "s_mov_b64 exec, 1\n" \
  "ds_add_u32 v12, v13\n" \
  "s_mov_b64 exec, -1\n" \
  "v_mfma_f32_16x16x32_bf16 v[92:95], v[224:227], v[48:51], 0\n" \
  "s_lshl_b32 s70, s67, 7\n" \
  "s_or_b32 s70, s70, s58\n" \
  "s_lshr_b32 s68, s70, 6\n" \
  "s_add_u32 s68, s68, s70\n" \
  "s_lshl_b32 s68, s68, 12\n" \
  "buffer_load_dword v5, off, s[44:47], s68 sc1\n" \
  "s_waitcnt lgkmcnt(3)\n" \
  "v_mfma_f32_16x16x32_bf16 v[80:83], v[132:135], v[52:55], v[80:83]\n" \
  "ds_read_b128 v[48:51], v1 offset:45056\n" \
  "v_mfma_f32_16x16x32_bf16 v[84:87], v[164:167], v[52:55], v[84:87]\n" \
  "v_cvt_pk_bf16_f32 v64, v64, v65\n" \
  "v_mfma_f32_16x16x32_bf16 v[88:91], v[196:199], v[52:55], v[88:91]\n" \
  "v_cvt_pk_bf16_f32 v65, v66, v67\n" \
  "v_mfma_f32_16x16x32_bf16 v[92:95], v[228:231], v[52:55], v[92:95]\n" \
  "s_waitcnt lgkmcnt(3)\n" \
  "v_mfma_f32_16x16x32_bf16 v[80:83], v[136:139], v[56:59], v[80:83]\n" \
  "ds_read_b128 v[52:55], v1 offset:46080\n" \
  "v_mfma_f32_16x16x32_bf16 v[84:87], v[168:171], v[56:59], v[84:87]\n" \
  "v_cvt_pk_bf16_f32 v66, v68, v69\n" \
  "v_mfma_f32_16x16x32_bf16 v[88:91], v[200:203], v[56:59], v[88:91]\n" \
  "v_cvt_pk_bf16_f32 v67, v70, v71\n" \
  "v_mfma_f32_16x16x32_bf16 v[92:95], v[232:235], v[56:59], v[92:95]\n" \
  "s_waitcnt lgkmcnt(3)\n" \
  "v_mfma_f32_16x16x32_bf16 v[80:83], v[140:143], v[60:63], v[80:83]\n" \
  "ds_read_b128 v[56:59], v1 offset:47104\n" \
  "v_mfma_f32_16x16x32_bf16 v[84:87], v[172:175], v[60:63], v[84:87]\n" \
  "buffer_store_dwordx4 v[64:67], v2, s[40:43], s75 offen offset:0 sc1\n" \
  "v_mfma_f32_16x16x32_bf16 v[88:91], v[204:207], v[60:63], v[88:91]\n" \
  "v_cvt_pk_bf16_f32 v72, v72, v73\n" \
  "v_mfma_f32_16x16x32_bf16 v[92:95], v[236:239], v[60:63], v[92:95]\n" \
  "s_waitcnt lgkmcnt(2)\n" \
  "v_mfma_f32_16x16x32_bf16 v[80:83], v[144:147], v[48:51], v[80:83]\n" \
  "ds_read_b128 v[60:63], v1 offset:48128\n" \
  "v_mfma_f32_16x16x32_bf16 v[84:87], v[176:179], v[48:51], v[84:87]\n" \
  "v_cvt_pk_bf16_f32 v73, v74, v75\n" \
  "v_mfma_f32_16x16x32_bf16 v[88:91], v[208:211], v[48:51], v[88:91]\n" \
  "v_cvt_pk_bf16_f32 v74, v76, v77\n" \
  "v_mfma_f32_16x16x32_bf16 v[92:95], v[240:243], v[48:51], v[92:95]\n" \
  "s_waitcnt lgkmcnt(2)\n" \
  "v_mfma_f32_16x16x32_bf16 v[80:83], v[148:151], v[52:55], v[80:83]\n" \
  "v_mfma_f32_16x16x32_bf16 v[84:87], v[180:183], v[52:55], v[84:87]\n" \
  "v_cvt_pk_bf16_f32 v75, v78, v79\n" \
  "v_mfma_f32_16x16x32_bf16 v[88:91], v[212:215], v[52:55], v[88:91]\n" \
  "buffer_store_dwordx4 v[72:75], v2, s[40:43], s75 offen offset:128 sc1\n" \
  "v_mfma_f32_16x16x32_bf16 v[92:95], v[244:247], v[52:55], v[92:95]\n" \
  "s_waitcnt lgkmcnt(1)\n" \
  "v_mfma_f32_16x16x32_bf16 v[80:83], v[152:155], v[56:59], v[80:83]\n" \
  "v_mfma_f32_16x16x32_bf16 v[84:87], v[184:187], v[56:59], v[84:87]\n" \
  "v_mfma_f32_16x16x32_bf16 v[88:91], v[216:219], v[56:59], v[88:91]\n" \
  "v_mfma_f32_16x16x32_bf16 v[92:95], v[248:251], v[56:59], v[92:95]\n" \
  "s_waitcnt lgkmcnt(0)\n" \
  "v_mfma_f32_16x16x32_bf16 v[80:83], v[156:159], v[60:63], v[80:83]\n" \
  "v_mfma_f32_16x16x32_bf16 v[84:87], v[188:191], v[60:63], v[84:87]\n" \
  "v_mfma_f32_16x16x32_bf16 v[88:91], v[220:223], v[60:63], v[88:91]\n" \
  "v_mfma_f32_16x16x32_bf16 v[92:95], v[252:255], v[60:63], v[92:95]\n" \
  "s_nop 4\n" \
  "v_cvt_pk_bf16_f32 v80, v80, v81\n" \
  "v_cvt_pk_bf16_f32 v81, v82, v83\n" \
  "v_cvt_pk_bf16_f32 v82, v84, v85\n" \
  "v_cvt_pk_bf16_f32 v83, v86, v87\n" \
  "buffer_store_dwordx4 v[80:83], v2, s[40:43], s76 offen offset:0 sc1\n" \
  "v_cvt_pk_bf16_f32 v88, v88, v89\n" \
  "v_cvt_pk_bf16_f32 v89, v90, v91\n" \
  "v_cvt_pk_bf16_f32 v90, v92, v93\n" \
  "v_cvt_pk_bf16_f32 v91, v94, v95\n" \
  "buffer_store_dwordx4 v[88:91], v2, s[40:43], s76 offen offset:128 sc1\n" \
  "s_mov_b32 s79, s57\n" \
  "s_waitcnt vmcnt(4)\n" \
  "v_cmp_eq_u32 vcc, 0, v5\n" \
  "s_cmp_eq_u64 vcc, 0\n" \
  "s_cbranch_scc0 .LWB_NEXT_%=\n" \
  "s_mov_b32 s57, s58\n" \
  "s_setprio 0\n" \
  "s_branch .LWB_SEEN_%=\n"
#define CONV_ASM \
  "v_mov_b64 v[64:65], 0\n" \
  "v_mov_b64 v[66:67], 0\n" \
  "v_mov_b64 v[68:69], 0\n" \
  "v_mov_b64 v[70:71], 0\n" \
  "v_mov_b64 v[72:73], 0\n" \
  "v_mov_b64 v[74:75], 0\n" \
  "v_mov_b64 v[76:77], 0\n" \
  "v_mov_b64 v[78:79], 0\n" \
  "v_mov_b64 v[80:81], 0\n" \
  "v_mov_b64 v[82:83], 0\n" \
  "v_mov_b64 v[84:85], 0\n" \
  "v_mov_b64 v[86:87], 0\n" \
  "v_mov_b64 v[88:89], 0\n" \
  "v_mov_b64 v[90:91], 0\n" \
  "v_mov_b64 v[92:93], 0\n" \
  "v_mov_b64 v[94:95], 0\n" \
  "v_mov_b64 v[96:97], 0\n" \
  "v_mov_b64 v[98:99], 0\n" \
  "v_mov_b64 v[100:101], 0\n" \
  "v_mov_b64 v[102:103], 0\n" \
  "v_mov_b64 v[104:105], 0\n" \
  "v_mov_b64 v[106:107], 0\n" \
  "v_mov_b64 v[108:109], 0\n" \
  "v_mov_b64 v[110:111], 0\n" \
  "v_mov_b64 v[112:113], 0\n" \
  "v_mov_b64 v[114:115], 0\n" \
  "v_mov_b64 v[116:117], 0\n" \
  "v_mov_b64 v[118:119], 0\n" \
  "v_mov_b64 v[120:121], 0\n" \
  "v_mov_b64 v[122:123], 0\n" \
  "v_mov_b64 v[124:125], 0\n" \
  "v_mov_b64 v[126:127], 0\n" \
  "v_mov_b32 v20, 0\n" \
  "v_mov_b32 v41, 0\n" \
  "v_mov_b32 v29, 0\n" \
  "s_branch .LVA_TAPS_%=\n" \
  "s_branch .LVA_AL_%=\n" \
  ".p2alignl 12, 0xbf800000, 3700\n" \
  ".p2align 6\n" \
  ".Lpad0xb0:\n" \
  "s_setpc_b64 s[98:99]\n" \
  ".p2align 6\n" \
  ".LVA_AL_%=:\n" \
  ".LVA_TOP_%=:\n" \
  "s_cmp_lg_u32 s63, 1\n" \
  "s_cbranch_scc1 .LVA_BAR_%=\n" \
  "s_waitcnt vmcnt(0)\n" \
  "v_min3_u32 v56, v42, v43, v41\n" \
  "v_readfirstlane_b32 s78, v29\n" \
  "v_cmp_eq_u32 vcc, 0, v56\n" \
  "s_cbranch_vccz .LVA_SEEN_%=\n" \
  "s_mov_b32 s79, 0\n" \
  "s_cmp_lg_u32 s78, 0\n" \
  "s_cbranch_scc1 .LVA_KNOWN_%=\n" \
  ".LVA_GATE_%=:\n" \
  "s_memrealtime s[74:75]\n" \
  "s_waitcnt lgkmcnt(0)\n" \
  "s_sub_i32 s74, s69, s74\n" \
  "s_cmp_gt_u32 s74, %[gv]\n" \
  "s_cbranch_scc1 .LVA_WHEN_%=\n" \
  "s_sleep 2\n" \
  "s_branch .LVA_GATE_%=\n" \
  ".LVA_NONE_%=:\n" \
  "s_sleep %[sv]\n" \
  ".LVA_WHEN_%=:\n" \
  "buffer_load_dword v29, off, s[44:47], s71 sc1\n" \
  "s_add_u32 s79, s79, 1\n" \
  "s_cmp_gt_u32 s79, 0x40000\n" \
  "s_cbranch_scc1 .LVA_KNOWN_%=\n" \
  "s_waitcnt vmcnt(0)\n" \
  "v_readfirstlane_b32 s78, v29\n" \
  "s_cmp_eq_u32 s78, 0\n" \
  "s_cbranch_scc1 .LVA_NONE_%=\n" \
  ".LVA_KNOWN_%=:\n" \
  "s_memrealtime s[74:75]\n" \
  "s_waitcnt lgkmcnt(0)\n" \
  "s_sub_i32 s74, s78, s74\n" \
  "s_cmp_lt_i32 s74, %[lt]\n" \
  "s_cselect_b32 s70, 0, 0x200000\n" \
  "s_branch .LVA_DUE_%=\n" \
  ".LVA_SOON_%=:\n" \
  "s_cmp_lt_u32 s74, 8\n" \
  "s_cbranch_scc1 .LVA_NOW_%=\n" \
  "s_sleep 1\n" \
  ".LVA_NOW_%=:\n" \
  "s_memrealtime s[74:75]\n" \
  "s_waitcnt lgkmcnt(0)\n" \
  "s_sub_i32 s74, s78, s74\n" \
  ".LVA_DUE_%=:\n" \
  "s_cmp_lt_u32 s74, 1000\n" \
  "s_cbranch_scc1 .LVA_SOON_%=\n" \
  "s_branch .LVA_ASK_%=\n" \
  ".LVA_AGAIN_%=:\n" \
  "s_sleep %[sv]\n" \
  ".LVA_ASK_%=:\n" \
  "buffer_load_dwordx2 v[42:43], v37, s[40:43], s65 offen sc1\n" \
  "buffer_load_dword v41, v38, s[44:47], s66 offen sc1\n" \
  "s_add_u32 s79, s79, 1\n" \
  "s_cmp_gt_u32 s79, 0x40000\n" \
  "s_cbranch_scc1 .LVA_SEEN_%=\n" \
  "s_waitcnt vmcnt(0)\n" \
  "v_min3_u32 v56, v42, v43, v41\n" \
  "v_cmp_eq_u32 vcc, 0, v56\n" \
  "s_cbranch_vccnz .LVA_AGAIN_%=\n" \
  ".LVA_SEEN_%=:\n" \
  "v_and_b32 v62, 0xffff0000, v41\n" \
  "v_lshlrev_b32 v60, 16, v41\n" \
  "v_lshlrev_b32 v56, 16, v42\n" \
  "v_and_b32 v57, 0xffff0000, v42\n" \
  "v_lshlrev_b32 v58, 16, v43\n" \
  "v_and_b32 v59, 0xffff0000, v43\n" \
  "v_rcp_f32 v62, v62\n" \
  "v_pk_add_f32 v[56:57], v[56:57], v[60:61] op_sel_hi:[1,0] neg_lo:[0,1] neg_hi:[0,1]\n" \
  "v_pk_add_f32 v[58:59], v[58:59], v[60:61] op_sel_hi:[1,0] neg_lo:[0,1] neg_hi:[0,1]\n" \
  "v_cvt_pk_bf16_f32 v57, v56, v57\n" \
  "v_cvt_pk_bf16_f32 v59, v58, v59\n" \
  "v_lshlrev_b32 v56, 16, v57\n" \
  "v_and_b32 v57, 0xffff0000, v57\n" \
  "v_lshlrev_b32 v58, 16, v59\n" \
  "v_and_b32 v59, 0xffff0000, v59\n" \
  "v_pk_mul_f32 v[56:57], v[56:57], v[62:63] op_sel_hi:[1,0]\n" \
  "v_pk_mul_f32 v[58:59], v[58:59], v[62:63] op_sel_hi:[1,0]\n" \
  "v_cvt_pk_bf16_f32 v56, v56, v57\n" \
  "v_cvt_pk_bf16_f32 v58, v58, v59\n" \
  "ds_write_b16 v39, v56\n" \
  "ds_write_b16_d16_hi v39, v56 offset:4288\n" \
  "ds_write_b16 v39, v58 offset:8576\n" \
  "ds_write_b16_d16_hi v39, v58 offset:12864\n" \
  "ds_write_b64 v40, v[42:43] offset:512\n" \
  ".LVA_BAR_%=:\n" \
  "s_waitcnt lgkmcnt(0)\n" \
  "s_barrier\n" \
  "ds_read_b128 v[56:59], v0 offset:0\n" \
  "ds_read_u16 v60, v2 offset:0\n" \
  "ds_read_u16 v61, v3 offset:512\n" \
  "ds_read2_b32 v[244:245], v4 offset0:0 offset1:1\n" \
  "ds_read2_b32 v[246:247], v4 offset0:2 offset1:3\n" \
  "ds_read_b32 v248, v4 offset:16\n" \
  "ds_read2_b32 v[250:251], v4 offset0:16 offset1:17\n" \
  "ds_read2_b32 v[252:253], v4 offset0:18 offset1:19\n" \
  "ds_read_b32 v254, v4 offset:80\n" \
  "s_setprio 3\n" \
  "s_waitcnt lgkmcnt(8)\n" \
  "v_mfma_f32_16x16x32_bf16 v[20:23], v[56:59], v[12:15], v[20:23]\n" \
  "v_mfma_f32_16x16x32_bf16 v[20:23], v[56:59], v[16:19], v[20:23]\n" \
  "s_waitcnt lgkmcnt(6)\n" \
  "v_lshlrev_b32 v60, 16, v60\n" \
  "v_lshlrev_b32 v61, 16, v61\n" \
  "v_mul_f32 v60, s64, v60\n" \
  "v_add_u32 v0, v1, v0\n" \
  "v_add_u32 v2, 64, v2\n" \
  "v_add_u32 v4, 64, v4\n" \
  "s_nop 0\n" \
  "v_cvt_pk_bf16_f32 v62, v20, v60\n" \
  "v_lshlrev_b32 v63, 16, v62\n" \
  "v_and_b32 v62, 0xffff0000, v62\n" \
  "v_add_f32 v62, v62, v63\n" \
  "v_cvt_pk_bf16_f32 v62, v62, v62\n" \
  "v_lshlrev_b32 v62, 16, v62\n" \
  "v_add_f32 v62, v62, v61\n" \
  "v_cvt_pk_bf16_f32 v62, v62, v62\n" \
  "s_mov_b64 exec, s[72:73]\n" \
  "ds_write_b16 v3, v62\n" \
  "s_mov_b64 exec, -1\n" \
  "s_cmp_gt_u32 s63, 1\n" \
  "s_waitcnt lgkmcnt(0)\n" \
  "s_barrier\n" \
  "s_cbranch_scc1 .LVA_REST_%=\n" \
  "s_cmp_eq_u32 s63, 1\n" \
  "s_cbranch_scc1 .LVA_ONE_%=\n" \
  "ds_read_b128 v[56:59], v40 offset:0\n" \
  "s_mov_b32 exec_hi, 0\n" \
  "s_waitcnt lgkmcnt(0)\n" \
  "buffer_store_dwordx4 v[56:59], v7, s[48:51], s67 offen sc1\n" \
  "s_mov_b32 exec_hi, -1\n" \
  "s_add_u32 s67, s67, 0x8000\n" \
  "s_branch .LVA_GO_%=\n" \
  ".LVA_ONE_%=:\n" \
  "s_add_u32 s65, s65, 0x8000\n" \
  "s_add_u32 s66, s66, 2048\n" \
  "s_add_u32 s71, s71, 4\n" \
  "s_max_u32 s76, s65, s70\n" \
  "s_max_u32 s78, s66, s70\n" \
  "buffer_load_dword v29, off, s[44:47], s71 sc1\n" \
  "buffer_load_dwordx2 v[42:43], v37, s[40:43], s76 offen sc1\n" \
  "buffer_load_dword v41, v38, s[44:47], s78 offen sc1\n" \
  "s_memrealtime s[74:75]\n" \
  "v_add_u32 v39, 64, v39\n" \
  "s_waitcnt lgkmcnt(0)\n" \
  "s_add_u32 s69, s74, %[gv]\n" \
  "s_branch .LVA_GO_%=\n" \
  ".LVA_REST_%=:\n" \
  "s_setprio 0\n" \
  ".LVA_GO_%=:\n" \
  "ds_read2_b32 v[58:59], v28 offset0:0 offset1:1\n" \
  "ds_read2_b32 v[60:61], v28 offset0:2 offset1:3\n" \
  "ds_read_b32 v62, v28 offset:16\n" \
  "v_alignbit_b32 v244, v245, v244, v6\n" \
  "v_alignbit_b32 v245, v246, v245, v6\n" \
  "v_alignbit_b32 v246, v247, v246, v6\n" \
  "v_alignbit_b32 v247, v248, v247, v6\n" \
  "v_alignbit_b32 v250, v251, v250, v6\n" \
  "v_alignbit_b32 v251, v252, v251, v6\n" \
  "v_alignbit_b32 v252, v253, v252, v6\n" \
  "v_alignbit_b32 v253, v254, v253, v6\n" \
  "v_and_b32 v250, v250, v8\n" \
  "v_and_b32 v251, v251, v9\n" \
  "v_and_b32 v252, v252, v10\n" \
  "v_and_b32 v253, v253, v11\n" \
  "s_bitcmp1_b32 s62, 1\n" \
  "s_cbranch_scc1 .LVA_QH_%=\n" \
  "s_bitcmp1_b32 s62, 0\n" \
  "s_cbranch_scc1 .LVA_QB_%=\n" \
  ".LVA_QA_%=:\n" \
  "v_mfma_f32_16x16x32_bf16 v[64:67], v[128:131], v[244:247], v[68:71]\n" \
  "v_mfma_f32_16x16x32_bf16 v[68:71], v[132:135], v[244:247], v[72:75]\n" \
  "v_mfma_f32_16x16x32_bf16 v[72:75], v[136:139], v[244:247], v[76:79]\n" \
  "v_mfma_f32_16x16x32_bf16 v[76:79], v[140:143], v[244:247], v[80:83]\n" \
  "v_mfma_f32_16x16x32_bf16 v[20:23], v[192:195], v[250:253], v[64:67]\n" \
  "s_nop 7\n" \
  "v_permlane32_swap_b32 v22, v23\n" \
  "v_add_f32 v20, v22, v23\n" \
  "s_cmp_lg_u32 s62, 0\n" \
  "s_cbranch_scc1 .LVA_MOVE_%=\n" \
  "s_branch .LVA_FAR_%=\n" \
  ".Lpad0xb1:\n" \
  "s_setpc_b64 s[98:99]\n" \
  ".LVA_QB_%=:\n" \
  "v_mfma_f32_16x16x32_bf16 v[64:67], v[128:131], v[244:247], v[64:67]\n" \
  "v_mfma_f32_16x16x32_bf16 v[68:71], v[132:135], v[244:247], v[68:71]\n" \
  "v_mfma_f32_16x16x32_bf16 v[72:75], v[136:139], v[244:247], v[72:75]\n" \
  "v_mfma_f32_16x16x32_bf16 v[76:79], v[140:143], v[244:247], v[76:79]\n" \
  "v_mfma_f32_16x16x32_bf16 v[20:23], v[192:195], v[250:253], v[64:67]\n" \
  "s_not_b64 s[72:73], s[72:73]\n" \
  "s_nop 6\n" \
  "v_permlane32_swap_b32 v20, v21\n" \
  "v_add_f32 v20, v20, v21\n" \
  "s_branch .LVA_FAR_%=\n" \
  ".LVA_QH_%=:\n" \
  "s_bitcmp1_b32 s62, 0\n" \
  "s_cbranch_scc1 .LVA_QD_%=\n" \
  ".LVA_QC_%=:\n" \
  "v_mfma_f32_16x16x32_bf16 v[64:67], v[128:131], v[244:247], v[64:67]\n" \
  "v_mfma_f32_16x16x32_bf16 v[68:71], v[132:135], v[244:247], v[68:71]\n" \
  "v_mfma_f32_16x16x32_bf16 v[72:75], v[136:139], v[244:247], v[72:75]\n" \
  "v_mfma_f32_16x16x32_bf16 v[76:79], v[140:143], v[244:247], v[76:79]\n" \
  "v_mfma_f32_16x16x32_bf16 v[20:23], v[192:195], v[250:253], v[64:67]\n" \
  "s_nop 7\n" \
  "v_permlane32_swap_b32 v22, v23\n" \
  "v_add_f32 v20, v22, v23\n" \
  "s_branch .LVA_FAR_%=\n" \
  ".LVA_QD_%=:\n" \
  "v_mfma_f32_16x16x32_bf16 v[64:67], v[128:131], v[244:247], v[64:67]\n" \
  "v_mfma_f32_16x16x32_bf16 v[68:71], v[132:135], v[244:247], v[68:71]\n" \
  "v_mfma_f32_16x16x32_bf16 v[72:75], v[136:139], v[244:247], v[72:75]\n" \
  "v_mfma_f32_16x16x32_bf16 v[76:79], v[140:143], v[244:247], v[76:79]\n" \
  "v_mfma_f32_16x16x32_bf16 v[20:23], v[192:195], v[250:253], v[68:71]\n" \
  "s_not_b64 s[72:73], s[72:73]\n" \
  "s_nop 6\n" \
  "v_permlane32_swap_b32 v20, v21\n" \
  "v_add_f32 v20, v20, v21\n" \
  ".LVA_FAR_%=:\n" \
  "s_waitcnt lgkmcnt(0)\n" \
  "v_alignbit_b32 v58, v59, v58, v6\n" \
  "v_alignbit_b32 v59, v60, v59, v6\n" \
  "v_alignbit_b32 v60, v61, v60, v6\n" \
  "v_alignbit_b32 v61, v62, v61, v6\n" \
  "v_add_u32 v28, 64, v28\n" \
  "s_cmp_ge_u32 s80, 12\n" \
  "s_cbranch_scc1 .LVA_PD_%=\n" \
  "v_mfma_f32_16x16x32_bf16 v[80:83], v[144:147], v[244:247], v[80:83]\n" \
  "v_mfma_f32_16x16x32_bf16 v[196:199], v[128:131], v[58:61], v[196:199]\n" \
  "s_cmp_ge_u32 s80, 11\n" \
  "s_cbranch_scc1 .LVA_PD_%=\n" \
  "v_mfma_f32_16x16x32_bf16 v[84:87], v[148:151], v[244:247], v[84:87]\n" \
  "v_mfma_f32_16x16x32_bf16 v[200:203], v[132:135], v[58:61], v[200:203]\n" \
  "s_cmp_ge_u32 s80, 10\n" \
  "s_cbranch_scc1 .LVA_PD_%=\n" \
  "v_mfma_f32_16x16x32_bf16 v[88:91], v[152:155], v[244:247], v[88:91]\n" \
  "v_mfma_f32_16x16x32_bf16 v[204:207], v[136:139], v[58:61], v[204:207]\n" \
  "s_cmp_ge_u32 s80, 9\n" \
  "s_cbranch_scc1 .LVA_PD_%=\n" \
  "v_mfma_f32_16x16x32_bf16 v[92:95], v[156:159], v[244:247], v[92:95]\n" \
  "v_mfma_f32_16x16x32_bf16 v[208:211], v[140:143], v[58:61], v[208:211]\n" \
  "s_cmp_ge_u32 s80, 8\n" \
  "s_cbranch_scc1 .LVA_PD_%=\n" \
  "v_mfma_f32_16x16x32_bf16 v[96:99], v[160:163], v[244:247], v[96:99]\n" \
  "v_mfma_f32_16x16x32_bf16 v[212:215], v[144:147], v[58:61], v[212:215]\n" \
  "s_cmp_ge_u32 s80, 7\n" \
  "s_cbranch_scc1 .LVA_PD_%=\n" \
  "v_mfma_f32_16x16x32_bf16 v[100:103], v[164:167], v[244:247], v[100:103]\n" \
  "v_mfma_f32_16x16x32_bf16 v[216:219], v[148:151], v[58:61], v[216:219]\n" \
  "s_cmp_ge_u32 s80, 6\n" \
  "s_cbranch_scc1 .LVA_PD_%=\n" \
  "v_mfma_f32_16x16x32_bf16 v[104:107], v[168:171], v[244:247], v[104:107]\n" \
  "v_mfma_f32_16x16x32_bf16 v[220:223], v[152:155], v[58:61], v[220:223]\n" \
  "s_cmp_ge_u32 s80, 5\n" \
  "s_cbranch_scc1 .LVA_PD_%=\n" \
  "v_mfma_f32_16x16x32_bf16 v[108:111], v[172:175], v[244:247], v[108:111]\n" \
  "v_mfma_f32_16x16x32_bf16 v[224:227], v[156:159], v[58:61], v[224:227]\n" \
  "s_cmp_ge_u32 s80, 4\n" \
  "s_cbranch_scc1 .LVA_PD_%=\n" \
  "v_mfma_f32_16x16x32_bf16 v[112:115], v[176:179], v[244:247], v[112:115]\n" \
  "v_mfma_f32_16x16x32_bf16 v[228:231], v[160:163], v[58:61], v[228:231]\n" \
  "s_cmp_ge_u32 s80, 3\n" \
  "s_cbranch_scc1 .LVA_PD_%=\n" \
  "v_mfma_f32_16x16x32_bf16 v[116:119], v[180:183], v[244:247], v[116:119]\n" \
  "v_mfma_f32_16x16x32_bf16 v[232:235], v[164:167], v[58:61], v[232:235]\n" \
  "s_cmp_ge_u32 s80, 2\n" \
  "s_cbranch_scc1 .LVA_PD_%=\n" \
  "v_mfma_f32_16x16x32_bf16 v[120:123], v[184:187], v[244:247], v[120:123]\n" \
  "v_mfma_f32_16x16x32_bf16 v[236:239], v[168:171], v[58:61], v[236:239]\n" \
  "s_cmp_ge_u32 s80, 1\n" \
  "s_cbranch_scc1 .LVA_PD_%=\n" \
  "v_mfma_f32_16x16x32_bf16 v[124:127], v[188:191], v[244:247], v[124:127]\n" \
  "v_mfma_f32_16x16x32_bf16 v[240:243], v[172:175], v[58:61], v[240:243]\n" \
  ".LVA_PD_%=:\n" \
  "s_cmp_lg_u32 s63, 0\n" \
  "s_cbranch_scc1 .LVA_NOF_%=\n" \
  "v_lshlrev_b32 v56, 6, v7\n" \
  "s_waitcnt vmcnt(0)\n" \
  "s_mov_b64 exec, 15\n" \
  "global_store_dword v56, v5, s[58:59] sc1\n" \
  "s_mov_b64 exec, -1\n" \
  "s_add_u32 s58, s58, 4096\n" \
  "s_addc_u32 s59, s59, 0\n" \
  ".LVA_NOF_%=:\n" \
  "s_add_u32 s74, s62, 12\n" \
  "s_lshl_b32 s76, s63, 1\n" \
  "s_and_b32 s76, s76, 12\n" \
  "s_xor_b32 s74, s74, s76\n" \
  "s_and_b32 s79, s74, 15\n" \
  "s_sub_u32 s74, s62, 4\n" \
  "s_cmp_lt_u32 s74, 48\n" \
  "s_cselect_b32 s79, s79, 1\n" \
  "s_cmp_lg_u32 s79, 0\n" \
  "s_cbranch_scc1 .LVA_NOS_%=\n" \
  "ds_read_u16 v128, v35 offset:0\n" \
  "ds_read_u16 v129, v35 offset:784\n" \
  "ds_read_u16 v130, v35 offset:1568\n" \
  "ds_read_u16 v131, v35 offset:2352\n" \
  "ds_read_u16 v132, v35 offset:3136\n" \
  "ds_read_u16 v133, v35 offset:3920\n" \
  "ds_read_u16 v134, v35 offset:4704\n" \
  "ds_read_u16 v135, v35 offset:5488\n" \
  "v_lshlrev_b32 v136, 16, v44\n" \
  "v_and_b32 v137, 0xffff0000, v44\n" \
  "v_lshlrev_b32 v138, 16, v45\n" \
  "v_and_b32 v139, 0xffff0000, v45\n" \
  "v_lshlrev_b32 v140, 16, v46\n" \
  "v_and_b32 v141, 0xffff0000, v46\n" \
  "v_lshlrev_b32 v142, 16, v47\n" \
  "v_and_b32 v143, 0xffff0000, v47\n" \
  "v_mov_b64 v[44:45], v[48:49]\n" \
  "v_mov_b64 v[46:47], v[50:51]\n" \
  "v_mov_b64 v[48:49], v[52:53]\n" \
  "v_mov_b64 v[50:51], v[54:55]\n" \
  "s_waitcnt lgkmcnt(0)\n" \
  "v_lshlrev_b32 v128, 16, v128\n" \
  "v_lshlrev_b32 v129, 16, v129\n" \
  "v_lshlrev_b32 v130, 16, v130\n" \
  "v_lshlrev_b32 v131, 16, v131\n" \
  "v_lshlrev_b32 v132, 16, v132\n" \
  "v_lshlrev_b32 v133, 16, v133\n" \
  "v_lshlrev_b32 v134, 16, v134\n" \
  "v_lshlrev_b32 v135, 16, v135\n" \
  "v_pk_add_f32 v[128:129], v[128:129], v[136:137]\n" \
  "v_pk_add_f32 v[130:131], v[130:131], v[138:139]\n" \
  "v_pk_add_f32 v[132:133], v[132:133], v[140:141]\n" \
  "v_pk_add_f32 v[134:135], v[134:135], v[142:143]\n" \
  "v_cvt_pk_bf16_f32 v56, v128, v129\n" \
  "v_cvt_pk_bf16_f32 v57, v130, v131\n" \
  "v_cvt_pk_bf16_f32 v58, v132, v133\n" \
  "v_cvt_pk_bf16_f32 v59, v134, v135\n" \
  "buffer_store_dwordx4 v[56:59], v36, s[52:55], s68 offen sc1\n" \
  "s_add_u32 s68, s68, 0x80000\n" \
  "s_mov_b32 s0, 0xbe801d62\n" \
  ".set .Lpad0xb2, . - 4\n" \
  ".LVA_NOS_%=:\n" \
  "s_and_b32 s74, s62, 3\n" \
  "s_cmp_lt_u32 s62, 48\n" \
  "s_cselect_b32 s74, s74, 0\n" \
  "s_cmp_lg_u32 s74, 3\n" \
  "s_cbranch_scc1 .LVA_FIN_%=\n" \
  "ds_read2_b32 v[58:59], v28 offset0:0 offset1:1\n" \
  "ds_read2_b32 v[60:61], v28 offset0:2 offset1:3\n" \
  "ds_read_b32 v62, v28 offset:16\n" \
  "ds_read_u16 v56, v33 offset:0\n" \
  "ds_read_u16 v57, v33 offset:64\n" \
  "v_add_u32 v33, 256, v33\n" \
  "s_mov_b32 s0, 0xbe801d62\n" \
  ".set .Lpad0xb3, . - 4\n" \
  "s_waitcnt lgkmcnt(2)\n" \
  "v_alignbit_b32 v58, v59, v58, v6\n" \
  "v_alignbit_b32 v59, v60, v59, v6\n" \
  "v_alignbit_b32 v60, v61, v60, v6\n" \
  "v_alignbit_b32 v61, v62, v61, v6\n" \
  "s_nop 1\n" \
  "v_mfma_f32_16x16x32_bf16 v[196:199], v[24:27], v[58:61], v[196:199]\n" \
  "s_nop 7\n" \
  "v_permlane32_swap_b32 v196, v197\n" \
  "v_permlane32_swap_b32 v198, v199\n" \
  "v_add_f32 v196, v196, v197\n" \
  "v_add_f32 v198, v198, v199\n" \
  "s_waitcnt lgkmcnt(0)\n" \
  "v_lshlrev_b32 v56, 16, v56\n" \
  "v_lshlrev_b32 v57, 16, v57\n" \
  "v_mul_f32 v56, s64, v56\n" \
  "v_mul_f32 v57, s64, v57\n" \
  "v_cvt_pk_bf16_f32 v197, v196, v198\n" \
  "v_cvt_pk_bf16_f32 v57, v56, v57\n" \
  "v_lshlrev_b32 v196, 16, v197\n" \
  "v_and_b32 v197, 0xffff0000, v197\n" \
  "v_lshlrev_b32 v56, 16, v57\n" \
  "v_and_b32 v57, 0xffff0000, v57\n" \
  "v_add_f32 v56, v56, v196\n" \
  "v_add_f32 v57, v57, v197\n" \
  "v_cvt_pk_bf16_f32 v58, v56, v57\n" \
  "ds_write_b16 v34, v58\n" \
  "ds_write_b16_d16_hi v34, v58 offset:64\n" \
  ".LVA_FIN_%=:\n" \
  "s_cmp_lg_u32 s79, 0\n" \
  "s_cbranch_scc1 .LVA_NOG_%=\n" \
  "v_lshlrev_b32 v60, 6, v7\n" \
  "s_waitcnt vmcnt(0)\n" \
  "s_mov_b64 exec, 255\n" \
  "global_store_dword v60, v5, s[60:61] sc1\n" \
  "s_mov_b64 exec, -1\n" \
  "s_add_u32 s60, s60, 0x10000\n" \
  "s_addc_u32 s61, s61, 0\n" \
  ".LVA_NOG_%=:\n" \
  "s_add_u32 s62, s62, 1\n" \
  "s_cmp_eq_u32 s62, 64\n" \
  "s_cbranch_scc1 .LVA_END_%=\n" \
  ".LVA_TAPS_%=:\n" \
  "s_and_b32 s74, s62, 3\n" \
  "s_lshl_b32 s76, s74, 6\n" \
  "v_add_u32 v56, s76, v5\n" \
  "s_cmp_eq_u32 s74, 3\n" \
  "s_cselect_b32 s74, 0, 256\n" \
  "v_add_u32 v57, s74, v56\n" \
  "ds_read_b128 v[128:131], v56 offset:3840\n" \
  "ds_read_b128 v[132:135], v56 offset:3584\n" \
  "ds_read_b128 v[136:139], v56 offset:3328\n" \
  "ds_read_b128 v[140:143], v56 offset:3072\n" \
  "ds_read_b128 v[192:195], v57 offset:3648\n" \
  "s_lshr_b32 s80, s62, 2\n" \
  "s_cmp_ge_u32 s80, 12\n" \
  "s_cbranch_scc1 .LVA_TOP_%=\n" \
  "ds_read_b128 v[144:147], v56 offset:2816\n" \
  "s_cmp_ge_u32 s80, 11\n" \
  "s_cbranch_scc1 .LVA_TOP_%=\n" \
  "ds_read_b128 v[148:151], v56 offset:2560\n" \
  "s_cmp_ge_u32 s80, 10\n" \
  "s_cbranch_scc1 .LVA_TOP_%=\n" \
  "ds_read_b128 v[152:155], v56 offset:2304\n" \
  "s_cmp_ge_u32 s80, 9\n" \
  "s_cbranch_scc1 .LVA_TOP_%=\n" \
  "ds_read_b128 v[156:159], v56 offset:2048\n" \
  "s_cmp_ge_u32 s80, 8\n" \
  "s_cbranch_scc1 .LVA_TOP_%=\n" \
  "ds_read_b128 v[160:163], v56 offset:1792\n" \
  "s_cmp_ge_u32 s80, 7\n" \
  "s_cbranch_scc1 .LVA_TOP_%=\n" \
  "ds_read_b128 v[164:167], v56 offset:1536\n" \
  "s_cmp_ge_u32 s80, 6\n" \
  "s_cbranch_scc1 .LVA_TOP_%=\n" \
  "ds_read_b128 v[168:171], v56 offset:1280\n" \
  "s_cmp_ge_u32 s80, 5\n" \
  "s_cbranch_scc1 .LVA_TOP_%=\n" \
  "ds_read_b128 v[172:175], v56 offset:1024\n" \
  "s_cmp_ge_u32 s80, 4\n" \
  "s_cbranch_scc1 .LVA_TOP_%=\n" \
  "ds_read_b128 v[176:179], v56 offset:768\n" \
  "s_cmp_ge_u32 s80, 3\n" \
  "s_cbranch_scc1 .LVA_TOP_%=\n" \
  "ds_read_b128 v[180:183], v56 offset:512\n" \
  "s_cmp_ge_u32 s80, 2\n" \
  "s_cbranch_scc1 .LVA_TOP_%=\n" \
  "ds_read_b128 v[184:187], v56 offset:256\n" \
  "s_cmp_ge_u32 s80, 1\n" \
  "s_cbranch_scc1 .LVA_TOP_%=\n" \
  "ds_read_b128 v[188:191], v56 offset:0\n" \
  "s_branch .LVA_TOP_%=\n" \
  ".LVA_MOVE_%=:\n" \
  "s_waitcnt lgkmcnt(0)\n" \
  "v_alignbit_b32 v58, v59, v58, v6\n" \
  "v_alignbit_b32 v59, v60, v59, v6\n" \
  "v_alignbit_b32 v60, v61, v60, v6\n" \
  "v_alignbit_b32 v61, v62, v61, v6\n" \
  "v_add_u32 v28, 64, v28\n" \
  "s_cmp_ge_u32 s80, 12\n" \
  "s_cbranch_scc1 .LVA_PD_%=\n" \
  "v_mfma_f32_16x16x32_bf16 v[80:83], v[144:147], v[244:247], v[84:87]\n" \
  "v_mfma_f32_16x16x32_bf16 v[196:199], v[128:131], v[58:61], v[200:203]\n" \
  "s_cmp_ge_u32 s80, 11\n" \
  "s_cbranch_scc1 .LVA_PD_%=\n" \
  "v_mfma_f32_16x16x32_bf16 v[84:87], v[148:151], v[244:247], v[88:91]\n" \
  "v_mfma_f32_16x16x32_bf16 v[200:203], v[132:135], v[58:61], v[204:207]\n" \
  "s_cmp_ge_u32 s80, 10\n" \
  "s_cbranch_scc1 .LVA_PD_%=\n" \
  "v_mfma_f32_16x16x32_bf16 v[88:91], v[152:155], v[244:247], v[92:95]\n" \
  "v_mfma_f32_16x16x32_bf16 v[204:207], v[136:139], v[58:61], v[208:211]\n" \
  "s_cmp_ge_u32 s80, 9\n" \
  "s_cbranch_scc1 .LVA_PD_%=\n" \
  "v_mfma_f32_16x16x32_bf16 v[92:95], v[156:159], v[244:247], v[96:99]\n" \
  "v_mfma_f32_16x16x32_bf16 v[208:211], v[140:143], v[58:61], v[212:215]\n" \
  "s_cmp_ge_u32 s80, 8\n" \
  "s_cbranch_scc1 .LVA_PD_%=\n" \
  "v_mfma_f32_16x16x32_bf16 v[96:99], v[160:163], v[244:247], v[100:103]\n" \
  "v_mfma_f32_16x16x32_bf16 v[212:215], v[144:147], v[58:61], v[216:219]\n" \
  "s_cmp_ge_u32 s80, 7\n" \
  "s_cbranch_scc1 .LVA_PD_%=\n" \
  "v_mfma_f32_16x16x32_bf16 v[100:103], v[164:167], v[244:247], v[104:107]\n" \
  "v_mfma_f32_16x16x32_bf16 v[216:219], v[148:151], v[58:61], v[220:223]\n" \
  "s_cmp_ge_u32 s80, 6\n" \
  "s_cbranch_scc1 .LVA_PD_%=\n" \
  "v_mfma_f32_16x16x32_bf16 v[104:107], v[168:171], v[244:247], v[108:111]\n" \
  "v_mfma_f32_16x16x32_bf16 v[220:223], v[152:155], v[58:61], v[224:227]\n" \
  "s_cmp_ge_u32 s80, 5\n" \
  "s_cbranch_scc1 .LVA_PD_%=\n" \
  "v_mfma_f32_16x16x32_bf16 v[108:111], v[172:175], v[244:247], v[112:115]\n" \
  "v_mfma_f32_16x16x32_bf16 v[224:227], v[156:159], v[58:61], v[228:231]\n" \
  "s_cmp_ge_u32 s80, 4\n" \
  "s_cbranch_scc1 .LVA_PD_%=\n" \
  "v_mfma_f32_16x16x32_bf16 v[112:115], v[176:179], v[244:247], v[116:119]\n" \
  "v_mfma_f32_16x16x32_bf16 v[228:231], v[160:163], v[58:61], v[232:235]\n" \
  "s_cmp_ge_u32 s80, 3\n" \
  "s_cbranch_scc1 .LVA_PD_%=\n" \
  "v_mfma_f32_16x16x32_bf16 v[116:119], v[180:183], v[244:247], v[120:123]\n" \
  "v_mfma_f32_16x16x32_bf16 v[232:235], v[164:167], v[58:61], v[236:239]\n" \
  "s_cmp_ge_u32 s80, 2\n" \
  "s_cbranch_scc1 .LVA_PD_%=\n" \
  "v_mfma_f32_16x16x32_bf16 v[120:123], v[184:187], v[244:247], v[124:127]\n" \
  "v_mfma_f32_16x16x32_bf16 v[236:239], v[168:171], v[58:61], v[240:243]\n" \
  "s_branch .LVA_PD_%=\n" \
  ".LVA_END_%=:\n" \
  "s_endpgm\n"
__device__ __forceinline__ v4u mk_srd(const void* p, const unsigned bytes) {
  const unsigned long long a = (unsigned long long)p;
  return v4u{(unsigned)a, (unsigned)(a >> 32) & 0xffffu, bytes, 0x00020000u};
}
template <bool ATT>
__device__ __forceinline__ void role_chain_t(const MegaArgs& A, char* smem, const int L, const int h, const int rep) {
  __builtin_amdgcn_s_waitcnt(0x0F70);
  MARK();
  u16* swo = reinterpret_cast<u16*>(smem);
  const int w = __builtin_amdgcn_readfirstlane(threadIdx.x >> 6);
  const int wc = (w + ROT) & 7, psw = PSWH;
  char* ws = A.ws;
  unsigned* FL = (unsigned*)(ws + OFF_FL);
  unsigned* TMO = FL + FL_TMO;
#define LANE_IDS()                         \
  const int l = lane_id();                 \
  const int c = l & 15, g = l >> 4;        \
  (void)c; (void)g;
  float* sgm = reinterpret_cast<float*>(smem + C_X);
  float* sgf = sgm + 8;
  unsigned* FQ = FL + FL_FQ + L * 65 * 32 * 32;
  v8u w0[8], w1[8], w2[8];
  PSV;
  {
    LANE_IDS();
    v4u m0r;
    const unsigned sb = lds_base(smem);
    const int q = (l >> 2) & 3, p = l & 3;
    const int f = (q & 1) | ((g & 1) << 1) | ((q >> 1) << 2);
    const unsigned ra = sb + (g >> 1) * 16384 + (4 * (g & 1) + (q & 1) + 8 * (q >> 1)) * 256 + (w ^ f) * 32 + p * 8;
#define WP(k) (WM(k) == 0 ? A.Wv : WM(k) == 1 ? A.Wk : A.Wq)
#define WC(k) (WJ(k) ^ WXC)
#define W_READ(W, X)                                                          \
  {                                                                           \
    const unsigned a_ = (ra ^ (X)) + j * 4096 + i * 32768;                    \
    const v2u x0 = trd(a_), x1 = trd(a_ + 512);                               \
    W[4 * WC(k) + i] = v4u{x0.x, x0.y, x1.x, x1.y};                           \
  }
    v4u u0[16], u1[16], u2[16], zt;
    v2u ze;
    float f2[4] = {};
    unsigned txt = 0u;
    if (CSL && L) gate(now10() + CSL);
    PST(0);
    asm volatile("s_nop 4\n\tglobal_load_dwordx4 %0, %1, %2" : "=v"(m0r) : "v"(((2 * (c >> 2) + (c & 1)) * DM + 64 * w + 16 * g + 8 * (((c >> 1) ^ (c >> 3)) & 1)) * 2), "s"(A.mem0) : "memory");
#pragma unroll
    for (int k = 0; k < 12; ++k) w_issue<true>(WP(k >> 2), WC(k >> 2), k >> 2, k & 3, h, w, l, sb);
    constexpr int PB = ATT ? 100 : 120;
    tour<PB + 2, PB + 12, 4>(w);
#pragma unroll
    for (int k = 0; k < 13; ++k) {
      const int j = k & 3;
      if (!ATT && k == 4) pad<PB + 3>(txt);
      if (k == (ATT ? 4 : 7)) pad<PB + 4>(txt);
      if (k == (ATT ? 6 : 10)) pad<PB + 1, 600>(txt);
      if (k == (ATT ? 10 : 9)) pad<PB + 5>(txt);
      if (k == ZC + 1) wait_vm<10>();
      else if (k < 12 || ZC == 10) wait_vm<8>();
      if (CTH && L) __builtin_amdgcn_s_sleep(CTH);
      RAW_BAR();
      if (k == 0) {
        asm volatile("" : "+v"(m0r));
        *reinterpret_cast<v4u*>(smem + C_SM + w * 1024 + l * 16) = m0r;
      }
      if (k == IPC) txt = pull_text((h & 7) + 8 * w);
      if (k == IPC + 2) asm volatile("" ::"v"(txt));
      if (k == ZC)
        asm volatile("s_nop 4\n\tglobal_load_dwordx4 %0, %2, %3 sc1\n\tglobal_load_dwordx2 %1, %4, %5 sc1"
                     : "=&v"(zt), "=&v"(ze)
                     : "v"(l * 16), "s"(ws + OFF_TAG + (blockIdx.x & 7) * 1024), "v"(0), "s"(ws + OFF_TAG + 8192)
                     : "memory");
#pragma unroll
      for (int i = 0; i < 4; ++i) {
        if (k < 9) w_issue<true>(WP(k + 3), WC(k + 3), (k + 3) & 3, i, h, w, l, sb);
        else {
          const int jo = k - 9, fd = (i & 1) | ((jo & 1) << 1) | ((w & 1) << 2);
          glds16((const char*)A.Wo + (size_t)(h * DH + 64 * (jo >> 1) + 8 * w + 4 * (jo & 1)) * (DM * 2), (unsigned)(i * 1024 + ((l ^ (fd << 1)) << 4)),
                 sb + w * 16384 + jo * 4096 + i * 1024);
        }
        if (k < 12 && WM(k) == (ATT ? 0 : 2)) { W_READ(u1, 0); W_READ(u2, 128); }
        if (k < 12 && WM(k) == 1) W_READ(u0, 0);
        if (ATT && k == 1 && i == 3) pad<PB + 3>(txt);
        if (ATT && k > 0 && WM(k - 1) == 0) {
#pragma unroll
          for (int e = 0; e < 4; ++e) {
            f2[e & 1] = dot2sq(u1[4 * WC(k - 1) + i][e], f2[e & 1]);
            f2[2 + (e & 1)] = dot2sq(u2[4 * WC(k - 1) + i][e], f2[2 + (e & 1)]);
          }
          asm volatile("" : "+v"(f2[0]), "+v"(f2[1]), "+v"(f2[2]), "+v"(f2[3]));
        }
      }
      if (k == (WORD ? 10 : 4)) {
        const float f = wave_sum_dpp((f2[0] + f2[1]) + (f2[2] + f2[3]));
        if (l == 0) { sgf[w] = f; sgm[16] = 0.f; sgm[32 + w] = 1.f; }
      }
      if (k == (WORD ? 11 : ZC + 2)) {
        asm volatile("" : "+v"(zt), "+v"(ze));
        if (w == 0) {
          if (__builtin_expect(!zero_seen(zt, ze), 0)) zero_wait(ws);
          const float fs = (sgf[0] + sgf[1]) + (sgf[2] + sgf[3]);
          __builtin_amdgcn_raw_buffer_store_b32(__float_as_uint(fs) | 1u, MKRS(FL + FL_G, L ? 0 : 4 * 512), l * 512 + h * 4, 0, AUX_SC1);
        }
      }
      if (k == 1) PST(1);
      if (k == 3) PST(2);
      if (k == 5) PST(3);
      if (k == 7) PST(4);
      if (k == 9) PST(5);
      if (k == 11) PST(6);
    }
#undef W_READ
#undef WP
#undef WC
#pragma unroll
    for (int t = 0; t < 8; ++t) {
      const v8u a0 = __builtin_shufflevector(u0[2 * t], u0[2 * t + 1], 0, 1, 2, 3, 4, 5, 6, 7), a1 = __builtin_shufflevector(u1[2 * t], u1[2 * t + 1], 0, 1, 2, 3, 4, 5, 6, 7);
      w0[t] = ATT ? a0 : a1;
      w1[t] = ATT ? a1 : a0;
      w2[t] = __builtin_shufflevector(u2[2 * t], u2[2 * t + 1], 0, 1, 2, 3, 4, 5, 6, 7);
    }
    const unsigned rb = sb + (2 * (g >> 1) + (q >> 1)) * 16384 + (4 * (g & 1) + (q & 1)) * 1024 + p * 8;
#pragma unroll
    for (int hf = 0; hf < 2; ++hf) {
      if (hf == 0) WAIT_VM(8); else WAIT_VM(0);
      if (hf == 1 && ATT) pad<PB + 6>();
      RAW_BAR();
      if (hf == 1 && w == 0) __builtin_amdgcn_raw_buffer_store_b32((now10() + CRD) | 1u, MKRS(FL + FL_S0 + L * 512, 4 * 512), l * 512 + (h >> 3) * 128 + 32 + (h & 7) * 4, 0, AUX_SC1);
      v4u fr[8];
#pragma unroll
      for (int i = 0; i < 8; ++i) {
        const unsigned a_ = rb + hf * 8192 + (i & 1) * 65536 + ((4 * wc + ((i >> 1) ^ psw)) ^ f) * 32;
        const v2u r0 = trd(a_), r1 = trd(a_ + 2048);
        fr[i] = v4u{r0.x, r0.y, r1.x, r1.y};
      }
#pragma unroll
      for (int i = 0; i < 8; ++i) asm volatile("" : "+v"(fr[i]));
      if (hf == 0 && !ATT) pad<PB + 6>();
      RAW_BAR();
#pragma unroll
      for (int i = 0; i < 8; ++i) *reinterpret_cast<v4u*>(swo + w * 8192 + hf * 4096 + i * 512 + l * 8) = fr[i];
    }
  }
  PST(7);
  WSTAMP2();
  const unsigned sb = lds_base(smem);
  const char* gK = ws + OFF_QK + L * SZ_QK + ((size_t)h * 16 + 2 * w) * 1024;
  const char* gV = ws + OFF_VF + L * SZ_O + ((size_t)h * 8 + 2 * w - 8) * 1024;
  PST(8);
  pad<ATT ? 108 : 128>();
  v8u pv;
  {
    LANE_IDS();
    const unsigned voff = g * 256 + c * 8, wp = sb + w * 16384 + l * 16;
    pv[0] = (((c >> 1) ^ (c >> 3)) & 1) ? 0xEEEE : 0x4444;
    pv[1] = sb + C_SM + l * 16;
    pv[2] = ATT ? sb + ((c & 2) ? C_SK : C_SQ) + ((2 * (c >> 2) + (c & 1)) * QS + 8 * g) * 2 : voff;
    pv[3] = ATT ? l * 8 : wp;
    pv[4] = sb + ((c & 2) ? C_SQ : C_SO) + w * 256 + ((c & 13) + ((g ^ (c >> 2)) & 2)) * 16 + 8 * ((g ^ (c >> 1)) & 1);
    pv[5] = voff;
    pv[6] = wp;
    pv[7] = sb + C_SO + (l >> 5) * 1024 + w * 256 + (l & 31) * 8;
  }
  PST(10);
  PSD();
  SST(8);
  const v4u rsA = mk_srd(ws + OFF_ACC + L * SZ_ACC, SZ_ACC);
  const v4u rsF = mk_srd(FQ, 64 * 32 * 32 * 4);
  const v4u rsMK = mk_srd(ws + OFF_MEMK + L * SZ_MEM, SZ_MEM);
  const v4u rsMV = mk_srd(ATT ? ws + OFF_MEMV + L * SZ_MEM : (char*)(FL + FL_G + REP * 128), SZ_MEM);
  const int wr = (w + RR) & 7;
  const char* pAB = ws + OFF_ACC + L * SZ_ACC + (wc - wr) * 4096;
  const char* pGV = ATT ? (const char*)(size_t)(CDS + (XG(h) >> 1) * CDL) : gV;
  unsigned sc[8];
  sc[0] = ATT ? L * 1024 + XG(h) * 256 : h * 128;
  sc[1] = !ATT ? (h * 4 + w - 4) * 512 : (h & 7) == 1 ? L * 1024 + (h >> 3) * 256 + 128 : 1032 - L * 1024 + h * 4096;
  sc[2] = h * 2048 + w * 512;
  sc[3] = NSEG - 1;
  sc[5] = w;
  sc[6] = sb;
  const char* dbg = ws;
  const unsigned tg = (now10() / GR) * GR;
  const v4u rsT = mk_srd(FL + FL_TK, 2048);
#define CHAIN_OPS                                                                                                          \
  ::"{v[64:71]}"(w0[0]), "{v[72:79]}"(w0[1]), "{v[80:87]}"(w0[2]), "{v[88:95]}"(w0[3]), "{v[96:103]}"(w0[4]),              \
      "{v[104:111]}"(w0[5]), "{v[112:119]}"(w0[6]), "{v[120:127]}"(w0[7]), "{v[128:135]}"(w1[0]), "{v[136:143]}"(w1[1]),    \
      "{v[144:151]}"(w1[2]), "{v[152:159]}"(w1[3]), "{v[160:167]}"(w1[4]), "{v[168:175]}"(w1[5]), "{v[176:183]}"(w1[6]),    \
      "{v[184:191]}"(w1[7]), "{v[192:199]}"(w2[0]), "{v[200:207]}"(w2[1]), "{v[208:215]}"(w2[2]), "{v[216:223]}"(w2[3]),    \
      "{v[224:231]}"(w2[4]), "{v[232:239]}"(w2[5]), "{v[240:247]}"(w2[6]), "{v[248:255]}"(w2[7]), "{v[0:7]}"(pv),          \
      "{s[40:43]}"(rsA), "{s[44:47]}"(rsF), "{s[48:51]}"(rsMK), "{s[52:55]}"(rsMV), "{s[56:57]}"(pAB), "{s60}"(wr * 4096), "{s61}"(h * 1024 - wr * 4096), \
      "{s[62:63]}"(gK), "{s[64:65]}"(pGV), "{s68}"(sc[0]), "{s69}"(sc[1]), "{s70}"(sc[2]),    \
      "{s71}"(sc[3]), "{s73}"(sc[5]), "{s74}"(sc[6]), "{s[36:37]}"(dbg),                   \
      "{s34}"(tg), "{s35}"(psw * 1024), [gp] "n"(GP), [gr] "n"(GR), [gm] "n"(GM), \
      "{s[16:19]}"(rsT), "{s20}"(0), "{s22}"(sb + C_X + 128 + 8 * (w & 3)), "{s24}"(L * 1024), [cd] "n"(CD - CDS), \
      "{s25}"(APA - (APN + 1) * APP + L * (APB - APA + (2 * APN + 1) * APP)), [ka] "n"(APL), [an] "n"(APN), [d0] "n"(16 * CD0), [d1] "n"(CD1), [d2] "n"(CD2), [d3] "n"(CD1 + 63 * APP), [d4] "n"(CD4), [so] "n"((FL_S0 - FL_G) * 4), [sg] "n"(SLP_2), [g1] "n"(63 * APP + CG1 + CD2), [sf] "n"(SLP_1), [rq] "n"(RQM), [pp] "n"(APP) \
      : "memory", "vcc", "scc", "s99", "v63"
  if (ATT) asm volatile("s_mov_b64 exec, -1\ns_nop 4\n" CHAIN_ASM_ATT ".Lcode_end:\n" CHAIN_OPS);
  else asm volatile("s_mov_b64 exec, -1\ns_nop 4\n" CHAIN_ASM_VO CHAIN_OPS);
#undef CHAIN_OPS
#undef LANE_IDS
}

__device__ __forceinline__ void role_chain(const MegaArgs& A, char* smem, const int L, const int h, const int rep) {
  if (__builtin_amdgcn_readfirstlane(threadIdx.x >> 6) < 4) role_chain_t<true>(A, smem, L, h, rep);
  else role_chain_t<false>(A, smem, L, h, rep);
}

constexpr int AW_TK = 0, AW_SO = 114688, AW_M = AW_SO + 16384, AW_P = 24576, AW_MK = AW_P + 2048, AW_CNT = AW_SO + AW_MK + 4096;
__device__ __forceinline__ unsigned umin3(unsigned a, unsigned b, unsigned c) {
  return __builtin_elementwise_min(__builtin_elementwise_min(a, b), c);
}
__device__ __forceinline__ bool all_set(const unsigned r) {
  unsigned long long m;
  asm volatile("v_cmp_eq_u32 %0, 0, %1" : "=s"(m) : "v"(r) : "memory");
  return m == 0ull;
}
__device__ __forceinline__ unsigned long long zero_mask(const unsigned r) {
  unsigned long long m;
  asm volatile("v_cmp_eq_u32 %0, 0, %1" : "=s"(m) : "v"(r) : "memory");
  return m;
}
__device__ __forceinline__ int job_seg(const int n) {
  const int m = n - AWD;
  const int r = (m & 1) ? 64 + (m >> 1) : AWD + (m >> 1);
  return __builtin_amdgcn_readfirstlane(n > 127 ? 128 : (n < AWD || n >= 128 - AWD) ? n : r);
}
template <bool AT>
__device__ __forceinline__ void role_aw_t(const MegaArgs& A, char* smem, const int mode, const int a) {
  __builtin_amdgcn_s_waitcnt(0x0F70);
  MARK();
  const int w = __builtin_amdgcn_readfirstlane(threadIdx.x >> 6);
  const int l = threadIdx.x & 63, c = l & 15, g = l >> 4;
  const int qt = w & 1, hh = (w >> 1) & 1, h = 2 * a + hh;
  char* ws = A.ws;
  unsigned* FL = (unsigned*)(ws + OFF_FL);
  const unsigned sb = lds_base(smem);
  v4u wo[4][8];
  PSV;
  PST(0);
  const unsigned* FQ = FL + FL_FQ + 1024 + h * 32 + 16;
  const v4u rsW = mk_srd(ws + OFF_PW, SZ_PW);
  const v4u rsF = mk_srd(FQ, (129 * 32 - h) * 128 - 64);
  const char* gQ = ws + OFF_QK + ((size_t)h * 16 + 8 * qt) * 1024;
  const char* gV = ws + OFF_VF + ((size_t)h * 8 + 4 * qt) * 1024;
  const char* gM = ws + (qt ? OFF_MEMV : OFF_MEMK) + h * 2048;
  const unsigned mo = sb + AW_M + hh * 4096;
  const unsigned tq0 = __builtin_amdgcn_readfirstlane(sb + AW_TK + (hh * 24 + 8 * qt) * 1024);
  const unsigned tv0 = __builtin_amdgcn_readfirstlane(sb + AW_TK + (hh * 24 + 16 + 4 * qt) * 1024);
  const int gs = job_seg(mode);
  bool got = false;
  {
    const int q = (l >> 2) & 3, p = l & 3, f = q | ((g & 1) << 2);
    const unsigned ra = sb + (4 * g + q) * 256 + (p & 1) * 16;
    unsigned n0 = 0u;
#pragma unroll
    for (int i = 0; i < 8; ++i) wo_issue(A.Wo, a, 2 + (i >> 2), w, i & 3, sb);
#pragma unroll
    for (int ks = 0; ks < 8; ++ks) {
      if (ks >= 2 && ks <= 4) WAIT_VM(12);
      if (ks == 5) wait_vm<8 + (!AT && AWK < 5)>();
      if (ks == 6) wait_vm<4 + (!AT && AWK < 6)>();
      if (ks == 7) WAIT_VM(0);
      if (ks) RAW_BAR();
      if (!AT && ks == AWK)
        asm volatile("s_nop 4\n\tbuffer_load_dword %0, %1, %2, %3 offen sc1" : "=v"(n0) : "v"(0), "s"(rsF), "s"(gs * 4096) : "memory");
      if (ks == 5 && threadIdx.x < 4) ((lu32*)(unsigned long)(sb + AW_CNT))[threadIdx.x] = 0u;
      if (!AT && ks == 7 && __builtin_expect(all_set(n0), 1)) {
#pragma unroll
        for (int i = 0; i < 8; ++i) glds16c(gQ + (size_t)gs * (NH * 16 * 1024) + i * 1024, l * 16, tq0 + i * 1024);
#pragma unroll
        for (int i = 0; i < 4; ++i) glds16c(gV + (size_t)gs * (NH * 8 * 1024) + i * 1024, l * 16, tv0 + i * 1024);
        got = true;
      }
#pragma unroll
      for (int ct = 0; ct < 4; ++ct) {
        if (ks < 4) wo_issue(A.Wo, a, ks + 4, w, ct, sb);
        const int cp = 2 * w + (ct >> 1);
        const unsigned a_ = ra + (ks % 5) * 32768 + (cp >> 2) * 8192 + ((2 * (cp & 3) + (p >> 1)) ^ f) * 32 + (ct & 1) * 8;
        const v2u lo = trd(a_), hi = trd(a_ + 4096);
        wo[ct][ks] = v4u{lo.x, hi.x, lo.y, hi.y};
      }
#pragma unroll
      for (int ct = 0; ct < 4; ++ct) asm volatile("" : "+v"(wo[ct][ks]));
      if (ks == 1) PST(1); if (ks == 3) PST(2); if (ks == 5) PST(3); if (ks == 7) PST(4);
    }
    RAW_BAR();
  }
  if (got) PST(11);
  PSD();
  if (AT) tour<220, 232, 4>(w);
  v8u pv, mk, wr[16];
  {
    const unsigned so = sb + AW_SO + l * 16, ta = sb + AW_TK + hh * 24576 + l * 16;
    const unsigned ix = (c & 2) ? 0xEEEE : 0x4444;
    pv = v8u{AT ? ix : l * 16u, so, (unsigned)(HK3 ? (l >> 3) * 2048 + (l & 7) * 16 : (c >> 1) * 2048 + (c & 1) * 64 + g * 16), AT ? ta : mo + qt * 2048 + l * 16, AT ? ta + qt * 4096 : ix,
             mo + (g * 8 + 2 * (c >> 2) + (c & 1)) * 16, sb + AW_M + 2048 + (g >> 1) * 512 + (c & 3) * 128 + (8 * (g & 1) + (c >> 2)) * 8,
             so + (hh * 2 + qt) * 4096};
#pragma unroll
    for (int i = 0; i < 8; ++i) mk[i] = 16 * (i >> 2) + 4 * g + (i & 3) > 16 * qt + c ? __float_as_uint(-3e38f) : 0u;
#pragma unroll
    for (int i = 0; i < 16; ++i) wr[i] = __builtin_shufflevector(wo[i >> 2][2 * (i & 3)], wo[i >> 2][2 * (i & 3) + 1], 0, 1, 2, 3, 4, 5, 6, 7);
  }
  const char* dbg = ws;
#define AW_OPS                                                                                                               \
  ::"{v[128:135]}"(wr[0]), "{v[136:143]}"(wr[1]), "{v[144:151]}"(wr[2]), "{v[152:159]}"(wr[3]), "{v[160:167]}"(wr[4]),        \
      "{v[168:175]}"(wr[5]), "{v[176:183]}"(wr[6]), "{v[184:191]}"(wr[7]), "{v[192:199]}"(wr[8]), "{v[200:207]}"(wr[9]),      \
      "{v[208:215]}"(wr[10]), "{v[216:223]}"(wr[11]), "{v[224:231]}"(wr[12]), "{v[232:239]}"(wr[13]), "{v[240:247]}"(wr[14]), \
      "{v[248:255]}"(wr[15]), "{v[0:7]}"(pv), "{v[8:15]}"(mk), "{s[40:43]}"(rsW), "{s[44:47]}"(rsF), "{s[48:49]}"(gQ),        \
      "{s[50:51]}"(gV), "{s[52:53]}"(gM), "{s[54:55]}"(FL + FL_FW + (a >> 2) * 32 + (a & 3)), "{s56}"(mode), "{s57}"(gs),      \
      "{s60}"(HK2 ? (a >> 1) * 32768 + w * 256 + (a & 1) * 128 : a * 32768 + w * 256), "{s[92:95]}"(mk_srd(ws, 0)), "{s61}"(AT ? AW_MK + qt * 2048 : tq0), "{s64}"(AT ? 0 : sb + AW_CNT + 8), "{s65}"(sb + AW_CNT + hh * 4), "{s62}"(tv0), "{s63}"(AT ? mo + 2048 - (sb + AW_SO) / 2 : mo + qt * 2048), "{s[84:87]}"(mk_srd(FL + FL_TK + (a >> 2) * 64 + 32, 1028)), "{s[88:89]}"(ws + WS_TOTAL + (28 << 20) + 1024 + qt * 4),            \
      "{s66}"(AT ? AW_P + qt * 1024 + hh * 4 : HK2 ? (a >> 1) * 32768 + (w - 4) * 256 + (a & 1) * 128 : a * 32768 + (w - 4) * 8192), "{s[82:83]}"(w == 4 ? 15ull : 0ull), "{s67}"(__builtin_amdgcn_readfirstlane((int)got)), "{s80}"(0x3e0293ee), "{s[36:37]}"(dbg),        \
      [wd] "n"(AWD), [w2] "n"(128 - 2 * AWD), [w3] "n"(131 - AWD), [dt] "n"(AWDT), [du] "n"(AWDU), [pp] "n"(APP), [mo] "n"(AWMO - 63 * APP), [lm] "n"(63 * APP - AWLM), [ld] "n"(AWLD), [sm] "n"(SLP_M), [sa] "n"(SLP_A) \
      : "memory", "vcc", "scc", "s81", "v127"
  if (AT) asm volatile("s_mov_b64 exec, -1\ns_nop 4\n" AW_ASM_0 AW_OPS);
  else asm volatile("s_mov_b64 exec, -1\ns_nop 4\n" AW_ASM_1 AW_OPS);
#undef AW_OPS
}
__device__ __forceinline__ void role_aw(const MegaArgs& A, char* smem, const int L, const int a) {
  if (__builtin_amdgcn_readfirstlane(threadIdx.x >> 6) < 4) role_aw_t<true>(A, smem, L, a);
  else role_aw_t<false>(A, smem, L, a);
}

__device__ __forceinline__ void role_red(const MegaArgs& A, char* smem, const int idx) {
  const int w = __builtin_amdgcn_readfirstlane(threadIdx.x >> 6);
  const int l = threadIdx.x & 63;
  const int L = w >> 2, gr = (w >> 1) & 1, r = idx * 2 + (w & 1);
  char* ws = A.ws;
  unsigned* FL = (unsigned*)(ws + OFF_FL);
  unsigned* TMO = FL + FL_TMO;
  const __amdgpu_buffer_rsrc_t rsW = MKRS(ws + OFF_PW, SZ_PW);
  const __amdgpu_buffer_rsrc_t rsX = MKRS(ws + OFF_XA, OFF_X3 - OFF_XA + SZ_X);
  volatile lu32* sm32 = (lu32*)(unsigned long)lds_base(smem);
  volatile lu32* sw = sm32 + (w >> 1) * 32;
  if (threadIdx.x < 128) sm32[threadIdx.x] = 0u;
  __syncthreads();
  unsigned tb = now10() + (L * (APN + 2) + 1 + gr) * GPER;
  const int rn1 = idx < 8 ? 96 + idx : 224 + idx;
  (void)rn1;
  if (L) {
    if (RSL) gate(now10() + RSL);
    const __amdgpu_buffer_rsrc_t rz = MKRS(ws + OFF_X2, SZ_X);
    for (int s = gr; s < NSEG; s += 2) ST128((v4u{0u, 0u, 0u, 0u}), rz, l * 16, (s * SEG + r) * DM * 2);
    DRAIN();
    if (l < 4) set_flag(FL + FL_FZ + (2 * l + (idx >> 3)) * 32 + (idx & 7) * 4 + (w & 3));
    if (l == 0) __hip_atomic_fetch_add((lu32*)(unsigned long)(lds_base(smem) + 480), 1u, __ATOMIC_RELAXED, __HIP_MEMORY_SCOPE_WORKGROUP);
  }
  for (unsigned spins = 0; sm32[120] < 4u && spins < 64u * SPIN_CAP; ++spins) __builtin_amdgcn_s_sleep(2);
  const int c = l & 15, g = l >> 4;
  const int pv = g * 32768 + (c >> 2) * 128 + (c & 3) * 16, xv = (16 * (g >> 1) + c) * 512 + (g & 1) * 8, ov = (g >> 1) * 256 + c * 16 + (g & 1) * 8;
  v4u sel0, sel1;
#pragma unroll
  for (int e = 0; e < 4; ++e) {
    const unsigned one = ((c & 7) >> 1) == e ? 0x3f80u << (16 * (c & 1)) : 0u;
    sel0[e] = c < 8 ? one : 0u;
    sel1[e] = c < 8 ? 0u : one;
  }
  unsigned long long en = __hip_atomic_load((gu64*)(ws + OFF_TAG + 8192), RLX_AGENT);
  asm volatile("" : "+v"(en));
  int tries = SPIN_CAP;
  tour<232, 234, 2>(w);
  for (int s = gr; s < NSEG; s += 2) {
    const int gs = 64 * L + s;
    const unsigned* fw = FL + FL_FW + (gs * 4 + REP) * 4 * 32;
    const unsigned due = grp_wait<SLP_R, 4>((w & 1) == 0, sw, s + 1, fw, 16, TMO, tb);
    tb = due + (gs >= 125 - AWD || gs < RQ0 ? GATE_RT : 2 * GPER - (L ? GMS : gs == 61 ? GMR + AWLM : GMR));
    {
      const unsigned t = now10(), e = (int)(due - t) > 0 ? due : t;
      __builtin_amdgcn_raw_buffer_store_b32((e + RDT) | 1u, MKRS(FL + FL_TW, r == 2 * RTW && !L ? 2048 : 0), l * 512, s * 4, AUX_SC1);
    }
#define RTS(k, t) TSR(L ? A.rn : rn1, k, s, (w & 6) * 64 + (t))
    pad<232>();
    RST(0);
    RTS(0, 0);
    RTS(4, 64);
    const int xo_ = L * (int)(OFF_X3 - OFF_XA) + s * 32768 + r * 16;
    const v2u xa = LD64(rsX, xv, xo_), xb = LD64(rsX, xv + 16384, xo_);
#pragma unroll 1
    for (int i = 0; i < 256; ++i) {
      const int d = (int)(due - now10());
      if (d <= 0) break;
      if (d > 8) __builtin_amdgcn_s_sleep(1);
    }
    if (L) {
      const unsigned g0 = sm32[124];
#pragma unroll 1
      for (int i = 0; i < RGW && g0 < NSEG && sm32[124] == g0; ++i) __builtin_amdgcn_s_sleep(1);
    }
    RST(1);
    f32x4 a0, a1;
    for (;; --tries) {
      v4u p[16];
#pragma unroll
      for (int k = 0; k < 16; ++k) {
        p[k] = LD128(rsW, pv + ((k & 1) + (k < 8 ? 2 : 0)) * 512, (gs * 16 + 2 * (k & 6)) * 32768 + (r >> 1) * 2048 + (r & 1) * 64);
        if ((k & 7) == 7) __builtin_amdgcn_sched_barrier(0);
      }
      if ((w & 5) == 1 && l == 0) __hip_atomic_fetch_max((lu32*)(unsigned long)(lds_base(smem) + 496), s + 1u, __ATOMIC_RELAXED, __HIP_MEMORY_SCOPE_WORKGROUP);
      __builtin_amdgcn_sched_barrier(0);
      a0 = f32x4{};
      a1 = f32x4{};
#pragma unroll
      for (int k = 0; k < 8; ++k) a1 = MFMA16(BF8((k & 1) ? sel1 : sel0), BF8(p[k]), a1);
#pragma unroll
      for (int k = 8; k < 16; ++k) a0 = MFMA16(BF8((k & 1) ? sel1 : sel0), BF8(p[k]), a0);
      if (!__any(__builtin_isunordered(a0[0], a1[0]) | __builtin_isunordered(a0[1], a1[1]) | __builtin_isunordered(a0[2], a1[2]) |
                 __builtin_isunordered(a0[3], a1[3])) || tries <= 0) break;
      asm volatile("s_sleep %0" ::"n"(SLP_P) : "memory");
    }
    pad<233>();
    RST(2);
    const unsigned nz = L ? 0u : 1u;
    const v4u xr = {xa.x, xa.y, xb.x, xb.y}, ar = {pk2(a0[0], a0[1]), pk2(a0[2], a0[3]), pk2(a1[0], a1[1]), pk2(a1[2], a1[3])};
    v4u xo;
#pragma unroll
    for (int e = 0; e < 4; ++e)
      xo[e] = __builtin_elementwise_max(pk2(lo16(xr[e]) + lo16(ar[e]), hi16(xr[e]) + hi16(ar[e])), nz);
    const __amdgpu_buffer_rsrc_t rsO = MKRS(L ? (char*)A.out : ws + OFF_X2, SZ_X);
    ST64((v2u{xo.x, xo.y}), rsO, ov, (s * SEG + r) * DM * 2);
    ST64((v2u{xo.z, xo.w}), rsO, ov + 512, (s * SEG + r) * DM * 2);
    RTS(1, 0);
    RST(3);
    RTS(5, 64);
    if (L == 0) {
      float sm = 0.f, sq = 0.f;
#pragma unroll
      for (int e = 0; e < 4; ++e) {
        const unsigned x = xo[e];
        sm = __builtin_amdgcn_fdot2_f32_bf16(__builtin_bit_cast(bf16x2_t, x), __builtin_bit_cast(bf16x2_t, 0x3f803f80u), sm, false);
        sq = dot2sq(x, sq);
      }
      float t = swap_add32(sm, sq);
      t += dppr<0x121>(t);
      t += dppr<0x122>(t);
      t += dppr<0x124>(t);
      t += dppr<0x128>(t);
      t = xsum16(t) * (1.0f / DM);
      const float ms = __builtin_bit_cast(float, __builtin_amdgcn_readlane(__builtin_bit_cast(int, t), 32));
      const float var = fmaxf(ms - t * t, 0.f);
      const unsigned st = pk2(t, __builtin_amdgcn_sqrtf(lo16(pk2(lo16(pk2(var, var)) + 1e-5f, 0.f))));
      if (l < 4) __hip_atomic_store((gu32*)(FL + FL_FR + ((s * 4 + l) * 4 + (r >> 3)) * 32 + (r & 7)), st, RLX_AGENT);
    }
    RTS(6, 64);
    RST(4);
    RTS(3, 0);
#undef RTS
  }
  RSD();
  if (idx == 0 && threadIdx.x == 384) {
    __hip_atomic_store((gu64*)(ws + OFF_TAG + 8192), en + TAG_STEP, RLX_AGENT);
  }
  if (TOURS) asm volatile("s_endpgm\n\t.p2alignl 12, 0xbf800000\n.Lpad0xa0:\n\ts_setpc_b64 s[98:99]" ::: "memory");
}

constexpr int CUN = 2144;
constexpr int STN = 392;
__device__ __forceinline__ bf16x8 conv_bfrag(const unsigned* up, const unsigned sh) {
  const unsigned d0 = up[0], d1 = up[1], d2 = up[2], d3 = up[3], d4 = up[4];
  const v4u r = {__builtin_amdgcn_alignbit(d1, d0, sh), __builtin_amdgcn_alignbit(d2, d1, sh),
                 __builtin_amdgcn_alignbit(d3, d2, sh), __builtin_amdgcn_alignbit(d4, d3, sh)};
  return BF8(r);
}
__device__ __forceinline__ void role_conv(const MegaArgs& A, char* smem, const int cv) {
  __builtin_amdgcn_s_waitcnt(0x0F70);
  MARK();
  u16* Kr = reinterpret_cast<u16*>(smem);
  u16* Uc = Kr + 8 * 2 * CKN;
  u16* To = Uc + 8 * 2 * CUN;
  u16* Sx = To + 32 * 8;
  u16* St = Sx + 32 * 8;
  const int tid = threadIdx.x;
  const int w = __builtin_amdgcn_readfirstlane(tid >> 6);
  const int l = tid & 63, c = l & 15, g = l >> 4;
  const int d = 8 * cv + w;
  char* ws = A.ws;
  unsigned* FL = (unsigned*)(ws + OFF_FL);
  unsigned* TMO = FL + FL_TMO;
  u16* kr = Kr + w * 2 * CKN;
  float* prm = reinterpret_cast<float*>(kr);
  v4u tx[4];
  f32x4 ac[16] = {};
  LST(16);
  const v2u en = LD64(MKRS(ws + OFF_TAG, 8192 + 8), 0, 8192);
  {
    const unsigned pa = *reinterpret_cast<const unsigned*>(A.logAre + d * NST + 2 * l);
    const unsigned pb = *reinterpret_cast<const unsigned*>(A.Aim + d * NST + 2 * l);
    const unsigned pc = *reinterpret_cast<const unsigned*>(A.Cre + d * NST + 2 * l);
    const unsigned pd = *reinterpret_cast<const unsigned*>(A.Cim + d * NST + 2 * l);
    const float dt = bfr(expf(bf2f(A.logdt[d])));
    tx[0] = __builtin_amdgcn_raw_buffer_load_b128(MKRS(A.x0 + 8 * cv, 128 * DM * 2), tid * (DM * 2), 0, 0);
    tour<193, 200, 8>(w);
    reinterpret_cast<unsigned*>(Uc + w * CUN + ((l & 32) ? 32 + 512 : 0))[(l & 32) ? l & 31 : c] = 0u;
    reinterpret_cast<unsigned*>(Uc + w * CUN + 32 + ((l & 32) ? L_SEQ : 128))[l & 31] = 0u;
    if (l < 50) {
      reinterpret_cast<v4u*>(kr + 1808)[l] = v4u{0u, 0u, 0u, 0u};
      reinterpret_cast<v4u*>(kr + CKN + 1808)[l] = v4u{0u, 0u, 0u, 0u};
    }
#pragma unroll
    for (int e = 0; e < 2; ++e) {
      const float ar = -bfr(expf(e ? hi16(pa) : lo16(pa))) * dt, ai = (e ? hi16(pb) : lo16(pb)) * dt;
      const float ex = expf(ar);
      float sn, cs;
      sincos1(ai, &sn, &cs);
      if (e == 1) pad<187, 1050>(sn);
      prm[2 * l + e] = ar;
      prm[NST + 2 * l + e] = ai;
      prm[2 * NST + 2 * l + e] = (e ? hi16(pc) : lo16(pc)) * dt;
      prm[3 * NST + 2 * l + e] = (e ? hi16(pd) : lo16(pd)) * dt;
      prm[4 * NST + 2 * l + e] = ex * cs;
      prm[5 * NST + 2 * l + e] = ex * sn;
    }
  }
  LST(21); LSC(62);
  const float dsk = bf2f(A.Dskip[d]);
  const u16* ka = kr + (c >> 3) * CKN + 2080 - 16 * (c & 7) + 8 * g;
  const unsigned ush = (c & 1) * 16;
#define KFRAG(D0) (*reinterpret_cast<const bf16x8*>(ka - (D0)))
  const __amdgpu_buffer_rsrc_t rz1 = MKRS(ws + OFF_XA, SZ_X);
  const int t0 = 16 * (4 * (g & 1) + (g >> 1)) + c;
  {
    const u16* uc = Uc + w * CUN;
    const unsigned* ub = reinterpret_cast<const unsigned*>(uc + (c & ~1) + 8 * g + 32);
    const __amdgpu_buffer_rsrc_t rst = MKRS(ws + OFF_GT, L_SEQ * 4);
#define TILE_OUT(dst, av, T)                                                                   \
  {                                                                                            \
    const u16* u_ = Uc + w * CUN + 32 + 128 * (T) + t0;                                        \
    const unsigned uu = pk2(bf2f(u_[0]) * dsk, bf2f(u_[32]) * dsk);                            \
    const unsigned yy = pk2(swap_add32(av[0], av[1]), swap_add32(av[2], av[3]));               \
    const unsigned aa = pk2(lo16(yy) + lo16(uu), hi16(yy) + hi16(uu));                         \
    (dst)[t0] = (u16)aa;                                                                       \
    (dst)[t0 + 32] = (u16)(aa >> 16);                                                          \
  }
#define CHUNK_ST(ck, xr, src, n)                                                               \
  {                                                                                            \
    v4u o;                                                                                     \
    _Pragma("unroll") for (int e = 0; e < 4; ++e)                                              \
      o[e] = pk2(lo16(xr[e]) + bf2f((src)[(2 * e) * (n) + l]), hi16(xr[e]) + bf2f((src)[(2 * e + 1) * (n) + l])); \
    ST128(o, rz1, (l >> 5) * 32768 + (l & 31) * 16, (2 * (ck) * 64 + cv) * 512);               \
  }
#define U_ROW(row, xr_) U_ROW2(row, xr_, __builtin_amdgcn_raw_buffer_load_b32(rst, (row) * 4, 0, AUX_SC1))
#define U_ROW2(row, xr_, stv)                                                                       \
  {                                                                                            \
    const unsigned st_ = (stv);                                                                \
    const float mb_ = lo16(st_), ri_ = 1.0f / hi16(st_);                                       \
    _Pragma("unroll") for (int e = 0; e < 4; ++e) {                                            \
      const unsigned t_ = pk2(lo16(xr_[e]) - mb_, hi16(xr_[e]) - mb_);                         \
      const unsigned q_ = pk2(lo16(t_) * ri_, hi16(t_) * ri_);                                 \
      Uc[(2 * e) * CUN + 32 + (row)] = (u16)q_;                                                \
      Uc[(2 * e + 1) * CUN + 32 + (row)] = (u16)(q_ >> 16);                                    \
    }                                                                                          \
  }
    const unsigned* FS = FL + FL_FS + REP * 32 * 32;
    const int gi = 4 * (g * 8 + (l & 7));
    const int tl = 32 * ((l >> 3) & 1) + 8 * g + (l & 7);
    unsigned khl = 0u, fs = 0u, st0 = 0u;
    const int fo = (w + 8 * ((l >> 3) & 3)) * 32 + (l & 7);
#pragma unroll 1
    for (int ps = 0; ps < 2; ++ps) {
      float v[32];
      {
        const f32x4 ar = *reinterpret_cast<const f32x4*>(prm + gi), ai = *reinterpret_cast<const f32x4*>(prm + NST + gi);
        const f32x4 cr = *reinterpret_cast<const f32x4*>(prm + 2 * NST + gi), ci = *reinterpret_cast<const f32x4*>(prm + 3 * NST + gi);
        const f32x4 wr = *reinterpret_cast<const f32x4*>(prm + 4 * NST + gi), wi = *reinterpret_cast<const f32x4*>(prm + 5 * NST + gi);
        float p0 = (float)(64 * ps + 32 * ((l >> 3) & 1));
        pad<188, 1050>(p0);
        float yr0[4], yi0[4];
#pragma unroll
        for (int k = 0; k < 4; ++k) {
          const float e = __expf(ar[k] * p0);
          float sn, cs;
          __sincosf(ai[k] * p0, &sn, &cs);
          const float zr = e * cs, zi = e * sn;
          yr0[k] = cr[k] * zr - ci[k] * zi;
          yi0[k] = cr[k] * zi + ci[k] * zr;
        }
        RUN_START
#pragma unroll
        for (int i = 0; i < 32; ++i) {
          const f32x2_t sm2 = ya + yb;
          v[i] = sm2.x + sm2.y;
          if (i == 18) pad<189, 1000>(za);
          RUN_STEP
        }
      }
#pragma unroll
      for (int i = 0; i < 16; ++i) v[i] = swap_add32(v[i], v[i + 16]);
#pragma unroll
      for (int i = 0; i < 8; ++i) {
        if (i == 6) pad<190, 1000>(v[i]);
        v[i] = swap_add16(v[i], v[i + 8]);
        v[i] += dppf<0xB1>(0.f, v[i]);
        v[i] += dppf<0x4E>(0.f, v[i]);
        v[i] += dppf<0x141>(0.f, v[i]);
      }
      float s0 = v[0], s1 = v[1], s2 = v[2], s3 = v[3], s4 = v[4], s5 = v[5], s6 = v[6], s7 = v[7];
      asm volatile("" : "+v"(s0), "+v"(s1), "+v"(s2), "+v"(s3), "+v"(s4), "+v"(s5), "+v"(s6), "+v"(s7));
      s0 = (l & 4) ? s4 : s0; s1 = (l & 4) ? s5 : s1; s2 = (l & 4) ? s6 : s2; s3 = (l & 4) ? s7 : s3;
      s0 = (l & 2) ? s2 : s0; s1 = (l & 2) ? s3 : s1;
      const float kv = 2.0f * ((l & 1) ? s1 : s0);
      khl = pk2(kv, 0.f);
      khl = pk2(kv, kv - lo16(khl));
      kr[2080 - 64 * ps - tl] = (u16)khl;
      kr[CKN + 2080 - 64 * ps - tl] = (u16)(khl >> 16);
      SST(ps);
      LST(22 + 6 * ps);
      {
        if (ps == 0) {
          if (w < 2) {
            const unsigned long long m = (((unsigned long long)en.y << 32) | en.x) + TAG_STEP;
            v4u g0;
#pragma unroll 1
            for (unsigned spins = 0; spins < 16u * SPIN_CAP; ++spins) {
              g0 = LD128(MKRS(ws + OFF_G0, 8192), tid * 16, REP * 2048);
              if (__all(((g0.x ^ g0.z) | (g0.y ^ (unsigned)m) | (g0.w ^ (unsigned)(m >> 32))) == 0u)) break;
              asm volatile("s_sleep 2" ::: "memory");
            }
            LST(23);
            U_ROW2(tid, tx[0], g0.x);
          }
          __syncthreads();
          LST(24);
        }
        f32x4 a0t = {};
        pad<191, 1050>(a0t);
#pragma unroll
        for (int b = -1; b < 2; ++b) a0t = MFMA16(KFRAG(-32 * b), conv_bfrag(ub + 16 * b, ush), a0t);
        if (ps) {
#pragma unroll
          for (int b = 2; b < 4; ++b) a0t = MFMA16(KFRAG(-32 * b), conv_bfrag(ub + 16 * b, ush), a0t);
        }
        TILE_OUT(St + w * STN + 128 * ps, a0t, 0);
        __syncthreads();
        LST(25 + 6 * ps);
        if (w == ps) {
          CHUNK_ST(w, tx[0], St + 192 * w, STN);
          LST(26 + 6 * ps);
          pad<192, 900>();
          DRAIN();
          LST(27 + 6 * ps);
          if (ps == 0) {
            if (l < 4) __hip_atomic_store((gu64*)(ws + OFF_X0) + l * 128 + (cv >> 3) * 16 + (cv & 7), (((unsigned long long)en.y << 32) | en.x) + TAG_STEP, RLX_AGENT);
            zero_wait(ws);
          }
          if (l < 8) set_flag(FL + FL_FC + ((2 * w * 4 + l) * 8 + (cv >> 3)) * 32 + (cv & 7));
        }
        SST(4 + ps);
        if (ps == 0 && w >= 2) tx[0] = *reinterpret_cast<const v4u*>(A.x0 + (size_t)tid * DM + 8 * cv);
      }
#define FS_WAIT(o)                                                                             \
  {                                                                                            \
    _Pragma("unroll 1") for (unsigned spins = 0; !__all(fs != 0u) && !spin_timeout(spins, TMO); ++spins) { \
      __builtin_amdgcn_s_sleep(4);                                                             \
      fs = __hip_atomic_load((gu32*)FS + (o), RLX_AGENT);                                      \
    }                                                                                          \
    asm volatile("" ::: "memory");                                                             \
  }
    }
    pad<193, 950>();
    const gu32* kg = (gu32*)(FL + FL_KG + cv * 32 + w);
    fs = __hip_atomic_load((gu32*)FS + w * 32 + (l & 7), RLX_AGENT);
    unsigned kf = __hip_atomic_load(kg, RLX_AGENT);
    tour<180, 186, 8>(w + 6);
    FS_WAIT(w * 32 + (l & 7));
    st0 = __builtin_amdgcn_raw_buffer_load_b32(rst, tid * 4, 0, AUX_SC1);
    SST(6);
    LST(0); LSC(63);
    const __amdgpu_buffer_rsrc_t rk = MKRS(ws + OFF_KF + (size_t)d * 2 * CKN * 2, 2 * CKN * 2);
    v4u t[8];
    if (__builtin_amdgcn_readfirstlane(kf) == 0u) (void)wait_n<8, 1>(FL + FL_KG + cv * 32 + w, 1, TMO);
    asm volatile("" ::: "memory");
    LST(1);
#pragma unroll
    for (int i = 0; i < 4; ++i) {
      t[i] = LD128(rk, (64 * i + l) * 16, 0);
      t[4 + i] = LD128(rk, (64 * i + l) * 16, CKN * 2);
    }
    pad<195, 800>();
    if (w >= 2) U_ROW2(tid, tx[0], st0);
    unsigned* f1 = FL + FL_FC + (64 * w + (cv >> 3)) * 32 + (cv & 7);
    SST(7);
    LST(4);
    {
#pragma unroll
      for (int i = 0; i < 4; ++i)
        if (64 * i + l < 245) {
          reinterpret_cast<v4u*>(kr)[64 * i + l] = t[i];
          reinterpret_cast<v4u*>(kr + CKN)[64 * i + l] = t[4 + i];
        }
      kr[2080 - 64 - tl] = (u16)khl;
      kr[CKN + 2080 - 64 - tl] = (u16)(khl >> 16);
      if (l < 33) kr[l] = kr[CKN + l] = 0;
    }
    pad<196, 1050>();
    RAW_BAR();
    LST(5);
    fs = __hip_atomic_load((gu32*)FS + fo, RLX_AGENT);
    {
      f32x4 a1 = {};
#pragma unroll
      for (int b = -1; b < 8; ++b) a1 = MFMA16(KFRAG(128 - 32 * b), conv_bfrag(ub + 16 * b, ush), a1);
      pad<197, 300>(a1);
      TILE_OUT(St + w * STN, a1, 1);
    }
    bf16x8 kq[16], bw[4];
#pragma unroll
    for (int t = 0; t < 4; ++t) bw[t] = conv_bfrag(ub + 64 * t - 16, ush);
#pragma unroll
    for (int i = 0; i < 16; ++i) kq[i] = KFRAG(128 * i + 32);
    ac[2] = MFMA16(KFRAG(-96), bw[3], ac[2]);
    ac[3] = MFMA16(KFRAG(-96), conv_bfrag(ub + 16 * 15, ush), ac[3]);
    LST(2);
    RAW_BAR();
    if ((w >> 1) == 1) {
      CHUNK_ST(w, tx[0], St + 64 * w - 128, STN);
      DRAIN();
      if (l < 8) set_flag(f1 + l * 256);
    }
    pad<198, 300>();
    FS_WAIT(fo);
    LST(3);
    unsigned sx[4];
#pragma unroll
    for (int i = 1; i < 4; ++i) sx[i] = __builtin_amdgcn_raw_buffer_load_b32(rst, (tid + 512 * i) * 4, 0, AUX_SC1);
#pragma unroll
    for (int i = 2; i < 16; ++i) asm volatile("" : "+v"(ac[i]));
    asm volatile("s_nop 1" : "+v"(bw[0]), "+v"(bw[1]), "+v"(bw[2]), "+v"(bw[3]));
#pragma unroll
    for (int q = 0; q < 4; ++q) {
      unsigned r[20];
      v4u nw[4];
      float ri;
#pragma unroll
      for (int i = 15; i >= -1; --i) {
#pragma unroll
        for (int t = 0; t < 4; ++t) {
          if (i >= 0 && i + t >= 2 && i + t < 16) asm volatile("v_mfma_f32_16x16x32_bf16 %0, %1, %2, %0" : "+v"(ac[i + t]) : "v"(kq[i]), "v"(bw[t]));
          if (q < 3 && t == 0 && i < 15) kq[i + 1] = KFRAG(128 * i + 128 - 32 * q);
          if (q < 3 && (t & 1) && i >= 8 && i < 13) {
            r[10 * (t >> 1) + i - 8] = ub[128 * (t >> 1) + 16 * q + i - 8];
            r[10 * (t >> 1) + i - 3] = ub[128 * (t >> 1) + 16 * q + i + 56];
          }
          if (q < 3 && i >= 2 && i < 6) nw[t][i - 2] = __builtin_amdgcn_alignbit(r[5 * t + i - 1], r[5 * t + i - 2], ush);
          if (q < TXQ && t == 2 && i == 12) tx[q + 1] = *reinterpret_cast<const v4u*>(A.x0 + (size_t)(tid + 512 * (q + 1)) * DM + 8 * cv);
          if (q == TXQ - 1 && t == 2 && i > 9 + TXQ - 1 && i < 12) tx[13 + q - i] = *reinterpret_cast<const v4u*>(A.x0 + (size_t)(tid + 512 * (13 + q - i)) * DM + 8 * cv);
          if (q && t == 2 && i == 8) ri = 1.0f / hi16(sx[q]);
          if (q && (t & 1) && (i == 7 || i == 6)) {
            const int e = 2 * (i & 1) + (t >> 1);
            const unsigned t_ = pk2(lo16(tx[q][e]) - lo16(sx[q]), hi16(tx[q][e]) - lo16(sx[q]));
            const unsigned q_ = pk2(lo16(t_) * ri, hi16(t_) * ri);
            Uc[(2 * e) * CUN + 32 + tid + 512 * q] = (u16)q_;
            Uc[(2 * e + 1) * CUN + 32 + tid + 512 * q] = (u16)(q_ >> 16);
          }
          __builtin_amdgcn_sched_barrier(0);
        }
        if (q == 0 && i == 12) pad<199, 200>();
        if (q == 0 && i == 1) pad<180, 200>();
        if (q == 1 && i == 8) pad<181, 200>();
        if (q == 2 && i == 12) pad<182, 200>();
        if (q == 2 && i == 5) pad<183, 200>();
        if (q == 3 && i == 8) pad<184, 200>();
      }
#pragma unroll
      for (int t = 0; t < 4 && q < 3; ++t) bw[t] = BF8(nw[t]);
    }
    asm volatile("s_nop 7" : "+v"(ac[2]), "+v"(ac[3]));
    pad<194, 300>(ac[3]);
    TILE_OUT(St + w * STN + 128, ac[2], 2);
    TILE_OUT(St + w * STN + 256, ac[3], 3);
    SST(10);
    LST(6); LSC(64);
    RAW_BAR();
    if (w >= 4) {
      CHUNK_ST(w, tx[0], St + 64 * w - 128, STN);
      DRAIN();
      if (l < 8) set_flag(f1 + l * 256);
    }
#undef FS_WAIT
    if (w < 4) tour<176, 180, 4>(w);
    if (w == 1) (void)(wait_n<SLP_V, 32>(FL + FL_FZ + REP * 64, 64, TMO));
    pad<185, 500>();
    asm volatile("" ::: "memory");
    SST(9);
    LST(9);
    LSD();
#undef U_ROW
#undef U_ROW2
#undef TILE_OUT
#undef CHUNK_ST
  }
  const unsigned sb = lds_base(smem);
  const unsigned u1 = sb + (16 * CKN + w * CUN) * 2, u2 = u1 + 16 * CUN, to = sb + (16 * CKN + 16 * CUN) * 2, st = to + 1024;
  const unsigned win = ((c & ~1) + 8 * g) * 2, row = 16 * (g >> 1) + c;
  const bool nil = c < 8 && g < 2;
  if (l < 16) reinterpret_cast<unsigned*>(Uc + (8 + w) * CUN)[l] = 0u;
  bf16x8 th, tw;
#pragma unroll
  for (int i = 0; i < 8; ++i) {
    th[i] = (short)kr[2064 - c + 8 * g + i];
    tw[i] = (short)kr[CKN + 2064 - c + 8 * g + i];
  }
  v4u known;
#pragma unroll
  for (int e = 0; e < 4; ++e) known[e] = (c + 8 * g + 2 * e < 32 ? 0xffffu : 0u) | (c + 8 * g + 2 * e < 31 ? 0xffff0000u : 0u);
  const bf16x8 kz = KFRAG(-96);
  const v8u pa = {u2 + (nil ? 0 : c < 8 ? 32 + 16 * g : 64 + 16 * g), nil ? 0u : 64u, u2 + 64 + 2 * row, to + 16 * row + 2 * w, u2 + win,
                  sb + (unsigned)((w * 2 + (c >> 3)) * CKN + 2080 - 16 * (c & 7) + 8 * g) * 2 - 3904, ush, (unsigned)l * 16u};
  const v8u pb = {u1 + win + 1024, 0u, 0u, 0u, 0u, u1 + (32 + 512 + t0) * 2, st + (w * STN + t0) * 2, st + (64 * (w & 1) + l) * 2};
  const v4u pc = {(unsigned)((l >> 5) * 32768 + (l & 31) * 16), (unsigned)((l >> 1) * (DM * 2) + (l & 1) * 8), (unsigned)((l >> 4) * 128 + ((l >> 1) & 7) * 4),
                  sb + (16 * CKN + (8 + 4 * (l & 1)) * CUN + 32 + (l >> 1)) * 2};
  const char* dbg = ws;
  asm volatile("s_mov_b64 exec, -1\ns_nop 4\n" CONV_ASM
               ::"{v[0:7]}"(pa), "{v[8:11]}"(known), "{v[12:15]}"(th), "{v[16:19]}"(tw), "{v[24:27]}"(kz), "{v[28:35]}"(pb), "{v[36:39]}"(pc),
               "{v40}"(to + (w ? l * 8 : (l & 31) * 16)), "{v[44:47]}"(tx[1]), "{v[48:51]}"(tx[2]), "{v[52:55]}"(tx[3]),
               "{v[196:199]}"(ac[4]), "{v[200:203]}"(ac[5]), "{v[204:207]}"(ac[6]), "{v[208:211]}"(ac[7]), "{v[212:215]}"(ac[8]), "{v[216:219]}"(ac[9]),
               "{v[220:223]}"(ac[10]), "{v[224:227]}"(ac[11]), "{v[228:231]}"(ac[12]), "{v[232:235]}"(ac[13]), "{v[236:239]}"(ac[14]), "{v[240:243]}"(ac[15]),
               "{s[40:43]}"(mk_srd(ws + OFF_X2, SZ_X)), "{s[44:47]}"(mk_srd(FL + FL_FR, 64 * 16 * 128 + 2048)), "{s71}"(64 * 16 * 128 + REP * 512), "{s[48:51]}"(mk_srd(ws + OFF_X3, SZ_X)),
               "{s[52:55]}"(mk_srd(ws + OFF_XA, SZ_X)), "{s[58:59]}"(FL + FL_FC + (64 * 32 + (cv >> 3)) * 32 + (cv & 7)),
               "{s[60:61]}"(FL + FL_FC + (64 * (w + 8) + (cv >> 3)) * 32 + (cv & 7)), "{s62}"(0), "{s63}"(w),
               "{s64}"(__builtin_amdgcn_readfirstlane(__float_as_uint(dsk))), "{s65}"(cv * 16), "{s66}"(REP * 512), "{s67}"(cv * 512),
               "{s68}"((w + 8) * 65536 + cv * 512), "{s69}"(now10()), "{s[72:73]}"(0x0000ffff0000ffffull), "{s[36:37]}"(dbg),
               [lt] "n"(-CVLT), [gv] "n"(GATE_V), [sv] "n"(SLP_V)
               : "memory", "vcc", "scc", "s81", "v255");
#undef KFRAG
}

__device__ __forceinline__ v4u ln_row_packed(const v4u xr) {
  float v[8];
#pragma unroll
  for (int e = 0; e < 4; ++e) { v[2 * e] = lo16(xr[e]); v[2 * e + 1] = hi16(xr[e]); }
  float s = ((v[0] + v[1]) + (v[2] + v[3])) + ((v[4] + v[5]) + (v[6] + v[7]));
  s = wave_sum(s);
  const float mean = s * (1.0f / DM);
  float q = 0.f;
#pragma unroll
  for (int j = 0; j < 8; ++j) { float t = v[j] - mean; q += t * t; }
  q = wave_sum(q);
  const float var = q * (1.0f / DM);
  const float mb = bfr(mean);
  const float sd = bfr(sqrtf(bfr(bfr(var) + 1e-5f)));
  const float ri = 1.0f / sd;
  v4u o;
#pragma unroll
  for (int e = 0; e < 4; ++e) {
    const unsigned t = pk2(v[2 * e] - mb, v[2 * e + 1] - mb);
    const float x0 = lo16(t), x1 = hi16(t);
    float q0 = x0 * ri, q1 = x1 * ri;
    q0 = __builtin_fmaf(__builtin_fmaf(-q0, sd, x0), ri, q0);
    q1 = __builtin_fmaf(__builtin_fmaf(-q1, sd, x1), ri, q1);
    o[e] = pk2(q0, q1);
  }
  return o;
}

__device__ __forceinline__ float bfr1(float x) { return lo16(pk2(x, x)); }
__device__ __forceinline__ void ln4_packed(v4u (&x)[4]) {
  f32x2_t v[16];
#pragma unroll
  for (int e = 0; e < 16; ++e) v[e] = f32x2_t{lo16(x[e >> 2][e & 3]), hi16(x[e >> 2][e & 3])};
  const f32x2_t t = (((v[0] + v[1]) + (v[2] + v[3])) + ((v[4] + v[5]) + (v[6] + v[7]))) +
                    (((v[8] + v[9]) + (v[10] + v[11])) + ((v[12] + v[13]) + (v[14] + v[15])));
  const float mean = red_mod4(t.x + t.y) * (1.0f / DM);
  const float mb = bfr1(mean);
  f32x2_t q0 = {0.f, 0.f}, q1 = {0.f, 0.f};
#pragma unroll
  for (int e = 0; e < 16; e += 2) {
    v[e] -= mb;
    v[e + 1] -= mb;
    q0 += v[e] * v[e];
    q1 += v[e + 1] * v[e + 1];
  }
  q0 += q1;
  const float dm = mean - mb;
  const float var = fmaxf(red_mod4(q0.x + q0.y) * (1.0f / DM) - dm * dm, 0.f);
  const float sd = bfr1(sqrtf(bfr1(bfr1(var) + 1e-5f)));
  const float ri = 1.0f / sd;
#pragma unroll
  for (int e = 0; e < 16; ++e) {
    const unsigned t2 = pk2(v[e].x, v[e].y);
    const f32x2_t q = f32x2_t{lo16(t2), hi16(t2)} * ri;
    x[e >> 2][e & 3] = pk2(q.x, q.y);
  }
}

extern "C" __device__ float mul1(float, float) __asm("llvm.amdgcn.fmul.legacy");
template <bool G0>
__device__ __forceinline__ void role_qkv_t(const MegaArgs& A, char* smem, const int L, const int h) {
  __builtin_amdgcn_s_waitcnt(0x0F70);
  MARK();
  const int w = __builtin_amdgcn_readfirstlane(threadIdx.x >> 6);
  char* ws = A.ws;
  unsigned* FL = (unsigned*)(ws + OFF_FL);
  unsigned* TMO = FL + FL_TMO;
#define LANE_IDS()                         \
  const int l = lane_id();                 \
  const int c = l & 15, g = l >> 4;        \
  (void)c; (void)g;
  unsigned* FC = FL + FL_FC + REP * 8 * 32;
  const int s0 = L * QN0;
  bf16x8 wa[16], wb[16], wv[16];
  const char* Xb = ws + OFF_XA + s0 * 32768 + (w & 3) * 8192;
  const v4u rsF = mk_srd(FC + (w & 3) * 64, ((L ? 64 : QN0) * 32 - REP * 8 - 2 * (w & 3)) * 128);
  constexpr unsigned Q_R = 131072;
  bool zs, got = false;
  v2u n0 = {};
  f32x4 cs[3] = {};
  PSV;
  {
    LANE_IDS();
    const unsigned sb = lds_base(smem);
    const int q = (l >> 2) & 3, p = l & 3, f = q | ((g & 1) << 2);
    const unsigned r0 = sb + (g >> 1) * 16384 + (8 * (g & 1) + q) * 256;
    const unsigned ra = r0 + (w ^ f) * 32 + p * 8, rb = r0 + ((2 * (w & 3) + (p >> 1)) ^ f) * 32 + (p & 1) * 16;
    const v4u one = {0x3f803f80u, 0x3f803f80u, 0x3f803f80u, 0x3f803f80u};
    v4u zt;
    v2u ze;
    unsigned txt = 0u;
#define WP(k) (WM(k) == 0 ? A.Wv : WM(k) == 1 ? A.Wk : A.Wq)
#define W_READ(W, a_)                                    \
  {                                                      \
    const v2u x0 = trd(a_), x1 = trd((a_) + 1024);       \
    W = BF8((v4u{x0.x, x0.y, x1.x, x1.y}));              \
  }
    if (QSL && L) gate(now10() + QSL);
    PST(0);
#pragma unroll
    for (int k = 0; k < 12; ++k) w_issue<false>(WP(k >> 2), WJ(k >> 2), k >> 2, k & 3, h, w, l, sb);
    PST(1);
    constexpr int PB = G0 ? 140 : 160;
    tour<PB + 5, PB + 12 + !G0, 4>(w);
#pragma unroll
    for (int k = 0; k < 12; ++k) {
      const int j = k & 3;
      if (k == (G0 ? 5 : 7)) pad<PB + 6>(cs[2]);
      if (k == (G0 ? 11 : 9)) pad<PB + 7>(cs[2]);
      if (k == (G0 ? 7 : 11)) pad<PB + 1, 700>(cs[2]);
      if (k == ZK + 1) wait_vm<10>();
      else if (k < 10) wait_vm<8>();
      else if (k == 10) wait_vm<G0 && ZK < 8 ? 5 : 4>();
      else wait_vm<G0 ? 1 : 0>();
      RAW_BAR();
      if (k == IPQ) txt = pull_text((h & 7) + 8 * w);
      if (k == IPQ + 2) asm volatile("" ::"v"(txt));
      if (k == ZK)
        asm volatile("s_nop 4\n\tglobal_load_dwordx4 %0, %2, %3 sc1\n\tglobal_load_dwordx2 %1, %4, %5 sc1"
                     : "=&v"(zt), "=&v"(ze)
                     : "v"(l * 16), "s"(ws + OFF_TAG + (blockIdx.x & 7) * 1024), "v"(0), "s"(ws + OFF_TAG + 8192)
                     : "memory");
#define X_LOOK() asm volatile("s_nop 4\n\tglobal_load_dwordx2 %0, %1, %2 sc1" : "=v"(n0) : "v"((l & 7) * 8 + (l & 8) * 16), "s"(ws + OFF_X0 + REP * 1024 + w * 256) : "memory")
#define X_SEEN() zero_seen(v4u{n0.x, n0.y, n0.x, n0.y}, ze)
      if (k == ZK + 2) {
        asm volatile("" : "+v"(zt), "+v"(ze));
        zs = zero_seen(zt, ze);
        if (G0) X_LOOK();
      }
#define X_ASK()                                                                                                \
  {                                                                                                            \
    asm volatile("" : "+v"(n0));                                                                               \
    got = !L && X_SEEN();                                                                                      \
    if (__builtin_expect(got, 1)) {                                                                            \
      _Pragma("unroll 1") for (int i = 0; i < 8192; i += 1024) glds16c(Xb + i, l * 16, sb + Q_R + w * 8192 + i); \
    }                                                                                                          \
  }
      if (k == 11 && G0) {
        WAIT_VM(0);
        X_ASK();
        X_LOOK();
      }
#pragma unroll
      for (int i = 0; i < 4; ++i) {
        if (k == 2 && i == (G0 ? 0 : 3)) pad<PB + 5>(cs[2]);
        if (k < 9) w_issue<false>(WP(k + 3), WJ(k + 3), (k + 3) & 3, i, h, w, l, sb);
        if (WM(k) == 0) W_READ(wv[4 * WJ(k) + i], ra + j * 4096 + i * 32768);
        if (WM(k) == (G0 ? 1 : 2)) {
          W_READ(wa[4 * WJ(k) + i], rb + j * 4096 + i * 32768);
          W_READ(wb[4 * WJ(k) + i], rb + 8 + j * 4096 + i * 32768);
        }
        if (k > 0 && WM(k - 1) == 0) cs[2] = MFMA16(BF8(one), wv[4 * WJ(k - 1) + i], cs[2]);
        if (k > 0 && WM(k - 1) == (G0 ? 1 : 2)) {
          cs[0] = MFMA16(BF8(one), wa[4 * WJ(k - 1) + i], cs[0]);
          cs[1] = MFMA16(BF8(one), wb[4 * WJ(k - 1) + i], cs[1]);
        }
        asm volatile("" : "+v"(cs[0]), "+v"(cs[1]), "+v"(cs[2]));
      }
      if (k == 1) PST(2);
      if (k == 3) PST(3);
      if (k == 5) PST(4);
      if (k == 7) PST(5);
      if (k == 9) PST(6);
    }
    if (got) PST(11);
#pragma unroll
    for (int i = 12; i < 16 && !G0; ++i) {
      cs[0] = MFMA16(BF8(one), wa[i], cs[0]);
      cs[1] = MFMA16(BF8(one), wb[i], cs[1]);
    }
    asm volatile("s_nop 7" : "+v"(cs[0]), "+v"(cs[1]));
    RAW_BAR();
    PST(8);
    if (G0 && __builtin_expect(!got, 1)) {
      WAIT_VM(0);
      X_ASK();
    }
    if (got) PST(12);
#undef X_LOOK
#undef X_SEEN
#undef X_ASK
#undef W_READ
#undef WP
  }
  WSTAMP2();
  constexpr int Q_X = 98304;
  const unsigned sb = lds_base(smem);
  v2u ea, eb, ev;
  {
    LANE_IDS();
    v2u e[3];
#pragma unroll
    for (int i = 0; i < 3; ++i) {
      const float x = g ? 0.f : cs[i][0], x1 = x - bfr1(x);
      e[i] = v2u{pk2(x, x1), pk2(x1 - bfr1(x1), 0.f)};
    }
    ea = e[0]; eb = e[1]; ev = e[2];
    asm volatile("" : "+v"(ea), "+v"(eb), "+v"(ev));
    if (__builtin_expect(!zs, 0)) zero_wait(ws);
    *reinterpret_cast<v2u*>(smem + Q_X + w * 2048 + (l >> 5) * 16384 + (l & 31) * 8) = v2u{0x3f803f80u, 0x3f803f80u};
  }
  pad<G0 ? 148 : 168>();
  constexpr int Q_ST = Q_X + 8 * 2048 + 256, Q_CNT = Q_X + 11 * 2048 + 256;
  *(lu32*)(unsigned long)(sb + Q_CNT + (threadIdx.x & 1) * 4) = 0u;
  v8u pv, wr[24];
  v4u pw;
  unsigned sw;
  {
    LANE_IDS();
    const unsigned fa = sb + Q_R + g * 512 + c * 16;
    pv = v8u{ea.x, ea.y, eb.x, eb.y, ev.x, ev.y, (unsigned)l * 16u, fa};
    pw = v4u{sb + Q_ST + c * 4, sb + Q_ST + g * 16,
             G0 ? (l & 7) * 4 + (l & 8) * 16 : (c & 8) ? sb + Q_X + ((w + 1) & 1) * 128 : fa + (w & 3) * 128, (c & 8) ? 0u : Q_R};
    sw = sb + Q_ST + (w & 3) * 32 + (l & 7) * 4;
#pragma unroll
    for (int i = 0; i < 8; ++i) {
      wr[i] = __builtin_shufflevector(__builtin_bit_cast(v4u, wa[2 * i]), __builtin_bit_cast(v4u, wa[2 * i + 1]), 0, 1, 2, 3, 4, 5, 6, 7);
      wr[8 + i] = __builtin_shufflevector(__builtin_bit_cast(v4u, wb[2 * i]), __builtin_bit_cast(v4u, wb[2 * i + 1]), 0, 1, 2, 3, 4, 5, 6, 7);
      wr[16 + i] = __builtin_shufflevector(__builtin_bit_cast(v4u, wv[2 * i]), __builtin_bit_cast(v4u, wv[2 * i + 1]), 0, 1, 2, 3, 4, 5, 6, 7);
    }
  }
  const v4u rsQ = mk_srd(ws + OFF_QK, 2 * SZ_QK), rsV = mk_srd(ws + OFF_VF, 2 * SZ_O);
  const v4u rsM = mk_srd(ws + (G0 ? OFF_MEMK : OFF_MEMV), 2 * SZ_MEM);
  const unsigned* FQ = FL + FL_FQ + (s0 * 32 + h) * 32;
  const char* dbg = ws;
  PST(9);
  __builtin_amdgcn_s_waitcnt(0x0F70);
  asm volatile("" ::"v"(n0));
  RAW_BAR();
  PST(10);
  PSD();
#define QKV_OPS                                                                                                              \
  ::"{v[64:71]}"(wr[0]), "{v[72:79]}"(wr[1]), "{v[80:87]}"(wr[2]), "{v[88:95]}"(wr[3]), "{v[96:103]}"(wr[4]),                \
      "{v[104:111]}"(wr[5]), "{v[112:119]}"(wr[6]), "{v[120:127]}"(wr[7]), "{v[128:135]}"(wr[8]), "{v[136:143]}"(wr[9]),      \
      "{v[144:151]}"(wr[10]), "{v[152:159]}"(wr[11]), "{v[160:167]}"(wr[12]), "{v[168:175]}"(wr[13]), "{v[176:183]}"(wr[14]), \
      "{v[184:191]}"(wr[15]), "{v[192:199]}"(wr[16]), "{v[200:207]}"(wr[17]), "{v[208:215]}"(wr[18]), "{v[216:223]}"(wr[19]), \
      "{v[224:231]}"(wr[20]), "{v[232:239]}"(wr[21]), "{v[240:247]}"(wr[22]), "{v[248:255]}"(wr[23]), "{v[0:7]}"(pv),        \
      "{v[8:11]}"(pw), "{v62}"(sw), "{s[40:43]}"(rsQ), "{s[44:47]}"(rsV), "{s[48:51]}"(rsM), "{s[52:55]}"(rsF),              \
      "{s[56:57]}"(Xb), "{s58}"(sb + Q_R + (w & 3) * 8192), "{s82}"(sb + Q_CNT + 16 + (w & 3) * 4), "{s69}"(__builtin_amdgcn_readfirstlane((int)got)), "{s59}"((s0 * 512 + h * 16 + (G0 ? 8 : 0) + (w & 3)) * 1024), "{s60}"((s0 * 256 + h * 8 + w) * 1024), \
      "{s61}"(s0 * 65536 + h * 2048 + (w & 3) * 512), "{s62}"(s0 * 4096), "{s63}"(L ? QFAST - QN0 : 0), "{s[64:65]}"(FQ), "{s66}"(L ? NSEG - 1 - QN0 : QN0 - 1), "{s67}"(0), \
      "{s83}"(G0 ? 4 - L : L ? NSEG - 1 - QN0 : QN0 - 1), "{s86}"(1 - L), [n0] "n"(QN0), [x3] "n"(OFF_X3 - OFF_XA - QN0 * 32768),            \
      "{s[70:71]}"(0xaaaaaaaaaaaaaaaaull), "{s[72:73]}"(0xccccccccccccccccull), "{s78}"(w), "{s80}"(sb + Q_CNT), "{s[84:85]}"(FL + FL_S0 + (h >> 3) * 32 + (h & 7)),             \
      "{s[36:37]}"(dbg), [gq] "n"(GATE_Q), [sq] "n"(SLP_Q), [s1] "n"(SLP_Q1 - SLP_Q)                                         \
      : "memory", "vcc", "scc", "s81", "v63"
  if (G0) asm volatile("s_mov_b64 exec, -1\ns_nop 4\n" QKV_ASM_0 QKV_OPS);
  else asm volatile("s_mov_b64 exec, -1\ns_nop 4\n" QKV_ASM_1 QKV_OPS);
#undef QKV_OPS
#undef LANE_IDS
}
__device__ __forceinline__ void role_qkv(const MegaArgs& A, char* smem, const int L, const int h) {
  if (__builtin_amdgcn_readfirstlane(threadIdx.x >> 6) < 4) role_qkv_t<true>(A, smem, L, h);
  else role_qkv_t<false>(A, smem, L, h);
}

constexpr int MEGA_WGS = 256;
constexpr size_t MEGA_LDS = 163840;
static_assert(MEGA_LDS <= 163840 && MEGA_LDS >= 162816 + 256, "lds");
static_assert(AW_SO + 49152 <= MEGA_LDS, "lds");
static_assert(MEGA_LDS >= (size_t)(8 * 2 * CKN + 8 * 2 * CUN + 2 * 32 * 8 + 8 * STN) * 2, "lds");
static_assert(MEGA_LDS >= (size_t)2 * 32 * SMS * 2 && MEGA_LDS >= 131072, "lds");
static_assert(131072 >= (size_t)(40 * NST + 32 * 400) * 4, "lds");
__global__ __launch_bounds__(512, 2) void mega_kernel(const u16* mem0, const u16* Dskip, char* ws, u16* out, const u16* x0,
                                                      const u16* logAre, const u16* Aim, const u16* Cre, const u16* Cim,
                                                      const u16* logdt, const u16* Wq, const u16* Wk, const u16* Wv,
                                                      const u16* Wo) {
  extern __shared__ __attribute__((aligned(16))) char smem[];
  if (TOURS & 4) {
    const unsigned cls = (0xfdac >> (((blockIdx.x & 1) * 4 + (blockIdx.x >> 6)) * 2)) & 3;
#define TO(a, b, c, d, e, f, g, h) "s_branch .Lpad" #a "\n\ts_branch .Lpad" #b "\n\ts_branch .Lpad" #c "\n\ts_branch .Lpad" #d "\n\ts_branch .Lpad" #e \
                                   "\n\ts_branch .Lpad" #f "\n\ts_branch .Lpad" #g "\n\ts_branch .Lpad" #h "\n\t"
    asm volatile("s_load_dword s94, %1, 0\n\ts_load_dword s95, %1, 64\n\ts_getpc_b64 s[96:97]\n\ts_add_u32 s96, s96, %0\n\ts_addc_u32 s97, s97, 0\n\ts_swappc_b64 s[98:99], s[96:97]\n\ts_branch 2f\n\t"
                 TO(0xc8, 0x78, 0x64, 0x79, 0x65, 0x81, 0x6d, 0x6e) TO(0xc8, 0xa0, 0x8c, 0xa1, 0x8d, 0xa9, 0x95, 0x97)
                 TO(0xc8, 0xba, 0xbb, 0xbc, 0xbd, 0xbe, 0xbf, 0xc0) TO(0xc9, 0xca, 0xcb, 0xcc, 0xcd, 0xce, 0xcf, 0xc8) "2:\n\ts_waitcnt lgkmcnt(0)"
                 ::"s"(cls * 32 + (__builtin_amdgcn_readfirstlane(threadIdx.x) >> 4) + 16), "s"(__builtin_amdgcn_kernarg_segment_ptr()) : "s94", "s95", "s96", "s97", "s98", "s99", "scc");
#undef TO
  }
  const int b = __builtin_amdgcn_readfirstlane(role_no());
  const MegaArgs A{mem0, Dskip, ws, out, x0, logAre, Aim, Cre, Cim, logdt, Wq, Wk, Wv, Wo, b};
  {
    const int hi = (b >= 32 && b < 96) ? b - 32 : (b >= 200 && b < 248) ? b - 136 : (b >= 96 && b < 104) ? b + 16 : (b >= 248 ? b - 128 : -1);
    if (hi >= 0) start_kgen(A, smem, hi, b < 248 - XI0 + 8 ? b - 248 + XI0 : -1, (b >= 64 && b < 104) || (b >= 232 && b < 240) ? b & 15 : -1);
  }
  if ((EXP_OFF & 1) && b >= 64 && b < 80) return;
  if ((EXP_OFF & 8) && b >= 200 && b < 232) return;
  if ((EXP_OFF & 16) && b >= 32 && b < 64) return;
  if ((EXP_OFF & 32) && b >= 80 && b < 96) return;
  if (b < 64) role_chain(A, smem, b >> 5, b & 31, 0);
  else if (b < 104 || (b >= 232 && b < 240)) role_aw(A, smem, b < 80 ? 0 : b < 96 ? 2 : 1, b & 15);
  else if (b < 168) role_conv(A, smem, b - 104);
  else if (b < 232) role_qkv(A, smem, (b - 168) >> 5, (b - 168) & 31);
  else role_red(A, smem, b - 240);
}

extern "C" void kernel_launch(void* const* d_in, const int* in_sizes, int n_in, void* d_out, int out_size, void* d_ws,
                              size_t ws_size, hipStream_t stream) {
  MegaArgs A;
  A.x0 = (const u16*)d_in[0];
  A.logAre = (const u16*)d_in[1];
  A.Aim = (const u16*)d_in[2];
  A.Cre = (const u16*)d_in[3];
  A.Cim = (const u16*)d_in[4];
  A.logdt = (const u16*)d_in[5];
  A.Dskip = (const u16*)d_in[6];
  A.mem0 = (const u16*)d_in[8];
  A.Wq = (const u16*)d_in[9];
  A.Wk = (const u16*)d_in[10];
  A.Wv = (const u16*)d_in[11];
  A.Wo = (const u16*)d_in[12];
  A.ws = (char*)d_ws;
  A.out = (u16*)d_out;

  (void)hipFuncSetAttribute((const void*)mega_kernel, hipFuncAttributeMaxDynamicSharedMemorySize, (int)MEGA_LDS);
  mega_kernel<<<MEGA_WGS, 512, MEGA_LDS, stream>>>(A.mem0, A.Dskip, A.ws, A.out, A.x0, A.logAre, A.Aim, A.Cre, A.Cim, A.logdt,
                                                   A.Wq, A.Wk, A.Wv, A.Wo);
}
```
